```python
import torch

def _softmax(x, axis=-1):
    return torch.softmax(x, dim=axis)

def _gelu(x):
    return torch.nn.functional.gelu(x, approximate='tanh')

def _silu(x):
    return torch.nn.functional.silu(x)

def _sigmoid(x):
    return torch.sigmoid(x)

def _layernorm(x, axis=-1, eps=1e-05):
    m = x.mean(dim=axis, keepdim=True)
    v = x.var(dim=axis, keepdim=True, unbiased=False)
    return (x - m) / torch.sqrt(v + eps)

def _scatter_add(tgt, idx, src, axis=0):
    return tgt.clone().index_add_(axis, idx, src)

def _segment_sum(src, seg, n):
    out = torch.zeros((n,) + src.shape[1:], dtype=src.dtype)
    return out.index_add_(0, seg, src)

def _pad(x, pw):
    flat = tuple((v for pair in reversed(pw) for v in pair))
    return torch.nn.functional.pad(x, flat)
kernel_name = 'adaLN_cond__compressed_kv_cla__moe_arch_mod__prompt'

def setup_inputs():

    _w = lambda a: (lambda t: t.to(torch.bfloat16) if t.is_floating_point() else t)(torch.as_tensor(a))

    def _A_setup():
        B, H, Sq, Sk, D, M = (1, 32, 2048, 2048, 128, 512)
        Dc = 4 * D
        return {'x': _w((lambda *s: torch.randn(*s, dtype=torch.bfloat16) * 0.1)(B, Sq, M)), 'c_kv': _w((lambda *s: torch.randn(*s, dtype=torch.bfloat16) * 3.2)(B, Sk, Dc)), 'w_q': _w((lambda *s: torch.randn(*s, dtype=torch.bfloat16) * 0.8)(M, H, D) * M ** (-0.5)), 'w_uk': _w((lambda *s: torch.randn(*s, dtype=torch.bfloat16) * 0.8)(Dc, H, D) * Dc ** (-0.5)), 'w_uv': _w((lambda *s: torch.randn(*s, dtype=torch.bfloat16) * 3.2)(Dc, H, D) * Dc ** (-0.5)), 'w_o': _w((lambda *s: torch.randn(*s, dtype=torch.bfloat16) * 3.2)(H, D, M) * (H * D) ** (-0.5))}
    _A_in = _A_setup()

    def _B_setup():
        B, S, D, F, C = (1, 2048, 512, 1792, 1)
        return {'x': _w((lambda *s: torch.randn(*s, dtype=torch.bfloat16) * 0.1)(B, S, D)), 'w_r': _w((lambda *s: torch.randn(*s, dtype=torch.bfloat16) * 3.2)(D)), 'w_gate': _w((lambda *s: torch.randn(*s, dtype=torch.bfloat16) * 3.2)(D, F)), 'w_up': _w((lambda *s: torch.randn(*s, dtype=torch.bfloat16) * 3.2)(D, F)), 'w_down': _w((lambda *s: torch.randn(*s, dtype=torch.bfloat16) * 3.2)(F, D))}
    _B_in = _B_setup()
    return {'A_x': _A_in['x'], 'A_c_kv': _A_in['c_kv'], 'A_w_q': _A_in['w_q'], 'A_w_uk': _A_in['w_uk'], 'A_w_uv': _A_in['w_uv'], 'A_w_o': _A_in['w_o'], 'B_x': _B_in['x'], 'B_w_r': _B_in['w_r'], 'B_w_gate': _B_in['w_gate'], 'B_w_up': _B_in['w_up'], 'B_w_down': _B_in['w_down'], 'c': _w((lambda *s: torch.randn(*s, dtype=torch.bfloat16) * 0.2)(1, 512) * 8.0), 'w_mod': _w((lambda *s: torch.randn(*s, dtype=torch.bfloat16) * 0.2)(512, 6 * 512) * 512 ** (-0.5) * 8.0)}

def reference(A_x, A_c_kv, A_w_q, A_w_uk, A_w_uv, A_w_o, B_x, B_w_r, B_w_gate, B_w_up, B_w_down, c, w_mod):

    def _A_ref(x, c_kv, w_q, w_uk, w_uv, w_o):
        B, Sq, M = (x.shape[0], x.shape[1], x.shape[2])
        Sk = c_kv.shape[1]
        H, D = (w_q.shape[1], w_q.shape[2])
        q = torch.einsum('bqm,mhd->bhqd', x, w_q)
        k = torch.einsum('bkc,chd->bhkd', c_kv, w_uk)
        v = torch.einsum('bkc,chd->bhkd', c_kv, w_uv)
        scores = torch.einsum('bhqd,bhkd->bhqk', q, k) * D ** (-0.5)
        off = Sk - Sq
        qi = torch.arange(Sq)[:, None]
        kj = torch.arange(Sk)[None, :]
        cmask = kj <= qi + off
        scores = torch.where(cmask[None, None], scores, -1000000000.0)
        p = _softmax(scores, axis=-1)
        ctx = torch.einsum('bhqk,bhkd->bhqd', p, v)
        return torch.einsum('bhqd,hdm->bqm', ctx, w_o)

    def _B_ref(x, w_r, w_gate, w_up, w_down):
        B, S, D = x.shape
        C = 1
        r = x @ w_r
        idx = torch.argsort(-r, axis=-1)[:, :C]
        idx_e = (lambda x, n, axis: torch.repeat_interleave(x, n, dim=axis))(idx.reshape(B, C, 1), D, axis=2)
        x_sel = (lambda x, i, axis: torch.take_along_dim(x, i, dim=axis))(x, idx_e, axis=1)
        g = _silu(x_sel @ w_gate)
        u = x_sel @ w_up
        y = g * u @ w_down
        r_sel = _sigmoid((lambda x, i, axis: torch.take_along_dim(x, i, dim=axis))(r, idx, axis=1))
        y = y * r_sel.reshape(B, C, 1)
        b_off = (torch.arange(B) * S).reshape(B, 1)
        flat_idx = (idx + b_off).reshape(B * C)
        zero = (x * 0.0).reshape(B * S, D)
        upd = _scatter_add(zero, flat_idx, y.reshape(B * C, D), axis=0)
        return x + upd.reshape(B, S, D)
    x = A_x
    mod = (_silu(c) @ w_mod).reshape(c.shape[0], 6, 1, -1)
    sa, ca, ga, sb, cb, gb = (mod[:, 0].clone(), mod[:, 1].clone(), mod[:, 2].clone(), mod[:, 3].clone(), mod[:, 4].clone(), mod[:, 5].clone())
    h = _layernorm(x, axis=-1) * (1 + ca) + sa
    x = x + ga * _A_ref(h, A_c_kv, A_w_q, A_w_uk, A_w_uv, A_w_o)
    h = _layernorm(x, axis=-1) * (1 + cb) + sb
    x = x + gb * _B_ref(h, B_w_r, B_w_gate, B_w_up, B_w_down)
    return x
```

```cpp
#include <hip/hip_runtime.h>
#include <stdint.h>
constexpr int SEQ = 2048, DM = 512, HD = 4096, FF = 1792;
#define ACLOB_ALL "a0","a1","a2","a3","a4","a5","a6","a7","a8","a9","a10","a11","a12","a13","a14","a15","a16","a17","a18","a19","a20","a21","a22","a23","a24","a25","a26","a27","a28","a29","a30","a31","a32","a33","a34","a35","a36","a37","a38","a39","a40","a41","a42","a43","a44","a45","a46","a47","a48","a49","a50","a51","a52","a53","a54","a55","a56","a57","a58","a59","a60","a61","a62","a63","a64","a65","a66","a67","a68","a69","a70","a71","a72","a73","a74","a75","a76","a77","a78","a79","a80","a81","a82","a83","a84","a85","a86","a87","a88","a89","a90","a91","a92","a93","a94","a95","a96","a97","a98","a99","a100","a101","a102","a103","a104","a105","a106","a107","a108","a109","a110","a111","a112","a113","a114","a115","a116","a117","a118","a119","a120","a121","a122","a123","a124","a125","a126","a127","a128","a129","a130","a131","a132","a133","a134","a135","a136","a137","a138","a139","a140","a141","a142","a143","a144","a145","a146","a147","a148","a149","a150","a151","a152","a153","a154","a155","a156","a157","a158","a159","a160","a161","a162","a163","a164","a165","a166","a167","a168","a169","a170","a171","a172","a173","a174","a175","a176","a177","a178","a179","a180","a181","a182","a183","a184","a185","a186","a187","a188","a189","a190","a191","a192","a193","a194","a195","a196","a197","a198","a199","a200","a201","a202","a203","a204","a205","a206","a207","a208","a209","a210","a211","a212","a213","a214","a215","a216","a217","a218","a219","a220","a221","a222","a223","a224","a225","a226","a227","a228","a229","a230","a231","a232","a233","a234","a235","a236","a237","a238","a239","a240","a241","a242","a243","a244","a245","a246","a247","a248","a249","a250","a251","a252","a253","a254","a255"
#define MAIN_ASM \
  \
".p2align 4\n" \
"s_load_dwordx16 s[16:31], %[ka], 0\n" \
"s_load_dwordx16 s[32:47], %[ka], 64\n" \
"s_load_dwordx16 s[48:63], %[ka], 128\n" \
"v_mbcnt_lo_u32_b32 v251, -1, 0\n" \
"v_mbcnt_hi_u32_b32 v251, -1, v251\n" \
"s_and_b32 s64, %[bid], 7\n" \
"s_lshr_b32 s65, %[bid], 3\n" \
"s_and_b32 s66, s65, 3\n" \
"s_lshr_b32 s67, %[bid], 5\n" \
"s_lshl2_add_u32 s68, s64, s66\n" \
"v_lshlrev_b32 v248, 4, v251\n" \
"v_lshlrev_b32 v0, 7, v251\n" \
"s_getpc_b64 s[96:97]\n" \
".Lpf0_%=:\n" \
"s_and_b32 s97, s97, 0xffff\n" \
"s_mov_b32 s98, .Lloop2_%=-.Lpf0_%=\n" \
"s_mov_b32 s99, 0x20000\n" \
"s_lshl2_add_u32 s74, s66, %[wid]\n" \
"s_lshl_b32 s74, s74, 13\n" \
"buffer_load_dword v245, v0, s[96:99], s74 offen\n" \
"v_bfe_u32 v1, v251, 2, 2\n" \
"v_bfe_u32 v2, v251, 4, 1\n" \
"v_lshl_add_u32 v1, v2, 3, v1\n" \
"v_lshlrev_b32 v1, 13, v1\n" \
"v_lshrrev_b32 v3, 5, v251\n" \
"v_lshl_or_b32 v1, v3, 6, v1\n" \
"v_and_b32 v4, 3, v251\n" \
"v_lshlrev_b32 v5, 1, v2\n" \
"v_xor_b32 v4, v4, v5\n" \
"v_lshl_or_b32 v246, v4, 4, v1\n" \
"v_lshrrev_b32 v6, 3, v251\n" \
"v_and_b32 v7, 7, v251\n" \
"v_lshrrev_b32 v8, 4, v251\n" \
"v_xor_b32 v9, v7, v8\n" \
"v_lshlrev_b32 v9, 4, v9\n" \
"v_lshl_or_b32 v249, v6, 10, v9\n" \
"v_xor_b32 v250, 64, v249\n" \
"s_sub_u32 s76, %[bid], 192\n" \
"s_min_u32 s76, s76, %[bid]\n" \
"s_mul_i32 s77, s76, 1366\n" \
"s_lshr_b32 s77, s77, 16\n" \
"s_mul_i32 s78, s77, 48\n" \
"s_sub_u32 s76, s76, s78\n" \
"s_lshl2_add_u32 s78, s77, %[wid]\n" \
"s_lshl_b32 s78, s78, 5\n" \
"v_lshl_add_u32 v10, v6, 2, s78\n" \
"v_lshlrev_b32 v11, 1, v10\n" \
"v_mul_u32_u24 v12, 0x1800, v10\n" \
"v_lshl_add_u32 v12, v7, 4, v12\n" \
"s_lshl_b32 s74, s76, 7\n" \
"s_add_u32 s74, s74, 3072\n" \
"v_add_u32 v12, s74, v12\n" \
"v_add_u32 v13, 0x3000, v12\n" \
"v_mov_b32 v14, 720896\n" \
"s_mul_i32 s81, s77, 12288\n" \
"s_lshl_b32 s74, s76, 8\n" \
"s_add_u32 s81, s81, s74\n" \
"s_add_u32 s81, s81, 917504\n" \
"s_lshl_b32 s74, %[wid], 8\n" \
"s_or_b32 s74, s74, %[bid]\n" \
"s_cmp_lt_u32 s74, 192\n" \
"s_cselect_b32 s81, s81, 1073741824\n" \
"s_lshr_b32 s74, %[wid], 1\n" \
"s_lshl_b32 s69, s74, 17\n" \
"s_and_b32 s74, %[wid], 1\n" \
"s_lshl_b32 s74, s74, 15\n" \
"s_add_u32 s69, s69, s74\n" \
"s_lshl_b32 s74, s68, 8\n" \
"s_add_u32 s69, s69, s74\n" \
"s_lshl_b32 s70, %[wid], 11\n" \
"s_add_u32 s70, s70, 65536\n" \
"s_lshl2_add_u32 s71, s67, %[wid]\n" \
"s_lshl_b32 s71, s71, 15\n" \
"s_lshl_b32 s72, %[wid], 14\n" \
"s_lshl_b32 s74, s67, 18\n" \
"s_sub_u32 s94, s71, s74\n" \
"s_add_u32 s94, s94, 1933312\n" \
"s_sub_u32 s74, 15, s67\n" \
"s_cmp_lt_u32 s68, 16\n" \
"s_cselect_b32 s74, s67, s74\n" \
"s_lshl_b32 s74, s74, 4\n" \
"s_bfe_u32 s76, s68, 0x20002\n" \
"s_sub_u32 s78, 7, s76\n" \
"s_bitcmp1_b32 s68, 1\n" \
"s_cselect_b32 s76, s78, s76\n" \
"s_lshl1_add_u32 s74, s76, s74\n" \
"s_and_b32 s76, s68, 1\n" \
"s_add_u32 s74, s74, s76\n" \
"s_lshl_b32 s78, s74, 3\n" \
"s_lshl1_add_u32 s74, %[wid], s78\n" \
"s_lshl_b32 s75, s74, 10\n" \
"s_waitcnt lgkmcnt(0)\n" \
"global_load_dwordx2 v[16:17], v11, s[40:41]\n" \
"global_load_dwordx4 v[20:23], v12, s[36:37] offset:-3072 nt\n" \
"global_load_dwordx4 v[24:27], v12, s[36:37] offset:3072 nt\n" \
"global_load_dwordx4 v[28:31], v13, s[36:37] offset:-3072 nt\n" \
"global_load_dwordx4 v[32:35], v13, s[36:37] offset:3072 nt\n" \
"s_add_u32 m0, s70, 0\n" \
"s_add_u32 s74, s69, 0\n" \
"buffer_load_dwordx4 v246, s[20:23], s74 offen lds\n" \
"s_add_u32 m0, s70, 1024\n" \
"s_add_u32 s74, s69, 128\n" \
"buffer_load_dwordx4 v246, s[20:23], s74 offen lds\n" \
"s_add_u32 m0, s72, 0\n" \
"s_add_u32 s74, s71, 0\n" \
"buffer_load_dwordx4 v249, s[16:19], s74 offen lds\n" \
"s_add_u32 m0, s72, 1024\n" \
"s_add_u32 s74, s71, 8192\n" \
"buffer_load_dwordx4 v250, s[16:19], s74 offen lds\n" \
"s_add_u32 m0, s72, 2048\n" \
"s_add_u32 s74, s71, 16384\n" \
"buffer_load_dwordx4 v249, s[16:19], s74 offen lds\n" \
"s_add_u32 m0, s72, 3072\n" \
"s_add_u32 s74, s71, 24576\n" \
"buffer_load_dwordx4 v250, s[16:19], s74 offen lds\n" \
"s_add_u32 m0, s72, 4096\n" \
"s_add_u32 s74, s94, 32768\n" \
"buffer_load_dwordx4 v249, s[16:19], s74 offen lds\n" \
"s_add_u32 m0, s72, 5120\n" \
"s_add_u32 s74, s94, 40960\n" \
"buffer_load_dwordx4 v250, s[16:19], s74 offen lds\n" \
"s_add_u32 m0, s72, 6144\n" \
"s_add_u32 s74, s94, 49152\n" \
"buffer_load_dwordx4 v249, s[16:19], s74 offen lds\n" \
"s_add_u32 m0, s72, 7168\n" \
"s_add_u32 s74, s94, 57344\n" \
"buffer_load_dwordx4 v250, s[16:19], s74 offen lds\n" \
"s_add_u32 m0, s70, 8192\n" \
"s_add_u32 s74, s69, 262144\n" \
"buffer_load_dwordx4 v246, s[20:23], s74 offen lds\n" \
"s_add_u32 m0, s70, 9216\n" \
"s_add_u32 s74, s69, 262272\n" \
"buffer_load_dwordx4 v246, s[20:23], s74 offen lds\n" \
"s_mov_b64 exec, 0\n" \
"s_cmp_eq_u32 %[wid], 0\n" \
"s_cbranch_scc1 .Lst0_0_%=\n" \
"s_cmp_eq_u32 %[wid], 1\n" \
"s_cbranch_scc1 .Lst0_1_%=\n" \
"s_cmp_eq_u32 %[wid], 2\n" \
"s_cbranch_scc1 .Lst0_2_%=\n" \
"s_branch .Lst0_3_%=\n" \
".Lstb0_%=:\n" \
"s_mov_b64 exec, -1\n" \
"v_mov_b32 v224, -1\n" \
"v_mov_b32 v225, -1\n" \
"s_min_u32 s74, %[bid], 223\n" \
"s_lshl_b32 s74, s74, 11\n" \
"s_lshl_b32 s76, %[wid], 9\n" \
"s_add_u32 s74, s74, s76\n" \
"s_add_u32 s74, s74, 98304\n" \
"v_lshl_add_u32 v228, v251, 3, s74\n" \
"s_lshl_b32 s74, %[wid], 8\n" \
"s_or_b32 s74, s74, %[bid]\n" \
"s_lshl_b32 s76, %[bid], 7\n" \
"s_add_u32 s76, s76, 65536\n" \
"s_cmp_lt_u32 s74, 256\n" \
"s_cselect_b32 s77, s76, 1073741824\n" \
"s_add_u32 s76, s76, 491392\n" \
"s_cmp_lt_u32 s74, 128\n" \
"s_cselect_b32 s76, s76, 1073741824\n" \
"v_mov_b32 v37, s77\n" \
"v_mov_b32 v38, s76\n" \
"v_cmp_gt_u32 vcc, 32, v251\n" \
"v_mov_b32 v230, 0\n" \
"s_lshl_b32 s77, %[wid], 16\n" \
"v_cndmask_b32 v37, v38, v37, vcc\n" \
"s_lshl_b32 s76, %[bid], 8\n" \
"s_add_u32 s76, s76, s77\n" \
"s_add_u32 s76, s76, 524288\n" \
"v_mov_b32 v38, s76\n" \
"s_sub_u32 s77, %[wid], 1\n" \
"s_cmp_lt_u32 s77, 2\n" \
"s_cselect_b64 vcc, -1, 0\n" \
"v_cndmask_b32 v37, v37, v38, vcc\n" \
"v_lshl_add_u32 v229, v251, 2, v37\n" \
"v_lshlrev_b32 v40, 12, v3\n" \
"v_lshl_or_b32 v40, v2, 8, v40\n" \
"v_bfe_u32 v41, v251, 2, 2\n" \
"v_lshl_or_b32 v40, v41, 6, v40\n" \
"v_lshl_or_b32 v40, v2, 5, v40\n" \
"v_and_b32 v41, 3, v251\n" \
"v_lshl_or_b32 v40, v41, 3, v40\n" \
"v_add_u32 v240, 65536, v40\n" \
"v_xor_b32 v241, 32, v240\n" \
"v_and_b32 v42, 15, v251\n" \
"v_bfe_u32 v43, v251, 1, 3\n" \
"v_xor_b32 v43, v43, v8\n" \
"v_lshlrev_b32 v43, 4, v43\n" \
"v_lshl_or_b32 v43, v42, 7, v43\n" \
"v_add_u32 v242, s72, v43\n" \
"v_xor_b32 v244, 64, v242\n" \
"s_mov_b32 s76, 0x05040100\n" \
"s_mov_b32 s77, 0x07060302\n" \
"s_add_u32 m0, s70, 16384\n" \
"s_add_u32 s74, s69, 0\n" \
"buffer_load_dwordx4 v246, s[24:27], s74 offen lds\n" \
"s_add_u32 m0, s70, 17408\n" \
"s_add_u32 s74, s69, 128\n" \
"buffer_load_dwordx4 v246, s[24:27], s74 offen lds\n" \
"s_waitcnt vmcnt(14)\n" \
"v_lshlrev_b32 v44, 16, v16\n" \
"v_and_b32 v45, 0xffff0000, v16\n" \
"v_lshlrev_b32 v46, 16, v17\n" \
"v_and_b32 v47, 0xffff0000, v17\n" \
"v_mul_f32 v48, 0xbfb8aa3b, v44\n" \
"v_mul_f32 v49, 0xbfb8aa3b, v45\n" \
"v_mul_f32 v50, 0xbfb8aa3b, v46\n" \
"v_mul_f32 v51, 0xbfb8aa3b, v47\n" \
"v_exp_f32 v48, v48\n" \
"v_exp_f32 v49, v49\n" \
"v_exp_f32 v50, v50\n" \
"v_exp_f32 v51, v51\n" \
"v_add_f32 v48, 1.0, v48\n" \
"v_add_f32 v49, 1.0, v49\n" \
"v_add_f32 v50, 1.0, v50\n" \
"v_add_f32 v51, 1.0, v51\n" \
"v_rcp_f32 v48, v48\n" \
"v_rcp_f32 v49, v49\n" \
"v_rcp_f32 v50, v50\n" \
"v_rcp_f32 v51, v51\n" \
"v_mul_f32 v44, v44, v48\n" \
"v_mul_f32 v45, v45, v49\n" \
"v_mul_f32 v46, v46, v50\n" \
"v_mul_f32 v47, v47, v51\n" \
"v_cvt_pk_bf16_f32 v44, v44, v45\n" \
"v_cvt_pk_bf16_f32 v45, v46, v47\n" \
"s_add_u32 m0, s70, 24576\n" \
"s_add_u32 s74, s69, 262144\n" \
"buffer_load_dwordx4 v246, s[24:27], s74 offen lds\n" \
"v_perm_b32 v64, v24, v20, s76\n" \
"v_perm_b32 v65, v24, v20, s77\n" \
"v_perm_b32 v66, v25, v21, s76\n" \
"v_perm_b32 v67, v25, v21, s77\n" \
"v_perm_b32 v68, v26, v22, s76\n" \
"v_perm_b32 v69, v26, v22, s77\n" \
"v_perm_b32 v70, v27, v23, s76\n" \
"v_perm_b32 v71, v27, v23, s77\n" \
"v_dot2_f32_bf16 v52, v64, v44, 0\n" \
"v_dot2_f32_bf16 v53, v65, v44, 0\n" \
"v_dot2_f32_bf16 v54, v66, v44, 0\n" \
"v_dot2_f32_bf16 v55, v67, v44, 0\n" \
"v_dot2_f32_bf16 v56, v68, v44, 0\n" \
"v_dot2_f32_bf16 v57, v69, v44, 0\n" \
"v_dot2_f32_bf16 v58, v70, v44, 0\n" \
"v_dot2_f32_bf16 v59, v71, v44, 0\n" \
"s_add_u32 m0, s70, 25600\n" \
"s_add_u32 s74, s69, 262272\n" \
"buffer_load_dwordx4 v246, s[24:27], s74 offen lds\n" \
"v_perm_b32 v64, v32, v28, s76\n" \
"v_perm_b32 v65, v32, v28, s77\n" \
"v_perm_b32 v66, v33, v29, s76\n" \
"v_perm_b32 v67, v33, v29, s77\n" \
"v_perm_b32 v68, v34, v30, s76\n" \
"v_perm_b32 v69, v34, v30, s77\n" \
"v_perm_b32 v70, v35, v31, s76\n" \
"v_perm_b32 v71, v35, v31, s77\n" \
"v_dot2_f32_bf16 v52, v64, v45, v52\n" \
"v_dot2_f32_bf16 v53, v65, v45, v53\n" \
"v_dot2_f32_bf16 v54, v66, v45, v54\n" \
"v_dot2_f32_bf16 v55, v67, v45, v55\n" \
"v_dot2_f32_bf16 v56, v68, v45, v56\n" \
"v_dot2_f32_bf16 v57, v69, v45, v57\n" \
"v_dot2_f32_bf16 v58, v70, v45, v58\n" \
"v_dot2_f32_bf16 v59, v71, v45, v59\n" \
"buffer_load_dwordx2 v[202:203], v14, s[32:35], 0 offen sc1\n" \
"s_nop 2\n" \
"v_permlane32_swap_b32 v52, v56\n" \
"v_permlane32_swap_b32 v53, v57\n" \
"v_permlane32_swap_b32 v54, v58\n" \
"v_permlane32_swap_b32 v55, v59\n" \
"v_add_f32 v52, v52, v56\n" \
"v_add_f32 v53, v53, v57\n" \
"v_add_f32 v54, v54, v58\n" \
"v_add_f32 v55, v55, v59\n" \
"s_nop 0\n" \
"v_permlane16_swap_b32 v52, v54\n" \
"v_permlane16_swap_b32 v53, v55\n" \
"v_add_f32 v52, v52, v54\n" \
"v_add_f32 v53, v53, v55\n" \
"s_lshl_b32 s74, %[wid], 8\n" \
"s_add_u32 s74, s74, 147456\n" \
"v_lshl_add_u32 v60, v7, 5, s74\n" \
"v_lshl_add_u32 v60, v8, 3, v60\n" \
"v_lshlrev_b32 v231, 2, v251\n" \
"v_add_u32 v62, 147456, v231\n" \
"v_add_f32_dpp v52, v52, v52 row_ror:8 row_mask:0xf bank_mask:0xf\n" \
"v_add_f32_dpp v53, v53, v53 row_ror:8 row_mask:0xf bank_mask:0xf\n" \
"ds_write_b64 v60, v[52:53]\n" \
"buffer_load_dwordx4 v[128:131], v248, s[44:47], s75 offen\n" \
"buffer_load_dwordx4 v[132:135], v248, s[44:47], s75 offen offset:1024\n" \
"s_waitcnt vmcnt(7) lgkmcnt(0)\n" \
"s_barrier\n" \
"ds_read2st64_b32 v[232:233], v62 offset1:1\n" \
"ds_read2st64_b32 v[234:235], v62 offset0:2 offset1:3\n" \
"ds_read_b64_tr_b16 a[192:193], v240 offset:0\n" \
"ds_read_b64_tr_b16 a[194:195], v240 offset:2048\n" \
"ds_read_b64_tr_b16 a[196:197], v240 offset:8192\n" \
"ds_read_b64_tr_b16 a[198:199], v240 offset:10240\n" \
"ds_read_b64_tr_b16 a[200:201], v241 offset:0\n" \
"ds_read_b64_tr_b16 a[202:203], v241 offset:2048\n" \
"ds_read_b64_tr_b16 a[204:205], v241 offset:8192\n" \
"ds_read_b64_tr_b16 a[206:207], v241 offset:10240\n" \
"ds_read_b64_tr_b16 a[208:209], v240 offset:512\n" \
"ds_read_b64_tr_b16 a[210:211], v240 offset:2560\n" \
"ds_read_b64_tr_b16 a[212:213], v240 offset:8704\n" \
"ds_read_b64_tr_b16 a[214:215], v240 offset:10752\n" \
"ds_read_b128 a[128:131], v242 offset:0\n" \
"ds_read_b128 a[132:135], v242 offset:2048\n" \
"ds_read_b128 a[136:139], v242 offset:4096\n" \
"ds_read_b128 a[140:143], v242 offset:6144\n" \
"ds_read_b128 a[144:147], v244 offset:0\n" \
"ds_read_b128 a[148:151], v244 offset:2048\n" \
"ds_read_b128 a[152:155], v244 offset:4096\n" \
"ds_read_b128 a[156:159], v244 offset:6144\n" \
"s_waitcnt lgkmcnt(0)\n" \
  \
"v_mfma_f32_16x16x32_bf16 v[0:3], a[192:195], a[128:131], 0\n" \
"s_add_u32 m0, s72, 8192\n" \
"ds_read_b64_tr_b16 a[216:217], v241 offset:512\n" \
"v_add_f32 v232, v232, v233\n" \
"v_mfma_f32_16x16x32_bf16 v[0:3], a[196:199], a[144:147], v[0:3]\n" \
"s_add_u32 s74, s71, 128\n" \
"buffer_load_dwordx4 v249, s[16:19], s74 offen lds\n" \
"v_mfma_f32_16x16x32_bf16 v[32:35], a[196:199], a[148:151], 0\n" \
"s_add_u32 m0, s72, 9216\n" \
"ds_read_b64_tr_b16 a[218:219], v241 offset:2560\n" \
"v_add_f32 v234, v234, v235\n" \
"v_mfma_f32_16x16x32_bf16 v[32:35], a[192:195], a[132:135], v[32:35]\n" \
"s_add_u32 s74, s71, 8320\n" \
"v_add_f32 v232, v232, v234\n" \
"v_mfma_f32_16x16x32_bf16 v[64:67], a[192:195], a[136:139], 0\n" \
"buffer_load_dwordx4 v250, s[16:19], s74 offen lds\n" \
"v_mfma_f32_16x16x32_bf16 v[64:67], a[196:199], a[152:155], v[64:67]\n" \
"s_add_u32 m0, s72, 10240\n" \
"buffer_store_dword v232, v231, s[32:35], s81 offen sc1\n" \
"v_mov_b32 v238, 0\n" \
"v_mfma_f32_16x16x32_bf16 v[96:99], a[196:199], a[156:159], 0\n" \
"s_add_u32 s74, s71, 16512\n" \
"buffer_store_dwordx2 v[224:225], v228, s[32:35], 0 offen sc1\n" \
"v_mov_b32 v239, 1\n" \
"v_mfma_f32_16x16x32_bf16 v[96:99], a[192:195], a[140:143], v[96:99]\n" \
"ds_read_b64_tr_b16 a[220:221], v241 offset:8704\n" \
"ds_read_b64_tr_b16 a[222:223], v241 offset:10752\n" \
"v_mfma_f32_16x16x32_bf16 v[4:7], a[200:203], a[128:131], 0\n" \
"buffer_store_dword v230, v229, s[32:35], 0 offen sc1\n" \
"v_bfe_u32 v210, v251, 1, 3\n" \
"v_mfma_f32_16x16x32_bf16 v[4:7], a[204:207], a[144:147], v[4:7]\n" \
"ds_read_b64_tr_b16 a[192:193], v240 offset:1024\n" \
"v_lshlrev_b32 v210, 13, v210\n" \
"v_mfma_f32_16x16x32_bf16 v[36:39], a[204:207], a[148:151], 0\n" \
"ds_read_b64_tr_b16 a[194:195], v240 offset:3072\n" \
"v_and_b32 v208, 1, v251\n" \
"v_mfma_f32_16x16x32_bf16 v[36:39], a[200:203], a[132:135], v[36:39]\n" \
"buffer_load_dwordx4 v249, s[16:19], s74 offen lds\n" \
"v_mfma_f32_16x16x32_bf16 v[68:71], a[200:203], a[136:139], 0\n" \
"s_add_u32 m0, s72, 11264\n" \
"ds_read_b64_tr_b16 a[196:197], v240 offset:9216\n" \
"v_lshl_or_b32 v208, v208, 7, v210\n" \
"v_mfma_f32_16x16x32_bf16 v[68:71], a[204:207], a[152:155], v[68:71]\n" \
"s_add_u32 s74, s71, 24704\n" \
"v_and_b32 v209, 15, v251\n" \
"v_lshlrev_b32 v209, 7, v209\n" \
"v_mfma_f32_16x16x32_bf16 v[100:103], a[204:207], a[156:159], 0\n" \
"buffer_load_dwordx4 v250, s[16:19], s74 offen lds\n" \
"v_mfma_f32_16x16x32_bf16 v[100:103], a[200:203], a[140:143], v[100:103]\n" \
"s_add_u32 m0, s72, 12288\n" \
"ds_read_b64_tr_b16 a[198:199], v240 offset:11264\n" \
"v_and_b32 v211, 0x7fff, v208\n" \
"v_mfma_f32_16x16x32_bf16 v[8:11], a[208:211], a[128:131], 0\n" \
"s_add_u32 s74, s94, 32896\n" \
"ds_read_b64_tr_b16 a[200:201], v241 offset:1024\n" \
"v_mfma_f32_16x16x32_bf16 v[8:11], a[212:215], a[144:147], v[8:11]\n" \
"buffer_load_dwordx4 v249, s[16:19], s74 offen lds\n" \
"v_mfma_f32_16x16x32_bf16 v[40:43], a[212:215], a[148:151], 0\n" \
"s_add_u32 m0, s72, 13312\n" \
"s_add_u32 s74, s94, 41088\n" \
"ds_read_b64_tr_b16 a[202:203], v241 offset:3072\n" \
"v_mfma_f32_16x16x32_bf16 v[40:43], a[208:211], a[132:135], v[40:43]\n" \
"v_mfma_f32_16x16x32_bf16 v[72:75], a[208:211], a[136:139], 0\n" \
"buffer_load_dwordx4 v250, s[16:19], s74 offen lds\n" \
"v_mfma_f32_16x16x32_bf16 v[72:75], a[212:215], a[152:155], v[72:75]\n" \
"s_add_u32 m0, s72, 14336\n" \
"s_add_u32 s74, s94, 49280\n" \
"ds_read_b64_tr_b16 a[204:205], v241 offset:9216\n" \
"v_mfma_f32_16x16x32_bf16 v[104:107], a[212:215], a[156:159], 0\n" \
"ds_read_b64_tr_b16 a[206:207], v241 offset:11264\n" \
"v_mfma_f32_16x16x32_bf16 v[104:107], a[208:211], a[140:143], v[104:107]\n" \
"s_waitcnt lgkmcnt(8)\n" \
"buffer_load_dwordx4 v249, s[16:19], s74 offen lds\n" \
"v_mfma_f32_16x16x32_bf16 v[12:15], a[216:219], a[128:131], 0\n" \
"s_add_u32 m0, s72, 15360\n" \
"s_add_u32 s74, s94, 57472\n" \
"ds_read_b64_tr_b16 a[208:209], v240 offset:1536\n" \
"v_mfma_f32_16x16x32_bf16 v[12:15], a[220:223], a[144:147], v[12:15]\n" \
"v_mfma_f32_16x16x32_bf16 v[44:47], a[220:223], a[148:151], 0\n" \
"buffer_load_dwordx4 v250, s[16:19], s74 offen lds\n" \
"v_mfma_f32_16x16x32_bf16 v[44:47], a[216:219], a[132:135], v[44:47]\n" \
"s_add_u32 m0, s70, 32768\n" \
"s_add_u32 s74, s69, 524288\n" \
"ds_read_b64_tr_b16 a[210:211], v240 offset:3584\n" \
"v_mfma_f32_16x16x32_bf16 v[76:79], a[216:219], a[136:139], 0\n" \
"ds_read_b64_tr_b16 a[212:213], v240 offset:9728\n" \
"v_mfma_f32_16x16x32_bf16 v[76:79], a[220:223], a[152:155], v[76:79]\n" \
"buffer_load_dwordx4 v246, s[20:23], s74 offen lds\n" \
"v_mfma_f32_16x16x32_bf16 v[108:111], a[220:223], a[156:159], 0\n" \
"s_add_u32 m0, s70, 33792\n" \
"s_add_u32 s74, s69, 524416\n" \
"ds_read_b64_tr_b16 a[214:215], v240 offset:11776\n" \
"v_mfma_f32_16x16x32_bf16 v[108:111], a[216:219], a[140:143], v[108:111]\n" \
"s_waitcnt lgkmcnt(8)\n" \
"v_mfma_f32_16x16x32_bf16 v[16:19], a[192:195], a[128:131], 0\n" \
"buffer_load_dwordx4 v246, s[20:23], s74 offen lds\n" \
"v_mfma_f32_16x16x32_bf16 v[16:19], a[196:199], a[144:147], v[16:19]\n" \
"s_add_u32 m0, s70, 40960\n" \
"s_add_u32 s74, s69, 786432\n" \
"ds_read_b64_tr_b16 a[216:217], v241 offset:1536\n" \
"v_mfma_f32_16x16x32_bf16 v[48:51], a[196:199], a[148:151], 0\n" \
"ds_read_b64_tr_b16 a[218:219], v241 offset:3584\n" \
"v_mfma_f32_16x16x32_bf16 v[48:51], a[192:195], a[132:135], v[48:51]\n" \
"buffer_load_dwordx4 v246, s[20:23], s74 offen lds\n" \
"v_mfma_f32_16x16x32_bf16 v[80:83], a[192:195], a[136:139], 0\n" \
"s_add_u32 m0, s70, 41984\n" \
"ds_read_b64_tr_b16 a[220:221], v241 offset:9728\n" \
"ds_read_b64_tr_b16 a[222:223], v241 offset:11776\n" \
"v_mfma_f32_16x16x32_bf16 v[80:83], a[196:199], a[152:155], v[80:83]\n" \
"s_add_u32 s74, s69, 786560\n" \
"v_mfma_f32_16x16x32_bf16 v[112:115], a[196:199], a[156:159], 0\n" \
"buffer_load_dwordx4 v246, s[20:23], s74 offen lds\n" \
"v_mfma_f32_16x16x32_bf16 v[112:115], a[192:195], a[140:143], v[112:115]\n" \
"s_waitcnt lgkmcnt(8)\n" \
"s_add_u32 m0, s70, 49152\n" \
"s_add_u32 s74, s69, 524288\n" \
"v_mfma_f32_16x16x32_bf16 v[20:23], a[200:203], a[128:131], 0\n" \
"s_mov_b32 s77, 0x7f4a7c15\n" \
"v_mov_b32 v253, 0x9e3779b9\n" \
"v_mfma_f32_16x16x32_bf16 v[20:23], a[204:207], a[144:147], v[20:23]\n" \
"buffer_load_dwordx4 v246, s[24:27], s74 offen lds\n" \
"v_mfma_f32_16x16x32_bf16 v[52:55], a[204:207], a[148:151], 0\n" \
"s_waitcnt vmcnt(18)\n" \
"s_add_u32 m0, s70, 50176\n" \
"v_add_co_u32 v202, vcc, s77, v202\n" \
"v_mfma_f32_16x16x32_bf16 v[52:55], a[200:203], a[132:135], v[52:55]\n" \
"s_waitcnt vmcnt(16)\n" \
"s_add_u32 s74, s69, 524416\n" \
"v_lshlrev_b32 v136, 16, v128\n" \
"v_mfma_f32_16x16x32_bf16 v[84:87], a[200:203], a[136:139], 0\n" \
"s_waitcnt vmcnt(11) lgkmcnt(0)\n" \
"s_barrier\n" \
"v_addc_co_u32 v203, vcc, v253, v203, vcc\n" \
"v_mfma_f32_16x16x32_bf16 v[84:87], a[204:207], a[152:155], v[84:87]\n" \
"s_lshl_b32 s85, %[bid], 3\n" \
"buffer_load_dwordx4 v246, s[24:27], s74 offen lds\n" \
"v_mfma_f32_16x16x32_bf16 v[116:119], a[204:207], a[156:159], 0\n" \
"s_add_u32 m0, s70, 57344\n" \
"ds_read_b64_tr_b16 a[192:193], v240 offset:16384\n" \
"ds_read_b64_tr_b16 a[194:195], v240 offset:18432\n" \
"v_mfma_f32_16x16x32_bf16 v[116:119], a[200:203], a[140:143], v[116:119]\n" \
"s_add_u32 s74, s69, 786432\n" \
"ds_read_b64_tr_b16 a[196:197], v240 offset:24576\n" \
"ds_read_b64_tr_b16 a[198:199], v240 offset:26624\n" \
"v_mfma_f32_16x16x32_bf16 v[24:27], a[208:211], a[128:131], 0\n" \
"s_add_u32 s85, s85, 573440\n" \
"buffer_load_dwordx4 v246, s[24:27], s74 offen lds\n" \
"v_mfma_f32_16x16x32_bf16 v[24:27], a[212:215], a[144:147], v[24:27]\n" \
"s_add_u32 m0, s70, 58368\n" \
"s_cmp_eq_u32 %[wid], 0\n" \
"s_cselect_b32 s85, s85, 1073741824\n" \
"v_mfma_f32_16x16x32_bf16 v[56:59], a[212:215], a[148:151], 0\n" \
"s_add_u32 s74, s69, 786560\n" \
"ds_read_b64_tr_b16 a[200:201], v241 offset:16384\n" \
"ds_read_b64_tr_b16 a[202:203], v241 offset:18432\n" \
"v_mfma_f32_16x16x32_bf16 v[56:59], a[208:211], a[132:135], v[56:59]\n" \
"buffer_load_dwordx4 v246, s[24:27], s74 offen lds\n" \
"v_mfma_f32_16x16x32_bf16 v[88:91], a[208:211], a[136:139], 0\n" \
"s_add_u32 m0, s72, 0\n" \
"ds_read_b64_tr_b16 a[204:205], v241 offset:24576\n" \
"v_and_b32 v252, 7, v251\n" \
"v_mfma_f32_16x16x32_bf16 v[88:91], a[212:215], a[152:155], v[88:91]\n" \
"s_add_u32 s74, s71, 256\n" \
"v_lshlrev_b32 v252, 11, v252\n" \
"v_and_b32 v137, 0xffff0000, v128\n" \
"v_mfma_f32_16x16x32_bf16 v[120:123], a[212:215], a[156:159], 0\n" \
"buffer_load_dwordx4 v249, s[16:19], s74 offen lds\n" \
"v_mfma_f32_16x16x32_bf16 v[120:123], a[208:211], a[140:143], v[120:123]\n" \
"s_add_u32 m0, s72, 1024\n" \
"buffer_store_dwordx2 v[202:203], v252, s[32:35], s85 offen sc1\n" \
"v_lshrrev_b32 v253, 1, v248\n" \
"v_mfma_f32_16x16x32_bf16 v[28:31], a[216:219], a[128:131], 0\n" \
"s_add_u32 s74, s71, 8448\n" \
"ds_read_b64_tr_b16 a[206:207], v241 offset:26624\n" \
"ds_read_b64_tr_b16 a[208:209], v240 offset:16896\n" \
"v_mfma_f32_16x16x32_bf16 v[28:31], a[220:223], a[144:147], v[28:31]\n" \
"v_lshlrev_b32 v138, 16, v129\n" \
"v_and_b32 v139, 0xffff0000, v129\n" \
"v_mfma_f32_16x16x32_bf16 v[60:63], a[220:223], a[148:151], 0\n" \
"buffer_load_dwordx4 v250, s[16:19], s74 offen lds\n" \
"v_mfma_f32_16x16x32_bf16 v[60:63], a[216:219], a[132:135], v[60:63]\n" \
"s_add_u32 m0, s72, 2048\n" \
"ds_read_b64_tr_b16 a[210:211], v240 offset:18944\n" \
"v_lshlrev_b32 v140, 16, v130\n" \
"v_mfma_f32_16x16x32_bf16 v[92:95], a[216:219], a[136:139], 0\n" \
"s_add_u32 s74, s71, 16640\n" \
"ds_read_b64_tr_b16 a[212:213], v240 offset:25088\n" \
"v_and_b32 v141, 0xffff0000, v130\n" \
"v_mfma_f32_16x16x32_bf16 v[92:95], a[220:223], a[152:155], v[92:95]\n" \
"buffer_load_dwordx4 v249, s[16:19], s74 offen lds\n" \
"v_mfma_f32_16x16x32_bf16 v[124:127], a[220:223], a[156:159], 0\n" \
"s_add_u32 m0, s72, 3072\n" \
"ds_read_b64_tr_b16 a[214:215], v240 offset:27136\n" \
"v_lshlrev_b32 v142, 16, v131\n" \
"v_mfma_f32_16x16x32_bf16 v[124:127], a[216:219], a[140:143], v[124:127]\n" \
"s_waitcnt lgkmcnt(8)\n" \
"v_and_b32 v143, 0xffff0000, v131\n" \
"v_lshlrev_b32 v144, 16, v132\n" \
"v_mfma_f32_16x16x32_bf16 a[0:3], a[128:131], a[192:195], 0\n" \
"s_add_u32 s74, s71, 24832\n" \
"buffer_load_dwordx4 v250, s[16:19], s74 offen lds\n" \
"v_mfma_f32_16x16x32_bf16 a[0:3], a[144:147], a[196:199], a[0:3]\n" \
"s_add_u32 m0, s72, 4096\n" \
"ds_read_b64_tr_b16 a[216:217], v241 offset:16896\n" \
"v_and_b32 v145, 0xffff0000, v132\n" \
"v_mfma_f32_16x16x32_bf16 a[4:7], a[148:151], a[196:199], 0\n" \
"s_add_u32 s74, s94, 33024\n" \
"ds_read_b64_tr_b16 a[218:219], v241 offset:18944\n" \
"v_lshlrev_b32 v146, 16, v133\n" \
"v_mfma_f32_16x16x32_bf16 a[4:7], a[132:135], a[192:195], a[4:7]\n" \
"buffer_load_dwordx4 v249, s[16:19], s74 offen lds\n" \
"v_mfma_f32_16x16x32_bf16 a[64:67], a[136:139], a[192:195], 0\n" \
"s_add_u32 m0, s70, 0\n" \
"ds_read_b64_tr_b16 a[220:221], v241 offset:25088\n" \
"v_and_b32 v147, 0xffff0000, v133\n" \
"v_mfma_f32_16x16x32_bf16 a[64:67], a[152:155], a[196:199], a[64:67]\n" \
"s_add_u32 s74, s69, 1048576\n" \
"v_lshlrev_b32 v148, 16, v134\n" \
"v_and_b32 v149, 0xffff0000, v134\n" \
"v_mfma_f32_16x16x32_bf16 a[68:71], a[156:159], a[196:199], 0\n" \
"buffer_load_dwordx4 v246, s[20:23], s74 offen lds\n" \
"v_mfma_f32_16x16x32_bf16 a[68:71], a[140:143], a[192:195], a[68:71]\n" \
"s_waitcnt lgkmcnt(7)\n" \
"ds_read_b64_tr_b16 a[222:223], v241 offset:27136\n" \
"v_lshlrev_b32 v150, 16, v135\n" \
"v_mfma_f32_16x16x32_bf16 a[8:11], a[128:131], a[200:203], 0\n" \
"s_add_u32 m0, s72, 5120\n" \
"ds_read_b64_tr_b16 a[192:193], v240 offset:17408\n" \
"v_and_b32 v151, 0xffff0000, v135\n" \
"v_mfma_f32_16x16x32_bf16 a[8:11], a[144:147], a[204:207], a[8:11]\n" \
"s_add_u32 s74, s94, 41216\n" \
"buffer_load_dwordx4 v250, s[16:19], s74 offen lds\n" \
"v_mfma_f32_16x16x32_bf16 a[12:15], a[148:151], a[204:207], 0\n" \
"s_add_u32 m0, s72, 6144\n" \
"ds_read_b64_tr_b16 a[194:195], v240 offset:19456\n" \
"v_add_f32 v152, v136, v137\n" \
"v_mfma_f32_16x16x32_bf16 a[12:15], a[132:135], a[200:203], a[12:15]\n" \
"s_add_u32 s74, s94, 49408\n" \
"v_add_f32 v154, v138, v139\n" \
"v_add_f32 v153, v144, v145\n" \
"v_mfma_f32_16x16x32_bf16 a[72:75], a[136:139], a[200:203], 0\n" \
"buffer_load_dwordx4 v249, s[16:19], s74 offen lds\n" \
"v_mfma_f32_16x16x32_bf16 a[72:75], a[152:155], a[204:207], a[72:75]\n" \
"s_add_u32 m0, s70, 1024\n" \
"ds_read_b64_tr_b16 a[196:197], v240 offset:25600\n" \
"v_add_f32 v155, v146, v147\n" \
"v_mfma_f32_16x16x32_bf16 a[76:79], a[156:159], a[204:207], 0\n" \
"s_add_u32 s74, s69, 1048704\n" \
"ds_read_b64_tr_b16 a[198:199], v240 offset:27648\n" \
"v_add_f32 v152, v152, v140\n" \
"v_mfma_f32_16x16x32_bf16 a[76:79], a[140:143], a[200:203], a[76:79]\n" \
"s_waitcnt lgkmcnt(8)\n" \
"buffer_load_dwordx4 v246, s[20:23], s74 offen lds\n" \
"v_mfma_f32_16x16x32_bf16 a[16:19], a[128:131], a[208:211], 0\n" \
"s_add_u32 m0, s72, 7168\n" \
"ds_read_b64_tr_b16 a[200:201], v241 offset:17408\n" \
"v_add_f32 v154, v154, v141\n" \
"v_mfma_f32_16x16x32_bf16 a[16:19], a[144:147], a[212:215], a[16:19]\n" \
"s_add_u32 s74, s94, 57600\n" \
"v_add_f32 v153, v153, v148\n" \
"v_add_f32 v155, v155, v149\n" \
"v_mfma_f32_16x16x32_bf16 a[20:23], a[148:151], a[212:215], 0\n" \
"buffer_load_dwordx4 v250, s[16:19], s74 offen lds\n" \
"v_mfma_f32_16x16x32_bf16 a[20:23], a[132:135], a[208:211], a[20:23]\n" \
"s_add_u32 m0, s70, 8192\n" \
"ds_read_b64_tr_b16 a[202:203], v241 offset:19456\n" \
"v_add_f32 v152, v152, v142\n" \
"v_mfma_f32_16x16x32_bf16 a[80:83], a[136:139], a[208:211], 0\n" \
"s_add_u32 s74, s69, 1310720\n" \
"ds_read_b64_tr_b16 a[204:205], v241 offset:25600\n" \
"v_add_f32 v154, v154, v143\n" \
"v_mfma_f32_16x16x32_bf16 a[80:83], a[152:155], a[212:215], a[80:83]\n" \
"buffer_load_dwordx4 v246, s[20:23], s74 offen lds\n" \
"v_mfma_f32_16x16x32_bf16 a[84:87], a[156:159], a[212:215], 0\n" \
"s_add_u32 m0, s70, 9216\n" \
"ds_read_b64_tr_b16 a[206:207], v241 offset:27648\n" \
"v_add_f32 v153, v153, v150\n" \
"v_mfma_f32_16x16x32_bf16 a[84:87], a[140:143], a[208:211], a[84:87]\n" \
"s_waitcnt lgkmcnt(8)\n" \
"v_add_f32 v155, v155, v151\n" \
"v_add_f32 v152, v152, v154\n" \
"v_mfma_f32_16x16x32_bf16 a[24:27], a[128:131], a[216:219], 0\n" \
"s_add_u32 s74, s69, 1310848\n" \
"buffer_load_dwordx4 v246, s[20:23], s74 offen lds\n" \
"v_mfma_f32_16x16x32_bf16 a[24:27], a[144:147], a[220:223], a[24:27]\n" \
"ds_read_b64_tr_b16 a[208:209], v240 offset:17920\n" \
"v_add_f32 v153, v153, v155\n" \
"v_mfma_f32_16x16x32_bf16 a[28:31], a[148:151], a[220:223], 0\n" \
"ds_read_b64_tr_b16 a[210:211], v240 offset:19968\n" \
"v_add_f32_dpp v152, v152, v152 quad_perm:[1,0,3,2] row_mask:0xf bank_mask:0xf\n" \
"v_mfma_f32_16x16x32_bf16 a[28:31], a[132:135], a[216:219], a[28:31]\n" \
"v_add_f32_dpp v153, v153, v153 quad_perm:[1,0,3,2] row_mask:0xf bank_mask:0xf\n" \
"v_mfma_f32_16x16x32_bf16 a[88:91], a[136:139], a[216:219], 0\n" \
"ds_read_b64_tr_b16 a[212:213], v240 offset:26112\n" \
"v_add_f32_dpp v152, v152, v152 quad_perm:[2,3,0,1] row_mask:0xf bank_mask:0xf\n" \
"v_mfma_f32_16x16x32_bf16 a[88:91], a[152:155], a[220:223], a[88:91]\n" \
".Lst0_0_%=:\n" \
"s_cbranch_execz .Lst0_4_%=\n" \
"v_add_f32_dpp v153, v153, v153 quad_perm:[2,3,0,1] row_mask:0xf bank_mask:0xf\n" \
"v_mfma_f32_16x16x32_bf16 a[92:95], a[156:159], a[220:223], 0\n" \
"ds_read_b64_tr_b16 a[214:215], v240 offset:28160\n" \
"v_add_f32_dpp v152, v152, v152 row_half_mirror row_mask:0xf bank_mask:0xf\n" \
"v_mfma_f32_16x16x32_bf16 a[92:95], a[140:143], a[216:219], a[92:95]\n" \
"s_waitcnt lgkmcnt(8)\n" \
"v_add_f32_dpp v153, v153, v153 row_half_mirror row_mask:0xf bank_mask:0xf\n" \
"v_mfma_f32_16x16x32_bf16 a[32:35], a[128:131], a[192:195], 0\n" \
"s_waitcnt vmcnt(21)\n" \
"ds_read_b64_tr_b16 a[216:217], v241 offset:17920\n" \
"ds_read_b128 a[160:163], v242 offset:8192\n" \
"v_mfma_f32_16x16x32_bf16 a[32:35], a[144:147], a[196:199], a[32:35]\n" \
"v_add_f32_dpp v152, v152, v152 row_mirror row_mask:0xf bank_mask:0xf\n" \
"v_add_f32_dpp v153, v153, v153 row_mirror row_mask:0xf bank_mask:0xf\n" \
"v_mfma_f32_16x16x32_bf16 a[36:39], a[148:151], a[196:199], 0\n" \
"ds_read_b64_tr_b16 a[218:219], v241 offset:19968\n" \
"ds_read_b128 a[164:167], v242 offset:10240\n" \
"v_mfma_f32_16x16x32_bf16 a[36:39], a[132:135], a[192:195], a[36:39]\n" \
"ds_read_b64_tr_b16 a[220:221], v241 offset:26112\n" \
"v_mov_b32 v154, v152\n" \
"v_mfma_f32_16x16x32_bf16 a[96:99], a[136:139], a[192:195], 0\n" \
"ds_read_b64_tr_b16 a[222:223], v241 offset:28160\n" \
"ds_read_b128 a[168:171], v242 offset:12288\n" \
"v_mfma_f32_16x16x32_bf16 a[96:99], a[152:155], a[196:199], a[96:99]\n" \
"v_mov_b32 v155, v153\n" \
"v_mfma_f32_16x16x32_bf16 a[100:103], a[156:159], a[196:199], 0\n" \
"ds_read_b128 a[172:175], v242 offset:14336\n" \
"v_mfma_f32_16x16x32_bf16 a[100:103], a[140:143], a[192:195], a[100:103]\n" \
"s_waitcnt lgkmcnt(12)\n" \
"v_permlane16_swap_b32 v152, v154\n" \
"v_mfma_f32_16x16x32_bf16 a[40:43], a[128:131], a[200:203], 0\n" \
"v_permlane16_swap_b32 v153, v155\n" \
"v_mfma_f32_16x16x32_bf16 a[40:43], a[144:147], a[204:207], a[40:43]\n" \
"v_add_f32 v152, v152, v154\n" \
"v_add_f32 v153, v153, v155\n" \
"v_mfma_f32_16x16x32_bf16 a[44:47], a[148:151], a[204:207], 0\n" \
"v_mov_b32 v154, v152\n" \
"v_mov_b32 v155, v153\n" \
"v_mfma_f32_16x16x32_bf16 a[44:47], a[132:135], a[200:203], a[44:47]\n" \
"v_mfma_f32_16x16x32_bf16 a[104:107], a[136:139], a[200:203], 0\n" \
"s_waitcnt vmcnt(17) lgkmcnt(2)\n" \
"s_barrier\n" \
"ds_read_b128 a[176:179], v244 offset:8192\n" \
"v_mfma_f32_16x16x32_bf16 a[104:107], a[152:155], a[204:207], a[104:107]\n" \
"s_add_u32 m0, s70, 16384\n" \
"ds_read_b64_tr_b16 a[192:193], v240 offset:32768\n" \
"ds_read_b64_tr_b16 a[194:195], v240 offset:34816\n" \
"v_mfma_f32_16x16x32_bf16 a[108:111], a[156:159], a[204:207], 0\n" \
"s_add_u32 s74, s69, 1048576\n" \
"buffer_load_dwordx4 v246, s[24:27], s74 offen lds\n" \
"v_mfma_f32_16x16x32_bf16 a[108:111], a[140:143], a[200:203], a[108:111]\n" \
"ds_read_b64_tr_b16 a[196:197], v240 offset:40960\n" \
"ds_read_b64_tr_b16 a[198:199], v240 offset:43008\n" \
"v_mfma_f32_16x16x32_bf16 a[48:51], a[128:131], a[208:211], 0\n" \
"ds_read_b64_tr_b16 a[200:201], v241 offset:32768\n" \
"ds_read_b128 a[180:183], v244 offset:10240\n" \
"v_mfma_f32_16x16x32_bf16 a[48:51], a[144:147], a[212:215], a[48:51]\n" \
"ds_read_b128 a[184:187], v244 offset:12288\n" \
"v_mfma_f32_16x16x32_bf16 a[52:55], a[148:151], a[212:215], 0\n" \
"ds_read_b64_tr_b16 a[202:203], v241 offset:34816\n" \
"ds_read_b128 a[188:191], v244 offset:14336\n" \
"v_mfma_f32_16x16x32_bf16 a[52:55], a[132:135], a[208:211], a[52:55]\n" \
"v_permlane32_swap_b32 v152, v154\n" \
"v_mfma_f32_16x16x32_bf16 a[112:115], a[136:139], a[208:211], 0\n" \
"ds_read_b64_tr_b16 a[204:205], v241 offset:40960\n" \
"v_mfma_f32_16x16x32_bf16 a[112:115], a[152:155], a[212:215], a[112:115]\n" \
"v_permlane32_swap_b32 v153, v155\n" \
"v_mfma_f32_16x16x32_bf16 a[116:119], a[156:159], a[212:215], 0\n" \
"ds_read_b64_tr_b16 a[206:207], v241 offset:43008\n" \
"v_add_f32 v152, v152, v154\n" \
"v_mfma_f32_16x16x32_bf16 a[116:119], a[140:143], a[208:211], a[116:119]\n" \
"v_add_f32 v153, v153, v155\n" \
"v_mul_f32 v152, 0x3b000000, v152\n" \
"v_mfma_f32_16x16x32_bf16 a[56:59], a[128:131], a[216:219], 0\n" \
"ds_read_b64_tr_b16 a[208:209], v240 offset:33280\n" \
"v_mul_f32 v153, 0x3b000000, v153\n" \
"v_mfma_f32_16x16x32_bf16 a[56:59], a[144:147], a[220:223], a[56:59]\n" \
"v_sub_f32 v254, v136, v152\n" \
"v_sub_f32 v255, v144, v153\n" \
"v_mfma_f32_16x16x32_bf16 a[60:63], a[148:151], a[220:223], 0\n" \
"s_add_u32 m0, s70, 17408\n" \
"ds_read_b64_tr_b16 a[210:211], v240 offset:35328\n" \
"v_mul_f32 v156, v254, v254\n" \
"v_mfma_f32_16x16x32_bf16 a[60:63], a[132:135], a[216:219], a[60:63]\n" \
"s_add_u32 s74, s69, 1048704\n" \
"v_mul_f32 v157, v255, v255\n" \
"v_sub_f32 v254, v137, v152\n" \
"v_mfma_f32_16x16x32_bf16 a[120:123], a[136:139], a[216:219], 0\n" \
"buffer_load_dwordx4 v246, s[24:27], s74 offen lds\n" \
"v_mfma_f32_16x16x32_bf16 a[120:123], a[152:155], a[220:223], a[120:123]\n" \
"ds_read_b64_tr_b16 a[212:213], v240 offset:41472\n" \
"v_sub_f32 v255, v145, v153\n" \
"v_mfma_f32_16x16x32_bf16 a[124:127], a[156:159], a[220:223], 0\n" \
"s_waitcnt lgkmcnt(5)\n" \
"ds_read_b64_tr_b16 a[214:215], v240 offset:43520\n" \
"v_mfma_f32_16x16x32_bf16 a[124:127], a[140:143], a[216:219], a[124:127]\n" \
"s_add_u32 m0, s72, 8192\n" \
"v_mul_f32 v158, v254, v254\n" \
"v_mul_f32 v159, v255, v255\n" \
"v_mfma_f32_16x16x32_bf16 v[0:3], a[192:195], a[160:163], v[0:3]\n" \
"s_add_u32 s74, s71, 384\n" \
"buffer_load_dwordx4 v249, s[16:19], s74 offen lds\n" \
"v_mfma_f32_16x16x32_bf16 v[0:3], a[196:199], a[176:179], v[0:3]\n" \
"ds_read_b64_tr_b16 a[216:217], v241 offset:33280\n" \
"v_sub_f32 v254, v138, v152\n" \
"v_mfma_f32_16x16x32_bf16 v[32:35], a[196:199], a[180:183], v[32:35]\n" \
"ds_read_b64_tr_b16 a[218:219], v241 offset:35328\n" \
"v_sub_f32 v255, v146, v153\n" \
"v_mfma_f32_16x16x32_bf16 v[32:35], a[192:195], a[164:167], v[32:35]\n" \
"v_fmac_f32 v156, v254, v254\n" \
"v_fmac_f32 v157, v255, v255\n" \
"v_mfma_f32_16x16x32_bf16 v[64:67], a[192:195], a[168:171], v[64:67]\n" \
"s_add_u32 m0, s72, 9216\n" \
"ds_read_b64_tr_b16 a[220:221], v241 offset:41472\n" \
"v_sub_f32 v254, v139, v152\n" \
"v_mfma_f32_16x16x32_bf16 v[64:67], a[196:199], a[184:187], v[64:67]\n" \
"s_add_u32 s74, s71, 8576\n" \
"v_sub_f32 v255, v147, v153\n" \
"v_fmac_f32 v158, v254, v254\n" \
"v_mfma_f32_16x16x32_bf16 v[96:99], a[196:199], a[188:191], v[96:99]\n" \
"buffer_load_dwordx4 v250, s[16:19], s74 offen lds\n" \
"v_mfma_f32_16x16x32_bf16 v[96:99], a[192:195], a[172:175], v[96:99]\n" \
"s_waitcnt lgkmcnt(7)\n" \
"ds_read_b64_tr_b16 a[222:223], v241 offset:43520\n" \
"v_fmac_f32 v159, v255, v255\n" \
"v_mfma_f32_16x16x32_bf16 v[4:7], a[200:203], a[160:163], v[4:7]\n" \
"s_add_u32 m0, s70, 24576\n" \
"ds_read_b64_tr_b16 a[192:193], v240 offset:33792\n" \
"v_sub_f32 v254, v140, v152\n" \
"v_mfma_f32_16x16x32_bf16 v[4:7], a[204:207], a[176:179], v[4:7]\n" \
"s_add_u32 s74, s69, 1310720\n" \
"v_sub_f32 v255, v148, v153\n" \
"v_fmac_f32 v156, v254, v254\n" \
"v_mfma_f32_16x16x32_bf16 v[36:39], a[204:207], a[180:183], v[36:39]\n" \
"buffer_load_dwordx4 v246, s[24:27], s74 offen lds\n" \
"v_mfma_f32_16x16x32_bf16 v[36:39], a[200:203], a[164:167], v[36:39]\n" \
"s_add_u32 m0, s72, 10240\n" \
"ds_read_b64_tr_b16 a[194:195], v240 offset:35840\n" \
"v_fmac_f32 v157, v255, v255\n" \
"v_mfma_f32_16x16x32_bf16 v[68:71], a[200:203], a[168:171], v[68:71]\n" \
"s_add_u32 s74, s71, 16768\n" \
"ds_read_b64_tr_b16 a[196:197], v240 offset:41984\n" \
"v_sub_f32 v254, v141, v152\n" \
"v_mfma_f32_16x16x32_bf16 v[68:71], a[204:207], a[184:187], v[68:71]\n" \
"buffer_load_dwordx4 v249, s[16:19], s74 offen lds\n" \
"v_mfma_f32_16x16x32_bf16 v[100:103], a[204:207], a[188:191], v[100:103]\n" \
"ds_read_b64_tr_b16 a[198:199], v240 offset:44032\n" \
"v_sub_f32 v255, v149, v153\n" \
"v_mfma_f32_16x16x32_bf16 v[100:103], a[200:203], a[172:175], v[100:103]\n" \
"s_waitcnt lgkmcnt(8)\n" \
"v_fmac_f32 v158, v254, v254\n" \
"v_fmac_f32 v159, v255, v255\n" \
"v_mfma_f32_16x16x32_bf16 v[8:11], a[208:211], a[160:163], v[8:11]\n" \
"s_add_u32 m0, s72, 11264\n" \
"ds_read_b64_tr_b16 a[200:201], v241 offset:33792\n" \
"v_sub_f32 v254, v142, v152\n" \
"v_mfma_f32_16x16x32_bf16 v[8:11], a[212:215], a[176:179], v[8:11]\n" \
"s_add_u32 s74, s71, 24960\n" \
"v_sub_f32 v255, v150, v153\n" \
"v_fmac_f32 v156, v254, v254\n" \
"v_mfma_f32_16x16x32_bf16 v[40:43], a[212:215], a[180:183], v[40:43]\n" \
"s_lshl2_add_u32 s85, s67, %[wid]\n" \
"buffer_load_dwordx4 v250, s[16:19], s74 offen lds\n" \
"v_mfma_f32_16x16x32_bf16 v[40:43], a[208:211], a[164:167], v[40:43]\n" \
"s_lshl_b32 s85, s85, 16\n" \
"ds_read_b64_tr_b16 a[202:203], v241 offset:35840\n" \
"v_fmac_f32 v157, v255, v255\n" \
"v_mfma_f32_16x16x32_bf16 v[72:75], a[208:211], a[168:171], v[72:75]\n" \
"s_lshl_b32 s86, s68, 8\n" \
"ds_read_b64_tr_b16 a[204:205], v241 offset:41984\n" \
"v_sub_f32 v254, v143, v152\n" \
"v_mfma_f32_16x16x32_bf16 v[72:75], a[212:215], a[184:187], v[72:75]\n" \
"s_add_u32 s85, s85, s86\n" \
"v_sub_f32 v255, v151, v153\n" \
"v_fmac_f32 v158, v254, v254\n" \
"v_mfma_f32_16x16x32_bf16 v[104:107], a[212:215], a[188:191], v[104:107]\n" \
"s_add_u32 m0, s70, 25600\n" \
"ds_read_b64_tr_b16 a[206:207], v241 offset:44032\n" \
"v_fmac_f32 v159, v255, v255\n" \
"v_mfma_f32_16x16x32_bf16 v[104:107], a[208:211], a[172:175], v[104:107]\n" \
"s_waitcnt lgkmcnt(8)\n" \
"v_add_f32 v156, v156, v158\n" \
"v_add_f32 v157, v157, v159\n" \
"v_mfma_f32_16x16x32_bf16 v[12:15], a[216:219], a[160:163], v[12:15]\n" \
"s_add_u32 s74, s69, 1310848\n" \
"buffer_load_dwordx4 v246, s[24:27], s74 offen lds\n" \
"v_mfma_f32_16x16x32_bf16 v[12:15], a[220:223], a[176:179], v[12:15]\n" \
"s_add_u32 m0, s72, 12288\n" \
"ds_read_b64_tr_b16 a[208:209], v240 offset:34304\n" \
"v_add_f32_dpp v156, v156, v156 quad_perm:[1,0,3,2] row_mask:0xf bank_mask:0xf\n" \
"v_mfma_f32_16x16x32_bf16 v[44:47], a[220:223], a[180:183], v[44:47]\n" \
"s_add_u32 s74, s94, 33152\n" \
"ds_read_b64_tr_b16 a[210:211], v240 offset:36352\n" \
"v_add_f32_dpp v157, v157, v157 quad_perm:[1,0,3,2] row_mask:0xf bank_mask:0xf\n" \
"v_mfma_f32_16x16x32_bf16 v[44:47], a[216:219], a[164:167], v[44:47]\n" \
"s_add_u32 s85, s85, 2097152\n" \
"buffer_load_dwordx4 v249, s[16:19], s74 offen lds\n" \
"v_mfma_f32_16x16x32_bf16 v[76:79], a[216:219], a[168:171], v[76:79]\n" \
"s_add_u32 m0, s72, 13312\n" \
"buffer_load_dword v245, v208, s[20:23], s85 offen\n" \
"v_add_f32_dpp v156, v156, v156 quad_perm:[2,3,0,1] row_mask:0xf bank_mask:0xf\n" \
"v_mfma_f32_16x16x32_bf16 v[76:79], a[220:223], a[184:187], v[76:79]\n" \
"s_add_u32 s74, s94, 41344\n" \
"ds_read_b64_tr_b16 a[212:213], v240 offset:42496\n" \
"v_add_f32_dpp v157, v157, v157 quad_perm:[2,3,0,1] row_mask:0xf bank_mask:0xf\n" \
"v_mfma_f32_16x16x32_bf16 v[108:111], a[220:223], a[188:191], v[108:111]\n" \
"ds_read_b64_tr_b16 a[214:215], v240 offset:44544\n" \
"v_add_f32_dpp v156, v156, v156 row_half_mirror row_mask:0xf bank_mask:0xf\n" \
"v_mfma_f32_16x16x32_bf16 v[108:111], a[216:219], a[172:175], v[108:111]\n" \
"s_waitcnt lgkmcnt(8)\n" \
"buffer_load_dwordx4 v250, s[16:19], s74 offen lds\n" \
"v_mfma_f32_16x16x32_bf16 v[16:19], a[192:195], a[160:163], v[16:19]\n" \
"ds_read_b64_tr_b16 a[216:217], v241 offset:34304\n" \
"v_add_f32_dpp v157, v157, v157 row_half_mirror row_mask:0xf bank_mask:0xf\n" \
"v_mfma_f32_16x16x32_bf16 v[16:19], a[196:199], a[176:179], v[16:19]\n" \
"v_add_f32_dpp v156, v156, v156 row_mirror row_mask:0xf bank_mask:0xf\n" \
"v_mfma_f32_16x16x32_bf16 v[48:51], a[196:199], a[180:183], v[48:51]\n" \
"s_add_u32 m0, s72, 14336\n" \
"ds_read_b64_tr_b16 a[218:219], v241 offset:36352\n" \
"v_add_f32_dpp v157, v157, v157 row_mirror row_mask:0xf bank_mask:0xf\n" \
"v_mfma_f32_16x16x32_bf16 v[48:51], a[192:195], a[164:167], v[48:51]\n" \
"s_add_u32 s74, s94, 49536\n" \
"ds_read_b64_tr_b16 a[220:221], v241 offset:42496\n" \
"v_mov_b32 v158, v156\n" \
"v_mfma_f32_16x16x32_bf16 v[80:83], a[192:195], a[168:171], v[80:83]\n" \
"buffer_load_dwordx4 v249, s[16:19], s74 offen lds\n" \
"v_mfma_f32_16x16x32_bf16 v[80:83], a[196:199], a[184:187], v[80:83]\n" \
"ds_read_b64_tr_b16 a[222:223], v241 offset:44544\n" \
"v_mov_b32 v159, v157\n" \
"v_mfma_f32_16x16x32_bf16 v[112:115], a[196:199], a[188:191], v[112:115]\n" \
"v_permlane16_swap_b32 v156, v158\n" \
"v_mfma_f32_16x16x32_bf16 v[112:115], a[192:195], a[172:175], v[112:115]\n" \
"s_waitcnt lgkmcnt(8)\n" \
"v_permlane16_swap_b32 v157, v159\n" \
"v_mfma_f32_16x16x32_bf16 v[20:23], a[200:203], a[160:163], v[20:23]\n" \
"s_add_u32 m0, s72, 15360\n" \
"v_add_f32 v156, v156, v158\n" \
"v_add_f32 v157, v157, v159\n" \
"v_mfma_f32_16x16x32_bf16 v[20:23], a[204:207], a[176:179], v[20:23]\n" \
"s_add_u32 s74, s94, 57728\n" \
"v_mov_b32 v158, v156\n" \
"v_mov_b32 v159, v157\n" \
"v_mfma_f32_16x16x32_bf16 v[52:55], a[204:207], a[180:183], v[52:55]\n" \
"buffer_load_dwordx4 v250, s[16:19], s74 offen lds\n" \
"v_mfma_f32_16x16x32_bf16 v[52:55], a[200:203], a[164:167], v[52:55]\n" \
"v_permlane32_swap_b32 v156, v158\n" \
"v_mfma_f32_16x16x32_bf16 v[84:87], a[200:203], a[168:171], v[84:87]\n" \
"s_waitcnt vmcnt(26) lgkmcnt(0)\n" \
"s_barrier\n" \
"s_add_u32 m0, s70, 32768\n" \
"v_mfma_f32_16x16x32_bf16 v[84:87], a[204:207], a[184:187], v[84:87]\n" \
"s_add_u32 s74, s69, 1572864\n" \
"ds_read_b64_tr_b16 a[192:193], v240 offset:49152\n" \
"ds_read_b64_tr_b16 a[194:195], v240 offset:51200\n" \
"v_mfma_f32_16x16x32_bf16 v[116:119], a[204:207], a[188:191], v[116:119]\n" \
"buffer_load_dwordx4 v246, s[20:23], s74 offen lds\n" \
"v_mfma_f32_16x16x32_bf16 v[116:119], a[200:203], a[172:175], v[116:119]\n" \
"ds_read_b64_tr_b16 a[196:197], v240 offset:57344\n" \
"ds_read_b64_tr_b16 a[198:199], v240 offset:59392\n" \
"v_mfma_f32_16x16x32_bf16 v[24:27], a[208:211], a[160:163], v[24:27]\n" \
"ds_read_b64_tr_b16 a[200:201], v241 offset:49152\n" \
"v_mfma_f32_16x16x32_bf16 v[24:27], a[212:215], a[176:179], v[24:27]\n" \
"v_permlane32_swap_b32 v157, v159\n" \
"v_mfma_f32_16x16x32_bf16 v[56:59], a[212:215], a[180:183], v[56:59]\n" \
"ds_read_b64_tr_b16 a[202:203], v241 offset:51200\n" \
"v_add_f32 v156, v156, v158\n" \
"v_mfma_f32_16x16x32_bf16 v[56:59], a[208:211], a[164:167], v[56:59]\n" \
"v_add_f32 v157, v157, v159\n" \
"v_mul_f32 v156, 0x3b000000, v156\n" \
"v_mfma_f32_16x16x32_bf16 v[88:91], a[208:211], a[168:171], v[88:91]\n" \
"ds_read_b64_tr_b16 a[204:205], v241 offset:57344\n" \
"v_mul_f32 v157, 0x3b000000, v157\n" \
"v_mfma_f32_16x16x32_bf16 v[88:91], a[212:215], a[184:187], v[88:91]\n" \
"v_cvt_pk_bf16_f32 v254, v156, v157\n" \
"v_cvt_pk_bf16_f32 v255, v152, v153\n" \
"v_mfma_f32_16x16x32_bf16 v[120:123], a[212:215], a[188:191], v[120:123]\n" \
"ds_read_b64_tr_b16 a[206:207], v241 offset:59392\n" \
"v_lshlrev_b32 v156, 16, v254\n" \
"v_mfma_f32_16x16x32_bf16 v[120:123], a[208:211], a[172:175], v[120:123]\n" \
"v_and_b32 v157, 0xffff0000, v254\n" \
"v_lshlrev_b32 v152, 16, v255\n" \
"v_mfma_f32_16x16x32_bf16 v[28:31], a[216:219], a[160:163], v[28:31]\n" \
"ds_read_b64_tr_b16 a[208:209], v240 offset:49664\n" \
"v_and_b32 v153, 0xffff0000, v255\n" \
"v_mfma_f32_16x16x32_bf16 v[28:31], a[220:223], a[176:179], v[28:31]\n" \
"v_add_f32 v156, 0x3727c5ac, v156\n" \
"v_add_f32 v157, 0x3727c5ac, v157\n" \
"v_mfma_f32_16x16x32_bf16 v[60:63], a[220:223], a[180:183], v[60:63]\n" \
"s_add_u32 m0, s70, 33792\n" \
"ds_read_b64_tr_b16 a[210:211], v240 offset:51712\n" \
"v_cvt_pk_bf16_f32 v254, v156, v157\n" \
"v_mfma_f32_16x16x32_bf16 v[60:63], a[216:219], a[164:167], v[60:63]\n" \
"s_add_u32 s74, s69, 1572992\n" \
"v_lshlrev_b32 v156, 16, v254\n" \
"v_and_b32 v157, 0xffff0000, v254\n" \
"v_mfma_f32_16x16x32_bf16 v[92:95], a[216:219], a[168:171], v[92:95]\n" \
"buffer_load_dwordx4 v246, s[20:23], s74 offen lds\n" \
"v_mfma_f32_16x16x32_bf16 v[92:95], a[220:223], a[184:187], v[92:95]\n" \
"ds_read_b64_tr_b16 a[212:213], v240 offset:57856\n" \
"v_mfma_f32_16x16x32_bf16 v[124:127], a[220:223], a[188:191], v[124:127]\n" \
"s_waitcnt vmcnt(17)\n" \
"ds_read_b64_tr_b16 a[214:215], v240 offset:59904\n" \
"ds_read_b128 a[224:227], v242 offset:0\n" \
"v_mfma_f32_16x16x32_bf16 v[124:127], a[216:219], a[172:175], v[124:127]\n" \
"s_waitcnt lgkmcnt(9)\n" \
"v_sqrt_f32 v156, v156\n" \
"v_mfma_f32_16x16x32_bf16 a[0:3], a[160:163], a[192:195], a[0:3]\n" \
"ds_read_b64_tr_b16 a[216:217], v241 offset:49664\n" \
"ds_read_b128 a[228:231], v242 offset:2048\n" \
"v_mfma_f32_16x16x32_bf16 a[0:3], a[176:179], a[196:199], a[0:3]\n" \
"v_sqrt_f32 v157, v157\n" \
"v_mfma_f32_16x16x32_bf16 a[4:7], a[180:183], a[196:199], a[4:7]\n" \
"ds_read_b64_tr_b16 a[218:219], v241 offset:51712\n" \
"ds_read_b128 a[232:235], v242 offset:4096\n" \
"v_mfma_f32_16x16x32_bf16 a[4:7], a[164:167], a[192:195], a[4:7]\n" \
"v_cvt_pk_bf16_f32 v254, v156, v157\n" \
"v_lshlrev_b32 v156, 16, v254\n" \
"v_mfma_f32_16x16x32_bf16 a[64:67], a[168:171], a[192:195], a[64:67]\n" \
"ds_read_b64_tr_b16 a[220:221], v241 offset:57856\n" \
"ds_read_b128 a[236:239], v242 offset:6144\n" \
"v_mfma_f32_16x16x32_bf16 a[64:67], a[184:187], a[196:199], a[64:67]\n" \
"v_and_b32 v157, 0xffff0000, v254\n" \
"v_mfma_f32_16x16x32_bf16 a[68:71], a[188:191], a[196:199], a[68:71]\n" \
"ds_read_b64_tr_b16 a[222:223], v241 offset:59904\n" \
"v_mfma_f32_16x16x32_bf16 a[68:71], a[172:175], a[192:195], a[68:71]\n" \
"s_waitcnt lgkmcnt(12)\n" \
"v_rcp_f32 v156, v156\n" \
"v_mfma_f32_16x16x32_bf16 a[8:11], a[160:163], a[200:203], a[8:11]\n" \
"s_add_u32 m0, s70, 40960\n" \
"ds_read_b64_tr_b16 a[192:193], v240 offset:50176\n" \
"v_mfma_f32_16x16x32_bf16 a[8:11], a[176:179], a[204:207], a[8:11]\n" \
"s_add_u32 s74, s69, 1835008\n" \
"v_rcp_f32 v157, v157\n" \
"v_mfma_f32_16x16x32_bf16 a[12:15], a[180:183], a[204:207], a[12:15]\n" \
"buffer_load_dwordx4 v246, s[20:23], s74 offen lds\n" \
"v_mfma_f32_16x16x32_bf16 a[12:15], a[164:167], a[200:203], a[12:15]\n" \
"ds_read_b64_tr_b16 a[194:195], v240 offset:52224\n" \
"ds_read_b128 a[240:243], v244 offset:0\n" \
"v_mfma_f32_16x16x32_bf16 a[72:75], a[168:171], a[200:203], a[72:75]\n" \
"ds_read_b64_tr_b16 a[196:197], v240 offset:58368\n" \
"ds_read_b128 a[244:247], v244 offset:2048\n" \
"v_mfma_f32_16x16x32_bf16 a[72:75], a[184:187], a[204:207], a[72:75]\n" \
"v_sub_f32 v136, v136, v152\n" \
"v_sub_f32 v137, v137, v152\n" \
"v_mfma_f32_16x16x32_bf16 a[76:79], a[188:191], a[204:207], a[76:79]\n" \
"ds_read_b64_tr_b16 a[198:199], v240 offset:60416\n" \
"ds_read_b128 a[248:251], v244 offset:4096\n" \
"v_mfma_f32_16x16x32_bf16 a[76:79], a[172:175], a[200:203], a[76:79]\n" \
"s_waitcnt lgkmcnt(15)\n" \
"v_cvt_pk_bf16_f32 v128, v136, v137\n" \
"v_lshlrev_b32 v136, 16, v128\n" \
"v_mfma_f32_16x16x32_bf16 a[16:19], a[160:163], a[208:211], a[16:19]\n" \
"s_lshl2_add_u32 s81, s64, %[wid]\n" \
"ds_read_b64_tr_b16 a[200:201], v241 offset:50176\n" \
"ds_read_b128 a[252:255], v244 offset:6144\n" \
"v_mfma_f32_16x16x32_bf16 a[16:19], a[176:179], a[212:215], a[16:19]\n" \
"s_lshl_b32 s81, s81, 9\n" \
"v_and_b32 v137, 0xffff0000, v128\n" \
"v_mul_f32 v136, v136, v156\n" \
"v_mfma_f32_16x16x32_bf16 a[20:23], a[180:183], a[212:215], a[20:23]\n" \
"s_add_u32 s81, s81, 573440\n" \
"buffer_load_dwordx2 v[204:205], v253, s[32:35], s81 offen sc1\n" \
"v_mul_f32 v137, v137, v156\n" \
"v_mfma_f32_16x16x32_bf16 a[20:23], a[164:167], a[208:211], a[20:23]\n" \
"ds_read_b64_tr_b16 a[202:203], v241 offset:52224\n" \
"v_cvt_pk_bf16_f32 v128, v136, v137\n" \
"v_mfma_f32_16x16x32_bf16 a[80:83], a[168:171], a[208:211], a[80:83]\n" \
"ds_read_b64_tr_b16 a[204:205], v241 offset:58368\n" \
"v_sub_f32 v138, v138, v152\n" \
"v_mfma_f32_16x16x32_bf16 a[80:83], a[184:187], a[212:215], a[80:83]\n" \
"v_sub_f32 v139, v139, v152\n" \
"v_cvt_pk_bf16_f32 v129, v138, v139\n" \
"v_mfma_f32_16x16x32_bf16 a[84:87], a[188:191], a[212:215], a[84:87]\n" \
"s_add_u32 m0, s70, 41984\n" \
"ds_read_b64_tr_b16 a[206:207], v241 offset:60416\n" \
"v_lshlrev_b32 v138, 16, v129\n" \
"v_mfma_f32_16x16x32_bf16 a[84:87], a[172:175], a[208:211], a[84:87]\n" \
"s_waitcnt lgkmcnt(12)\n" \
"v_and_b32 v139, 0xffff0000, v129\n" \
"v_mul_f32 v138, v138, v156\n" \
"v_mfma_f32_16x16x32_bf16 a[24:27], a[160:163], a[216:219], a[24:27]\n" \
"s_add_u32 s74, s69, 1835136\n" \
"buffer_load_dwordx4 v246, s[20:23], s74 offen lds\n" \
"v_mfma_f32_16x16x32_bf16 a[24:27], a[176:179], a[220:223], a[24:27]\n" \
"ds_read_b64_tr_b16 a[208:209], v240 offset:50688\n" \
"v_mul_f32 v139, v139, v156\n" \
"v_mfma_f32_16x16x32_bf16 a[28:31], a[180:183], a[220:223], a[28:31]\n" \
"ds_read_b64_tr_b16 a[210:211], v240 offset:52736\n" \
"v_cvt_pk_bf16_f32 v129, v138, v139\n" \
"v_mfma_f32_16x16x32_bf16 a[28:31], a[164:167], a[216:219], a[28:31]\n" \
"v_sub_f32 v140, v140, v152\n" \
"v_sub_f32 v141, v141, v152\n" \
"v_mfma_f32_16x16x32_bf16 a[88:91], a[168:171], a[216:219], a[88:91]\n" \
"s_add_u32 m0, s72, 0\n" \
"ds_read_b64_tr_b16 a[212:213], v240 offset:58880\n" \
"v_cvt_pk_bf16_f32 v130, v140, v141\n" \
"v_mfma_f32_16x16x32_bf16 a[88:91], a[184:187], a[220:223], a[88:91]\n" \
"s_add_u32 s74, s71, 512\n" \
"v_lshlrev_b32 v140, 16, v130\n" \
"v_and_b32 v141, 0xffff0000, v130\n" \
"v_mfma_f32_16x16x32_bf16 a[92:95], a[188:191], a[220:223], a[92:95]\n" \
"s_waitcnt lgkmcnt(6)\n" \
"buffer_load_dwordx4 v249, s[16:19], s74 offen lds\n" \
"v_mfma_f32_16x16x32_bf16 a[92:95], a[172:175], a[216:219], a[92:95]\n" \
"ds_read_b64_tr_b16 a[214:215], v240 offset:60928\n" \
"v_mul_f32 v140, v140, v156\n" \
"v_mfma_f32_16x16x32_bf16 a[32:35], a[160:163], a[192:195], a[32:35]\n" \
"ds_read_b64_tr_b16 a[216:217], v241 offset:50688\n" \
"v_mul_f32 v141, v141, v156\n" \
"v_mfma_f32_16x16x32_bf16 a[32:35], a[176:179], a[196:199], a[32:35]\n" \
"s_lshl2_add_u32 s85, s67, %[wid]\n" \
"v_cvt_pk_bf16_f32 v130, v140, v141\n" \
"v_sub_f32 v142, v142, v152\n" \
"v_mfma_f32_16x16x32_bf16 a[36:39], a[180:183], a[196:199], a[36:39]\n" \
"s_add_u32 m0, s72, 1024\n" \
"ds_read_b64_tr_b16 a[218:219], v241 offset:52736\n" \
"v_sub_f32 v143, v143, v152\n" \
"v_mfma_f32_16x16x32_bf16 a[36:39], a[164:167], a[192:195], a[36:39]\n" \
"s_add_u32 s74, s71, 8704\n" \
"ds_read_b64_tr_b16 a[220:221], v241 offset:58880\n" \
"v_cvt_pk_bf16_f32 v131, v142, v143\n" \
"v_mfma_f32_16x16x32_bf16 a[96:99], a[168:171], a[192:195], a[96:99]\n" \
"s_lshl_b32 s85, s85, 16\n" \
"buffer_load_dwordx4 v250, s[16:19], s74 offen lds\n" \
"v_mfma_f32_16x16x32_bf16 a[96:99], a[184:187], a[196:199], a[96:99]\n" \
"s_lshl_b32 s86, s68, 8\n" \
"ds_read_b64_tr_b16 a[222:223], v241 offset:60928\n" \
"v_lshlrev_b32 v142, 16, v131\n" \
"v_mfma_f32_16x16x32_bf16 a[100:103], a[188:191], a[196:199], a[100:103]\n" \
"s_add_u32 s85, s85, s86\n" \
"v_and_b32 v143, 0xffff0000, v131\n" \
"v_mul_f32 v142, v142, v156\n" \
"v_mfma_f32_16x16x32_bf16 a[100:103], a[172:175], a[192:195], a[100:103]\n" \
"s_waitcnt lgkmcnt(8)\n" \
"v_mul_f32 v143, v143, v156\n" \
"v_cvt_pk_bf16_f32 v131, v142, v143\n" \
"v_mfma_f32_16x16x32_bf16 a[40:43], a[160:163], a[200:203], a[40:43]\n" \
"s_add_u32 m0, s72, 2048\n" \
"v_sub_f32 v144, v144, v153\n" \
"v_sub_f32 v145, v145, v153\n" \
"v_mfma_f32_16x16x32_bf16 a[40:43], a[176:179], a[204:207], a[40:43]\n" \
"s_add_u32 s74, s71, 16896\n" \
"v_cvt_pk_bf16_f32 v132, v144, v145\n" \
"v_lshlrev_b32 v144, 16, v132\n" \
"v_mfma_f32_16x16x32_bf16 a[44:47], a[180:183], a[204:207], a[44:47]\n" \
"s_add_u32 s85, s85, 2097152\n" \
"buffer_load_dwordx4 v249, s[16:19], s74 offen lds\n" \
"v_mfma_f32_16x16x32_bf16 a[44:47], a[164:167], a[200:203], a[44:47]\n" \
"buffer_load_dword v245, v208, s[24:27], s85 offen\n" \
"v_and_b32 v145, 0xffff0000, v132\n" \
"v_mfma_f32_16x16x32_bf16 a[104:107], a[168:171], a[200:203], a[104:107]\n" \
"s_waitcnt vmcnt(22) lgkmcnt(0)\n" \
"s_barrier\n" \
"v_mul_f32 v144, v144, v157\n" \
"v_mfma_f32_16x16x32_bf16 a[104:107], a[184:187], a[204:207], a[104:107]\n" \
"s_add_u32 m0, s70, 49152\n" \
"ds_read_b64_tr_b16 a[192:193], v240 offset:0\n" \
"ds_read_b64_tr_b16 a[194:195], v240 offset:2048\n" \
"v_mfma_f32_16x16x32_bf16 a[108:111], a[188:191], a[204:207], a[108:111]\n" \
"s_add_u32 s74, s69, 1572864\n" \
"buffer_load_dwordx4 v246, s[24:27], s74 offen lds\n" \
"v_mfma_f32_16x16x32_bf16 a[108:111], a[172:175], a[200:203], a[108:111]\n" \
"s_add_u32 m0, s72, 3072\n" \
"ds_read_b64_tr_b16 a[196:197], v240 offset:8192\n" \
"ds_read_b64_tr_b16 a[198:199], v240 offset:10240\n" \
"v_mfma_f32_16x16x32_bf16 a[48:51], a[160:163], a[208:211], a[48:51]\n" \
"s_add_u32 s74, s71, 25088\n" \
"ds_read_b64_tr_b16 a[200:201], v241 offset:0\n" \
"v_mul_f32 v145, v145, v157\n" \
"v_mfma_f32_16x16x32_bf16 a[48:51], a[176:179], a[212:215], a[48:51]\n" \
"buffer_load_dwordx4 v250, s[16:19], s74 offen lds\n" \
"v_mfma_f32_16x16x32_bf16 a[52:55], a[180:183], a[212:215], a[52:55]\n" \
"ds_read_b64_tr_b16 a[202:203], v241 offset:2048\n" \
"v_cvt_pk_bf16_f32 v132, v144, v145\n" \
"v_mfma_f32_16x16x32_bf16 a[52:55], a[164:167], a[208:211], a[52:55]\n" \
"v_sub_f32 v146, v146, v153\n" \
"v_sub_f32 v147, v147, v153\n" \
"v_mfma_f32_16x16x32_bf16 a[112:115], a[168:171], a[208:211], a[112:115]\n" \
"s_add_u32 m0, s72, 4096\n" \
"ds_read_b64_tr_b16 a[204:205], v241 offset:8192\n" \
"v_cvt_pk_bf16_f32 v133, v146, v147\n" \
"v_mfma_f32_16x16x32_bf16 a[112:115], a[184:187], a[212:215], a[112:115]\n" \
"s_add_u32 s74, s94, 33280\n" \
"v_lshlrev_b32 v146, 16, v133\n" \
"v_and_b32 v147, 0xffff0000, v133\n" \
"v_mfma_f32_16x16x32_bf16 a[116:119], a[188:191], a[212:215], a[116:119]\n" \
"buffer_load_dwordx4 v249, s[16:19], s74 offen lds\n" \
"v_mfma_f32_16x16x32_bf16 a[116:119], a[172:175], a[208:211], a[116:119]\n" \
"ds_read_b64_tr_b16 a[206:207], v241 offset:10240\n" \
"v_mul_f32 v146, v146, v157\n" \
"v_mfma_f32_16x16x32_bf16 a[56:59], a[160:163], a[216:219], a[56:59]\n" \
"ds_read_b64_tr_b16 a[208:209], v240 offset:512\n" \
"v_mul_f32 v147, v147, v157\n" \
"v_mfma_f32_16x16x32_bf16 a[56:59], a[176:179], a[220:223], a[56:59]\n" \
"v_cvt_pk_bf16_f32 v133, v146, v147\n" \
"v_sub_f32 v148, v148, v153\n" \
"v_mfma_f32_16x16x32_bf16 a[60:63], a[180:183], a[220:223], a[60:63]\n" \
"s_add_u32 m0, s70, 50176\n" \
"ds_read_b64_tr_b16 a[210:211], v240 offset:2560\n" \
"v_sub_f32 v149, v149, v153\n" \
"v_mfma_f32_16x16x32_bf16 a[60:63], a[164:167], a[216:219], a[60:63]\n" \
"s_add_u32 s74, s69, 1572992\n" \
"v_cvt_pk_bf16_f32 v134, v148, v149\n" \
"v_lshlrev_b32 v148, 16, v134\n" \
"v_mfma_f32_16x16x32_bf16 a[120:123], a[168:171], a[216:219], a[120:123]\n" \
"buffer_load_dwordx4 v246, s[24:27], s74 offen lds\n" \
"v_mfma_f32_16x16x32_bf16 a[120:123], a[184:187], a[220:223], a[120:123]\n" \
"s_add_u32 m0, s72, 5120\n" \
"ds_read_b64_tr_b16 a[212:213], v240 offset:8704\n" \
"v_and_b32 v149, 0xffff0000, v134\n" \
"v_mfma_f32_16x16x32_bf16 a[124:127], a[188:191], a[220:223], a[124:127]\n" \
"s_add_u32 s74, s94, 41472\n" \
"ds_read_b64_tr_b16 a[214:215], v240 offset:10752\n" \
"v_mul_f32 v148, v148, v157\n" \
"v_mfma_f32_16x16x32_bf16 a[124:127], a[172:175], a[216:219], a[124:127]\n" \
"s_waitcnt lgkmcnt(8)\n" \
"buffer_load_dwordx4 v250, s[16:19], s74 offen lds\n" \
"v_mfma_f32_16x16x32_bf16 v[0:3], a[192:195], a[224:227], v[0:3]\n" \
"s_add_u32 m0, s72, 6144\n" \
"ds_read_b64_tr_b16 a[216:217], v241 offset:512\n" \
"v_mul_f32 v149, v149, v157\n" \
"v_mfma_f32_16x16x32_bf16 v[0:3], a[196:199], a[240:243], v[0:3]\n" \
"s_add_u32 s74, s94, 49664\n" \
"v_cvt_pk_bf16_f32 v134, v148, v149\n" \
"v_sub_f32 v150, v150, v153\n" \
"v_mfma_f32_16x16x32_bf16 v[32:35], a[196:199], a[244:247], v[32:35]\n" \
"buffer_load_dwordx4 v249, s[16:19], s74 offen lds\n" \
"v_mfma_f32_16x16x32_bf16 v[32:35], a[192:195], a[228:231], v[32:35]\n" \
"ds_read_b64_tr_b16 a[218:219], v241 offset:2560\n" \
"v_sub_f32 v151, v151, v153\n" \
"v_mfma_f32_16x16x32_bf16 v[64:67], a[192:195], a[232:235], v[64:67]\n" \
"ds_read_b64_tr_b16 a[220:221], v241 offset:8704\n" \
"v_cvt_pk_bf16_f32 v135, v150, v151\n" \
"v_mfma_f32_16x16x32_bf16 v[64:67], a[196:199], a[248:251], v[64:67]\n" \
"v_lshlrev_b32 v150, 16, v135\n" \
"v_and_b32 v151, 0xffff0000, v135\n" \
"v_mfma_f32_16x16x32_bf16 v[96:99], a[196:199], a[252:255], v[96:99]\n" \
"s_add_u32 m0, s72, 7168\n" \
"ds_read_b64_tr_b16 a[222:223], v241 offset:10752\n" \
"v_mul_f32 v150, v150, v157\n" \
"v_mfma_f32_16x16x32_bf16 v[96:99], a[192:195], a[236:239], v[96:99]\n" \
"s_waitcnt lgkmcnt(8)\n" \
"v_mul_f32 v151, v151, v157\n" \
"v_cvt_pk_bf16_f32 v135, v150, v151\n" \
"v_mfma_f32_16x16x32_bf16 v[4:7], a[200:203], a[224:227], v[4:7]\n" \
"s_add_u32 s74, s94, 57856\n" \
"buffer_load_dwordx4 v250, s[16:19], s74 offen lds\n" \
"v_mfma_f32_16x16x32_bf16 v[4:7], a[204:207], a[240:243], v[4:7]\n" \
"s_add_u32 m0, s70, 57344\n" \
"s_add_u32 s74, s69, 1835008\n" \
"ds_read_b64_tr_b16 a[192:193], v240 offset:1024\n" \
"v_mfma_f32_16x16x32_bf16 v[36:39], a[204:207], a[244:247], v[36:39]\n" \
"s_mul_i32 s76, %[wid], 2080\n" \
"s_add_u32 s76, s76, 149504\n" \
"ds_read_b64_tr_b16 a[194:195], v240 offset:3072\n" \
"v_mfma_f32_16x16x32_bf16 v[36:39], a[200:203], a[228:231], v[36:39]\n" \
"buffer_load_dwordx4 v246, s[24:27], s74 offen lds\n" \
"v_mfma_f32_16x16x32_bf16 v[68:71], a[200:203], a[232:235], v[68:71]\n" \
"ds_read_b64_tr_b16 a[196:197], v240 offset:9216\n" \
"v_add_u32 v254, s76, v248\n" \
"v_mfma_f32_16x16x32_bf16 v[68:71], a[204:207], a[248:251], v[68:71]\n" \
"ds_write_b128 v254, v[128:131]\n" \
"v_mfma_f32_16x16x32_bf16 v[100:103], a[204:207], a[252:255], v[100:103]\n" \
"ds_read_b64_tr_b16 a[198:199], v240 offset:11264\n" \
"v_mfma_f32_16x16x32_bf16 v[100:103], a[200:203], a[236:239], v[100:103]\n" \
"s_waitcnt lgkmcnt(9)\n" \
"ds_write_b128 v254, v[132:135] offset:1040\n" \
"v_and_b32 v254, 7, v251\n" \
"v_mfma_f32_16x16x32_bf16 v[8:11], a[208:211], a[224:227], v[8:11]\n" \
"ds_read_b64_tr_b16 a[200:201], v241 offset:1024\n" \
"v_mul_u32_u24 v254, 1040, v254\n" \
"v_mfma_f32_16x16x32_bf16 v[8:11], a[212:215], a[240:243], v[8:11]\n" \
"v_lshrrev_b32 v255, 3, v251\n" \
"v_lshl_add_u32 v254, v255, 4, v254\n" \
"v_mfma_f32_16x16x32_bf16 v[40:43], a[212:215], a[244:247], v[40:43]\n" \
"s_lshl_b32 s76, %[wid], 7\n" \
"s_add_u32 s76, s76, 149504\n" \
"ds_read_b64_tr_b16 a[202:203], v241 offset:3072\n" \
"v_mfma_f32_16x16x32_bf16 v[40:43], a[208:211], a[228:231], v[40:43]\n" \
"v_add_u32 v254, s76, v254\n" \
"v_mfma_f32_16x16x32_bf16 v[72:75], a[208:211], a[232:235], v[72:75]\n" \
"ds_read_b64_tr_b16 a[204:205], v241 offset:9216\n" \
"v_mfma_f32_16x16x32_bf16 v[72:75], a[212:215], a[248:251], v[72:75]\n" \
"v_mfma_f32_16x16x32_bf16 v[104:107], a[212:215], a[252:255], v[104:107]\n" \
"s_add_u32 m0, s70, 58368\n" \
"ds_read_b64_tr_b16 a[206:207], v241 offset:11264\n" \
"v_mfma_f32_16x16x32_bf16 v[104:107], a[208:211], a[236:239], v[104:107]\n" \
".Lst0_1_%=:\n" \
"s_cbranch_execz .Lst0_5_%=\n" \
"s_waitcnt lgkmcnt(10)\n" \
"s_add_u32 s74, s69, 1835136\n" \
"v_mfma_f32_16x16x32_bf16 v[12:15], a[216:219], a[224:227], v[12:15]\n" \
"buffer_load_dwordx4 v246, s[24:27], s74 offen lds\n" \
"v_mfma_f32_16x16x32_bf16 v[12:15], a[220:223], a[240:243], v[12:15]\n" \
"ds_read_b64_tr_b16 a[208:209], v240 offset:1536\n" \
"v_mfma_f32_16x16x32_bf16 v[44:47], a[220:223], a[244:247], v[44:47]\n" \
"ds_read_b64_tr_b16 a[210:211], v240 offset:3584\n" \
"v_mfma_f32_16x16x32_bf16 v[44:47], a[216:219], a[228:231], v[44:47]\n" \
"v_mfma_f32_16x16x32_bf16 v[76:79], a[216:219], a[232:235], v[76:79]\n" \
"ds_read_b64_tr_b16 a[212:213], v240 offset:9728\n" \
"v_mfma_f32_16x16x32_bf16 v[76:79], a[220:223], a[248:251], v[76:79]\n" \
"v_mfma_f32_16x16x32_bf16 v[108:111], a[220:223], a[252:255], v[108:111]\n" \
"ds_read_b64_tr_b16 a[214:215], v240 offset:11776\n" \
"v_mfma_f32_16x16x32_bf16 v[108:111], a[216:219], a[236:239], v[108:111]\n" \
"s_waitcnt lgkmcnt(9)\n" \
"v_mfma_f32_16x16x32_bf16 v[16:19], a[192:195], a[224:227], v[16:19]\n" \
"s_waitcnt vmcnt(14)\n" \
"ds_read_b64_tr_b16 a[216:217], v241 offset:1536\n" \
"v_cmp_ne_u64 vcc, v[204:205], v[202:203]\n" \
"v_mfma_f32_16x16x32_bf16 v[16:19], a[196:199], a[240:243], v[16:19]\n" \
"v_mfma_f32_16x16x32_bf16 v[48:51], a[196:199], a[244:247], v[48:51]\n" \
"ds_read_b64_tr_b16 a[218:219], v241 offset:3584\n" \
"v_mfma_f32_16x16x32_bf16 v[48:51], a[192:195], a[228:231], v[48:51]\n" \
"s_cbranch_vccnz .Lspq_%=\n" \
".Lsbq_%=:\n" \
"ds_read_b64_tr_b16 a[220:221], v241 offset:9728\n" \
"v_mfma_f32_16x16x32_bf16 v[80:83], a[192:195], a[232:235], v[80:83]\n" \
"ds_read_b64_tr_b16 a[222:223], v241 offset:11776\n" \
"v_mfma_f32_16x16x32_bf16 v[80:83], a[196:199], a[248:251], v[80:83]\n" \
"v_mfma_f32_16x16x32_bf16 v[112:115], a[196:199], a[252:255], v[112:115]\n" \
"v_mfma_f32_16x16x32_bf16 v[112:115], a[192:195], a[236:239], v[112:115]\n" \
"s_waitcnt lgkmcnt(8)\n" \
"v_mfma_f32_16x16x32_bf16 v[20:23], a[200:203], a[224:227], v[20:23]\n" \
"v_mfma_f32_16x16x32_bf16 v[20:23], a[204:207], a[240:243], v[20:23]\n" \
"v_mfma_f32_16x16x32_bf16 v[52:55], a[204:207], a[244:247], v[52:55]\n" \
"v_mfma_f32_16x16x32_bf16 v[52:55], a[200:203], a[228:231], v[52:55]\n" \
"v_mfma_f32_16x16x32_bf16 v[84:87], a[200:203], a[232:235], v[84:87]\n" \
"s_waitcnt lgkmcnt(0)\n" \
"s_barrier\n" \
"ds_read_b128 a[128:131], v242 offset:8192\n" \
"v_mfma_f32_16x16x32_bf16 v[84:87], a[204:207], a[248:251], v[84:87]\n" \
"s_add_u32 m0, s70, 0\n" \
"ds_read_b64_tr_b16 a[192:193], v240 offset:16384\n" \
"ds_read_b64_tr_b16 a[194:195], v240 offset:18432\n" \
"v_mfma_f32_16x16x32_bf16 v[116:119], a[204:207], a[252:255], v[116:119]\n" \
"s_add_u32 s74, s69, 2097152\n" \
"buffer_load_dwordx4 v246, s[20:23], s74 offen lds\n" \
"v_mfma_f32_16x16x32_bf16 v[116:119], a[200:203], a[236:239], v[116:119]\n" \
"s_lshl_b32 s85, %[wid], 10\n" \
"ds_read_b64_tr_b16 a[196:197], v240 offset:24576\n" \
"ds_read_b64_tr_b16 a[198:199], v240 offset:26624\n" \
"v_mfma_f32_16x16x32_bf16 v[24:27], a[208:211], a[224:227], v[24:27]\n" \
"s_add_u32 s85, s85, 917504\n" \
"buffer_load_dwordx4 v[168:171], v248, s[32:35], s85 offen sc1\n" \
"v_and_b32 v252, 3, v251\n" \
"v_mfma_f32_16x16x32_bf16 v[24:27], a[212:215], a[240:243], v[24:27]\n" \
"s_lshl_b32 s85, %[wid], 10\n" \
"ds_read_b64_tr_b16 a[200:201], v241 offset:16384\n" \
"ds_read_b128 a[132:135], v242 offset:10240\n" \
"v_mfma_f32_16x16x32_bf16 v[56:59], a[212:215], a[244:247], v[56:59]\n" \
"s_add_u32 s85, s85, 929792\n" \
"buffer_load_dwordx4 v[172:175], v248, s[32:35], s85 offen sc1\n" \
"v_mul_u32_u24 v252, 0x3000, v252\n" \
"v_mfma_f32_16x16x32_bf16 v[56:59], a[208:211], a[228:231], v[56:59]\n" \
"s_lshl_b32 s85, %[wid], 10\n" \
"ds_read_b64_tr_b16 a[202:203], v241 offset:18432\n" \
"ds_read_b128 a[136:139], v242 offset:12288\n" \
"v_mfma_f32_16x16x32_bf16 v[88:91], a[208:211], a[232:235], v[88:91]\n" \
"s_add_u32 s85, s85, 942080\n" \
"buffer_load_dwordx4 v[176:179], v248, s[32:35], s85 offen sc1\n" \
"v_and_b32 v253, 28, v251\n" \
"v_mfma_f32_16x16x32_bf16 v[88:91], a[212:215], a[248:251], v[88:91]\n" \
"s_lshl_b32 s85, %[wid], 10\n" \
"ds_read_b64_tr_b16 a[204:205], v241 offset:24576\n" \
"ds_read_b128 a[140:143], v242 offset:14336\n" \
"v_mfma_f32_16x16x32_bf16 v[120:123], a[212:215], a[252:255], v[120:123]\n" \
"s_add_u32 s85, s85, 954368\n" \
"buffer_load_dwordx4 v[180:183], v248, s[32:35], s85 offen sc1\n" \
"v_add_u32 v252, v252, v253\n" \
"v_mfma_f32_16x16x32_bf16 v[120:123], a[208:211], a[236:239], v[120:123]\n" \
"s_lshl_b32 s85, %[bid], 5\n" \
"s_add_u32 s85, s85, 921600\n" \
"ds_read_b64_tr_b16 a[206:207], v241 offset:26624\n" \
"v_mfma_f32_16x16x32_bf16 v[28:31], a[216:219], a[224:227], v[28:31]\n" \
"s_cmp_lt_u32 %[wid], 2\n" \
"s_cselect_b32 s83, 0, 1.0\n" \
"buffer_load_dword v200, v252, s[32:35], s85 offen sc1\n" \
"v_mfma_f32_16x16x32_bf16 v[28:31], a[220:223], a[240:243], v[28:31]\n" \
"ds_read_b64_tr_b16 a[208:209], v240 offset:16896\n" \
"ds_read_b128 a[144:147], v244 offset:8192\n" \
"v_mfma_f32_16x16x32_bf16 v[60:63], a[220:223], a[244:247], v[60:63]\n" \
"s_add_u32 m0, s70, 1024\n" \
"ds_read_b64_tr_b16 a[210:211], v240 offset:18944\n" \
"ds_read_b128 a[148:151], v244 offset:10240\n" \
"v_mfma_f32_16x16x32_bf16 v[60:63], a[216:219], a[228:231], v[60:63]\n" \
"s_add_u32 s74, s69, 2097280\n" \
"v_mfma_f32_16x16x32_bf16 v[92:95], a[216:219], a[232:235], v[92:95]\n" \
"buffer_load_dwordx4 v246, s[20:23], s74 offen lds\n" \
"v_mfma_f32_16x16x32_bf16 v[92:95], a[220:223], a[248:251], v[92:95]\n" \
"ds_read_b64_tr_b16 a[212:213], v240 offset:25088\n" \
"ds_read_b128 a[152:155], v244 offset:12288\n" \
"v_mfma_f32_16x16x32_bf16 v[124:127], a[220:223], a[252:255], v[124:127]\n" \
"ds_read_b64_tr_b16 a[214:215], v240 offset:27136\n" \
"ds_read_b128 a[156:159], v244 offset:14336\n" \
"v_mfma_f32_16x16x32_bf16 v[124:127], a[216:219], a[236:239], v[124:127]\n" \
"s_waitcnt lgkmcnt(15)\n" \
"v_mfma_f32_16x16x32_bf16 a[0:3], a[224:227], a[192:195], a[0:3]\n" \
"ds_read_b64_tr_b16 a[216:217], v241 offset:16896\n" \
"v_mfma_f32_16x16x32_bf16 a[0:3], a[240:243], a[196:199], a[0:3]\n" \
"v_mfma_f32_16x16x32_bf16 a[4:7], a[244:247], a[196:199], a[4:7]\n" \
"ds_read_b64_tr_b16 a[218:219], v241 offset:18944\n" \
"v_mfma_f32_16x16x32_bf16 a[4:7], a[228:231], a[192:195], a[4:7]\n" \
"v_mfma_f32_16x16x32_bf16 a[64:67], a[232:235], a[192:195], a[64:67]\n" \
"ds_read_b64_tr_b16 a[220:221], v241 offset:25088\n" \
"v_mfma_f32_16x16x32_bf16 a[64:67], a[248:251], a[196:199], a[64:67]\n" \
"v_mfma_f32_16x16x32_bf16 a[68:71], a[252:255], a[196:199], a[68:71]\n" \
"ds_read_b64_tr_b16 a[222:223], v241 offset:27136\n" \
"v_mfma_f32_16x16x32_bf16 a[68:71], a[236:239], a[192:195], a[68:71]\n" \
"s_waitcnt lgkmcnt(12)\n" \
"v_mfma_f32_16x16x32_bf16 a[8:11], a[224:227], a[200:203], a[8:11]\n" \
"s_add_u32 m0, s70, 8192\n" \
"ds_read_b64_tr_b16 a[192:193], v240 offset:17408\n" \
"v_mfma_f32_16x16x32_bf16 a[8:11], a[240:243], a[204:207], a[8:11]\n" \
"s_add_u32 s74, s69, 2359296\n" \
"v_mfma_f32_16x16x32_bf16 a[12:15], a[244:247], a[204:207], a[12:15]\n" \
"s_lshl_b32 s89, %[wid], 10\n" \
"buffer_load_dwordx4 v246, s[20:23], s74 offen lds\n" \
"v_mfma_f32_16x16x32_bf16 a[12:15], a[228:231], a[200:203], a[12:15]\n" \
"s_add_u32 m0, s72, 8192\n" \
"s_add_u32 s74, s71, 640\n" \
"ds_read_b64_tr_b16 a[194:195], v240 offset:19456\n" \
"v_mfma_f32_16x16x32_bf16 a[72:75], a[232:235], a[200:203], a[72:75]\n" \
"s_add_u32 s89, s89, 159744\n" \
"ds_read_b64_tr_b16 a[196:197], v240 offset:25600\n" \
"v_add_u32 v236, s89, v248\n" \
"v_mfma_f32_16x16x32_bf16 a[72:75], a[248:251], a[204:207], a[72:75]\n" \
"s_waitcnt lgkmcnt(7)\n" \
"buffer_load_dwordx4 v249, s[16:19], s74 offen lds\n" \
"v_mfma_f32_16x16x32_bf16 a[76:79], a[252:255], a[204:207], a[76:79]\n" \
"s_mov_b32 s77, 0x80b583ec\n" \
"ds_read_b64_tr_b16 a[198:199], v240 offset:27648\n" \
"v_lshrrev_b32 v237, 3, v251\n" \
"v_mfma_f32_16x16x32_bf16 a[76:79], a[236:239], a[200:203], a[76:79]\n" \
"v_lshlrev_b32 v237, 5, v237\n" \
"v_mov_b32 v253, 0x61c88646\n" \
"v_mfma_f32_16x16x32_bf16 a[16:19], a[224:227], a[208:211], a[16:19]\n" \
"s_add_u32 m0, s72, 9216\n" \
"ds_read_b64_tr_b16 a[200:201], v241 offset:17408\n" \
"v_add_co_u32 v202, vcc, s77, v202\n" \
"v_mfma_f32_16x16x32_bf16 a[16:19], a[240:243], a[212:215], a[16:19]\n" \
"s_add_u32 s74, s71, 8832\n" \
"s_lshl_b32 s89, %[wid], 8\n" \
"s_add_u32 s89, s89, 159744\n" \
"v_mfma_f32_16x16x32_bf16 a[20:23], a[244:247], a[212:215], a[20:23]\n" \
"buffer_load_dwordx4 v250, s[16:19], s74 offen lds\n" \
"v_mfma_f32_16x16x32_bf16 a[20:23], a[228:231], a[208:211], a[20:23]\n" \
"s_or_b32 s85, %[bid], %[wid]\n" \
"ds_read_b64_tr_b16 a[202:203], v241 offset:19456\n" \
"v_add_u32 v237, s89, v237\n" \
"v_mfma_f32_16x16x32_bf16 a[80:83], a[232:235], a[208:211], a[80:83]\n" \
"s_cmp_eq_u32 s85, 0\n" \
"s_cselect_b32 s85, 720896, -1\n" \
"ds_read_b64_tr_b16 a[204:205], v241 offset:25600\n" \
"v_mfma_f32_16x16x32_bf16 a[80:83], a[248:251], a[212:215], a[80:83]\n" \
"s_lshr_b32 s93, %[bid], 6\n" \
"v_addc_co_u32 v203, vcc, v253, v203, vcc\n" \
"v_mov_b32 v253, 0\n" \
"v_mfma_f32_16x16x32_bf16 a[84:87], a[252:255], a[212:215], a[84:87]\n" \
"s_add_u32 m0, s70, 9216\n" \
"buffer_store_dwordx2 v[202:203], v253, s[32:35], s85 offen sc1\n" \
"v_mfma_f32_16x16x32_bf16 a[84:87], a[236:239], a[208:211], a[84:87]\n" \
"s_waitcnt lgkmcnt(7)\n" \
"s_add_u32 s74, s69, 2359424\n" \
"ds_read_b64_tr_b16 a[206:207], v241 offset:27648\n" \
"v_mfma_f32_16x16x32_bf16 a[24:27], a[224:227], a[216:219], a[24:27]\n" \
"s_cmp_eq_u32 s93, 2\n" \
"s_cselect_b32 s93, 1.0, 0\n" \
"ds_read_b64_tr_b16 a[208:209], v240 offset:17920\n" \
"v_mfma_f32_16x16x32_bf16 a[24:27], a[240:243], a[220:223], a[24:27]\n" \
"buffer_load_dwordx4 v246, s[20:23], s74 offen lds\n" \
"v_mfma_f32_16x16x32_bf16 a[28:31], a[244:247], a[220:223], a[28:31]\n" \
"s_add_u32 m0, s72, 10240\n" \
"s_add_u32 s74, s71, 17024\n" \
"ds_read_b64_tr_b16 a[210:211], v240 offset:19968\n" \
"v_mfma_f32_16x16x32_bf16 a[28:31], a[228:231], a[216:219], a[28:31]\n" \
"v_mfma_f32_16x16x32_bf16 a[88:91], a[232:235], a[216:219], a[88:91]\n" \
"buffer_load_dwordx4 v249, s[16:19], s74 offen lds\n" \
"v_mfma_f32_16x16x32_bf16 a[88:91], a[248:251], a[220:223], a[88:91]\n" \
"s_add_u32 m0, s72, 11264\n" \
"s_add_u32 s74, s71, 25216\n" \
"ds_read_b64_tr_b16 a[212:213], v240 offset:26112\n" \
"v_mfma_f32_16x16x32_bf16 a[92:95], a[252:255], a[220:223], a[92:95]\n" \
"ds_read_b64_tr_b16 a[214:215], v240 offset:28160\n" \
"v_mfma_f32_16x16x32_bf16 a[92:95], a[236:239], a[216:219], a[92:95]\n" \
"s_waitcnt lgkmcnt(8)\n" \
"buffer_load_dwordx4 v250, s[16:19], s74 offen lds\n" \
"v_mfma_f32_16x16x32_bf16 a[32:35], a[224:227], a[192:195], a[32:35]\n" \
"ds_read_b64_tr_b16 a[216:217], v241 offset:17920\n" \
"v_mfma_f32_16x16x32_bf16 a[32:35], a[240:243], a[196:199], a[32:35]\n" \
"v_mfma_f32_16x16x32_bf16 a[36:39], a[244:247], a[196:199], a[36:39]\n" \
"s_add_u32 m0, s72, 12288\n" \
"ds_read_b64_tr_b16 a[218:219], v241 offset:19968\n" \
"v_mfma_f32_16x16x32_bf16 a[36:39], a[228:231], a[192:195], a[36:39]\n" \
"s_add_u32 s74, s94, 33408\n" \
"ds_read_b64_tr_b16 a[220:221], v241 offset:26112\n" \
"v_mfma_f32_16x16x32_bf16 a[96:99], a[232:235], a[192:195], a[96:99]\n" \
"buffer_load_dwordx4 v249, s[16:19], s74 offen lds\n" \
"v_mfma_f32_16x16x32_bf16 a[96:99], a[248:251], a[196:199], a[96:99]\n" \
"ds_read_b64_tr_b16 a[222:223], v241 offset:28160\n" \
"v_mfma_f32_16x16x32_bf16 a[100:103], a[252:255], a[196:199], a[100:103]\n" \
"v_mfma_f32_16x16x32_bf16 a[100:103], a[236:239], a[192:195], a[100:103]\n" \
"s_waitcnt lgkmcnt(8)\n" \
"v_mfma_f32_16x16x32_bf16 a[40:43], a[224:227], a[200:203], a[40:43]\n" \
"s_add_u32 m0, s72, 13312\n" \
"v_mfma_f32_16x16x32_bf16 a[40:43], a[240:243], a[204:207], a[40:43]\n" \
"s_add_u32 s74, s94, 41600\n" \
"v_mfma_f32_16x16x32_bf16 a[44:47], a[244:247], a[204:207], a[44:47]\n" \
"buffer_load_dwordx4 v250, s[16:19], s74 offen lds\n" \
"v_mfma_f32_16x16x32_bf16 a[44:47], a[228:231], a[200:203], a[44:47]\n" \
"v_mfma_f32_16x16x32_bf16 a[104:107], a[232:235], a[200:203], a[104:107]\n" \
"s_waitcnt vmcnt(29) lgkmcnt(0)\n" \
"s_barrier\n" \
"s_add_u32 m0, s70, 16384\n" \
"v_mfma_f32_16x16x32_bf16 a[104:107], a[248:251], a[204:207], a[104:107]\n" \
"s_add_u32 s74, s69, 2621440\n" \
"ds_read_b64_tr_b16 a[192:193], v240 offset:32768\n" \
"ds_read_b64_tr_b16 a[194:195], v240 offset:34816\n" \
"v_mfma_f32_16x16x32_bf16 a[108:111], a[252:255], a[204:207], a[108:111]\n" \
"buffer_load_dwordx4 v246, s[20:23], s74 offen lds\n" \
"v_mfma_f32_16x16x32_bf16 a[108:111], a[236:239], a[200:203], a[108:111]\n" \
"s_add_u32 m0, s72, 14336\n" \
"ds_read_b64_tr_b16 a[196:197], v240 offset:40960\n" \
"ds_read_b64_tr_b16 a[198:199], v240 offset:43008\n" \
"v_mfma_f32_16x16x32_bf16 a[48:51], a[224:227], a[208:211], a[48:51]\n" \
"s_add_u32 s74, s94, 49792\n" \
"ds_read_b64_tr_b16 a[200:201], v241 offset:32768\n" \
"ds_read_b128 v[160:163], v254\n" \
"v_mfma_f32_16x16x32_bf16 a[48:51], a[240:243], a[212:215], a[48:51]\n" \
"s_lshl2_add_u32 s85, s65, %[wid]\n" \
"buffer_load_dwordx4 v249, s[16:19], s74 offen lds\n" \
"v_mfma_f32_16x16x32_bf16 a[52:55], a[244:247], a[212:215], a[52:55]\n" \
"s_lshl_b32 s85, s85, 15\n" \
"ds_read_b64_tr_b16 a[202:203], v241 offset:34816\n" \
"ds_read_b128 v[164:167], v254 offset:512\n" \
"v_mfma_f32_16x16x32_bf16 a[52:55], a[228:231], a[208:211], a[52:55]\n" \
"s_waitcnt vmcnt(13)\n" \
"s_lshl_b32 s86, s64, 10\n" \
"v_add_f32 v168, v168, v172\n" \
"v_mfma_f32_16x16x32_bf16 a[112:115], a[232:235], a[208:211], a[112:115]\n" \
"s_add_u32 m0, s72, 15360\n" \
"ds_read_b64_tr_b16 a[204:205], v241 offset:40960\n" \
"v_add_f32 v176, v176, v180\n" \
"v_mfma_f32_16x16x32_bf16 a[112:115], a[248:251], a[212:215], a[112:115]\n" \
"s_add_u32 s74, s94, 57984\n" \
"v_add_f32 v169, v169, v173\n" \
"v_add_f32 v177, v177, v181\n" \
"v_mfma_f32_16x16x32_bf16 a[116:119], a[252:255], a[212:215], a[116:119]\n" \
"s_add_u32 s85, s85, s86\n" \
"buffer_load_dwordx4 v250, s[16:19], s74 offen lds\n" \
"v_mfma_f32_16x16x32_bf16 a[116:119], a[236:239], a[208:211], a[116:119]\n" \
"buffer_load_dword v245, v211, s[28:31], s85 offen offset:256\n" \
"v_add_f32 v170, v170, v174\n" \
"v_mfma_f32_16x16x32_bf16 a[56:59], a[224:227], a[216:219], a[56:59]\n" \
"s_getpc_b64 s[96:97]\n" \
".Lpf1_%=:\n" \
"s_add_u32 s96, s96, .Lat_b_%=-.Lpf1_%=\n" \
"s_addc_u32 s97, s97, 0\n" \
"v_mfma_f32_16x16x32_bf16 a[56:59], a[240:243], a[220:223], a[56:59]\n" \
"s_and_b32 s97, s97, 0xffff\n" \
"ds_read_b64_tr_b16 a[206:207], v241 offset:43008\n" \
"ds_read_b64_tr_b16 a[208:209], v240 offset:33280\n" \
"v_mfma_f32_16x16x32_bf16 a[60:63], a[244:247], a[220:223], a[60:63]\n" \
"s_add_u32 m0, s70, 17408\n" \
"ds_read_b64_tr_b16 a[210:211], v240 offset:35328\n" \
"v_add_f32 v178, v178, v182\n" \
"v_mfma_f32_16x16x32_bf16 a[60:63], a[228:231], a[216:219], a[60:63]\n" \
"s_add_u32 s74, s69, 2621568\n" \
"v_add_f32 v171, v171, v175\n" \
"v_add_f32 v179, v179, v183\n" \
"v_mfma_f32_16x16x32_bf16 a[120:123], a[232:235], a[216:219], a[120:123]\n" \
"s_mov_b32 s98, .Lat_code_e-.Lat_b_%=\n" \
"buffer_load_dwordx4 v246, s[20:23], s74 offen lds\n" \
"v_mfma_f32_16x16x32_bf16 a[120:123], a[248:251], a[220:223], a[120:123]\n" \
"s_lshl2_add_u32 s85, s65, %[wid]\n" \
"ds_read_b64_tr_b16 a[212:213], v240 offset:41472\n" \
"v_add_f32 v168, v168, v176\n" \
"v_mfma_f32_16x16x32_bf16 a[124:127], a[252:255], a[220:223], a[124:127]\n" \
"s_and_b32 s85, s85, 31\n" \
"ds_read_b64_tr_b16 a[214:215], v240 offset:43520\n" \
"v_add_f32 v169, v169, v177\n" \
"v_mfma_f32_16x16x32_bf16 a[124:127], a[236:239], a[216:219], a[124:127]\n" \
"s_waitcnt lgkmcnt(10)\n" \
"v_add_f32 v170, v170, v178\n" \
"v_add_f32 v171, v171, v179\n" \
"v_mfma_f32_16x16x32_bf16 v[0:3], a[192:195], a[128:131], v[0:3]\n" \
"s_lshl_b32 s85, s85, 11\n" \
"buffer_load_dword v245, v209, s[96:99], s85 offen\n" \
"v_cvt_pk_bf16_f32 v172, v168, v169\n" \
"v_mfma_f32_16x16x32_bf16 v[0:3], a[196:199], a[144:147], v[0:3]\n" \
"ds_read_b64_tr_b16 a[216:217], v241 offset:33280\n" \
"v_cvt_pk_bf16_f32 v173, v170, v171\n" \
"v_mfma_f32_16x16x32_bf16 v[32:35], a[196:199], a[148:151], v[32:35]\n" \
"ds_read_b64_tr_b16 a[218:219], v241 offset:35328\n" \
"v_lshlrev_b32 v168, 16, v172\n" \
"v_mfma_f32_16x16x32_bf16 v[32:35], a[192:195], a[132:135], v[32:35]\n" \
"v_and_b32 v169, 0xffff0000, v172\n" \
"v_lshlrev_b32 v170, 16, v173\n" \
"v_mfma_f32_16x16x32_bf16 v[64:67], a[192:195], a[136:139], v[64:67]\n" \
"ds_read_b64_tr_b16 a[220:221], v241 offset:41472\n" \
"v_and_b32 v171, 0xffff0000, v173\n" \
"v_mfma_f32_16x16x32_bf16 v[64:67], a[196:199], a[152:155], v[64:67]\n" \
"v_add_f32 v168, s83, v168\n" \
"v_add_f32 v169, s83, v169\n" \
"v_mfma_f32_16x16x32_bf16 v[96:99], a[196:199], a[156:159], v[96:99]\n" \
"ds_read_b64_tr_b16 a[222:223], v241 offset:43520\n" \
"v_add_f32 v170, s83, v170\n" \
"v_mfma_f32_16x16x32_bf16 v[96:99], a[192:195], a[140:143], v[96:99]\n" \
"s_waitcnt lgkmcnt(8)\n" \
"v_add_f32 v171, s83, v171\n" \
"v_cvt_pk_bf16_f32 v172, v168, v169\n" \
"v_mfma_f32_16x16x32_bf16 v[4:7], a[200:203], a[128:131], v[4:7]\n" \
"s_add_u32 m0, s70, 24576\n" \
"ds_read_b64_tr_b16 a[192:193], v240 offset:33792\n" \
"v_cvt_pk_bf16_f32 v173, v170, v171\n" \
"v_mfma_f32_16x16x32_bf16 v[4:7], a[204:207], a[144:147], v[4:7]\n" \
"s_add_u32 s74, s69, 2883584\n" \
"v_lshlrev_b32 v168, 16, v172\n" \
"v_and_b32 v169, 0xffff0000, v172\n" \
"v_mfma_f32_16x16x32_bf16 v[36:39], a[204:207], a[148:151], v[36:39]\n" \
"buffer_load_dwordx4 v246, s[20:23], s74 offen lds\n" \
"v_mfma_f32_16x16x32_bf16 v[36:39], a[200:203], a[132:135], v[36:39]\n" \
"ds_read_b64_tr_b16 a[194:195], v240 offset:35840\n" \
"v_lshlrev_b32 v170, 16, v173\n" \
"v_mfma_f32_16x16x32_bf16 v[68:71], a[200:203], a[136:139], v[68:71]\n" \
"ds_read_b64_tr_b16 a[196:197], v240 offset:41984\n" \
"v_and_b32 v171, 0xffff0000, v173\n" \
"v_mfma_f32_16x16x32_bf16 v[68:71], a[204:207], a[152:155], v[68:71]\n" \
"s_waitcnt vmcnt(17)\n" \
"ds_write_b128 v236, v[168:171]\n" \
"v_add_f32_dpp v200, v200, v200 quad_perm:[1,0,3,2] row_mask:0xf bank_mask:0xf\n" \
"v_mfma_f32_16x16x32_bf16 v[100:103], a[204:207], a[156:159], v[100:103]\n" \
"ds_read_b64_tr_b16 a[198:199], v240 offset:44032\n" \
"v_lshrrev_b32 v152, 5, v251\n" \
"v_mfma_f32_16x16x32_bf16 v[100:103], a[200:203], a[140:143], v[100:103]\n" \
"s_waitcnt lgkmcnt(9)\n" \
"v_add_f32_dpp v200, v200, v200 quad_perm:[2,3,0,1] row_mask:0xf bank_mask:0xf\n" \
"v_cvt_pk_bf16_f32 v200, v200, v200\n" \
"v_mfma_f32_16x16x32_bf16 v[8:11], a[208:211], a[128:131], v[8:11]\n" \
"s_lshl3_add_u32 s79, s67, %[wid]\n" \
"ds_read_b64_tr_b16 a[200:201], v241 offset:33792\n" \
"ds_read_b128 a[160:163], v242 offset:0\n" \
"v_mfma_f32_16x16x32_bf16 v[8:11], a[212:215], a[144:147], v[8:11]\n" \
"s_lshl_b32 s79, s79, 14\n" \
"v_lshlrev_b32 v200, 16, v200\n" \
"v_and_b32 v252, 0x23, v251\n" \
"v_mfma_f32_16x16x32_bf16 v[40:43], a[212:215], a[148:151], v[40:43]\n" \
"s_add_u32 s79, s79, 1048576\n" \
"ds_read_b64_tr_b16 a[202:203], v241 offset:35840\n" \
"ds_read_b128 a[164:167], v242 offset:2048\n" \
"v_mfma_f32_16x16x32_bf16 v[40:43], a[208:211], a[132:135], v[40:43]\n" \
"s_lshl_b32 s90, %[wid], 15\n" \
"v_cmp_eq_u32 vcc, 0, v252\n" \
"v_add_f32 v200, s93, v200\n" \
"v_mfma_f32_16x16x32_bf16 v[72:75], a[208:211], a[136:139], v[72:75]\n" \
"s_sub_u32 s80, s79, s90\n" \
"ds_read_b64_tr_b16 a[204:205], v241 offset:41984\n" \
"ds_read_b128 a[168:171], v242 offset:4096\n" \
"v_mfma_f32_16x16x32_bf16 v[72:75], a[212:215], a[152:155], v[72:75]\n" \
"s_add_u32 s80, s80, 114688\n" \
"v_cvt_pk_bf16_f32 v200, v200, v200\n" \
"v_bfe_u32 v201, v251, 1, 3\n" \
"v_mfma_f32_16x16x32_bf16 v[104:107], a[212:215], a[156:159], v[104:107]\n" \
"s_add_u32 m0, s70, 25600\n" \
"ds_read_b64_tr_b16 a[206:207], v241 offset:44032\n" \
"ds_read_b128 a[172:175], v242 offset:6144\n" \
"v_mfma_f32_16x16x32_bf16 v[104:107], a[208:211], a[140:143], v[104:107]\n" \
"s_waitcnt lgkmcnt(13)\n" \
"v_and_or_b32 v201, v251, 16, v201\n" \
"v_mov_b32 v252, 1073741824\n" \
"v_mfma_f32_16x16x32_bf16 v[12:15], a[216:219], a[128:131], v[12:15]\n" \
"s_add_u32 s74, s69, 2883712\n" \
"buffer_load_dwordx4 v246, s[20:23], s74 offen lds\n" \
"v_mfma_f32_16x16x32_bf16 v[12:15], a[220:223], a[144:147], v[12:15]\n" \
"s_lshl_b32 s90, s67, 18\n" \
"ds_read_b64_tr_b16 a[208:209], v240 offset:34304\n" \
"v_cndmask_b32 v252, v252, v201, vcc\n" \
"v_mfma_f32_16x16x32_bf16 v[44:47], a[220:223], a[148:151], v[44:47]\n" \
"s_sub_u32 s90, 1966080, s90\n" \
"ds_read_b64_tr_b16 a[210:211], v240 offset:36352\n" \
"v_lshlrev_b32 v152, 10, v152\n" \
"v_mfma_f32_16x16x32_bf16 v[44:47], a[216:219], a[132:135], v[44:47]\n" \
"s_add_u32 s36, s79, s90\n" \
"v_bfe_u32 v255, v251, 3, 2\n" \
"v_lshl_or_b32 v152, v255, 8, v152\n" \
"v_mfma_f32_16x16x32_bf16 v[76:79], a[216:219], a[136:139], v[76:79]\n" \
"s_add_u32 s37, s80, s90\n" \
"ds_read_b64_tr_b16 a[212:213], v240 offset:42496\n" \
"ds_read_b128 a[176:179], v244 offset:0\n" \
"v_mfma_f32_16x16x32_bf16 v[76:79], a[220:223], a[152:155], v[76:79]\n" \
"s_mul_i32 s73, s68, 1179648\n" \
"v_and_b32 v255, 7, v251\n" \
"v_lshl_or_b32 v152, v255, 4, v152\n" \
"v_mfma_f32_16x16x32_bf16 v[108:111], a[220:223], a[156:159], v[108:111]\n" \
"s_add_u32 s73, s73, 3145728\n" \
"ds_read_b64_tr_b16 a[214:215], v240 offset:44544\n" \
"ds_read_b128 a[180:183], v244 offset:2048\n" \
"v_mfma_f32_16x16x32_bf16 v[108:111], a[216:219], a[140:143], v[108:111]\n" \
"s_waitcnt lgkmcnt(14)\n" \
"v_lshlrev_b32 v136, 16, v160\n" \
"v_and_b32 v137, 0xffff0000, v160\n" \
"v_mfma_f32_16x16x32_bf16 v[16:19], a[192:195], a[128:131], v[16:19]\n" \
"s_lshl1_add_u32 s87, s66, s67\n" \
"ds_read_b64_tr_b16 a[216:217], v241 offset:34304\n" \
"ds_read_b128 a[184:187], v244 offset:4096\n" \
"v_mfma_f32_16x16x32_bf16 v[16:19], a[196:199], a[144:147], v[16:19]\n" \
"s_and_b32 s87, s87, 7\n" \
"v_lshlrev_b32 v138, 16, v161\n" \
"v_and_b32 v139, 0xffff0000, v161\n" \
"v_mfma_f32_16x16x32_bf16 v[48:51], a[196:199], a[148:151], v[48:51]\n" \
"s_lshl3_add_u32 s87, s64, s87\n" \
"ds_read_b64_tr_b16 a[218:219], v241 offset:36352\n" \
"ds_read_b128 a[188:191], v244 offset:6144\n" \
"v_mfma_f32_16x16x32_bf16 v[48:51], a[192:195], a[132:135], v[48:51]\n" \
"s_lshr_b32 s90, %[wid], 1\n" \
"ds_read_b64_tr_b16 a[220:221], v241 offset:42496\n" \
"v_lshlrev_b32 v140, 16, v162\n" \
"v_mfma_f32_16x16x32_bf16 v[80:83], a[192:195], a[136:139], v[80:83]\n" \
"s_lshl1_add_u32 s87, s87, s90\n" \
"ds_read_b64_tr_b16 a[222:223], v241 offset:44544\n" \
"v_and_b32 v141, 0xffff0000, v162\n" \
"v_mfma_f32_16x16x32_bf16 v[80:83], a[196:199], a[152:155], v[80:83]\n" \
"s_lshl_b32 s87, s87, 15\n" \
"v_lshlrev_b32 v142, 16, v163\n" \
"v_and_b32 v143, 0xffff0000, v163\n" \
"v_mfma_f32_16x16x32_bf16 v[112:115], a[196:199], a[156:159], v[112:115]\n" \
"s_and_b32 s90, %[wid], 1\n" \
"v_lshlrev_b32 v144, 16, v164\n" \
"v_and_b32 v145, 0xffff0000, v164\n" \
"v_mfma_f32_16x16x32_bf16 v[112:115], a[192:195], a[140:143], v[112:115]\n" \
"s_waitcnt lgkmcnt(13)\n" \
"v_lshlrev_b32 v146, 16, v165\n" \
"v_and_b32 v147, 0xffff0000, v165\n" \
"v_mfma_f32_16x16x32_bf16 v[20:23], a[200:203], a[128:131], v[20:23]\n" \
"s_lshl1_add_u32 s90, s66, s90\n" \
"v_lshlrev_b32 v148, 16, v166\n" \
"v_and_b32 v149, 0xffff0000, v166\n" \
"v_mfma_f32_16x16x32_bf16 v[20:23], a[204:207], a[144:147], v[20:23]\n" \
"s_lshl_b32 s90, s90, 7\n" \
"v_lshlrev_b32 v150, 16, v167\n" \
"v_and_b32 v151, 0xffff0000, v167\n" \
"v_mfma_f32_16x16x32_bf16 v[52:55], a[204:207], a[148:151], v[52:55]\n" \
"s_add_u32 s87, s87, s90\n" \
"s_lshl_b32 s90, %[wid], 12\n" \
"s_add_u32 s91, s90, 131072\n" \
"v_mfma_f32_16x16x32_bf16 v[52:55], a[200:203], a[132:135], v[52:55]\n" \
"v_mfma_f32_16x16x32_bf16 v[84:87], a[200:203], a[136:139], v[84:87]\n" \
"s_waitcnt lgkmcnt(0)\n" \
"s_barrier\n" \
"s_add_u32 m0, s70, 32768\n" \
"v_mfma_f32_16x16x32_bf16 v[84:87], a[204:207], a[152:155], v[84:87]\n" \
"s_add_u32 s74, s69, 3145728\n" \
"ds_read_b128 v[184:187], v237 offset:2048\n" \
"ds_read_b128 v[188:191], v237 offset:2064\n" \
"v_mfma_f32_16x16x32_bf16 v[116:119], a[204:207], a[156:159], v[116:119]\n" \
"s_lshr_b32 s85, %[bid], 2\n" \
"buffer_load_dwordx4 v246, s[20:23], s74 offen lds\n" \
"v_mfma_f32_16x16x32_bf16 v[116:119], a[200:203], a[140:143], v[116:119]\n" \
"s_add_u32 m0, s72, 0\n" \
"ds_read_b128 v[168:171], v237 offset:0\n" \
"ds_read_b128 v[172:175], v237 offset:16\n" \
"v_mfma_f32_16x16x32_bf16 v[24:27], a[208:211], a[128:131], v[24:27]\n" \
"s_add_u32 s74, s71, 768\n" \
"ds_read_b128 v[192:195], v237 offset:3072\n" \
"ds_read_b128 v[196:199], v237 offset:3088\n" \
"v_mfma_f32_16x16x32_bf16 v[24:27], a[212:215], a[144:147], v[24:27]\n" \
"s_lshl_b32 s85, s85, 6\n" \
"buffer_load_dwordx4 v249, s[16:19], s74 offen lds\n" \
"v_mfma_f32_16x16x32_bf16 v[56:59], a[212:215], a[148:151], v[56:59]\n" \
"s_and_b32 s86, %[bid], 2\n" \
"ds_read_b128 v[176:179], v237 offset:1024\n" \
"ds_read_b128 v[180:183], v237 offset:1040\n" \
"v_mfma_f32_16x16x32_bf16 v[56:59], a[208:211], a[132:135], v[56:59]\n" \
"s_lshl2_add_u32 s85, s86, s85\n" \
"ds_read_b64_tr_b16 a[192:193], v240 offset:49152\n" \
"ds_read_b64_tr_b16 a[194:195], v240 offset:51200\n" \
"v_mfma_f32_16x16x32_bf16 v[88:91], a[208:211], a[136:139], v[88:91]\n" \
"s_add_u32 m0, s72, 1024\n" \
"ds_read_b64_tr_b16 a[196:197], v240 offset:57344\n" \
"ds_read_b64_tr_b16 a[198:199], v240 offset:59392\n" \
"v_mfma_f32_16x16x32_bf16 v[88:91], a[212:215], a[152:155], v[88:91]\n" \
"s_add_u32 s74, s71, 8960\n" \
"ds_read_b64_tr_b16 a[200:201], v241 offset:49152\n" \
"ds_read_b64_tr_b16 a[202:203], v241 offset:51200\n" \
"v_mfma_f32_16x16x32_bf16 v[120:123], a[212:215], a[156:159], v[120:123]\n" \
"s_and_b32 s86, %[bid], 1\n" \
"buffer_load_dwordx4 v250, s[16:19], s74 offen lds\n" \
"v_mfma_f32_16x16x32_bf16 v[120:123], a[208:211], a[140:143], v[120:123]\n" \
"s_lshl_b32 s86, s86, 5\n" \
"ds_read_b64_tr_b16 a[204:205], v241 offset:57344\n" \
"ds_read_b64_tr_b16 a[206:207], v241 offset:59392\n" \
"v_mfma_f32_16x16x32_bf16 v[28:31], a[216:219], a[128:131], v[28:31]\n" \
"s_add_u32 s85, s85, s86\n" \
"s_cmp_eq_u32 %[wid], 0\n" \
"s_cselect_b32 s85, s85, 1073741824\n" \
"v_mfma_f32_16x16x32_bf16 v[28:31], a[220:223], a[144:147], v[28:31]\n" \
"s_mov_b32 s84, 1040356334\n" \
"buffer_store_short v200, v252, s[32:35], s85 offen sc1\n" \
"v_and_b32 v212, 0x70, v246\n" \
"v_mfma_f32_16x16x32_bf16 v[60:63], a[220:223], a[148:151], v[60:63]\n" \
"s_add_u32 m0, s70, 33792\n" \
"ds_read_b64_tr_b16 a[208:209], v240 offset:49664\n" \
"ds_read_b64_tr_b16 a[210:211], v240 offset:51712\n" \
"v_mfma_f32_16x16x32_bf16 v[60:63], a[216:219], a[132:135], v[60:63]\n" \
"s_add_u32 s74, s69, 3145856\n" \
"v_bfe_u32 v213, v251, 2, 3\n" \
"v_lshl_or_b32 v212, v213, 10, v212\n" \
"v_mfma_f32_16x16x32_bf16 v[92:95], a[216:219], a[136:139], v[92:95]\n" \
"buffer_load_dwordx4 v246, s[20:23], s74 offen lds\n" \
"v_mfma_f32_16x16x32_bf16 v[92:95], a[220:223], a[152:155], v[92:95]\n" \
"s_add_u32 m0, s72, 2048\n" \
"ds_read_b64_tr_b16 a[212:213], v240 offset:57856\n" \
"v_lshrrev_b32 v213, 5, v251\n" \
"v_mfma_f32_16x16x32_bf16 v[124:127], a[220:223], a[156:159], v[124:127]\n" \
"s_add_u32 s74, s71, 17152\n" \
"ds_read_b64_tr_b16 a[214:215], v240 offset:59904\n" \
"v_lshlrev_b32 v213, 11, v213\n" \
"v_mfma_f32_16x16x32_bf16 v[124:127], a[216:219], a[140:143], v[124:127]\n" \
"s_waitcnt lgkmcnt(8)\n" \
"buffer_load_dwordx4 v249, s[16:19], s74 offen lds\n" \
"v_mfma_f32_16x16x32_bf16 a[0:3], a[128:131], a[192:195], a[0:3]\n" \
"s_add_u32 m0, s72, 3072\n" \
"ds_read_b64_tr_b16 a[216:217], v241 offset:49664\n" \
"v_and_b32 v214, 0x1ff, v240\n" \
"v_mfma_f32_16x16x32_bf16 a[0:3], a[144:147], a[196:199], a[0:3]\n" \
"s_add_u32 s74, s71, 25344\n" \
"v_add3_u32 v213, v213, v214, s91\n" \
"v_xor_b32 v214, 32, v213\n" \
"v_mfma_f32_16x16x32_bf16 a[4:7], a[148:151], a[196:199], a[4:7]\n" \
"buffer_load_dwordx4 v250, s[16:19], s74 offen lds\n" \
"v_mfma_f32_16x16x32_bf16 a[4:7], a[132:135], a[192:195], a[4:7]\n" \
"ds_read_b64_tr_b16 a[218:219], v241 offset:51712\n" \
"v_mul_f32 v136, v136, v184\n" \
"v_mfma_f32_16x16x32_bf16 a[64:67], a[136:139], a[192:195], a[64:67]\n" \
"ds_read_b64_tr_b16 a[220:221], v241 offset:57856\n" \
"v_mul_f32 v138, v138, v186\n" \
"v_mfma_f32_16x16x32_bf16 a[64:67], a[152:155], a[196:199], a[64:67]\n" \
"v_mul_f32 v137, v137, v185\n" \
"v_mul_f32 v139, v139, v187\n" \
"v_mfma_f32_16x16x32_bf16 a[68:71], a[156:159], a[196:199], a[68:71]\n" \
"s_add_u32 m0, s72, 4096\n" \
"ds_read_b64_tr_b16 a[222:223], v241 offset:59904\n" \
"v_cvt_pk_bf16_f32 v160, v136, v137\n" \
"v_mfma_f32_16x16x32_bf16 a[68:71], a[140:143], a[192:195], a[68:71]\n" \
"s_waitcnt lgkmcnt(8)\n" \
"v_cvt_pk_bf16_f32 v161, v138, v139\n" \
"v_lshlrev_b32 v136, 16, v160\n" \
"v_mfma_f32_16x16x32_bf16 a[8:11], a[128:131], a[200:203], a[8:11]\n" \
"s_add_u32 s74, s94, 33536\n" \
"buffer_load_dwordx4 v249, s[16:19], s74 offen lds\n" \
"v_mfma_f32_16x16x32_bf16 a[8:11], a[144:147], a[204:207], a[8:11]\n" \
"s_add_u32 m0, s70, 40960\n" \
"ds_read_b64_tr_b16 a[192:193], v240 offset:50176\n" \
"v_lshlrev_b32 v138, 16, v161\n" \
"v_mfma_f32_16x16x32_bf16 a[12:15], a[148:151], a[204:207], a[12:15]\n" \
"s_add_u32 s74, s69, 3407872\n" \
"ds_read_b64_tr_b16 a[194:195], v240 offset:52224\n" \
"v_and_b32 v137, 0xffff0000, v160\n" \
"v_mfma_f32_16x16x32_bf16 a[12:15], a[132:135], a[200:203], a[12:15]\n" \
"buffer_load_dwordx4 v246, s[20:23], s74 offen lds\n" \
"v_mfma_f32_16x16x32_bf16 a[72:75], a[136:139], a[200:203], a[72:75]\n" \
"s_add_u32 m0, s72, 5120\n" \
"ds_read_b64_tr_b16 a[196:197], v240 offset:58368\n" \
"v_and_b32 v139, 0xffff0000, v161\n" \
"v_mfma_f32_16x16x32_bf16 a[72:75], a[152:155], a[204:207], a[72:75]\n" \
"s_add_u32 s74, s94, 41728\n" \
"v_add_f32 v136, v136, v168\n" \
"v_add_f32 v138, v138, v170\n" \
"v_mfma_f32_16x16x32_bf16 a[76:79], a[156:159], a[204:207], a[76:79]\n" \
"s_lshr_b32 s85, s78, 4\n" \
"buffer_load_dwordx4 v250, s[16:19], s74 offen lds\n" \
"v_mfma_f32_16x16x32_bf16 a[76:79], a[140:143], a[200:203], a[76:79]\n" \
".Lst0_2_%=:\n" \
"s_cbranch_execz .Lst0_6_%=\n" \
"s_waitcnt lgkmcnt(7)\n" \
"ds_read_b64_tr_b16 a[198:199], v240 offset:60416\n" \
"v_add_f32 v137, v137, v169\n" \
"v_mfma_f32_16x16x32_bf16 a[16:19], a[128:131], a[208:211], a[16:19]\n" \
"s_lshl_b32 s85, s85, 14\n" \
"ds_read_b64_tr_b16 a[200:201], v241 offset:50176\n" \
"v_add_f32 v139, v139, v171\n" \
"v_mfma_f32_16x16x32_bf16 a[16:19], a[144:147], a[212:215], a[16:19]\n" \
"s_and_b32 s86, s78, 8\n" \
"v_cvt_pk_bf16_f32 v160, v136, v137\n" \
"v_cvt_pk_bf16_f32 v161, v138, v139\n" \
"v_mfma_f32_16x16x32_bf16 a[20:23], a[148:151], a[212:215], a[20:23]\n" \
"s_add_u32 m0, s72, 6144\n" \
"ds_read_b64_tr_b16 a[202:203], v241 offset:52224\n" \
"v_mul_f32 v140, v140, v188\n" \
"v_mfma_f32_16x16x32_bf16 a[20:23], a[132:135], a[208:211], a[20:23]\n" \
"s_add_u32 s74, s94, 49920\n" \
"v_mul_f32 v142, v142, v190\n" \
"v_mul_f32 v141, v141, v189\n" \
"v_mfma_f32_16x16x32_bf16 a[80:83], a[136:139], a[208:211], a[80:83]\n" \
"s_lshl4_add_u32 s85, s86, s85\n" \
"buffer_load_dwordx4 v249, s[16:19], s74 offen lds\n" \
"v_mfma_f32_16x16x32_bf16 a[80:83], a[152:155], a[212:215], a[80:83]\n" \
"s_add_u32 m0, s91, 0\n" \
"ds_read_b64_tr_b16 a[204:205], v241 offset:58368\n" \
"v_mul_f32 v143, v143, v191\n" \
"v_mfma_f32_16x16x32_bf16 a[84:87], a[156:159], a[212:215], a[84:87]\n" \
"s_add_u32 s74, s87, 0\n" \
"ds_read_b64_tr_b16 a[206:207], v241 offset:60416\n" \
"v_cvt_pk_bf16_f32 v162, v140, v141\n" \
"v_mfma_f32_16x16x32_bf16 a[84:87], a[140:143], a[208:211], a[84:87]\n" \
"s_waitcnt lgkmcnt(8)\n" \
"buffer_load_dwordx4 v212, s[48:51], s74 offen nt lds\n" \
"v_mfma_f32_16x16x32_bf16 a[24:27], a[128:131], a[216:219], a[24:27]\n" \
"s_add_u32 m0, s70, 41984\n" \
"ds_read_b64_tr_b16 a[208:209], v240 offset:50688\n" \
"v_cvt_pk_bf16_f32 v163, v142, v143\n" \
"v_mfma_f32_16x16x32_bf16 a[24:27], a[144:147], a[220:223], a[24:27]\n" \
"s_add_u32 s74, s69, 3408000\n" \
"v_lshlrev_b32 v140, 16, v162\n" \
"v_lshlrev_b32 v142, 16, v163\n" \
"v_mfma_f32_16x16x32_bf16 a[28:31], a[148:151], a[220:223], a[28:31]\n" \
"s_lshl_b32 s86, %[wid], 11\n" \
"buffer_load_dwordx4 v246, s[20:23], s74 offen lds\n" \
"v_mfma_f32_16x16x32_bf16 a[28:31], a[132:135], a[216:219], a[28:31]\n" \
"s_add_u32 m0, s72, 7168\n" \
"ds_read_b64_tr_b16 a[210:211], v240 offset:52736\n" \
"v_and_b32 v141, 0xffff0000, v162\n" \
"v_mfma_f32_16x16x32_bf16 a[88:91], a[136:139], a[216:219], a[88:91]\n" \
"s_add_u32 s74, s94, 58112\n" \
"ds_read_b64_tr_b16 a[212:213], v240 offset:58880\n" \
"v_and_b32 v143, 0xffff0000, v163\n" \
"v_mfma_f32_16x16x32_bf16 a[88:91], a[152:155], a[220:223], a[88:91]\n" \
"s_add_u32 s85, s85, s86\n" \
"buffer_load_dwordx4 v250, s[16:19], s74 offen lds\n" \
"v_mfma_f32_16x16x32_bf16 a[92:95], a[156:159], a[220:223], a[92:95]\n" \
"s_add_u32 m0, s91, 1024\n" \
"ds_read_b64_tr_b16 a[214:215], v240 offset:60928\n" \
"v_add_f32 v140, v140, v172\n" \
"v_mfma_f32_16x16x32_bf16 a[92:95], a[140:143], a[216:219], a[92:95]\n" \
"s_waitcnt lgkmcnt(8)\n" \
"v_add_f32 v142, v142, v174\n" \
"v_add_f32 v141, v141, v173\n" \
"v_mfma_f32_16x16x32_bf16 a[32:35], a[128:131], a[192:195], a[32:35]\n" \
"s_add_u32 s74, s87, 16384\n" \
"buffer_load_dwordx4 v212, s[48:51], s74 offen nt lds\n" \
"v_mfma_f32_16x16x32_bf16 a[32:35], a[144:147], a[196:199], a[32:35]\n" \
"s_add_u32 m0, s91, 2048\n" \
"ds_read_b64_tr_b16 a[216:217], v241 offset:50688\n" \
"v_add_f32 v143, v143, v175\n" \
"v_mfma_f32_16x16x32_bf16 a[36:39], a[148:151], a[196:199], a[36:39]\n" \
"s_add_u32 s74, s87, 8192\n" \
"ds_read_b64_tr_b16 a[218:219], v241 offset:52736\n" \
"v_cvt_pk_bf16_f32 v162, v140, v141\n" \
"v_mfma_f32_16x16x32_bf16 a[36:39], a[132:135], a[192:195], a[36:39]\n" \
"s_add_u32 s85, s85, 1048576\n" \
"buffer_load_dwordx4 v212, s[48:51], s74 offen nt lds\n" \
"v_mfma_f32_16x16x32_bf16 a[96:99], a[136:139], a[192:195], a[96:99]\n" \
"ds_read_b64_tr_b16 a[220:221], v241 offset:58880\n" \
"ds_read_b64_tr_b16 a[222:223], v241 offset:60928\n" \
"v_mfma_f32_16x16x32_bf16 a[96:99], a[152:155], a[196:199], a[96:99]\n" \
"s_waitcnt vmcnt(21)\n" \
"ds_read_b128 a[224:227], v242 offset:8192\n" \
"ds_read_b128 a[228:231], v242 offset:10240\n" \
"v_mfma_f32_16x16x32_bf16 a[100:103], a[156:159], a[196:199], a[100:103]\n" \
"s_add_u32 m0, s91, 3072\n" \
"ds_read_b128 a[232:235], v242 offset:12288\n" \
"ds_read_b128 a[236:239], v242 offset:14336\n" \
"v_mfma_f32_16x16x32_bf16 a[100:103], a[140:143], a[192:195], a[100:103]\n" \
"s_waitcnt lgkmcnt(12)\n" \
"v_cvt_pk_bf16_f32 v163, v142, v143\n" \
"v_mul_f32 v144, v144, v192\n" \
"v_mfma_f32_16x16x32_bf16 a[40:43], a[128:131], a[200:203], a[40:43]\n" \
"s_add_u32 s74, s87, 24576\n" \
"buffer_load_dwordx4 v212, s[48:51], s74 offen nt lds\n" \
"v_mfma_f32_16x16x32_bf16 a[40:43], a[144:147], a[204:207], a[40:43]\n" \
"v_mul_f32 v146, v146, v194\n" \
"v_mul_f32 v145, v145, v193\n" \
"v_mfma_f32_16x16x32_bf16 a[44:47], a[148:151], a[204:207], a[44:47]\n" \
"v_mul_f32 v147, v147, v195\n" \
"v_cvt_pk_bf16_f32 v164, v144, v145\n" \
"v_mfma_f32_16x16x32_bf16 a[44:47], a[132:135], a[200:203], a[44:47]\n" \
"buffer_store_dwordx4 v[160:163], v152, s[32:35], s85 offen sc1\n" \
"v_mfma_f32_16x16x32_bf16 a[104:107], a[136:139], a[200:203], a[104:107]\n" \
"s_waitcnt lgkmcnt(4)\n" \
"s_barrier\n" \
"ds_read_b128 a[240:243], v244 offset:8192\n" \
"v_mfma_f32_16x16x32_bf16 a[104:107], a[152:155], a[204:207], a[104:107]\n" \
"s_add_u32 m0, s70, 49152\n" \
"ds_read_b64_tr_b16 a[192:193], v240 offset:0\n" \
"ds_read_b64_tr_b16 a[194:195], v240 offset:2048\n" \
"v_mfma_f32_16x16x32_bf16 a[108:111], a[156:159], a[204:207], a[108:111]\n" \
"s_add_u32 s74, s69, 3670016\n" \
"buffer_load_dwordx4 v246, s[20:23], s74 offen lds\n" \
"v_mfma_f32_16x16x32_bf16 a[108:111], a[140:143], a[200:203], a[108:111]\n" \
"s_add_u32 s85, s85, 8192\n" \
"ds_read_b64_tr_b16 a[196:197], v240 offset:8192\n" \
"ds_read_b64_tr_b16 a[198:199], v240 offset:10240\n" \
"v_mfma_f32_16x16x32_bf16 a[48:51], a[128:131], a[208:211], a[48:51]\n" \
"ds_read_b64_tr_b16 a[200:201], v241 offset:0\n" \
"ds_read_b128 a[244:247], v244 offset:10240\n" \
"v_mfma_f32_16x16x32_bf16 a[48:51], a[144:147], a[212:215], a[48:51]\n" \
"ds_read_b128 a[248:251], v244 offset:12288\n" \
"v_cvt_pk_bf16_f32 v165, v146, v147\n" \
"v_mfma_f32_16x16x32_bf16 a[52:55], a[148:151], a[212:215], a[52:55]\n" \
"ds_read_b64_tr_b16 a[202:203], v241 offset:2048\n" \
"ds_read_b128 a[252:255], v244 offset:14336\n" \
"v_mfma_f32_16x16x32_bf16 a[52:55], a[132:135], a[208:211], a[52:55]\n" \
"v_lshlrev_b32 v144, 16, v164\n" \
"v_lshlrev_b32 v146, 16, v165\n" \
"v_mfma_f32_16x16x32_bf16 a[112:115], a[136:139], a[208:211], a[112:115]\n" \
"ds_read_b64_tr_b16 a[204:205], v241 offset:8192\n" \
"v_and_b32 v145, 0xffff0000, v164\n" \
"v_mfma_f32_16x16x32_bf16 a[112:115], a[152:155], a[212:215], a[112:115]\n" \
"v_and_b32 v147, 0xffff0000, v165\n" \
"v_add_f32 v144, v144, v176\n" \
"v_mfma_f32_16x16x32_bf16 a[116:119], a[156:159], a[212:215], a[116:119]\n" \
"ds_read_b64_tr_b16 a[206:207], v241 offset:10240\n" \
"v_add_f32 v146, v146, v178\n" \
"v_mfma_f32_16x16x32_bf16 a[116:119], a[140:143], a[208:211], a[116:119]\n" \
"v_add_f32 v145, v145, v177\n" \
"v_add_f32 v147, v147, v179\n" \
"v_mfma_f32_16x16x32_bf16 a[56:59], a[128:131], a[216:219], a[56:59]\n" \
"ds_read_b64_tr_b16 a[208:209], v240 offset:512\n" \
"v_cvt_pk_bf16_f32 v164, v144, v145\n" \
"v_mfma_f32_16x16x32_bf16 a[56:59], a[144:147], a[220:223], a[56:59]\n" \
"v_cvt_pk_bf16_f32 v165, v146, v147\n" \
"v_mul_f32 v148, v148, v196\n" \
"v_mfma_f32_16x16x32_bf16 a[60:63], a[148:151], a[220:223], a[60:63]\n" \
"s_add_u32 m0, s70, 50176\n" \
"ds_read_b64_tr_b16 a[210:211], v240 offset:2560\n" \
"v_mul_f32 v150, v150, v198\n" \
"v_mfma_f32_16x16x32_bf16 a[60:63], a[132:135], a[216:219], a[60:63]\n" \
"s_add_u32 s74, s69, 3670144\n" \
"v_mul_f32 v149, v149, v197\n" \
"v_mul_f32 v151, v151, v199\n" \
"v_mfma_f32_16x16x32_bf16 a[120:123], a[136:139], a[216:219], a[120:123]\n" \
"buffer_load_dwordx4 v246, s[20:23], s74 offen lds\n" \
"v_mfma_f32_16x16x32_bf16 a[120:123], a[152:155], a[220:223], a[120:123]\n" \
"ds_read_b64_tr_b16 a[212:213], v240 offset:8704\n" \
"v_cvt_pk_bf16_f32 v166, v148, v149\n" \
"v_mfma_f32_16x16x32_bf16 a[124:127], a[156:159], a[220:223], a[124:127]\n" \
"s_add_u32 m0, s72, 8192\n" \
"ds_read_b64_tr_b16 a[214:215], v240 offset:10752\n" \
"v_cvt_pk_bf16_f32 v167, v150, v151\n" \
"v_mfma_f32_16x16x32_bf16 a[124:127], a[140:143], a[216:219], a[124:127]\n" \
"s_waitcnt lgkmcnt(11)\n" \
"v_lshlrev_b32 v148, 16, v166\n" \
"v_lshlrev_b32 v150, 16, v167\n" \
  \
"v_mfma_f32_16x16x32_bf16 v[0:3], a[192:195], a[160:163], v[0:3]\n" \
"s_add_u32 s74, s71, 896\n" \
"ds_read_b64_tr_b16 a[216:217], v241 offset:512\n" \
"v_and_b32 v149, 0xffff0000, v166\n" \
"v_mfma_f32_16x16x32_bf16 v[0:3], a[196:199], a[176:179], v[0:3]\n" \
"s_waitcnt lgkmcnt(7)\n" \
"buffer_load_dwordx4 v249, s[16:19], s74 offen lds\n" \
"v_mfma_f32_16x16x32_bf16 v[32:35], a[196:199], a[180:183], v[32:35]\n" \
"ds_read_b64_tr_b16 a[218:219], v241 offset:2560\n" \
"v_and_b32 v151, 0xffff0000, v167\n" \
"v_mfma_f32_16x16x32_bf16 v[32:35], a[192:195], a[164:167], v[32:35]\n" \
"v_add_f32 v148, v148, v180\n" \
"v_add_f32 v150, v150, v182\n" \
"v_mfma_f32_16x16x32_bf16 v[64:67], a[192:195], a[168:171], v[64:67]\n" \
"s_add_u32 m0, s72, 9216\n" \
"ds_read_b64_tr_b16 a[220:221], v241 offset:8704\n" \
"v_add_f32 v149, v149, v181\n" \
"v_mfma_f32_16x16x32_bf16 v[64:67], a[196:199], a[184:187], v[64:67]\n" \
"s_add_u32 s74, s71, 9088\n" \
"v_add_f32 v151, v151, v183\n" \
"v_cvt_pk_bf16_f32 v166, v148, v149\n" \
"v_mfma_f32_16x16x32_bf16 v[96:99], a[196:199], a[188:191], v[96:99]\n" \
"buffer_load_dwordx4 v250, s[16:19], s74 offen lds\n" \
"v_mfma_f32_16x16x32_bf16 v[96:99], a[192:195], a[172:175], v[96:99]\n" \
"s_waitcnt lgkmcnt(7)\n" \
"ds_read_b64_tr_b16 a[222:223], v241 offset:10752\n" \
"v_cvt_pk_bf16_f32 v167, v150, v151\n" \
"v_mfma_f32_16x16x32_bf16 v[4:7], a[200:203], a[160:163], v[4:7]\n" \
"s_add_u32 m0, s70, 57344\n" \
"ds_read_b64_tr_b16 a[192:193], v240 offset:1024\n" \
"v_mfma_f32_16x16x32_bf16 v[4:7], a[204:207], a[176:179], v[4:7]\n" \
"s_add_u32 s74, s69, 3932160\n" \
"buffer_store_dwordx4 v[164:167], v152, s[32:35], s85 offen sc1\n" \
"v_mfma_f32_16x16x32_bf16 v[36:39], a[204:207], a[180:183], v[36:39]\n" \
"ds_read_b64_tr_b16 a[194:195], v240 offset:3072\n" \
"v_mfma_f32_16x16x32_bf16 v[36:39], a[200:203], a[164:167], v[36:39]\n" \
"v_mfma_f32_16x16x32_bf16 v[68:71], a[200:203], a[168:171], v[68:71]\n" \
"buffer_load_dwordx4 v246, s[20:23], s74 offen lds\n" \
"v_mfma_f32_16x16x32_bf16 v[68:71], a[204:207], a[184:187], v[68:71]\n" \
"s_add_u32 m0, s72, 10240\n" \
"s_add_u32 s74, s71, 17280\n" \
"ds_read_b64_tr_b16 a[196:197], v240 offset:9216\n" \
"v_mfma_f32_16x16x32_bf16 v[100:103], a[204:207], a[188:191], v[100:103]\n" \
"ds_read_b64_tr_b16 a[198:199], v240 offset:11264\n" \
"v_mfma_f32_16x16x32_bf16 v[100:103], a[200:203], a[172:175], v[100:103]\n" \
"s_waitcnt lgkmcnt(8)\n" \
"buffer_load_dwordx4 v249, s[16:19], s74 offen lds\n" \
"v_mfma_f32_16x16x32_bf16 v[8:11], a[208:211], a[160:163], v[8:11]\n" \
"s_add_u32 m0, s72, 11264\n" \
"ds_read_b64_tr_b16 a[200:201], v241 offset:1024\n" \
"v_mfma_f32_16x16x32_bf16 v[8:11], a[212:215], a[176:179], v[8:11]\n" \
"s_add_u32 s74, s71, 25472\n" \
"v_mfma_f32_16x16x32_bf16 v[40:43], a[212:215], a[180:183], v[40:43]\n" \
"buffer_load_dwordx4 v250, s[16:19], s74 offen lds\n" \
"v_mfma_f32_16x16x32_bf16 v[40:43], a[208:211], a[164:167], v[40:43]\n" \
"ds_read_b64_tr_b16 a[202:203], v241 offset:3072\n" \
"v_mfma_f32_16x16x32_bf16 v[72:75], a[208:211], a[168:171], v[72:75]\n" \
"ds_read_b64_tr_b16 a[204:205], v241 offset:9216\n" \
"v_mfma_f32_16x16x32_bf16 v[72:75], a[212:215], a[184:187], v[72:75]\n" \
"v_mfma_f32_16x16x32_bf16 v[104:107], a[212:215], a[188:191], v[104:107]\n" \
"s_add_u32 m0, s70, 58368\n" \
"ds_read_b64_tr_b16 a[206:207], v241 offset:11264\n" \
"v_mfma_f32_16x16x32_bf16 v[104:107], a[208:211], a[172:175], v[104:107]\n" \
"s_waitcnt lgkmcnt(8)\n" \
"s_add_u32 s74, s69, 3932288\n" \
"v_mfma_f32_16x16x32_bf16 v[12:15], a[216:219], a[160:163], v[12:15]\n" \
"buffer_load_dwordx4 v246, s[20:23], s74 offen lds\n" \
"v_mfma_f32_16x16x32_bf16 v[12:15], a[220:223], a[176:179], v[12:15]\n" \
"s_add_u32 m0, s72, 12288\n" \
"s_add_u32 s74, s94, 33664\n" \
"ds_read_b64_tr_b16 a[208:209], v240 offset:1536\n" \
"v_mfma_f32_16x16x32_bf16 v[44:47], a[220:223], a[180:183], v[44:47]\n" \
"ds_read_b64_tr_b16 a[210:211], v240 offset:3584\n" \
"v_mfma_f32_16x16x32_bf16 v[44:47], a[216:219], a[164:167], v[44:47]\n" \
"buffer_load_dwordx4 v249, s[16:19], s74 offen lds\n" \
"v_mfma_f32_16x16x32_bf16 v[76:79], a[216:219], a[168:171], v[76:79]\n" \
"s_add_u32 m0, s72, 13312\n" \
"ds_read_b64_tr_b16 a[212:213], v240 offset:9728\n" \
"v_mfma_f32_16x16x32_bf16 v[76:79], a[220:223], a[184:187], v[76:79]\n" \
"s_add_u32 s74, s94, 41856\n" \
"v_mfma_f32_16x16x32_bf16 v[108:111], a[220:223], a[188:191], v[108:111]\n" \
"buffer_load_dwordx4 v250, s[16:19], s74 offen lds\n" \
"v_mfma_f32_16x16x32_bf16 v[108:111], a[216:219], a[172:175], v[108:111]\n" \
"s_waitcnt lgkmcnt(7)\n" \
"ds_read_b64_tr_b16 a[214:215], v240 offset:11776\n" \
"v_mfma_f32_16x16x32_bf16 v[16:19], a[192:195], a[160:163], v[16:19]\n" \
"ds_read_b64_tr_b16 a[216:217], v241 offset:1536\n" \
"v_mfma_f32_16x16x32_bf16 v[16:19], a[196:199], a[176:179], v[16:19]\n" \
"v_mfma_f32_16x16x32_bf16 v[48:51], a[196:199], a[180:183], v[48:51]\n" \
"s_add_u32 m0, s72, 14336\n" \
"ds_read_b64_tr_b16 a[218:219], v241 offset:3584\n" \
"v_mfma_f32_16x16x32_bf16 v[48:51], a[192:195], a[164:167], v[48:51]\n" \
"s_add_u32 s74, s94, 50048\n" \
"ds_read_b64_tr_b16 a[220:221], v241 offset:9728\n" \
"v_mfma_f32_16x16x32_bf16 v[80:83], a[192:195], a[168:171], v[80:83]\n" \
"buffer_load_dwordx4 v249, s[16:19], s74 offen lds\n" \
"v_mfma_f32_16x16x32_bf16 v[80:83], a[196:199], a[184:187], v[80:83]\n" \
"ds_read_b64_tr_b16 a[222:223], v241 offset:11776\n" \
"v_mfma_f32_16x16x32_bf16 v[112:115], a[196:199], a[188:191], v[112:115]\n" \
"v_mfma_f32_16x16x32_bf16 v[112:115], a[192:195], a[172:175], v[112:115]\n" \
"s_waitcnt lgkmcnt(8)\n" \
"v_mfma_f32_16x16x32_bf16 v[20:23], a[200:203], a[160:163], v[20:23]\n" \
"s_add_u32 m0, s72, 15360\n" \
"v_mfma_f32_16x16x32_bf16 v[20:23], a[204:207], a[176:179], v[20:23]\n" \
"s_add_u32 s74, s94, 58240\n" \
"v_mfma_f32_16x16x32_bf16 v[52:55], a[204:207], a[180:183], v[52:55]\n" \
"buffer_load_dwordx4 v250, s[16:19], s74 offen lds\n" \
"v_mfma_f32_16x16x32_bf16 v[52:55], a[200:203], a[164:167], v[52:55]\n" \
"v_mfma_f32_16x16x32_bf16 v[84:87], a[200:203], a[168:171], v[84:87]\n" \
"s_waitcnt vmcnt(31) lgkmcnt(0)\n" \
"s_barrier\n" \
"s_add_u32 m0, s70, 0\n" \
"v_mfma_f32_16x16x32_bf16 v[84:87], a[204:207], a[184:187], v[84:87]\n" \
"s_add_u32 s74, s69, 3670016\n" \
"ds_read_b64_tr_b16 a[192:193], v240 offset:16384\n" \
"ds_read_b64_tr_b16 a[194:195], v240 offset:18432\n" \
"v_mfma_f32_16x16x32_bf16 v[116:119], a[204:207], a[188:191], v[116:119]\n" \
"buffer_load_dwordx4 v246, s[24:27], s74 offen lds\n" \
"v_mfma_f32_16x16x32_bf16 v[116:119], a[200:203], a[172:175], v[116:119]\n" \
"ds_read_b64_tr_b16 a[196:197], v240 offset:24576\n" \
"ds_read_b64_tr_b16 a[198:199], v240 offset:26624\n" \
"v_mfma_f32_16x16x32_bf16 v[24:27], a[208:211], a[160:163], v[24:27]\n" \
"s_waitcnt vmcnt(18)\n" \
"ds_read_b64_tr_b16 a[200:201], v241 offset:16384\n" \
"ds_read_b128 a[128:131], v242 offset:0\n" \
"v_mfma_f32_16x16x32_bf16 v[24:27], a[212:215], a[176:179], v[24:27]\n" \
"v_mfma_f32_16x16x32_bf16 v[56:59], a[212:215], a[180:183], v[56:59]\n" \
"ds_read_b64_tr_b16 a[202:203], v241 offset:18432\n" \
"ds_read_b128 a[132:135], v242 offset:2048\n" \
"v_mfma_f32_16x16x32_bf16 v[56:59], a[208:211], a[164:167], v[56:59]\n" \
"v_mfma_f32_16x16x32_bf16 v[88:91], a[208:211], a[168:171], v[88:91]\n" \
"ds_read_b64_tr_b16 a[204:205], v241 offset:24576\n" \
"ds_read_b128 a[136:139], v242 offset:4096\n" \
"v_mfma_f32_16x16x32_bf16 v[88:91], a[212:215], a[184:187], v[88:91]\n" \
"v_mfma_f32_16x16x32_bf16 v[120:123], a[212:215], a[188:191], v[120:123]\n" \
"ds_read_b64_tr_b16 a[206:207], v241 offset:26624\n" \
"ds_read_b128 a[140:143], v242 offset:6144\n" \
"v_mfma_f32_16x16x32_bf16 v[120:123], a[208:211], a[172:175], v[120:123]\n" \
"v_mfma_f32_16x16x32_bf16 v[28:31], a[216:219], a[160:163], v[28:31]\n" \
"ds_read_b64_tr_b16 a[208:209], v240 offset:16896\n" \
"v_mfma_f32_16x16x32_bf16 v[28:31], a[220:223], a[176:179], v[28:31]\n" \
"v_mfma_f32_16x16x32_bf16 v[60:63], a[220:223], a[180:183], v[60:63]\n" \
"s_add_u32 m0, s70, 1024\n" \
"ds_read_b64_tr_b16 a[210:211], v240 offset:18944\n" \
"v_mfma_f32_16x16x32_bf16 v[60:63], a[216:219], a[164:167], v[60:63]\n" \
"s_add_u32 s74, s69, 3670144\n" \
"v_mfma_f32_16x16x32_bf16 v[92:95], a[216:219], a[168:171], v[92:95]\n" \
"buffer_load_dwordx4 v246, s[24:27], s74 offen lds\n" \
"v_mfma_f32_16x16x32_bf16 v[92:95], a[220:223], a[184:187], v[92:95]\n" \
"ds_read_b64_tr_b16 a[212:213], v240 offset:25088\n" \
"ds_read_b128 a[144:147], v244 offset:0\n" \
"v_mfma_f32_16x16x32_bf16 v[124:127], a[220:223], a[188:191], v[124:127]\n" \
"ds_read_b64_tr_b16 a[214:215], v240 offset:27136\n" \
"ds_read_b128 a[148:151], v244 offset:2048\n" \
"v_mfma_f32_16x16x32_bf16 v[124:127], a[216:219], a[172:175], v[124:127]\n" \
"s_waitcnt lgkmcnt(14)\n" \
"v_mfma_f32_16x16x32_bf16 v[0:3], a[192:195], a[224:227], v[0:3]\n" \
"ds_read_b64_tr_b16 a[216:217], v241 offset:16896\n" \
"ds_read_b128 a[152:155], v244 offset:4096\n" \
"v_mfma_f32_16x16x32_bf16 v[0:3], a[196:199], a[240:243], v[0:3]\n" \
"v_mfma_f32_16x16x32_bf16 v[32:35], a[196:199], a[244:247], v[32:35]\n" \
"ds_read_b64_tr_b16 a[218:219], v241 offset:18944\n" \
"ds_read_b128 a[156:159], v244 offset:6144\n" \
"v_mfma_f32_16x16x32_bf16 v[32:35], a[192:195], a[228:231], v[32:35]\n" \
"v_mfma_f32_16x16x32_bf16 v[64:67], a[192:195], a[232:235], v[64:67]\n" \
"ds_read_b64_tr_b16 a[220:221], v241 offset:25088\n" \
"v_mfma_f32_16x16x32_bf16 v[64:67], a[196:199], a[248:251], v[64:67]\n" \
"v_mfma_f32_16x16x32_bf16 v[96:99], a[196:199], a[252:255], v[96:99]\n" \
"ds_read_b64_tr_b16 a[222:223], v241 offset:27136\n" \
"v_mfma_f32_16x16x32_bf16 v[96:99], a[192:195], a[236:239], v[96:99]\n" \
"s_waitcnt lgkmcnt(13)\n" \
"s_lshl_b32 s85, s67, 5\n" \
"v_mfma_f32_16x16x32_bf16 v[4:7], a[200:203], a[224:227], v[4:7]\n" \
"s_add_u32 m0, s70, 8192\n" \
"s_add_u32 s85, s85, s68\n" \
"ds_read_b64_tr_b16 a[192:193], v240 offset:17408\n" \
"v_mfma_f32_16x16x32_bf16 v[4:7], a[204:207], a[240:243], v[4:7]\n" \
"s_add_u32 s74, s69, 3932160\n" \
"s_lshl2_add_u32 s85, s85, %[wid]\n" \
"v_mfma_f32_16x16x32_bf16 v[36:39], a[204:207], a[244:247], v[36:39]\n" \
"s_lshl_b32 s85, s85, 2\n" \
"buffer_load_dwordx4 v246, s[24:27], s74 offen lds\n" \
"v_mfma_f32_16x16x32_bf16 v[36:39], a[200:203], a[228:231], v[36:39]\n" \
"s_add_u32 s85, s85, 90112\n" \
"ds_read_b64_tr_b16 a[194:195], v240 offset:19456\n" \
"v_mfma_f32_16x16x32_bf16 v[68:71], a[200:203], a[232:235], v[68:71]\n" \
"s_waitcnt vmcnt(11)\n" \
"buffer_store_dword v239, v238, s[32:35], s85 offen sc1\n" \
"v_mfma_f32_16x16x32_bf16 v[68:71], a[204:207], a[248:251], v[68:71]\n" \
"ds_read_b64_tr_b16 a[196:197], v240 offset:25600\n" \
"v_mfma_f32_16x16x32_bf16 v[100:103], a[204:207], a[252:255], v[100:103]\n" \
"ds_read_b64_tr_b16 a[198:199], v240 offset:27648\n" \
"v_mfma_f32_16x16x32_bf16 v[100:103], a[200:203], a[236:239], v[100:103]\n" \
"s_waitcnt lgkmcnt(11)\n" \
"v_mfma_f32_16x16x32_bf16 v[8:11], a[208:211], a[224:227], v[8:11]\n" \
"ds_read_b64_tr_b16 a[200:201], v241 offset:17408\n" \
"v_mfma_f32_16x16x32_bf16 v[8:11], a[212:215], a[240:243], v[8:11]\n" \
"v_mfma_f32_16x16x32_bf16 v[40:43], a[212:215], a[244:247], v[40:43]\n" \
"ds_read_b64_tr_b16 a[202:203], v241 offset:19456\n" \
"v_mfma_f32_16x16x32_bf16 v[40:43], a[208:211], a[228:231], v[40:43]\n" \
"v_mfma_f32_16x16x32_bf16 v[72:75], a[208:211], a[232:235], v[72:75]\n" \
"ds_read_b64_tr_b16 a[204:205], v241 offset:25600\n" \
"v_mfma_f32_16x16x32_bf16 v[72:75], a[212:215], a[248:251], v[72:75]\n" \
"v_mfma_f32_16x16x32_bf16 v[104:107], a[212:215], a[252:255], v[104:107]\n" \
"s_add_u32 m0, s70, 9216\n" \
"ds_read_b64_tr_b16 a[206:207], v241 offset:27648\n" \
"v_mfma_f32_16x16x32_bf16 v[104:107], a[208:211], a[236:239], v[104:107]\n" \
"s_waitcnt lgkmcnt(8)\n" \
"s_add_u32 s74, s69, 3932288\n" \
"v_mfma_f32_16x16x32_bf16 v[12:15], a[216:219], a[224:227], v[12:15]\n" \
"buffer_load_dwordx4 v246, s[24:27], s74 offen lds\n" \
"v_mfma_f32_16x16x32_bf16 v[12:15], a[220:223], a[240:243], v[12:15]\n" \
"ds_read_b64_tr_b16 a[208:209], v240 offset:17920\n" \
"v_mfma_f32_16x16x32_bf16 v[44:47], a[220:223], a[244:247], v[44:47]\n" \
"ds_read_b64_tr_b16 a[210:211], v240 offset:19968\n" \
"v_mfma_f32_16x16x32_bf16 v[44:47], a[216:219], a[228:231], v[44:47]\n" \
"v_mfma_f32_16x16x32_bf16 v[76:79], a[216:219], a[232:235], v[76:79]\n" \
"ds_read_b64_tr_b16 a[212:213], v240 offset:26112\n" \
"v_mfma_f32_16x16x32_bf16 v[76:79], a[220:223], a[248:251], v[76:79]\n" \
"v_mfma_f32_16x16x32_bf16 v[108:111], a[220:223], a[252:255], v[108:111]\n" \
"ds_read_b64_tr_b16 a[214:215], v240 offset:28160\n" \
"v_mfma_f32_16x16x32_bf16 v[108:111], a[216:219], a[236:239], v[108:111]\n" \
"s_waitcnt lgkmcnt(8)\n" \
"v_mfma_f32_16x16x32_bf16 v[16:19], a[192:195], a[224:227], v[16:19]\n" \
"ds_read_b64_tr_b16 a[216:217], v241 offset:17920\n" \
"v_mfma_f32_16x16x32_bf16 v[16:19], a[196:199], a[240:243], v[16:19]\n" \
"v_mfma_f32_16x16x32_bf16 v[48:51], a[196:199], a[244:247], v[48:51]\n" \
"ds_read_b64_tr_b16 a[218:219], v241 offset:19968\n" \
"v_mfma_f32_16x16x32_bf16 v[48:51], a[192:195], a[228:231], v[48:51]\n" \
"ds_read_b64_tr_b16 a[220:221], v241 offset:26112\n" \
"v_mfma_f32_16x16x32_bf16 v[80:83], a[192:195], a[232:235], v[80:83]\n" \
"ds_read_b64_tr_b16 a[222:223], v241 offset:28160\n" \
"v_mfma_f32_16x16x32_bf16 v[80:83], a[196:199], a[248:251], v[80:83]\n" \
"v_mfma_f32_16x16x32_bf16 v[112:115], a[196:199], a[252:255], v[112:115]\n" \
"v_mfma_f32_16x16x32_bf16 v[112:115], a[192:195], a[236:239], v[112:115]\n" \
"s_waitcnt lgkmcnt(8)\n" \
"v_mfma_f32_16x16x32_bf16 v[20:23], a[200:203], a[224:227], v[20:23]\n" \
"v_mfma_f32_16x16x32_bf16 v[20:23], a[204:207], a[240:243], v[20:23]\n" \
"v_mfma_f32_16x16x32_bf16 v[52:55], a[204:207], a[244:247], v[52:55]\n" \
"v_mfma_f32_16x16x32_bf16 v[52:55], a[200:203], a[228:231], v[52:55]\n" \
"v_mfma_f32_16x16x32_bf16 v[84:87], a[200:203], a[232:235], v[84:87]\n" \
"s_waitcnt lgkmcnt(0)\n" \
"s_barrier\n" \
"ds_read_b64_tr_b16 v[128:129], v213 offset:0\n" \
"v_mfma_f32_16x16x32_bf16 v[84:87], a[204:207], a[248:251], v[84:87]\n" \
"s_add_u32 m0, s70, 16384\n" \
"ds_read_b64_tr_b16 a[192:193], v240 offset:32768\n" \
"ds_read_b64_tr_b16 a[194:195], v240 offset:34816\n" \
"v_mfma_f32_16x16x32_bf16 v[116:119], a[204:207], a[252:255], v[116:119]\n" \
"s_add_u32 s74, s69, 2097152\n" \
"buffer_load_dwordx4 v246, s[24:27], s74 offen lds\n" \
"v_mfma_f32_16x16x32_bf16 v[116:119], a[200:203], a[236:239], v[116:119]\n" \
"ds_read_b64_tr_b16 a[196:197], v240 offset:40960\n" \
"ds_read_b64_tr_b16 a[198:199], v240 offset:43008\n" \
"v_mfma_f32_16x16x32_bf16 v[24:27], a[208:211], a[224:227], v[24:27]\n" \
"ds_read_b64_tr_b16 a[200:201], v241 offset:32768\n" \
"ds_read_b64_tr_b16 v[130:131], v213 offset:1024\n" \
"v_mfma_f32_16x16x32_bf16 v[24:27], a[212:215], a[240:243], v[24:27]\n" \
"ds_read_b64_tr_b16 v[132:133], v214 offset:0\n" \
"v_mfma_f32_16x16x32_bf16 v[56:59], a[212:215], a[244:247], v[56:59]\n" \
"ds_read_b64_tr_b16 a[202:203], v241 offset:34816\n" \
"ds_read_b64_tr_b16 v[134:135], v214 offset:1024\n" \
"v_mfma_f32_16x16x32_bf16 v[56:59], a[208:211], a[228:231], v[56:59]\n" \
"v_mfma_f32_16x16x32_bf16 v[88:91], a[208:211], a[232:235], v[88:91]\n" \
"ds_read_b64_tr_b16 a[204:205], v241 offset:40960\n" \
"ds_read_b64_tr_b16 v[136:137], v213 offset:512\n" \
"v_mfma_f32_16x16x32_bf16 v[88:91], a[212:215], a[248:251], v[88:91]\n" \
"v_mfma_f32_16x16x32_bf16 v[120:123], a[212:215], a[252:255], v[120:123]\n" \
"ds_read_b64_tr_b16 a[206:207], v241 offset:43008\n" \
"ds_read_b64_tr_b16 v[138:139], v213 offset:1536\n" \
"v_mfma_f32_16x16x32_bf16 v[120:123], a[208:211], a[236:239], v[120:123]\n" \
"v_mfma_f32_16x16x32_bf16 v[28:31], a[216:219], a[224:227], v[28:31]\n" \
"ds_read_b64_tr_b16 a[208:209], v240 offset:33280\n" \
"ds_read_b64_tr_b16 v[140:141], v214 offset:512\n" \
"v_mfma_f32_16x16x32_bf16 v[28:31], a[220:223], a[240:243], v[28:31]\n" \
"v_mfma_f32_16x16x32_bf16 v[60:63], a[220:223], a[244:247], v[60:63]\n" \
"s_add_u32 m0, s70, 17408\n" \
"ds_read_b64_tr_b16 a[210:211], v240 offset:35328\n" \
"ds_read_b64_tr_b16 v[142:143], v214 offset:1536\n" \
"v_mfma_f32_16x16x32_bf16 v[60:63], a[216:219], a[228:231], v[60:63]\n" \
"s_add_u32 s74, s69, 2097280\n" \
"s_lshl2_add_u32 s85, s67, %[wid]\n" \
"v_mfma_f32_16x16x32_bf16 v[92:95], a[216:219], a[232:235], v[92:95]\n" \
"s_lshl_b32 s85, s85, 16\n" \
"buffer_load_dwordx4 v246, s[24:27], s74 offen lds\n" \
"v_mfma_f32_16x16x32_bf16 v[92:95], a[220:223], a[248:251], v[92:95]\n" \
"s_lshl_b32 s86, s68, 8\n" \
"ds_read_b64_tr_b16 a[212:213], v240 offset:41472\n" \
"v_mfma_f32_16x16x32_bf16 v[124:127], a[220:223], a[252:255], v[124:127]\n" \
"s_add_u32 s85, s85, s86\n" \
"ds_read_b64_tr_b16 a[214:215], v240 offset:43520\n" \
"v_mfma_f32_16x16x32_bf16 v[124:127], a[216:219], a[236:239], v[124:127]\n" \
"s_waitcnt lgkmcnt(15)\n" \
"s_add_u32 s85, s85, 0\n" \
"v_mfma_f32_16x16x32_bf16 v[0:3], a[192:195], a[128:131], v[0:3]\n" \
"buffer_load_dword v245, v208, s[28:31], s85 offen\n" \
"v_mfma_f32_16x16x32_bf16 v[0:3], a[196:199], a[144:147], v[0:3]\n" \
"s_lshl2_add_u32 s85, s67, %[wid]\n" \
"s_lshl_b32 s85, s85, 16\n" \
"ds_read_b64_tr_b16 a[216:217], v241 offset:33280\n" \
"v_mfma_f32_16x16x32_bf16 v[32:35], a[196:199], a[148:151], v[32:35]\n" \
"s_lshl_b32 s86, s68, 8\n" \
"s_add_u32 s85, s85, s86\n" \
"ds_read_b64_tr_b16 a[218:219], v241 offset:35328\n" \
"v_mfma_f32_16x16x32_bf16 v[32:35], a[192:195], a[132:135], v[32:35]\n" \
"s_add_u32 s85, s85, 2097152\n" \
"v_mfma_f32_16x16x32_bf16 v[64:67], a[192:195], a[136:139], v[64:67]\n" \
"buffer_load_dword v245, v208, s[28:31], s85 offen\n" \
"v_mfma_f32_16x16x32_bf16 v[64:67], a[196:199], a[152:155], v[64:67]\n" \
"s_mov_b32 s38, 0x45442120\n" \
"s_mov_b32 s39, 0x47234622\n" \
"ds_read_b64_tr_b16 a[220:221], v241 offset:41472\n" \
"v_mfma_f32_16x16x32_bf16 v[96:99], a[196:199], a[156:159], v[96:99]\n" \
"s_lshl_b32 s85, s67, 3\n" \
"s_lshr_b64 s[38:39], s[38:39], s85\n" \
"ds_read_b64_tr_b16 a[222:223], v241 offset:43520\n" \
"v_mfma_f32_16x16x32_bf16 v[96:99], a[192:195], a[140:143], v[96:99]\n" \
"s_waitcnt lgkmcnt(11)\n" \
"s_and_b32 s85, s38, 0xff\n" \
"s_lshl_b32 s85, s85, 14\n" \
"v_mfma_f32_16x16x32_bf16 v[4:7], a[200:203], a[128:131], v[4:7]\n" \
"s_add_u32 m0, s70, 24576\n" \
"s_add_u32 s85, s85, s73\n" \
"ds_read_b64_tr_b16 a[192:193], v240 offset:33792\n" \
"v_mfma_f32_16x16x32_bf16 v[4:7], a[204:207], a[144:147], v[4:7]\n" \
"s_add_u32 s74, s69, 2359296\n" \
"s_add_u32 s92, s85, s90\n" \
"v_mfma_f32_16x16x32_bf16 v[36:39], a[204:207], a[148:151], v[36:39]\n" \
"buffer_load_dwordx4 v246, s[24:27], s74 offen lds\n" \
"v_mfma_f32_16x16x32_bf16 v[36:39], a[200:203], a[132:135], v[36:39]\n" \
"s_waitcnt vmcnt(10)\n" \
"ds_read_b64_tr_b16 a[194:195], v240 offset:35840\n" \
"ds_read_b128 v[192:195], v242 offset:8192\n" \
"v_mfma_f32_16x16x32_bf16 v[68:71], a[200:203], a[136:139], v[68:71]\n" \
"ds_read_b64_tr_b16 a[196:197], v240 offset:41984\n" \
"ds_read_b128 v[196:199], v242 offset:10240\n" \
"v_mfma_f32_16x16x32_bf16 v[68:71], a[204:207], a[152:155], v[68:71]\n" \
"s_waitcnt lgkmcnt(11)\n" \
"buffer_store_dwordx4 v[128:131], v248, s[32:35], s92 offen offset:0 sc1\n" \
"v_mfma_f32_16x16x32_bf16 v[100:103], a[204:207], a[156:159], v[100:103]\n" \
"ds_read_b64_tr_b16 a[198:199], v240 offset:44032\n" \
"ds_read_b128 v[200:203], v242 offset:12288\n" \
"v_mfma_f32_16x16x32_bf16 v[100:103], a[200:203], a[140:143], v[100:103]\n" \
"s_waitcnt lgkmcnt(11)\n" \
"v_mfma_f32_16x16x32_bf16 v[8:11], a[208:211], a[128:131], v[8:11]\n" \
"ds_read_b64_tr_b16 a[200:201], v241 offset:33792\n" \
"ds_read_b128 v[204:207], v242 offset:14336\n" \
"v_mfma_f32_16x16x32_bf16 v[8:11], a[212:215], a[144:147], v[8:11]\n" \
"v_mfma_f32_16x16x32_bf16 v[40:43], a[212:215], a[148:151], v[40:43]\n" \
"ds_read_b64_tr_b16 a[202:203], v241 offset:35840\n" \
"v_mfma_f32_16x16x32_bf16 v[40:43], a[208:211], a[132:135], v[40:43]\n" \
"v_mfma_f32_16x16x32_bf16 v[72:75], a[208:211], a[136:139], v[72:75]\n" \
"buffer_store_dwordx4 v[132:135], v248, s[32:35], s92 offen offset:1024 sc1\n" \
"v_mfma_f32_16x16x32_bf16 v[72:75], a[212:215], a[152:155], v[72:75]\n" \
"ds_read_b64_tr_b16 a[204:205], v241 offset:41984\n" \
"ds_read_b128 v[208:211], v244 offset:8192\n" \
"v_mfma_f32_16x16x32_bf16 v[104:107], a[212:215], a[156:159], v[104:107]\n" \
"s_add_u32 m0, s70, 25600\n" \
"ds_read_b64_tr_b16 a[206:207], v241 offset:44032\n" \
"ds_read_b128 v[212:215], v244 offset:10240\n" \
"v_mfma_f32_16x16x32_bf16 v[104:107], a[208:211], a[140:143], v[104:107]\n" \
"s_waitcnt lgkmcnt(14)\n" \
"s_add_u32 s74, s69, 2359424\n" \
"v_mfma_f32_16x16x32_bf16 v[12:15], a[216:219], a[128:131], v[12:15]\n" \
"buffer_load_dwordx4 v246, s[24:27], s74 offen lds\n" \
"v_mfma_f32_16x16x32_bf16 v[12:15], a[220:223], a[144:147], v[12:15]\n" \
"ds_read_b64_tr_b16 a[208:209], v240 offset:34304\n" \
"ds_read_b128 v[216:219], v244 offset:12288\n" \
"v_mfma_f32_16x16x32_bf16 v[44:47], a[220:223], a[148:151], v[44:47]\n" \
"ds_read_b64_tr_b16 a[210:211], v240 offset:36352\n" \
"ds_read_b128 v[220:223], v244 offset:14336\n" \
"v_mfma_f32_16x16x32_bf16 v[44:47], a[216:219], a[132:135], v[44:47]\n" \
"v_mfma_f32_16x16x32_bf16 v[76:79], a[216:219], a[136:139], v[76:79]\n" \
"ds_read_b64_tr_b16 a[212:213], v240 offset:42496\n" \
"v_mfma_f32_16x16x32_bf16 v[76:79], a[220:223], a[152:155], v[76:79]\n" \
"v_mfma_f32_16x16x32_bf16 v[108:111], a[220:223], a[156:159], v[108:111]\n" \
"buffer_store_dwordx4 v[136:139], v248, s[32:35], s92 offen offset:2048 sc1\n" \
"v_mfma_f32_16x16x32_bf16 v[108:111], a[216:219], a[140:143], v[108:111]\n" \
"s_waitcnt lgkmcnt(13)\n" \
"ds_read_b64_tr_b16 a[214:215], v240 offset:44544\n" \
"v_mfma_f32_16x16x32_bf16 v[16:19], a[192:195], a[128:131], v[16:19]\n" \
"ds_read_b64_tr_b16 a[216:217], v241 offset:34304\n" \
"v_mfma_f32_16x16x32_bf16 v[16:19], a[196:199], a[144:147], v[16:19]\n" \
"v_mfma_f32_16x16x32_bf16 v[48:51], a[196:199], a[148:151], v[48:51]\n" \
"ds_read_b64_tr_b16 a[218:219], v241 offset:36352\n" \
"v_mfma_f32_16x16x32_bf16 v[48:51], a[192:195], a[132:135], v[48:51]\n" \
"ds_read_b64_tr_b16 a[220:221], v241 offset:42496\n" \
"v_mfma_f32_16x16x32_bf16 v[80:83], a[192:195], a[136:139], v[80:83]\n" \
".Lst0_3_%=:\n" \
"s_cbranch_execz .Lst0_7_%=\n" \
"ds_read_b64_tr_b16 a[222:223], v241 offset:44544\n" \
"v_mfma_f32_16x16x32_bf16 v[80:83], a[196:199], a[152:155], v[80:83]\n" \
"v_mfma_f32_16x16x32_bf16 v[112:115], a[196:199], a[156:159], v[112:115]\n" \
"v_mfma_f32_16x16x32_bf16 v[112:115], a[192:195], a[140:143], v[112:115]\n" \
"s_waitcnt lgkmcnt(11)\n" \
"v_mfma_f32_16x16x32_bf16 v[20:23], a[200:203], a[128:131], v[20:23]\n" \
"buffer_store_dwordx4 v[140:143], v248, s[32:35], s92 offen offset:3072 sc1\n" \
"v_mfma_f32_16x16x32_bf16 v[20:23], a[204:207], a[144:147], v[20:23]\n" \
"v_mfma_f32_16x16x32_bf16 v[52:55], a[204:207], a[148:151], v[52:55]\n" \
"v_mfma_f32_16x16x32_bf16 v[52:55], a[200:203], a[132:135], v[52:55]\n" \
"v_mfma_f32_16x16x32_bf16 v[84:87], a[200:203], a[136:139], v[84:87]\n" \
"s_waitcnt lgkmcnt(0)\n" \
"s_barrier\n" \
"s_add_u32 m0, s70, 32768\n" \
"v_mfma_f32_16x16x32_bf16 v[84:87], a[204:207], a[152:155], v[84:87]\n" \
"s_add_u32 s74, s69, 2621440\n" \
"ds_read_b64_tr_b16 a[192:193], v240 offset:49152\n" \
"ds_read_b64_tr_b16 a[194:195], v240 offset:51200\n" \
"v_mfma_f32_16x16x32_bf16 v[116:119], a[204:207], a[156:159], v[116:119]\n" \
"buffer_load_dwordx4 v246, s[24:27], s74 offen lds\n" \
"v_mfma_f32_16x16x32_bf16 v[116:119], a[200:203], a[140:143], v[116:119]\n" \
"ds_read_b64_tr_b16 a[196:197], v240 offset:57344\n" \
"ds_read_b64_tr_b16 a[198:199], v240 offset:59392\n" \
"v_mfma_f32_16x16x32_bf16 v[24:27], a[208:211], a[128:131], v[24:27]\n" \
"ds_read_b64_tr_b16 a[200:201], v241 offset:49152\n" \
"v_mfma_f32_16x16x32_bf16 v[24:27], a[212:215], a[144:147], v[24:27]\n" \
"v_mfma_f32_16x16x32_bf16 v[56:59], a[212:215], a[148:151], v[56:59]\n" \
"ds_read_b64_tr_b16 a[202:203], v241 offset:51200\n" \
"v_mfma_f32_16x16x32_bf16 v[56:59], a[208:211], a[132:135], v[56:59]\n" \
"v_mfma_f32_16x16x32_bf16 v[88:91], a[208:211], a[136:139], v[88:91]\n" \
"ds_read_b64_tr_b16 a[204:205], v241 offset:57344\n" \
"v_mfma_f32_16x16x32_bf16 v[88:91], a[212:215], a[152:155], v[88:91]\n" \
"v_mfma_f32_16x16x32_bf16 v[120:123], a[212:215], a[156:159], v[120:123]\n" \
"ds_read_b64_tr_b16 a[206:207], v241 offset:59392\n" \
"v_mfma_f32_16x16x32_bf16 v[120:123], a[208:211], a[140:143], v[120:123]\n" \
"v_mfma_f32_16x16x32_bf16 v[28:31], a[216:219], a[128:131], v[28:31]\n" \
"ds_read_b64_tr_b16 a[208:209], v240 offset:49664\n" \
"v_mfma_f32_16x16x32_bf16 v[28:31], a[220:223], a[144:147], v[28:31]\n" \
"v_mfma_f32_16x16x32_bf16 v[60:63], a[220:223], a[148:151], v[60:63]\n" \
"s_add_u32 m0, s70, 33792\n" \
"ds_read_b64_tr_b16 a[210:211], v240 offset:51712\n" \
"v_mfma_f32_16x16x32_bf16 v[60:63], a[216:219], a[132:135], v[60:63]\n" \
"s_add_u32 s74, s69, 2621568\n" \
"v_mfma_f32_16x16x32_bf16 v[92:95], a[216:219], a[136:139], v[92:95]\n" \
"s_lshr_b32 s71, s71, 2\n" \
"buffer_load_dwordx4 v246, s[24:27], s74 offen lds\n" \
"v_mfma_f32_16x16x32_bf16 v[92:95], a[220:223], a[152:155], v[92:95]\n" \
"s_add_u32 s71, s71, s73\n" \
"s_add_u32 s94, s94, 32768\n" \
"ds_read_b64_tr_b16 a[212:213], v240 offset:57856\n" \
"v_mfma_f32_16x16x32_bf16 v[124:127], a[220:223], a[156:159], v[124:127]\n" \
"s_lshr_b32 s94, s94, 2\n" \
"s_add_u32 s94, s94, s73\n" \
"ds_read_b64_tr_b16 a[214:215], v240 offset:59904\n" \
"v_mfma_f32_16x16x32_bf16 v[124:127], a[216:219], a[140:143], v[124:127]\n" \
"s_waitcnt lgkmcnt(8)\n" \
"s_lshl_b32 s72, %[wid], 13\n" \
"v_mfma_f32_16x16x32_bf16 v[0:3], a[192:195], v[192:195], v[0:3]\n" \
"ds_read_b64_tr_b16 a[216:217], v241 offset:49664\n" \
"v_mfma_f32_16x16x32_bf16 v[0:3], a[196:199], v[208:211], v[0:3]\n" \
"v_mfma_f32_16x16x32_bf16 v[32:35], a[196:199], v[212:215], v[32:35]\n" \
"ds_read_b64_tr_b16 a[218:219], v241 offset:51712\n" \
"v_add_u32 v244, s72, v248\n" \
"v_mfma_f32_16x16x32_bf16 v[32:35], a[192:195], v[196:199], v[32:35]\n" \
"v_add_u32 v249, 0x10000, v244\n" \
"v_mov_b32 v250, 0\n" \
"v_mfma_f32_16x16x32_bf16 v[64:67], a[192:195], v[200:203], v[64:67]\n" \
"ds_read_b64_tr_b16 a[220:221], v241 offset:57856\n" \
"v_mfma_f32_16x16x32_bf16 v[64:67], a[196:199], v[216:219], v[64:67]\n" \
"v_mfma_f32_16x16x32_bf16 v[96:99], a[196:199], v[220:223], v[96:99]\n" \
"ds_read_b64_tr_b16 a[222:223], v241 offset:59904\n" \
"v_mfma_f32_16x16x32_bf16 v[96:99], a[192:195], v[204:207], v[96:99]\n" \
"s_waitcnt lgkmcnt(8)\n" \
"v_mfma_f32_16x16x32_bf16 v[4:7], a[200:203], v[192:195], v[4:7]\n" \
"s_add_u32 m0, s70, 40960\n" \
"ds_read_b64_tr_b16 a[192:193], v240 offset:50176\n" \
"v_mfma_f32_16x16x32_bf16 v[4:7], a[204:207], v[208:211], v[4:7]\n" \
"s_add_u32 s74, s69, 2883584\n" \
"v_mfma_f32_16x16x32_bf16 v[36:39], a[204:207], v[212:215], v[36:39]\n" \
"buffer_load_dwordx4 v246, s[24:27], s74 offen lds\n" \
"v_mfma_f32_16x16x32_bf16 v[36:39], a[200:203], v[196:199], v[36:39]\n" \
"ds_read_b64_tr_b16 a[194:195], v240 offset:52224\n" \
"v_mfma_f32_16x16x32_bf16 v[68:71], a[200:203], v[200:203], v[68:71]\n" \
"ds_read_b64_tr_b16 a[196:197], v240 offset:58368\n" \
"v_mfma_f32_16x16x32_bf16 v[68:71], a[204:207], v[216:219], v[68:71]\n" \
"v_mfma_f32_16x16x32_bf16 v[100:103], a[204:207], v[220:223], v[100:103]\n" \
"ds_read_b64_tr_b16 a[198:199], v240 offset:60416\n" \
"v_mfma_f32_16x16x32_bf16 v[100:103], a[200:203], v[204:207], v[100:103]\n" \
"s_waitcnt lgkmcnt(8)\n" \
"v_mfma_f32_16x16x32_bf16 v[8:11], a[208:211], v[192:195], v[8:11]\n" \
"ds_read_b64_tr_b16 a[200:201], v241 offset:50176\n" \
"v_mfma_f32_16x16x32_bf16 v[8:11], a[212:215], v[208:211], v[8:11]\n" \
"v_mfma_f32_16x16x32_bf16 v[40:43], a[212:215], v[212:215], v[40:43]\n" \
"ds_read_b64_tr_b16 a[202:203], v241 offset:52224\n" \
"v_mfma_f32_16x16x32_bf16 v[40:43], a[208:211], v[196:199], v[40:43]\n" \
"v_mfma_f32_16x16x32_bf16 v[72:75], a[208:211], v[200:203], v[72:75]\n" \
"ds_read_b64_tr_b16 a[204:205], v241 offset:58368\n" \
"v_mfma_f32_16x16x32_bf16 v[72:75], a[212:215], v[216:219], v[72:75]\n" \
"v_mfma_f32_16x16x32_bf16 v[104:107], a[212:215], v[220:223], v[104:107]\n" \
"s_add_u32 m0, s70, 41984\n" \
"ds_read_b64_tr_b16 a[206:207], v241 offset:60416\n" \
"v_mfma_f32_16x16x32_bf16 v[104:107], a[208:211], v[204:207], v[104:107]\n" \
"s_waitcnt lgkmcnt(8)\n" \
"s_add_u32 s74, s69, 2883712\n" \
"v_mfma_f32_16x16x32_bf16 v[12:15], a[216:219], v[192:195], v[12:15]\n" \
"buffer_load_dwordx4 v246, s[24:27], s74 offen lds\n" \
"v_mfma_f32_16x16x32_bf16 v[12:15], a[220:223], v[208:211], v[12:15]\n" \
"ds_read_b64_tr_b16 a[208:209], v240 offset:50688\n" \
"v_mfma_f32_16x16x32_bf16 v[44:47], a[220:223], v[212:215], v[44:47]\n" \
"ds_read_b64_tr_b16 a[210:211], v240 offset:52736\n" \
"v_mfma_f32_16x16x32_bf16 v[44:47], a[216:219], v[196:199], v[44:47]\n" \
"v_mfma_f32_16x16x32_bf16 v[76:79], a[216:219], v[200:203], v[76:79]\n" \
"ds_read_b64_tr_b16 a[212:213], v240 offset:58880\n" \
"v_mfma_f32_16x16x32_bf16 v[76:79], a[220:223], v[216:219], v[76:79]\n" \
"v_mfma_f32_16x16x32_bf16 v[108:111], a[220:223], v[220:223], v[108:111]\n" \
"ds_read_b64_tr_b16 a[214:215], v240 offset:60928\n" \
"v_mfma_f32_16x16x32_bf16 v[108:111], a[216:219], v[204:207], v[108:111]\n" \
"s_waitcnt lgkmcnt(8)\n" \
"v_mfma_f32_16x16x32_bf16 v[16:19], a[192:195], v[192:195], v[16:19]\n" \
"ds_read_b64_tr_b16 a[216:217], v241 offset:50688\n" \
"v_mfma_f32_16x16x32_bf16 v[16:19], a[196:199], v[208:211], v[16:19]\n" \
"v_mfma_f32_16x16x32_bf16 v[48:51], a[196:199], v[212:215], v[48:51]\n" \
"ds_read_b64_tr_b16 a[218:219], v241 offset:52736\n" \
"v_mfma_f32_16x16x32_bf16 v[48:51], a[192:195], v[196:199], v[48:51]\n" \
"ds_read_b64_tr_b16 a[220:221], v241 offset:58880\n" \
"v_mfma_f32_16x16x32_bf16 v[80:83], a[192:195], v[200:203], v[80:83]\n" \
"ds_read_b64_tr_b16 a[222:223], v241 offset:60928\n" \
"v_mul_f32 v0, s84, v0\n" \
"v_mfma_f32_16x16x32_bf16 v[80:83], a[196:199], v[216:219], v[80:83]\n" \
"v_mul_f32 v1, s84, v1\n" \
"v_mul_f32 v2, s84, v2\n" \
"v_mfma_f32_16x16x32_bf16 v[112:115], a[196:199], v[220:223], v[112:115]\n" \
"v_mul_f32 v3, s84, v3\n" \
"v_mul_f32 v4, s84, v4\n" \
"v_mfma_f32_16x16x32_bf16 v[112:115], a[192:195], v[204:207], v[112:115]\n" \
"s_waitcnt lgkmcnt(8)\n" \
"v_mul_f32 v5, s84, v5\n" \
"v_mul_f32 v6, s84, v6\n" \
"v_mfma_f32_16x16x32_bf16 v[20:23], a[200:203], v[192:195], v[20:23]\n" \
"v_mul_f32 v7, s84, v7\n" \
"v_cvt_pk_bf16_f32 v128, v0, v1\n" \
"v_mfma_f32_16x16x32_bf16 v[20:23], a[204:207], v[208:211], v[20:23]\n" \
"v_cvt_pk_bf16_f32 v129, v2, v3\n" \
"v_cvt_pk_bf16_f32 v130, v4, v5\n" \
"v_mfma_f32_16x16x32_bf16 v[52:55], a[204:207], v[212:215], v[52:55]\n" \
"v_cvt_pk_bf16_f32 v131, v6, v7\n" \
"v_mfma_f32_16x16x32_bf16 v[52:55], a[200:203], v[196:199], v[52:55]\n" \
"ds_write_b128 v244, v[128:131] offset:0\n" \
"v_mfma_f32_16x16x32_bf16 v[84:87], a[200:203], v[200:203], v[84:87]\n" \
"s_waitcnt vmcnt(14) lgkmcnt(1)\n" \
"s_barrier\n" \
"s_add_u32 m0, s70, 49152\n" \
"v_mfma_f32_16x16x32_bf16 v[84:87], a[204:207], v[216:219], v[84:87]\n" \
"s_add_u32 s74, s69, 3145728\n" \
"ds_read_b64_tr_b16 a[192:193], v240 offset:0\n" \
"ds_read_b64_tr_b16 a[194:195], v240 offset:2048\n" \
"v_mfma_f32_16x16x32_bf16 v[116:119], a[204:207], v[220:223], v[116:119]\n" \
"buffer_load_dwordx4 v246, s[24:27], s74 offen lds\n" \
"v_mfma_f32_16x16x32_bf16 v[116:119], a[200:203], v[204:207], v[116:119]\n" \
"ds_read_b64_tr_b16 a[196:197], v240 offset:8192\n" \
"ds_read_b64_tr_b16 a[198:199], v240 offset:10240\n" \
"v_mfma_f32_16x16x32_bf16 v[24:27], a[208:211], v[192:195], v[24:27]\n" \
"ds_read_b64_tr_b16 a[200:201], v241 offset:0\n" \
"v_mfma_f32_16x16x32_bf16 v[24:27], a[212:215], v[208:211], v[24:27]\n" \
"v_mfma_f32_16x16x32_bf16 v[56:59], a[212:215], v[212:215], v[56:59]\n" \
"ds_read_b64_tr_b16 a[202:203], v241 offset:2048\n" \
"v_mul_f32 v8, s84, v8\n" \
"v_mfma_f32_16x16x32_bf16 v[56:59], a[208:211], v[196:199], v[56:59]\n" \
"v_mul_f32 v9, s84, v9\n" \
"v_mul_f32 v10, s84, v10\n" \
"v_mfma_f32_16x16x32_bf16 v[88:91], a[208:211], v[200:203], v[88:91]\n" \
"ds_read_b64_tr_b16 a[204:205], v241 offset:8192\n" \
"v_and_b32 v252, 15, v251\n" \
"v_mfma_f32_16x16x32_bf16 v[88:91], a[212:215], v[216:219], v[88:91]\n" \
"v_lshlrev_b32 v252, 2, v252\n" \
"v_and_b32 v253, 16, v251\n" \
"v_mfma_f32_16x16x32_bf16 v[120:123], a[212:215], v[220:223], v[120:123]\n" \
"ds_read_b64_tr_b16 a[206:207], v241 offset:10240\n" \
"v_lshl_or_b32 v252, v253, 4, v252\n" \
"v_mfma_f32_16x16x32_bf16 v[120:123], a[208:211], v[204:207], v[120:123]\n" \
"v_mul_f32 v11, s84, v11\n" \
"v_mul_f32 v12, s84, v12\n" \
"v_mfma_f32_16x16x32_bf16 v[28:31], a[216:219], v[192:195], v[28:31]\n" \
"ds_read_b64_tr_b16 a[208:209], v240 offset:512\n" \
"v_mul_f32 v13, s84, v13\n" \
"v_mfma_f32_16x16x32_bf16 v[28:31], a[220:223], v[208:211], v[28:31]\n" \
"v_mul_f32 v14, s84, v14\n" \
"v_mul_f32 v15, s84, v15\n" \
"v_mfma_f32_16x16x32_bf16 v[60:63], a[220:223], v[212:215], v[60:63]\n" \
"s_add_u32 m0, s70, 50176\n" \
"ds_read_b64_tr_b16 a[210:211], v240 offset:2560\n" \
"v_cvt_pk_bf16_f32 v132, v8, v9\n" \
"v_mfma_f32_16x16x32_bf16 v[60:63], a[216:219], v[196:199], v[60:63]\n" \
"s_add_u32 s74, s69, 3145856\n" \
"v_cvt_pk_bf16_f32 v133, v10, v11\n" \
"v_cvt_pk_bf16_f32 v134, v12, v13\n" \
"v_mfma_f32_16x16x32_bf16 v[92:95], a[216:219], v[200:203], v[92:95]\n" \
"buffer_load_dwordx4 v246, s[24:27], s74 offen lds\n" \
"v_mfma_f32_16x16x32_bf16 v[92:95], a[220:223], v[216:219], v[92:95]\n" \
"ds_read_b64_tr_b16 a[212:213], v240 offset:8704\n" \
"v_cvt_pk_bf16_f32 v135, v14, v15\n" \
"v_mfma_f32_16x16x32_bf16 v[124:127], a[220:223], v[220:223], v[124:127]\n" \
"ds_read_b64_tr_b16 a[214:215], v240 offset:10752\n" \
"v_mfma_f32_16x16x32_bf16 v[124:127], a[216:219], v[204:207], v[124:127]\n" \
"s_waitcnt lgkmcnt(8)\n" \
"ds_write_b128 v244, v[132:135] offset:1024\n" \
  \
"v_mfma_f32_16x16x32_bf16 a[0:3], v[192:195], a[192:195], a[0:3]\n" \
"ds_read_b64_tr_b16 a[216:217], v241 offset:512\n" \
"v_mul_f32 v16, s84, v16\n" \
"v_mfma_f32_16x16x32_bf16 a[0:3], v[208:211], a[196:199], a[0:3]\n" \
"v_mul_f32 v17, s84, v17\n" \
"v_mul_f32 v18, s84, v18\n" \
"v_mfma_f32_16x16x32_bf16 a[4:7], v[212:215], a[196:199], a[4:7]\n" \
"ds_read_b64_tr_b16 a[218:219], v241 offset:2560\n" \
"v_mul_f32 v19, s84, v19\n" \
"v_mfma_f32_16x16x32_bf16 a[4:7], v[196:199], a[192:195], a[4:7]\n" \
"v_mul_f32 v20, s84, v20\n" \
"v_mul_f32 v21, s84, v21\n" \
"v_mfma_f32_16x16x32_bf16 a[64:67], v[200:203], a[192:195], a[64:67]\n" \
"ds_read_b64_tr_b16 a[220:221], v241 offset:8704\n" \
"v_mul_f32 v22, s84, v22\n" \
"v_mfma_f32_16x16x32_bf16 a[64:67], v[216:219], a[196:199], a[64:67]\n" \
"v_mul_f32 v23, s84, v23\n" \
"v_cvt_pk_bf16_f32 v136, v16, v17\n" \
"v_mfma_f32_16x16x32_bf16 a[68:71], v[220:223], a[196:199], a[68:71]\n" \
"ds_read_b64_tr_b16 a[222:223], v241 offset:10752\n" \
"v_cvt_pk_bf16_f32 v137, v18, v19\n" \
"v_mfma_f32_16x16x32_bf16 a[68:71], v[204:207], a[192:195], a[68:71]\n" \
"s_waitcnt lgkmcnt(9)\n" \
"v_cvt_pk_bf16_f32 v138, v20, v21\n" \
"v_cvt_pk_bf16_f32 v139, v22, v23\n" \
"v_mfma_f32_16x16x32_bf16 a[8:11], v[192:195], a[200:203], a[8:11]\n" \
"s_add_u32 m0, s70, 57344\n" \
"ds_read_b64_tr_b16 a[192:193], v240 offset:1024\n" \
"v_mfma_f32_16x16x32_bf16 a[8:11], v[208:211], a[204:207], a[8:11]\n" \
"s_add_u32 s74, s69, 3407872\n" \
"ds_write_b128 v244, v[136:139] offset:2048\n" \
"v_mfma_f32_16x16x32_bf16 a[12:15], v[212:215], a[204:207], a[12:15]\n" \
"buffer_load_dwordx4 v246, s[24:27], s74 offen lds\n" \
"v_mfma_f32_16x16x32_bf16 a[12:15], v[196:199], a[200:203], a[12:15]\n" \
"ds_read_b64_tr_b16 a[194:195], v240 offset:3072\n" \
"v_mfma_f32_16x16x32_bf16 a[72:75], v[200:203], a[200:203], a[72:75]\n" \
"ds_read_b64_tr_b16 a[196:197], v240 offset:9216\n" \
"v_mfma_f32_16x16x32_bf16 a[72:75], v[216:219], a[204:207], a[72:75]\n" \
"v_mfma_f32_16x16x32_bf16 a[76:79], v[220:223], a[204:207], a[76:79]\n" \
"ds_read_b64_tr_b16 a[198:199], v240 offset:11264\n" \
"v_mul_f32 v24, s84, v24\n" \
"v_mfma_f32_16x16x32_bf16 a[76:79], v[204:207], a[200:203], a[76:79]\n" \
"s_waitcnt lgkmcnt(10)\n" \
"v_mul_f32 v25, s84, v25\n" \
"v_mul_f32 v26, s84, v26\n" \
"v_mfma_f32_16x16x32_bf16 a[16:19], v[192:195], a[208:211], a[16:19]\n" \
"ds_read_b64_tr_b16 a[200:201], v241 offset:1024\n" \
"v_mul_f32 v27, s84, v27\n" \
"v_mfma_f32_16x16x32_bf16 a[16:19], v[208:211], a[212:215], a[16:19]\n" \
"v_mul_f32 v28, s84, v28\n" \
"v_mul_f32 v29, s84, v29\n" \
"v_mfma_f32_16x16x32_bf16 a[20:23], v[212:215], a[212:215], a[20:23]\n" \
"ds_read_b64_tr_b16 a[202:203], v241 offset:3072\n" \
"v_mul_f32 v30, s84, v30\n" \
"v_mfma_f32_16x16x32_bf16 a[20:23], v[196:199], a[208:211], a[20:23]\n" \
"v_mul_f32 v31, s84, v31\n" \
"v_cvt_pk_bf16_f32 v140, v24, v25\n" \
"v_mfma_f32_16x16x32_bf16 a[80:83], v[200:203], a[208:211], a[80:83]\n" \
"ds_read_b64_tr_b16 a[204:205], v241 offset:9216\n" \
"v_cvt_pk_bf16_f32 v141, v26, v27\n" \
"v_mfma_f32_16x16x32_bf16 a[80:83], v[216:219], a[212:215], a[80:83]\n" \
"s_add_u32 s85, s71, 0\n" \
"v_cvt_pk_bf16_f32 v142, v28, v29\n" \
"v_cvt_pk_bf16_f32 v143, v30, v31\n" \
"v_mfma_f32_16x16x32_bf16 a[84:87], v[220:223], a[212:215], a[84:87]\n" \
"s_add_u32 m0, s70, 58368\n" \
"buffer_store_dwordx4 v[128:131], v248, s[32:35], s85 offen offset:0 sc1\n" \
"v_mfma_f32_16x16x32_bf16 a[84:87], v[204:207], a[208:211], a[84:87]\n" \
"s_waitcnt lgkmcnt(8)\n" \
"s_add_u32 s74, s69, 3408000\n" \
"ds_read_b64_tr_b16 a[206:207], v241 offset:11264\n" \
"v_mfma_f32_16x16x32_bf16 a[24:27], v[192:195], a[216:219], a[24:27]\n" \
"s_lshl_b32 s81, s67, 3\n" \
"ds_read_b64_tr_b16 a[208:209], v240 offset:1536\n" \
"v_mfma_f32_16x16x32_bf16 a[24:27], v[208:211], a[220:223], a[24:27]\n" \
"s_add_u32 s81, s81, %[wid]\n" \
"buffer_load_dwordx4 v246, s[24:27], s74 offen lds\n" \
"v_mfma_f32_16x16x32_bf16 a[28:31], v[212:215], a[220:223], a[28:31]\n" \
"s_lshl_b32 s81, s81, 6\n" \
"ds_read_b64_tr_b16 a[210:211], v240 offset:3584\n" \
"v_mfma_f32_16x16x32_bf16 a[28:31], v[196:199], a[216:219], a[28:31]\n" \
"s_add_u32 s81, s81, 90112\n" \
"ds_write_b128 v244, v[140:143] offset:3072\n" \
"v_mfma_f32_16x16x32_bf16 a[88:91], v[200:203], a[216:219], a[88:91]\n" \
"buffer_load_dword v247, v252, s[32:35], s81 offen sc1\n" \
"v_mul_f32 v32, s84, v32\n" \
"v_mfma_f32_16x16x32_bf16 a[88:91], v[216:219], a[220:223], a[88:91]\n" \
"ds_read_b64_tr_b16 a[212:213], v240 offset:9728\n" \
"v_mul_f32 v33, s84, v33\n" \
"v_mfma_f32_16x16x32_bf16 a[92:95], v[220:223], a[220:223], a[92:95]\n" \
"ds_read_b64_tr_b16 a[214:215], v240 offset:11776\n" \
"v_mul_f32 v34, s84, v34\n" \
"v_mfma_f32_16x16x32_bf16 a[92:95], v[204:207], a[216:219], a[92:95]\n" \
"s_waitcnt lgkmcnt(9)\n" \
"v_mul_f32 v35, s84, v35\n" \
"v_mul_f32 v36, s84, v36\n" \
"v_mfma_f32_16x16x32_bf16 a[32:35], v[192:195], a[192:195], a[32:35]\n" \
"ds_read_b64_tr_b16 a[216:217], v241 offset:1536\n" \
"v_mul_f32 v37, s84, v37\n" \
"v_mfma_f32_16x16x32_bf16 a[32:35], v[208:211], a[196:199], a[32:35]\n" \
"v_mul_f32 v38, s84, v38\n" \
"v_mul_f32 v39, s84, v39\n" \
"v_mfma_f32_16x16x32_bf16 a[36:39], v[212:215], a[196:199], a[36:39]\n" \
"ds_read_b64_tr_b16 a[218:219], v241 offset:3584\n" \
"v_cvt_pk_bf16_f32 v144, v32, v33\n" \
"v_mfma_f32_16x16x32_bf16 a[36:39], v[196:199], a[192:195], a[36:39]\n" \
"ds_read_b64_tr_b16 a[220:221], v241 offset:9728\n" \
"v_cvt_pk_bf16_f32 v145, v34, v35\n" \
"v_mfma_f32_16x16x32_bf16 a[96:99], v[200:203], a[192:195], a[96:99]\n" \
"ds_read_b64_tr_b16 a[222:223], v241 offset:11776\n" \
"v_cvt_pk_bf16_f32 v146, v36, v37\n" \
"v_mfma_f32_16x16x32_bf16 a[96:99], v[216:219], a[196:199], a[96:99]\n" \
"v_cvt_pk_bf16_f32 v147, v38, v39\n" \
"v_mfma_f32_16x16x32_bf16 a[100:103], v[220:223], a[196:199], a[100:103]\n" \
"ds_write_b128 v244, v[144:147] offset:4096\n" \
"v_mfma_f32_16x16x32_bf16 a[100:103], v[204:207], a[192:195], a[100:103]\n" \
"s_waitcnt lgkmcnt(10)\n" \
"v_mfma_f32_16x16x32_bf16 a[40:43], v[192:195], a[200:203], a[40:43]\n" \
"v_mfma_f32_16x16x32_bf16 a[40:43], v[208:211], a[204:207], a[40:43]\n" \
"v_mfma_f32_16x16x32_bf16 a[44:47], v[212:215], a[204:207], a[44:47]\n" \
"v_mul_f32 v40, s84, v40\n" \
"v_mul_f32 v41, s84, v41\n" \
"v_mfma_f32_16x16x32_bf16 a[44:47], v[196:199], a[200:203], a[44:47]\n" \
"v_mul_f32 v42, s84, v42\n" \
"v_mul_f32 v43, s84, v43\n" \
"v_mfma_f32_16x16x32_bf16 a[104:107], v[200:203], a[200:203], a[104:107]\n" \
"s_waitcnt vmcnt(12) lgkmcnt(1)\n" \
"s_barrier\n" \
"v_mul_f32 v44, s84, v44\n" \
"v_mfma_f32_16x16x32_bf16 a[104:107], v[216:219], a[204:207], a[104:107]\n" \
"s_add_u32 m0, s70, 65536\n" \
"ds_read_b64_tr_b16 a[192:193], v240 offset:16384\n" \
"ds_read_b64_tr_b16 a[194:195], v240 offset:18432\n" \
"v_mfma_f32_16x16x32_bf16 a[108:111], v[220:223], a[204:207], a[108:111]\n" \
"s_add_u32 s74, s69, 0\n" \
"buffer_load_dwordx4 v246, s[28:31], s74 offen lds\n" \
"v_mfma_f32_16x16x32_bf16 a[108:111], v[204:207], a[200:203], a[108:111]\n" \
"ds_read_b64_tr_b16 a[196:197], v240 offset:24576\n" \
"ds_read_b64_tr_b16 a[198:199], v240 offset:26624\n" \
"v_mfma_f32_16x16x32_bf16 a[48:51], v[192:195], a[208:211], a[48:51]\n" \
"ds_read_b64_tr_b16 a[200:201], v241 offset:16384\n" \
"v_mul_f32 v45, s84, v45\n" \
"v_mfma_f32_16x16x32_bf16 a[48:51], v[208:211], a[212:215], a[48:51]\n" \
"s_add_u32 s85, s71, 0\n" \
"v_mul_f32 v46, s84, v46\n" \
"v_mul_f32 v47, s84, v47\n" \
"v_mfma_f32_16x16x32_bf16 a[52:55], v[212:215], a[212:215], a[52:55]\n" \
"buffer_store_dwordx4 v[132:135], v248, s[32:35], s85 offen offset:1024 sc1\n" \
"v_mfma_f32_16x16x32_bf16 a[52:55], v[196:199], a[208:211], a[52:55]\n" \
"ds_read_b64_tr_b16 a[202:203], v241 offset:18432\n" \
"v_cvt_pk_bf16_f32 v148, v40, v41\n" \
"v_mfma_f32_16x16x32_bf16 a[112:115], v[200:203], a[208:211], a[112:115]\n" \
"ds_read_b64_tr_b16 a[204:205], v241 offset:24576\n" \
"v_cvt_pk_bf16_f32 v149, v42, v43\n" \
"v_mfma_f32_16x16x32_bf16 a[112:115], v[216:219], a[212:215], a[112:115]\n" \
"v_cvt_pk_bf16_f32 v150, v44, v45\n" \
"v_cvt_pk_bf16_f32 v151, v46, v47\n" \
"v_mfma_f32_16x16x32_bf16 a[116:119], v[220:223], a[212:215], a[116:119]\n" \
"ds_read_b64_tr_b16 a[206:207], v241 offset:26624\n" \
"v_mfma_f32_16x16x32_bf16 a[116:119], v[204:207], a[208:211], a[116:119]\n" \
"ds_write_b128 v244, v[148:151] offset:5120\n" \
"v_mfma_f32_16x16x32_bf16 a[56:59], v[192:195], a[216:219], a[56:59]\n" \
"ds_read_b64_tr_b16 a[208:209], v240 offset:16896\n" \
"v_mul_f32 v48, s84, v48\n" \
"v_mfma_f32_16x16x32_bf16 a[56:59], v[208:211], a[220:223], a[56:59]\n" \
"v_mul_f32 v49, s84, v49\n" \
"v_mul_f32 v50, s84, v50\n" \
"v_mfma_f32_16x16x32_bf16 a[60:63], v[212:215], a[220:223], a[60:63]\n" \
"s_add_u32 m0, s70, 66560\n" \
"ds_read_b64_tr_b16 a[210:211], v240 offset:18944\n" \
"v_mul_f32 v51, s84, v51\n" \
"v_mfma_f32_16x16x32_bf16 a[60:63], v[196:199], a[216:219], a[60:63]\n" \
"s_add_u32 s74, s69, 128\n" \
"v_mul_f32 v52, s84, v52\n" \
"v_mul_f32 v53, s84, v53\n" \
"v_mfma_f32_16x16x32_bf16 a[120:123], v[200:203], a[216:219], a[120:123]\n" \
"buffer_load_dwordx4 v246, s[28:31], s74 offen lds\n" \
"v_mfma_f32_16x16x32_bf16 a[120:123], v[216:219], a[220:223], a[120:123]\n" \
"ds_read_b64_tr_b16 a[212:213], v240 offset:25088\n" \
"v_mul_f32 v54, s84, v54\n" \
"v_mfma_f32_16x16x32_bf16 a[124:127], v[220:223], a[220:223], a[124:127]\n" \
"ds_read_b64_tr_b16 a[214:215], v240 offset:27136\n" \
"v_mul_f32 v55, s84, v55\n" \
"v_mfma_f32_16x16x32_bf16 a[124:127], v[204:207], a[216:219], a[124:127]\n" \
"s_waitcnt lgkmcnt(9)\n" \
"v_cvt_pk_bf16_f32 v152, v48, v49\n" \
"v_cvt_pk_bf16_f32 v153, v50, v51\n" \
"v_mfma_f32_16x16x32_bf16 a[0:3], a[160:163], a[192:195], a[0:3]\n" \
"ds_read_b64_tr_b16 a[216:217], v241 offset:16896\n" \
"v_cvt_pk_bf16_f32 v154, v52, v53\n" \
"v_mfma_f32_16x16x32_bf16 a[0:3], a[176:179], a[196:199], a[0:3]\n" \
"v_cvt_pk_bf16_f32 v155, v54, v55\n" \
"v_mfma_f32_16x16x32_bf16 a[4:7], a[180:183], a[196:199], a[4:7]\n" \
"ds_read_b64_tr_b16 a[218:219], v241 offset:18944\n" \
"v_mfma_f32_16x16x32_bf16 a[4:7], a[164:167], a[192:195], a[4:7]\n" \
"ds_write_b128 v244, v[152:155] offset:6144\n" \
"v_mfma_f32_16x16x32_bf16 a[64:67], a[168:171], a[192:195], a[64:67]\n" \
"ds_read_b64_tr_b16 a[220:221], v241 offset:25088\n" \
"v_mfma_f32_16x16x32_bf16 a[64:67], a[184:187], a[196:199], a[64:67]\n" \
"v_mfma_f32_16x16x32_bf16 a[68:71], a[188:191], a[196:199], a[68:71]\n" \
"ds_read_b64_tr_b16 a[222:223], v241 offset:27136\n" \
"v_mul_f32 v56, s84, v56\n" \
"v_mfma_f32_16x16x32_bf16 a[68:71], a[172:175], a[192:195], a[68:71]\n" \
"s_waitcnt lgkmcnt(10)\n" \
"v_mul_f32 v57, s84, v57\n" \
"v_mul_f32 v58, s84, v58\n" \
"v_mfma_f32_16x16x32_bf16 a[8:11], a[160:163], a[200:203], a[8:11]\n" \
"s_add_u32 m0, s70, 73728\n" \
"ds_read_b64_tr_b16 a[192:193], v240 offset:17408\n" \
"v_mul_f32 v59, s84, v59\n" \
"v_mfma_f32_16x16x32_bf16 a[8:11], a[176:179], a[204:207], a[8:11]\n" \
"s_add_u32 s74, s69, 262144\n" \
"v_mul_f32 v60, s84, v60\n" \
"v_mul_f32 v61, s84, v61\n" \
"v_mfma_f32_16x16x32_bf16 a[12:15], a[180:183], a[204:207], a[12:15]\n" \
"buffer_load_dwordx4 v246, s[28:31], s74 offen lds\n" \
"v_mfma_f32_16x16x32_bf16 a[12:15], a[164:167], a[200:203], a[12:15]\n" \
"ds_read_b64_tr_b16 a[194:195], v240 offset:19456\n" \
"v_mul_f32 v62, s84, v62\n" \
"v_mfma_f32_16x16x32_bf16 a[72:75], a[168:171], a[200:203], a[72:75]\n" \
"ds_read_b64_tr_b16 a[196:197], v240 offset:25600\n" \
"v_mul_f32 v63, s84, v63\n" \
"v_mfma_f32_16x16x32_bf16 a[72:75], a[184:187], a[204:207], a[72:75]\n" \
"s_add_u32 s85, s71, 0\n" \
"v_cvt_pk_bf16_f32 v156, v56, v57\n" \
"v_cvt_pk_bf16_f32 v157, v58, v59\n" \
"v_mfma_f32_16x16x32_bf16 a[76:79], a[188:191], a[204:207], a[76:79]\n" \
"buffer_store_dwordx4 v[136:139], v248, s[32:35], s85 offen offset:2048 sc1\n" \
"v_mfma_f32_16x16x32_bf16 a[76:79], a[172:175], a[200:203], a[76:79]\n" \
"s_waitcnt lgkmcnt(8)\n" \
"ds_read_b64_tr_b16 a[198:199], v240 offset:27648\n" \
"v_cvt_pk_bf16_f32 v158, v60, v61\n" \
"v_mfma_f32_16x16x32_bf16 a[16:19], a[160:163], a[208:211], a[16:19]\n" \
"ds_read_b64_tr_b16 a[200:201], v241 offset:17408\n" \
"v_cvt_pk_bf16_f32 v159, v62, v63\n" \
"v_mfma_f32_16x16x32_bf16 a[16:19], a[176:179], a[212:215], a[16:19]\n" \
"ds_write_b128 v244, v[156:159] offset:7168\n" \
"v_mfma_f32_16x16x32_bf16 a[20:23], a[180:183], a[212:215], a[20:23]\n" \
"ds_read_b64_tr_b16 a[202:203], v241 offset:19456\n" \
"v_mfma_f32_16x16x32_bf16 a[20:23], a[164:167], a[208:211], a[20:23]\n" \
"v_mfma_f32_16x16x32_bf16 a[80:83], a[168:171], a[208:211], a[80:83]\n" \
"s_waitcnt vmcnt(5)\n" \
"ds_read_b64_tr_b16 a[204:205], v241 offset:25600\n" \
"v_cmp_eq_u32 vcc, 0, v247\n" \
"v_mfma_f32_16x16x32_bf16 a[80:83], a[184:187], a[212:215], a[80:83]\n" \
"v_mul_f32 v64, s84, v64\n" \
"v_mul_f32 v65, s84, v65\n" \
"v_mfma_f32_16x16x32_bf16 a[84:87], a[188:191], a[212:215], a[84:87]\n" \
"s_add_u32 m0, s70, 74752\n" \
"ds_read_b64_tr_b16 a[206:207], v241 offset:27648\n" \
"v_mul_f32 v66, s84, v66\n" \
"v_mfma_f32_16x16x32_bf16 a[84:87], a[172:175], a[208:211], a[84:87]\n" \
"s_waitcnt lgkmcnt(9)\n" \
"s_cbranch_vccnz .Lsph_%=\n" \
".Lsbh_%=:\n" \
"v_mfma_f32_16x16x32_bf16 a[24:27], a[160:163], a[216:219], a[24:27]\n" \
"s_add_u32 s74, s69, 262272\n" \
"buffer_load_dwordx4 v246, s[28:31], s74 offen lds\n" \
"v_mfma_f32_16x16x32_bf16 a[24:27], a[176:179], a[220:223], a[24:27]\n" \
"ds_read_b64_tr_b16 a[208:209], v240 offset:17920\n" \
"v_mul_f32 v67, s84, v67\n" \
"v_mfma_f32_16x16x32_bf16 a[28:31], a[180:183], a[220:223], a[28:31]\n" \
"buffer_load_dwordx4 v[192:195], v248, s[32:35], s79 offen offset:0 sc1\n" \
"v_mul_f32 v68, s84, v68\n" \
"v_mfma_f32_16x16x32_bf16 a[28:31], a[164:167], a[216:219], a[28:31]\n" \
"ds_read_b64_tr_b16 a[210:211], v240 offset:19968\n" \
"v_mul_f32 v69, s84, v69\n" \
"v_mfma_f32_16x16x32_bf16 a[88:91], a[168:171], a[216:219], a[88:91]\n" \
"ds_read_b64_tr_b16 a[212:213], v240 offset:26112\n" \
"v_mul_f32 v70, s84, v70\n" \
"v_mfma_f32_16x16x32_bf16 a[88:91], a[184:187], a[220:223], a[88:91]\n" \
"v_mul_f32 v71, s84, v71\n" \
"v_cvt_pk_bf16_f32 v160, v64, v65\n" \
"v_mfma_f32_16x16x32_bf16 a[92:95], a[188:191], a[220:223], a[92:95]\n" \
"ds_read_b64_tr_b16 a[214:215], v240 offset:28160\n" \
"v_cvt_pk_bf16_f32 v161, v66, v67\n" \
"v_mfma_f32_16x16x32_bf16 a[92:95], a[172:175], a[216:219], a[92:95]\n" \
"s_waitcnt lgkmcnt(9)\n" \
"v_cvt_pk_bf16_f32 v162, v68, v69\n" \
"v_cvt_pk_bf16_f32 v163, v70, v71\n" \
"v_mfma_f32_16x16x32_bf16 a[32:35], a[160:163], a[192:195], a[32:35]\n" \
"ds_read_b64_tr_b16 a[216:217], v241 offset:17920\n" \
"v_xor_b32 v244, 0x4000, v244\n" \
"v_mfma_f32_16x16x32_bf16 a[32:35], a[176:179], a[196:199], a[32:35]\n" \
"ds_write_b128 v244, v[160:163] offset:32768\n" \
"v_mfma_f32_16x16x32_bf16 a[36:39], a[180:183], a[196:199], a[36:39]\n" \
"buffer_load_dwordx4 v[196:199], v248, s[32:35], s80 offen offset:0 sc1\n" \
"v_mul_f32 v72, s84, v72\n" \
"v_mfma_f32_16x16x32_bf16 a[36:39], a[164:167], a[192:195], a[36:39]\n" \
"ds_read_b64_tr_b16 a[218:219], v241 offset:19968\n" \
"ds_read_b64_tr_b16 a[220:221], v241 offset:26112\n" \
"v_mfma_f32_16x16x32_bf16 a[96:99], a[168:171], a[192:195], a[96:99]\n" \
"ds_read_b64_tr_b16 a[222:223], v241 offset:28160\n" \
"v_mul_f32 v73, s84, v73\n" \
"v_mfma_f32_16x16x32_bf16 a[96:99], a[184:187], a[196:199], a[96:99]\n" \
"v_mul_f32 v74, s84, v74\n" \
"v_mul_f32 v75, s84, v75\n" \
"v_mfma_f32_16x16x32_bf16 a[100:103], a[188:191], a[196:199], a[100:103]\n" \
"v_mul_f32 v76, s84, v76\n" \
"v_mul_f32 v77, s84, v77\n" \
"v_mfma_f32_16x16x32_bf16 a[100:103], a[172:175], a[192:195], a[100:103]\n" \
"s_waitcnt lgkmcnt(9)\n" \
"v_mul_f32 v78, s84, v78\n" \
"v_mul_f32 v79, s84, v79\n" \
"v_mfma_f32_16x16x32_bf16 a[40:43], a[160:163], a[200:203], a[40:43]\n" \
"v_cvt_pk_bf16_f32 v164, v72, v73\n" \
"v_cvt_pk_bf16_f32 v165, v74, v75\n" \
"v_mfma_f32_16x16x32_bf16 a[40:43], a[176:179], a[204:207], a[40:43]\n" \
"s_add_u32 s85, s71, 0\n" \
"v_cvt_pk_bf16_f32 v166, v76, v77\n" \
"v_cvt_pk_bf16_f32 v167, v78, v79\n" \
"v_mfma_f32_16x16x32_bf16 a[44:47], a[180:183], a[204:207], a[44:47]\n" \
"buffer_load_dwordx4 v[200:203], v248, s[32:35], s36 offen offset:0 sc1\n" \
"v_mfma_f32_16x16x32_bf16 a[44:47], a[164:167], a[200:203], a[44:47]\n" \
"ds_write_b128 v244, v[164:167] offset:33792\n" \
"v_mfma_f32_16x16x32_bf16 a[104:107], a[168:171], a[200:203], a[104:107]\n" \
"s_waitcnt lgkmcnt(1)\n" \
"s_barrier\n" \
"s_add_u32 m0, s70, 81920\n" \
"v_mfma_f32_16x16x32_bf16 a[104:107], a[184:187], a[204:207], a[104:107]\n" \
"s_add_u32 s74, s69, 524288\n" \
"buffer_store_dwordx4 v[140:143], v248, s[32:35], s85 offen offset:3072 sc1\n" \
"v_mfma_f32_16x16x32_bf16 a[108:111], a[188:191], a[204:207], a[108:111]\n" \
"ds_read_b64_tr_b16 a[192:193], v240 offset:32768\n" \
"ds_read_b64_tr_b16 a[194:195], v240 offset:34816\n" \
"v_mfma_f32_16x16x32_bf16 a[108:111], a[172:175], a[200:203], a[108:111]\n" \
"ds_read_b64_tr_b16 a[196:197], v240 offset:40960\n" \
"ds_read_b64_tr_b16 a[198:199], v240 offset:43008\n" \
"v_mfma_f32_16x16x32_bf16 a[48:51], a[160:163], a[208:211], a[48:51]\n" \
"buffer_load_dwordx4 v246, s[28:31], s74 offen lds\n" \
"v_mfma_f32_16x16x32_bf16 a[48:51], a[176:179], a[212:215], a[48:51]\n" \
"ds_read_b64_tr_b16 a[200:201], v241 offset:32768\n" \
"v_mul_f32 v80, s84, v80\n" \
"v_mfma_f32_16x16x32_bf16 a[52:55], a[180:183], a[212:215], a[52:55]\n" \
"buffer_load_dwordx4 v[204:207], v248, s[32:35], s37 offen offset:0 sc1\n" \
"v_mul_f32 v81, s84, v81\n" \
"v_mfma_f32_16x16x32_bf16 a[52:55], a[164:167], a[208:211], a[52:55]\n" \
"ds_read_b64_tr_b16 a[202:203], v241 offset:34816\n" \
"v_mul_f32 v82, s84, v82\n" \
"v_mfma_f32_16x16x32_bf16 a[112:115], a[168:171], a[208:211], a[112:115]\n" \
"ds_read_b64_tr_b16 a[204:205], v241 offset:40960\n" \
"v_mul_f32 v83, s84, v83\n" \
"v_mfma_f32_16x16x32_bf16 a[112:115], a[184:187], a[212:215], a[112:115]\n" \
"v_mul_f32 v84, s84, v84\n" \
"v_mul_f32 v85, s84, v85\n" \
"v_mfma_f32_16x16x32_bf16 a[116:119], a[188:191], a[212:215], a[116:119]\n" \
"ds_read_b64_tr_b16 a[206:207], v241 offset:43008\n" \
"v_mul_f32 v86, s84, v86\n" \
"v_mfma_f32_16x16x32_bf16 a[116:119], a[172:175], a[208:211], a[116:119]\n" \
"v_mul_f32 v87, s84, v87\n" \
"v_cvt_pk_bf16_f32 v168, v80, v81\n" \
"v_mfma_f32_16x16x32_bf16 a[56:59], a[160:163], a[216:219], a[56:59]\n" \
"ds_read_b64_tr_b16 a[208:209], v240 offset:33280\n" \
"v_cvt_pk_bf16_f32 v169, v82, v83\n" \
"v_mfma_f32_16x16x32_bf16 a[56:59], a[176:179], a[220:223], a[56:59]\n" \
"v_cvt_pk_bf16_f32 v170, v84, v85\n" \
"v_cvt_pk_bf16_f32 v171, v86, v87\n" \
"v_mfma_f32_16x16x32_bf16 a[60:63], a[180:183], a[220:223], a[60:63]\n" \
"s_add_u32 m0, s70, 82944\n" \
"ds_read_b64_tr_b16 a[210:211], v240 offset:35328\n" \
"v_mfma_f32_16x16x32_bf16 a[60:63], a[164:167], a[216:219], a[60:63]\n" \
"s_add_u32 s74, s69, 524416\n" \
"ds_write_b128 v244, v[168:171] offset:34816\n" \
"v_mfma_f32_16x16x32_bf16 a[120:123], a[168:171], a[216:219], a[120:123]\n" \
"buffer_load_dwordx4 v246, s[28:31], s74 offen lds\n" \
"v_mfma_f32_16x16x32_bf16 a[120:123], a[184:187], a[220:223], a[120:123]\n" \
"ds_read_b64_tr_b16 a[212:213], v240 offset:41472\n" \
"v_mfma_f32_16x16x32_bf16 a[124:127], a[188:191], a[220:223], a[124:127]\n" \
"ds_read_b64_tr_b16 a[214:215], v240 offset:43520\n" \
"v_mul_f32 v88, s84, v88\n" \
"v_mfma_f32_16x16x32_bf16 a[124:127], a[172:175], a[216:219], a[124:127]\n" \
"s_waitcnt lgkmcnt(9)\n" \
"v_mul_f32 v89, s84, v89\n" \
"v_mul_f32 v90, s84, v90\n" \
"v_mfma_f32_16x16x32_bf16 a[0:3], a[224:227], a[192:195], a[0:3]\n" \
".Lst0_4_%=:\n" \
"s_cbranch_execz .Lst0_8_%=\n" \
"ds_read_b64_tr_b16 a[216:217], v241 offset:33280\n" \
"v_mul_f32 v91, s84, v91\n" \
"v_mfma_f32_16x16x32_bf16 a[0:3], a[240:243], a[196:199], a[0:3]\n" \
"buffer_load_dwordx4 a[160:163], v248, s[32:35], s79 offen offset:1024 sc1\n" \
"v_mul_f32 v92, s84, v92\n" \
"v_mfma_f32_16x16x32_bf16 a[4:7], a[244:247], a[196:199], a[4:7]\n" \
"ds_read_b64_tr_b16 a[218:219], v241 offset:35328\n" \
"v_mul_f32 v93, s84, v93\n" \
"v_mfma_f32_16x16x32_bf16 a[4:7], a[228:231], a[192:195], a[4:7]\n" \
"v_mul_f32 v94, s84, v94\n" \
"v_mul_f32 v95, s84, v95\n" \
"v_mfma_f32_16x16x32_bf16 a[64:67], a[232:235], a[192:195], a[64:67]\n" \
"ds_read_b64_tr_b16 a[220:221], v241 offset:41472\n" \
"v_cvt_pk_bf16_f32 v172, v88, v89\n" \
"v_mfma_f32_16x16x32_bf16 a[64:67], a[248:251], a[196:199], a[64:67]\n" \
"s_add_u32 s85, s71, 4096\n" \
"v_cvt_pk_bf16_f32 v173, v90, v91\n" \
"v_cvt_pk_bf16_f32 v174, v92, v93\n" \
"v_mfma_f32_16x16x32_bf16 a[68:71], a[252:255], a[196:199], a[68:71]\n" \
"buffer_store_dwordx4 v[144:147], v248, s[32:35], s85 offen offset:0 sc1\n" \
"v_mfma_f32_16x16x32_bf16 a[68:71], a[236:239], a[192:195], a[68:71]\n" \
"s_waitcnt lgkmcnt(8)\n" \
"ds_read_b64_tr_b16 a[222:223], v241 offset:43520\n" \
"v_cvt_pk_bf16_f32 v175, v94, v95\n" \
"v_mfma_f32_16x16x32_bf16 a[8:11], a[224:227], a[200:203], a[8:11]\n" \
"ds_read_b64_tr_b16 a[192:193], v240 offset:33792\n" \
"v_mfma_f32_16x16x32_bf16 a[8:11], a[240:243], a[204:207], a[8:11]\n" \
"buffer_load_dwordx4 a[164:167], v248, s[32:35], s80 offen offset:1024 sc1\n" \
"v_mfma_f32_16x16x32_bf16 a[12:15], a[244:247], a[204:207], a[12:15]\n" \
"s_add_u32 m0, s70, 90112\n" \
"s_add_u32 s74, s69, 786432\n" \
"ds_read_b64_tr_b16 a[194:195], v240 offset:35840\n" \
"v_mfma_f32_16x16x32_bf16 a[12:15], a[228:231], a[200:203], a[12:15]\n" \
"ds_write_b128 v244, v[172:175] offset:35840\n" \
"v_mfma_f32_16x16x32_bf16 a[72:75], a[232:235], a[200:203], a[72:75]\n" \
"buffer_load_dwordx4 v246, s[28:31], s74 offen lds\n" \
"v_mfma_f32_16x16x32_bf16 a[72:75], a[248:251], a[204:207], a[72:75]\n" \
"ds_read_b64_tr_b16 a[196:197], v240 offset:41984\n" \
"v_mul_f32 v96, s84, v96\n" \
"v_mfma_f32_16x16x32_bf16 a[76:79], a[252:255], a[204:207], a[76:79]\n" \
"ds_read_b64_tr_b16 a[198:199], v240 offset:44032\n" \
"v_mul_f32 v97, s84, v97\n" \
"v_mfma_f32_16x16x32_bf16 a[76:79], a[236:239], a[200:203], a[76:79]\n" \
"s_waitcnt lgkmcnt(9)\n" \
"v_mul_f32 v98, s84, v98\n" \
"v_mul_f32 v99, s84, v99\n" \
"v_mfma_f32_16x16x32_bf16 a[16:19], a[224:227], a[208:211], a[16:19]\n" \
"ds_read_b64_tr_b16 a[200:201], v241 offset:33792\n" \
"v_mul_f32 v100, s84, v100\n" \
"v_mfma_f32_16x16x32_bf16 a[16:19], a[240:243], a[212:215], a[16:19]\n" \
"buffer_load_dwordx4 a[168:171], v248, s[32:35], s36 offen offset:1024 sc1\n" \
"v_mul_f32 v101, s84, v101\n" \
"v_mfma_f32_16x16x32_bf16 a[20:23], a[244:247], a[212:215], a[20:23]\n" \
"ds_read_b64_tr_b16 a[202:203], v241 offset:35840\n" \
"v_mul_f32 v102, s84, v102\n" \
"v_mfma_f32_16x16x32_bf16 a[20:23], a[228:231], a[208:211], a[20:23]\n" \
"v_mul_f32 v103, s84, v103\n" \
"v_cvt_pk_bf16_f32 v176, v96, v97\n" \
"v_mfma_f32_16x16x32_bf16 a[80:83], a[232:235], a[208:211], a[80:83]\n" \
"ds_read_b64_tr_b16 a[204:205], v241 offset:41984\n" \
"v_cvt_pk_bf16_f32 v177, v98, v99\n" \
"v_mfma_f32_16x16x32_bf16 a[80:83], a[248:251], a[212:215], a[80:83]\n" \
"v_cvt_pk_bf16_f32 v178, v100, v101\n" \
"v_cvt_pk_bf16_f32 v179, v102, v103\n" \
"v_mfma_f32_16x16x32_bf16 a[84:87], a[252:255], a[212:215], a[84:87]\n" \
"s_add_u32 m0, s70, 91136\n" \
"ds_read_b64_tr_b16 a[206:207], v241 offset:44032\n" \
"v_mfma_f32_16x16x32_bf16 a[84:87], a[236:239], a[208:211], a[84:87]\n" \
"s_waitcnt lgkmcnt(9)\n" \
"s_add_u32 s74, s69, 786560\n" \
"ds_write_b128 v244, v[176:179] offset:36864\n" \
"v_mfma_f32_16x16x32_bf16 a[24:27], a[224:227], a[216:219], a[24:27]\n" \
"buffer_load_dwordx4 v246, s[28:31], s74 offen lds\n" \
"v_mfma_f32_16x16x32_bf16 a[24:27], a[240:243], a[220:223], a[24:27]\n" \
"buffer_load_dwordx4 a[172:175], v248, s[32:35], s37 offen offset:1024 sc1\n" \
"v_mfma_f32_16x16x32_bf16 a[28:31], a[244:247], a[220:223], a[28:31]\n" \
"buffer_load_dwordx4 a[176:179], v248, s[32:35], s79 offen offset:2048 sc1\n" \
"v_mul_f32 v104, s84, v104\n" \
"v_mfma_f32_16x16x32_bf16 a[28:31], a[228:231], a[216:219], a[28:31]\n" \
"ds_read_b64_tr_b16 a[208:209], v240 offset:34304\n" \
"ds_read_b64_tr_b16 a[210:211], v240 offset:36352\n" \
"v_mfma_f32_16x16x32_bf16 a[88:91], a[232:235], a[216:219], a[88:91]\n" \
"ds_read_b64_tr_b16 a[212:213], v240 offset:42496\n" \
"v_mul_f32 v105, s84, v105\n" \
"v_mfma_f32_16x16x32_bf16 a[88:91], a[248:251], a[220:223], a[88:91]\n" \
"v_mul_f32 v106, s84, v106\n" \
"v_mul_f32 v107, s84, v107\n" \
"v_mfma_f32_16x16x32_bf16 a[92:95], a[252:255], a[220:223], a[92:95]\n" \
"ds_read_b64_tr_b16 a[214:215], v240 offset:44544\n" \
"v_mul_f32 v108, s84, v108\n" \
"v_mfma_f32_16x16x32_bf16 a[92:95], a[236:239], a[216:219], a[92:95]\n" \
"s_waitcnt lgkmcnt(9)\n" \
"v_mul_f32 v109, s84, v109\n" \
"v_mul_f32 v110, s84, v110\n" \
"v_mfma_f32_16x16x32_bf16 a[32:35], a[224:227], a[192:195], a[32:35]\n" \
"ds_read_b64_tr_b16 a[216:217], v241 offset:34304\n" \
"v_mul_f32 v111, s84, v111\n" \
"v_mfma_f32_16x16x32_bf16 a[32:35], a[240:243], a[196:199], a[32:35]\n" \
"s_add_u32 s85, s71, 4096\n" \
"v_cvt_pk_bf16_f32 v180, v104, v105\n" \
"v_cvt_pk_bf16_f32 v181, v106, v107\n" \
"v_mfma_f32_16x16x32_bf16 a[36:39], a[244:247], a[196:199], a[36:39]\n" \
"buffer_load_dwordx4 a[180:183], v248, s[32:35], s80 offen offset:2048 sc1\n" \
"v_cvt_pk_bf16_f32 v182, v108, v109\n" \
"v_mfma_f32_16x16x32_bf16 a[36:39], a[228:231], a[192:195], a[36:39]\n" \
"ds_read_b64_tr_b16 a[218:219], v241 offset:36352\n" \
"ds_read_b64_tr_b16 a[220:221], v241 offset:42496\n" \
"v_mfma_f32_16x16x32_bf16 a[96:99], a[232:235], a[192:195], a[96:99]\n" \
"ds_read_b64_tr_b16 a[222:223], v241 offset:44544\n" \
"v_cvt_pk_bf16_f32 v183, v110, v111\n" \
"v_mfma_f32_16x16x32_bf16 a[96:99], a[248:251], a[196:199], a[96:99]\n" \
"buffer_store_dwordx4 v[148:151], v248, s[32:35], s85 offen offset:1024 sc1\n" \
"v_mfma_f32_16x16x32_bf16 a[100:103], a[252:255], a[196:199], a[100:103]\n" \
"ds_write_b128 v244, v[180:183] offset:37888\n" \
"v_mfma_f32_16x16x32_bf16 a[100:103], a[236:239], a[192:195], a[100:103]\n" \
"s_waitcnt lgkmcnt(10)\n" \
"v_mfma_f32_16x16x32_bf16 a[40:43], a[224:227], a[200:203], a[40:43]\n" \
"v_mul_f32 v112, s84, v112\n" \
"v_mul_f32 v113, s84, v113\n" \
"v_mfma_f32_16x16x32_bf16 a[40:43], a[240:243], a[204:207], a[40:43]\n" \
"v_mul_f32 v114, s84, v114\n" \
"v_mul_f32 v115, s84, v115\n" \
"v_mfma_f32_16x16x32_bf16 a[44:47], a[244:247], a[204:207], a[44:47]\n" \
"buffer_load_dwordx4 a[184:187], v248, s[32:35], s36 offen offset:2048 sc1\n" \
"v_mul_f32 v116, s84, v116\n" \
"v_mfma_f32_16x16x32_bf16 a[44:47], a[228:231], a[200:203], a[44:47]\n" \
"v_mul_f32 v117, s84, v117\n" \
"v_mul_f32 v118, s84, v118\n" \
"v_mfma_f32_16x16x32_bf16 a[104:107], a[232:235], a[200:203], a[104:107]\n" \
"s_waitcnt lgkmcnt(1)\n" \
"s_barrier\n" \
"v_mul_f32 v119, s84, v119\n" \
"v_mfma_f32_16x16x32_bf16 a[104:107], a[248:251], a[204:207], a[104:107]\n" \
"s_add_u32 m0, s70, 32768\n" \
"ds_read_b64_tr_b16 a[192:193], v240 offset:49152\n" \
"ds_read_b64_tr_b16 a[194:195], v240 offset:51200\n" \
"v_mfma_f32_16x16x32_bf16 a[108:111], a[252:255], a[204:207], a[108:111]\n" \
"s_add_u32 s74, s69, 1048576\n" \
"buffer_load_dwordx4 v246, s[28:31], s74 offen lds\n" \
"v_mfma_f32_16x16x32_bf16 a[108:111], a[236:239], a[200:203], a[108:111]\n" \
"ds_read_b64_tr_b16 a[196:197], v240 offset:57344\n" \
"ds_read_b64_tr_b16 a[198:199], v240 offset:59392\n" \
"v_mfma_f32_16x16x32_bf16 a[48:51], a[224:227], a[208:211], a[48:51]\n" \
"ds_read_b64_tr_b16 a[200:201], v241 offset:49152\n" \
"v_cvt_pk_bf16_f32 v184, v112, v113\n" \
"v_mfma_f32_16x16x32_bf16 a[48:51], a[240:243], a[212:215], a[48:51]\n" \
"v_cvt_pk_bf16_f32 v185, v114, v115\n" \
"v_cvt_pk_bf16_f32 v186, v116, v117\n" \
"v_mfma_f32_16x16x32_bf16 a[52:55], a[244:247], a[212:215], a[52:55]\n" \
"buffer_load_dwordx4 a[188:191], v248, s[32:35], s37 offen offset:2048 sc1\n" \
"v_cvt_pk_bf16_f32 v187, v118, v119\n" \
"v_mfma_f32_16x16x32_bf16 a[52:55], a[228:231], a[208:211], a[52:55]\n" \
"ds_read_b64_tr_b16 a[202:203], v241 offset:51200\n" \
"v_mfma_f32_16x16x32_bf16 a[112:115], a[232:235], a[208:211], a[112:115]\n" \
"ds_read_b64_tr_b16 a[204:205], v241 offset:57344\n" \
"v_mfma_f32_16x16x32_bf16 a[112:115], a[248:251], a[212:215], a[112:115]\n" \
"ds_write_b128 v244, v[184:187] offset:38912\n" \
"v_mfma_f32_16x16x32_bf16 a[116:119], a[252:255], a[212:215], a[116:119]\n" \
"ds_read_b64_tr_b16 a[206:207], v241 offset:59392\n" \
"v_mul_f32 v120, s84, v120\n" \
"v_mfma_f32_16x16x32_bf16 a[116:119], a[236:239], a[208:211], a[116:119]\n" \
"v_mul_f32 v121, s84, v121\n" \
"v_mul_f32 v122, s84, v122\n" \
"v_mfma_f32_16x16x32_bf16 a[56:59], a[224:227], a[216:219], a[56:59]\n" \
"ds_read_b64_tr_b16 a[208:209], v240 offset:49664\n" \
"v_mul_f32 v123, s84, v123\n" \
"v_mfma_f32_16x16x32_bf16 a[56:59], a[240:243], a[220:223], a[56:59]\n" \
"v_mul_f32 v124, s84, v124\n" \
"v_mul_f32 v125, s84, v125\n" \
"v_mfma_f32_16x16x32_bf16 a[60:63], a[244:247], a[220:223], a[60:63]\n" \
"s_add_u32 m0, s70, 33792\n" \
"ds_read_b64_tr_b16 a[210:211], v240 offset:51712\n" \
"v_mul_f32 v126, s84, v126\n" \
"v_mfma_f32_16x16x32_bf16 a[60:63], a[228:231], a[216:219], a[60:63]\n" \
"s_add_u32 s74, s69, 1048704\n" \
"v_mul_f32 v127, s84, v127\n" \
"v_cvt_pk_bf16_f32 v188, v120, v121\n" \
"v_mfma_f32_16x16x32_bf16 a[120:123], a[232:235], a[216:219], a[120:123]\n" \
"buffer_load_dwordx4 v246, s[28:31], s74 offen lds\n" \
"v_mfma_f32_16x16x32_bf16 a[120:123], a[248:251], a[220:223], a[120:123]\n" \
"s_add_u32 s85, s71, 4096\n" \
"ds_read_b64_tr_b16 a[212:213], v240 offset:57856\n" \
"v_cvt_pk_bf16_f32 v189, v122, v123\n" \
"v_mfma_f32_16x16x32_bf16 a[124:127], a[252:255], a[220:223], a[124:127]\n" \
"ds_read_b64_tr_b16 a[214:215], v240 offset:59904\n" \
"v_cvt_pk_bf16_f32 v190, v124, v125\n" \
"v_mfma_f32_16x16x32_bf16 a[124:127], a[236:239], a[216:219], a[124:127]\n" \
"s_waitcnt lgkmcnt(9)\n" \
"buffer_store_dwordx4 v[152:155], v248, s[32:35], s85 offen offset:2048 sc1\n" \
"v_mfma_f32_16x16x32_bf16 a[0:3], a[128:131], a[192:195], a[0:3]\n" \
"ds_read_b64_tr_b16 a[216:217], v241 offset:49664\n" \
"v_cvt_pk_bf16_f32 v191, v126, v127\n" \
"v_mfma_f32_16x16x32_bf16 a[0:3], a[144:147], a[196:199], a[0:3]\n" \
"buffer_load_dwordx4 a[224:227], v248, s[32:35], s79 offen offset:3072 sc1\n" \
"v_mfma_f32_16x16x32_bf16 a[4:7], a[148:151], a[196:199], a[4:7]\n" \
"ds_read_b64_tr_b16 a[218:219], v241 offset:51712\n" \
"v_mfma_f32_16x16x32_bf16 a[4:7], a[132:135], a[192:195], a[4:7]\n" \
"ds_write_b128 v244, v[188:191] offset:39936\n" \
"v_mfma_f32_16x16x32_bf16 a[64:67], a[136:139], a[192:195], a[64:67]\n" \
"ds_read_b64_tr_b16 a[220:221], v241 offset:57856\n" \
"v_mfma_f32_16x16x32_bf16 a[64:67], a[152:155], a[196:199], a[64:67]\n" \
"v_mfma_f32_16x16x32_bf16 a[68:71], a[156:159], a[196:199], a[68:71]\n" \
"ds_read_b64_tr_b16 a[222:223], v241 offset:59904\n" \
"v_mfma_f32_16x16x32_bf16 a[68:71], a[140:143], a[192:195], a[68:71]\n" \
"s_waitcnt lgkmcnt(9)\n" \
"v_mfma_f32_16x16x32_bf16 a[8:11], a[128:131], a[200:203], a[8:11]\n" \
"ds_read_b64_tr_b16 a[192:193], v240 offset:50176\n" \
"v_mfma_f32_16x16x32_bf16 a[8:11], a[144:147], a[204:207], a[8:11]\n" \
"buffer_load_dwordx4 a[228:231], v248, s[32:35], s80 offen offset:3072 sc1\n" \
"v_mfma_f32_16x16x32_bf16 a[12:15], a[148:151], a[204:207], a[12:15]\n" \
"s_add_u32 m0, s70, 40960\n" \
"s_add_u32 s74, s69, 1310720\n" \
"ds_read_b64_tr_b16 a[194:195], v240 offset:52224\n" \
"v_mfma_f32_16x16x32_bf16 a[12:15], a[132:135], a[200:203], a[12:15]\n" \
"v_mfma_f32_16x16x32_bf16 a[72:75], a[136:139], a[200:203], a[72:75]\n" \
"buffer_load_dwordx4 v246, s[28:31], s74 offen lds\n" \
"v_mfma_f32_16x16x32_bf16 a[72:75], a[152:155], a[204:207], a[72:75]\n" \
"ds_read_b64_tr_b16 a[196:197], v240 offset:58368\n" \
"v_mfma_f32_16x16x32_bf16 a[76:79], a[156:159], a[204:207], a[76:79]\n" \
"ds_read_b64_tr_b16 a[198:199], v240 offset:60416\n" \
"v_mfma_f32_16x16x32_bf16 a[76:79], a[140:143], a[200:203], a[76:79]\n" \
"s_waitcnt lgkmcnt(9)\n" \
"v_mfma_f32_16x16x32_bf16 a[16:19], a[128:131], a[208:211], a[16:19]\n" \
"ds_read_b64_tr_b16 a[200:201], v241 offset:50176\n" \
"v_mfma_f32_16x16x32_bf16 a[16:19], a[144:147], a[212:215], a[16:19]\n" \
"buffer_load_dwordx4 a[232:235], v248, s[32:35], s36 offen offset:3072 sc1\n" \
"v_mfma_f32_16x16x32_bf16 a[20:23], a[148:151], a[212:215], a[20:23]\n" \
"ds_read_b64_tr_b16 a[202:203], v241 offset:52224\n" \
"v_mfma_f32_16x16x32_bf16 a[20:23], a[132:135], a[208:211], a[20:23]\n" \
"v_mfma_f32_16x16x32_bf16 a[80:83], a[136:139], a[208:211], a[80:83]\n" \
"ds_read_b64_tr_b16 a[204:205], v241 offset:58368\n" \
"v_mfma_f32_16x16x32_bf16 a[80:83], a[152:155], a[212:215], a[80:83]\n" \
"v_mfma_f32_16x16x32_bf16 a[84:87], a[156:159], a[212:215], a[84:87]\n" \
"s_add_u32 m0, s70, 41984\n" \
"ds_read_b64_tr_b16 a[206:207], v241 offset:60416\n" \
"v_mfma_f32_16x16x32_bf16 a[84:87], a[140:143], a[208:211], a[84:87]\n" \
"s_waitcnt lgkmcnt(8)\n" \
"s_add_u32 s74, s69, 1310848\n" \
"v_mfma_f32_16x16x32_bf16 a[24:27], a[128:131], a[216:219], a[24:27]\n" \
"buffer_load_dwordx4 v246, s[28:31], s74 offen lds\n" \
"v_mfma_f32_16x16x32_bf16 a[24:27], a[144:147], a[220:223], a[24:27]\n" \
"s_add_u32 s85, s71, 4096\n" \
"buffer_load_dwordx4 a[236:239], v248, s[32:35], s37 offen offset:3072 sc1\n" \
"v_mfma_f32_16x16x32_bf16 a[28:31], a[148:151], a[220:223], a[28:31]\n" \
"s_add_u32 s79, s79, 4096\n" \
"buffer_load_dwordx4 a[240:243], v248, s[32:35], s79 offen offset:0 sc1\n" \
"v_mfma_f32_16x16x32_bf16 a[28:31], a[132:135], a[216:219], a[28:31]\n" \
"ds_read_b64_tr_b16 a[208:209], v240 offset:50688\n" \
"ds_read_b64_tr_b16 a[210:211], v240 offset:52736\n" \
"v_mfma_f32_16x16x32_bf16 a[88:91], a[136:139], a[216:219], a[88:91]\n" \
"ds_read_b64_tr_b16 a[212:213], v240 offset:58880\n" \
"v_mfma_f32_16x16x32_bf16 a[88:91], a[152:155], a[220:223], a[88:91]\n" \
"buffer_store_dwordx4 v[156:159], v248, s[32:35], s85 offen offset:3072 sc1\n" \
"v_mfma_f32_16x16x32_bf16 a[92:95], a[156:159], a[220:223], a[92:95]\n" \
"ds_read_b64_tr_b16 a[214:215], v240 offset:60928\n" \
"v_mfma_f32_16x16x32_bf16 a[92:95], a[140:143], a[216:219], a[92:95]\n" \
"s_waitcnt lgkmcnt(8)\n" \
"v_mfma_f32_16x16x32_bf16 a[32:35], a[128:131], a[192:195], a[32:35]\n" \
"ds_read_b64_tr_b16 a[216:217], v241 offset:50688\n" \
"v_mfma_f32_16x16x32_bf16 a[32:35], a[144:147], a[196:199], a[32:35]\n" \
"s_add_u32 s80, s80, 4096\n" \
"v_mfma_f32_16x16x32_bf16 a[36:39], a[148:151], a[196:199], a[36:39]\n" \
"buffer_load_dwordx4 a[244:247], v248, s[32:35], s80 offen offset:0 sc1\n" \
"v_mfma_f32_16x16x32_bf16 a[36:39], a[132:135], a[192:195], a[36:39]\n" \
"ds_read_b64_tr_b16 a[218:219], v241 offset:52736\n" \
"ds_read_b64_tr_b16 a[220:221], v241 offset:58880\n" \
"v_mfma_f32_16x16x32_bf16 a[96:99], a[136:139], a[192:195], a[96:99]\n" \
"ds_read_b64_tr_b16 a[222:223], v241 offset:60928\n" \
"v_xor_b32 v240, 0x30000, v240\n" \
"v_mfma_f32_16x16x32_bf16 a[96:99], a[152:155], a[196:199], a[96:99]\n" \
"v_xor_b32 v241, 0x30000, v241\n" \
"v_mfma_f32_16x16x32_bf16 a[100:103], a[156:159], a[196:199], a[100:103]\n" \
"v_mfma_f32_16x16x32_bf16 a[100:103], a[140:143], a[192:195], a[100:103]\n" \
"s_waitcnt lgkmcnt(8)\n" \
"v_mfma_f32_16x16x32_bf16 a[40:43], a[128:131], a[200:203], a[40:43]\n" \
"v_mfma_f32_16x16x32_bf16 a[40:43], a[144:147], a[204:207], a[40:43]\n" \
"s_add_u32 s36, s36, 4096\n" \
"v_mfma_f32_16x16x32_bf16 a[44:47], a[148:151], a[204:207], a[44:47]\n" \
"buffer_load_dwordx4 a[248:251], v248, s[32:35], s36 offen offset:0 sc1\n" \
"v_mfma_f32_16x16x32_bf16 a[44:47], a[132:135], a[200:203], a[44:47]\n" \
"v_mfma_f32_16x16x32_bf16 a[104:107], a[136:139], a[200:203], a[104:107]\n" \
"s_waitcnt vmcnt(32) lgkmcnt(0)\n" \
"s_barrier\n" \
"s_add_u32 m0, s70, 49152\n" \
"v_mfma_f32_16x16x32_bf16 a[104:107], a[152:155], a[204:207], a[104:107]\n" \
"s_add_u32 s74, s69, 1572864\n" \
"ds_read_b64_tr_b16 a[192:193], v240 offset:0\n" \
"ds_read_b64_tr_b16 a[194:195], v240 offset:2048\n" \
"v_mfma_f32_16x16x32_bf16 a[108:111], a[156:159], a[204:207], a[108:111]\n" \
"buffer_load_dwordx4 v246, s[28:31], s74 offen lds\n" \
"v_mfma_f32_16x16x32_bf16 a[108:111], a[140:143], a[200:203], a[108:111]\n" \
"ds_read_b64_tr_b16 a[196:197], v240 offset:8192\n" \
"ds_read_b64_tr_b16 a[198:199], v240 offset:10240\n" \
"v_mfma_f32_16x16x32_bf16 a[48:51], a[128:131], a[208:211], a[48:51]\n" \
"ds_read_b64_tr_b16 a[200:201], v241 offset:0\n" \
"v_mfma_f32_16x16x32_bf16 a[48:51], a[144:147], a[212:215], a[48:51]\n" \
"s_add_u32 s37, s37, 4096\n" \
"v_mfma_f32_16x16x32_bf16 a[52:55], a[148:151], a[212:215], a[52:55]\n" \
"buffer_load_dwordx4 a[252:255], v248, s[32:35], s37 offen offset:0 sc1\n" \
"v_mfma_f32_16x16x32_bf16 a[52:55], a[132:135], a[208:211], a[52:55]\n" \
"ds_read_b64_tr_b16 a[202:203], v241 offset:2048\n" \
"v_mfma_f32_16x16x32_bf16 a[112:115], a[136:139], a[208:211], a[112:115]\n" \
"ds_read_b64_tr_b16 a[204:205], v241 offset:8192\n" \
"v_mfma_f32_16x16x32_bf16 a[112:115], a[152:155], a[212:215], a[112:115]\n" \
"s_add_u32 s85, s94, 0\n" \
"v_mfma_f32_16x16x32_bf16 a[116:119], a[156:159], a[212:215], a[116:119]\n" \
"buffer_store_dwordx4 v[160:163], v248, s[32:35], s85 offen offset:0 sc1\n" \
"v_mfma_f32_16x16x32_bf16 a[116:119], a[140:143], a[208:211], a[116:119]\n" \
"ds_read_b64_tr_b16 a[206:207], v241 offset:10240\n" \
"v_mfma_f32_16x16x32_bf16 a[56:59], a[128:131], a[216:219], a[56:59]\n" \
"ds_read_b64_tr_b16 a[208:209], v240 offset:512\n" \
"v_mfma_f32_16x16x32_bf16 a[56:59], a[144:147], a[220:223], a[56:59]\n" \
"v_mfma_f32_16x16x32_bf16 a[60:63], a[148:151], a[220:223], a[60:63]\n" \
"s_add_u32 m0, s70, 50176\n" \
"ds_read_b64_tr_b16 a[210:211], v240 offset:2560\n" \
"v_mfma_f32_16x16x32_bf16 a[60:63], a[132:135], a[216:219], a[60:63]\n" \
"s_add_u32 s74, s69, 1572992\n" \
"v_mfma_f32_16x16x32_bf16 a[120:123], a[136:139], a[216:219], a[120:123]\n" \
"buffer_load_dwordx4 v246, s[28:31], s74 offen lds\n" \
"v_mfma_f32_16x16x32_bf16 a[120:123], a[152:155], a[220:223], a[120:123]\n" \
"ds_read_b64_tr_b16 a[212:213], v240 offset:8704\n" \
"v_accvgpr_read_b32 v144, a0\n" \
"v_mfma_f32_16x16x32_bf16 a[124:127], a[156:159], a[220:223], a[124:127]\n" \
"s_waitcnt vmcnt(22)\n" \
"ds_read_b64_tr_b16 a[214:215], v240 offset:10752\n" \
"v_mfma_f32_16x16x32_bf16 a[124:127], a[140:143], a[216:219], a[124:127]\n" \
"s_waitcnt lgkmcnt(8)\n" \
"v_accvgpr_read_b32 v145, a1\n" \
"v_accvgpr_read_b32 v146, a2\n" \
  \
"v_mfma_f32_16x16x32_bf16 v[0:3], a[192:195], v[192:195], 0\n" \
"ds_read_b64_tr_b16 a[216:217], v241 offset:512\n" \
"v_accvgpr_read_b32 v147, a3\n" \
"v_mfma_f32_16x16x32_bf16 v[0:3], a[196:199], a[160:163], v[0:3]\n" \
"buffer_load_dwordx4 a[128:131], v248, s[32:35], s79 offen offset:1024 sc1\n" \
"v_accvgpr_read_b32 v148, a4\n" \
"v_mfma_f32_16x16x32_bf16 v[32:35], a[196:199], a[164:167], 0\n" \
"ds_read_b64_tr_b16 a[218:219], v241 offset:2560\n" \
"v_accvgpr_read_b32 v149, a5\n" \
"v_mfma_f32_16x16x32_bf16 v[32:35], a[192:195], v[196:199], v[32:35]\n" \
"v_accvgpr_read_b32 v150, a6\n" \
"v_accvgpr_read_b32 v151, a7\n" \
"v_mfma_f32_16x16x32_bf16 v[64:67], a[192:195], v[200:203], 0\n" \
"ds_read_b64_tr_b16 a[220:221], v241 offset:8704\n" \
"v_max3_f32 v250, |v144|, |v145|, v250\n" \
"v_mfma_f32_16x16x32_bf16 v[64:67], a[196:199], a[168:171], v[64:67]\n" \
"v_max3_f32 v250, |v146|, |v147|, v250\n" \
"v_max3_f32 v250, |v148|, |v149|, v250\n" \
"v_mfma_f32_16x16x32_bf16 v[96:99], a[196:199], a[172:175], 0\n" \
"ds_read_b64_tr_b16 a[222:223], v241 offset:10752\n" \
"v_max3_f32 v250, |v150|, |v151|, v250\n" \
"v_mfma_f32_16x16x32_bf16 v[96:99], a[192:195], v[204:207], v[96:99]\n" \
"s_waitcnt lgkmcnt(8)\n" \
"v_cvt_pk_bf16_f32 v144, v144, v145\n" \
"v_cvt_pk_bf16_f32 v145, v146, v147\n" \
"v_mfma_f32_16x16x32_bf16 v[4:7], a[200:203], v[192:195], 0\n" \
"ds_read_b64_tr_b16 a[192:193], v240 offset:1024\n" \
"v_cvt_pk_bf16_f32 v146, v148, v149\n" \
"v_mfma_f32_16x16x32_bf16 v[4:7], a[204:207], a[160:163], v[4:7]\n" \
"buffer_load_dwordx4 a[132:135], v248, s[32:35], s80 offen offset:1024 sc1\n" \
"v_cvt_pk_bf16_f32 v147, v150, v151\n" \
"v_mfma_f32_16x16x32_bf16 v[36:39], a[204:207], a[164:167], 0\n" \
"s_add_u32 m0, s70, 57344\n" \
"s_add_u32 s74, s69, 1835008\n" \
"ds_read_b64_tr_b16 a[194:195], v240 offset:3072\n" \
"v_mfma_f32_16x16x32_bf16 v[36:39], a[200:203], v[196:199], v[36:39]\n" \
"ds_write_b128 v249, v[144:147] offset:0\n" \
"v_accvgpr_read_b32 v152, a64\n" \
"v_mfma_f32_16x16x32_bf16 v[68:71], a[200:203], v[200:203], 0\n" \
"buffer_load_dwordx4 v246, s[28:31], s74 offen lds\n" \
"v_mfma_f32_16x16x32_bf16 v[68:71], a[204:207], a[168:171], v[68:71]\n" \
"ds_read_b64_tr_b16 a[196:197], v240 offset:9216\n" \
"v_accvgpr_read_b32 v153, a65\n" \
"v_mfma_f32_16x16x32_bf16 v[100:103], a[204:207], a[172:175], 0\n" \
"ds_read_b64_tr_b16 a[198:199], v240 offset:11264\n" \
"v_accvgpr_read_b32 v154, a66\n" \
"v_mfma_f32_16x16x32_bf16 v[100:103], a[200:203], v[204:207], v[100:103]\n" \
"s_waitcnt lgkmcnt(9)\n" \
"v_accvgpr_read_b32 v155, a67\n" \
"v_accvgpr_read_b32 v156, a68\n" \
"v_mfma_f32_16x16x32_bf16 v[8:11], a[208:211], v[192:195], 0\n" \
"s_add_u32 s85, s71, 589824\n" \
"buffer_store_dwordx4 v[144:147], v248, s[32:35], s85 offen offset:0 sc1\n" \
"v_mfma_f32_16x16x32_bf16 v[8:11], a[212:215], a[160:163], v[8:11]\n" \
"buffer_load_dwordx4 a[136:139], v248, s[32:35], s36 offen offset:1024 sc1\n" \
"v_accvgpr_read_b32 v157, a69\n" \
"v_mfma_f32_16x16x32_bf16 v[40:43], a[212:215], a[164:167], 0\n" \
"ds_read_b64_tr_b16 a[200:201], v241 offset:1024\n" \
"ds_read_b64_tr_b16 a[202:203], v241 offset:3072\n" \
"v_mfma_f32_16x16x32_bf16 v[40:43], a[208:211], v[196:199], v[40:43]\n" \
"v_accvgpr_read_b32 v158, a70\n" \
"v_accvgpr_read_b32 v159, a71\n" \
"v_mfma_f32_16x16x32_bf16 v[72:75], a[208:211], v[200:203], 0\n" \
"ds_read_b64_tr_b16 a[204:205], v241 offset:9216\n" \
"v_max3_f32 v250, |v152|, |v153|, v250\n" \
"v_mfma_f32_16x16x32_bf16 v[72:75], a[212:215], a[168:171], v[72:75]\n" \
"v_max3_f32 v250, |v154|, |v155|, v250\n" \
"v_max3_f32 v250, |v156|, |v157|, v250\n" \
"v_mfma_f32_16x16x32_bf16 v[104:107], a[212:215], a[172:175], 0\n" \
"s_add_u32 m0, s70, 58368\n" \
"ds_read_b64_tr_b16 a[206:207], v241 offset:11264\n" \
"v_max3_f32 v250, |v158|, |v159|, v250\n" \
"v_mfma_f32_16x16x32_bf16 v[104:107], a[208:211], v[204:207], v[104:107]\n" \
"s_waitcnt lgkmcnt(9)\n" \
"v_cvt_pk_bf16_f32 v152, v152, v153\n" \
"v_cvt_pk_bf16_f32 v153, v154, v155\n" \
"v_mfma_f32_16x16x32_bf16 v[12:15], a[216:219], v[192:195], 0\n" \
"s_add_u32 s74, s69, 1835136\n" \
"buffer_load_dwordx4 v246, s[28:31], s74 offen lds\n" \
"v_mfma_f32_16x16x32_bf16 v[12:15], a[220:223], a[160:163], v[12:15]\n" \
"buffer_load_dwordx4 a[140:143], v248, s[32:35], s37 offen offset:1024 sc1\n" \
"v_cvt_pk_bf16_f32 v154, v156, v157\n" \
"v_mfma_f32_16x16x32_bf16 v[44:47], a[220:223], a[164:167], 0\n" \
"buffer_load_dwordx4 a[144:147], v248, s[32:35], s79 offen offset:2048 sc1\n" \
"v_cvt_pk_bf16_f32 v155, v158, v159\n" \
"v_mfma_f32_16x16x32_bf16 v[44:47], a[216:219], v[196:199], v[44:47]\n" \
"ds_read_b64_tr_b16 a[208:209], v240 offset:1536\n" \
"ds_read_b64_tr_b16 a[210:211], v240 offset:3584\n" \
"v_mfma_f32_16x16x32_bf16 v[76:79], a[216:219], v[200:203], 0\n" \
"ds_read_b64_tr_b16 a[212:213], v240 offset:9728\n" \
"v_accvgpr_write_b32 a64, v152\n" \
"v_mfma_f32_16x16x32_bf16 v[76:79], a[220:223], a[168:171], v[76:79]\n" \
"v_accvgpr_write_b32 a65, v153\n" \
"v_accvgpr_write_b32 a66, v154\n" \
"v_mfma_f32_16x16x32_bf16 v[108:111], a[220:223], a[172:175], 0\n" \
"ds_read_b64_tr_b16 a[214:215], v240 offset:11776\n" \
"v_accvgpr_write_b32 a67, v155\n" \
"v_mfma_f32_16x16x32_bf16 v[108:111], a[216:219], v[204:207], v[108:111]\n" \
"s_waitcnt lgkmcnt(8)\n" \
"v_mfma_f32_16x16x32_bf16 v[16:19], a[192:195], v[192:195], 0\n" \
"ds_read_b64_tr_b16 a[216:217], v241 offset:1536\n" \
"v_mfma_f32_16x16x32_bf16 v[16:19], a[196:199], a[160:163], v[16:19]\n" \
"v_mfma_f32_16x16x32_bf16 v[48:51], a[196:199], a[164:167], 0\n" \
"buffer_load_dwordx4 a[148:151], v248, s[32:35], s80 offen offset:2048 sc1\n" \
"v_mfma_f32_16x16x32_bf16 v[48:51], a[192:195], v[196:199], v[48:51]\n" \
"s_add_u32 s85, s94, 0\n" \
"ds_read_b64_tr_b16 a[218:219], v241 offset:3584\n" \
"ds_read_b64_tr_b16 a[220:221], v241 offset:9728\n" \
"v_mfma_f32_16x16x32_bf16 v[80:83], a[192:195], v[200:203], 0\n" \
"ds_read_b64_tr_b16 a[222:223], v241 offset:11776\n" \
"v_mfma_f32_16x16x32_bf16 v[80:83], a[196:199], a[168:171], v[80:83]\n" \
"buffer_store_dwordx4 v[164:167], v248, s[32:35], s85 offen offset:1024 sc1\n" \
"v_mfma_f32_16x16x32_bf16 v[112:115], a[196:199], a[172:175], 0\n" \
"v_mfma_f32_16x16x32_bf16 v[112:115], a[192:195], v[204:207], v[112:115]\n" \
"s_waitcnt lgkmcnt(8)\n" \
"v_mfma_f32_16x16x32_bf16 v[20:23], a[200:203], v[192:195], 0\n" \
"v_mfma_f32_16x16x32_bf16 v[20:23], a[204:207], a[160:163], v[20:23]\n" \
"v_mfma_f32_16x16x32_bf16 v[52:55], a[204:207], a[164:167], 0\n" \
"buffer_load_dwordx4 a[152:155], v248, s[32:35], s36 offen offset:2048 sc1\n" \
"v_mfma_f32_16x16x32_bf16 v[52:55], a[200:203], v[196:199], v[52:55]\n" \
"v_mfma_f32_16x16x32_bf16 v[84:87], a[200:203], v[200:203], 0\n" \
"s_waitcnt lgkmcnt(0)\n" \
"s_barrier\n" \
"s_add_u32 m0, s70, 65536\n" \
"v_mfma_f32_16x16x32_bf16 v[84:87], a[204:207], a[168:171], v[84:87]\n" \
"s_add_u32 s74, s69, 2097152\n" \
"ds_read_b64_tr_b16 a[192:193], v240 offset:16384\n" \
"ds_read_b64_tr_b16 a[194:195], v240 offset:18432\n" \
"v_mfma_f32_16x16x32_bf16 v[116:119], a[204:207], a[172:175], 0\n" \
"buffer_load_dwordx4 v246, s[28:31], s74 offen lds\n" \
"v_mfma_f32_16x16x32_bf16 v[116:119], a[200:203], v[204:207], v[116:119]\n" \
"s_lshl_b32 s85, s68, 4\n" \
"ds_read_b64_tr_b16 a[196:197], v240 offset:24576\n" \
"ds_read_b64_tr_b16 a[198:199], v240 offset:26624\n" \
"v_mfma_f32_16x16x32_bf16 v[24:27], a[208:211], v[192:195], 0\n" \
"s_add_u32 s85, s85, s67\n" \
"ds_read_b64_tr_b16 a[200:201], v241 offset:16384\n" \
"v_accvgpr_read_b32 v144, a8\n" \
"v_mfma_f32_16x16x32_bf16 v[24:27], a[212:215], a[160:163], v[24:27]\n" \
"s_lshl2_add_u32 s85, s85, %[wid]\n" \
"v_accvgpr_read_b32 v145, a9\n" \
"v_accvgpr_read_b32 v146, a10\n" \
"v_mfma_f32_16x16x32_bf16 v[56:59], a[212:215], a[164:167], 0\n" \
"s_lshl_b32 s85, s85, 2\n" \
"buffer_load_dwordx4 a[156:159], v248, s[32:35], s37 offen offset:2048 sc1\n" \
"v_accvgpr_read_b32 v147, a11\n" \
"v_mfma_f32_16x16x32_bf16 v[56:59], a[208:211], v[196:199], v[56:59]\n" \
"s_add_u32 s85, s85, 81920\n" \
"ds_read_b64_tr_b16 a[202:203], v241 offset:18432\n" \
"v_accvgpr_read_b32 v148, a12\n" \
"v_mfma_f32_16x16x32_bf16 v[88:91], a[208:211], v[200:203], 0\n" \
"s_waitcnt vmcnt(19)\n" \
"buffer_store_dword v239, v238, s[32:35], s85 offen sc1\n" \
"v_accvgpr_read_b32 v149, a13\n" \
"v_mfma_f32_16x16x32_bf16 v[88:91], a[212:215], a[168:171], v[88:91]\n" \
"ds_read_b64_tr_b16 a[204:205], v241 offset:24576\n" \
"v_accvgpr_read_b32 v150, a14\n" \
"v_mfma_f32_16x16x32_bf16 v[120:123], a[212:215], a[172:175], 0\n" \
"ds_read_b64_tr_b16 a[206:207], v241 offset:26624\n" \
"v_accvgpr_read_b32 v151, a15\n" \
"v_mfma_f32_16x16x32_bf16 v[120:123], a[208:211], v[204:207], v[120:123]\n" \
"v_max3_f32 v250, |v144|, |v145|, v250\n" \
"v_max3_f32 v250, |v146|, |v147|, v250\n" \
"v_mfma_f32_16x16x32_bf16 v[28:31], a[216:219], v[192:195], 0\n" \
"ds_read_b64_tr_b16 a[208:209], v240 offset:16896\n" \
"v_max3_f32 v250, |v148|, |v149|, v250\n" \
"v_mfma_f32_16x16x32_bf16 v[28:31], a[220:223], a[160:163], v[28:31]\n" \
"v_max3_f32 v250, |v150|, |v151|, v250\n" \
"v_cvt_pk_bf16_f32 v144, v144, v145\n" \
"v_mfma_f32_16x16x32_bf16 v[60:63], a[220:223], a[164:167], 0\n" \
"s_add_u32 m0, s70, 66560\n" \
"ds_read_b64_tr_b16 a[210:211], v240 offset:18944\n" \
"v_cvt_pk_bf16_f32 v145, v146, v147\n" \
"v_mfma_f32_16x16x32_bf16 v[60:63], a[216:219], v[196:199], v[60:63]\n" \
"s_add_u32 s74, s69, 2097280\n" \
"v_cvt_pk_bf16_f32 v146, v148, v149\n" \
"v_cvt_pk_bf16_f32 v147, v150, v151\n" \
"v_mfma_f32_16x16x32_bf16 v[92:95], a[216:219], v[200:203], 0\n" \
"buffer_load_dwordx4 v246, s[28:31], s74 offen lds\n" \
"v_mfma_f32_16x16x32_bf16 v[92:95], a[220:223], a[168:171], v[92:95]\n" \
"ds_read_b64_tr_b16 a[212:213], v240 offset:25088\n" \
"v_mfma_f32_16x16x32_bf16 v[124:127], a[220:223], a[172:175], 0\n" \
"ds_read_b64_tr_b16 a[214:215], v240 offset:27136\n" \
"v_mfma_f32_16x16x32_bf16 v[124:127], a[216:219], v[204:207], v[124:127]\n" \
"s_waitcnt lgkmcnt(8)\n" \
"ds_write_b128 v249, v[144:147] offset:1024\n" \
"v_accvgpr_read_b32 v152, a72\n" \
"v_mfma_f32_16x16x32_bf16 v[0:3], a[192:195], a[176:179], v[0:3]\n" \
"ds_read_b64_tr_b16 a[216:217], v241 offset:16896\n" \
"v_accvgpr_read_b32 v153, a73\n" \
"v_mfma_f32_16x16x32_bf16 v[0:3], a[196:199], a[224:227], v[0:3]\n" \
"s_add_u32 s85, s71, 589824\n" \
"buffer_load_dwordx4 v[192:195], v248, s[32:35], s79 offen offset:3072 sc1\n" \
"v_accvgpr_read_b32 v154, a74\n" \
"v_mfma_f32_16x16x32_bf16 v[32:35], a[196:199], a[228:231], v[32:35]\n" \
"ds_read_b64_tr_b16 a[218:219], v241 offset:18944\n" \
"v_accvgpr_read_b32 v155, a75\n" \
"v_mfma_f32_16x16x32_bf16 v[32:35], a[192:195], a[180:183], v[32:35]\n" \
"v_accvgpr_read_b32 v156, a76\n" \
"v_accvgpr_read_b32 v157, a77\n" \
"v_mfma_f32_16x16x32_bf16 v[64:67], a[192:195], a[184:187], v[64:67]\n" \
"buffer_store_dwordx4 v[144:147], v248, s[32:35], s85 offen offset:1024 sc1\n" \
"v_mfma_f32_16x16x32_bf16 v[64:67], a[196:199], a[232:235], v[64:67]\n" \
"ds_read_b64_tr_b16 a[220:221], v241 offset:25088\n" \
"v_accvgpr_read_b32 v158, a78\n" \
"v_mfma_f32_16x16x32_bf16 v[96:99], a[196:199], a[236:239], v[96:99]\n" \
"ds_read_b64_tr_b16 a[222:223], v241 offset:27136\n" \
"v_accvgpr_read_b32 v159, a79\n" \
"v_mfma_f32_16x16x32_bf16 v[96:99], a[192:195], a[188:191], v[96:99]\n" \
"s_waitcnt lgkmcnt(9)\n" \
"v_max3_f32 v250, |v152|, |v153|, v250\n" \
"v_max3_f32 v250, |v154|, |v155|, v250\n" \
"v_mfma_f32_16x16x32_bf16 v[4:7], a[200:203], a[176:179], v[4:7]\n" \
"ds_read_b64_tr_b16 a[192:193], v240 offset:17408\n" \
"v_max3_f32 v250, |v156|, |v157|, v250\n" \
"v_mfma_f32_16x16x32_bf16 v[4:7], a[204:207], a[224:227], v[4:7]\n" \
"buffer_load_dwordx4 v[196:199], v248, s[32:35], s80 offen offset:3072 sc1\n" \
"v_max3_f32 v250, |v158|, |v159|, v250\n" \
"v_mfma_f32_16x16x32_bf16 v[36:39], a[204:207], a[228:231], v[36:39]\n" \
".Lst0_5_%=:\n" \
"s_cbranch_execz .Lst0_9_%=\n" \
"s_add_u32 m0, s70, 73728\n" \
"ds_read_b64_tr_b16 a[194:195], v240 offset:19456\n" \
"v_cvt_pk_bf16_f32 v152, v152, v153\n" \
"v_mfma_f32_16x16x32_bf16 v[36:39], a[200:203], a[180:183], v[36:39]\n" \
"s_add_u32 s74, s69, 2359296\n" \
"v_cvt_pk_bf16_f32 v153, v154, v155\n" \
"v_cvt_pk_bf16_f32 v154, v156, v157\n" \
"v_mfma_f32_16x16x32_bf16 v[68:71], a[200:203], a[184:187], v[68:71]\n" \
"buffer_load_dwordx4 v246, s[28:31], s74 offen lds\n" \
"v_mfma_f32_16x16x32_bf16 v[68:71], a[204:207], a[232:235], v[68:71]\n" \
"ds_read_b64_tr_b16 a[196:197], v240 offset:25600\n" \
"v_cvt_pk_bf16_f32 v155, v158, v159\n" \
"v_mfma_f32_16x16x32_bf16 v[100:103], a[204:207], a[236:239], v[100:103]\n" \
"ds_read_b64_tr_b16 a[198:199], v240 offset:27648\n" \
"v_accvgpr_write_b32 a72, v152\n" \
"v_mfma_f32_16x16x32_bf16 v[100:103], a[200:203], a[188:191], v[100:103]\n" \
"s_waitcnt lgkmcnt(9)\n" \
"v_accvgpr_write_b32 a73, v153\n" \
"v_accvgpr_write_b32 a74, v154\n" \
"v_mfma_f32_16x16x32_bf16 v[8:11], a[208:211], a[176:179], v[8:11]\n" \
"ds_read_b64_tr_b16 a[200:201], v241 offset:17408\n" \
"v_accvgpr_write_b32 a75, v155\n" \
"v_mfma_f32_16x16x32_bf16 v[8:11], a[212:215], a[224:227], v[8:11]\n" \
"buffer_load_dwordx4 v[200:203], v248, s[32:35], s36 offen offset:3072 sc1\n" \
"v_mfma_f32_16x16x32_bf16 v[40:43], a[212:215], a[228:231], v[40:43]\n" \
"ds_read_b64_tr_b16 a[202:203], v241 offset:19456\n" \
"v_mfma_f32_16x16x32_bf16 v[40:43], a[208:211], a[180:183], v[40:43]\n" \
"v_mfma_f32_16x16x32_bf16 v[72:75], a[208:211], a[184:187], v[72:75]\n" \
"ds_read_b64_tr_b16 a[204:205], v241 offset:25600\n" \
"v_mfma_f32_16x16x32_bf16 v[72:75], a[212:215], a[232:235], v[72:75]\n" \
"v_mfma_f32_16x16x32_bf16 v[104:107], a[212:215], a[236:239], v[104:107]\n" \
"s_add_u32 m0, s70, 74752\n" \
"ds_read_b64_tr_b16 a[206:207], v241 offset:27648\n" \
"v_mfma_f32_16x16x32_bf16 v[104:107], a[208:211], a[188:191], v[104:107]\n" \
"s_waitcnt lgkmcnt(8)\n" \
"s_add_u32 s74, s69, 2359424\n" \
"v_mfma_f32_16x16x32_bf16 v[12:15], a[216:219], a[176:179], v[12:15]\n" \
"buffer_load_dwordx4 v246, s[28:31], s74 offen lds\n" \
"v_mfma_f32_16x16x32_bf16 v[12:15], a[220:223], a[224:227], v[12:15]\n" \
"buffer_load_dwordx4 v[204:207], v248, s[32:35], s37 offen offset:3072 sc1\n" \
"v_mfma_f32_16x16x32_bf16 v[44:47], a[220:223], a[228:231], v[44:47]\n" \
"s_add_u32 s79, s79, 4096\n" \
"buffer_load_dwordx4 a[160:163], v248, s[32:35], s79 offen offset:0 sc1\n" \
"v_mfma_f32_16x16x32_bf16 v[44:47], a[216:219], a[180:183], v[44:47]\n" \
"ds_read_b64_tr_b16 a[208:209], v240 offset:17920\n" \
"ds_read_b64_tr_b16 a[210:211], v240 offset:19968\n" \
"v_mfma_f32_16x16x32_bf16 v[76:79], a[216:219], a[184:187], v[76:79]\n" \
"ds_read_b64_tr_b16 a[212:213], v240 offset:26112\n" \
"v_mfma_f32_16x16x32_bf16 v[76:79], a[220:223], a[232:235], v[76:79]\n" \
"v_mfma_f32_16x16x32_bf16 v[108:111], a[220:223], a[236:239], v[108:111]\n" \
"ds_read_b64_tr_b16 a[214:215], v240 offset:28160\n" \
"v_mfma_f32_16x16x32_bf16 v[108:111], a[216:219], a[188:191], v[108:111]\n" \
"s_waitcnt lgkmcnt(8)\n" \
"s_add_u32 s85, s94, 0\n" \
"v_mfma_f32_16x16x32_bf16 v[16:19], a[192:195], a[176:179], v[16:19]\n" \
"buffer_store_dwordx4 v[168:171], v248, s[32:35], s85 offen offset:2048 sc1\n" \
"v_mfma_f32_16x16x32_bf16 v[16:19], a[196:199], a[224:227], v[16:19]\n" \
"s_add_u32 s80, s80, 4096\n" \
"ds_read_b64_tr_b16 a[216:217], v241 offset:17920\n" \
"v_accvgpr_read_b32 v144, a16\n" \
"v_mfma_f32_16x16x32_bf16 v[48:51], a[196:199], a[228:231], v[48:51]\n" \
"buffer_load_dwordx4 a[164:167], v248, s[32:35], s80 offen offset:0 sc1\n" \
"v_accvgpr_read_b32 v145, a17\n" \
"v_mfma_f32_16x16x32_bf16 v[48:51], a[192:195], a[180:183], v[48:51]\n" \
"ds_read_b64_tr_b16 a[218:219], v241 offset:19968\n" \
"ds_read_b64_tr_b16 a[220:221], v241 offset:26112\n" \
"v_mfma_f32_16x16x32_bf16 v[80:83], a[192:195], a[184:187], v[80:83]\n" \
"ds_read_b64_tr_b16 a[222:223], v241 offset:28160\n" \
"v_xor_b32 v240, 0x30000, v240\n" \
"v_mfma_f32_16x16x32_bf16 v[80:83], a[196:199], a[232:235], v[80:83]\n" \
"v_xor_b32 v241, 0x30000, v241\n" \
"v_accvgpr_read_b32 v146, a18\n" \
"v_mfma_f32_16x16x32_bf16 v[112:115], a[196:199], a[236:239], v[112:115]\n" \
"v_accvgpr_read_b32 v147, a19\n" \
"v_accvgpr_read_b32 v148, a20\n" \
"v_mfma_f32_16x16x32_bf16 v[112:115], a[192:195], a[188:191], v[112:115]\n" \
"s_waitcnt lgkmcnt(8)\n" \
"v_accvgpr_read_b32 v149, a21\n" \
"v_accvgpr_read_b32 v150, a22\n" \
"v_mfma_f32_16x16x32_bf16 v[20:23], a[200:203], a[176:179], v[20:23]\n" \
"v_accvgpr_read_b32 v151, a23\n" \
"v_max3_f32 v250, |v144|, |v145|, v250\n" \
"v_mfma_f32_16x16x32_bf16 v[20:23], a[204:207], a[224:227], v[20:23]\n" \
"s_add_u32 s36, s36, 4096\n" \
"v_max3_f32 v250, |v146|, |v147|, v250\n" \
"v_max3_f32 v250, |v148|, |v149|, v250\n" \
"v_mfma_f32_16x16x32_bf16 v[52:55], a[204:207], a[228:231], v[52:55]\n" \
"buffer_load_dwordx4 a[168:171], v248, s[32:35], s36 offen offset:0 sc1\n" \
"v_max3_f32 v250, |v150|, |v151|, v250\n" \
"v_mfma_f32_16x16x32_bf16 v[52:55], a[200:203], a[180:183], v[52:55]\n" \
"v_cvt_pk_bf16_f32 v144, v144, v145\n" \
"v_cvt_pk_bf16_f32 v145, v146, v147\n" \
"v_mfma_f32_16x16x32_bf16 v[84:87], a[200:203], a[184:187], v[84:87]\n" \
"s_waitcnt lgkmcnt(0)\n" \
"s_barrier\n" \
"v_cvt_pk_bf16_f32 v146, v148, v149\n" \
"v_mfma_f32_16x16x32_bf16 v[84:87], a[204:207], a[232:235], v[84:87]\n" \
"s_add_u32 m0, s70, 81920\n" \
"ds_read_b64_tr_b16 a[192:193], v240 offset:32768\n" \
"ds_read_b64_tr_b16 a[194:195], v240 offset:34816\n" \
"v_mfma_f32_16x16x32_bf16 v[116:119], a[204:207], a[236:239], v[116:119]\n" \
"s_add_u32 s74, s69, 2621440\n" \
"buffer_load_dwordx4 v246, s[28:31], s74 offen lds\n" \
"v_mfma_f32_16x16x32_bf16 v[116:119], a[200:203], a[188:191], v[116:119]\n" \
"ds_read_b64_tr_b16 a[196:197], v240 offset:40960\n" \
"ds_read_b64_tr_b16 a[198:199], v240 offset:43008\n" \
"v_mfma_f32_16x16x32_bf16 v[24:27], a[208:211], a[176:179], v[24:27]\n" \
"ds_read_b64_tr_b16 a[200:201], v241 offset:32768\n" \
"v_cvt_pk_bf16_f32 v147, v150, v151\n" \
"v_mfma_f32_16x16x32_bf16 v[24:27], a[212:215], a[224:227], v[24:27]\n" \
"s_add_u32 s37, s37, 4096\n" \
"ds_write_b128 v249, v[144:147] offset:2048\n" \
"v_accvgpr_read_b32 v152, a80\n" \
"v_mfma_f32_16x16x32_bf16 v[56:59], a[212:215], a[228:231], v[56:59]\n" \
"buffer_load_dwordx4 a[172:175], v248, s[32:35], s37 offen offset:0 sc1\n" \
"v_accvgpr_read_b32 v153, a81\n" \
"v_mfma_f32_16x16x32_bf16 v[56:59], a[208:211], a[180:183], v[56:59]\n" \
"s_add_u32 s85, s71, 589824\n" \
"ds_read_b64_tr_b16 a[202:203], v241 offset:34816\n" \
"v_accvgpr_read_b32 v154, a82\n" \
"v_mfma_f32_16x16x32_bf16 v[88:91], a[208:211], a[184:187], v[88:91]\n" \
"ds_read_b64_tr_b16 a[204:205], v241 offset:40960\n" \
"v_accvgpr_read_b32 v155, a83\n" \
"v_mfma_f32_16x16x32_bf16 v[88:91], a[212:215], a[232:235], v[88:91]\n" \
"buffer_store_dwordx4 v[144:147], v248, s[32:35], s85 offen offset:2048 sc1\n" \
"v_mfma_f32_16x16x32_bf16 v[120:123], a[212:215], a[236:239], v[120:123]\n" \
"ds_read_b64_tr_b16 a[206:207], v241 offset:43008\n" \
"v_accvgpr_read_b32 v156, a84\n" \
"v_mfma_f32_16x16x32_bf16 v[120:123], a[208:211], a[188:191], v[120:123]\n" \
"v_accvgpr_read_b32 v157, a85\n" \
"v_accvgpr_read_b32 v158, a86\n" \
"v_mfma_f32_16x16x32_bf16 v[28:31], a[216:219], a[176:179], v[28:31]\n" \
"ds_read_b64_tr_b16 a[208:209], v240 offset:33280\n" \
"v_accvgpr_read_b32 v159, a87\n" \
"v_mfma_f32_16x16x32_bf16 v[28:31], a[220:223], a[224:227], v[28:31]\n" \
"v_max3_f32 v250, |v152|, |v153|, v250\n" \
"v_max3_f32 v250, |v154|, |v155|, v250\n" \
"v_mfma_f32_16x16x32_bf16 v[60:63], a[220:223], a[228:231], v[60:63]\n" \
"s_add_u32 m0, s70, 82944\n" \
"ds_read_b64_tr_b16 a[210:211], v240 offset:35328\n" \
"v_max3_f32 v250, |v156|, |v157|, v250\n" \
"v_mfma_f32_16x16x32_bf16 v[60:63], a[216:219], a[180:183], v[60:63]\n" \
"s_add_u32 s74, s69, 2621568\n" \
"v_max3_f32 v250, |v158|, |v159|, v250\n" \
"v_cvt_pk_bf16_f32 v152, v152, v153\n" \
"v_mfma_f32_16x16x32_bf16 v[92:95], a[216:219], a[184:187], v[92:95]\n" \
"buffer_load_dwordx4 v246, s[28:31], s74 offen lds\n" \
"v_mfma_f32_16x16x32_bf16 v[92:95], a[220:223], a[232:235], v[92:95]\n" \
"ds_read_b64_tr_b16 a[212:213], v240 offset:41472\n" \
"v_cvt_pk_bf16_f32 v153, v154, v155\n" \
"v_mfma_f32_16x16x32_bf16 v[124:127], a[220:223], a[236:239], v[124:127]\n" \
"s_waitcnt vmcnt(23)\n" \
"ds_read_b64_tr_b16 a[214:215], v240 offset:43520\n" \
"v_mfma_f32_16x16x32_bf16 v[124:127], a[216:219], a[188:191], v[124:127]\n" \
"s_waitcnt lgkmcnt(9)\n" \
"v_cvt_pk_bf16_f32 v154, v156, v157\n" \
"v_cvt_pk_bf16_f32 v155, v158, v159\n" \
"v_mfma_f32_16x16x32_bf16 v[0:3], a[192:195], a[240:243], v[0:3]\n" \
"ds_read_b64_tr_b16 a[216:217], v241 offset:33280\n" \
"v_accvgpr_write_b32 a80, v152\n" \
"v_mfma_f32_16x16x32_bf16 v[0:3], a[196:199], a[128:131], v[0:3]\n" \
"buffer_load_dwordx4 a[176:179], v248, s[32:35], s79 offen offset:1024 sc1\n" \
"v_accvgpr_write_b32 a81, v153\n" \
"v_mfma_f32_16x16x32_bf16 v[32:35], a[196:199], a[132:135], v[32:35]\n" \
"ds_read_b64_tr_b16 a[218:219], v241 offset:35328\n" \
"v_accvgpr_write_b32 a82, v154\n" \
"v_mfma_f32_16x16x32_bf16 v[32:35], a[192:195], a[244:247], v[32:35]\n" \
"v_accvgpr_write_b32 a83, v155\n" \
"v_mfma_f32_16x16x32_bf16 v[64:67], a[192:195], a[248:251], v[64:67]\n" \
"ds_read_b64_tr_b16 a[220:221], v241 offset:41472\n" \
"v_mfma_f32_16x16x32_bf16 v[64:67], a[196:199], a[136:139], v[64:67]\n" \
"v_mfma_f32_16x16x32_bf16 v[96:99], a[196:199], a[140:143], v[96:99]\n" \
"ds_read_b64_tr_b16 a[222:223], v241 offset:43520\n" \
"v_mfma_f32_16x16x32_bf16 v[96:99], a[192:195], a[252:255], v[96:99]\n" \
"s_waitcnt lgkmcnt(8)\n" \
"v_mfma_f32_16x16x32_bf16 v[4:7], a[200:203], a[240:243], v[4:7]\n" \
"ds_read_b64_tr_b16 a[192:193], v240 offset:33792\n" \
"v_mfma_f32_16x16x32_bf16 v[4:7], a[204:207], a[128:131], v[4:7]\n" \
"buffer_load_dwordx4 a[180:183], v248, s[32:35], s80 offen offset:1024 sc1\n" \
"v_mfma_f32_16x16x32_bf16 v[36:39], a[204:207], a[132:135], v[36:39]\n" \
"s_add_u32 m0, s70, 90112\n" \
"s_add_u32 s74, s69, 2883584\n" \
"ds_read_b64_tr_b16 a[194:195], v240 offset:35840\n" \
"v_mfma_f32_16x16x32_bf16 v[36:39], a[200:203], a[244:247], v[36:39]\n" \
"v_mfma_f32_16x16x32_bf16 v[68:71], a[200:203], a[248:251], v[68:71]\n" \
"buffer_load_dwordx4 v246, s[28:31], s74 offen lds\n" \
"v_mfma_f32_16x16x32_bf16 v[68:71], a[204:207], a[136:139], v[68:71]\n" \
"ds_read_b64_tr_b16 a[196:197], v240 offset:41984\n" \
"v_mfma_f32_16x16x32_bf16 v[100:103], a[204:207], a[140:143], v[100:103]\n" \
"ds_read_b64_tr_b16 a[198:199], v240 offset:44032\n" \
"v_mfma_f32_16x16x32_bf16 v[100:103], a[200:203], a[252:255], v[100:103]\n" \
"s_waitcnt lgkmcnt(8)\n" \
"v_mfma_f32_16x16x32_bf16 v[8:11], a[208:211], a[240:243], v[8:11]\n" \
"ds_read_b64_tr_b16 a[200:201], v241 offset:33792\n" \
"v_mfma_f32_16x16x32_bf16 v[8:11], a[212:215], a[128:131], v[8:11]\n" \
"buffer_load_dwordx4 a[184:187], v248, s[32:35], s36 offen offset:1024 sc1\n" \
"v_mfma_f32_16x16x32_bf16 v[40:43], a[212:215], a[132:135], v[40:43]\n" \
"ds_read_b64_tr_b16 a[202:203], v241 offset:35840\n" \
"v_accvgpr_read_b32 v144, a24\n" \
"v_mfma_f32_16x16x32_bf16 v[40:43], a[208:211], a[244:247], v[40:43]\n" \
"v_accvgpr_read_b32 v145, a25\n" \
"v_accvgpr_read_b32 v146, a26\n" \
"v_mfma_f32_16x16x32_bf16 v[72:75], a[208:211], a[248:251], v[72:75]\n" \
"ds_read_b64_tr_b16 a[204:205], v241 offset:41984\n" \
"v_accvgpr_read_b32 v147, a27\n" \
"v_mfma_f32_16x16x32_bf16 v[72:75], a[212:215], a[136:139], v[72:75]\n" \
"v_accvgpr_read_b32 v148, a28\n" \
"v_accvgpr_read_b32 v149, a29\n" \
"v_mfma_f32_16x16x32_bf16 v[104:107], a[212:215], a[140:143], v[104:107]\n" \
"s_add_u32 m0, s70, 91136\n" \
"ds_read_b64_tr_b16 a[206:207], v241 offset:44032\n" \
"v_accvgpr_read_b32 v150, a30\n" \
"v_mfma_f32_16x16x32_bf16 v[104:107], a[208:211], a[252:255], v[104:107]\n" \
"s_waitcnt lgkmcnt(8)\n" \
"v_accvgpr_read_b32 v151, a31\n" \
"v_max3_f32 v250, |v144|, |v145|, v250\n" \
"v_mfma_f32_16x16x32_bf16 v[12:15], a[216:219], a[240:243], v[12:15]\n" \
"s_add_u32 s74, s69, 2883712\n" \
"buffer_load_dwordx4 v246, s[28:31], s74 offen lds\n" \
"v_mfma_f32_16x16x32_bf16 v[12:15], a[220:223], a[128:131], v[12:15]\n" \
"buffer_load_dwordx4 a[188:191], v248, s[32:35], s37 offen offset:1024 sc1\n" \
"v_max3_f32 v250, |v146|, |v147|, v250\n" \
"v_mfma_f32_16x16x32_bf16 v[44:47], a[220:223], a[132:135], v[44:47]\n" \
"buffer_load_dwordx4 a[0:3], v248, s[32:35], s79 offen offset:2048 sc1\n" \
"v_max3_f32 v250, |v148|, |v149|, v250\n" \
"v_mfma_f32_16x16x32_bf16 v[44:47], a[216:219], a[244:247], v[44:47]\n" \
"s_add_u32 s85, s94, 0\n" \
"ds_read_b64_tr_b16 a[208:209], v240 offset:34304\n" \
"ds_read_b64_tr_b16 a[210:211], v240 offset:36352\n" \
"v_mfma_f32_16x16x32_bf16 v[76:79], a[216:219], a[248:251], v[76:79]\n" \
"ds_read_b64_tr_b16 a[212:213], v240 offset:42496\n" \
"v_max3_f32 v250, |v150|, |v151|, v250\n" \
"v_mfma_f32_16x16x32_bf16 v[76:79], a[220:223], a[136:139], v[76:79]\n" \
"buffer_store_dwordx4 v[172:175], v248, s[32:35], s85 offen offset:3072 sc1\n" \
"v_mfma_f32_16x16x32_bf16 v[108:111], a[220:223], a[140:143], v[108:111]\n" \
"ds_read_b64_tr_b16 a[214:215], v240 offset:44544\n" \
"v_cvt_pk_bf16_f32 v144, v144, v145\n" \
"v_mfma_f32_16x16x32_bf16 v[108:111], a[216:219], a[252:255], v[108:111]\n" \
"s_waitcnt lgkmcnt(8)\n" \
"v_cvt_pk_bf16_f32 v145, v146, v147\n" \
"v_cvt_pk_bf16_f32 v146, v148, v149\n" \
"v_mfma_f32_16x16x32_bf16 v[16:19], a[192:195], a[240:243], v[16:19]\n" \
"ds_read_b64_tr_b16 a[216:217], v241 offset:34304\n" \
"v_cvt_pk_bf16_f32 v147, v150, v151\n" \
"v_mfma_f32_16x16x32_bf16 v[16:19], a[196:199], a[128:131], v[16:19]\n" \
"ds_write_b128 v249, v[144:147] offset:3072\n" \
"v_accvgpr_read_b32 v152, a88\n" \
"v_mfma_f32_16x16x32_bf16 v[48:51], a[196:199], a[132:135], v[48:51]\n" \
"buffer_load_dwordx4 a[4:7], v248, s[32:35], s80 offen offset:2048 sc1\n" \
"v_accvgpr_read_b32 v153, a89\n" \
"v_mfma_f32_16x16x32_bf16 v[48:51], a[192:195], a[244:247], v[48:51]\n" \
"ds_read_b64_tr_b16 a[218:219], v241 offset:36352\n" \
"ds_read_b64_tr_b16 a[220:221], v241 offset:42496\n" \
"v_mfma_f32_16x16x32_bf16 v[80:83], a[192:195], a[248:251], v[80:83]\n" \
"ds_read_b64_tr_b16 a[222:223], v241 offset:44544\n" \
"v_accvgpr_read_b32 v154, a90\n" \
"v_mfma_f32_16x16x32_bf16 v[80:83], a[196:199], a[136:139], v[80:83]\n" \
"s_add_u32 s85, s71, 589824\n" \
"v_accvgpr_read_b32 v155, a91\n" \
"v_accvgpr_read_b32 v156, a92\n" \
"v_mfma_f32_16x16x32_bf16 v[112:115], a[196:199], a[140:143], v[112:115]\n" \
"buffer_store_dwordx4 v[144:147], v248, s[32:35], s85 offen offset:3072 sc1\n" \
"v_mfma_f32_16x16x32_bf16 v[112:115], a[192:195], a[252:255], v[112:115]\n" \
"s_waitcnt lgkmcnt(9)\n" \
"v_accvgpr_read_b32 v157, a93\n" \
"v_accvgpr_read_b32 v158, a94\n" \
"v_mfma_f32_16x16x32_bf16 v[20:23], a[200:203], a[240:243], v[20:23]\n" \
"v_accvgpr_read_b32 v159, a95\n" \
"v_max3_f32 v250, |v152|, |v153|, v250\n" \
"v_mfma_f32_16x16x32_bf16 v[20:23], a[204:207], a[128:131], v[20:23]\n" \
"v_max3_f32 v250, |v154|, |v155|, v250\n" \
"v_max3_f32 v250, |v156|, |v157|, v250\n" \
"v_mfma_f32_16x16x32_bf16 v[52:55], a[204:207], a[132:135], v[52:55]\n" \
"buffer_load_dwordx4 a[8:11], v248, s[32:35], s36 offen offset:2048 sc1\n" \
"v_max3_f32 v250, |v158|, |v159|, v250\n" \
"v_mfma_f32_16x16x32_bf16 v[52:55], a[200:203], a[244:247], v[52:55]\n" \
"v_cvt_pk_bf16_f32 v152, v152, v153\n" \
"v_cvt_pk_bf16_f32 v153, v154, v155\n" \
"v_mfma_f32_16x16x32_bf16 v[84:87], a[200:203], a[248:251], v[84:87]\n" \
"s_waitcnt lgkmcnt(0)\n" \
"s_barrier\n" \
"v_cvt_pk_bf16_f32 v154, v156, v157\n" \
"v_mfma_f32_16x16x32_bf16 v[84:87], a[204:207], a[136:139], v[84:87]\n" \
"s_add_u32 m0, s70, 32768\n" \
"ds_read_b64_tr_b16 a[192:193], v240 offset:49152\n" \
"ds_read_b64_tr_b16 a[194:195], v240 offset:51200\n" \
"v_mfma_f32_16x16x32_bf16 v[116:119], a[204:207], a[140:143], v[116:119]\n" \
"s_add_u32 s74, s69, 3145728\n" \
"buffer_load_dwordx4 v246, s[28:31], s74 offen lds\n" \
"v_mfma_f32_16x16x32_bf16 v[116:119], a[200:203], a[252:255], v[116:119]\n" \
"ds_read_b64_tr_b16 a[196:197], v240 offset:57344\n" \
"ds_read_b64_tr_b16 a[198:199], v240 offset:59392\n" \
"v_mfma_f32_16x16x32_bf16 v[24:27], a[208:211], a[240:243], v[24:27]\n" \
"ds_read_b64_tr_b16 a[200:201], v241 offset:49152\n" \
"v_cvt_pk_bf16_f32 v155, v158, v159\n" \
"v_mfma_f32_16x16x32_bf16 v[24:27], a[212:215], a[128:131], v[24:27]\n" \
"v_accvgpr_write_b32 a88, v152\n" \
"v_accvgpr_write_b32 a89, v153\n" \
"v_mfma_f32_16x16x32_bf16 v[56:59], a[212:215], a[132:135], v[56:59]\n" \
"buffer_load_dwordx4 a[12:15], v248, s[32:35], s37 offen offset:2048 sc1\n" \
"v_accvgpr_write_b32 a90, v154\n" \
"v_mfma_f32_16x16x32_bf16 v[56:59], a[208:211], a[244:247], v[56:59]\n" \
"ds_read_b64_tr_b16 a[202:203], v241 offset:51200\n" \
"v_accvgpr_write_b32 a91, v155\n" \
"v_mfma_f32_16x16x32_bf16 v[88:91], a[208:211], a[248:251], v[88:91]\n" \
"ds_read_b64_tr_b16 a[204:205], v241 offset:57344\n" \
"v_mfma_f32_16x16x32_bf16 v[88:91], a[212:215], a[136:139], v[88:91]\n" \
"v_mfma_f32_16x16x32_bf16 v[120:123], a[212:215], a[140:143], v[120:123]\n" \
"ds_read_b64_tr_b16 a[206:207], v241 offset:59392\n" \
"v_mfma_f32_16x16x32_bf16 v[120:123], a[208:211], a[252:255], v[120:123]\n" \
"v_mfma_f32_16x16x32_bf16 v[28:31], a[216:219], a[240:243], v[28:31]\n" \
"ds_read_b64_tr_b16 a[208:209], v240 offset:49664\n" \
"v_mfma_f32_16x16x32_bf16 v[28:31], a[220:223], a[128:131], v[28:31]\n" \
"v_mfma_f32_16x16x32_bf16 v[60:63], a[220:223], a[132:135], v[60:63]\n" \
"buffer_load_dwordx4 a[16:19], v248, s[32:35], s79 offen offset:3072 sc1\n" \
"v_mfma_f32_16x16x32_bf16 v[60:63], a[216:219], a[244:247], v[60:63]\n" \
"s_add_u32 m0, s70, 33792\n" \
"s_add_u32 s74, s69, 3145856\n" \
"ds_read_b64_tr_b16 a[210:211], v240 offset:51712\n" \
"v_mfma_f32_16x16x32_bf16 v[92:95], a[216:219], a[248:251], v[92:95]\n" \
"ds_read_b64_tr_b16 a[212:213], v240 offset:57856\n" \
"v_mfma_f32_16x16x32_bf16 v[92:95], a[220:223], a[136:139], v[92:95]\n" \
"buffer_load_dwordx4 v246, s[28:31], s74 offen lds\n" \
"v_mfma_f32_16x16x32_bf16 v[124:127], a[220:223], a[140:143], v[124:127]\n" \
"s_waitcnt vmcnt(23)\n" \
"ds_read_b64_tr_b16 a[214:215], v240 offset:59904\n" \
"v_mfma_f32_16x16x32_bf16 v[124:127], a[216:219], a[252:255], v[124:127]\n" \
"s_waitcnt lgkmcnt(8)\n" \
"v_mfma_f32_16x16x32_bf16 v[0:3], a[192:195], a[144:147], v[0:3]\n" \
"ds_read_b64_tr_b16 a[216:217], v241 offset:49664\n" \
"v_mfma_f32_16x16x32_bf16 v[0:3], a[196:199], v[192:195], v[0:3]\n" \
"v_mfma_f32_16x16x32_bf16 v[32:35], a[196:199], v[196:199], v[32:35]\n" \
"buffer_load_dwordx4 a[20:23], v248, s[32:35], s80 offen offset:3072 sc1\n" \
"v_mfma_f32_16x16x32_bf16 v[32:35], a[192:195], a[148:151], v[32:35]\n" \
"ds_read_b64_tr_b16 a[218:219], v241 offset:51712\n" \
"v_mfma_f32_16x16x32_bf16 v[64:67], a[192:195], a[152:155], v[64:67]\n" \
"ds_read_b64_tr_b16 a[220:221], v241 offset:57856\n" \
"v_mfma_f32_16x16x32_bf16 v[64:67], a[196:199], v[200:203], v[64:67]\n" \
"v_mfma_f32_16x16x32_bf16 v[96:99], a[196:199], v[204:207], v[96:99]\n" \
"ds_read_b64_tr_b16 a[222:223], v241 offset:59904\n" \
"v_mfma_f32_16x16x32_bf16 v[96:99], a[192:195], a[156:159], v[96:99]\n" \
"s_waitcnt lgkmcnt(8)\n" \
"v_mfma_f32_16x16x32_bf16 v[4:7], a[200:203], a[144:147], v[4:7]\n" \
"s_add_u32 m0, s70, 40960\n" \
"ds_read_b64_tr_b16 a[192:193], v240 offset:50176\n" \
"v_mfma_f32_16x16x32_bf16 v[4:7], a[204:207], v[192:195], v[4:7]\n" \
"s_add_u32 s74, s69, 3407872\n" \
"v_mfma_f32_16x16x32_bf16 v[36:39], a[204:207], v[196:199], v[36:39]\n" \
"buffer_load_dwordx4 v246, s[28:31], s74 offen lds\n" \
"v_mfma_f32_16x16x32_bf16 v[36:39], a[200:203], a[148:151], v[36:39]\n" \
"buffer_load_dwordx4 a[24:27], v248, s[32:35], s36 offen offset:3072 sc1\n" \
"v_mfma_f32_16x16x32_bf16 v[68:71], a[200:203], a[152:155], v[68:71]\n" \
"ds_read_b64_tr_b16 a[194:195], v240 offset:52224\n" \
"ds_read_b64_tr_b16 a[196:197], v240 offset:58368\n" \
"v_mfma_f32_16x16x32_bf16 v[68:71], a[204:207], v[200:203], v[68:71]\n" \
"v_mfma_f32_16x16x32_bf16 v[100:103], a[204:207], v[204:207], v[100:103]\n" \
"ds_read_b64_tr_b16 a[198:199], v240 offset:60416\n" \
"v_mfma_f32_16x16x32_bf16 v[100:103], a[200:203], a[156:159], v[100:103]\n" \
"s_waitcnt lgkmcnt(8)\n" \
"v_mfma_f32_16x16x32_bf16 v[8:11], a[208:211], a[144:147], v[8:11]\n" \
"ds_read_b64_tr_b16 a[200:201], v241 offset:50176\n" \
"v_mfma_f32_16x16x32_bf16 v[8:11], a[212:215], v[192:195], v[8:11]\n" \
"v_mfma_f32_16x16x32_bf16 v[40:43], a[212:215], v[196:199], v[40:43]\n" \
"buffer_load_dwordx4 a[28:31], v248, s[32:35], s37 offen offset:3072 sc1\n" \
"v_mfma_f32_16x16x32_bf16 v[40:43], a[208:211], a[148:151], v[40:43]\n" \
"ds_read_b64_tr_b16 a[202:203], v241 offset:52224\n" \
"v_mfma_f32_16x16x32_bf16 v[72:75], a[208:211], a[152:155], v[72:75]\n" \
"ds_read_b64_tr_b16 a[204:205], v241 offset:58368\n" \
"v_mfma_f32_16x16x32_bf16 v[72:75], a[212:215], v[200:203], v[72:75]\n" \
"v_mfma_f32_16x16x32_bf16 v[104:107], a[212:215], v[204:207], v[104:107]\n" \
"s_add_u32 m0, s70, 41984\n" \
"ds_read_b64_tr_b16 a[206:207], v241 offset:60416\n" \
"v_mfma_f32_16x16x32_bf16 v[104:107], a[208:211], a[156:159], v[104:107]\n" \
"s_waitcnt lgkmcnt(8)\n" \
"s_add_u32 s74, s69, 3408000\n" \
"s_add_u32 s85, s94, 4096\n" \
"v_mfma_f32_16x16x32_bf16 v[12:15], a[216:219], a[144:147], v[12:15]\n" \
"buffer_load_dwordx4 v246, s[28:31], s74 offen lds\n" \
"v_mfma_f32_16x16x32_bf16 v[12:15], a[220:223], v[192:195], v[12:15]\n" \
"s_add_u32 s79, s79, 4096\n" \
"ds_read_b64_tr_b16 a[208:209], v240 offset:50688\n" \
"v_mfma_f32_16x16x32_bf16 v[44:47], a[220:223], v[196:199], v[44:47]\n" \
"buffer_load_dwordx4 a[128:131], v248, s[32:35], s79 offen offset:0 sc1\n" \
"v_mfma_f32_16x16x32_bf16 v[44:47], a[216:219], a[148:151], v[44:47]\n" \
"ds_read_b64_tr_b16 a[210:211], v240 offset:52736\n" \
"v_mfma_f32_16x16x32_bf16 v[76:79], a[216:219], a[152:155], v[76:79]\n" \
"ds_read_b64_tr_b16 a[212:213], v240 offset:58880\n" \
"v_mfma_f32_16x16x32_bf16 v[76:79], a[220:223], v[200:203], v[76:79]\n" \
"buffer_store_dwordx4 v[176:179], v248, s[32:35], s85 offen offset:0 sc1\n" \
"v_mfma_f32_16x16x32_bf16 v[108:111], a[220:223], v[204:207], v[108:111]\n" \
"ds_read_b64_tr_b16 a[214:215], v240 offset:60928\n" \
"v_mfma_f32_16x16x32_bf16 v[108:111], a[216:219], a[156:159], v[108:111]\n" \
"s_waitcnt lgkmcnt(8)\n" \
"v_mfma_f32_16x16x32_bf16 v[16:19], a[192:195], a[144:147], v[16:19]\n" \
"ds_read_b64_tr_b16 a[216:217], v241 offset:50688\n" \
"v_mfma_f32_16x16x32_bf16 v[16:19], a[196:199], v[192:195], v[16:19]\n" \
"s_add_u32 s80, s80, 4096\n" \
"v_mfma_f32_16x16x32_bf16 v[48:51], a[196:199], v[196:199], v[48:51]\n" \
"buffer_load_dwordx4 a[132:135], v248, s[32:35], s80 offen offset:0 sc1\n" \
"v_accvgpr_read_b32 v144, a32\n" \
"v_mfma_f32_16x16x32_bf16 v[48:51], a[192:195], a[148:151], v[48:51]\n" \
"ds_read_b64_tr_b16 a[218:219], v241 offset:52736\n" \
"ds_read_b64_tr_b16 a[220:221], v241 offset:58880\n" \
"v_mfma_f32_16x16x32_bf16 v[80:83], a[192:195], a[152:155], v[80:83]\n" \
"ds_read_b64_tr_b16 a[222:223], v241 offset:60928\n" \
"v_xor_b32 v240, 0x30000, v240\n" \
"v_mfma_f32_16x16x32_bf16 v[80:83], a[196:199], v[200:203], v[80:83]\n" \
"v_xor_b32 v241, 0x30000, v241\n" \
"v_accvgpr_read_b32 v145, a33\n" \
"v_mfma_f32_16x16x32_bf16 v[112:115], a[196:199], v[204:207], v[112:115]\n" \
"v_accvgpr_read_b32 v146, a34\n" \
"v_accvgpr_read_b32 v147, a35\n" \
"v_mfma_f32_16x16x32_bf16 v[112:115], a[192:195], a[156:159], v[112:115]\n" \
"s_waitcnt lgkmcnt(8)\n" \
"v_accvgpr_read_b32 v148, a36\n" \
"v_accvgpr_read_b32 v149, a37\n" \
"v_mfma_f32_16x16x32_bf16 v[20:23], a[200:203], a[144:147], v[20:23]\n" \
"v_accvgpr_read_b32 v150, a38\n" \
"v_accvgpr_read_b32 v151, a39\n" \
"v_mfma_f32_16x16x32_bf16 v[20:23], a[204:207], v[192:195], v[20:23]\n" \
"s_add_u32 s36, s36, 4096\n" \
"v_max3_f32 v250, |v144|, |v145|, v250\n" \
"v_max3_f32 v250, |v146|, |v147|, v250\n" \
"v_mfma_f32_16x16x32_bf16 v[52:55], a[204:207], v[196:199], v[52:55]\n" \
"buffer_load_dwordx4 a[136:139], v248, s[32:35], s36 offen offset:0 sc1\n" \
"v_max3_f32 v250, |v148|, |v149|, v250\n" \
"v_mfma_f32_16x16x32_bf16 v[52:55], a[200:203], a[148:151], v[52:55]\n" \
"v_max3_f32 v250, |v150|, |v151|, v250\n" \
"v_cvt_pk_bf16_f32 v144, v144, v145\n" \
"v_mfma_f32_16x16x32_bf16 v[84:87], a[200:203], a[152:155], v[84:87]\n" \
"s_waitcnt lgkmcnt(0)\n" \
"s_barrier\n" \
"v_cvt_pk_bf16_f32 v145, v146, v147\n" \
"v_mfma_f32_16x16x32_bf16 v[84:87], a[204:207], v[200:203], v[84:87]\n" \
"s_add_u32 m0, s70, 49152\n" \
"ds_read_b64_tr_b16 a[192:193], v240 offset:0\n" \
"ds_read_b64_tr_b16 a[194:195], v240 offset:2048\n" \
"v_mfma_f32_16x16x32_bf16 v[116:119], a[204:207], v[204:207], v[116:119]\n" \
"s_add_u32 s74, s69, 3670016\n" \
"buffer_load_dwordx4 v246, s[28:31], s74 offen lds\n" \
"v_mfma_f32_16x16x32_bf16 v[116:119], a[200:203], a[156:159], v[116:119]\n" \
"ds_read_b64_tr_b16 a[196:197], v240 offset:8192\n" \
"ds_read_b64_tr_b16 a[198:199], v240 offset:10240\n" \
"v_mfma_f32_16x16x32_bf16 v[24:27], a[208:211], a[144:147], v[24:27]\n" \
"ds_read_b64_tr_b16 a[200:201], v241 offset:0\n" \
"v_cvt_pk_bf16_f32 v146, v148, v149\n" \
"v_mfma_f32_16x16x32_bf16 v[24:27], a[212:215], v[192:195], v[24:27]\n" \
"s_add_u32 s37, s37, 4096\n" \
"v_cvt_pk_bf16_f32 v147, v150, v151\n" \
"v_mfma_f32_16x16x32_bf16 v[56:59], a[212:215], v[196:199], v[56:59]\n" \
"buffer_load_dwordx4 a[140:143], v248, s[32:35], s37 offen offset:0 sc1\n" \
"v_mfma_f32_16x16x32_bf16 v[56:59], a[208:211], a[148:151], v[56:59]\n" \
"ds_read_b64_tr_b16 a[202:203], v241 offset:2048\n" \
"v_mfma_f32_16x16x32_bf16 v[88:91], a[208:211], a[152:155], v[88:91]\n" \
"ds_read_b64_tr_b16 a[204:205], v241 offset:8192\n" \
"v_mfma_f32_16x16x32_bf16 v[88:91], a[212:215], v[200:203], v[88:91]\n" \
"s_add_u32 s85, s71, 593920\n" \
"ds_write_b128 v249, v[144:147] offset:4096\n" \
"v_accvgpr_read_b32 v152, a96\n" \
"v_mfma_f32_16x16x32_bf16 v[120:123], a[212:215], v[204:207], v[120:123]\n" \
"buffer_store_dwordx4 v[144:147], v248, s[32:35], s85 offen offset:0 sc1\n" \
"v_mfma_f32_16x16x32_bf16 v[120:123], a[208:211], a[156:159], v[120:123]\n" \
"ds_read_b64_tr_b16 a[206:207], v241 offset:10240\n" \
"v_accvgpr_read_b32 v153, a97\n" \
"v_mfma_f32_16x16x32_bf16 v[28:31], a[216:219], a[144:147], v[28:31]\n" \
"ds_read_b64_tr_b16 a[208:209], v240 offset:512\n" \
"v_accvgpr_read_b32 v154, a98\n" \
"v_mfma_f32_16x16x32_bf16 v[28:31], a[220:223], v[192:195], v[28:31]\n" \
"v_accvgpr_read_b32 v155, a99\n" \
"v_accvgpr_read_b32 v156, a100\n" \
"v_mfma_f32_16x16x32_bf16 v[60:63], a[220:223], v[196:199], v[60:63]\n" \
"s_add_u32 m0, s70, 50176\n" \
"ds_read_b64_tr_b16 a[210:211], v240 offset:2560\n" \
"v_accvgpr_read_b32 v157, a101\n" \
"v_mfma_f32_16x16x32_bf16 v[60:63], a[216:219], a[148:151], v[60:63]\n" \
"s_add_u32 s74, s69, 3670144\n" \
"v_accvgpr_read_b32 v158, a102\n" \
"v_accvgpr_read_b32 v159, a103\n" \
"v_mfma_f32_16x16x32_bf16 v[92:95], a[216:219], a[152:155], v[92:95]\n" \
"buffer_load_dwordx4 v246, s[28:31], s74 offen lds\n" \
"v_mfma_f32_16x16x32_bf16 v[92:95], a[220:223], v[200:203], v[92:95]\n" \
"ds_read_b64_tr_b16 a[212:213], v240 offset:8704\n" \
"v_max3_f32 v250, |v152|, |v153|, v250\n" \
"v_mfma_f32_16x16x32_bf16 v[124:127], a[220:223], v[204:207], v[124:127]\n" \
"s_waitcnt vmcnt(22)\n" \
"ds_read_b64_tr_b16 a[214:215], v240 offset:10752\n" \
"v_mfma_f32_16x16x32_bf16 v[124:127], a[216:219], a[156:159], v[124:127]\n" \
"s_waitcnt lgkmcnt(9)\n" \
"v_max3_f32 v250, |v154|, |v155|, v250\n" \
"v_max3_f32 v250, |v156|, |v157|, v250\n" \
"v_mfma_f32_16x16x32_bf16 v[0:3], a[192:195], a[160:163], v[0:3]\n" \
"ds_read_b64_tr_b16 a[216:217], v241 offset:512\n" \
"v_max3_f32 v250, |v158|, |v159|, v250\n" \
"v_mfma_f32_16x16x32_bf16 v[0:3], a[196:199], a[176:179], v[0:3]\n" \
"buffer_load_dwordx4 a[144:147], v248, s[32:35], s79 offen offset:1024 sc1\n" \
"v_cvt_pk_bf16_f32 v152, v152, v153\n" \
"v_mfma_f32_16x16x32_bf16 v[32:35], a[196:199], a[180:183], v[32:35]\n" \
"ds_read_b64_tr_b16 a[218:219], v241 offset:2560\n" \
"v_cvt_pk_bf16_f32 v153, v154, v155\n" \
"v_mfma_f32_16x16x32_bf16 v[32:35], a[192:195], a[164:167], v[32:35]\n" \
"v_cvt_pk_bf16_f32 v154, v156, v157\n" \
"v_cvt_pk_bf16_f32 v155, v158, v159\n" \
"v_mfma_f32_16x16x32_bf16 v[64:67], a[192:195], a[168:171], v[64:67]\n" \
"ds_read_b64_tr_b16 a[220:221], v241 offset:8704\n" \
"v_accvgpr_write_b32 a96, v152\n" \
"v_mfma_f32_16x16x32_bf16 v[64:67], a[196:199], a[184:187], v[64:67]\n" \
"v_accvgpr_write_b32 a97, v153\n" \
"v_accvgpr_write_b32 a98, v154\n" \
"v_mfma_f32_16x16x32_bf16 v[96:99], a[196:199], a[188:191], v[96:99]\n" \
"ds_read_b64_tr_b16 a[222:223], v241 offset:10752\n" \
"v_accvgpr_write_b32 a99, v155\n" \
"v_mfma_f32_16x16x32_bf16 v[96:99], a[192:195], a[172:175], v[96:99]\n" \
"s_waitcnt lgkmcnt(8)\n" \
"v_mfma_f32_16x16x32_bf16 v[4:7], a[200:203], a[160:163], v[4:7]\n" \
"ds_read_b64_tr_b16 a[192:193], v240 offset:1024\n" \
"v_mfma_f32_16x16x32_bf16 v[4:7], a[204:207], a[176:179], v[4:7]\n" \
"buffer_load_dwordx4 a[148:151], v248, s[32:35], s80 offen offset:1024 sc1\n" \
"v_mfma_f32_16x16x32_bf16 v[36:39], a[204:207], a[180:183], v[36:39]\n" \
"s_add_u32 m0, s70, 57344\n" \
"s_add_u32 s74, s69, 3932160\n" \
"ds_read_b64_tr_b16 a[194:195], v240 offset:3072\n" \
"v_mfma_f32_16x16x32_bf16 v[36:39], a[200:203], a[164:167], v[36:39]\n" \
"v_mfma_f32_16x16x32_bf16 v[68:71], a[200:203], a[168:171], v[68:71]\n" \
"buffer_load_dwordx4 v246, s[28:31], s74 offen lds\n" \
"v_mfma_f32_16x16x32_bf16 v[68:71], a[204:207], a[184:187], v[68:71]\n" \
".Lst0_6_%=:\n" \
"s_cbranch_execz .Lst0_10_%=\n" \
"ds_read_b64_tr_b16 a[196:197], v240 offset:9216\n" \
"v_mfma_f32_16x16x32_bf16 v[100:103], a[204:207], a[188:191], v[100:103]\n" \
"ds_read_b64_tr_b16 a[198:199], v240 offset:11264\n" \
"v_mfma_f32_16x16x32_bf16 v[100:103], a[200:203], a[172:175], v[100:103]\n" \
"s_waitcnt lgkmcnt(8)\n" \
"v_mfma_f32_16x16x32_bf16 v[8:11], a[208:211], a[160:163], v[8:11]\n" \
"ds_read_b64_tr_b16 a[200:201], v241 offset:1024\n" \
"v_mfma_f32_16x16x32_bf16 v[8:11], a[212:215], a[176:179], v[8:11]\n" \
"buffer_load_dwordx4 a[152:155], v248, s[32:35], s36 offen offset:1024 sc1\n" \
"v_mfma_f32_16x16x32_bf16 v[40:43], a[212:215], a[180:183], v[40:43]\n" \
"ds_read_b64_tr_b16 a[202:203], v241 offset:3072\n" \
"v_mfma_f32_16x16x32_bf16 v[40:43], a[208:211], a[164:167], v[40:43]\n" \
"s_add_u32 s85, s94, 4096\n" \
"v_mfma_f32_16x16x32_bf16 v[72:75], a[208:211], a[168:171], v[72:75]\n" \
"s_lshl_b32 s91, %[wid], 4\n" \
"buffer_store_dwordx4 v[180:183], v248, s[32:35], s85 offen offset:1024 sc1\n" \
"v_mfma_f32_16x16x32_bf16 v[72:75], a[212:215], a[184:187], v[72:75]\n" \
"ds_read_b64_tr_b16 a[204:205], v241 offset:9216\n" \
"v_and_b32 v228, 15, v251\n" \
"v_mfma_f32_16x16x32_bf16 v[104:107], a[212:215], a[188:191], v[104:107]\n" \
"s_add_u32 m0, s70, 58368\n" \
"ds_read_b64_tr_b16 a[206:207], v241 offset:11264\n" \
"v_lshrrev_b32 v229, 4, v251\n" \
"v_mfma_f32_16x16x32_bf16 v[104:107], a[208:211], a[172:175], v[104:107]\n" \
"s_waitcnt lgkmcnt(8)\n" \
"v_lshlrev_b32 v229, 2, v229\n" \
"v_sub_u32 v228, v228, v229\n" \
"v_mfma_f32_16x16x32_bf16 v[12:15], a[216:219], a[160:163], v[12:15]\n" \
"s_add_u32 s74, s69, 3932288\n" \
"buffer_load_dwordx4 v246, s[28:31], s74 offen lds\n" \
"v_mfma_f32_16x16x32_bf16 v[12:15], a[220:223], a[176:179], v[12:15]\n" \
"buffer_load_dwordx4 a[156:159], v248, s[32:35], s37 offen offset:1024 sc1\n" \
"v_add_u32 v228, s91, v228\n" \
"v_mfma_f32_16x16x32_bf16 v[44:47], a[220:223], a[180:183], v[44:47]\n" \
"buffer_load_dwordx4 a[224:227], v248, s[32:35], s79 offen offset:2048 sc1\n" \
"v_mov_b32 v229, 0xff000000\n" \
"v_mfma_f32_16x16x32_bf16 v[44:47], a[216:219], a[164:167], v[44:47]\n" \
"ds_read_b64_tr_b16 a[208:209], v240 offset:1536\n" \
"ds_read_b64_tr_b16 a[210:211], v240 offset:3584\n" \
"v_mfma_f32_16x16x32_bf16 v[76:79], a[216:219], a[168:171], v[76:79]\n" \
"ds_read_b64_tr_b16 a[212:213], v240 offset:9728\n" \
"v_cmp_gt_i32 s[96:97], 0, v228\n" \
"v_mfma_f32_16x16x32_bf16 v[76:79], a[220:223], a[184:187], v[76:79]\n" \
"v_cmp_gt_i32 s[98:99], 1, v228\n" \
"v_mfma_f32_16x16x32_bf16 v[108:111], a[220:223], a[188:191], v[108:111]\n" \
"ds_read_b64_tr_b16 a[214:215], v240 offset:11776\n" \
"v_cndmask_b32 v208, 0, v229, s[96:97]\n" \
"v_mfma_f32_16x16x32_bf16 v[108:111], a[216:219], a[172:175], v[108:111]\n" \
"s_waitcnt lgkmcnt(8)\n" \
"v_cndmask_b32 v209, 0, v229, s[98:99]\n" \
"v_cmp_gt_i32 s[96:97], 2, v228\n" \
"v_mfma_f32_16x16x32_bf16 v[16:19], a[192:195], a[160:163], v[16:19]\n" \
"ds_read_b64_tr_b16 a[216:217], v241 offset:1536\n" \
"v_cmp_gt_i32 s[98:99], 3, v228\n" \
"v_mfma_f32_16x16x32_bf16 v[16:19], a[196:199], a[176:179], v[16:19]\n" \
"v_cndmask_b32 v210, 0, v229, s[96:97]\n" \
"v_accvgpr_read_b32 v144, a40\n" \
"v_mfma_f32_16x16x32_bf16 v[48:51], a[196:199], a[180:183], v[48:51]\n" \
"buffer_load_dwordx4 a[228:231], v248, s[32:35], s80 offen offset:2048 sc1\n" \
"v_cndmask_b32 v211, 0, v229, s[98:99]\n" \
"v_mfma_f32_16x16x32_bf16 v[48:51], a[192:195], a[164:167], v[48:51]\n" \
"ds_read_b64_tr_b16 a[218:219], v241 offset:3584\n" \
"ds_read_b64_tr_b16 a[220:221], v241 offset:9728\n" \
"v_mfma_f32_16x16x32_bf16 v[80:83], a[192:195], a[168:171], v[80:83]\n" \
"ds_read_b64_tr_b16 a[222:223], v241 offset:11776\n" \
"v_cmp_gt_i32 s[96:97], 16, v228\n" \
"v_mfma_f32_16x16x32_bf16 v[80:83], a[196:199], a[184:187], v[80:83]\n" \
"v_cmp_gt_i32 s[98:99], 17, v228\n" \
"v_accvgpr_read_b32 v145, a41\n" \
"v_mfma_f32_16x16x32_bf16 v[112:115], a[196:199], a[188:191], v[112:115]\n" \
"v_cndmask_b32 v212, 0, v229, s[96:97]\n" \
"v_accvgpr_read_b32 v146, a42\n" \
"v_mfma_f32_16x16x32_bf16 v[112:115], a[192:195], a[172:175], v[112:115]\n" \
"s_waitcnt lgkmcnt(8)\n" \
"v_cndmask_b32 v213, 0, v229, s[98:99]\n" \
"v_cmp_gt_i32 s[96:97], 18, v228\n" \
"v_mfma_f32_16x16x32_bf16 v[20:23], a[200:203], a[160:163], v[20:23]\n" \
"v_cmp_gt_i32 s[98:99], 19, v228\n" \
"v_accvgpr_read_b32 v147, a43\n" \
"v_mfma_f32_16x16x32_bf16 v[20:23], a[204:207], a[176:179], v[20:23]\n" \
"v_cndmask_b32 v214, 0, v229, s[96:97]\n" \
"v_accvgpr_read_b32 v148, a44\n" \
"v_mfma_f32_16x16x32_bf16 v[52:55], a[204:207], a[180:183], v[52:55]\n" \
"buffer_load_dwordx4 a[232:235], v248, s[32:35], s36 offen offset:2048 sc1\n" \
"v_cndmask_b32 v215, 0, v229, s[98:99]\n" \
"v_mfma_f32_16x16x32_bf16 v[52:55], a[200:203], a[164:167], v[52:55]\n" \
"v_cmp_gt_i32 s[96:97], 32, v228\n" \
"v_cmp_gt_i32 s[98:99], 33, v228\n" \
"v_mfma_f32_16x16x32_bf16 v[84:87], a[200:203], a[168:171], v[84:87]\n" \
"s_waitcnt lgkmcnt(0)\n" \
"s_barrier\n" \
"v_accvgpr_read_b32 v149, a45\n" \
"v_mfma_f32_16x16x32_bf16 v[84:87], a[204:207], a[184:187], v[84:87]\n" \
"ds_read_b64_tr_b16 a[192:193], v240 offset:16384\n" \
"ds_read_b64_tr_b16 a[194:195], v240 offset:18432\n" \
"v_mfma_f32_16x16x32_bf16 v[116:119], a[204:207], a[188:191], v[116:119]\n" \
"ds_read_b64_tr_b16 a[196:197], v240 offset:24576\n" \
"ds_read_b64_tr_b16 a[198:199], v240 offset:26624\n" \
"v_mfma_f32_16x16x32_bf16 v[116:119], a[200:203], a[172:175], v[116:119]\n" \
"v_cndmask_b32 v216, 0, v229, s[96:97]\n" \
"v_cndmask_b32 v217, 0, v229, s[98:99]\n" \
"v_mfma_f32_16x16x32_bf16 v[24:27], a[208:211], a[160:163], v[24:27]\n" \
"ds_read_b64_tr_b16 a[200:201], v241 offset:16384\n" \
"v_cmp_gt_i32 s[96:97], 34, v228\n" \
"v_mfma_f32_16x16x32_bf16 v[24:27], a[212:215], a[176:179], v[24:27]\n" \
"s_add_u32 s85, s71, 593920\n" \
"v_cmp_gt_i32 s[98:99], 35, v228\n" \
"v_accvgpr_read_b32 v150, a46\n" \
"v_mfma_f32_16x16x32_bf16 v[56:59], a[212:215], a[180:183], v[56:59]\n" \
"buffer_load_dwordx4 a[236:239], v248, s[32:35], s37 offen offset:2048 sc1\n" \
"v_cndmask_b32 v218, 0, v229, s[96:97]\n" \
"v_mfma_f32_16x16x32_bf16 v[56:59], a[208:211], a[164:167], v[56:59]\n" \
"ds_read_b64_tr_b16 a[202:203], v241 offset:18432\n" \
"v_cndmask_b32 v219, 0, v229, s[98:99]\n" \
"v_mfma_f32_16x16x32_bf16 v[88:91], a[208:211], a[168:171], v[88:91]\n" \
"ds_read_b64_tr_b16 a[204:205], v241 offset:24576\n" \
"v_cmp_gt_i32 s[96:97], 48, v228\n" \
"v_mfma_f32_16x16x32_bf16 v[88:91], a[212:215], a[184:187], v[88:91]\n" \
"v_cmp_gt_i32 s[98:99], 49, v228\n" \
"v_accvgpr_read_b32 v151, a47\n" \
"v_mfma_f32_16x16x32_bf16 v[120:123], a[212:215], a[188:191], v[120:123]\n" \
"ds_read_b64_tr_b16 a[206:207], v241 offset:26624\n" \
"v_cndmask_b32 v220, 0, v229, s[96:97]\n" \
"v_mfma_f32_16x16x32_bf16 v[120:123], a[208:211], a[172:175], v[120:123]\n" \
"v_cndmask_b32 v221, 0, v229, s[98:99]\n" \
"v_cmp_gt_i32 s[96:97], 50, v228\n" \
"v_mfma_f32_16x16x32_bf16 v[28:31], a[216:219], a[160:163], v[28:31]\n" \
"ds_read_b64_tr_b16 a[208:209], v240 offset:16896\n" \
"v_cmp_gt_i32 s[98:99], 51, v228\n" \
"v_mfma_f32_16x16x32_bf16 v[28:31], a[220:223], a[176:179], v[28:31]\n" \
"v_cndmask_b32 v222, 0, v229, s[96:97]\n" \
"v_max3_f32 v250, |v144|, |v145|, v250\n" \
"v_mfma_f32_16x16x32_bf16 v[60:63], a[220:223], a[180:183], v[60:63]\n" \
"buffer_load_dwordx4 a[240:243], v248, s[32:35], s79 offen offset:3072 sc1\n" \
"v_cndmask_b32 v223, 0, v229, s[98:99]\n" \
"v_mfma_f32_16x16x32_bf16 v[60:63], a[216:219], a[164:167], v[60:63]\n" \
"s_lshl_b32 s91, %[wid], 5\n" \
"ds_read_b64_tr_b16 a[210:211], v240 offset:18944\n" \
"v_max3_f32 v250, |v146|, |v147|, v250\n" \
"v_mfma_f32_16x16x32_bf16 v[92:95], a[216:219], a[168:171], v[92:95]\n" \
"s_sub_u32 s91, 48, s91\n" \
"ds_read_b64_tr_b16 a[212:213], v240 offset:25088\n" \
"v_add_u32 v228, s91, v228\n" \
"v_mfma_f32_16x16x32_bf16 v[92:95], a[220:223], a[184:187], v[92:95]\n" \
"v_cmp_gt_i32 s[96:97], 0, v228\n" \
"v_cmp_gt_i32 s[98:99], 1, v228\n" \
"v_mfma_f32_16x16x32_bf16 v[124:127], a[220:223], a[188:191], v[124:127]\n" \
"s_waitcnt vmcnt(21)\n" \
"ds_read_b64_tr_b16 a[214:215], v240 offset:27136\n" \
"v_mfma_f32_16x16x32_bf16 v[124:127], a[216:219], a[172:175], v[124:127]\n" \
"s_waitcnt lgkmcnt(8)\n" \
"v_cndmask_b32 v192, 0, v229, s[96:97]\n" \
"v_cndmask_b32 v193, 0, v229, s[98:99]\n" \
"v_mfma_f32_16x16x32_bf16 v[0:3], a[192:195], a[0:3], v[0:3]\n" \
"ds_read_b64_tr_b16 a[216:217], v241 offset:16896\n" \
"v_cmp_gt_i32 s[96:97], 2, v228\n" \
"v_mfma_f32_16x16x32_bf16 v[0:3], a[196:199], a[16:19], v[0:3]\n" \
"ds_read_b128 a[160:163], v248 offset:8192\n" \
"v_cmp_gt_i32 s[98:99], 3, v228\n" \
"v_mfma_f32_16x16x32_bf16 v[32:35], a[196:199], a[20:23], v[32:35]\n" \
"buffer_load_dwordx4 a[244:247], v248, s[32:35], s80 offen offset:3072 sc1\n" \
"v_cndmask_b32 v194, 0, v229, s[96:97]\n" \
"v_mfma_f32_16x16x32_bf16 v[32:35], a[192:195], a[4:7], v[32:35]\n" \
"ds_read_b64_tr_b16 a[218:219], v241 offset:18944\n" \
"v_cndmask_b32 v195, 0, v229, s[98:99]\n" \
"v_mfma_f32_16x16x32_bf16 v[64:67], a[192:195], a[8:11], v[64:67]\n" \
"ds_read_b64_tr_b16 a[220:221], v241 offset:25088\n" \
"ds_read_b128 a[164:167], v248 offset:9216\n" \
"v_mfma_f32_16x16x32_bf16 v[64:67], a[196:199], a[24:27], v[64:67]\n" \
"v_cmp_gt_i32 s[96:97], 16, v228\n" \
"v_cmp_gt_i32 s[98:99], 17, v228\n" \
"v_mfma_f32_16x16x32_bf16 v[96:99], a[196:199], a[28:31], v[96:99]\n" \
"ds_read_b64_tr_b16 a[222:223], v241 offset:27136\n" \
"v_max3_f32 v250, |v148|, |v149|, v250\n" \
"v_mfma_f32_16x16x32_bf16 v[96:99], a[192:195], a[12:15], v[96:99]\n" \
"s_waitcnt lgkmcnt(10)\n" \
"ds_read_b128 a[168:171], v248 offset:10240\n" \
"v_cndmask_b32 v196, 0, v229, s[96:97]\n" \
"v_mfma_f32_16x16x32_bf16 v[4:7], a[200:203], a[0:3], v[4:7]\n" \
"ds_read_b64_tr_b16 a[192:193], v240 offset:17408\n" \
"v_cndmask_b32 v197, 0, v229, s[98:99]\n" \
"v_mfma_f32_16x16x32_bf16 v[4:7], a[204:207], a[16:19], v[4:7]\n" \
"v_cmp_gt_i32 s[96:97], 18, v228\n" \
"v_cmp_gt_i32 s[98:99], 19, v228\n" \
"v_mfma_f32_16x16x32_bf16 v[36:39], a[204:207], a[20:23], v[36:39]\n" \
"buffer_load_dwordx4 a[248:251], v248, s[32:35], s36 offen offset:3072 sc1\n" \
"v_max3_f32 v250, |v150|, |v151|, v250\n" \
"v_mfma_f32_16x16x32_bf16 v[36:39], a[200:203], a[4:7], v[36:39]\n" \
"ds_read_b64_tr_b16 a[194:195], v240 offset:19456\n" \
"ds_read_b128 a[172:175], v248 offset:11264\n" \
"v_mfma_f32_16x16x32_bf16 v[68:71], a[200:203], a[8:11], v[68:71]\n" \
"ds_read_b64_tr_b16 a[196:197], v240 offset:25600\n" \
"v_cndmask_b32 v198, 0, v229, s[96:97]\n" \
"v_mfma_f32_16x16x32_bf16 v[68:71], a[204:207], a[24:27], v[68:71]\n" \
"ds_read_b128 a[188:191], v248 offset:15360\n" \
"v_cndmask_b32 v199, 0, v229, s[98:99]\n" \
"v_mfma_f32_16x16x32_bf16 v[100:103], a[204:207], a[28:31], v[100:103]\n" \
"ds_read_b64_tr_b16 a[198:199], v240 offset:27648\n" \
"v_cmp_gt_i32 s[96:97], 32, v228\n" \
"v_mfma_f32_16x16x32_bf16 v[100:103], a[200:203], a[12:15], v[100:103]\n" \
"s_waitcnt lgkmcnt(13)\n" \
"v_cmp_gt_i32 s[98:99], 33, v228\n" \
"v_cvt_pk_bf16_f32 v144, v144, v145\n" \
"v_mfma_f32_16x16x32_bf16 v[8:11], a[208:211], a[0:3], v[8:11]\n" \
"ds_read_b64_tr_b16 a[200:201], v241 offset:17408\n" \
"ds_read_b128 a[184:187], v248 offset:14336\n" \
"v_mfma_f32_16x16x32_bf16 v[8:11], a[212:215], a[16:19], v[8:11]\n" \
"v_cndmask_b32 v200, 0, v229, s[96:97]\n" \
"v_cndmask_b32 v201, 0, v229, s[98:99]\n" \
"v_mfma_f32_16x16x32_bf16 v[40:43], a[212:215], a[20:23], v[40:43]\n" \
"buffer_load_dwordx4 a[252:255], v248, s[32:35], s37 offen offset:3072 sc1\n" \
"v_cmp_gt_i32 s[96:97], 34, v228\n" \
"v_mfma_f32_16x16x32_bf16 v[40:43], a[208:211], a[4:7], v[40:43]\n" \
"ds_read_b64_tr_b16 a[202:203], v241 offset:19456\n" \
"ds_read_b128 a[180:183], v248 offset:13312\n" \
"v_mfma_f32_16x16x32_bf16 v[72:75], a[208:211], a[8:11], v[72:75]\n" \
"ds_read_b64_tr_b16 a[204:205], v241 offset:25600\n" \
"v_cmp_gt_i32 s[98:99], 35, v228\n" \
"v_mfma_f32_16x16x32_bf16 v[72:75], a[212:215], a[24:27], v[72:75]\n" \
"v_cndmask_b32 v202, 0, v229, s[96:97]\n" \
"v_cvt_pk_bf16_f32 v145, v146, v147\n" \
"v_mfma_f32_16x16x32_bf16 v[104:107], a[212:215], a[28:31], v[104:107]\n" \
"ds_read_b64_tr_b16 a[206:207], v241 offset:27648\n" \
"ds_read_b128 a[176:179], v248 offset:12288\n" \
"v_mfma_f32_16x16x32_bf16 v[104:107], a[208:211], a[12:15], v[104:107]\n" \
"s_waitcnt lgkmcnt(14)\n" \
"v_cndmask_b32 v203, 0, v229, s[98:99]\n" \
"v_cmp_gt_i32 s[96:97], 48, v228\n" \
"v_mfma_f32_16x16x32_bf16 v[12:15], a[216:219], a[0:3], v[12:15]\n" \
"ds_read_b64_tr_b16 a[208:209], v240 offset:17920\n" \
"v_cmp_gt_i32 s[98:99], 49, v228\n" \
"v_mfma_f32_16x16x32_bf16 v[12:15], a[220:223], a[16:19], v[12:15]\n" \
"v_cndmask_b32 v204, 0, v229, s[96:97]\n" \
"v_cvt_pk_bf16_f32 v146, v148, v149\n" \
"v_mfma_f32_16x16x32_bf16 v[44:47], a[220:223], a[20:23], v[44:47]\n" \
"ds_read_b64_tr_b16 a[210:211], v240 offset:19968\n" \
"v_cndmask_b32 v205, 0, v229, s[98:99]\n" \
"v_mfma_f32_16x16x32_bf16 v[44:47], a[216:219], a[4:7], v[44:47]\n" \
"v_cmp_gt_i32 s[96:97], 50, v228\n" \
"v_cmp_gt_i32 s[98:99], 51, v228\n" \
"v_mfma_f32_16x16x32_bf16 v[76:79], a[216:219], a[8:11], v[76:79]\n" \
"ds_read_b64_tr_b16 a[212:213], v240 offset:26112\n" \
"v_cvt_pk_bf16_f32 v147, v150, v151\n" \
"v_mfma_f32_16x16x32_bf16 v[76:79], a[220:223], a[24:27], v[76:79]\n" \
"buffer_store_dwordx4 v[144:147], v248, s[32:35], s85 offen offset:1024 sc1\n" \
"v_mfma_f32_16x16x32_bf16 v[108:111], a[220:223], a[28:31], v[108:111]\n" \
"s_add_u32 s85, s94, 4096\n" \
"ds_read_b64_tr_b16 a[214:215], v240 offset:28160\n" \
"v_cndmask_b32 v206, 0, v229, s[96:97]\n" \
"v_mfma_f32_16x16x32_bf16 v[108:111], a[216:219], a[12:15], v[108:111]\n" \
"s_waitcnt lgkmcnt(11)\n" \
"ds_write_b128 v249, v[144:147] offset:5120\n" \
"v_cndmask_b32 v207, 0, v229, s[98:99]\n" \
"v_mfma_f32_16x16x32_bf16 v[16:19], a[192:195], a[0:3], v[16:19]\n" \
"buffer_store_dwordx4 v[184:187], v248, s[32:35], s85 offen offset:2048 sc1\n" \
"v_mfma_f32_16x16x32_bf16 v[16:19], a[196:199], a[16:19], v[16:19]\n" \
"ds_read_b64_tr_b16 a[216:217], v241 offset:17920\n" \
"v_accvgpr_read_b32 v152, a104\n" \
"v_mfma_f32_16x16x32_bf16 v[48:51], a[196:199], a[20:23], v[48:51]\n" \
"ds_read_b64_tr_b16 a[218:219], v241 offset:19968\n" \
"v_accvgpr_read_b32 v153, a105\n" \
"v_mfma_f32_16x16x32_bf16 v[48:51], a[192:195], a[4:7], v[48:51]\n" \
"ds_read_b64_tr_b16 a[220:221], v241 offset:26112\n" \
"v_accvgpr_read_b32 v154, a106\n" \
"v_mfma_f32_16x16x32_bf16 v[80:83], a[192:195], a[8:11], v[80:83]\n" \
"ds_read_b64_tr_b16 a[222:223], v241 offset:28160\n" \
"v_xor_b32 v240, 0x30000, v240\n" \
"v_mfma_f32_16x16x32_bf16 v[80:83], a[196:199], a[24:27], v[80:83]\n" \
"v_xor_b32 v241, 0x30000, v241\n" \
"v_accvgpr_read_b32 v155, a107\n" \
"v_mfma_f32_16x16x32_bf16 v[112:115], a[196:199], a[28:31], v[112:115]\n" \
"v_accvgpr_read_b32 v156, a108\n" \
"v_accvgpr_read_b32 v157, a109\n" \
"v_mfma_f32_16x16x32_bf16 v[112:115], a[192:195], a[12:15], v[112:115]\n" \
"s_waitcnt lgkmcnt(10)\n" \
"v_accvgpr_read_b32 v158, a110\n" \
"v_accvgpr_read_b32 v159, a111\n" \
"v_mfma_f32_16x16x32_bf16 v[20:23], a[200:203], a[0:3], v[20:23]\n" \
"v_max3_f32 v250, |v152|, |v153|, v250\n" \
"v_max3_f32 v250, |v154|, |v155|, v250\n" \
"v_mfma_f32_16x16x32_bf16 v[20:23], a[204:207], a[16:19], v[20:23]\n" \
"v_max3_f32 v250, |v156|, |v157|, v250\n" \
"v_max3_f32 v250, |v158|, |v159|, v250\n" \
"v_mfma_f32_16x16x32_bf16 v[52:55], a[204:207], a[20:23], v[52:55]\n" \
"v_cvt_pk_bf16_f32 v152, v152, v153\n" \
"v_cvt_pk_bf16_f32 v153, v154, v155\n" \
"v_mfma_f32_16x16x32_bf16 v[52:55], a[200:203], a[4:7], v[52:55]\n" \
"v_cvt_pk_bf16_f32 v154, v156, v157\n" \
"v_cvt_pk_bf16_f32 v155, v158, v159\n" \
"v_mfma_f32_16x16x32_bf16 v[84:87], a[200:203], a[8:11], v[84:87]\n" \
"s_waitcnt vmcnt(25) lgkmcnt(0)\n" \
"s_barrier\n" \
"v_accvgpr_write_b32 a104, v152\n" \
"v_mfma_f32_16x16x32_bf16 v[84:87], a[204:207], a[24:27], v[84:87]\n" \
"s_add_u32 s85, s71, 593920\n" \
"ds_read_b64_tr_b16 a[192:193], v240 offset:32768\n" \
"ds_read_b64_tr_b16 a[194:195], v240 offset:34816\n" \
"v_mfma_f32_16x16x32_bf16 v[116:119], a[204:207], a[28:31], v[116:119]\n" \
"ds_read_b64_tr_b16 a[196:197], v240 offset:40960\n" \
"ds_read_b64_tr_b16 a[198:199], v240 offset:43008\n" \
"v_mfma_f32_16x16x32_bf16 v[116:119], a[200:203], a[12:15], v[116:119]\n" \
"v_accvgpr_write_b32 a105, v153\n" \
"v_accvgpr_write_b32 a106, v154\n" \
"v_mfma_f32_16x16x32_bf16 v[24:27], a[208:211], a[0:3], v[24:27]\n" \
"ds_read_b64_tr_b16 a[200:201], v241 offset:32768\n" \
"v_accvgpr_write_b32 a107, v155\n" \
"v_mfma_f32_16x16x32_bf16 v[24:27], a[212:215], a[16:19], v[24:27]\n" \
"v_accvgpr_read_b32 v144, a48\n" \
"v_accvgpr_read_b32 v145, a49\n" \
"v_mfma_f32_16x16x32_bf16 v[56:59], a[212:215], a[20:23], v[56:59]\n" \
"ds_read_b64_tr_b16 a[202:203], v241 offset:34816\n" \
"v_accvgpr_read_b32 v146, a50\n" \
"v_mfma_f32_16x16x32_bf16 v[56:59], a[208:211], a[4:7], v[56:59]\n" \
"v_accvgpr_read_b32 v147, a51\n" \
"v_accvgpr_read_b32 v148, a52\n" \
"v_mfma_f32_16x16x32_bf16 v[88:91], a[208:211], a[8:11], v[88:91]\n" \
"ds_read_b64_tr_b16 a[204:205], v241 offset:40960\n" \
"v_accvgpr_read_b32 v149, a53\n" \
"v_mfma_f32_16x16x32_bf16 v[88:91], a[212:215], a[24:27], v[88:91]\n" \
"v_accvgpr_read_b32 v150, a54\n" \
"v_accvgpr_read_b32 v151, a55\n" \
"v_mfma_f32_16x16x32_bf16 v[120:123], a[212:215], a[28:31], v[120:123]\n" \
"ds_read_b64_tr_b16 a[206:207], v241 offset:43008\n" \
"v_max3_f32 v250, |v144|, |v145|, v250\n" \
"v_mfma_f32_16x16x32_bf16 v[120:123], a[208:211], a[12:15], v[120:123]\n" \
"v_max3_f32 v250, |v146|, |v147|, v250\n" \
"v_max3_f32 v250, |v148|, |v149|, v250\n" \
"v_mfma_f32_16x16x32_bf16 v[28:31], a[216:219], a[0:3], v[28:31]\n" \
"s_lshl2_add_u32 s76, s67, %[wid]\n" \
"ds_read_b64_tr_b16 a[208:209], v240 offset:33280\n" \
"v_max3_f32 v250, |v150|, |v151|, v250\n" \
"v_mfma_f32_16x16x32_bf16 v[28:31], a[220:223], a[16:19], v[28:31]\n" \
"s_sub_u32 s77, 15, s67\n" \
"v_cvt_pk_bf16_f32 v144, v144, v145\n" \
"v_cvt_pk_bf16_f32 v145, v146, v147\n" \
"v_mfma_f32_16x16x32_bf16 v[60:63], a[220:223], a[20:23], v[60:63]\n" \
"s_lshl_b32 s17, s77, 15\n" \
"ds_read_b64_tr_b16 a[210:211], v240 offset:35328\n" \
"v_cvt_pk_bf16_f32 v146, v148, v149\n" \
"v_mfma_f32_16x16x32_bf16 v[60:63], a[216:219], a[4:7], v[60:63]\n" \
"s_lshl2_add_u32 s77, s77, %[wid]\n" \
"s_lshl_b32 s28, %[wid], 12\n" \
"v_cvt_pk_bf16_f32 v147, v150, v151\n" \
"v_mfma_f32_16x16x32_bf16 v[92:95], a[216:219], a[8:11], v[92:95]\n" \
"s_add_u32 s88, s73, s28\n" \
"buffer_store_dwordx4 v[144:147], v248, s[32:35], s85 offen offset:2048 sc1\n" \
"v_mfma_f32_16x16x32_bf16 v[92:95], a[220:223], a[24:27], v[92:95]\n" \
"s_add_u32 s17, s17, s88\n" \
"s_add_u32 s18, s88, 524288\n" \
"ds_read_b64_tr_b16 a[212:213], v240 offset:41472\n" \
"v_mfma_f32_16x16x32_bf16 v[124:127], a[220:223], a[28:31], v[124:127]\n" \
"s_waitcnt vmcnt(11)\n" \
"ds_read_b64_tr_b16 a[214:215], v240 offset:43520\n" \
"v_mfma_f32_16x16x32_bf16 v[124:127], a[216:219], a[12:15], v[124:127]\n" \
"s_waitcnt lgkmcnt(8)\n" \
"ds_write_b128 v249, v[144:147] offset:6144\n" \
"v_accvgpr_read_b32 v152, a112\n" \
"v_mfma_f32_16x16x32_bf16 v[0:3], a[192:195], a[128:131], v[0:3]\n" \
"s_add_u32 s24, s88, 229376\n" \
"ds_read_b64_tr_b16 a[216:217], v241 offset:33280\n" \
"v_accvgpr_read_b32 v153, a113\n" \
"v_mfma_f32_16x16x32_bf16 v[0:3], a[196:199], a[144:147], v[0:3]\n" \
"s_lshl_b32 s16, s67, 15\n" \
"v_accvgpr_read_b32 v154, a114\n" \
"v_accvgpr_read_b32 v155, a115\n" \
"v_mfma_f32_16x16x32_bf16 v[32:35], a[196:199], a[148:151], v[32:35]\n" \
"s_add_u32 s16, s16, s88\n" \
"ds_read_b64_tr_b16 a[218:219], v241 offset:35328\n" \
"v_accvgpr_read_b32 v156, a116\n" \
"v_mfma_f32_16x16x32_bf16 v[32:35], a[192:195], a[132:135], v[32:35]\n" \
"s_add_u32 s21, s16, 0x8000\n" \
"s_cmp_eq_u32 s88, s16\n" \
"s_cmov_b32 s88, s21\n" \
"v_mfma_f32_16x16x32_bf16 v[64:67], a[192:195], a[136:139], v[64:67]\n" \
"s_mov_b32 s19, s28\n" \
"ds_read_b64_tr_b16 a[220:221], v241 offset:41472\n" \
"v_accvgpr_read_b32 v157, a117\n" \
"v_mfma_f32_16x16x32_bf16 v[64:67], a[196:199], a[152:155], v[64:67]\n" \
"s_add_u32 s20, s28, 0x10000\n" \
"v_accvgpr_read_b32 v158, a118\n" \
"v_accvgpr_read_b32 v159, a119\n" \
"v_mfma_f32_16x16x32_bf16 v[96:99], a[196:199], a[156:159], v[96:99]\n" \
"s_lshl_b32 s36, s64, 6\n" \
"ds_read_b64_tr_b16 a[222:223], v241 offset:43520\n" \
"v_max3_f32 v250, |v152|, |v153|, v250\n" \
"v_mfma_f32_16x16x32_bf16 v[96:99], a[192:195], a[140:143], v[96:99]\n" \
"s_waitcnt lgkmcnt(9)\n" \
"v_max3_f32 v250, |v154|, |v155|, v250\n" \
"v_max3_f32 v250, |v156|, |v157|, v250\n" \
"v_mfma_f32_16x16x32_bf16 v[4:7], a[200:203], a[128:131], v[4:7]\n" \
"s_add_u32 s36, s36, s77\n" \
"ds_read_b64_tr_b16 a[192:193], v240 offset:33792\n" \
"v_max3_f32 v250, |v158|, |v159|, v250\n" \
"v_mfma_f32_16x16x32_bf16 v[4:7], a[204:207], a[144:147], v[4:7]\n" \
"s_lshl2_add_u32 s36, s36, s66\n" \
"v_cvt_pk_bf16_f32 v152, v152, v153\n" \
"v_cvt_pk_bf16_f32 v153, v154, v155\n" \
"v_mfma_f32_16x16x32_bf16 v[36:39], a[204:207], a[148:151], v[36:39]\n" \
"s_lshl_b32 s36, s36, 13\n" \
"ds_read_b64_tr_b16 a[194:195], v240 offset:35840\n" \
"v_cvt_pk_bf16_f32 v154, v156, v157\n" \
"v_mfma_f32_16x16x32_bf16 v[36:39], a[200:203], a[132:135], v[36:39]\n" \
"s_add_u32 s85, s94, 4096\n" \
"v_cvt_pk_bf16_f32 v155, v158, v159\n" \
"v_accvgpr_write_b32 a112, v152\n" \
"v_mfma_f32_16x16x32_bf16 v[68:71], a[200:203], a[136:139], v[68:71]\n" \
"s_add_u32 s36, s36, 59768832\n" \
"buffer_store_dwordx4 v[188:191], v248, s[32:35], s85 offen offset:3072 sc1\n" \
"v_mfma_f32_16x16x32_bf16 v[68:71], a[204:207], a[152:155], v[68:71]\n" \
"s_lshr_b32 s83, %[wid], 1\n" \
"ds_read_b64_tr_b16 a[196:197], v240 offset:41984\n" \
"v_accvgpr_write_b32 a113, v153\n" \
"v_mfma_f32_16x16x32_bf16 v[100:103], a[204:207], a[156:159], v[100:103]\n" \
"s_mul_i32 s93, s83, 573440\n" \
"ds_read_b64_tr_b16 a[198:199], v240 offset:44032\n" \
"v_accvgpr_write_b32 a114, v154\n" \
"v_mfma_f32_16x16x32_bf16 v[100:103], a[200:203], a[140:143], v[100:103]\n" \
"s_waitcnt lgkmcnt(9)\n" \
"s_lshl_b32 s87, %[wid], 13\n" \
"v_accvgpr_write_b32 a115, v155\n" \
"v_mfma_f32_16x16x32_bf16 v[8:11], a[208:211], a[128:131], v[8:11]\n" \
"s_add_u32 s93, s93, s87\n" \
"s_add_u32 s93, s93, s73\n" \
"ds_read_b64_tr_b16 a[200:201], v241 offset:33792\n" \
"v_mfma_f32_16x16x32_bf16 v[8:11], a[212:215], a[144:147], v[8:11]\n" \
"s_add_u32 s93, s93, 573440\n" \
"s_add_u32 s87, s87, 0x20000\n" \
"s_mov_b32 s25, 1073741824\n" \
"v_mfma_f32_16x16x32_bf16 v[40:43], a[212:215], a[148:151], v[40:43]\n" \
"s_lshl_b32 s26, s68, 7\n" \
"s_add_u32 s26, s26, 655360\n" \
"ds_read_b64_tr_b16 a[202:203], v241 offset:35840\n" \
"v_mfma_f32_16x16x32_bf16 v[40:43], a[208:211], a[132:135], v[40:43]\n" \
"s_lshl_b32 s81, s68, 8\n" \
"s_add_u32 s81, s81, 81920\n" \
"s_lshl_b32 s84, s64, 15\n" \
"v_mfma_f32_16x16x32_bf16 v[72:75], a[208:211], a[136:139], v[72:75]\n" \
"s_add_u32 s84, s84, 41943040\n" \
"s_lshl_b32 s29, s76, 18\n" \
"ds_read_b64_tr_b16 a[204:205], v241 offset:41984\n" \
"v_mfma_f32_16x16x32_bf16 v[72:75], a[212:215], a[152:155], v[72:75]\n" \
"s_add_u32 s29, s29, s84\n" \
"s_lshl_b32 s37, s77, 18\n" \
"s_add_u32 s37, s37, s84\n" \
"v_mfma_f32_16x16x32_bf16 v[104:107], a[212:215], a[156:159], v[104:107]\n" \
"s_lshl_b32 s83, s66, 13\n" \
"ds_read_b64_tr_b16 a[206:207], v241 offset:44032\n" \
"v_accvgpr_read_b32 v144, a56\n" \
"v_mfma_f32_16x16x32_bf16 v[104:107], a[208:211], a[140:143], v[104:107]\n" \
"s_waitcnt lgkmcnt(8)\n" \
"v_accvgpr_read_b32 v145, a57\n" \
"v_accvgpr_read_b32 v146, a58\n" \
"v_mfma_f32_16x16x32_bf16 v[12:15], a[216:219], a[128:131], v[12:15]\n" \
"s_add_u32 s30, s29, s83\n" \
"ds_read_b64_tr_b16 a[208:209], v240 offset:34304\n" \
"v_accvgpr_read_b32 v147, a59\n" \
"v_mfma_f32_16x16x32_bf16 v[12:15], a[220:223], a[144:147], v[12:15]\n" \
"s_lshl3_add_u32 s83, s67, s64\n" \
"v_accvgpr_read_b32 v148, a60\n" \
"v_accvgpr_read_b32 v149, a61\n" \
"v_mfma_f32_16x16x32_bf16 v[44:47], a[220:223], a[148:151], v[44:47]\n" \
"s_lshl3_add_u32 s83, s83, %[wid]\n" \
"ds_read_b64_tr_b16 a[210:211], v240 offset:36352\n" \
"v_accvgpr_read_b32 v150, a62\n" \
"v_mfma_f32_16x16x32_bf16 v[44:47], a[216:219], a[132:135], v[44:47]\n" \
"s_lshl_b32 s83, s83, 4\n" \
"v_accvgpr_read_b32 v151, a63\n" \
"v_max3_f32 v250, |v144|, |v145|, v250\n" \
"v_mfma_f32_16x16x32_bf16 v[76:79], a[216:219], a[136:139], v[76:79]\n" \
"s_add_u32 s31, s83, 73728\n" \
"ds_read_b64_tr_b16 a[212:213], v240 offset:42496\n" \
"v_max3_f32 v250, |v146|, |v147|, v250\n" \
"v_mfma_f32_16x16x32_bf16 v[76:79], a[220:223], a[152:155], v[76:79]\n" \
"s_mul_i32 s69, s64, 28\n" \
"s_cmp_lt_u32 s65, 28\n" \
"s_cselect_b32 s83, s65, 0\n" \
"v_mfma_f32_16x16x32_bf16 v[108:111], a[220:223], a[156:159], v[108:111]\n" \
"s_add_u32 s69, s69, s83\n" \
"ds_read_b64_tr_b16 a[214:215], v240 offset:44544\n" \
"v_max3_f32 v250, |v148|, |v149|, v250\n" \
"v_mfma_f32_16x16x32_bf16 v[108:111], a[216:219], a[140:143], v[108:111]\n" \
"s_waitcnt lgkmcnt(8)\n" \
"v_max3_f32 v250, |v150|, |v151|, v250\n" \
"v_cvt_pk_bf16_f32 v144, v144, v145\n" \
"v_mfma_f32_16x16x32_bf16 v[16:19], a[192:195], a[128:131], v[16:19]\n" \
"s_lshl_b32 s79, s69, 13\n" \
"ds_read_b64_tr_b16 a[216:217], v241 offset:34304\n" \
"v_cvt_pk_bf16_f32 v145, v146, v147\n" \
"v_mfma_f32_16x16x32_bf16 v[16:19], a[196:199], a[144:147], v[16:19]\n" \
"s_lshl_b32 s83, %[wid], 8\n" \
"v_cvt_pk_bf16_f32 v146, v148, v149\n" \
"v_cvt_pk_bf16_f32 v147, v150, v151\n" \
"v_mfma_f32_16x16x32_bf16 v[48:51], a[196:199], a[148:151], v[48:51]\n" \
"s_add_u32 s79, s79, s83\n" \
"s_add_u32 s80, s79, 4096\n" \
"ds_read_b64_tr_b16 a[218:219], v241 offset:36352\n" \
"v_mfma_f32_16x16x32_bf16 v[48:51], a[192:195], a[132:135], v[48:51]\n" \
"s_mov_b64 s[42:43], s[54:55]\n" \
"ds_read_b64_tr_b16 a[220:221], v241 offset:42496\n" \
"v_mfma_f32_16x16x32_bf16 v[80:83], a[192:195], a[136:139], v[80:83]\n" \
"ds_read_b64_tr_b16 a[222:223], v241 offset:44544\n" \
"v_mfma_f32_16x16x32_bf16 v[80:83], a[196:199], a[152:155], v[80:83]\n" \
"s_bitcmp1_b32 %[bid], 6\n" \
"s_cselect_b64 s[40:41], s[56:57], s[52:53]\n" \
"s_cselect_b32 s39, 2048, 0\n" \
"v_mfma_f32_16x16x32_bf16 v[112:115], a[196:199], a[156:159], v[112:115]\n" \
"s_bfe_u32 s83, %[bid], 0x40002\n" \
"ds_write_b128 v249, v[144:147] offset:7168\n" \
"v_accvgpr_read_b32 v152, a120\n" \
"v_mfma_f32_16x16x32_bf16 v[112:115], a[192:195], a[140:143], v[112:115]\n" \
"s_waitcnt lgkmcnt(9)\n" \
"v_mov_b32 v228, 0x3f803f80\n" \
"v_accvgpr_write_b32 a48, v228\n" \
"v_mfma_f32_16x16x32_bf16 v[20:23], a[200:203], a[128:131], v[20:23]\n" \
"s_and_b32 s84, %[bid], 3\n" \
"v_accvgpr_write_b32 a49, v228\n" \
"v_accvgpr_write_b32 a50, v228\n" \
"v_mfma_f32_16x16x32_bf16 v[20:23], a[204:207], a[144:147], v[20:23]\n" \
"s_add_u32 s85, s71, 593920\n" \
"v_accvgpr_write_b32 a51, v228\n" \
"v_accvgpr_write_b32 a56, 1\n" \
"v_mfma_f32_16x16x32_bf16 v[52:55], a[204:207], a[148:151], v[52:55]\n" \
"s_lshl_b32 s22, %[wid], 3\n" \
"buffer_store_dwordx4 v[144:147], v248, s[32:35], s85 offen offset:3072 sc1\n" \
"v_mfma_f32_16x16x32_bf16 v[52:55], a[200:203], a[132:135], v[52:55]\n" \
"s_and_b32 s22, s22, 16\n" \
"v_accvgpr_read_b32 v153, a121\n" \
"v_accvgpr_read_b32 v154, a122\n" \
"v_mfma_f32_16x16x32_bf16 v[84:87], a[200:203], a[136:139], v[84:87]\n" \
"s_waitcnt lgkmcnt(1)\n" \
"s_barrier\n" \
"v_accvgpr_read_b32 v155, a123\n" \
"v_mfma_f32_16x16x32_bf16 v[84:87], a[204:207], a[152:155], v[84:87]\n" \
"s_and_b32 s23, %[wid], 1\n" \
"ds_read_b64_tr_b16 a[192:193], v240 offset:49152\n" \
"ds_read_b64_tr_b16 a[194:195], v240 offset:51200\n" \
"v_mfma_f32_16x16x32_bf16 v[116:119], a[204:207], a[156:159], v[116:119]\n" \
"s_add_u32 s38, s22, s23\n" \
"ds_read_b64_tr_b16 a[196:197], v240 offset:57344\n" \
"ds_read_b64_tr_b16 a[198:199], v240 offset:59392\n" \
"v_mfma_f32_16x16x32_bf16 v[116:119], a[200:203], a[140:143], v[116:119]\n" \
"s_lshl_b32 s72, s83, 5\n" \
"v_accvgpr_read_b32 v156, a124\n" \
"v_accvgpr_read_b32 v157, a125\n" \
"v_mfma_f32_16x16x32_bf16 v[24:27], a[208:211], a[128:131], v[24:27]\n" \
"s_add_u32 s38, s38, s72\n" \
"ds_read_b64_tr_b16 a[200:201], v241 offset:49152\n" \
"v_accvgpr_read_b32 v158, a126\n" \
"v_mfma_f32_16x16x32_bf16 v[24:27], a[212:215], a[144:147], v[24:27]\n" \
"s_mul_i32 s38, s38, 3584\n" \
"v_accvgpr_read_b32 v159, a127\n" \
"v_max3_f32 v250, |v152|, |v153|, v250\n" \
"v_mfma_f32_16x16x32_bf16 v[56:59], a[212:215], a[148:151], v[56:59]\n" \
".Lst0_7_%=:\n" \
"s_cbranch_execz .Lst0_11_%=\n" \
"s_lshl_b32 s72, s84, 7\n" \
"ds_read_b64_tr_b16 a[202:203], v241 offset:51200\n" \
"v_max3_f32 v250, |v154|, |v155|, v250\n" \
"v_mfma_f32_16x16x32_bf16 v[56:59], a[208:211], a[132:135], v[56:59]\n" \
"s_add_u32 s38, s38, s72\n" \
"v_max3_f32 v250, |v156|, |v157|, v250\n" \
"v_max3_f32 v250, |v158|, |v159|, v250\n" \
"v_mfma_f32_16x16x32_bf16 v[88:91], a[208:211], a[136:139], v[88:91]\n" \
"s_lshl3_add_u32 s39, s22, s39\n" \
"ds_read_b64_tr_b16 a[204:205], v241 offset:57344\n" \
"v_and_b32 v244, 31, v251\n" \
"v_mfma_f32_16x16x32_bf16 v[88:91], a[212:215], a[152:155], v[88:91]\n" \
"s_lshl_b32 s23, s23, 10\n" \
"v_lshlrev_b32 v244, 2, v244\n" \
"v_cvt_pk_bf16_f32 v152, v152, v153\n" \
"v_mfma_f32_16x16x32_bf16 v[120:123], a[212:215], a[156:159], v[120:123]\n" \
"s_add_u32 s39, s39, s23\n" \
"ds_read_b64_tr_b16 a[206:207], v241 offset:59392\n" \
"v_cvt_pk_bf16_f32 v153, v154, v155\n" \
"v_mfma_f32_16x16x32_bf16 v[120:123], a[208:211], a[140:143], v[120:123]\n" \
"s_lshl_b32 s22, s84, 17\n" \
"v_cvt_pk_bf16_f32 v154, v156, v157\n" \
"v_cvt_pk_bf16_f32 v155, v158, v159\n" \
"v_mfma_f32_16x16x32_bf16 v[28:31], a[216:219], a[128:131], v[28:31]\n" \
"s_add_u32 s39, s39, s22\n" \
"ds_read_b64_tr_b16 a[208:209], v240 offset:49664\n" \
"v_accvgpr_write_b32 a120, v152\n" \
"v_mfma_f32_16x16x32_bf16 v[28:31], a[220:223], a[144:147], v[28:31]\n" \
"s_lshr_b32 s22, s83, 2\n" \
"v_accvgpr_write_b32 a121, v153\n" \
"v_accvgpr_write_b32 a122, v154\n" \
"v_mfma_f32_16x16x32_bf16 v[60:63], a[220:223], a[148:151], v[60:63]\n" \
"s_lshl_b32 s22, s22, 12\n" \
"ds_read_b64_tr_b16 a[210:211], v240 offset:51712\n" \
"v_accvgpr_write_b32 a123, v155\n" \
"v_mfma_f32_16x16x32_bf16 v[60:63], a[216:219], a[132:135], v[60:63]\n" \
"s_add_u32 s39, s39, s22\n" \
"v_max_u32_dpp v250, v250, v250 quad_perm:[1,0,3,2] row_mask:0xf bank_mask:0xf\n" \
"v_lshlrev_b32 v252, 2, v251\n" \
"v_mfma_f32_16x16x32_bf16 v[92:95], a[216:219], a[136:139], v[92:95]\n" \
"s_and_b32 s22, s83, 3\n" \
"ds_read_b64_tr_b16 a[212:213], v240 offset:57856\n" \
"v_add_u32 v242, 0x10000, v248\n" \
"v_mfma_f32_16x16x32_bf16 v[92:95], a[220:223], a[152:155], v[92:95]\n" \
"s_lshl_b32 s22, s22, 8\n" \
"v_max_u32_dpp v250, v250, v250 quad_perm:[2,3,0,1] row_mask:0xf bank_mask:0xf\n" \
"v_xor_b32 v249, 0x4000, v249\n" \
"v_mfma_f32_16x16x32_bf16 v[124:127], a[220:223], a[156:159], v[124:127]\n" \
"s_waitcnt vmcnt(5)\n" \
"ds_read_b64_tr_b16 a[214:215], v240 offset:59904\n" \
"v_mfma_f32_16x16x32_bf16 v[124:127], a[216:219], a[140:143], v[124:127]\n" \
"s_waitcnt lgkmcnt(8)\n" \
"s_add_u32 s39, s39, s22\n" \
"v_max_u32_dpp v250, v250, v250 row_half_mirror row_mask:0xf bank_mask:0xf\n" \
"v_mfma_f32_16x16x32_bf16 v[0:3], a[192:195], a[224:227], v[0:3]\n" \
"s_add_u32 s39, s39, 77070336\n" \
"s_and_b32 s83, %[bid], 127\n" \
"ds_read_b64_tr_b16 a[216:217], v241 offset:49664\n" \
"v_mfma_f32_16x16x32_bf16 v[0:3], a[196:199], a[240:243], v[0:3]\n" \
"s_lshl_b32 s78, %[wid], 7\n" \
"ds_read_b64_tr_b16 a[218:219], v241 offset:51712\n" \
"ds_read_b64_tr_b16 a[220:221], v241 offset:57856\n" \
"v_mfma_f32_16x16x32_bf16 v[32:35], a[196:199], a[244:247], v[32:35]\n" \
"s_add_u32 s78, s78, s83\n" \
"ds_read_b64_tr_b16 a[222:223], v241 offset:59904\n" \
"ds_read_b128 a[128:131], v248 offset:0\n" \
"v_mfma_f32_16x16x32_bf16 v[32:35], a[192:195], a[228:231], v[32:35]\n" \
"s_waitcnt lgkmcnt(9)\n" \
"v_max_u32_dpp v250, v250, v250 row_mirror row_mask:0xf bank_mask:0xf\n" \
"v_mov_b32 v152, v250\n" \
"v_mfma_f32_16x16x32_bf16 v[4:7], a[200:203], a[224:227], v[4:7]\n" \
"s_lshl_b32 s78, s78, 2\n" \
"ds_read_b64_tr_b16 a[192:193], v240 offset:50176\n" \
"ds_read_b128 a[132:135], v248 offset:1024\n" \
"v_mfma_f32_16x16x32_bf16 v[4:7], a[204:207], a[240:243], v[4:7]\n" \
"s_add_u32 s78, s78, 659456\n" \
"ds_read_b64_tr_b16 a[194:195], v240 offset:52224\n" \
"ds_read_b64_tr_b16 a[196:197], v240 offset:58368\n" \
"v_mfma_f32_16x16x32_bf16 v[36:39], a[204:207], a[244:247], v[36:39]\n" \
"s_cmp_lt_u32 s67, 4\n" \
"s_cselect_b32 s78, 1073741824, s78\n" \
"ds_read_b64_tr_b16 a[198:199], v240 offset:60416\n" \
"v_mfma_f32_16x16x32_bf16 v[36:39], a[200:203], a[228:231], v[36:39]\n" \
"s_waitcnt lgkmcnt(10)\n" \
"s_lshl2_add_u32 s72, s83, %[wid]\n" \
"ds_read_b128 a[136:139], v248 offset:2048\n" \
"v_mfma_f32_16x16x32_bf16 v[8:11], a[208:211], a[224:227], v[8:11]\n" \
"s_mul_i32 s72, s72, 3584\n" \
"s_mov_b32 s76, 0\n" \
"ds_read_b64_tr_b16 a[200:201], v241 offset:50176\n" \
"v_mfma_f32_16x16x32_bf16 v[8:11], a[212:215], a[240:243], v[8:11]\n" \
"s_lshl_b32 s84, s67, 1\n" \
"ds_read_b64_tr_b16 a[202:203], v241 offset:52224\n" \
"ds_read_b64_tr_b16 a[204:205], v241 offset:58368\n" \
"v_mfma_f32_16x16x32_bf16 v[40:43], a[212:215], a[244:247], v[40:43]\n" \
"s_sub_u32 s84, s84, 11\n" \
"ds_read_b64_tr_b16 a[206:207], v241 offset:60416\n" \
"ds_read_b128 a[140:143], v248 offset:3072\n" \
"v_mfma_f32_16x16x32_bf16 v[40:43], a[208:211], a[228:231], v[40:43]\n" \
"s_waitcnt lgkmcnt(12)\n" \
"v_permlane16_swap_b32 v250, v152\n" \
"v_mfma_f32_16x16x32_bf16 v[12:15], a[216:219], a[224:227], v[12:15]\n" \
"s_max_i32 s84, s84, 0\n" \
"ds_read_b64_tr_b16 a[208:209], v240 offset:50688\n" \
"v_max_u32 v250, v250, v152\n" \
"v_mfma_f32_16x16x32_bf16 v[12:15], a[220:223], a[240:243], v[12:15]\n" \
"s_add_u32 s83, s84, s67\n" \
"ds_read_b64_tr_b16 a[210:211], v240 offset:52736\n" \
"ds_read_b64_tr_b16 a[212:213], v240 offset:58880\n" \
"v_mfma_f32_16x16x32_bf16 v[44:47], a[220:223], a[244:247], v[44:47]\n" \
"s_and_b32 s83, s83, 1\n" \
"ds_read_b64_tr_b16 a[214:215], v240 offset:60928\n" \
"ds_read_b128 a[156:159], v248 offset:7168\n" \
"v_mfma_f32_16x16x32_bf16 v[44:47], a[216:219], a[228:231], v[44:47]\n" \
"s_waitcnt lgkmcnt(11)\n" \
"s_lshl_b32 s83, s83, 15\n" \
"v_mov_b32 v152, v250\n" \
"v_mfma_f32_16x16x32_bf16 v[16:19], a[192:195], a[224:227], v[16:19]\n" \
"s_movk_i32 s27, 0x4000\n" \
"ds_read_b64_tr_b16 a[216:217], v241 offset:50688\n" \
"ds_read_b128 a[152:155], v248 offset:6144\n" \
"v_mfma_f32_16x16x32_bf16 v[16:19], a[196:199], a[240:243], v[16:19]\n" \
"s_mov_b32 s92, s21\n" \
"ds_read_b64_tr_b16 a[218:219], v241 offset:52736\n" \
"ds_read_b64_tr_b16 a[220:221], v241 offset:58880\n" \
"v_mfma_f32_16x16x32_bf16 v[48:51], a[196:199], a[244:247], v[48:51]\n" \
"s_sub_u32 s22, s18, 524288\n" \
"s_lshl_b32 s77, s84, 16\n" \
"ds_read_b64_tr_b16 a[222:223], v241 offset:60928\n" \
"v_mfma_f32_16x16x32_bf16 v[48:51], a[192:195], a[228:231], v[48:51]\n" \
"s_waitcnt lgkmcnt(11)\n" \
"s_add_u32 s77, s77, s22\n" \
"ds_read_b128 a[148:151], v248 offset:5120\n" \
"v_mfma_f32_16x16x32_bf16 v[20:23], a[200:203], a[224:227], v[20:23]\n" \
"s_movk_i32 s91, 0x4000\n" \
"ds_read_b64_tr_b16 a[192:193], v240 offset:49152\n" \
"v_mfma_f32_16x16x32_bf16 v[20:23], a[204:207], a[240:243], v[20:23]\n" \
"ds_read_b64_tr_b16 a[194:195], v240 offset:51200\n" \
"ds_read_b64_tr_b16 a[196:197], v240 offset:57344\n" \
"v_mfma_f32_16x16x32_bf16 v[52:55], a[204:207], a[244:247], v[52:55]\n" \
"ds_read_b64_tr_b16 a[198:199], v240 offset:59392\n" \
"ds_read_b128 a[144:147], v248 offset:4096\n" \
"v_mfma_f32_16x16x32_bf16 v[52:55], a[200:203], a[228:231], v[52:55]\n" \
"s_waitcnt lgkmcnt(12)\n" \
"v_permlane32_swap_b32 v250, v152\n" \
"v_mfma_f32_16x16x32_bf16 v[24:27], a[208:211], a[224:227], v[24:27]\n" \
"ds_read_b64_tr_b16 a[200:201], v241 offset:49152\n" \
"v_max_u32 v250, v250, v152\n" \
"v_mfma_f32_16x16x32_bf16 v[24:27], a[212:215], a[240:243], v[24:27]\n" \
"ds_read_b64_tr_b16 a[202:203], v241 offset:51200\n" \
"ds_read_b64_tr_b16 a[204:205], v241 offset:57344\n" \
"v_mfma_f32_16x16x32_bf16 v[56:59], a[212:215], a[244:247], v[56:59]\n" \
"ds_read_b64_tr_b16 a[206:207], v241 offset:59392\n" \
"v_max_u32 v250, 1, v250\n" \
"v_mfma_f32_16x16x32_bf16 v[56:59], a[208:211], a[228:231], v[56:59]\n" \
"s_waitcnt lgkmcnt(10)\n" \
"v_cvt_pk_bf16_f32 v144, v0, v1\n" \
"v_cvt_pk_bf16_f32 v145, v2, v3\n" \
"v_mfma_f32_16x16x32_bf16 v[28:31], a[216:219], a[224:227], v[28:31]\n" \
"ds_read_b64_tr_b16 a[208:209], v240 offset:49664\n" \
"v_cvt_pk_bf16_f32 v146, v4, v5\n" \
"v_mfma_f32_16x16x32_bf16 v[28:31], a[220:223], a[240:243], v[28:31]\n" \
"ds_read_b64_tr_b16 a[210:211], v240 offset:51712\n" \
"ds_read_b64_tr_b16 a[212:213], v240 offset:57856\n" \
"v_mfma_f32_16x16x32_bf16 v[60:63], a[220:223], a[244:247], v[60:63]\n" \
"ds_read_b64_tr_b16 a[214:215], v240 offset:59904\n" \
"v_cvt_pk_bf16_f32 v147, v6, v7\n" \
"v_mfma_f32_16x16x32_bf16 v[60:63], a[216:219], a[228:231], v[60:63]\n" \
"s_waitcnt lgkmcnt(9)\n" \
"v_cvt_pk_bf16_f32 v148, v8, v9\n" \
"v_cvt_pk_bf16_f32 v149, v10, v11\n" \
"v_mfma_f32_16x16x32_bf16 v[64:67], a[192:195], a[232:235], v[64:67]\n" \
"ds_read_b64_tr_b16 a[216:217], v241 offset:49664\n" \
"v_cvt_pk_bf16_f32 v150, v12, v13\n" \
"v_mfma_f32_16x16x32_bf16 v[64:67], a[196:199], a[248:251], v[64:67]\n" \
"ds_read_b64_tr_b16 a[218:219], v241 offset:51712\n" \
"ds_read_b64_tr_b16 a[220:221], v241 offset:57856\n" \
"v_mfma_f32_16x16x32_bf16 v[96:99], a[196:199], a[252:255], v[96:99]\n" \
"ds_read_b64_tr_b16 a[222:223], v241 offset:59904\n" \
"v_cvt_pk_bf16_f32 v151, v14, v15\n" \
"v_mfma_f32_16x16x32_bf16 v[96:99], a[192:195], a[236:239], v[96:99]\n" \
"s_waitcnt lgkmcnt(8)\n" \
"v_cvt_pk_bf16_f32 v152, v16, v17\n" \
"v_cvt_pk_bf16_f32 v153, v18, v19\n" \
"v_mfma_f32_16x16x32_bf16 v[68:71], a[200:203], a[232:235], v[68:71]\n" \
"ds_read_b64_tr_b16 a[192:193], v240 offset:50176\n" \
"v_cvt_pk_bf16_f32 v154, v20, v21\n" \
"v_mfma_f32_16x16x32_bf16 v[68:71], a[204:207], a[248:251], v[68:71]\n" \
"ds_read_b64_tr_b16 a[194:195], v240 offset:52224\n" \
"ds_read_b64_tr_b16 a[196:197], v240 offset:58368\n" \
"v_mfma_f32_16x16x32_bf16 v[100:103], a[204:207], a[252:255], v[100:103]\n" \
"ds_read_b64_tr_b16 a[198:199], v240 offset:60416\n" \
"v_cvt_pk_bf16_f32 v155, v22, v23\n" \
"v_mfma_f32_16x16x32_bf16 v[100:103], a[200:203], a[236:239], v[100:103]\n" \
"s_waitcnt lgkmcnt(8)\n" \
"v_cvt_pk_bf16_f32 v156, v24, v25\n" \
"v_cvt_pk_bf16_f32 v157, v26, v27\n" \
"v_mfma_f32_16x16x32_bf16 v[72:75], a[208:211], a[232:235], v[72:75]\n" \
"ds_read_b64_tr_b16 a[200:201], v241 offset:50176\n" \
"v_cvt_pk_bf16_f32 v158, v28, v29\n" \
"v_mfma_f32_16x16x32_bf16 v[72:75], a[212:215], a[248:251], v[72:75]\n" \
"ds_read_b64_tr_b16 a[202:203], v241 offset:52224\n" \
"ds_read_b64_tr_b16 a[204:205], v241 offset:58368\n" \
"v_mfma_f32_16x16x32_bf16 v[104:107], a[212:215], a[252:255], v[104:107]\n" \
"ds_read_b64_tr_b16 a[206:207], v241 offset:60416\n" \
"v_cvt_pk_bf16_f32 v159, v30, v31\n" \
"v_mfma_f32_16x16x32_bf16 v[104:107], a[208:211], a[236:239], v[104:107]\n" \
"s_waitcnt lgkmcnt(8)\n" \
"v_cvt_pk_bf16_f32 v228, v32, v33\n" \
"v_cvt_pk_bf16_f32 v229, v34, v35\n" \
"v_mfma_f32_16x16x32_bf16 v[76:79], a[216:219], a[232:235], v[76:79]\n" \
"ds_read_b64_tr_b16 a[208:209], v240 offset:50688\n" \
"v_cvt_pk_bf16_f32 v230, v36, v37\n" \
"v_mfma_f32_16x16x32_bf16 v[76:79], a[220:223], a[248:251], v[76:79]\n" \
"ds_read_b64_tr_b16 a[210:211], v240 offset:52736\n" \
"ds_read_b64_tr_b16 a[212:213], v240 offset:58880\n" \
"v_mfma_f32_16x16x32_bf16 v[108:111], a[220:223], a[252:255], v[108:111]\n" \
"ds_read_b64_tr_b16 a[214:215], v240 offset:60928\n" \
"v_cvt_pk_bf16_f32 v231, v38, v39\n" \
"v_mfma_f32_16x16x32_bf16 v[108:111], a[216:219], a[236:239], v[108:111]\n" \
"s_waitcnt lgkmcnt(8)\n" \
"v_accvgpr_write_b32 a16, v228\n" \
"v_accvgpr_write_b32 a17, v229\n" \
"v_mfma_f32_16x16x32_bf16 v[80:83], a[192:195], a[232:235], v[80:83]\n" \
"ds_read_b64_tr_b16 a[216:217], v241 offset:50688\n" \
"v_accvgpr_write_b32 a18, v230\n" \
"v_mfma_f32_16x16x32_bf16 v[80:83], a[196:199], a[248:251], v[80:83]\n" \
"ds_read_b64_tr_b16 a[218:219], v241 offset:52736\n" \
"ds_read_b64_tr_b16 a[220:221], v241 offset:58880\n" \
"v_mfma_f32_16x16x32_bf16 v[112:115], a[196:199], a[252:255], v[112:115]\n" \
"ds_read_b64_tr_b16 a[222:223], v241 offset:60928\n" \
"v_accvgpr_write_b32 a19, v231\n" \
"v_mfma_f32_16x16x32_bf16 v[112:115], a[192:195], a[236:239], v[112:115]\n" \
"s_waitcnt lgkmcnt(8)\n" \
"v_cvt_pk_bf16_f32 v232, v40, v41\n" \
"v_cvt_pk_bf16_f32 v233, v42, v43\n" \
"v_mfma_f32_16x16x32_bf16 v[84:87], a[200:203], a[232:235], v[84:87]\n" \
"s_cmp_lg_u32 s84, 0\n" \
"s_cmov_b32 s21, s22\n" \
"s_cmov_b32 s91, 0x8000\n" \
"v_mfma_f32_16x16x32_bf16 v[84:87], a[204:207], a[248:251], v[84:87]\n" \
"s_lshl_b32 s85, s68, 4\n" \
"v_cvt_pk_bf16_f32 v234, v44, v45\n" \
"v_cvt_pk_bf16_f32 v235, v46, v47\n" \
"v_mfma_f32_16x16x32_bf16 v[116:119], a[204:207], a[252:255], v[116:119]\n" \
"s_add_u32 s85, s85, s67\n" \
"v_accvgpr_write_b32 a20, v232\n" \
"v_accvgpr_write_b32 a21, v233\n" \
"v_mfma_f32_16x16x32_bf16 v[116:119], a[200:203], a[236:239], v[116:119]\n" \
"s_waitcnt lgkmcnt(4)\n" \
"v_accvgpr_write_b32 a22, v234\n" \
"v_accvgpr_write_b32 a23, v235\n" \
"v_mfma_f32_16x16x32_bf16 v[88:91], a[208:211], a[232:235], v[88:91]\n" \
"s_lshl2_add_u32 s85, s85, %[wid]\n" \
"v_cvt_pk_bf16_f32 v228, v48, v49\n" \
"v_cvt_pk_bf16_f32 v229, v50, v51\n" \
"v_mfma_f32_16x16x32_bf16 v[88:91], a[212:215], a[248:251], v[88:91]\n" \
"s_lshl_b32 s85, s85, 2\n" \
"v_cvt_pk_bf16_f32 v230, v52, v53\n" \
"v_cvt_pk_bf16_f32 v231, v54, v55\n" \
"v_mfma_f32_16x16x32_bf16 v[120:123], a[212:215], a[252:255], v[120:123]\n" \
"s_waitcnt vmcnt(0)\n" \
"s_add_u32 s85, s85, 82048\n" \
"buffer_store_dword v250, v238, s[32:35], s85 offen sc1\n" \
"v_mfma_f32_16x16x32_bf16 v[120:123], a[208:211], a[236:239], v[120:123]\n" \
"s_waitcnt lgkmcnt(0)\n" \
"v_accvgpr_write_b32 a24, v228\n" \
"v_accvgpr_write_b32 a25, v229\n" \
"v_mfma_f32_16x16x32_bf16 v[92:95], a[216:219], a[232:235], v[92:95]\n" \
"v_accvgpr_write_b32 a26, v230\n" \
"v_accvgpr_write_b32 a27, v231\n" \
"v_mfma_f32_16x16x32_bf16 v[92:95], a[220:223], a[248:251], v[92:95]\n" \
"v_cvt_pk_bf16_f32 v232, v56, v57\n" \
"v_cvt_pk_bf16_f32 v233, v58, v59\n" \
"v_mfma_f32_16x16x32_bf16 v[124:127], a[220:223], a[252:255], v[124:127]\n" \
"v_cvt_pk_bf16_f32 v234, v60, v61\n" \
"v_cvt_pk_bf16_f32 v235, v62, v63\n" \
"v_mfma_f32_16x16x32_bf16 v[124:127], a[216:219], a[236:239], v[124:127]\n" \
"v_accvgpr_write_b32 a28, v232\n" \
"v_accvgpr_write_b32 a29, v233\n" \
  \
".Lat_b_%=:\n" \
"v_mfma_f32_16x16x32_bf16 v[128:131], a[128:131], v[144:147], v[208:211]\n" \
"v_accvgpr_write_b32 a30, v234\n" \
"v_accvgpr_write_b32 a31, v235\n" \
"v_mfma_f32_16x16x32_bf16 v[128:131], a[132:135], v[148:151], v[128:131]\n" \
"s_barrier\n" \
"v_accvgpr_write_b32 a0, v144\n" \
"v_accvgpr_write_b32 a1, v145\n" \
"v_mfma_f32_16x16x32_bf16 v[128:131], a[136:139], v[152:155], v[128:131]\n" \
"ds_read_b128 a[224:227], v242 offset:8192\n" \
"v_accvgpr_write_b32 a2, v146\n" \
"v_mfma_f32_16x16x32_bf16 v[128:131], a[140:143], v[156:159], v[128:131]\n" \
"ds_read_b128 a[192:195], v242 offset:0\n" \
"v_accvgpr_write_b32 a3, v147\n" \
"v_mfma_f32_16x16x32_bf16 v[132:135], a[156:159], v[156:159], v[212:215]\n" \
"ds_read_b128 a[196:199], v242 offset:1024\n" \
"v_accvgpr_write_b32 a4, v148\n" \
"v_mfma_f32_16x16x32_bf16 v[132:135], a[152:155], v[152:155], v[132:135]\n" \
"ds_read_b128 a[228:231], v242 offset:9216\n" \
"v_accvgpr_write_b32 a5, v149\n" \
"v_mfma_f32_16x16x32_bf16 v[132:135], a[148:151], v[148:151], v[132:135]\n" \
"v_exp_f32 v128, v128\n" \
"v_mfma_f32_16x16x32_bf16 v[132:135], a[144:147], v[144:147], v[132:135]\n" \
"s_add_u32 s85, s94, 589824\n" \
"v_exp_f32 v129, v129\n" \
"v_mfma_f32_16x16x32_bf16 v[136:139], a[160:163], v[144:147], v[216:219]\n" \
"buffer_store_dwordx4 a[64:67], v248, s[32:35], s85 offen offset:0 sc1\n" \
"v_mfma_f32_16x16x32_bf16 v[136:139], a[164:167], v[148:151], v[136:139]\n" \
"v_exp_f32 v130, v130\n" \
"v_mfma_f32_16x16x32_bf16 v[136:139], a[168:171], v[152:155], v[136:139]\n" \
"v_exp_f32 v131, v131\n" \
"v_mfma_f32_16x16x32_bf16 v[136:139], a[172:175], v[156:159], v[136:139]\n" \
"v_cvt_pk_bf16_f32 v128, v128, v129\n" \
"v_cvt_pk_bf16_f32 v129, v130, v131\n" \
"v_mfma_f32_16x16x32_bf16 v[140:143], a[188:191], v[156:159], v[220:223]\n" \
"v_exp_f32 v132, v132\n" \
"v_mfma_f32_16x16x32_bf16 v[140:143], a[184:187], v[152:155], v[140:143]\n" \
"v_exp_f32 v133, v133\n" \
"v_mfma_f32_16x16x32_bf16 v[140:143], a[180:183], v[148:151], v[140:143]\n" \
"v_exp_f32 v134, v134\n" \
"v_mfma_f32_16x16x32_bf16 v[140:143], a[176:179], v[144:147], v[140:143]\n" \
"ds_read_b128 a[232:235], v242 offset:10240\n" \
"v_cvt_pk_bf16_f32 v130, v132, v133\n" \
"v_mfma_f32_16x16x32_bf16 v[144:147], a[128:131], a[16:19], 0\n" \
"v_exp_f32 v135, v135\n" \
"v_mfma_f32_16x16x32_bf16 v[144:147], a[132:135], a[20:23], v[144:147]\n" \
"v_exp_f32 v136, v136\n" \
"v_mfma_f32_16x16x32_bf16 v[144:147], a[136:139], a[24:27], v[144:147]\n" \
"ds_read_b128 a[200:203], v242 offset:2048\n" \
"v_cvt_pk_bf16_f32 v131, v134, v135\n" \
"v_accvgpr_write_b32 a6, v150\n" \
"v_accvgpr_write_b32 a7, v151\n" \
"v_mfma_f32_16x16x32_bf16 v[144:147], a[140:143], a[28:31], v[144:147]\n" \
"v_exp_f32 v137, v137\n" \
"v_mfma_f32_16x16x32_bf16 v[148:151], a[156:159], a[28:31], 0\n" \
"v_exp_f32 v138, v138\n" \
"v_mfma_f32_16x16x32_bf16 v[148:151], a[152:155], a[24:27], v[148:151]\n" \
"v_exp_f32 v139, v139\n" \
"v_mfma_f32_16x16x32_bf16 v[148:151], a[148:151], a[20:23], v[148:151]\n" \
"v_cvt_pk_bf16_f32 v136, v136, v137\n" \
"v_cvt_pk_bf16_f32 v137, v138, v139\n" \
"v_accvgpr_write_b32 a8, v152\n" \
"v_accvgpr_write_b32 a9, v153\n" \
"v_accvgpr_write_b32 a10, v154\n" \
"v_accvgpr_write_b32 a11, v155\n" \
"v_mfma_f32_16x16x32_bf16 v[148:151], a[144:147], a[16:19], v[148:151]\n" \
"v_exp_f32 v140, v140\n" \
"v_mfma_f32_16x16x32_bf16 v[152:155], a[160:163], a[16:19], 0\n" \
"v_exp_f32 v141, v141\n" \
"v_mfma_f32_16x16x32_bf16 v[152:155], a[164:167], a[20:23], v[152:155]\n" \
"s_add_u32 s85, s94, 589824\n" \
"ds_read_b128 a[204:207], v242 offset:3072\n" \
"v_cvt_pk_bf16_f32 v138, v140, v141\n" \
"v_mfma_f32_16x16x32_bf16 v[152:155], a[168:171], a[24:27], v[152:155]\n" \
"v_exp_f32 v142, v142\n" \
"v_accvgpr_write_b32 a12, v156\n" \
"v_accvgpr_write_b32 a13, v157\n" \
"v_accvgpr_write_b32 a14, v158\n" \
"v_accvgpr_write_b32 a15, v159\n" \
"v_mfma_f32_16x16x32_bf16 v[152:155], a[172:175], a[28:31], v[152:155]\n" \
"v_exp_f32 v143, v143\n" \
"v_mfma_f32_16x16x32_bf16 v[156:159], a[188:191], a[28:31], 0\n" \
"buffer_store_dwordx4 a[72:75], v248, s[32:35], s85 offen offset:1024 sc1\n" \
"v_mfma_f32_16x16x32_bf16 v[156:159], a[184:187], a[24:27], v[156:159]\n" \
"ds_read_b128 a[236:239], v242 offset:11264\n" \
"v_cvt_pk_bf16_f32 v139, v142, v143\n" \
"v_mfma_f32_16x16x32_bf16 v[156:159], a[180:183], a[20:23], v[156:159]\n" \
"v_exp_f32 v144, v144\n" \
"v_mfma_f32_16x16x32_bf16 v[156:159], a[176:179], a[16:19], v[156:159]\n" \
"ds_read_b128 a[240:243], v242 offset:12288\n" \
"ds_read_b128 a[208:211], v242 offset:4096\n" \
"v_mfma_f32_16x16x32_bf16 a[32:35], a[48:51], v[128:131], 0\n" \
"v_exp_f32 v145, v145\n" \
"v_mfma_f32_16x16x32_bf16 a[32:35], a[48:51], v[136:139], a[32:35]\n" \
"s_waitcnt lgkmcnt(6)\n" \
"ds_read_b128 a[212:215], v242 offset:5120\n" \
"v_cvt_pk_bf16_f32 v144, v144, v145\n" \
"v_mfma_f32_16x16x32_bf16 v[0:3], a[224:227], v[136:139], 0\n" \
"v_exp_f32 v146, v146\n" \
"v_mfma_f32_16x16x32_bf16 v[0:3], a[192:195], v[128:131], v[0:3]\n" \
"ds_read_b128 a[244:247], v242 offset:13312\n" \
"ds_read_b128 a[248:251], v242 offset:14336\n" \
"v_mfma_f32_16x16x32_bf16 v[4:7], a[196:199], v[128:131], 0\n" \
"v_exp_f32 v147, v147\n" \
"v_mfma_f32_16x16x32_bf16 v[4:7], a[228:231], v[136:139], v[4:7]\n" \
"s_waitcnt lgkmcnt(5)\n" \
"ds_read_b128 a[216:219], v242 offset:6144\n" \
"v_cvt_pk_bf16_f32 v145, v146, v147\n" \
"v_mfma_f32_16x16x32_bf16 v[8:11], a[232:235], v[136:139], 0\n" \
"v_exp_f32 v148, v148\n" \
"v_mfma_f32_16x16x32_bf16 v[8:11], a[200:203], v[128:131], v[8:11]\n" \
"ds_read_b128 a[220:223], v242 offset:7168\n" \
"ds_read_b128 a[252:255], v242 offset:15360\n" \
"v_mfma_f32_16x16x32_bf16 v[12:15], a[204:207], v[128:131], 0\n" \
"v_exp_f32 v149, v149\n" \
"v_mfma_f32_16x16x32_bf16 v[12:15], a[236:239], v[136:139], v[12:15]\n" \
"s_waitcnt lgkmcnt(4)\n" \
"v_cvt_pk_bf16_f32 v146, v148, v149\n" \
"v_cvt_pk_bf16_f32 v160, v64, v65\n" \
"v_mfma_f32_16x16x32_bf16 v[16:19], a[240:243], v[136:139], 0\n" \
"v_exp_f32 v150, v150\n" \
"v_mfma_f32_16x16x32_bf16 v[16:19], a[208:211], v[128:131], v[16:19]\n" \
"s_add_u32 s85, s94, 589824\n" \
"v_cvt_pk_bf16_f32 v161, v66, v67\n" \
"v_cvt_pk_bf16_f32 v162, v68, v69\n" \
"v_mfma_f32_16x16x32_bf16 v[20:23], a[212:215], v[128:131], 0\n" \
"v_exp_f32 v151, v151\n" \
"v_mfma_f32_16x16x32_bf16 v[20:23], a[244:247], v[136:139], v[20:23]\n" \
"s_waitcnt lgkmcnt(0)\n" \
"buffer_store_dwordx4 a[80:83], v248, s[32:35], s85 offen offset:2048 sc1\n" \
"v_mfma_f32_16x16x32_bf16 v[24:27], a[248:251], v[136:139], 0\n" \
"v_cvt_pk_bf16_f32 v147, v150, v151\n" \
"v_cvt_pk_bf16_f32 v163, v70, v71\n" \
"v_mfma_f32_16x16x32_bf16 v[24:27], a[216:219], v[128:131], v[24:27]\n" \
"v_exp_f32 v152, v152\n" \
"v_mfma_f32_16x16x32_bf16 v[28:31], a[220:223], v[128:131], 0\n" \
"v_cvt_pk_bf16_f32 v164, v72, v73\n" \
"v_cvt_pk_bf16_f32 v165, v74, v75\n" \
"v_mfma_f32_16x16x32_bf16 v[28:31], a[252:255], v[136:139], v[28:31]\n" \
"v_exp_f32 v153, v153\n" \
"v_cvt_pk_bf16_f32 v166, v76, v77\n" \
"v_cvt_pk_bf16_f32 v167, v78, v79\n" \
"v_mfma_f32_16x16x32_bf16 v[128:131], a[128:131], v[160:163], 0\n" \
"v_cvt_pk_bf16_f32 v152, v152, v153\n" \
"v_cvt_pk_bf16_f32 v168, v80, v81\n" \
"v_cvt_pk_bf16_f32 v169, v82, v83\n" \
"v_cvt_pk_bf16_f32 v170, v84, v85\n" \
"v_cvt_pk_bf16_f32 v171, v86, v87\n" \
"v_mfma_f32_16x16x32_bf16 v[128:131], a[132:135], v[164:167], v[128:131]\n" \
"v_exp_f32 v154, v154\n" \
"v_cvt_pk_bf16_f32 v172, v88, v89\n" \
"v_cvt_pk_bf16_f32 v173, v90, v91\n" \
"v_cvt_pk_bf16_f32 v174, v92, v93\n" \
"v_cvt_pk_bf16_f32 v175, v94, v95\n" \
"v_mfma_f32_16x16x32_bf16 v[128:131], a[136:139], v[168:171], v[128:131]\n" \
"v_cvt_pk_bf16_f32 v176, v96, v97\n" \
"v_cvt_pk_bf16_f32 v177, v98, v99\n" \
"v_mfma_f32_16x16x32_bf16 v[128:131], a[140:143], v[172:175], v[128:131]\n" \
"v_exp_f32 v155, v155\n" \
"v_mfma_f32_16x16x32_bf16 v[132:135], a[156:159], v[172:175], 0\n" \
"v_cvt_pk_bf16_f32 v153, v154, v155\n" \
"v_cvt_pk_bf16_f32 v178, v100, v101\n" \
"v_mfma_f32_16x16x32_bf16 v[132:135], a[152:155], v[168:171], v[132:135]\n" \
"v_exp_f32 v156, v156\n" \
"v_mfma_f32_16x16x32_bf16 v[132:135], a[148:151], v[164:167], v[132:135]\n" \
"v_cvt_pk_bf16_f32 v179, v102, v103\n" \
"v_cvt_pk_bf16_f32 v180, v104, v105\n" \
"v_mfma_f32_16x16x32_bf16 v[132:135], a[144:147], v[160:163], v[132:135]\n" \
"v_exp_f32 v157, v157\n" \
"v_mfma_f32_16x16x32_bf16 v[136:139], a[160:163], v[160:163], 0\n" \
"v_cvt_pk_bf16_f32 v154, v156, v157\n" \
"v_cvt_pk_bf16_f32 v181, v106, v107\n" \
"v_mfma_f32_16x16x32_bf16 v[136:139], a[164:167], v[164:167], v[136:139]\n" \
"v_exp_f32 v158, v158\n" \
"v_mfma_f32_16x16x32_bf16 v[136:139], a[168:171], v[168:171], v[136:139]\n" \
"ds_write_b128 v249, a[64:67] offset:32768\n" \
"v_cvt_pk_bf16_f32 v182, v108, v109\n" \
"v_mfma_f32_16x16x32_bf16 v[136:139], a[172:175], v[172:175], v[136:139]\n" \
"s_add_u32 s85, s94, 589824\n" \
"v_exp_f32 v159, v159\n" \
"v_mfma_f32_16x16x32_bf16 v[140:143], a[188:191], v[172:175], 0\n" \
"buffer_store_dwordx4 a[88:91], v248, s[32:35], s85 offen offset:3072 sc1\n" \
"v_mfma_f32_16x16x32_bf16 v[140:143], a[184:187], v[168:171], v[140:143]\n" \
"v_cvt_pk_bf16_f32 v155, v158, v159\n" \
"v_cvt_pk_bf16_f32 v183, v110, v111\n" \
"v_mfma_f32_16x16x32_bf16 v[140:143], a[180:183], v[164:167], v[140:143]\n" \
"v_exp_f32 v128, v128\n" \
"v_mfma_f32_16x16x32_bf16 v[140:143], a[176:179], v[160:163], v[140:143]\n" \
"v_cvt_pk_bf16_f32 v184, v112, v113\n" \
"v_cvt_pk_bf16_f32 v185, v114, v115\n" \
"v_mfma_f32_16x16x32_bf16 a[36:39], a[48:51], v[144:147], 0\n" \
"v_exp_f32 v129, v129\n" \
"v_mfma_f32_16x16x32_bf16 a[36:39], a[48:51], v[152:155], a[36:39]\n" \
"v_cvt_pk_bf16_f32 v128, v128, v129\n" \
"v_cvt_pk_bf16_f32 v186, v116, v117\n" \
"v_mfma_f32_16x16x32_bf16 v[32:35], a[224:227], v[152:155], 0\n" \
"v_exp_f32 v130, v130\n" \
"v_mfma_f32_16x16x32_bf16 v[32:35], a[192:195], v[144:147], v[32:35]\n" \
"v_cvt_pk_bf16_f32 v187, v118, v119\n" \
"v_cvt_pk_bf16_f32 v188, v120, v121\n" \
"v_mfma_f32_16x16x32_bf16 v[36:39], a[196:199], v[144:147], 0\n" \
"v_exp_f32 v131, v131\n" \
"v_mfma_f32_16x16x32_bf16 v[36:39], a[228:231], v[152:155], v[36:39]\n" \
"v_cvt_pk_bf16_f32 v129, v130, v131\n" \
"v_cvt_pk_bf16_f32 v189, v122, v123\n" \
"v_mfma_f32_16x16x32_bf16 v[40:43], a[232:235], v[152:155], 0\n" \
"v_exp_f32 v132, v132\n" \
"v_mfma_f32_16x16x32_bf16 v[40:43], a[200:203], v[144:147], v[40:43]\n" \
"v_cvt_pk_bf16_f32 v190, v124, v125\n" \
"v_cvt_pk_bf16_f32 v191, v126, v127\n" \
"v_mfma_f32_16x16x32_bf16 v[44:47], a[204:207], v[144:147], 0\n" \
"v_exp_f32 v133, v133\n" \
"v_mfma_f32_16x16x32_bf16 v[44:47], a[236:239], v[152:155], v[44:47]\n" \
"ds_write_b128 v249, a[72:75] offset:33792\n" \
"v_cvt_pk_bf16_f32 v130, v132, v133\n" \
"v_mfma_f32_16x16x32_bf16 v[48:51], a[240:243], v[152:155], 0\n" \
"v_exp_f32 v134, v134\n" \
"v_mfma_f32_16x16x32_bf16 v[48:51], a[208:211], v[144:147], v[48:51]\n" \
"v_mfma_f32_16x16x32_bf16 v[52:55], a[212:215], v[144:147], 0\n" \
"v_exp_f32 v135, v135\n" \
"v_mfma_f32_16x16x32_bf16 v[52:55], a[244:247], v[152:155], v[52:55]\n" \
"s_add_u32 s85, s94, 593920\n" \
"v_cvt_pk_bf16_f32 v131, v134, v135\n" \
"v_mfma_f32_16x16x32_bf16 v[56:59], a[248:251], v[152:155], 0\n" \
"buffer_store_dwordx4 a[96:99], v248, s[32:35], s85 offen offset:0 sc1\n" \
"v_mfma_f32_16x16x32_bf16 v[56:59], a[216:219], v[144:147], v[56:59]\n" \
"v_exp_f32 v136, v136\n" \
"v_mfma_f32_16x16x32_bf16 v[60:63], a[220:223], v[144:147], 0\n" \
"v_mfma_f32_16x16x32_bf16 v[60:63], a[252:255], v[152:155], v[60:63]\n" \
"v_exp_f32 v137, v137\n" \
"v_mfma_f32_16x16x32_bf16 v[144:147], a[128:131], v[176:179], 0\n" \
"v_cvt_pk_bf16_f32 v136, v136, v137\n" \
"v_mfma_f32_16x16x32_bf16 v[144:147], a[132:135], v[180:183], v[144:147]\n" \
"v_exp_f32 v138, v138\n" \
"v_mfma_f32_16x16x32_bf16 v[144:147], a[136:139], v[184:187], v[144:147]\n" \
"ds_read_b128 a[128:131], v248 offset:16384\n" \
"ds_read_b128 a[132:135], v248 offset:17408\n" \
"v_mfma_f32_16x16x32_bf16 v[144:147], a[140:143], v[188:191], v[144:147]\n" \
"v_exp_f32 v139, v139\n" \
"v_mfma_f32_16x16x32_bf16 v[148:151], a[156:159], v[188:191], 0\n" \
"ds_write_b128 v249, a[80:83] offset:34816\n" \
"v_cvt_pk_bf16_f32 v137, v138, v139\n" \
"v_mfma_f32_16x16x32_bf16 v[148:151], a[152:155], v[184:187], v[148:151]\n" \
"v_exp_f32 v140, v140\n" \
"v_mfma_f32_16x16x32_bf16 v[148:151], a[148:151], v[180:183], v[148:151]\n" \
"ds_read_b128 a[136:139], v248 offset:18432\n" \
"ds_read_b128 a[140:143], v248 offset:19456\n" \
"v_mfma_f32_16x16x32_bf16 v[148:151], a[144:147], v[176:179], v[148:151]\n" \
"v_exp_f32 v141, v141\n" \
"v_mfma_f32_16x16x32_bf16 v[152:155], a[160:163], v[176:179], 0\n" \
"ds_read_b128 a[156:159], v248 offset:23552\n" \
"v_cvt_pk_bf16_f32 v138, v140, v141\n" \
"v_mfma_f32_16x16x32_bf16 v[152:155], a[164:167], v[180:183], v[152:155]\n" \
"v_exp_f32 v142, v142\n" \
"v_mfma_f32_16x16x32_bf16 v[152:155], a[168:171], v[184:187], v[152:155]\n" \
"ds_read_b128 a[152:155], v248 offset:22528\n" \
"v_mfma_f32_16x16x32_bf16 v[152:155], a[172:175], v[188:191], v[152:155]\n" \
"v_exp_f32 v143, v143\n" \
"v_mfma_f32_16x16x32_bf16 v[156:159], a[188:191], v[188:191], 0\n" \
"ds_read_b128 a[148:151], v248 offset:21504\n" \
"v_cvt_pk_bf16_f32 v139, v142, v143\n" \
"v_mfma_f32_16x16x32_bf16 v[156:159], a[184:187], v[184:187], v[156:159]\n" \
"s_add_u32 s85, s94, 593920\n" \
"ds_read_b128 a[144:147], v248 offset:20480\n" \
"v_mfma_f32_16x16x32_bf16 v[156:159], a[180:183], v[180:183], v[156:159]\n" \
"v_exp_f32 v144, v144\n" \
"v_mfma_f32_16x16x32_bf16 v[156:159], a[176:179], v[176:179], v[156:159]\n" \
"buffer_store_dwordx4 a[104:107], v248, s[32:35], s85 offen offset:1024 sc1\n" \
"v_mfma_f32_16x16x32_bf16 a[40:43], a[48:51], v[128:131], 0\n" \
"v_exp_f32 v145, v145\n" \
"v_mfma_f32_16x16x32_bf16 a[40:43], a[48:51], v[136:139], a[40:43]\n" \
"v_mov_b32 v240, v248\n" \
"v_cvt_pk_bf16_f32 v144, v144, v145\n" \
"v_mfma_f32_16x16x32_bf16 v[64:67], a[224:227], v[136:139], 0\n" \
"v_exp_f32 v146, v146\n" \
"v_mfma_f32_16x16x32_bf16 v[64:67], a[192:195], v[128:131], v[64:67]\n" \
"ds_write_b128 v249, a[88:91] offset:35840\n" \
"v_and_b32 v230, 7, v251\n" \
"v_mfma_f32_16x16x32_bf16 v[68:71], a[196:199], v[128:131], 0\n" \
"v_exp_f32 v147, v147\n" \
"v_mfma_f32_16x16x32_bf16 v[68:71], a[228:231], v[136:139], v[68:71]\n" \
"ds_read_b128 a[160:163], v248 offset:24576\n" \
"v_cvt_pk_bf16_f32 v145, v146, v147\n" \
"v_mfma_f32_16x16x32_bf16 v[72:75], a[232:235], v[136:139], 0\n" \
"v_exp_f32 v148, v148\n" \
"v_mfma_f32_16x16x32_bf16 v[72:75], a[200:203], v[128:131], v[72:75]\n" \
"ds_read_b128 a[164:167], v248 offset:25600\n" \
"ds_read_b128 a[168:171], v248 offset:26624\n" \
"v_mfma_f32_16x16x32_bf16 v[76:79], a[204:207], v[128:131], 0\n" \
"v_exp_f32 v149, v149\n" \
"v_mfma_f32_16x16x32_bf16 v[76:79], a[236:239], v[136:139], v[76:79]\n" \
"ds_read_b128 a[172:175], v248 offset:27648\n" \
"v_cvt_pk_bf16_f32 v146, v148, v149\n" \
"v_mfma_f32_16x16x32_bf16 v[80:83], a[240:243], v[136:139], 0\n" \
"v_exp_f32 v150, v150\n" \
"v_mfma_f32_16x16x32_bf16 v[80:83], a[208:211], v[128:131], v[80:83]\n" \
".Lst0_8_%=:\n" \
"s_cbranch_execz .Lst0_12_%=\n" \
"buffer_load_dword v253, v252, s[32:35], s81 offen sc1\n" \
"v_lshrrev_b32 v232, 3, v251\n" \
"v_mfma_f32_16x16x32_bf16 v[84:87], a[212:215], v[128:131], 0\n" \
"v_exp_f32 v151, v151\n" \
"v_mfma_f32_16x16x32_bf16 v[84:87], a[244:247], v[136:139], v[84:87]\n" \
"ds_read_b128 a[188:191], v248 offset:31744\n" \
"v_cvt_pk_bf16_f32 v147, v150, v151\n" \
"v_mfma_f32_16x16x32_bf16 v[88:91], a[248:251], v[136:139], 0\n" \
"ds_read_b128 a[184:187], v248 offset:30720\n" \
"ds_read_b128 a[180:183], v248 offset:29696\n" \
"v_mfma_f32_16x16x32_bf16 v[88:91], a[216:219], v[128:131], v[88:91]\n" \
"s_add_u32 s85, s94, 593920\n" \
"v_exp_f32 v152, v152\n" \
"v_mfma_f32_16x16x32_bf16 v[92:95], a[220:223], v[128:131], 0\n" \
"buffer_store_dwordx4 a[112:115], v248, s[32:35], s85 offen offset:2048 sc1\n" \
"v_mfma_f32_16x16x32_bf16 v[92:95], a[252:255], v[136:139], v[92:95]\n" \
"s_waitcnt lgkmcnt(12)\n" \
"v_exp_f32 v153, v153\n" \
"v_mfma_f32_16x16x32_bf16 v[128:131], a[128:131], a[16:19], v[192:195]\n" \
"ds_write_b128 v249, a[96:99] offset:36864\n" \
"v_cvt_pk_bf16_f32 v152, v152, v153\n" \
"v_mfma_f32_16x16x32_bf16 v[128:131], a[132:135], a[20:23], v[128:131]\n" \
"v_exp_f32 v154, v154\n" \
"v_mfma_f32_16x16x32_bf16 v[128:131], a[136:139], a[24:27], v[128:131]\n" \
"ds_read_b128 a[176:179], v248 offset:28672\n" \
"v_lshlrev_b32 v231, 4, v230\n" \
"v_mfma_f32_16x16x32_bf16 v[128:131], a[140:143], a[28:31], v[128:131]\n" \
"s_waitcnt lgkmcnt(10)\n" \
"v_exp_f32 v155, v155\n" \
"v_mfma_f32_16x16x32_bf16 v[132:135], a[156:159], a[28:31], v[196:199]\n" \
"v_cvt_pk_bf16_f32 v153, v154, v155\n" \
"v_mul_u32_u24 v230, 7168, v230\n" \
"v_mfma_f32_16x16x32_bf16 v[132:135], a[152:155], a[24:27], v[132:135]\n" \
"v_exp_f32 v156, v156\n" \
"v_mfma_f32_16x16x32_bf16 v[132:135], a[148:151], a[20:23], v[132:135]\n" \
"v_lshl_add_u32 v230, v232, 4, v230\n" \
"v_lshl_or_b32 v232, v232, 14, v231\n" \
"v_mfma_f32_16x16x32_bf16 v[132:135], a[144:147], a[16:19], v[132:135]\n" \
"s_waitcnt lgkmcnt(5)\n" \
"v_exp_f32 v157, v157\n" \
"v_mfma_f32_16x16x32_bf16 v[136:139], a[160:163], a[16:19], v[200:203]\n" \
"v_cvt_pk_bf16_f32 v154, v156, v157\n" \
"v_and_b32 v231, 3, v251\n" \
"v_mfma_f32_16x16x32_bf16 v[136:139], a[164:167], a[20:23], v[136:139]\n" \
"v_exp_f32 v158, v158\n" \
"v_mfma_f32_16x16x32_bf16 v[136:139], a[168:171], a[24:27], v[136:139]\n" \
"v_lshlrev_b32 v231, 7, v231\n" \
"v_mfma_f32_16x16x32_bf16 v[136:139], a[172:175], a[28:31], v[136:139]\n" \
"s_waitcnt lgkmcnt(0)\n" \
"v_exp_f32 v159, v159\n" \
"v_mfma_f32_16x16x32_bf16 v[140:143], a[188:191], a[28:31], v[204:207]\n" \
"ds_write_b128 v249, a[104:107] offset:37888\n" \
"v_cvt_pk_bf16_f32 v155, v158, v159\n" \
"v_mfma_f32_16x16x32_bf16 v[140:143], a[184:187], a[24:27], v[140:143]\n" \
"v_mfma_f32_16x16x32_bf16 v[140:143], a[180:183], a[20:23], v[140:143]\n" \
"v_exp_f32 v128, v128\n" \
"v_mfma_f32_16x16x32_bf16 v[140:143], a[176:179], a[16:19], v[140:143]\n" \
"s_add_u32 s85, s94, 593920\n" \
"v_mfma_f32_16x16x32_bf16 a[44:47], a[48:51], v[144:147], 0\n" \
"v_exp_f32 v129, v129\n" \
"v_mfma_f32_16x16x32_bf16 a[44:47], a[48:51], v[152:155], a[44:47]\n" \
"buffer_store_dwordx4 a[120:123], v248, s[32:35], s85 offen offset:3072 sc1\n" \
"v_mfma_f32_16x16x32_bf16 v[96:99], a[224:227], v[152:155], 0\n" \
"v_exp_f32 v130, v130\n" \
"v_mfma_f32_16x16x32_bf16 v[96:99], a[192:195], v[144:147], v[96:99]\n" \
"ds_read_b128 a[224:227], v242 offset:24576\n" \
"v_cvt_pk_bf16_f32 v128, v128, v129\n" \
"v_mfma_f32_16x16x32_bf16 v[100:103], a[196:199], v[144:147], 0\n" \
"v_exp_f32 v131, v131\n" \
"v_mfma_f32_16x16x32_bf16 v[100:103], a[228:231], v[152:155], v[100:103]\n" \
"ds_read_b128 a[192:195], v242 offset:16384\n" \
"v_cvt_pk_bf16_f32 v129, v130, v131\n" \
"v_mfma_f32_16x16x32_bf16 v[104:107], a[232:235], v[152:155], 0\n" \
"v_exp_f32 v132, v132\n" \
"v_mfma_f32_16x16x32_bf16 v[104:107], a[200:203], v[144:147], v[104:107]\n" \
"ds_read_b128 a[196:199], v242 offset:17408\n" \
"ds_read_b128 a[228:231], v242 offset:25600\n" \
"v_mfma_f32_16x16x32_bf16 v[108:111], a[204:207], v[144:147], 0\n" \
"v_exp_f32 v133, v133\n" \
"v_mfma_f32_16x16x32_bf16 v[108:111], a[236:239], v[152:155], v[108:111]\n" \
"ds_read_b128 a[232:235], v242 offset:26624\n" \
"v_cvt_pk_bf16_f32 v130, v132, v133\n" \
"v_mfma_f32_16x16x32_bf16 v[112:115], a[240:243], v[152:155], 0\n" \
"v_exp_f32 v134, v134\n" \
"v_mfma_f32_16x16x32_bf16 v[112:115], a[208:211], v[144:147], v[112:115]\n" \
"ds_write_b128 v249, a[112:115] offset:38912\n" \
"v_mfma_f32_16x16x32_bf16 v[116:119], a[212:215], v[144:147], 0\n" \
"v_exp_f32 v135, v135\n" \
"v_mfma_f32_16x16x32_bf16 v[116:119], a[244:247], v[152:155], v[116:119]\n" \
"ds_read_b128 a[200:203], v242 offset:18432\n" \
"v_cvt_pk_bf16_f32 v131, v134, v135\n" \
"v_mfma_f32_16x16x32_bf16 v[120:123], a[248:251], v[152:155], 0\n" \
"ds_read_b128 a[204:207], v242 offset:19456\n" \
"ds_read_b128 a[236:239], v242 offset:27648\n" \
"v_mfma_f32_16x16x32_bf16 v[120:123], a[216:219], v[144:147], v[120:123]\n" \
"v_exp_f32 v136, v136\n" \
"v_mfma_f32_16x16x32_bf16 v[124:127], a[220:223], v[144:147], 0\n" \
"ds_read_b128 a[240:243], v242 offset:28672\n" \
"v_mfma_f32_16x16x32_bf16 v[124:127], a[252:255], v[152:155], v[124:127]\n" \
"v_exp_f32 v137, v137\n" \
"v_mfma_f32_16x16x32_bf16 v[144:147], a[128:131], v[160:163], 0\n" \
"ds_read_b128 a[208:211], v242 offset:20480\n" \
"v_cvt_pk_bf16_f32 v136, v136, v137\n" \
"v_mfma_f32_16x16x32_bf16 v[144:147], a[132:135], v[164:167], v[144:147]\n" \
"v_exp_f32 v138, v138\n" \
"v_mfma_f32_16x16x32_bf16 v[144:147], a[136:139], v[168:171], v[144:147]\n" \
"ds_read_b128 a[212:215], v242 offset:21504\n" \
"ds_read_b128 a[244:247], v242 offset:29696\n" \
"v_mfma_f32_16x16x32_bf16 v[144:147], a[140:143], v[172:175], v[144:147]\n" \
"v_exp_f32 v139, v139\n" \
"v_mfma_f32_16x16x32_bf16 v[148:151], a[156:159], v[172:175], 0\n" \
"ds_read_b128 a[248:251], v242 offset:30720\n" \
"v_cvt_pk_bf16_f32 v137, v138, v139\n" \
"v_mfma_f32_16x16x32_bf16 v[148:151], a[152:155], v[168:171], v[148:151]\n" \
"v_exp_f32 v140, v140\n" \
"v_mfma_f32_16x16x32_bf16 v[148:151], a[148:151], v[164:167], v[148:151]\n" \
"ds_write_b128 v249, a[120:123] offset:39936\n" \
"v_mfma_f32_16x16x32_bf16 v[148:151], a[144:147], v[160:163], v[148:151]\n" \
"v_exp_f32 v141, v141\n" \
"v_mfma_f32_16x16x32_bf16 v[152:155], a[160:163], v[160:163], 0\n" \
"ds_read_b128 a[216:219], v242 offset:22528\n" \
"v_cvt_pk_bf16_f32 v138, v140, v141\n" \
"v_mfma_f32_16x16x32_bf16 v[152:155], a[164:167], v[164:167], v[152:155]\n" \
"v_exp_f32 v142, v142\n" \
"v_mfma_f32_16x16x32_bf16 v[152:155], a[168:171], v[168:171], v[152:155]\n" \
"ds_read_b128 a[220:223], v242 offset:23552\n" \
"ds_read_b128 a[252:255], v242 offset:31744\n" \
"v_mfma_f32_16x16x32_bf16 v[152:155], a[172:175], v[172:175], v[152:155]\n" \
"v_exp_f32 v143, v143\n" \
"v_mfma_f32_16x16x32_bf16 v[156:159], a[188:191], v[172:175], 0\n" \
"v_cvt_pk_bf16_f32 v139, v142, v143\n" \
"v_mfma_f32_16x16x32_bf16 v[156:159], a[184:187], v[168:171], v[156:159]\n" \
"v_mfma_f32_16x16x32_bf16 v[156:159], a[180:183], v[164:167], v[156:159]\n" \
"s_waitcnt vmcnt(2)\n" \
"v_cmp_eq_u32 vcc, 0, v253\n" \
"v_mfma_f32_16x16x32_bf16 v[156:159], a[176:179], v[160:163], v[156:159]\n" \
"v_exp_f32 v144, v144\n" \
"v_mfma_f32_16x16x32_bf16 a[36:39], a[48:51], v[128:131], a[36:39]\n" \
"v_exp_f32 v145, v145\n" \
"v_mfma_f32_16x16x32_bf16 a[36:39], a[48:51], v[136:139], a[36:39]\n" \
"s_waitcnt lgkmcnt(14)\n" \
"s_cbranch_vccnz .Lspv_%=\n" \
".Lsbv_%=:\n" \
"v_mfma_f32_16x16x32_bf16 v[32:35], a[224:227], v[136:139], v[32:35]\n" \
"s_mov_b32 m0, s19\n" \
"v_exp_f32 v146, v146\n" \
"v_mfma_f32_16x16x32_bf16 v[32:35], a[192:195], v[128:131], v[32:35]\n" \
"v_cmp_gt_u32 s[98:99], 32, v251\n" \
"v_cvt_pk_bf16_f32 v144, v144, v145\n" \
"v_mfma_f32_16x16x32_bf16 v[36:39], a[196:199], v[128:131], v[36:39]\n" \
"v_exp_f32 v147, v147\n" \
"v_mfma_f32_16x16x32_bf16 v[36:39], a[228:231], v[136:139], v[36:39]\n" \
"s_waitcnt lgkmcnt(9)\n" \
"buffer_load_dwordx4 v248, s[32:35], s88 offen offset:0 sc1 lds\n" \
"v_mfma_f32_16x16x32_bf16 v[40:43], a[232:235], v[136:139], v[40:43]\n" \
"v_exp_f32 v148, v148\n" \
"v_mfma_f32_16x16x32_bf16 v[40:43], a[200:203], v[128:131], v[40:43]\n" \
"v_cndmask_b32 v253, v253, 0, s[98:99]\n" \
"v_cvt_pk_bf16_f32 v145, v146, v147\n" \
"v_mfma_f32_16x16x32_bf16 v[44:47], a[204:207], v[128:131], v[44:47]\n" \
"v_exp_f32 v149, v149\n" \
"v_mfma_f32_16x16x32_bf16 v[44:47], a[236:239], v[136:139], v[44:47]\n" \
"s_waitcnt lgkmcnt(5)\n" \
"buffer_load_dwordx4 v248, s[32:35], s88 offen offset:1024 sc1 lds\n" \
"v_mfma_f32_16x16x32_bf16 v[48:51], a[240:243], v[136:139], v[48:51]\n" \
"v_exp_f32 v150, v150\n" \
"v_mfma_f32_16x16x32_bf16 v[48:51], a[208:211], v[128:131], v[48:51]\n" \
"v_max_u32_dpp v253, v253, v253 quad_perm:[1,0,3,2] row_mask:0xf bank_mask:0xf\n" \
"v_cvt_pk_bf16_f32 v146, v148, v149\n" \
"v_mfma_f32_16x16x32_bf16 v[52:55], a[212:215], v[128:131], v[52:55]\n" \
"v_exp_f32 v151, v151\n" \
"v_mfma_f32_16x16x32_bf16 v[52:55], a[244:247], v[136:139], v[52:55]\n" \
"s_waitcnt lgkmcnt(0)\n" \
"v_max_u32_dpp v253, v253, v253 quad_perm:[2,3,0,1] row_mask:0xf bank_mask:0xf\n" \
"v_cvt_pk_bf16_f32 v147, v150, v151\n" \
"v_mfma_f32_16x16x32_bf16 v[56:59], a[248:251], v[136:139], v[56:59]\n" \
"buffer_load_dwordx4 v248, s[32:35], s88 offen offset:2048 sc1 lds\n" \
"v_mfma_f32_16x16x32_bf16 v[56:59], a[216:219], v[128:131], v[56:59]\n" \
"v_exp_f32 v152, v152\n" \
"v_mfma_f32_16x16x32_bf16 v[60:63], a[220:223], v[128:131], v[60:63]\n" \
"v_max_u32_dpp v253, v253, v253 row_half_mirror row_mask:0xf bank_mask:0xf\n" \
"v_mfma_f32_16x16x32_bf16 v[60:63], a[252:255], v[136:139], v[60:63]\n" \
"v_exp_f32 v153, v153\n" \
"v_mfma_f32_16x16x32_bf16 v[128:131], a[128:131], v[176:179], 0\n" \
"s_barrier\n" \
"v_max_u32_dpp v253, v253, v253 row_mirror row_mask:0xf bank_mask:0xf\n" \
"v_mov_b32 v254, v253\n" \
"v_mfma_f32_16x16x32_bf16 v[128:131], a[132:135], v[180:183], v[128:131]\n" \
"v_exp_f32 v154, v154\n" \
"v_mfma_f32_16x16x32_bf16 v[128:131], a[136:139], v[184:187], v[128:131]\n" \
"buffer_load_dwordx4 v248, s[32:35], s88 offen offset:3072 sc1 lds\n" \
"v_mfma_f32_16x16x32_bf16 v[128:131], a[140:143], v[188:191], v[128:131]\n" \
"s_add_u32 s89, s88, 589824\n" \
"v_exp_f32 v155, v155\n" \
"v_mfma_f32_16x16x32_bf16 v[132:135], a[156:159], v[188:191], 0\n" \
"s_add_u32 s88, s88, s27\n" \
"v_permlane16_swap_b32 v253, v254\n" \
"v_mfma_f32_16x16x32_bf16 v[132:135], a[152:155], v[184:187], v[132:135]\n" \
"s_add_u32 s19, s19, 0x4000\n" \
"v_exp_f32 v156, v156\n" \
"v_mfma_f32_16x16x32_bf16 v[132:135], a[148:151], v[180:183], v[132:135]\n" \
"s_bitset0_b32 s19, 16\n" \
"v_max_u32 v253, v253, v254\n" \
"v_mov_b32 v254, v253\n" \
"v_mfma_f32_16x16x32_bf16 v[132:135], a[144:147], v[176:179], v[132:135]\n" \
"s_cmp_eq_u32 s88, s16\n" \
"v_exp_f32 v157, v157\n" \
"v_mfma_f32_16x16x32_bf16 v[136:139], a[160:163], v[176:179], 0\n" \
"s_cmov_b32 s88, s21\n" \
"v_permlane32_swap_b32 v253, v254\n" \
"v_mfma_f32_16x16x32_bf16 v[136:139], a[164:167], v[180:183], v[136:139]\n" \
"s_cmov_b32 s27, s91\n" \
"v_exp_f32 v158, v158\n" \
"v_mfma_f32_16x16x32_bf16 v[136:139], a[168:171], v[184:187], v[136:139]\n" \
"s_cmov_b32 s16, s77\n" \
"v_max_u32 v253, v253, v254\n" \
"v_max_f32 v254, 1.0, v253\n" \
"v_mfma_f32_16x16x32_bf16 v[136:139], a[172:175], v[188:191], v[136:139]\n" \
"s_cmov_b32 s21, s92\n" \
"v_exp_f32 v159, v159\n" \
"v_mfma_f32_16x16x32_bf16 v[140:143], a[188:191], v[188:191], 0\n" \
"s_cmovk_i32 s91, 0x4000\n" \
"v_min_f32 v253, 1.0, v253\n" \
"v_cvt_pk_bf16_f32 v152, v152, v153\n" \
"v_mfma_f32_16x16x32_bf16 v[140:143], a[184:187], v[184:187], v[140:143]\n" \
"s_cmp_eq_u32 s88, s17\n" \
"v_rcp_f32 v254, v254\n" \
"v_mfma_f32_16x16x32_bf16 v[140:143], a[180:183], v[180:183], v[140:143]\n" \
"s_cmov_b32 s88, s18\n" \
"v_exp_f32 v128, v128\n" \
"v_mfma_f32_16x16x32_bf16 v[140:143], a[176:179], v[176:179], v[140:143]\n" \
"s_mov_b32 m0, s20\n" \
"v_rcp_f32 v253, v253\n" \
"v_cvt_pk_bf16_f32 v153, v154, v155\n" \
"v_cvt_pk_bf16_f32 v154, v156, v157\n" \
"v_cvt_pk_bf16_f32 v155, v158, v159\n" \
"v_mfma_f32_16x16x32_bf16 a[40:43], a[48:51], v[144:147], a[40:43]\n" \
"v_exp_f32 v129, v129\n" \
"v_mfma_f32_16x16x32_bf16 a[40:43], a[48:51], v[152:155], a[40:43]\n" \
"buffer_load_dwordx4 v248, s[32:35], s89 offen offset:0 sc1 lds\n" \
"v_mfma_f32_16x16x32_bf16 v[64:67], a[224:227], v[152:155], v[64:67]\n" \
"v_exp_f32 v130, v130\n" \
"v_mfma_f32_16x16x32_bf16 v[64:67], a[192:195], v[144:147], v[64:67]\n" \
"buffer_load_dwordx4 v248, s[32:35], s89 offen offset:1024 sc1 lds\n" \
"v_mfma_f32_16x16x32_bf16 v[68:71], a[196:199], v[144:147], v[68:71]\n" \
"v_exp_f32 v131, v131\n" \
"v_mfma_f32_16x16x32_bf16 v[68:71], a[228:231], v[152:155], v[68:71]\n" \
"buffer_load_dwordx4 v248, s[32:35], s89 offen offset:2048 sc1 lds\n" \
"v_mfma_f32_16x16x32_bf16 v[72:75], a[232:235], v[152:155], v[72:75]\n" \
"v_exp_f32 v132, v132\n" \
"v_mfma_f32_16x16x32_bf16 v[72:75], a[200:203], v[144:147], v[72:75]\n" \
"s_lshl_b32 s85, s68, 3\n" \
"buffer_load_dwordx4 v248, s[32:35], s89 offen offset:3072 sc1 lds\n" \
"v_mfma_f32_16x16x32_bf16 v[76:79], a[204:207], v[144:147], v[76:79]\n" \
"s_add_u32 s20, s20, 0x4000\n" \
"v_exp_f32 v133, v133\n" \
"v_mfma_f32_16x16x32_bf16 v[76:79], a[236:239], v[152:155], v[76:79]\n" \
"s_and_b32 s20, s20, 0xffff\n" \
"v_mul_f32 v227, 0x7e000000, v254\n" \
"v_mul_f32 v226, 0x0d800000, v253\n" \
"v_mfma_f32_16x16x32_bf16 v[80:83], a[240:243], v[152:155], v[80:83]\n" \
"s_bitset1_b32 s20, 16\n" \
"v_exp_f32 v134, v134\n" \
"v_mfma_f32_16x16x32_bf16 v[80:83], a[208:211], v[144:147], v[80:83]\n" \
"s_mov_b32 m0, s19\n" \
"ds_read_b128 a[128:131], v248 offset:32768\n" \
"ds_read_b128 a[132:135], v248 offset:33792\n" \
"ds_read_b128 a[136:139], v248 offset:34816\n" \
"ds_read_b128 a[140:143], v248 offset:35840\n" \
"v_cvt_pk_bf16_f32 v128, v128, v129\n" \
"v_cvt_pk_bf16_f32 v129, v130, v131\n" \
"v_mfma_f32_16x16x32_bf16 v[84:87], a[212:215], v[144:147], v[84:87]\n" \
"s_add_u32 s85, s85, s67\n" \
"v_exp_f32 v135, v135\n" \
"v_mfma_f32_16x16x32_bf16 v[84:87], a[244:247], v[152:155], v[84:87]\n" \
"s_lshl2_add_u32 s85, s85, %[wid]\n" \
"buffer_load_dwordx4 v248, s[32:35], s88 offen offset:0 sc1 lds\n" \
"v_mfma_f32_16x16x32_bf16 v[88:91], a[248:251], v[152:155], v[88:91]\n" \
"s_lshl_b32 s85, s85, 2\n" \
"buffer_load_dwordx4 v248, s[32:35], s88 offen offset:1024 sc1 lds\n" \
"v_mfma_f32_16x16x32_bf16 v[88:91], a[216:219], v[144:147], v[88:91]\n" \
"s_add_u32 s85, s85, 655360\n" \
"ds_read_b128 a[156:159], v248 offset:39936\n" \
"ds_read_b128 a[152:155], v248 offset:38912\n" \
"ds_read_b128 a[148:151], v248 offset:37888\n" \
"ds_read_b128 a[144:147], v248 offset:36864\n" \
"v_exp_f32 v136, v136\n" \
"v_mfma_f32_16x16x32_bf16 v[92:95], a[220:223], v[144:147], v[92:95]\n" \
"s_waitcnt vmcnt(10)\n" \
"buffer_store_dword v239, v238, s[32:35], s85 offen sc1\n" \
"v_cvt_pk_bf16_f32 v130, v132, v133\n" \
"v_mfma_f32_16x16x32_bf16 v[92:95], a[252:255], v[152:155], v[92:95]\n" \
"s_waitcnt lgkmcnt(4)\n" \
"v_exp_f32 v137, v137\n" \
"v_mfma_f32_16x16x32_bf16 v[144:147], a[128:131], v[176:179], v[192:195]\n" \
"v_cvt_pk_bf16_f32 v131, v134, v135\n" \
"v_cvt_pk_bf16_f32 v136, v136, v137\n" \
"v_mfma_f32_16x16x32_bf16 v[144:147], a[132:135], v[180:183], v[144:147]\n" \
"ds_read_b128 a[160:163], v248 offset:40960\n" \
"ds_read_b128 a[164:167], v248 offset:41984\n" \
"ds_read_b128 a[168:171], v248 offset:43008\n" \
"ds_read_b128 a[172:175], v248 offset:44032\n" \
"v_exp_f32 v138, v138\n" \
"v_mfma_f32_16x16x32_bf16 v[144:147], a[136:139], v[184:187], v[144:147]\n" \
"buffer_load_dwordx4 v248, s[32:35], s88 offen offset:2048 sc1 lds\n" \
"v_mfma_f32_16x16x32_bf16 v[144:147], a[140:143], v[188:191], v[144:147]\n" \
"s_waitcnt lgkmcnt(4)\n" \
"v_exp_f32 v139, v139\n" \
"v_mfma_f32_16x16x32_bf16 v[148:151], a[156:159], v[188:191], v[196:199]\n" \
"ds_read_b128 a[188:191], v248 offset:48128\n" \
"v_cvt_pk_bf16_f32 v137, v138, v139\n" \
"v_mfma_f32_16x16x32_bf16 v[148:151], a[152:155], v[184:187], v[148:151]\n" \
"ds_read_b128 a[184:187], v248 offset:47104\n" \
"ds_read_b128 a[180:183], v248 offset:46080\n" \
"ds_read_b128 a[176:179], v248 offset:45056\n" \
"v_exp_f32 v140, v140\n" \
"v_mfma_f32_16x16x32_bf16 v[148:151], a[148:151], v[180:183], v[148:151]\n" \
"ds_read_b128 a[128:131], v248 offset:49152\n" \
"ds_read_b128 a[132:135], v248 offset:50176\n" \
"v_mfma_f32_16x16x32_bf16 v[148:151], a[144:147], v[176:179], v[148:151]\n" \
"s_waitcnt lgkmcnt(6)\n" \
"v_exp_f32 v141, v141\n" \
"v_mfma_f32_16x16x32_bf16 v[152:155], a[160:163], v[176:179], v[200:203]\n" \
"buffer_load_dwordx4 v248, s[32:35], s88 offen offset:3072 sc1 lds\n" \
"v_mfma_f32_16x16x32_bf16 v[152:155], a[164:167], v[180:183], v[152:155]\n" \
"s_add_u32 s89, s88, 589824\n" \
"v_exp_f32 v142, v142\n" \
"v_mfma_f32_16x16x32_bf16 v[152:155], a[168:171], v[184:187], v[152:155]\n" \
"s_add_u32 s88, s88, s27\n" \
"ds_read_b128 a[136:139], v248 offset:51200\n" \
"v_cvt_pk_bf16_f32 v138, v140, v141\n" \
"v_mfma_f32_16x16x32_bf16 v[152:155], a[172:175], v[188:191], v[152:155]\n" \
"s_waitcnt lgkmcnt(3)\n" \
"v_exp_f32 v143, v143\n" \
"v_mfma_f32_16x16x32_bf16 v[156:159], a[188:191], v[188:191], v[204:207]\n" \
"s_add_u32 s19, s19, 0x4000\n" \
"ds_read_b128 a[140:143], v248 offset:52224\n" \
"v_cvt_pk_bf16_f32 v139, v142, v143\n" \
"v_mfma_f32_16x16x32_bf16 v[156:159], a[184:187], v[184:187], v[156:159]\n" \
"s_bitset0_b32 s19, 16\n" \
"ds_read_b128 a[156:159], v248 offset:56320\n" \
"ds_read_b128 a[152:155], v248 offset:55296\n" \
"v_mfma_f32_16x16x32_bf16 v[156:159], a[180:183], v[180:183], v[156:159]\n" \
"s_cmp_eq_u32 s88, s16\n" \
"v_exp_f32 v144, v144\n" \
"v_mfma_f32_16x16x32_bf16 v[156:159], a[176:179], v[176:179], v[156:159]\n" \
"s_cmov_b32 s88, s21\n" \
"ds_read_b128 a[148:151], v248 offset:54272\n" \
"ds_read_b128 a[144:147], v248 offset:53248\n" \
"v_mfma_f32_16x16x32_bf16 a[44:47], a[48:51], v[128:131], a[44:47]\n" \
"s_barrier\n" \
"v_exp_f32 v145, v145\n" \
"v_mfma_f32_16x16x32_bf16 a[44:47], a[48:51], v[136:139], a[44:47]\n" \
"s_cmov_b32 s27, s91\n" \
"ds_read_b128 a[160:163], v248 offset:57344\n" \
"v_cvt_pk_bf16_f32 v144, v144, v145\n" \
"v_mfma_f32_16x16x32_bf16 v[96:99], a[224:227], v[136:139], v[96:99]\n" \
"s_cmov_b32 s16, s77\n" \
"v_exp_f32 v146, v146\n" \
"v_mfma_f32_16x16x32_bf16 v[96:99], a[192:195], v[128:131], v[96:99]\n" \
"s_cmov_b32 s21, s92\n" \
"ds_read_b128 a[164:167], v248 offset:58368\n" \
"ds_read_b128 a[168:171], v248 offset:59392\n" \
"v_mfma_f32_16x16x32_bf16 v[100:103], a[196:199], v[128:131], v[100:103]\n" \
"s_cmovk_i32 s91, 0x4000\n" \
"v_exp_f32 v147, v147\n" \
"v_mfma_f32_16x16x32_bf16 v[100:103], a[228:231], v[136:139], v[100:103]\n" \
"s_cmp_eq_u32 s88, s17\n" \
"ds_read_b128 a[172:175], v248 offset:60416\n" \
"v_cvt_pk_bf16_f32 v145, v146, v147\n" \
"v_mfma_f32_16x16x32_bf16 v[104:107], a[232:235], v[136:139], v[104:107]\n" \
"s_cmov_b32 s88, s18\n" \
"v_exp_f32 v148, v148\n" \
"v_mfma_f32_16x16x32_bf16 v[104:107], a[200:203], v[128:131], v[104:107]\n" \
"s_mov_b32 m0, s20\n" \
"ds_read_b128 a[188:191], v248 offset:64512\n" \
"ds_read_b128 a[184:187], v248 offset:63488\n" \
"v_mfma_f32_16x16x32_bf16 v[108:111], a[204:207], v[128:131], v[108:111]\n" \
"v_exp_f32 v149, v149\n" \
"v_mfma_f32_16x16x32_bf16 v[108:111], a[236:239], v[136:139], v[108:111]\n" \
"buffer_load_dwordx4 v248, s[32:35], s89 offen offset:0 sc1 lds\n" \
"v_mfma_f32_16x16x32_bf16 v[112:115], a[240:243], v[136:139], v[112:115]\n" \
"v_exp_f32 v150, v150\n" \
"v_mfma_f32_16x16x32_bf16 v[112:115], a[208:211], v[128:131], v[112:115]\n" \
"buffer_load_dwordx4 v248, s[32:35], s89 offen offset:1024 sc1 lds\n" \
"v_mfma_f32_16x16x32_bf16 v[116:119], a[212:215], v[128:131], v[116:119]\n" \
"v_exp_f32 v151, v151\n" \
"v_mfma_f32_16x16x32_bf16 v[116:119], a[244:247], v[136:139], v[116:119]\n" \
"v_cvt_pk_bf16_f32 v146, v148, v149\n" \
"v_cvt_pk_bf16_f32 v147, v150, v151\n" \
"v_mfma_f32_16x16x32_bf16 v[120:123], a[248:251], v[136:139], v[120:123]\n" \
"buffer_load_dwordx4 v248, s[32:35], s89 offen offset:2048 sc1 lds\n" \
"v_mfma_f32_16x16x32_bf16 v[120:123], a[216:219], v[128:131], v[120:123]\n" \
"v_exp_f32 v152, v152\n" \
"v_mfma_f32_16x16x32_bf16 v[124:127], a[220:223], v[128:131], v[124:127]\n" \
"ds_read_b128 a[180:183], v248 offset:62464\n" \
"ds_read_b128 a[224:227], v242 offset:40960\n" \
"v_mfma_f32_16x16x32_bf16 v[124:127], a[252:255], v[136:139], v[124:127]\n" \
"s_waitcnt lgkmcnt(12)\n" \
"v_exp_f32 v153, v153\n" \
"v_mfma_f32_16x16x32_bf16 v[128:131], a[128:131], v[160:163], v[208:211]\n" \
"ds_read_b128 a[176:179], v248 offset:61440\n" \
"v_cvt_pk_bf16_f32 v152, v152, v153\n" \
"v_mfma_f32_16x16x32_bf16 v[128:131], a[132:135], v[164:167], v[128:131]\n" \
"v_exp_f32 v154, v154\n" \
"v_mfma_f32_16x16x32_bf16 v[128:131], a[136:139], v[168:171], v[128:131]\n" \
"buffer_load_dwordx4 v248, s[32:35], s89 offen offset:3072 sc1 lds\n" \
"v_mfma_f32_16x16x32_bf16 v[128:131], a[140:143], v[172:175], v[128:131]\n" \
"s_waitcnt lgkmcnt(9)\n" \
"v_exp_f32 v155, v155\n" \
"v_mfma_f32_16x16x32_bf16 v[132:135], a[156:159], v[172:175], v[212:215]\n" \
"s_add_u32 s20, s20, 0x4000\n" \
"ds_read_b128 a[192:195], v242 offset:32768\n" \
"v_cvt_pk_bf16_f32 v153, v154, v155\n" \
"v_mfma_f32_16x16x32_bf16 v[132:135], a[152:155], v[168:171], v[132:135]\n" \
"s_and_b32 s20, s20, 0xffff\n" \
"v_exp_f32 v156, v156\n" \
"v_mfma_f32_16x16x32_bf16 v[132:135], a[148:151], v[164:167], v[132:135]\n" \
"s_bitset1_b32 s20, 16\n" \
"ds_read_b128 a[196:199], v242 offset:33792\n" \
"ds_read_b128 a[228:231], v242 offset:41984\n" \
"v_mfma_f32_16x16x32_bf16 v[132:135], a[144:147], v[160:163], v[132:135]\n" \
"s_waitcnt lgkmcnt(8)\n" \
"v_exp_f32 v157, v157\n" \
"v_mfma_f32_16x16x32_bf16 v[136:139], a[160:163], v[160:163], v[216:219]\n" \
"s_mov_b32 m0, s19\n" \
"ds_read_b128 a[232:235], v242 offset:43008\n" \
"v_cvt_pk_bf16_f32 v154, v156, v157\n" \
"v_mfma_f32_16x16x32_bf16 v[136:139], a[164:167], v[164:167], v[136:139]\n" \
"v_exp_f32 v158, v158\n" \
"v_mfma_f32_16x16x32_bf16 v[136:139], a[168:171], v[168:171], v[136:139]\n" \
"buffer_load_dwordx4 v248, s[32:35], s88 offen offset:0 sc1 lds\n" \
"v_mfma_f32_16x16x32_bf16 v[136:139], a[172:175], v[172:175], v[136:139]\n" \
"s_waitcnt lgkmcnt(4)\n" \
"v_exp_f32 v159, v159\n" \
"v_mfma_f32_16x16x32_bf16 v[140:143], a[188:191], v[172:175], v[220:223]\n" \
"ds_read_b128 a[200:203], v242 offset:34816\n" \
"v_cvt_pk_bf16_f32 v155, v158, v159\n" \
"v_mfma_f32_16x16x32_bf16 v[140:143], a[184:187], v[168:171], v[140:143]\n" \
"ds_read_b128 a[204:207], v242 offset:35840\n" \
"ds_read_b128 a[236:239], v242 offset:44032\n" \
"v_mfma_f32_16x16x32_bf16 v[140:143], a[180:183], v[164:167], v[140:143]\n" \
"v_exp_f32 v128, v128\n" \
"v_mfma_f32_16x16x32_bf16 v[140:143], a[176:179], v[160:163], v[140:143]\n" \
"buffer_load_dwordx4 v248, s[32:35], s88 offen offset:1024 sc1 lds\n" \
"v_mfma_f32_16x16x32_bf16 a[44:47], a[48:51], v[144:147], a[44:47]\n" \
"v_exp_f32 v129, v129\n" \
"v_mfma_f32_16x16x32_bf16 a[44:47], a[48:51], v[152:155], a[44:47]\n" \
"s_waitcnt lgkmcnt(4)\n" \
"ds_read_b128 a[240:243], v242 offset:45056\n" \
"v_cvt_pk_bf16_f32 v128, v128, v129\n" \
"v_mfma_f32_16x16x32_bf16 v[96:99], a[224:227], v[152:155], v[96:99]\n" \
"v_exp_f32 v130, v130\n" \
"v_mfma_f32_16x16x32_bf16 v[96:99], a[192:195], v[144:147], v[96:99]\n" \
"ds_read_b128 a[208:211], v242 offset:36864\n" \
"ds_read_b128 a[212:215], v242 offset:37888\n" \
"ds_read_b128 a[244:247], v242 offset:46080\n" \
"v_mfma_f32_16x16x32_bf16 v[100:103], a[196:199], v[144:147], v[100:103]\n" \
"v_exp_f32 v131, v131\n" \
"v_mfma_f32_16x16x32_bf16 v[100:103], a[228:231], v[152:155], v[100:103]\n" \
"s_waitcnt lgkmcnt(4)\n" \
"buffer_load_dwordx4 v248, s[32:35], s88 offen offset:2048 sc1 lds\n" \
"v_mfma_f32_16x16x32_bf16 v[104:107], a[232:235], v[152:155], v[104:107]\n" \
"v_exp_f32 v132, v132\n" \
"v_mfma_f32_16x16x32_bf16 v[104:107], a[200:203], v[144:147], v[104:107]\n" \
"ds_read_b128 a[248:251], v242 offset:47104\n" \
"ds_read_b128 a[216:219], v242 offset:38912\n" \
"ds_read_b128 a[220:223], v242 offset:39936\n" \
"ds_read_b128 a[252:255], v242 offset:48128\n" \
"v_cvt_pk_bf16_f32 v129, v130, v131\n" \
"v_mfma_f32_16x16x32_bf16 v[108:111], a[204:207], v[144:147], v[108:111]\n" \
"v_exp_f32 v133, v133\n" \
"v_mfma_f32_16x16x32_bf16 v[108:111], a[236:239], v[152:155], v[108:111]\n" \
"s_waitcnt lgkmcnt(4)\n" \
"ds_read_b128 a[224:227], v242 offset:57344\n" \
"v_cvt_pk_bf16_f32 v130, v132, v133\n" \
"v_mfma_f32_16x16x32_bf16 v[112:115], a[240:243], v[152:155], v[112:115]\n" \
"v_exp_f32 v134, v134\n" \
"v_mfma_f32_16x16x32_bf16 v[112:115], a[208:211], v[144:147], v[112:115]\n" \
"buffer_load_dwordx4 v248, s[32:35], s88 offen offset:3072 sc1 lds\n" \
"v_mfma_f32_16x16x32_bf16 v[116:119], a[212:215], v[144:147], v[116:119]\n" \
"s_add_u32 s89, s88, 589824\n" \
"v_exp_f32 v135, v135\n" \
"v_mfma_f32_16x16x32_bf16 v[116:119], a[244:247], v[152:155], v[116:119]\n" \
"s_waitcnt lgkmcnt(1)\n" \
"ds_read_b128 a[192:195], v242 offset:49152\n" \
"v_cvt_pk_bf16_f32 v131, v134, v135\n" \
"v_mfma_f32_16x16x32_bf16 v[120:123], a[248:251], v[152:155], v[120:123]\n" \
"s_add_u32 s88, s88, s27\n" \
"ds_read_b128 a[196:199], v242 offset:50176\n" \
"ds_read_b128 a[228:231], v242 offset:58368\n" \
"v_mfma_f32_16x16x32_bf16 v[120:123], a[216:219], v[144:147], v[120:123]\n" \
"s_add_u32 s19, s19, 0x4000\n" \
"v_exp_f32 v136, v136\n" \
"v_mfma_f32_16x16x32_bf16 v[124:127], a[220:223], v[144:147], v[124:127]\n" \
"s_bitset0_b32 s19, 16\n" \
"ds_read_b128 a[232:235], v242 offset:59392\n" \
"ds_read_b128 a[200:203], v242 offset:51200\n" \
"v_mfma_f32_16x16x32_bf16 v[124:127], a[252:255], v[152:155], v[124:127]\n" \
"s_cmp_eq_u32 s88, s16\n" \
"v_exp_f32 v137, v137\n" \
"v_mfma_f32_16x16x32_bf16 v[144:147], a[128:131], v[176:179], 0\n" \
"s_waitcnt vmcnt(13)\n" \
"s_barrier\n" \
"v_cvt_pk_bf16_f32 v136, v136, v137\n" \
"v_mfma_f32_16x16x32_bf16 v[144:147], a[132:135], v[180:183], v[144:147]\n" \
"s_cmov_b32 s88, s21\n" \
"v_exp_f32 v138, v138\n" \
"v_mfma_f32_16x16x32_bf16 v[144:147], a[136:139], v[184:187], v[144:147]\n" \
"s_cmov_b32 s27, s91\n" \
"ds_read_b128 a[204:207], v242 offset:52224\n" \
"ds_read_b128 a[236:239], v242 offset:60416\n" \
"v_mfma_f32_16x16x32_bf16 v[144:147], a[140:143], v[188:191], v[144:147]\n" \
"s_cmov_b32 s16, s77\n" \
"v_exp_f32 v139, v139\n" \
"v_mfma_f32_16x16x32_bf16 v[148:151], a[156:159], v[188:191], 0\n" \
"s_cmov_b32 s21, s92\n" \
"ds_read_b128 a[240:243], v242 offset:61440\n" \
"v_cvt_pk_bf16_f32 v137, v138, v139\n" \
"v_mfma_f32_16x16x32_bf16 v[148:151], a[152:155], v[184:187], v[148:151]\n" \
"s_cmovk_i32 s91, 0x4000\n" \
"v_exp_f32 v140, v140\n" \
"v_mfma_f32_16x16x32_bf16 v[148:151], a[148:151], v[180:183], v[148:151]\n" \
"s_cmp_eq_u32 s88, s17\n" \
"ds_read_b128 a[208:211], v242 offset:53248\n" \
"ds_read_b128 a[212:215], v242 offset:54272\n" \
"v_mfma_f32_16x16x32_bf16 v[148:151], a[144:147], v[176:179], v[148:151]\n" \
"s_cmov_b32 s88, s18\n" \
"v_exp_f32 v141, v141\n" \
"v_mfma_f32_16x16x32_bf16 v[152:155], a[160:163], v[176:179], 0\n" \
"s_mov_b32 m0, s20\n" \
"ds_read_b128 a[244:247], v242 offset:62464\n" \
"v_cvt_pk_bf16_f32 v138, v140, v141\n" \
"v_mfma_f32_16x16x32_bf16 v[152:155], a[164:167], v[180:183], v[152:155]\n" \
"v_exp_f32 v142, v142\n" \
"v_mfma_f32_16x16x32_bf16 v[152:155], a[168:171], v[184:187], v[152:155]\n" \
"buffer_load_dwordx4 v248, s[32:35], s89 offen offset:0 sc1 lds\n" \
"v_mfma_f32_16x16x32_bf16 v[152:155], a[172:175], v[188:191], v[152:155]\n" \
"v_exp_f32 v143, v143\n" \
"v_mfma_f32_16x16x32_bf16 v[156:159], a[188:191], v[188:191], 0\n" \
"buffer_load_dwordx4 v248, s[32:35], s89 offen offset:1024 sc1 lds\n" \
"v_mfma_f32_16x16x32_bf16 v[156:159], a[184:187], v[184:187], v[156:159]\n" \
"ds_read_b128 a[248:251], v242 offset:63488\n" \
"v_cvt_pk_bf16_f32 v139, v142, v143\n" \
"v_mfma_f32_16x16x32_bf16 v[156:159], a[180:183], v[180:183], v[156:159]\n" \
"v_exp_f32 v144, v144\n" \
"v_mfma_f32_16x16x32_bf16 v[156:159], a[176:179], v[176:179], v[156:159]\n" \
"buffer_load_dwordx4 v248, s[32:35], s89 offen offset:2048 sc1 lds\n" \
"v_mfma_f32_16x16x32_bf16 a[40:43], a[48:51], v[128:131], a[40:43]\n" \
"v_exp_f32 v145, v145\n" \
"v_mfma_f32_16x16x32_bf16 a[40:43], a[48:51], v[136:139], a[40:43]\n" \
"s_waitcnt lgkmcnt(9)\n" \
"ds_read_b128 a[216:219], v242 offset:55296\n" \
"v_cvt_pk_bf16_f32 v144, v144, v145\n" \
"v_mfma_f32_16x16x32_bf16 v[64:67], a[224:227], v[136:139], v[64:67]\n" \
"v_exp_f32 v146, v146\n" \
"v_mfma_f32_16x16x32_bf16 v[64:67], a[192:195], v[128:131], v[64:67]\n" \
"ds_read_b128 a[128:131], v248 offset:0\n" \
"ds_read_b128 a[132:135], v248 offset:1024\n" \
"v_mfma_f32_16x16x32_bf16 v[68:71], a[196:199], v[128:131], v[68:71]\n" \
"v_exp_f32 v147, v147\n" \
"v_mfma_f32_16x16x32_bf16 v[68:71], a[228:231], v[136:139], v[68:71]\n" \
"s_waitcnt lgkmcnt(8)\n" \
"buffer_load_dwordx4 v248, s[32:35], s89 offen offset:3072 sc1 lds\n" \
"v_mfma_f32_16x16x32_bf16 v[72:75], a[232:235], v[136:139], v[72:75]\n" \
"s_add_u32 s20, s20, 0x4000\n" \
"v_exp_f32 v148, v148\n" \
"v_mfma_f32_16x16x32_bf16 v[72:75], a[200:203], v[128:131], v[72:75]\n" \
"s_and_b32 s20, s20, 0xffff\n" \
"ds_read_b128 a[220:223], v242 offset:56320\n" \
"v_cvt_pk_bf16_f32 v145, v146, v147\n" \
"v_mfma_f32_16x16x32_bf16 v[76:79], a[204:207], v[128:131], v[76:79]\n" \
"s_bitset1_b32 s20, 16\n" \
"v_exp_f32 v149, v149\n" \
"v_mfma_f32_16x16x32_bf16 v[76:79], a[236:239], v[136:139], v[76:79]\n" \
"s_waitcnt lgkmcnt(5)\n" \
"ds_read_b128 a[136:139], v248 offset:2048\n" \
"ds_read_b128 a[252:255], v242 offset:64512\n" \
"v_cvt_pk_bf16_f32 v146, v148, v149\n" \
"v_mfma_f32_16x16x32_bf16 v[80:83], a[240:243], v[136:139], v[80:83]\n" \
"v_exp_f32 v150, v150\n" \
"v_mfma_f32_16x16x32_bf16 v[80:83], a[208:211], v[128:131], v[80:83]\n" \
"ds_read_b128 a[140:143], v248 offset:3072\n" \
"ds_read_b128 a[156:159], v248 offset:7168\n" \
"v_mfma_f32_16x16x32_bf16 v[84:87], a[212:215], v[128:131], v[84:87]\n" \
"v_exp_f32 v151, v151\n" \
"v_mfma_f32_16x16x32_bf16 v[84:87], a[244:247], v[136:139], v[84:87]\n" \
"s_waitcnt lgkmcnt(2)\n" \
"ds_read_b128 a[152:155], v248 offset:6144\n" \
"v_cvt_pk_bf16_f32 v147, v150, v151\n" \
"v_mfma_f32_16x16x32_bf16 v[88:91], a[248:251], v[136:139], v[88:91]\n" \
"ds_read_b128 a[148:151], v248 offset:5120\n" \
"ds_read_b128 a[144:147], v248 offset:4096\n" \
"v_mfma_f32_16x16x32_bf16 v[88:91], a[216:219], v[128:131], v[88:91]\n" \
"v_exp_f32 v152, v152\n" \
"v_mfma_f32_16x16x32_bf16 v[92:95], a[220:223], v[128:131], v[92:95]\n" \
"v_exp_f32 v153, v153\n" \
"v_mfma_f32_16x16x32_bf16 v[92:95], a[252:255], v[136:139], v[92:95]\n" \
"s_waitcnt lgkmcnt(4)\n" \
"ds_read_b128 a[160:163], v248 offset:8192\n" \
"ds_read_b128 a[164:167], v248 offset:9216\n" \
"ds_read_b128 a[168:171], v248 offset:10240\n" \
"ds_read_b128 a[172:175], v248 offset:11264\n" \
"s_mov_b32 s86, 0\n" \
"s_lshl_b32 s95, s67, 1\n" \
"s_cmp_lt_u32 s67, 4\n" \
"s_cbranch_scc1 .Lld_%=\n" \
"s_lshl_b32 s95, s84, 1\n" \
"s_sub_u32 s95, s95, 1\n" \
"s_cbranch_scc1 .Ll0_%=\n" \
".p2align 3\n" \
".Lloop0_%=:\n" \
  \
"v_mfma_f32_16x16x32_bf16 v[128:131], a[128:131], a[0:3], 0\n" \
".Lst0_9_%=:\n" \
"s_cbranch_execz .Lst0_13_%=\n" \
"s_add_u32 s86, s86, 0x4000\n" \
"ds_read_b128 a[188:191], v240 offset:15360\n" \
"v_cvt_pk_bf16_f32 v152, v152, v153\n" \
"v_mfma_f32_16x16x32_bf16 v[128:131], a[132:135], a[4:7], v[128:131]\n" \
"s_bitset0_b32 s86, 16\n" \
"v_exp_f32 v154, v154\n" \
"v_mfma_f32_16x16x32_bf16 v[128:131], a[136:139], a[8:11], v[128:131]\n" \
"ds_read_b128 a[184:187], v240 offset:14336\n" \
"v_add_u32 v242, 0x10000, v240\n" \
"v_mfma_f32_16x16x32_bf16 v[128:131], a[140:143], a[12:15], v[128:131]\n" \
"s_waitcnt lgkmcnt(6)\n" \
"v_exp_f32 v155, v155\n" \
"v_mfma_f32_16x16x32_bf16 v[132:135], a[156:159], a[12:15], 0\n" \
"s_mov_b32 m0, s19\n" \
"ds_read_b128 a[180:183], v240 offset:13312\n" \
"v_cvt_pk_bf16_f32 v153, v154, v155\n" \
"v_mfma_f32_16x16x32_bf16 v[132:135], a[152:155], a[8:11], v[132:135]\n" \
"ds_read_b128 a[176:179], v240 offset:12288\n" \
"v_exp_f32 v156, v156\n" \
"v_mfma_f32_16x16x32_bf16 v[132:135], a[148:151], a[4:7], v[132:135]\n" \
"buffer_load_dwordx4 v248, s[32:35], s88 offen offset:0 sc1 lds\n" \
"v_mfma_f32_16x16x32_bf16 v[132:135], a[144:147], a[0:3], v[132:135]\n" \
"s_waitcnt lgkmcnt(4)\n" \
"v_exp_f32 v157, v157\n" \
"v_mfma_f32_16x16x32_bf16 v[136:139], a[160:163], a[0:3], 0\n" \
"v_cvt_pk_bf16_f32 v154, v156, v157\n" \
"v_mfma_f32_16x16x32_bf16 v[136:139], a[164:167], a[4:7], v[136:139]\n" \
"v_exp_f32 v158, v158\n" \
"v_mfma_f32_16x16x32_bf16 v[136:139], a[168:171], a[8:11], v[136:139]\n" \
"buffer_load_dwordx4 v248, s[32:35], s88 offen offset:1024 sc1 lds\n" \
"v_mfma_f32_16x16x32_bf16 v[136:139], a[172:175], a[12:15], v[136:139]\n" \
"s_waitcnt lgkmcnt(0)\n" \
"v_exp_f32 v159, v159\n" \
"v_mfma_f32_16x16x32_bf16 v[140:143], a[188:191], a[12:15], 0\n" \
"v_add_u32 v240, s86, v248\n" \
"v_cvt_pk_bf16_f32 v155, v158, v159\n" \
"v_mfma_f32_16x16x32_bf16 v[140:143], a[184:187], a[8:11], v[140:143]\n" \
"buffer_load_dwordx4 v248, s[32:35], s88 offen offset:2048 sc1 lds\n" \
"v_mfma_f32_16x16x32_bf16 v[140:143], a[180:183], a[4:7], v[140:143]\n" \
"v_exp_f32 v128, v128\n" \
"v_mfma_f32_16x16x32_bf16 v[140:143], a[176:179], a[0:3], v[140:143]\n" \
"buffer_load_dwordx4 v248, s[32:35], s88 offen offset:3072 sc1 lds\n" \
"v_mfma_f32_16x16x32_bf16 a[44:47], a[48:51], v[144:147], a[44:47]\n" \
"s_add_u32 s89, s88, 589824\n" \
"v_exp_f32 v129, v129\n" \
"v_mfma_f32_16x16x32_bf16 a[44:47], a[48:51], v[152:155], a[44:47]\n" \
"s_add_u32 s88, s88, s27\n" \
"s_add_u32 s19, s19, 0x4000\n" \
"v_cvt_pk_bf16_f32 v128, v128, v129\n" \
"v_mfma_f32_16x16x32_bf16 v[96:99], a[224:227], v[152:155], v[96:99]\n" \
"s_bitset0_b32 s19, 16\n" \
"v_exp_f32 v130, v130\n" \
"v_mfma_f32_16x16x32_bf16 v[96:99], a[192:195], v[144:147], v[96:99]\n" \
"s_cmp_eq_u32 s88, s16\n" \
"s_cmov_b32 s88, s21\n" \
"ds_read_b128 a[224:227], v242 offset:8192\n" \
"v_mfma_f32_16x16x32_bf16 v[100:103], a[196:199], v[144:147], v[100:103]\n" \
"s_cmov_b32 s27, s91\n" \
"v_exp_f32 v131, v131\n" \
"v_mfma_f32_16x16x32_bf16 v[100:103], a[228:231], v[152:155], v[100:103]\n" \
"s_cmov_b32 s16, s77\n" \
"ds_read_b128 a[192:195], v242 offset:0\n" \
"v_cvt_pk_bf16_f32 v129, v130, v131\n" \
"v_mfma_f32_16x16x32_bf16 v[104:107], a[232:235], v[152:155], v[104:107]\n" \
"s_cmov_b32 s21, s92\n" \
"v_exp_f32 v132, v132\n" \
"v_mfma_f32_16x16x32_bf16 v[104:107], a[200:203], v[144:147], v[104:107]\n" \
"s_cmovk_i32 s91, 0x4000\n" \
"ds_read_b128 a[196:199], v242 offset:1024\n" \
"ds_read_b128 a[228:231], v242 offset:9216\n" \
"v_mfma_f32_16x16x32_bf16 v[108:111], a[204:207], v[144:147], v[108:111]\n" \
"s_cmp_eq_u32 s88, s17\n" \
"v_exp_f32 v133, v133\n" \
"v_mfma_f32_16x16x32_bf16 v[108:111], a[236:239], v[152:155], v[108:111]\n" \
"s_cmov_b32 s88, s18\n" \
"ds_read_b128 a[232:235], v242 offset:10240\n" \
"v_cvt_pk_bf16_f32 v130, v132, v133\n" \
"v_mfma_f32_16x16x32_bf16 v[112:115], a[240:243], v[152:155], v[112:115]\n" \
"v_exp_f32 v134, v134\n" \
"v_mfma_f32_16x16x32_bf16 v[112:115], a[208:211], v[144:147], v[112:115]\n" \
"ds_read_b128 a[200:203], v242 offset:2048\n" \
"v_mfma_f32_16x16x32_bf16 v[116:119], a[212:215], v[144:147], v[116:119]\n" \
"v_exp_f32 v135, v135\n" \
"v_mfma_f32_16x16x32_bf16 v[116:119], a[244:247], v[152:155], v[116:119]\n" \
"ds_read_b128 a[204:207], v242 offset:3072\n" \
"v_cvt_pk_bf16_f32 v131, v134, v135\n" \
"v_mfma_f32_16x16x32_bf16 v[120:123], a[248:251], v[152:155], v[120:123]\n" \
"ds_read_b128 a[236:239], v242 offset:11264\n" \
"v_mfma_f32_16x16x32_bf16 v[120:123], a[216:219], v[144:147], v[120:123]\n" \
"v_exp_f32 v136, v136\n" \
"v_mfma_f32_16x16x32_bf16 v[124:127], a[220:223], v[144:147], v[124:127]\n" \
"ds_read_b128 a[240:243], v242 offset:12288\n" \
"v_mfma_f32_16x16x32_bf16 v[124:127], a[252:255], v[152:155], v[124:127]\n" \
"s_waitcnt vmcnt(12)\n" \
"v_exp_f32 v137, v137\n" \
"v_mfma_f32_16x16x32_bf16 v[144:147], a[128:131], a[16:19], 0\n" \
"s_barrier\n" \
"ds_read_b128 a[208:211], v242 offset:4096\n" \
"v_cvt_pk_bf16_f32 v136, v136, v137\n" \
"v_mfma_f32_16x16x32_bf16 v[144:147], a[132:135], a[20:23], v[144:147]\n" \
"s_mov_b32 m0, s20\n" \
"v_exp_f32 v138, v138\n" \
"v_mfma_f32_16x16x32_bf16 v[144:147], a[136:139], a[24:27], v[144:147]\n" \
"buffer_load_dwordx4 v248, s[32:35], s89 offen offset:0 sc1 lds\n" \
"v_mfma_f32_16x16x32_bf16 v[144:147], a[140:143], a[28:31], v[144:147]\n" \
"v_exp_f32 v139, v139\n" \
"v_mfma_f32_16x16x32_bf16 v[148:151], a[156:159], a[28:31], 0\n" \
"ds_read_b128 a[212:215], v242 offset:5120\n" \
"v_cvt_pk_bf16_f32 v137, v138, v139\n" \
"v_mfma_f32_16x16x32_bf16 v[148:151], a[152:155], a[24:27], v[148:151]\n" \
"v_exp_f32 v140, v140\n" \
"v_mfma_f32_16x16x32_bf16 v[148:151], a[148:151], a[20:23], v[148:151]\n" \
"ds_read_b128 a[244:247], v242 offset:13312\n" \
"ds_read_b128 a[248:251], v242 offset:14336\n" \
"v_mfma_f32_16x16x32_bf16 v[148:151], a[144:147], a[16:19], v[148:151]\n" \
"v_exp_f32 v141, v141\n" \
"v_mfma_f32_16x16x32_bf16 v[152:155], a[160:163], a[16:19], 0\n" \
"buffer_load_dwordx4 v248, s[32:35], s89 offen offset:1024 sc1 lds\n" \
"v_mfma_f32_16x16x32_bf16 v[152:155], a[164:167], a[20:23], v[152:155]\n" \
"v_exp_f32 v142, v142\n" \
"v_mfma_f32_16x16x32_bf16 v[152:155], a[168:171], a[24:27], v[152:155]\n" \
"ds_read_b128 a[216:219], v242 offset:6144\n" \
"v_cvt_pk_bf16_f32 v138, v140, v141\n" \
"v_mfma_f32_16x16x32_bf16 v[152:155], a[172:175], a[28:31], v[152:155]\n" \
"v_exp_f32 v143, v143\n" \
"v_mfma_f32_16x16x32_bf16 v[156:159], a[188:191], a[28:31], 0\n" \
"ds_read_b128 a[128:131], v240 offset:0\n" \
"v_cvt_pk_bf16_f32 v139, v142, v143\n" \
"v_mfma_f32_16x16x32_bf16 v[156:159], a[184:187], a[24:27], v[156:159]\n" \
"ds_read_b128 a[132:135], v240 offset:1024\n" \
"ds_read_b128 a[220:223], v242 offset:7168\n" \
"v_mfma_f32_16x16x32_bf16 v[156:159], a[180:183], a[20:23], v[156:159]\n" \
"v_exp_f32 v144, v144\n" \
"v_mfma_f32_16x16x32_bf16 v[156:159], a[176:179], a[16:19], v[156:159]\n" \
"buffer_load_dwordx4 v248, s[32:35], s89 offen offset:2048 sc1 lds\n" \
"v_mfma_f32_16x16x32_bf16 a[32:35], a[48:51], v[128:131], a[32:35]\n" \
"v_exp_f32 v145, v145\n" \
"v_mfma_f32_16x16x32_bf16 a[32:35], a[48:51], v[136:139], a[32:35]\n" \
"s_waitcnt lgkmcnt(13)\n" \
"ds_read_b128 a[136:139], v240 offset:2048\n" \
"v_cvt_pk_bf16_f32 v144, v144, v145\n" \
"v_mfma_f32_16x16x32_bf16 v[0:3], a[224:227], v[136:139], v[0:3]\n" \
"v_exp_f32 v146, v146\n" \
"v_mfma_f32_16x16x32_bf16 v[0:3], a[192:195], v[128:131], v[0:3]\n" \
"ds_read_b128 a[252:255], v242 offset:15360\n" \
"ds_read_b128 a[140:143], v240 offset:3072\n" \
"v_mfma_f32_16x16x32_bf16 v[4:7], a[196:199], v[128:131], v[4:7]\n" \
"v_exp_f32 v147, v147\n" \
"v_mfma_f32_16x16x32_bf16 v[4:7], a[228:231], v[136:139], v[4:7]\n" \
"s_waitcnt lgkmcnt(12)\n" \
"buffer_load_dwordx4 v248, s[32:35], s89 offen offset:3072 sc1 lds\n" \
"v_mfma_f32_16x16x32_bf16 v[8:11], a[232:235], v[136:139], v[8:11]\n" \
"s_add_u32 s20, s20, 0x4000\n" \
"v_exp_f32 v148, v148\n" \
"v_mfma_f32_16x16x32_bf16 v[8:11], a[200:203], v[128:131], v[8:11]\n" \
"s_and_b32 s20, s20, 0xffff\n" \
"ds_read_b128 a[156:159], v240 offset:7168\n" \
"v_cvt_pk_bf16_f32 v145, v146, v147\n" \
"v_mfma_f32_16x16x32_bf16 v[12:15], a[204:207], v[128:131], v[12:15]\n" \
"s_bitset1_b32 s20, 16\n" \
"v_exp_f32 v149, v149\n" \
"v_mfma_f32_16x16x32_bf16 v[12:15], a[236:239], v[136:139], v[12:15]\n" \
"s_waitcnt lgkmcnt(9)\n" \
"ds_read_b128 a[152:155], v240 offset:6144\n" \
"v_cvt_pk_bf16_f32 v146, v148, v149\n" \
"v_mfma_f32_16x16x32_bf16 v[16:19], a[240:243], v[136:139], v[16:19]\n" \
"v_exp_f32 v150, v150\n" \
"v_mfma_f32_16x16x32_bf16 v[16:19], a[208:211], v[128:131], v[16:19]\n" \
"ds_read_b128 a[148:151], v240 offset:5120\n" \
"ds_read_b128 a[144:147], v240 offset:4096\n" \
"v_mfma_f32_16x16x32_bf16 v[20:23], a[212:215], v[128:131], v[20:23]\n" \
"v_exp_f32 v151, v151\n" \
"v_mfma_f32_16x16x32_bf16 v[20:23], a[244:247], v[136:139], v[20:23]\n" \
"s_waitcnt lgkmcnt(5)\n" \
"ds_read_b128 a[160:163], v240 offset:8192\n" \
"v_cvt_pk_bf16_f32 v147, v150, v151\n" \
"v_mfma_f32_16x16x32_bf16 v[24:27], a[248:251], v[136:139], v[24:27]\n" \
"ds_read_b128 a[164:167], v240 offset:9216\n" \
"ds_read_b128 a[168:171], v240 offset:10240\n" \
"v_mfma_f32_16x16x32_bf16 v[24:27], a[216:219], v[128:131], v[24:27]\n" \
"v_exp_f32 v152, v152\n" \
"v_mfma_f32_16x16x32_bf16 v[28:31], a[220:223], v[128:131], v[28:31]\n" \
"ds_read_b128 a[172:175], v240 offset:11264\n" \
"ds_read_b128 a[188:191], v240 offset:15360\n" \
"v_mfma_f32_16x16x32_bf16 v[28:31], a[252:255], v[136:139], v[28:31]\n" \
"s_waitcnt lgkmcnt(9)\n" \
"v_exp_f32 v153, v153\n" \
"v_mfma_f32_16x16x32_bf16 v[128:131], a[128:131], a[0:3], 0\n" \
"s_add_u32 s86, s86, 0x4000\n" \
"ds_read_b128 a[184:187], v240 offset:14336\n" \
"v_cvt_pk_bf16_f32 v152, v152, v153\n" \
"v_mfma_f32_16x16x32_bf16 v[128:131], a[132:135], a[4:7], v[128:131]\n" \
"s_bitset0_b32 s86, 16\n" \
"v_exp_f32 v154, v154\n" \
"v_mfma_f32_16x16x32_bf16 v[128:131], a[136:139], a[8:11], v[128:131]\n" \
"ds_read_b128 a[180:183], v240 offset:13312\n" \
"v_add_u32 v242, 0x10000, v240\n" \
"v_mfma_f32_16x16x32_bf16 v[128:131], a[140:143], a[12:15], v[128:131]\n" \
"s_waitcnt lgkmcnt(7)\n" \
"v_exp_f32 v155, v155\n" \
"v_mfma_f32_16x16x32_bf16 v[132:135], a[156:159], a[12:15], 0\n" \
"s_mov_b32 m0, s19\n" \
"ds_read_b128 a[176:179], v240 offset:12288\n" \
"v_cvt_pk_bf16_f32 v153, v154, v155\n" \
"v_mfma_f32_16x16x32_bf16 v[132:135], a[152:155], a[8:11], v[132:135]\n" \
"v_exp_f32 v156, v156\n" \
"v_mfma_f32_16x16x32_bf16 v[132:135], a[148:151], a[4:7], v[132:135]\n" \
"buffer_load_dwordx4 v248, s[32:35], s88 offen offset:0 sc1 lds\n" \
"v_mfma_f32_16x16x32_bf16 v[132:135], a[144:147], a[0:3], v[132:135]\n" \
"s_waitcnt lgkmcnt(4)\n" \
"v_exp_f32 v157, v157\n" \
"v_mfma_f32_16x16x32_bf16 v[136:139], a[160:163], a[0:3], 0\n" \
"v_cvt_pk_bf16_f32 v154, v156, v157\n" \
"v_mfma_f32_16x16x32_bf16 v[136:139], a[164:167], a[4:7], v[136:139]\n" \
"v_exp_f32 v158, v158\n" \
"v_mfma_f32_16x16x32_bf16 v[136:139], a[168:171], a[8:11], v[136:139]\n" \
"v_add_u32 v240, s86, v248\n" \
"v_mfma_f32_16x16x32_bf16 v[136:139], a[172:175], a[12:15], v[136:139]\n" \
"s_waitcnt lgkmcnt(0)\n" \
"v_exp_f32 v159, v159\n" \
"v_mfma_f32_16x16x32_bf16 v[140:143], a[188:191], a[12:15], 0\n" \
"v_cvt_pk_bf16_f32 v155, v158, v159\n" \
"v_mfma_f32_16x16x32_bf16 v[140:143], a[184:187], a[8:11], v[140:143]\n" \
"buffer_load_dwordx4 v248, s[32:35], s88 offen offset:1024 sc1 lds\n" \
"v_mfma_f32_16x16x32_bf16 v[140:143], a[180:183], a[4:7], v[140:143]\n" \
"v_exp_f32 v128, v128\n" \
"v_mfma_f32_16x16x32_bf16 v[140:143], a[176:179], a[0:3], v[140:143]\n" \
"v_mfma_f32_16x16x32_bf16 a[36:39], a[48:51], v[144:147], a[36:39]\n" \
"v_exp_f32 v129, v129\n" \
"v_mfma_f32_16x16x32_bf16 a[36:39], a[48:51], v[152:155], a[36:39]\n" \
"v_cvt_pk_bf16_f32 v128, v128, v129\n" \
"v_mfma_f32_16x16x32_bf16 v[32:35], a[224:227], v[152:155], v[32:35]\n" \
"v_exp_f32 v130, v130\n" \
"v_mfma_f32_16x16x32_bf16 v[32:35], a[192:195], v[144:147], v[32:35]\n" \
"ds_read_b128 a[224:227], v242 offset:8192\n" \
"v_mfma_f32_16x16x32_bf16 v[36:39], a[196:199], v[144:147], v[36:39]\n" \
"v_exp_f32 v131, v131\n" \
"v_mfma_f32_16x16x32_bf16 v[36:39], a[228:231], v[152:155], v[36:39]\n" \
"ds_read_b128 a[192:195], v242 offset:0\n" \
"v_cvt_pk_bf16_f32 v129, v130, v131\n" \
"v_mfma_f32_16x16x32_bf16 v[40:43], a[232:235], v[152:155], v[40:43]\n" \
"v_exp_f32 v132, v132\n" \
"v_mfma_f32_16x16x32_bf16 v[40:43], a[200:203], v[144:147], v[40:43]\n" \
"ds_read_b128 a[196:199], v242 offset:1024\n" \
"ds_read_b128 a[228:231], v242 offset:9216\n" \
"v_mfma_f32_16x16x32_bf16 v[44:47], a[204:207], v[144:147], v[44:47]\n" \
"v_exp_f32 v133, v133\n" \
"v_mfma_f32_16x16x32_bf16 v[44:47], a[236:239], v[152:155], v[44:47]\n" \
"ds_read_b128 a[232:235], v242 offset:10240\n" \
"v_cvt_pk_bf16_f32 v130, v132, v133\n" \
"v_mfma_f32_16x16x32_bf16 v[48:51], a[240:243], v[152:155], v[48:51]\n" \
"v_exp_f32 v134, v134\n" \
"v_mfma_f32_16x16x32_bf16 v[48:51], a[208:211], v[144:147], v[48:51]\n" \
"ds_read_b128 a[200:203], v242 offset:2048\n" \
"v_mfma_f32_16x16x32_bf16 v[52:55], a[212:215], v[144:147], v[52:55]\n" \
"v_exp_f32 v135, v135\n" \
"v_mfma_f32_16x16x32_bf16 v[52:55], a[244:247], v[152:155], v[52:55]\n" \
"ds_read_b128 a[204:207], v242 offset:3072\n" \
"v_cvt_pk_bf16_f32 v131, v134, v135\n" \
"v_mfma_f32_16x16x32_bf16 v[56:59], a[248:251], v[152:155], v[56:59]\n" \
"ds_read_b128 a[236:239], v242 offset:11264\n" \
"v_mfma_f32_16x16x32_bf16 v[56:59], a[216:219], v[144:147], v[56:59]\n" \
"v_exp_f32 v136, v136\n" \
"v_mfma_f32_16x16x32_bf16 v[60:63], a[220:223], v[144:147], v[60:63]\n" \
"ds_read_b128 a[240:243], v242 offset:12288\n" \
"v_mfma_f32_16x16x32_bf16 v[60:63], a[252:255], v[152:155], v[60:63]\n" \
"v_exp_f32 v137, v137\n" \
"v_mfma_f32_16x16x32_bf16 v[144:147], a[128:131], a[16:19], 0\n" \
"ds_read_b128 a[208:211], v242 offset:4096\n" \
"v_cvt_pk_bf16_f32 v136, v136, v137\n" \
"v_mfma_f32_16x16x32_bf16 v[144:147], a[132:135], a[20:23], v[144:147]\n" \
"v_exp_f32 v138, v138\n" \
"v_mfma_f32_16x16x32_bf16 v[144:147], a[136:139], a[24:27], v[144:147]\n" \
"ds_read_b128 a[212:215], v242 offset:5120\n" \
"ds_read_b128 a[244:247], v242 offset:13312\n" \
"v_mfma_f32_16x16x32_bf16 v[144:147], a[140:143], a[28:31], v[144:147]\n" \
"v_exp_f32 v139, v139\n" \
"v_mfma_f32_16x16x32_bf16 v[148:151], a[156:159], a[28:31], 0\n" \
"ds_read_b128 a[248:251], v242 offset:14336\n" \
"v_cvt_pk_bf16_f32 v137, v138, v139\n" \
"v_mfma_f32_16x16x32_bf16 v[148:151], a[152:155], a[24:27], v[148:151]\n" \
"v_exp_f32 v140, v140\n" \
"v_mfma_f32_16x16x32_bf16 v[148:151], a[148:151], a[20:23], v[148:151]\n" \
"ds_read_b128 a[216:219], v242 offset:6144\n" \
"v_mfma_f32_16x16x32_bf16 v[148:151], a[144:147], a[16:19], v[148:151]\n" \
"v_exp_f32 v141, v141\n" \
"v_mfma_f32_16x16x32_bf16 v[152:155], a[160:163], a[16:19], 0\n" \
"ds_read_b128 a[220:223], v242 offset:7168\n" \
"v_cvt_pk_bf16_f32 v138, v140, v141\n" \
"v_mfma_f32_16x16x32_bf16 v[152:155], a[164:167], a[20:23], v[152:155]\n" \
"v_exp_f32 v142, v142\n" \
"v_mfma_f32_16x16x32_bf16 v[152:155], a[168:171], a[24:27], v[152:155]\n" \
"ds_read_b128 a[252:255], v242 offset:15360\n" \
"v_mfma_f32_16x16x32_bf16 v[152:155], a[172:175], a[28:31], v[152:155]\n" \
"v_exp_f32 v143, v143\n" \
"v_mfma_f32_16x16x32_bf16 v[156:159], a[188:191], a[28:31], 0\n" \
"v_cvt_pk_bf16_f32 v139, v142, v143\n" \
"v_mfma_f32_16x16x32_bf16 v[156:159], a[184:187], a[24:27], v[156:159]\n" \
"buffer_load_dwordx4 v248, s[32:35], s88 offen offset:2048 sc1 lds\n" \
"v_mfma_f32_16x16x32_bf16 v[156:159], a[180:183], a[20:23], v[156:159]\n" \
"v_exp_f32 v144, v144\n" \
"v_mfma_f32_16x16x32_bf16 v[156:159], a[176:179], a[16:19], v[156:159]\n" \
"v_mfma_f32_16x16x32_bf16 a[32:35], a[48:51], v[128:131], a[32:35]\n" \
"v_exp_f32 v145, v145\n" \
"v_mfma_f32_16x16x32_bf16 a[32:35], a[48:51], v[136:139], a[32:35]\n" \
"s_waitcnt lgkmcnt(12)\n" \
"v_cvt_pk_bf16_f32 v144, v144, v145\n" \
"v_mfma_f32_16x16x32_bf16 v[0:3], a[224:227], v[136:139], v[0:3]\n" \
"v_exp_f32 v146, v146\n" \
"v_mfma_f32_16x16x32_bf16 v[0:3], a[192:195], v[128:131], v[0:3]\n" \
"v_mfma_f32_16x16x32_bf16 v[4:7], a[196:199], v[128:131], v[4:7]\n" \
"v_exp_f32 v147, v147\n" \
"v_mfma_f32_16x16x32_bf16 v[4:7], a[228:231], v[136:139], v[4:7]\n" \
"s_waitcnt lgkmcnt(8)\n" \
"v_cvt_pk_bf16_f32 v145, v146, v147\n" \
"v_mfma_f32_16x16x32_bf16 v[8:11], a[232:235], v[136:139], v[8:11]\n" \
"v_exp_f32 v148, v148\n" \
"v_mfma_f32_16x16x32_bf16 v[8:11], a[200:203], v[128:131], v[8:11]\n" \
"v_mfma_f32_16x16x32_bf16 v[12:15], a[204:207], v[128:131], v[12:15]\n" \
"v_exp_f32 v149, v149\n" \
"v_mfma_f32_16x16x32_bf16 v[12:15], a[236:239], v[136:139], v[12:15]\n" \
"s_waitcnt lgkmcnt(4)\n" \
"v_cvt_pk_bf16_f32 v146, v148, v149\n" \
"v_mfma_f32_16x16x32_bf16 v[16:19], a[240:243], v[136:139], v[16:19]\n" \
"v_exp_f32 v150, v150\n" \
"v_mfma_f32_16x16x32_bf16 v[16:19], a[208:211], v[128:131], v[16:19]\n" \
"v_mfma_f32_16x16x32_bf16 v[20:23], a[212:215], v[128:131], v[20:23]\n" \
"v_exp_f32 v151, v151\n" \
"v_mfma_f32_16x16x32_bf16 v[20:23], a[244:247], v[136:139], v[20:23]\n" \
"s_waitcnt lgkmcnt(0)\n" \
"v_cvt_pk_bf16_f32 v147, v150, v151\n" \
"v_mfma_f32_16x16x32_bf16 v[24:27], a[248:251], v[136:139], v[24:27]\n" \
"buffer_load_dwordx4 v248, s[32:35], s88 offen offset:3072 sc1 lds\n" \
"v_mfma_f32_16x16x32_bf16 v[24:27], a[216:219], v[128:131], v[24:27]\n" \
"s_add_u32 s89, s88, 589824\n" \
"v_exp_f32 v152, v152\n" \
"v_mfma_f32_16x16x32_bf16 v[28:31], a[220:223], v[128:131], v[28:31]\n" \
"s_add_u32 s88, s88, s27\n" \
"s_add_u32 s19, s19, 0x4000\n" \
"s_bitset0_b32 s19, 16\n" \
"v_mfma_f32_16x16x32_bf16 v[28:31], a[252:255], v[136:139], v[28:31]\n" \
"s_cmp_eq_u32 s88, s16\n" \
"v_exp_f32 v153, v153\n" \
"v_mfma_f32_16x16x32_bf16 v[128:131], a[128:131], v[160:163], 0\n" \
"s_cmov_b32 s88, s21\n" \
"s_cmov_b32 s27, s91\n" \
"v_cvt_pk_bf16_f32 v152, v152, v153\n" \
"v_mfma_f32_16x16x32_bf16 v[128:131], a[132:135], v[164:167], v[128:131]\n" \
"s_cmov_b32 s16, s77\n" \
"v_exp_f32 v154, v154\n" \
"v_mfma_f32_16x16x32_bf16 v[128:131], a[136:139], v[168:171], v[128:131]\n" \
"s_cmov_b32 s21, s92\n" \
"s_waitcnt vmcnt(12)\n" \
"s_cmovk_i32 s91, 0x4000\n" \
"v_mfma_f32_16x16x32_bf16 v[128:131], a[140:143], v[172:175], v[128:131]\n" \
"s_cmp_eq_u32 s88, s17\n" \
"v_exp_f32 v155, v155\n" \
"v_mfma_f32_16x16x32_bf16 v[132:135], a[156:159], v[172:175], 0\n" \
"s_cmov_b32 s88, s18\n" \
"v_cvt_pk_bf16_f32 v153, v154, v155\n" \
"v_mfma_f32_16x16x32_bf16 v[132:135], a[152:155], v[168:171], v[132:135]\n" \
"v_exp_f32 v156, v156\n" \
"v_mfma_f32_16x16x32_bf16 v[132:135], a[148:151], v[164:167], v[132:135]\n" \
"buffer_store_dwordx4 a[60:63], v232, s[32:35], s39 offen sc1\n" \
"v_mfma_f32_16x16x32_bf16 v[132:135], a[144:147], v[160:163], v[132:135]\n" \
"v_exp_f32 v157, v157\n" \
"v_mfma_f32_16x16x32_bf16 v[136:139], a[160:163], v[160:163], 0\n" \
"v_cvt_pk_bf16_f32 v154, v156, v157\n" \
"v_mfma_f32_16x16x32_bf16 v[136:139], a[164:167], v[164:167], v[136:139]\n" \
"v_exp_f32 v158, v158\n" \
"v_mfma_f32_16x16x32_bf16 v[136:139], a[168:171], v[168:171], v[136:139]\n" \
"v_mfma_f32_16x16x32_bf16 v[136:139], a[172:175], v[172:175], v[136:139]\n" \
"v_exp_f32 v159, v159\n" \
"v_mfma_f32_16x16x32_bf16 v[140:143], a[188:191], v[172:175], 0\n" \
"buffer_load_dwordx4 a[60:63], v230, s[40:43], s38 offen\n" \
"v_cvt_pk_bf16_f32 v155, v158, v159\n" \
"v_mfma_f32_16x16x32_bf16 v[140:143], a[184:187], v[168:171], v[140:143]\n" \
"v_mfma_f32_16x16x32_bf16 v[140:143], a[180:183], v[164:167], v[140:143]\n" \
"v_exp_f32 v128, v128\n" \
"v_mfma_f32_16x16x32_bf16 v[140:143], a[176:179], v[160:163], v[140:143]\n" \
"v_mfma_f32_16x16x32_bf16 a[36:39], a[48:51], v[144:147], a[36:39]\n" \
"s_mov_b32 m0, s87\n" \
"v_exp_f32 v129, v129\n" \
"v_mfma_f32_16x16x32_bf16 a[36:39], a[48:51], v[152:155], a[36:39]\n" \
"v_cvt_pk_bf16_f32 v128, v128, v129\n" \
"v_mfma_f32_16x16x32_bf16 v[32:35], a[224:227], v[152:155], v[32:35]\n" \
"buffer_load_dwordx4 v248, s[32:35], s93 offen sc1 lds\n" \
"v_mfma_f32_16x16x32_bf16 v[32:35], a[192:195], v[144:147], v[32:35]\n" \
"s_add_u32 s93, s93, 1024\n" \
"v_exp_f32 v130, v130\n" \
"v_mfma_f32_16x16x32_bf16 v[36:39], a[196:199], v[144:147], v[36:39]\n" \
"s_add_u32 s87, s87, 1024\n" \
"v_exp_f32 v131, v131\n" \
"v_mfma_f32_16x16x32_bf16 v[36:39], a[228:231], v[152:155], v[36:39]\n" \
"v_cvt_pk_bf16_f32 v129, v130, v131\n" \
"v_mfma_f32_16x16x32_bf16 v[40:43], a[232:235], v[152:155], v[40:43]\n" \
"v_exp_f32 v132, v132\n" \
"v_mfma_f32_16x16x32_bf16 v[40:43], a[200:203], v[144:147], v[40:43]\n" \
"v_mfma_f32_16x16x32_bf16 v[44:47], a[204:207], v[144:147], v[44:47]\n" \
"v_exp_f32 v133, v133\n" \
"v_mfma_f32_16x16x32_bf16 v[44:47], a[236:239], v[152:155], v[44:47]\n" \
"v_cvt_pk_bf16_f32 v130, v132, v133\n" \
"v_mfma_f32_16x16x32_bf16 v[48:51], a[240:243], v[152:155], v[48:51]\n" \
"v_exp_f32 v134, v134\n" \
"v_mfma_f32_16x16x32_bf16 v[48:51], a[208:211], v[144:147], v[48:51]\n" \
"buffer_load_dword v245, v231, s[60:63], s72 offen\n" \
"v_mfma_f32_16x16x32_bf16 v[52:55], a[212:215], v[144:147], v[52:55]\n" \
"s_add_u32 s38, s38, 512\n" \
"v_exp_f32 v135, v135\n" \
"v_mfma_f32_16x16x32_bf16 v[52:55], a[244:247], v[152:155], v[52:55]\n" \
"s_add_u32 s39, s39, 0x80000\n" \
"s_add_u32 s72, s72, 512\n" \
"v_cvt_pk_bf16_f32 v131, v134, v135\n" \
"v_mfma_f32_16x16x32_bf16 v[56:59], a[248:251], v[152:155], v[56:59]\n" \
"v_mfma_f32_16x16x32_bf16 v[56:59], a[216:219], v[144:147], v[56:59]\n" \
"v_exp_f32 v136, v136\n" \
"v_mfma_f32_16x16x32_bf16 v[60:63], a[220:223], v[144:147], v[60:63]\n" \
"v_mfma_f32_16x16x32_bf16 v[60:63], a[252:255], v[152:155], v[60:63]\n" \
"s_waitcnt vmcnt(16)\n" \
"v_exp_f32 v137, v137\n" \
"v_mfma_f32_16x16x32_bf16 v[144:147], a[128:131], v[176:179], 0\n" \
"s_barrier\n" \
"s_mov_b32 m0, s20\n" \
"v_cvt_pk_bf16_f32 v136, v136, v137\n" \
"v_mfma_f32_16x16x32_bf16 v[144:147], a[132:135], v[180:183], v[144:147]\n" \
"v_exp_f32 v138, v138\n" \
"v_mfma_f32_16x16x32_bf16 v[144:147], a[136:139], v[184:187], v[144:147]\n" \
"buffer_load_dwordx4 v248, s[32:35], s89 offen offset:0 sc1 lds\n" \
"v_mfma_f32_16x16x32_bf16 v[144:147], a[140:143], v[188:191], v[144:147]\n" \
"v_exp_f32 v139, v139\n" \
"v_mfma_f32_16x16x32_bf16 v[148:151], a[156:159], v[188:191], 0\n" \
"ds_read_b128 a[128:131], v240 offset:0\n" \
"v_cvt_pk_bf16_f32 v137, v138, v139\n" \
"v_mfma_f32_16x16x32_bf16 v[148:151], a[152:155], v[184:187], v[148:151]\n" \
"v_exp_f32 v140, v140\n" \
"v_mfma_f32_16x16x32_bf16 v[148:151], a[148:151], v[180:183], v[148:151]\n" \
"ds_read_b128 a[132:135], v240 offset:1024\n" \
"ds_read_b128 a[136:139], v240 offset:2048\n" \
"v_mfma_f32_16x16x32_bf16 v[148:151], a[144:147], v[176:179], v[148:151]\n" \
"v_exp_f32 v141, v141\n" \
"v_mfma_f32_16x16x32_bf16 v[152:155], a[160:163], v[176:179], 0\n" \
"buffer_load_dwordx4 v248, s[32:35], s89 offen offset:1024 sc1 lds\n" \
"v_mfma_f32_16x16x32_bf16 v[152:155], a[164:167], v[180:183], v[152:155]\n" \
"v_exp_f32 v142, v142\n" \
"v_mfma_f32_16x16x32_bf16 v[152:155], a[168:171], v[184:187], v[152:155]\n" \
"ds_read_b128 a[140:143], v240 offset:3072\n" \
"v_cvt_pk_bf16_f32 v138, v140, v141\n" \
"v_mfma_f32_16x16x32_bf16 v[152:155], a[172:175], v[188:191], v[152:155]\n" \
"v_exp_f32 v143, v143\n" \
"v_mfma_f32_16x16x32_bf16 v[156:159], a[188:191], v[188:191], 0\n" \
"ds_read_b128 a[156:159], v240 offset:7168\n" \
"v_cvt_pk_bf16_f32 v139, v142, v143\n" \
"v_mfma_f32_16x16x32_bf16 v[156:159], a[184:187], v[184:187], v[156:159]\n" \
"ds_read_b128 a[152:155], v240 offset:6144\n" \
"ds_read_b128 a[148:151], v240 offset:5120\n" \
"v_mfma_f32_16x16x32_bf16 v[156:159], a[180:183], v[180:183], v[156:159]\n" \
"v_exp_f32 v144, v144\n" \
"v_mfma_f32_16x16x32_bf16 v[156:159], a[176:179], v[176:179], v[156:159]\n" \
"buffer_load_dwordx4 v248, s[32:35], s89 offen offset:2048 sc1 lds\n" \
"v_mfma_f32_16x16x32_bf16 a[40:43], a[48:51], v[128:131], a[40:43]\n" \
"v_exp_f32 v145, v145\n" \
"v_mfma_f32_16x16x32_bf16 a[40:43], a[48:51], v[136:139], a[40:43]\n" \
"ds_read_b128 a[144:147], v240 offset:4096\n" \
"v_cvt_pk_bf16_f32 v144, v144, v145\n" \
"v_mfma_f32_16x16x32_bf16 v[64:67], a[224:227], v[136:139], v[64:67]\n" \
"v_exp_f32 v146, v146\n" \
"v_mfma_f32_16x16x32_bf16 v[64:67], a[192:195], v[128:131], v[64:67]\n" \
"ds_read_b128 a[160:163], v240 offset:8192\n" \
"ds_read_b128 a[164:167], v240 offset:9216\n" \
"v_mfma_f32_16x16x32_bf16 v[68:71], a[196:199], v[128:131], v[68:71]\n" \
"v_exp_f32 v147, v147\n" \
"v_mfma_f32_16x16x32_bf16 v[68:71], a[228:231], v[136:139], v[68:71]\n" \
"buffer_load_dwordx4 v248, s[32:35], s89 offen offset:3072 sc1 lds\n" \
"v_mfma_f32_16x16x32_bf16 v[72:75], a[232:235], v[136:139], v[72:75]\n" \
"s_add_u32 s20, s20, 0x4000\n" \
"v_exp_f32 v148, v148\n" \
"v_mfma_f32_16x16x32_bf16 v[72:75], a[200:203], v[128:131], v[72:75]\n" \
"s_and_b32 s20, s20, 0xffff\n" \
"ds_read_b128 a[168:171], v240 offset:10240\n" \
"v_cvt_pk_bf16_f32 v145, v146, v147\n" \
"v_mfma_f32_16x16x32_bf16 v[76:79], a[204:207], v[128:131], v[76:79]\n" \
"s_bitset1_b32 s20, 16\n" \
"v_exp_f32 v149, v149\n" \
"v_mfma_f32_16x16x32_bf16 v[76:79], a[236:239], v[136:139], v[76:79]\n" \
"ds_read_b128 a[172:175], v240 offset:11264\n" \
"v_cvt_pk_bf16_f32 v146, v148, v149\n" \
"v_mfma_f32_16x16x32_bf16 v[80:83], a[240:243], v[136:139], v[80:83]\n" \
"v_exp_f32 v150, v150\n" \
"v_mfma_f32_16x16x32_bf16 v[80:83], a[208:211], v[128:131], v[80:83]\n" \
"v_mfma_f32_16x16x32_bf16 v[84:87], a[212:215], v[128:131], v[84:87]\n" \
"s_sub_u32 s95, s95, 1\n" \
"v_exp_f32 v151, v151\n" \
"v_mfma_f32_16x16x32_bf16 v[84:87], a[244:247], v[136:139], v[84:87]\n" \
"v_cvt_pk_bf16_f32 v147, v150, v151\n" \
"v_mfma_f32_16x16x32_bf16 v[88:91], a[248:251], v[136:139], v[88:91]\n" \
"v_mfma_f32_16x16x32_bf16 v[88:91], a[216:219], v[128:131], v[88:91]\n" \
"v_exp_f32 v152, v152\n" \
"v_mfma_f32_16x16x32_bf16 v[92:95], a[220:223], v[128:131], v[92:95]\n" \
"v_exp_f32 v153, v153\n" \
"v_mfma_f32_16x16x32_bf16 v[92:95], a[252:255], v[136:139], v[92:95]\n" \
"s_waitcnt lgkmcnt(8)\n" \
"s_cbranch_scc0 .Lloop0_%=\n" \
".Ll0_%=:\n" \
".Lst0_10_%=:\n" \
"s_cbranch_execz .Lst0_14_%=\n" \
"s_lshl_b32 s95, s84, 1\n" \
"s_sub_u32 s95, 7, s95\n" \
".p2align 3\n" \
".Lloop_%=:\n" \
  \
"v_mfma_f32_16x16x32_bf16 v[128:131], a[128:131], a[0:3], 0\n" \
"s_add_u32 s86, s86, 0x4000\n" \
"ds_read_b128 a[188:191], v240 offset:15360\n" \
"v_cvt_pk_bf16_f32 v152, v152, v153\n" \
"v_mfma_f32_16x16x32_bf16 v[128:131], a[132:135], a[4:7], v[128:131]\n" \
"s_bitset0_b32 s86, 16\n" \
"v_exp_f32 v154, v154\n" \
"v_mfma_f32_16x16x32_bf16 v[128:131], a[136:139], a[8:11], v[128:131]\n" \
"ds_read_b128 a[184:187], v240 offset:14336\n" \
"v_add_u32 v242, 0x10000, v240\n" \
"v_mfma_f32_16x16x32_bf16 v[128:131], a[140:143], a[12:15], v[128:131]\n" \
"s_waitcnt lgkmcnt(6)\n" \
"v_exp_f32 v155, v155\n" \
"v_mfma_f32_16x16x32_bf16 v[132:135], a[156:159], a[12:15], 0\n" \
"s_mov_b32 m0, s19\n" \
"ds_read_b128 a[180:183], v240 offset:13312\n" \
"v_cvt_pk_bf16_f32 v153, v154, v155\n" \
"v_mfma_f32_16x16x32_bf16 v[132:135], a[152:155], a[8:11], v[132:135]\n" \
"ds_read_b128 a[176:179], v240 offset:12288\n" \
"v_exp_f32 v156, v156\n" \
"v_mfma_f32_16x16x32_bf16 v[132:135], a[148:151], a[4:7], v[132:135]\n" \
"buffer_load_dwordx4 v248, s[32:35], s88 offen offset:0 sc1 lds\n" \
"v_mfma_f32_16x16x32_bf16 v[132:135], a[144:147], a[0:3], v[132:135]\n" \
"s_waitcnt lgkmcnt(4)\n" \
"v_exp_f32 v157, v157\n" \
"v_mfma_f32_16x16x32_bf16 v[136:139], a[160:163], a[0:3], 0\n" \
"v_cvt_pk_bf16_f32 v154, v156, v157\n" \
"v_mfma_f32_16x16x32_bf16 v[136:139], a[164:167], a[4:7], v[136:139]\n" \
"v_exp_f32 v158, v158\n" \
"v_mfma_f32_16x16x32_bf16 v[136:139], a[168:171], a[8:11], v[136:139]\n" \
"v_add_u32 v240, s86, v248\n" \
"v_mfma_f32_16x16x32_bf16 v[136:139], a[172:175], a[12:15], v[136:139]\n" \
"s_waitcnt lgkmcnt(0)\n" \
"v_exp_f32 v159, v159\n" \
"v_mfma_f32_16x16x32_bf16 v[140:143], a[188:191], a[12:15], 0\n" \
"v_cvt_pk_bf16_f32 v155, v158, v159\n" \
"v_mfma_f32_16x16x32_bf16 v[140:143], a[184:187], a[8:11], v[140:143]\n" \
"buffer_load_dwordx4 v248, s[32:35], s88 offen offset:1024 sc1 lds\n" \
"v_mfma_f32_16x16x32_bf16 v[140:143], a[180:183], a[4:7], v[140:143]\n" \
"v_exp_f32 v128, v128\n" \
"v_mfma_f32_16x16x32_bf16 v[140:143], a[176:179], a[0:3], v[140:143]\n" \
"v_mfma_f32_16x16x32_bf16 a[44:47], a[48:51], v[144:147], a[44:47]\n" \
"v_exp_f32 v129, v129\n" \
"v_mfma_f32_16x16x32_bf16 a[44:47], a[48:51], v[152:155], a[44:47]\n" \
"v_cvt_pk_bf16_f32 v128, v128, v129\n" \
"v_mfma_f32_16x16x32_bf16 v[96:99], a[224:227], v[152:155], v[96:99]\n" \
"v_exp_f32 v130, v130\n" \
"v_mfma_f32_16x16x32_bf16 v[96:99], a[192:195], v[144:147], v[96:99]\n" \
"ds_read_b128 a[224:227], v242 offset:8192\n" \
"v_mfma_f32_16x16x32_bf16 v[100:103], a[196:199], v[144:147], v[100:103]\n" \
"v_exp_f32 v131, v131\n" \
"v_mfma_f32_16x16x32_bf16 v[100:103], a[228:231], v[152:155], v[100:103]\n" \
"ds_read_b128 a[192:195], v242 offset:0\n" \
"v_cvt_pk_bf16_f32 v129, v130, v131\n" \
"v_mfma_f32_16x16x32_bf16 v[104:107], a[232:235], v[152:155], v[104:107]\n" \
"v_exp_f32 v132, v132\n" \
"v_mfma_f32_16x16x32_bf16 v[104:107], a[200:203], v[144:147], v[104:107]\n" \
"ds_read_b128 a[196:199], v242 offset:1024\n" \
"ds_read_b128 a[228:231], v242 offset:9216\n" \
"v_mfma_f32_16x16x32_bf16 v[108:111], a[204:207], v[144:147], v[108:111]\n" \
"v_exp_f32 v133, v133\n" \
"v_mfma_f32_16x16x32_bf16 v[108:111], a[236:239], v[152:155], v[108:111]\n" \
"ds_read_b128 a[232:235], v242 offset:10240\n" \
"v_cvt_pk_bf16_f32 v130, v132, v133\n" \
"v_mfma_f32_16x16x32_bf16 v[112:115], a[240:243], v[152:155], v[112:115]\n" \
"v_exp_f32 v134, v134\n" \
"v_mfma_f32_16x16x32_bf16 v[112:115], a[208:211], v[144:147], v[112:115]\n" \
"ds_read_b128 a[200:203], v242 offset:2048\n" \
"v_mfma_f32_16x16x32_bf16 v[116:119], a[212:215], v[144:147], v[116:119]\n" \
"v_exp_f32 v135, v135\n" \
"v_mfma_f32_16x16x32_bf16 v[116:119], a[244:247], v[152:155], v[116:119]\n" \
"ds_read_b128 a[204:207], v242 offset:3072\n" \
"v_cvt_pk_bf16_f32 v131, v134, v135\n" \
"v_mfma_f32_16x16x32_bf16 v[120:123], a[248:251], v[152:155], v[120:123]\n" \
"ds_read_b128 a[236:239], v242 offset:11264\n" \
"v_mfma_f32_16x16x32_bf16 v[120:123], a[216:219], v[144:147], v[120:123]\n" \
"v_exp_f32 v136, v136\n" \
"v_mfma_f32_16x16x32_bf16 v[124:127], a[220:223], v[144:147], v[124:127]\n" \
"ds_read_b128 a[240:243], v242 offset:12288\n" \
"v_mfma_f32_16x16x32_bf16 v[124:127], a[252:255], v[152:155], v[124:127]\n" \
"v_exp_f32 v137, v137\n" \
"v_mfma_f32_16x16x32_bf16 v[144:147], a[128:131], a[16:19], 0\n" \
"ds_read_b128 a[208:211], v242 offset:4096\n" \
"v_cvt_pk_bf16_f32 v136, v136, v137\n" \
"v_mfma_f32_16x16x32_bf16 v[144:147], a[132:135], a[20:23], v[144:147]\n" \
"v_exp_f32 v138, v138\n" \
"v_mfma_f32_16x16x32_bf16 v[144:147], a[136:139], a[24:27], v[144:147]\n" \
"ds_read_b128 a[212:215], v242 offset:5120\n" \
"ds_read_b128 a[244:247], v242 offset:13312\n" \
"v_mfma_f32_16x16x32_bf16 v[144:147], a[140:143], a[28:31], v[144:147]\n" \
"v_exp_f32 v139, v139\n" \
"v_mfma_f32_16x16x32_bf16 v[148:151], a[156:159], a[28:31], 0\n" \
"ds_read_b128 a[248:251], v242 offset:14336\n" \
"v_cvt_pk_bf16_f32 v137, v138, v139\n" \
"v_mfma_f32_16x16x32_bf16 v[148:151], a[152:155], a[24:27], v[148:151]\n" \
".Lst0_11_%=:\n" \
"s_cbranch_execz .Lstb0_%=\n" \
"v_exp_f32 v140, v140\n" \
"v_mfma_f32_16x16x32_bf16 v[148:151], a[148:151], a[20:23], v[148:151]\n" \
"ds_read_b128 a[216:219], v242 offset:6144\n" \
"v_mfma_f32_16x16x32_bf16 v[148:151], a[144:147], a[16:19], v[148:151]\n" \
"v_exp_f32 v141, v141\n" \
"v_mfma_f32_16x16x32_bf16 v[152:155], a[160:163], a[16:19], 0\n" \
"ds_read_b128 a[220:223], v242 offset:7168\n" \
"v_cvt_pk_bf16_f32 v138, v140, v141\n" \
"v_mfma_f32_16x16x32_bf16 v[152:155], a[164:167], a[20:23], v[152:155]\n" \
"v_exp_f32 v142, v142\n" \
"v_mfma_f32_16x16x32_bf16 v[152:155], a[168:171], a[24:27], v[152:155]\n" \
"ds_read_b128 a[252:255], v242 offset:15360\n" \
"v_mfma_f32_16x16x32_bf16 v[152:155], a[172:175], a[28:31], v[152:155]\n" \
"v_exp_f32 v143, v143\n" \
"v_mfma_f32_16x16x32_bf16 v[156:159], a[188:191], a[28:31], 0\n" \
"v_cvt_pk_bf16_f32 v139, v142, v143\n" \
"v_mfma_f32_16x16x32_bf16 v[156:159], a[184:187], a[24:27], v[156:159]\n" \
"buffer_load_dwordx4 v248, s[32:35], s88 offen offset:2048 sc1 lds\n" \
"v_mfma_f32_16x16x32_bf16 v[156:159], a[180:183], a[20:23], v[156:159]\n" \
"v_exp_f32 v144, v144\n" \
"v_mfma_f32_16x16x32_bf16 v[156:159], a[176:179], a[16:19], v[156:159]\n" \
"v_mfma_f32_16x16x32_bf16 a[32:35], a[48:51], v[128:131], a[32:35]\n" \
"v_exp_f32 v145, v145\n" \
"v_mfma_f32_16x16x32_bf16 a[32:35], a[48:51], v[136:139], a[32:35]\n" \
"s_waitcnt lgkmcnt(12)\n" \
"v_cvt_pk_bf16_f32 v144, v144, v145\n" \
"v_mfma_f32_16x16x32_bf16 v[0:3], a[224:227], v[136:139], v[0:3]\n" \
"v_exp_f32 v146, v146\n" \
"v_mfma_f32_16x16x32_bf16 v[0:3], a[192:195], v[128:131], v[0:3]\n" \
"v_mfma_f32_16x16x32_bf16 v[4:7], a[196:199], v[128:131], v[4:7]\n" \
"v_exp_f32 v147, v147\n" \
"v_mfma_f32_16x16x32_bf16 v[4:7], a[228:231], v[136:139], v[4:7]\n" \
"s_waitcnt lgkmcnt(8)\n" \
"v_cvt_pk_bf16_f32 v145, v146, v147\n" \
"v_mfma_f32_16x16x32_bf16 v[8:11], a[232:235], v[136:139], v[8:11]\n" \
"v_exp_f32 v148, v148\n" \
"v_mfma_f32_16x16x32_bf16 v[8:11], a[200:203], v[128:131], v[8:11]\n" \
"v_mfma_f32_16x16x32_bf16 v[12:15], a[204:207], v[128:131], v[12:15]\n" \
"v_exp_f32 v149, v149\n" \
"v_mfma_f32_16x16x32_bf16 v[12:15], a[236:239], v[136:139], v[12:15]\n" \
"s_waitcnt lgkmcnt(4)\n" \
"v_cvt_pk_bf16_f32 v146, v148, v149\n" \
"v_mfma_f32_16x16x32_bf16 v[16:19], a[240:243], v[136:139], v[16:19]\n" \
"v_exp_f32 v150, v150\n" \
"v_mfma_f32_16x16x32_bf16 v[16:19], a[208:211], v[128:131], v[16:19]\n" \
"v_mfma_f32_16x16x32_bf16 v[20:23], a[212:215], v[128:131], v[20:23]\n" \
"v_exp_f32 v151, v151\n" \
"v_mfma_f32_16x16x32_bf16 v[20:23], a[244:247], v[136:139], v[20:23]\n" \
"s_waitcnt lgkmcnt(0)\n" \
"v_cvt_pk_bf16_f32 v147, v150, v151\n" \
"v_mfma_f32_16x16x32_bf16 v[24:27], a[248:251], v[136:139], v[24:27]\n" \
"buffer_load_dwordx4 v248, s[32:35], s88 offen offset:3072 sc1 lds\n" \
"v_mfma_f32_16x16x32_bf16 v[24:27], a[216:219], v[128:131], v[24:27]\n" \
"s_add_u32 s89, s88, 589824\n" \
"v_exp_f32 v152, v152\n" \
"v_mfma_f32_16x16x32_bf16 v[28:31], a[220:223], v[128:131], v[28:31]\n" \
"s_add_u32 s88, s88, s27\n" \
"s_add_u32 s19, s19, 0x4000\n" \
"s_bitset0_b32 s19, 16\n" \
"v_mfma_f32_16x16x32_bf16 v[28:31], a[252:255], v[136:139], v[28:31]\n" \
"s_cmp_eq_u32 s88, s16\n" \
"v_exp_f32 v153, v153\n" \
"v_mfma_f32_16x16x32_bf16 v[128:131], a[128:131], v[160:163], 0\n" \
"s_cmov_b32 s88, s21\n" \
"s_cmov_b32 s27, s91\n" \
"v_cvt_pk_bf16_f32 v152, v152, v153\n" \
"v_mfma_f32_16x16x32_bf16 v[128:131], a[132:135], v[164:167], v[128:131]\n" \
"s_cmov_b32 s16, s77\n" \
"v_exp_f32 v154, v154\n" \
"v_mfma_f32_16x16x32_bf16 v[128:131], a[136:139], v[168:171], v[128:131]\n" \
"s_cmov_b32 s21, s92\n" \
"s_waitcnt vmcnt(4)\n" \
"s_cmovk_i32 s91, 0x4000\n" \
"v_mfma_f32_16x16x32_bf16 v[128:131], a[140:143], v[172:175], v[128:131]\n" \
"s_cmp_eq_u32 s88, s17\n" \
"v_exp_f32 v155, v155\n" \
"v_mfma_f32_16x16x32_bf16 v[132:135], a[156:159], v[172:175], 0\n" \
"s_cmov_b32 s88, s18\n" \
"v_cvt_pk_bf16_f32 v153, v154, v155\n" \
"v_mfma_f32_16x16x32_bf16 v[132:135], a[152:155], v[168:171], v[132:135]\n" \
"v_exp_f32 v156, v156\n" \
"v_mfma_f32_16x16x32_bf16 v[132:135], a[148:151], v[164:167], v[132:135]\n" \
"buffer_store_dwordx4 a[60:63], v232, s[32:35], s39 offen sc1\n" \
"v_mfma_f32_16x16x32_bf16 v[132:135], a[144:147], v[160:163], v[132:135]\n" \
"v_exp_f32 v157, v157\n" \
"v_mfma_f32_16x16x32_bf16 v[136:139], a[160:163], v[160:163], 0\n" \
"v_cvt_pk_bf16_f32 v154, v156, v157\n" \
"v_mfma_f32_16x16x32_bf16 v[136:139], a[164:167], v[164:167], v[136:139]\n" \
"v_exp_f32 v158, v158\n" \
"v_mfma_f32_16x16x32_bf16 v[136:139], a[168:171], v[168:171], v[136:139]\n" \
"v_mfma_f32_16x16x32_bf16 v[136:139], a[172:175], v[172:175], v[136:139]\n" \
"v_exp_f32 v159, v159\n" \
"v_mfma_f32_16x16x32_bf16 v[140:143], a[188:191], v[172:175], 0\n" \
"buffer_load_dwordx4 a[60:63], v230, s[40:43], s38 offen\n" \
"v_cvt_pk_bf16_f32 v155, v158, v159\n" \
"v_mfma_f32_16x16x32_bf16 v[140:143], a[184:187], v[168:171], v[140:143]\n" \
"v_mfma_f32_16x16x32_bf16 v[140:143], a[180:183], v[164:167], v[140:143]\n" \
"v_exp_f32 v128, v128\n" \
"v_mfma_f32_16x16x32_bf16 v[140:143], a[176:179], v[160:163], v[140:143]\n" \
"v_mfma_f32_16x16x32_bf16 a[36:39], a[48:51], v[144:147], a[36:39]\n" \
"s_mov_b32 m0, s87\n" \
"v_exp_f32 v129, v129\n" \
"v_mfma_f32_16x16x32_bf16 a[36:39], a[48:51], v[152:155], a[36:39]\n" \
"v_cvt_pk_bf16_f32 v128, v128, v129\n" \
"v_mfma_f32_16x16x32_bf16 v[32:35], a[224:227], v[152:155], v[32:35]\n" \
"buffer_load_dwordx4 v248, s[32:35], s93 offen sc1 lds\n" \
"v_mfma_f32_16x16x32_bf16 v[32:35], a[192:195], v[144:147], v[32:35]\n" \
"s_add_u32 s93, s93, 1024\n" \
"v_exp_f32 v130, v130\n" \
"v_mfma_f32_16x16x32_bf16 v[36:39], a[196:199], v[144:147], v[36:39]\n" \
"s_add_u32 s87, s87, 1024\n" \
"v_exp_f32 v131, v131\n" \
"v_mfma_f32_16x16x32_bf16 v[36:39], a[228:231], v[152:155], v[36:39]\n" \
"v_cvt_pk_bf16_f32 v129, v130, v131\n" \
"v_mfma_f32_16x16x32_bf16 v[40:43], a[232:235], v[152:155], v[40:43]\n" \
"v_exp_f32 v132, v132\n" \
"v_mfma_f32_16x16x32_bf16 v[40:43], a[200:203], v[144:147], v[40:43]\n" \
"v_mfma_f32_16x16x32_bf16 v[44:47], a[204:207], v[144:147], v[44:47]\n" \
"v_exp_f32 v133, v133\n" \
"v_mfma_f32_16x16x32_bf16 v[44:47], a[236:239], v[152:155], v[44:47]\n" \
"v_cvt_pk_bf16_f32 v130, v132, v133\n" \
"v_mfma_f32_16x16x32_bf16 v[48:51], a[240:243], v[152:155], v[48:51]\n" \
"v_exp_f32 v134, v134\n" \
"v_mfma_f32_16x16x32_bf16 v[48:51], a[208:211], v[144:147], v[48:51]\n" \
"buffer_load_dword v245, v231, s[60:63], s72 offen\n" \
"v_mfma_f32_16x16x32_bf16 v[52:55], a[212:215], v[144:147], v[52:55]\n" \
"s_add_u32 s38, s38, 512\n" \
"v_exp_f32 v135, v135\n" \
"v_mfma_f32_16x16x32_bf16 v[52:55], a[244:247], v[152:155], v[52:55]\n" \
"s_add_u32 s39, s39, 0x80000\n" \
"s_add_u32 s72, s72, 512\n" \
"v_cvt_pk_bf16_f32 v131, v134, v135\n" \
"v_mfma_f32_16x16x32_bf16 v[56:59], a[248:251], v[152:155], v[56:59]\n" \
"v_mfma_f32_16x16x32_bf16 v[56:59], a[216:219], v[144:147], v[56:59]\n" \
"v_exp_f32 v136, v136\n" \
"v_mfma_f32_16x16x32_bf16 v[60:63], a[220:223], v[144:147], v[60:63]\n" \
"v_mfma_f32_16x16x32_bf16 v[60:63], a[252:255], v[152:155], v[60:63]\n" \
"s_waitcnt vmcnt(16)\n" \
"v_exp_f32 v137, v137\n" \
"v_mfma_f32_16x16x32_bf16 v[144:147], a[128:131], v[176:179], 0\n" \
"s_barrier\n" \
"s_mov_b32 m0, s20\n" \
"v_cvt_pk_bf16_f32 v136, v136, v137\n" \
"v_mfma_f32_16x16x32_bf16 v[144:147], a[132:135], v[180:183], v[144:147]\n" \
"v_exp_f32 v138, v138\n" \
"v_mfma_f32_16x16x32_bf16 v[144:147], a[136:139], v[184:187], v[144:147]\n" \
"buffer_load_dwordx4 v248, s[32:35], s89 offen offset:0 sc1 lds\n" \
"v_mfma_f32_16x16x32_bf16 v[144:147], a[140:143], v[188:191], v[144:147]\n" \
"v_exp_f32 v139, v139\n" \
"v_mfma_f32_16x16x32_bf16 v[148:151], a[156:159], v[188:191], 0\n" \
"ds_read_b128 a[128:131], v240 offset:0\n" \
"v_cvt_pk_bf16_f32 v137, v138, v139\n" \
"v_mfma_f32_16x16x32_bf16 v[148:151], a[152:155], v[184:187], v[148:151]\n" \
"v_exp_f32 v140, v140\n" \
"v_mfma_f32_16x16x32_bf16 v[148:151], a[148:151], v[180:183], v[148:151]\n" \
"ds_read_b128 a[132:135], v240 offset:1024\n" \
"ds_read_b128 a[136:139], v240 offset:2048\n" \
"v_mfma_f32_16x16x32_bf16 v[148:151], a[144:147], v[176:179], v[148:151]\n" \
"v_exp_f32 v141, v141\n" \
"v_mfma_f32_16x16x32_bf16 v[152:155], a[160:163], v[176:179], 0\n" \
"buffer_load_dwordx4 v248, s[32:35], s89 offen offset:1024 sc1 lds\n" \
"v_mfma_f32_16x16x32_bf16 v[152:155], a[164:167], v[180:183], v[152:155]\n" \
"v_exp_f32 v142, v142\n" \
"v_mfma_f32_16x16x32_bf16 v[152:155], a[168:171], v[184:187], v[152:155]\n" \
"ds_read_b128 a[140:143], v240 offset:3072\n" \
"v_cvt_pk_bf16_f32 v138, v140, v141\n" \
"v_mfma_f32_16x16x32_bf16 v[152:155], a[172:175], v[188:191], v[152:155]\n" \
"v_exp_f32 v143, v143\n" \
"v_mfma_f32_16x16x32_bf16 v[156:159], a[188:191], v[188:191], 0\n" \
"ds_read_b128 a[156:159], v240 offset:7168\n" \
"v_cvt_pk_bf16_f32 v139, v142, v143\n" \
"v_mfma_f32_16x16x32_bf16 v[156:159], a[184:187], v[184:187], v[156:159]\n" \
"ds_read_b128 a[152:155], v240 offset:6144\n" \
"ds_read_b128 a[148:151], v240 offset:5120\n" \
"v_mfma_f32_16x16x32_bf16 v[156:159], a[180:183], v[180:183], v[156:159]\n" \
"v_exp_f32 v144, v144\n" \
"v_mfma_f32_16x16x32_bf16 v[156:159], a[176:179], v[176:179], v[156:159]\n" \
"buffer_load_dwordx4 v248, s[32:35], s89 offen offset:2048 sc1 lds\n" \
"v_mfma_f32_16x16x32_bf16 a[40:43], a[48:51], v[128:131], a[40:43]\n" \
"v_exp_f32 v145, v145\n" \
"v_mfma_f32_16x16x32_bf16 a[40:43], a[48:51], v[136:139], a[40:43]\n" \
"ds_read_b128 a[144:147], v240 offset:4096\n" \
"v_cvt_pk_bf16_f32 v144, v144, v145\n" \
"v_mfma_f32_16x16x32_bf16 v[64:67], a[224:227], v[136:139], v[64:67]\n" \
"v_exp_f32 v146, v146\n" \
"v_mfma_f32_16x16x32_bf16 v[64:67], a[192:195], v[128:131], v[64:67]\n" \
"ds_read_b128 a[160:163], v240 offset:8192\n" \
"ds_read_b128 a[164:167], v240 offset:9216\n" \
"v_mfma_f32_16x16x32_bf16 v[68:71], a[196:199], v[128:131], v[68:71]\n" \
"v_exp_f32 v147, v147\n" \
"v_mfma_f32_16x16x32_bf16 v[68:71], a[228:231], v[136:139], v[68:71]\n" \
"buffer_load_dwordx4 v248, s[32:35], s89 offen offset:3072 sc1 lds\n" \
"v_mfma_f32_16x16x32_bf16 v[72:75], a[232:235], v[136:139], v[72:75]\n" \
"s_add_u32 s20, s20, 0x4000\n" \
"v_exp_f32 v148, v148\n" \
"v_mfma_f32_16x16x32_bf16 v[72:75], a[200:203], v[128:131], v[72:75]\n" \
"s_and_b32 s20, s20, 0xffff\n" \
"ds_read_b128 a[168:171], v240 offset:10240\n" \
"v_cvt_pk_bf16_f32 v145, v146, v147\n" \
"v_mfma_f32_16x16x32_bf16 v[76:79], a[204:207], v[128:131], v[76:79]\n" \
"s_bitset1_b32 s20, 16\n" \
"v_exp_f32 v149, v149\n" \
"v_mfma_f32_16x16x32_bf16 v[76:79], a[236:239], v[136:139], v[76:79]\n" \
"ds_read_b128 a[172:175], v240 offset:11264\n" \
"v_cvt_pk_bf16_f32 v146, v148, v149\n" \
"v_mfma_f32_16x16x32_bf16 v[80:83], a[240:243], v[136:139], v[80:83]\n" \
"v_exp_f32 v150, v150\n" \
"v_mfma_f32_16x16x32_bf16 v[80:83], a[208:211], v[128:131], v[80:83]\n" \
"v_mfma_f32_16x16x32_bf16 v[84:87], a[212:215], v[128:131], v[84:87]\n" \
"s_sub_u32 s95, s95, 1\n" \
"v_exp_f32 v151, v151\n" \
"v_mfma_f32_16x16x32_bf16 v[84:87], a[244:247], v[136:139], v[84:87]\n" \
"v_cvt_pk_bf16_f32 v147, v150, v151\n" \
"v_mfma_f32_16x16x32_bf16 v[88:91], a[248:251], v[136:139], v[88:91]\n" \
"v_mfma_f32_16x16x32_bf16 v[88:91], a[216:219], v[128:131], v[88:91]\n" \
"v_exp_f32 v152, v152\n" \
"v_mfma_f32_16x16x32_bf16 v[92:95], a[220:223], v[128:131], v[92:95]\n" \
"v_exp_f32 v153, v153\n" \
"v_mfma_f32_16x16x32_bf16 v[92:95], a[252:255], v[136:139], v[92:95]\n" \
"s_waitcnt lgkmcnt(8)\n" \
"s_cbranch_scc0 .Lloop_%=\n" \
"s_sub_u32 s95, s67, s84\n" \
"s_lshl_b32 s95, s95, 1\n" \
"s_sub_u32 s95, s95, 8\n" \
"s_mov_b32 s76, 1\n" \
".Lld_%=:\n" \
".Lst0_12_%=:\n" \
"s_cbranch_execz .Lstb0_%=\n" \
"s_sub_u32 s95, s95, 1\n" \
"s_cbranch_scc1 .Lhook_%=\n" \
".p2align 3\n" \
".Lloop1_%=:\n" \
  \
"v_mfma_f32_16x16x32_bf16 v[128:131], a[128:131], a[0:3], 0\n" \
"s_add_u32 s86, s86, 0x4000\n" \
"ds_read_b128 a[188:191], v240 offset:15360\n" \
"v_cvt_pk_bf16_f32 v152, v152, v153\n" \
"v_mfma_f32_16x16x32_bf16 v[128:131], a[132:135], a[4:7], v[128:131]\n" \
"s_bitset0_b32 s86, 16\n" \
"v_exp_f32 v154, v154\n" \
"v_mfma_f32_16x16x32_bf16 v[128:131], a[136:139], a[8:11], v[128:131]\n" \
"s_cmp_eq_u32 s88, s24\n" \
"s_cbranch_scc1 .Lchd_%=\n" \
".Lsbhd_%=:\n" \
"v_mfma_f32_16x16x32_bf16 v[128:131], a[140:143], a[12:15], v[128:131]\n" \
"s_waitcnt lgkmcnt(5)\n" \
"v_exp_f32 v155, v155\n" \
"v_mfma_f32_16x16x32_bf16 v[132:135], a[156:159], a[12:15], 0\n" \
"s_mov_b32 m0, s19\n" \
"v_add_u32 v242, 0x10000, v240\n" \
"v_cvt_pk_bf16_f32 v153, v154, v155\n" \
"v_mfma_f32_16x16x32_bf16 v[132:135], a[152:155], a[8:11], v[132:135]\n" \
"ds_read_b128 a[184:187], v240 offset:14336\n" \
"ds_read_b128 a[180:183], v240 offset:13312\n" \
"ds_read_b128 a[176:179], v240 offset:12288\n" \
"v_exp_f32 v156, v156\n" \
"v_mfma_f32_16x16x32_bf16 v[132:135], a[148:151], a[4:7], v[132:135]\n" \
"buffer_load_dwordx4 v248, s[32:35], s88 offen offset:0 sc1 lds\n" \
"v_mfma_f32_16x16x32_bf16 v[132:135], a[144:147], a[0:3], v[132:135]\n" \
"s_waitcnt lgkmcnt(4)\n" \
"v_exp_f32 v157, v157\n" \
"v_mfma_f32_16x16x32_bf16 v[136:139], a[160:163], a[0:3], 0\n" \
"v_cvt_pk_bf16_f32 v154, v156, v157\n" \
"v_mfma_f32_16x16x32_bf16 v[136:139], a[164:167], a[4:7], v[136:139]\n" \
"v_exp_f32 v158, v158\n" \
"v_mfma_f32_16x16x32_bf16 v[136:139], a[168:171], a[8:11], v[136:139]\n" \
"v_add_u32 v240, s86, v248\n" \
"v_mfma_f32_16x16x32_bf16 v[136:139], a[172:175], a[12:15], v[136:139]\n" \
"s_waitcnt lgkmcnt(0)\n" \
"v_exp_f32 v159, v159\n" \
"v_mfma_f32_16x16x32_bf16 v[140:143], a[188:191], a[12:15], 0\n" \
"v_cvt_pk_bf16_f32 v155, v158, v159\n" \
"v_mfma_f32_16x16x32_bf16 v[140:143], a[184:187], a[8:11], v[140:143]\n" \
"buffer_load_dwordx4 v248, s[32:35], s88 offen offset:1024 sc1 lds\n" \
"v_mfma_f32_16x16x32_bf16 v[140:143], a[180:183], a[4:7], v[140:143]\n" \
"v_exp_f32 v128, v128\n" \
"v_mfma_f32_16x16x32_bf16 v[140:143], a[176:179], a[0:3], v[140:143]\n" \
"v_mfma_f32_16x16x32_bf16 a[44:47], a[48:51], v[144:147], a[44:47]\n" \
"v_exp_f32 v129, v129\n" \
"v_mfma_f32_16x16x32_bf16 a[44:47], a[48:51], v[152:155], a[44:47]\n" \
"v_cvt_pk_bf16_f32 v128, v128, v129\n" \
"v_mfma_f32_16x16x32_bf16 v[96:99], a[224:227], v[152:155], v[96:99]\n" \
"v_exp_f32 v130, v130\n" \
"v_mfma_f32_16x16x32_bf16 v[96:99], a[192:195], v[144:147], v[96:99]\n" \
"ds_read_b128 a[224:227], v242 offset:8192\n" \
"v_mfma_f32_16x16x32_bf16 v[100:103], a[196:199], v[144:147], v[100:103]\n" \
"v_exp_f32 v131, v131\n" \
"v_mfma_f32_16x16x32_bf16 v[100:103], a[228:231], v[152:155], v[100:103]\n" \
"ds_read_b128 a[192:195], v242 offset:0\n" \
"v_cvt_pk_bf16_f32 v129, v130, v131\n" \
"v_mfma_f32_16x16x32_bf16 v[104:107], a[232:235], v[152:155], v[104:107]\n" \
"v_exp_f32 v132, v132\n" \
"v_mfma_f32_16x16x32_bf16 v[104:107], a[200:203], v[144:147], v[104:107]\n" \
"ds_read_b128 a[196:199], v242 offset:1024\n" \
"ds_read_b128 a[228:231], v242 offset:9216\n" \
"v_mfma_f32_16x16x32_bf16 v[108:111], a[204:207], v[144:147], v[108:111]\n" \
"v_exp_f32 v133, v133\n" \
"v_mfma_f32_16x16x32_bf16 v[108:111], a[236:239], v[152:155], v[108:111]\n" \
"ds_read_b128 a[232:235], v242 offset:10240\n" \
"v_cvt_pk_bf16_f32 v130, v132, v133\n" \
"v_mfma_f32_16x16x32_bf16 v[112:115], a[240:243], v[152:155], v[112:115]\n" \
"v_exp_f32 v134, v134\n" \
"v_mfma_f32_16x16x32_bf16 v[112:115], a[208:211], v[144:147], v[112:115]\n" \
"ds_read_b128 a[200:203], v242 offset:2048\n" \
"v_mfma_f32_16x16x32_bf16 v[116:119], a[212:215], v[144:147], v[116:119]\n" \
"v_exp_f32 v135, v135\n" \
"v_mfma_f32_16x16x32_bf16 v[116:119], a[244:247], v[152:155], v[116:119]\n" \
"ds_read_b128 a[204:207], v242 offset:3072\n" \
"v_cvt_pk_bf16_f32 v131, v134, v135\n" \
"v_mfma_f32_16x16x32_bf16 v[120:123], a[248:251], v[152:155], v[120:123]\n" \
"ds_read_b128 a[236:239], v242 offset:11264\n" \
"v_mfma_f32_16x16x32_bf16 v[120:123], a[216:219], v[144:147], v[120:123]\n" \
"v_exp_f32 v136, v136\n" \
"v_mfma_f32_16x16x32_bf16 v[124:127], a[220:223], v[144:147], v[124:127]\n" \
"ds_read_b128 a[240:243], v242 offset:12288\n" \
"v_mfma_f32_16x16x32_bf16 v[124:127], a[252:255], v[152:155], v[124:127]\n" \
"v_exp_f32 v137, v137\n" \
"v_mfma_f32_16x16x32_bf16 v[144:147], a[128:131], a[16:19], 0\n" \
"ds_read_b128 a[208:211], v242 offset:4096\n" \
"v_cvt_pk_bf16_f32 v136, v136, v137\n" \
"v_mfma_f32_16x16x32_bf16 v[144:147], a[132:135], a[20:23], v[144:147]\n" \
"v_exp_f32 v138, v138\n" \
"v_mfma_f32_16x16x32_bf16 v[144:147], a[136:139], a[24:27], v[144:147]\n" \
"ds_read_b128 a[212:215], v242 offset:5120\n" \
"ds_read_b128 a[244:247], v242 offset:13312\n" \
"v_mfma_f32_16x16x32_bf16 v[144:147], a[140:143], a[28:31], v[144:147]\n" \
"v_exp_f32 v139, v139\n" \
"v_mfma_f32_16x16x32_bf16 v[148:151], a[156:159], a[28:31], 0\n" \
"ds_read_b128 a[248:251], v242 offset:14336\n" \
"v_cvt_pk_bf16_f32 v137, v138, v139\n" \
"v_mfma_f32_16x16x32_bf16 v[148:151], a[152:155], a[24:27], v[148:151]\n" \
"v_exp_f32 v140, v140\n" \
"v_mfma_f32_16x16x32_bf16 v[148:151], a[148:151], a[20:23], v[148:151]\n" \
"ds_read_b128 a[216:219], v242 offset:6144\n" \
"v_mfma_f32_16x16x32_bf16 v[148:151], a[144:147], a[16:19], v[148:151]\n" \
"v_exp_f32 v141, v141\n" \
"v_mfma_f32_16x16x32_bf16 v[152:155], a[160:163], a[16:19], 0\n" \
"ds_read_b128 a[220:223], v242 offset:7168\n" \
"v_cvt_pk_bf16_f32 v138, v140, v141\n" \
"v_mfma_f32_16x16x32_bf16 v[152:155], a[164:167], a[20:23], v[152:155]\n" \
"v_exp_f32 v142, v142\n" \
"v_mfma_f32_16x16x32_bf16 v[152:155], a[168:171], a[24:27], v[152:155]\n" \
"ds_read_b128 a[252:255], v242 offset:15360\n" \
"v_mfma_f32_16x16x32_bf16 v[152:155], a[172:175], a[28:31], v[152:155]\n" \
"v_exp_f32 v143, v143\n" \
"v_mfma_f32_16x16x32_bf16 v[156:159], a[188:191], a[28:31], 0\n" \
"v_cvt_pk_bf16_f32 v139, v142, v143\n" \
"v_mfma_f32_16x16x32_bf16 v[156:159], a[184:187], a[24:27], v[156:159]\n" \
"buffer_load_dwordx4 v248, s[32:35], s88 offen offset:2048 sc1 lds\n" \
"v_mfma_f32_16x16x32_bf16 v[156:159], a[180:183], a[20:23], v[156:159]\n" \
"v_exp_f32 v144, v144\n" \
"v_mfma_f32_16x16x32_bf16 v[156:159], a[176:179], a[16:19], v[156:159]\n" \
"v_mfma_f32_16x16x32_bf16 a[32:35], a[48:51], v[128:131], a[32:35]\n" \
"v_exp_f32 v145, v145\n" \
"v_mfma_f32_16x16x32_bf16 a[32:35], a[48:51], v[136:139], a[32:35]\n" \
"s_waitcnt lgkmcnt(12)\n" \
"v_cvt_pk_bf16_f32 v144, v144, v145\n" \
"v_mfma_f32_16x16x32_bf16 v[0:3], a[224:227], v[136:139], v[0:3]\n" \
"v_exp_f32 v146, v146\n" \
"v_mfma_f32_16x16x32_bf16 v[0:3], a[192:195], v[128:131], v[0:3]\n" \
"v_mfma_f32_16x16x32_bf16 v[4:7], a[196:199], v[128:131], v[4:7]\n" \
"v_exp_f32 v147, v147\n" \
"v_mfma_f32_16x16x32_bf16 v[4:7], a[228:231], v[136:139], v[4:7]\n" \
"s_waitcnt lgkmcnt(8)\n" \
"v_cvt_pk_bf16_f32 v145, v146, v147\n" \
"v_mfma_f32_16x16x32_bf16 v[8:11], a[232:235], v[136:139], v[8:11]\n" \
"v_exp_f32 v148, v148\n" \
"v_mfma_f32_16x16x32_bf16 v[8:11], a[200:203], v[128:131], v[8:11]\n" \
"v_mfma_f32_16x16x32_bf16 v[12:15], a[204:207], v[128:131], v[12:15]\n" \
"v_exp_f32 v149, v149\n" \
"v_mfma_f32_16x16x32_bf16 v[12:15], a[236:239], v[136:139], v[12:15]\n" \
"s_waitcnt lgkmcnt(4)\n" \
"v_cvt_pk_bf16_f32 v146, v148, v149\n" \
"v_mfma_f32_16x16x32_bf16 v[16:19], a[240:243], v[136:139], v[16:19]\n" \
"v_exp_f32 v150, v150\n" \
"v_mfma_f32_16x16x32_bf16 v[16:19], a[208:211], v[128:131], v[16:19]\n" \
"v_mfma_f32_16x16x32_bf16 v[20:23], a[212:215], v[128:131], v[20:23]\n" \
"v_exp_f32 v151, v151\n" \
"v_mfma_f32_16x16x32_bf16 v[20:23], a[244:247], v[136:139], v[20:23]\n" \
"s_waitcnt lgkmcnt(0)\n" \
"v_cvt_pk_bf16_f32 v147, v150, v151\n" \
"v_mfma_f32_16x16x32_bf16 v[24:27], a[248:251], v[136:139], v[24:27]\n" \
"buffer_load_dwordx4 v248, s[32:35], s88 offen offset:3072 sc1 lds\n" \
"v_mfma_f32_16x16x32_bf16 v[24:27], a[216:219], v[128:131], v[24:27]\n" \
"s_add_u32 s89, s88, 589824\n" \
"v_exp_f32 v152, v152\n" \
"v_mfma_f32_16x16x32_bf16 v[28:31], a[220:223], v[128:131], v[28:31]\n" \
"s_add_u32 s88, s88, s27\n" \
"s_add_u32 s19, s19, 0x4000\n" \
"s_bitset0_b32 s19, 16\n" \
"v_mfma_f32_16x16x32_bf16 v[28:31], a[252:255], v[136:139], v[28:31]\n" \
"s_cmp_eq_u32 s88, s16\n" \
"v_exp_f32 v153, v153\n" \
"v_mfma_f32_16x16x32_bf16 v[128:131], a[128:131], v[160:163], 0\n" \
"s_cmov_b32 s88, s21\n" \
"s_cmov_b32 s27, s91\n" \
"v_cvt_pk_bf16_f32 v152, v152, v153\n" \
"v_mfma_f32_16x16x32_bf16 v[128:131], a[132:135], v[164:167], v[128:131]\n" \
"s_cmov_b32 s16, s77\n" \
"v_exp_f32 v154, v154\n" \
"v_mfma_f32_16x16x32_bf16 v[128:131], a[136:139], v[168:171], v[128:131]\n" \
"s_cmov_b32 s21, s92\n" \
"s_cmovk_i32 s91, 0x4000\n" \
"s_cmp_eq_u32 s88, s17\n" \
"v_mfma_f32_16x16x32_bf16 v[128:131], a[140:143], v[172:175], v[128:131]\n" \
"s_cmov_b32 s88, s18\n" \
"v_exp_f32 v155, v155\n" \
"v_mfma_f32_16x16x32_bf16 v[132:135], a[156:159], v[172:175], 0\n" \
"s_cmp_lg_u32 s76, 0\n" \
"s_cbranch_scc1 .Land_%=\n" \
".Labd_%=:\n" \
"v_mfma_f32_16x16x32_bf16 v[132:135], a[152:155], v[168:171], v[132:135]\n" \
"v_exp_f32 v156, v156\n" \
"v_mfma_f32_16x16x32_bf16 v[132:135], a[148:151], v[164:167], v[132:135]\n" \
"v_cvt_pk_bf16_f32 v153, v154, v155\n" \
"v_mfma_f32_16x16x32_bf16 v[132:135], a[144:147], v[160:163], v[132:135]\n" \
"v_exp_f32 v157, v157\n" \
"v_mfma_f32_16x16x32_bf16 v[136:139], a[160:163], v[160:163], 0\n" \
"v_cvt_pk_bf16_f32 v154, v156, v157\n" \
"v_mfma_f32_16x16x32_bf16 v[136:139], a[164:167], v[164:167], v[136:139]\n" \
"v_exp_f32 v158, v158\n" \
"v_mfma_f32_16x16x32_bf16 v[136:139], a[168:171], v[168:171], v[136:139]\n" \
"v_mfma_f32_16x16x32_bf16 v[136:139], a[172:175], v[172:175], v[136:139]\n" \
"v_exp_f32 v159, v159\n" \
"v_mfma_f32_16x16x32_bf16 v[140:143], a[188:191], v[172:175], 0\n" \
"v_cvt_pk_bf16_f32 v155, v158, v159\n" \
"v_mfma_f32_16x16x32_bf16 v[140:143], a[184:187], v[168:171], v[140:143]\n" \
"v_mfma_f32_16x16x32_bf16 v[140:143], a[180:183], v[164:167], v[140:143]\n" \
"v_exp_f32 v128, v128\n" \
"v_mfma_f32_16x16x32_bf16 v[140:143], a[176:179], v[160:163], v[140:143]\n" \
"v_mfma_f32_16x16x32_bf16 a[36:39], a[48:51], v[144:147], a[36:39]\n" \
"v_exp_f32 v129, v129\n" \
"v_mfma_f32_16x16x32_bf16 a[36:39], a[48:51], v[152:155], a[36:39]\n" \
"v_cvt_pk_bf16_f32 v128, v128, v129\n" \
"v_mfma_f32_16x16x32_bf16 v[32:35], a[224:227], v[152:155], v[32:35]\n" \
"v_exp_f32 v130, v130\n" \
"v_mfma_f32_16x16x32_bf16 v[32:35], a[192:195], v[144:147], v[32:35]\n" \
"v_mfma_f32_16x16x32_bf16 v[36:39], a[196:199], v[144:147], v[36:39]\n" \
"v_exp_f32 v131, v131\n" \
"v_mfma_f32_16x16x32_bf16 v[36:39], a[228:231], v[152:155], v[36:39]\n" \
"v_cvt_pk_bf16_f32 v129, v130, v131\n" \
"v_mfma_f32_16x16x32_bf16 v[40:43], a[232:235], v[152:155], v[40:43]\n" \
"v_exp_f32 v132, v132\n" \
"v_mfma_f32_16x16x32_bf16 v[40:43], a[200:203], v[144:147], v[40:43]\n" \
"v_mfma_f32_16x16x32_bf16 v[44:47], a[204:207], v[144:147], v[44:47]\n" \
"v_exp_f32 v133, v133\n" \
"v_mfma_f32_16x16x32_bf16 v[44:47], a[236:239], v[152:155], v[44:47]\n" \
"v_cvt_pk_bf16_f32 v130, v132, v133\n" \
"v_mfma_f32_16x16x32_bf16 v[48:51], a[240:243], v[152:155], v[48:51]\n" \
"v_exp_f32 v134, v134\n" \
"v_mfma_f32_16x16x32_bf16 v[48:51], a[208:211], v[144:147], v[48:51]\n" \
"v_mfma_f32_16x16x32_bf16 v[52:55], a[212:215], v[144:147], v[52:55]\n" \
"v_exp_f32 v135, v135\n" \
"v_mfma_f32_16x16x32_bf16 v[52:55], a[244:247], v[152:155], v[52:55]\n" \
"v_cvt_pk_bf16_f32 v131, v134, v135\n" \
"v_mfma_f32_16x16x32_bf16 v[56:59], a[248:251], v[152:155], v[56:59]\n" \
"v_mfma_f32_16x16x32_bf16 v[56:59], a[216:219], v[144:147], v[56:59]\n" \
"v_exp_f32 v136, v136\n" \
"v_mfma_f32_16x16x32_bf16 v[60:63], a[220:223], v[144:147], v[60:63]\n" \
"v_mfma_f32_16x16x32_bf16 v[60:63], a[252:255], v[152:155], v[60:63]\n" \
"s_waitcnt vmcnt(12)\n" \
"v_exp_f32 v137, v137\n" \
"v_mfma_f32_16x16x32_bf16 v[144:147], a[128:131], v[176:179], 0\n" \
"s_barrier\n" \
"s_mov_b32 m0, s20\n" \
"v_cvt_pk_bf16_f32 v136, v136, v137\n" \
"v_mfma_f32_16x16x32_bf16 v[144:147], a[132:135], v[180:183], v[144:147]\n" \
"v_exp_f32 v138, v138\n" \
"v_mfma_f32_16x16x32_bf16 v[144:147], a[136:139], v[184:187], v[144:147]\n" \
"buffer_load_dwordx4 v248, s[32:35], s89 offen offset:0 sc1 lds\n" \
"v_mfma_f32_16x16x32_bf16 v[144:147], a[140:143], v[188:191], v[144:147]\n" \
"v_exp_f32 v139, v139\n" \
"v_mfma_f32_16x16x32_bf16 v[148:151], a[156:159], v[188:191], 0\n" \
"ds_read_b128 a[128:131], v240 offset:0\n" \
"v_cvt_pk_bf16_f32 v137, v138, v139\n" \
"v_mfma_f32_16x16x32_bf16 v[148:151], a[152:155], v[184:187], v[148:151]\n" \
"v_exp_f32 v140, v140\n" \
"v_mfma_f32_16x16x32_bf16 v[148:151], a[148:151], v[180:183], v[148:151]\n" \
"ds_read_b128 a[132:135], v240 offset:1024\n" \
"ds_read_b128 a[136:139], v240 offset:2048\n" \
"v_mfma_f32_16x16x32_bf16 v[148:151], a[144:147], v[176:179], v[148:151]\n" \
"v_exp_f32 v141, v141\n" \
"v_mfma_f32_16x16x32_bf16 v[152:155], a[160:163], v[176:179], 0\n" \
"buffer_load_dwordx4 v248, s[32:35], s89 offen offset:1024 sc1 lds\n" \
"v_mfma_f32_16x16x32_bf16 v[152:155], a[164:167], v[180:183], v[152:155]\n" \
"v_exp_f32 v142, v142\n" \
"v_mfma_f32_16x16x32_bf16 v[152:155], a[168:171], v[184:187], v[152:155]\n" \
"ds_read_b128 a[140:143], v240 offset:3072\n" \
"v_cvt_pk_bf16_f32 v138, v140, v141\n" \
"v_mfma_f32_16x16x32_bf16 v[152:155], a[172:175], v[188:191], v[152:155]\n" \
"v_exp_f32 v143, v143\n" \
"v_mfma_f32_16x16x32_bf16 v[156:159], a[188:191], v[188:191], 0\n" \
"ds_read_b128 a[156:159], v240 offset:7168\n" \
"v_cvt_pk_bf16_f32 v139, v142, v143\n" \
"v_mfma_f32_16x16x32_bf16 v[156:159], a[184:187], v[184:187], v[156:159]\n" \
"ds_read_b128 a[152:155], v240 offset:6144\n" \
"ds_read_b128 a[148:151], v240 offset:5120\n" \
"v_mfma_f32_16x16x32_bf16 v[156:159], a[180:183], v[180:183], v[156:159]\n" \
"v_exp_f32 v144, v144\n" \
"v_mfma_f32_16x16x32_bf16 v[156:159], a[176:179], v[176:179], v[156:159]\n" \
"buffer_load_dwordx4 v248, s[32:35], s89 offen offset:2048 sc1 lds\n" \
"v_mfma_f32_16x16x32_bf16 a[40:43], a[48:51], v[128:131], a[40:43]\n" \
"v_exp_f32 v145, v145\n" \
"v_mfma_f32_16x16x32_bf16 a[40:43], a[48:51], v[136:139], a[40:43]\n" \
"ds_read_b128 a[144:147], v240 offset:4096\n" \
"v_cvt_pk_bf16_f32 v144, v144, v145\n" \
"v_mfma_f32_16x16x32_bf16 v[64:67], a[224:227], v[136:139], v[64:67]\n" \
"v_exp_f32 v146, v146\n" \
"v_mfma_f32_16x16x32_bf16 v[64:67], a[192:195], v[128:131], v[64:67]\n" \
"ds_read_b128 a[160:163], v240 offset:8192\n" \
"ds_read_b128 a[164:167], v240 offset:9216\n" \
"v_mfma_f32_16x16x32_bf16 v[68:71], a[196:199], v[128:131], v[68:71]\n" \
"v_exp_f32 v147, v147\n" \
"v_mfma_f32_16x16x32_bf16 v[68:71], a[228:231], v[136:139], v[68:71]\n" \
"buffer_load_dwordx4 v248, s[32:35], s89 offen offset:3072 sc1 lds\n" \
"v_mfma_f32_16x16x32_bf16 v[72:75], a[232:235], v[136:139], v[72:75]\n" \
"s_add_u32 s20, s20, 0x4000\n" \
"v_exp_f32 v148, v148\n" \
"v_mfma_f32_16x16x32_bf16 v[72:75], a[200:203], v[128:131], v[72:75]\n" \
"s_and_b32 s20, s20, 0xffff\n" \
"ds_read_b128 a[168:171], v240 offset:10240\n" \
"v_cvt_pk_bf16_f32 v145, v146, v147\n" \
"v_mfma_f32_16x16x32_bf16 v[76:79], a[204:207], v[128:131], v[76:79]\n" \
"s_bitset1_b32 s20, 16\n" \
"v_exp_f32 v149, v149\n" \
"v_mfma_f32_16x16x32_bf16 v[76:79], a[236:239], v[136:139], v[76:79]\n" \
"ds_read_b128 a[172:175], v240 offset:11264\n" \
"v_cvt_pk_bf16_f32 v146, v148, v149\n" \
"v_mfma_f32_16x16x32_bf16 v[80:83], a[240:243], v[136:139], v[80:83]\n" \
"v_exp_f32 v150, v150\n" \
"v_mfma_f32_16x16x32_bf16 v[80:83], a[208:211], v[128:131], v[80:83]\n" \
"v_mfma_f32_16x16x32_bf16 v[84:87], a[212:215], v[128:131], v[84:87]\n" \
"s_sub_u32 s95, s95, 1\n" \
"v_exp_f32 v151, v151\n" \
"v_mfma_f32_16x16x32_bf16 v[84:87], a[244:247], v[136:139], v[84:87]\n" \
"v_cvt_pk_bf16_f32 v147, v150, v151\n" \
"v_mfma_f32_16x16x32_bf16 v[88:91], a[248:251], v[136:139], v[88:91]\n" \
"v_mfma_f32_16x16x32_bf16 v[88:91], a[216:219], v[128:131], v[88:91]\n" \
"v_exp_f32 v152, v152\n" \
"v_mfma_f32_16x16x32_bf16 v[92:95], a[220:223], v[128:131], v[92:95]\n" \
"v_exp_f32 v153, v153\n" \
"v_mfma_f32_16x16x32_bf16 v[92:95], a[252:255], v[136:139], v[92:95]\n" \
"s_waitcnt lgkmcnt(8)\n" \
"s_cbranch_scc0 .Lloop1_%=\n" \
".Lhook_%=:\n" \
".Lst0_13_%=:\n" \
"s_cbranch_execz .Lstb0_%=\n" \
  \
"s_load_dwordx4 s[48:51], %[ka], 192\n" \
"s_load_dwordx2 s[96:97], %[ka], 208\n" \
"v_accvgpr_read_b32 v252, a32\n" \
"v_accvgpr_read_b32 v254, a36\n" \
"v_min_f32 v253, v252, v254\n" \
"v_max_f32 v255, v252, v254\n" \
"v_cmp_nle_f32 s[98:99], v226, v253\n" \
"v_cmp_nle_f32 vcc, v255, v227\n" \
"v_cmp_u_f32 s[14:15], v252, v254\n" \
"s_nop 1\n" \
"s_or_b64 vcc, vcc, s[98:99]\n" \
"s_or_b64 vcc, vcc, s[14:15]\n" \
"s_cbranch_vccz .Loka_%=\n" \
"s_call_b64 s[98:99], .Lredo0_%=\n" \
"v_accvgpr_read_b32 v252, a32\n" \
"v_accvgpr_read_b32 v254, a36\n" \
".Loka_%=:\n" \
"s_add_u32 s23, s30, 4096\n" \
"v_rcp_f32 v252, v252\n" \
"v_rcp_f32 v254, v254\n" \
"s_nop 0\n" \
"v_mov_b32 v253, v252\n" \
"v_mov_b32 v255, v254\n" \
"v_pk_mul_f32 v[0:1], v[0:1], v[252:253]\n" \
"v_pk_mul_f32 v[2:3], v[2:3], v[252:253]\n" \
"v_pk_mul_f32 v[4:5], v[4:5], v[252:253]\n" \
"v_pk_mul_f32 v[6:7], v[6:7], v[252:253]\n" \
"v_cvt_pk_bf16_f32 v0, v0, v1\n" \
"v_cvt_pk_bf16_f32 v1, v2, v3\n" \
"v_cvt_pk_bf16_f32 v2, v4, v5\n" \
"v_cvt_pk_bf16_f32 v3, v6, v7\n" \
"buffer_store_dwordx4 v[0:3], v248, s[32:35], s30 offen offset:0 sc1\n" \
"v_pk_mul_f32 v[32:33], v[32:33], v[254:255]\n" \
"v_pk_mul_f32 v[34:35], v[34:35], v[254:255]\n" \
"v_pk_mul_f32 v[36:37], v[36:37], v[254:255]\n" \
"v_pk_mul_f32 v[38:39], v[38:39], v[254:255]\n" \
"v_cvt_pk_bf16_f32 v4, v32, v33\n" \
"v_cvt_pk_bf16_f32 v5, v34, v35\n" \
"v_cvt_pk_bf16_f32 v6, v36, v37\n" \
"v_cvt_pk_bf16_f32 v7, v38, v39\n" \
"buffer_store_dwordx4 v[4:7], v248, s[32:35], s30 offen offset:1024 sc1\n" \
"v_pk_mul_f32 v[8:9], v[8:9], v[252:253]\n" \
"v_pk_mul_f32 v[10:11], v[10:11], v[252:253]\n" \
"v_pk_mul_f32 v[12:13], v[12:13], v[252:253]\n" \
"v_pk_mul_f32 v[14:15], v[14:15], v[252:253]\n" \
"v_cvt_pk_bf16_f32 v8, v8, v9\n" \
"v_cvt_pk_bf16_f32 v9, v10, v11\n" \
"v_cvt_pk_bf16_f32 v10, v12, v13\n" \
"v_cvt_pk_bf16_f32 v11, v14, v15\n" \
"buffer_store_dwordx4 v[8:11], v248, s[32:35], s30 offen offset:2048 sc1\n" \
"v_pk_mul_f32 v[40:41], v[40:41], v[254:255]\n" \
"v_pk_mul_f32 v[42:43], v[42:43], v[254:255]\n" \
"v_pk_mul_f32 v[44:45], v[44:45], v[254:255]\n" \
"v_pk_mul_f32 v[46:47], v[46:47], v[254:255]\n" \
"v_cvt_pk_bf16_f32 v12, v40, v41\n" \
"v_cvt_pk_bf16_f32 v13, v42, v43\n" \
"v_cvt_pk_bf16_f32 v14, v44, v45\n" \
"v_cvt_pk_bf16_f32 v15, v46, v47\n" \
"buffer_store_dwordx4 v[12:15], v248, s[32:35], s30 offen offset:3072 sc1\n" \
"v_pk_mul_f32 v[16:17], v[16:17], v[252:253]\n" \
"v_pk_mul_f32 v[18:19], v[18:19], v[252:253]\n" \
"v_pk_mul_f32 v[20:21], v[20:21], v[252:253]\n" \
"v_pk_mul_f32 v[22:23], v[22:23], v[252:253]\n" \
"v_cvt_pk_bf16_f32 v16, v16, v17\n" \
"v_cvt_pk_bf16_f32 v17, v18, v19\n" \
"v_cvt_pk_bf16_f32 v18, v20, v21\n" \
"v_cvt_pk_bf16_f32 v19, v22, v23\n" \
"buffer_store_dwordx4 v[16:19], v248, s[32:35], s23 offen offset:0 sc1\n" \
"v_pk_mul_f32 v[48:49], v[48:49], v[254:255]\n" \
"v_pk_mul_f32 v[50:51], v[50:51], v[254:255]\n" \
"v_pk_mul_f32 v[52:53], v[52:53], v[254:255]\n" \
".Lst0_14_%=:\n" \
"s_cbranch_execz .Lstb0_%=\n" \
"v_pk_mul_f32 v[54:55], v[54:55], v[254:255]\n" \
"v_cvt_pk_bf16_f32 v20, v48, v49\n" \
"v_cvt_pk_bf16_f32 v21, v50, v51\n" \
"v_cvt_pk_bf16_f32 v22, v52, v53\n" \
"v_cvt_pk_bf16_f32 v23, v54, v55\n" \
"buffer_store_dwordx4 v[20:23], v248, s[32:35], s23 offen offset:1024 sc1\n" \
"v_pk_mul_f32 v[24:25], v[24:25], v[252:253]\n" \
"v_pk_mul_f32 v[26:27], v[26:27], v[252:253]\n" \
"v_pk_mul_f32 v[28:29], v[28:29], v[252:253]\n" \
"v_pk_mul_f32 v[30:31], v[30:31], v[252:253]\n" \
"v_cvt_pk_bf16_f32 v24, v24, v25\n" \
"v_cvt_pk_bf16_f32 v25, v26, v27\n" \
"v_cvt_pk_bf16_f32 v26, v28, v29\n" \
"v_cvt_pk_bf16_f32 v27, v30, v31\n" \
"buffer_store_dwordx4 v[24:27], v248, s[32:35], s23 offen offset:2048 sc1\n" \
"v_pk_mul_f32 v[56:57], v[56:57], v[254:255]\n" \
"v_pk_mul_f32 v[58:59], v[58:59], v[254:255]\n" \
"v_pk_mul_f32 v[60:61], v[60:61], v[254:255]\n" \
"v_pk_mul_f32 v[62:63], v[62:63], v[254:255]\n" \
"v_cvt_pk_bf16_f32 v28, v56, v57\n" \
"v_cvt_pk_bf16_f32 v29, v58, v59\n" \
"v_cvt_pk_bf16_f32 v30, v60, v61\n" \
"v_cvt_pk_bf16_f32 v31, v62, v63\n" \
"buffer_store_dwordx4 v[28:31], v248, s[32:35], s23 offen offset:3072 sc1\n" \
"s_lshl2_add_u32 s25, s66, s31\n" \
"s_mov_b32 s76, 1\n" \
"s_mov_b64 exec, 0\n" \
"s_cmp_eq_u32 %[wid], 0\n" \
"s_cbranch_scc1 .Lst1_0_%=\n" \
"s_cmp_eq_u32 %[wid], 1\n" \
"s_cbranch_scc1 .Lst1_1_%=\n" \
"s_cmp_eq_u32 %[wid], 2\n" \
"s_cbranch_scc1 .Lst1_2_%=\n" \
"s_branch .Lst1_3_%=\n" \
".Lstb1_%=:\n" \
"s_mov_b64 exec, -1\n" \
"s_lshl_b32 s95, s67, 1\n" \
"s_sub_u32 s95, 11, s95\n" \
"s_max_i32 s95, s95, 0\n" \
"s_waitcnt lgkmcnt(0)\n" \
"s_cmp_lt_u32 s67, 4\n" \
"s_cbranch_scc0 .Lloop3_%=\n" \
"s_mov_b32 s95, 3\n" \
".p2align 3\n" \
".Lloop2_%=:\n" \
  \
"v_mfma_f32_16x16x32_bf16 v[128:131], a[128:131], v[160:163], 0\n" \
"s_add_u32 s86, s86, 0x4000\n" \
"ds_read_b128 a[188:191], v240 offset:15360\n" \
"v_cvt_pk_bf16_f32 v152, v152, v153\n" \
"v_mfma_f32_16x16x32_bf16 v[128:131], a[132:135], v[164:167], v[128:131]\n" \
"s_bitset0_b32 s86, 16\n" \
"v_exp_f32 v154, v154\n" \
"v_mfma_f32_16x16x32_bf16 v[128:131], a[136:139], v[168:171], v[128:131]\n" \
"ds_read_b128 a[184:187], v240 offset:14336\n" \
"v_add_u32 v242, 0x10000, v240\n" \
"v_mfma_f32_16x16x32_bf16 v[128:131], a[140:143], v[172:175], v[128:131]\n" \
"s_waitcnt lgkmcnt(6)\n" \
"v_exp_f32 v155, v155\n" \
"v_mfma_f32_16x16x32_bf16 v[132:135], a[156:159], v[172:175], 0\n" \
"s_mov_b32 m0, s19\n" \
"ds_read_b128 a[180:183], v240 offset:13312\n" \
"v_cvt_pk_bf16_f32 v153, v154, v155\n" \
"v_mfma_f32_16x16x32_bf16 v[132:135], a[152:155], v[168:171], v[132:135]\n" \
"ds_read_b128 a[176:179], v240 offset:12288\n" \
"v_exp_f32 v156, v156\n" \
"v_mfma_f32_16x16x32_bf16 v[132:135], a[148:151], v[164:167], v[132:135]\n" \
"buffer_load_dwordx4 v248, s[32:35], s88 offen offset:0 sc1 lds\n" \
"v_mfma_f32_16x16x32_bf16 v[132:135], a[144:147], v[160:163], v[132:135]\n" \
"s_waitcnt lgkmcnt(4)\n" \
"v_exp_f32 v157, v157\n" \
"v_mfma_f32_16x16x32_bf16 v[136:139], a[160:163], v[160:163], 0\n" \
"v_cvt_pk_bf16_f32 v154, v156, v157\n" \
"v_mfma_f32_16x16x32_bf16 v[136:139], a[164:167], v[164:167], v[136:139]\n" \
"v_exp_f32 v158, v158\n" \
"v_mfma_f32_16x16x32_bf16 v[136:139], a[168:171], v[168:171], v[136:139]\n" \
"buffer_load_dwordx4 v248, s[32:35], s88 offen offset:1024 sc1 lds\n" \
"v_mfma_f32_16x16x32_bf16 v[136:139], a[172:175], v[172:175], v[136:139]\n" \
"s_waitcnt lgkmcnt(0)\n" \
"v_exp_f32 v159, v159\n" \
"v_mfma_f32_16x16x32_bf16 v[140:143], a[188:191], v[172:175], 0\n" \
"v_add_u32 v240, s86, v248\n" \
"v_cvt_pk_bf16_f32 v155, v158, v159\n" \
"v_mfma_f32_16x16x32_bf16 v[140:143], a[184:187], v[168:171], v[140:143]\n" \
"buffer_load_dwordx4 v248, s[32:35], s88 offen offset:2048 sc1 lds\n" \
"v_mfma_f32_16x16x32_bf16 v[140:143], a[180:183], v[164:167], v[140:143]\n" \
"v_exp_f32 v128, v128\n" \
"v_mfma_f32_16x16x32_bf16 v[140:143], a[176:179], v[160:163], v[140:143]\n" \
"buffer_load_dwordx4 v248, s[32:35], s88 offen offset:3072 sc1 lds\n" \
"v_mfma_f32_16x16x32_bf16 a[44:47], a[48:51], v[144:147], a[44:47]\n" \
"s_add_u32 s89, s88, 589824\n" \
"v_exp_f32 v129, v129\n" \
"v_mfma_f32_16x16x32_bf16 a[44:47], a[48:51], v[152:155], a[44:47]\n" \
"s_add_u32 s88, s88, s27\n" \
"s_add_u32 s19, s19, 0x4000\n" \
"v_cvt_pk_bf16_f32 v128, v128, v129\n" \
"v_mfma_f32_16x16x32_bf16 v[96:99], a[224:227], v[152:155], v[96:99]\n" \
"s_bitset0_b32 s19, 16\n" \
"v_exp_f32 v130, v130\n" \
"v_mfma_f32_16x16x32_bf16 v[96:99], a[192:195], v[144:147], v[96:99]\n" \
"s_cmp_eq_u32 s88, s16\n" \
"s_cmov_b32 s88, s21\n" \
"ds_read_b128 a[224:227], v242 offset:8192\n" \
"v_mfma_f32_16x16x32_bf16 v[100:103], a[196:199], v[144:147], v[100:103]\n" \
"s_cmov_b32 s27, s91\n" \
"v_exp_f32 v131, v131\n" \
"v_mfma_f32_16x16x32_bf16 v[100:103], a[228:231], v[152:155], v[100:103]\n" \
"s_cmov_b32 s16, s77\n" \
"ds_read_b128 a[192:195], v242 offset:0\n" \
"v_cvt_pk_bf16_f32 v129, v130, v131\n" \
"v_mfma_f32_16x16x32_bf16 v[104:107], a[232:235], v[152:155], v[104:107]\n" \
"s_cmov_b32 s21, s92\n" \
"v_exp_f32 v132, v132\n" \
"v_mfma_f32_16x16x32_bf16 v[104:107], a[200:203], v[144:147], v[104:107]\n" \
"s_cmovk_i32 s91, 0x4000\n" \
"ds_read_b128 a[196:199], v242 offset:1024\n" \
"ds_read_b128 a[228:231], v242 offset:9216\n" \
"v_mfma_f32_16x16x32_bf16 v[108:111], a[204:207], v[144:147], v[108:111]\n" \
"s_mov_b32 m0, s87\n" \
"v_exp_f32 v133, v133\n" \
"v_mfma_f32_16x16x32_bf16 v[108:111], a[236:239], v[152:155], v[108:111]\n" \
"s_cmp_eq_u32 s88, s17\n" \
"ds_read_b128 a[232:235], v242 offset:10240\n" \
"v_cvt_pk_bf16_f32 v130, v132, v133\n" \
"v_mfma_f32_16x16x32_bf16 v[112:115], a[240:243], v[152:155], v[112:115]\n" \
"s_cmov_b32 s88, s18\n" \
"buffer_load_dwordx4 v248, s[32:35], s93 offen sc1 lds\n" \
"v_mfma_f32_16x16x32_bf16 v[112:115], a[208:211], v[144:147], v[112:115]\n" \
"s_add_u32 s93, s93, 1024\n" \
"v_exp_f32 v134, v134\n" \
"v_mfma_f32_16x16x32_bf16 v[116:119], a[212:215], v[144:147], v[116:119]\n" \
"s_add_u32 s87, s87, 1024\n" \
"v_exp_f32 v135, v135\n" \
"v_mfma_f32_16x16x32_bf16 v[116:119], a[244:247], v[152:155], v[116:119]\n" \
"ds_read_b128 a[200:203], v242 offset:2048\n" \
"v_cvt_pk_bf16_f32 v131, v134, v135\n" \
"v_mfma_f32_16x16x32_bf16 v[120:123], a[248:251], v[152:155], v[120:123]\n" \
"ds_read_b128 a[204:207], v242 offset:3072\n" \
"ds_read_b128 a[236:239], v242 offset:11264\n" \
"v_mfma_f32_16x16x32_bf16 v[120:123], a[216:219], v[144:147], v[120:123]\n" \
"v_exp_f32 v136, v136\n" \
"v_mfma_f32_16x16x32_bf16 v[124:127], a[220:223], v[144:147], v[124:127]\n" \
"ds_read_b128 a[240:243], v242 offset:12288\n" \
"v_mfma_f32_16x16x32_bf16 v[124:127], a[252:255], v[152:155], v[124:127]\n" \
"s_waitcnt vmcnt(13)\n" \
"v_exp_f32 v137, v137\n" \
"v_mfma_f32_16x16x32_bf16 v[144:147], a[128:131], v[176:179], 0\n" \
"s_barrier\n" \
"ds_read_b128 a[208:211], v242 offset:4096\n" \
"v_cvt_pk_bf16_f32 v136, v136, v137\n" \
"v_mfma_f32_16x16x32_bf16 v[144:147], a[132:135], v[180:183], v[144:147]\n" \
"s_mov_b32 m0, s20\n" \
"v_exp_f32 v138, v138\n" \
"v_mfma_f32_16x16x32_bf16 v[144:147], a[136:139], v[184:187], v[144:147]\n" \
"buffer_load_dwordx4 v248, s[32:35], s89 offen offset:0 sc1 lds\n" \
"v_mfma_f32_16x16x32_bf16 v[144:147], a[140:143], v[188:191], v[144:147]\n" \
"v_exp_f32 v139, v139\n" \
"v_mfma_f32_16x16x32_bf16 v[148:151], a[156:159], v[188:191], 0\n" \
"ds_read_b128 a[212:215], v242 offset:5120\n" \
"v_cvt_pk_bf16_f32 v137, v138, v139\n" \
"v_mfma_f32_16x16x32_bf16 v[148:151], a[152:155], v[184:187], v[148:151]\n" \
"v_exp_f32 v140, v140\n" \
"v_mfma_f32_16x16x32_bf16 v[148:151], a[148:151], v[180:183], v[148:151]\n" \
"ds_read_b128 a[244:247], v242 offset:13312\n" \
"ds_read_b128 a[248:251], v242 offset:14336\n" \
"v_mfma_f32_16x16x32_bf16 v[148:151], a[144:147], v[176:179], v[148:151]\n" \
"v_exp_f32 v141, v141\n" \
"v_mfma_f32_16x16x32_bf16 v[152:155], a[160:163], v[176:179], 0\n" \
"buffer_load_dwordx4 v248, s[32:35], s89 offen offset:1024 sc1 lds\n" \
"v_mfma_f32_16x16x32_bf16 v[152:155], a[164:167], v[180:183], v[152:155]\n" \
"v_exp_f32 v142, v142\n" \
"v_mfma_f32_16x16x32_bf16 v[152:155], a[168:171], v[184:187], v[152:155]\n" \
"ds_read_b128 a[216:219], v242 offset:6144\n" \
"v_cvt_pk_bf16_f32 v138, v140, v141\n" \
"v_mfma_f32_16x16x32_bf16 v[152:155], a[172:175], v[188:191], v[152:155]\n" \
"v_exp_f32 v143, v143\n" \
"v_mfma_f32_16x16x32_bf16 v[156:159], a[188:191], v[188:191], 0\n" \
"ds_read_b128 a[128:131], v240 offset:0\n" \
"v_cvt_pk_bf16_f32 v139, v142, v143\n" \
"v_mfma_f32_16x16x32_bf16 v[156:159], a[184:187], v[184:187], v[156:159]\n" \
"ds_read_b128 a[132:135], v240 offset:1024\n" \
"ds_read_b128 a[220:223], v242 offset:7168\n" \
"v_mfma_f32_16x16x32_bf16 v[156:159], a[180:183], v[180:183], v[156:159]\n" \
"v_exp_f32 v144, v144\n" \
"v_mfma_f32_16x16x32_bf16 v[156:159], a[176:179], v[176:179], v[156:159]\n" \
"buffer_load_dwordx4 v248, s[32:35], s89 offen offset:2048 sc1 lds\n" \
"v_mfma_f32_16x16x32_bf16 a[40:43], a[48:51], v[128:131], a[40:43]\n" \
"v_exp_f32 v145, v145\n" \
"v_mfma_f32_16x16x32_bf16 a[40:43], a[48:51], v[136:139], a[40:43]\n" \
"s_waitcnt lgkmcnt(13)\n" \
"ds_read_b128 a[136:139], v240 offset:2048\n" \
"v_cvt_pk_bf16_f32 v144, v144, v145\n" \
"v_mfma_f32_16x16x32_bf16 v[64:67], a[224:227], v[136:139], v[64:67]\n" \
"v_exp_f32 v146, v146\n" \
"v_mfma_f32_16x16x32_bf16 v[64:67], a[192:195], v[128:131], v[64:67]\n" \
"ds_read_b128 a[252:255], v242 offset:15360\n" \
"ds_read_b128 a[140:143], v240 offset:3072\n" \
"v_mfma_f32_16x16x32_bf16 v[68:71], a[196:199], v[128:131], v[68:71]\n" \
"v_exp_f32 v147, v147\n" \
"v_mfma_f32_16x16x32_bf16 v[68:71], a[228:231], v[136:139], v[68:71]\n" \
"s_waitcnt lgkmcnt(12)\n" \
"buffer_load_dwordx4 v248, s[32:35], s89 offen offset:3072 sc1 lds\n" \
"v_mfma_f32_16x16x32_bf16 v[72:75], a[232:235], v[136:139], v[72:75]\n" \
"s_add_u32 s20, s20, 0x4000\n" \
"v_exp_f32 v148, v148\n" \
"v_mfma_f32_16x16x32_bf16 v[72:75], a[200:203], v[128:131], v[72:75]\n" \
"s_and_b32 s20, s20, 0xffff\n" \
"ds_read_b128 a[156:159], v240 offset:7168\n" \
"v_cvt_pk_bf16_f32 v145, v146, v147\n" \
"v_mfma_f32_16x16x32_bf16 v[76:79], a[204:207], v[128:131], v[76:79]\n" \
"s_bitset1_b32 s20, 16\n" \
"v_exp_f32 v149, v149\n" \
"v_mfma_f32_16x16x32_bf16 v[76:79], a[236:239], v[136:139], v[76:79]\n" \
"s_waitcnt lgkmcnt(9)\n" \
"ds_read_b128 a[152:155], v240 offset:6144\n" \
"v_cvt_pk_bf16_f32 v146, v148, v149\n" \
"v_mfma_f32_16x16x32_bf16 v[80:83], a[240:243], v[136:139], v[80:83]\n" \
"v_exp_f32 v150, v150\n" \
"v_mfma_f32_16x16x32_bf16 v[80:83], a[208:211], v[128:131], v[80:83]\n" \
"ds_read_b128 a[148:151], v240 offset:5120\n" \
"ds_read_b128 a[144:147], v240 offset:4096\n" \
"v_mfma_f32_16x16x32_bf16 v[84:87], a[212:215], v[128:131], v[84:87]\n" \
"v_exp_f32 v151, v151\n" \
"v_mfma_f32_16x16x32_bf16 v[84:87], a[244:247], v[136:139], v[84:87]\n" \
"s_waitcnt lgkmcnt(5)\n" \
"ds_read_b128 a[160:163], v240 offset:8192\n" \
"v_cvt_pk_bf16_f32 v147, v150, v151\n" \
"v_mfma_f32_16x16x32_bf16 v[88:91], a[248:251], v[136:139], v[88:91]\n" \
"ds_read_b128 a[164:167], v240 offset:9216\n" \
"ds_read_b128 a[168:171], v240 offset:10240\n" \
"v_mfma_f32_16x16x32_bf16 v[88:91], a[216:219], v[128:131], v[88:91]\n" \
"v_exp_f32 v152, v152\n" \
"v_mfma_f32_16x16x32_bf16 v[92:95], a[220:223], v[128:131], v[92:95]\n" \
"ds_read_b128 a[172:175], v240 offset:11264\n" \
"ds_read_b128 a[188:191], v240 offset:15360\n" \
"v_mfma_f32_16x16x32_bf16 v[92:95], a[252:255], v[136:139], v[92:95]\n" \
"s_waitcnt lgkmcnt(9)\n" \
"v_exp_f32 v153, v153\n" \
"v_mfma_f32_16x16x32_bf16 v[128:131], a[128:131], v[160:163], 0\n" \
"s_add_u32 s86, s86, 0x4000\n" \
"ds_read_b128 a[184:187], v240 offset:14336\n" \
"v_cvt_pk_bf16_f32 v152, v152, v153\n" \
"v_mfma_f32_16x16x32_bf16 v[128:131], a[132:135], v[164:167], v[128:131]\n" \
"s_bitset0_b32 s86, 16\n" \
"v_exp_f32 v154, v154\n" \
"v_mfma_f32_16x16x32_bf16 v[128:131], a[136:139], v[168:171], v[128:131]\n" \
"s_cmp_eq_u32 s88, s24\n" \
"s_cbranch_scc1 .Lchtx_%=\n" \
".Lsbhtx_%=:\n" \
"v_mfma_f32_16x16x32_bf16 v[128:131], a[140:143], v[172:175], v[128:131]\n" \
"s_waitcnt lgkmcnt(6)\n" \
"v_exp_f32 v155, v155\n" \
"v_mfma_f32_16x16x32_bf16 v[132:135], a[156:159], v[172:175], 0\n" \
"s_mov_b32 m0, s19\n" \
"v_add_u32 v242, 0x10000, v240\n" \
"v_cvt_pk_bf16_f32 v153, v154, v155\n" \
"v_mfma_f32_16x16x32_bf16 v[132:135], a[152:155], v[168:171], v[132:135]\n" \
"s_cmp_lg_u32 s76, 0\n" \
"ds_read_b128 a[180:183], v240 offset:13312\n" \
"ds_read_b128 a[176:179], v240 offset:12288\n" \
"v_exp_f32 v156, v156\n" \
"v_mfma_f32_16x16x32_bf16 v[132:135], a[148:151], v[164:167], v[132:135]\n" \
"buffer_load_dwordx4 v248, s[32:35], s88 offen offset:0 sc1 lds\n" \
"v_mfma_f32_16x16x32_bf16 v[132:135], a[144:147], v[160:163], v[132:135]\n" \
"s_waitcnt lgkmcnt(4)\n" \
"v_exp_f32 v157, v157\n" \
"v_mfma_f32_16x16x32_bf16 v[136:139], a[160:163], v[160:163], 0\n" \
"s_cbranch_scc1 .Lantx_%=\n" \
".Labtx_%=:\n" \
"v_cvt_pk_bf16_f32 v154, v156, v157\n" \
"v_mfma_f32_16x16x32_bf16 v[136:139], a[164:167], v[164:167], v[136:139]\n" \
"v_exp_f32 v158, v158\n" \
"v_mfma_f32_16x16x32_bf16 v[136:139], a[168:171], v[168:171], v[136:139]\n" \
"buffer_load_dwordx4 v248, s[32:35], s88 offen offset:1024 sc1 lds\n" \
"v_mfma_f32_16x16x32_bf16 v[136:139], a[172:175], v[172:175], v[136:139]\n" \
"s_waitcnt lgkmcnt(0)\n" \
"v_exp_f32 v159, v159\n" \
"v_mfma_f32_16x16x32_bf16 v[140:143], a[188:191], v[172:175], 0\n" \
"v_add_u32 v240, s86, v248\n" \
"v_cvt_pk_bf16_f32 v155, v158, v159\n" \
"v_mfma_f32_16x16x32_bf16 v[140:143], a[184:187], v[168:171], v[140:143]\n" \
"buffer_load_dwordx4 v248, s[32:35], s88 offen offset:2048 sc1 lds\n" \
"v_mfma_f32_16x16x32_bf16 v[140:143], a[180:183], v[164:167], v[140:143]\n" \
"v_exp_f32 v128, v128\n" \
"v_mfma_f32_16x16x32_bf16 v[140:143], a[176:179], v[160:163], v[140:143]\n" \
"buffer_load_dwordx4 v248, s[32:35], s88 offen offset:3072 sc1 lds\n" \
"v_mfma_f32_16x16x32_bf16 a[44:47], a[48:51], v[144:147], a[44:47]\n" \
"s_add_u32 s89, s88, 589824\n" \
"v_exp_f32 v129, v129\n" \
"v_mfma_f32_16x16x32_bf16 a[44:47], a[48:51], v[152:155], a[44:47]\n" \
"s_add_u32 s88, s88, s27\n" \
"s_add_u32 s19, s19, 0x4000\n" \
"v_cvt_pk_bf16_f32 v128, v128, v129\n" \
"v_mfma_f32_16x16x32_bf16 v[96:99], a[224:227], v[152:155], v[96:99]\n" \
"s_bitset0_b32 s19, 16\n" \
"v_exp_f32 v130, v130\n" \
"v_mfma_f32_16x16x32_bf16 v[96:99], a[192:195], v[144:147], v[96:99]\n" \
"s_cmp_eq_u32 s88, s16\n" \
"s_cmov_b32 s88, s21\n" \
"ds_read_b128 a[224:227], v242 offset:8192\n" \
"v_mfma_f32_16x16x32_bf16 v[100:103], a[196:199], v[144:147], v[100:103]\n" \
"s_cmov_b32 s27, s91\n" \
"v_exp_f32 v131, v131\n" \
"v_mfma_f32_16x16x32_bf16 v[100:103], a[228:231], v[152:155], v[100:103]\n" \
"s_cmov_b32 s16, s77\n" \
"ds_read_b128 a[192:195], v242 offset:0\n" \
"v_cvt_pk_bf16_f32 v129, v130, v131\n" \
"v_mfma_f32_16x16x32_bf16 v[104:107], a[232:235], v[152:155], v[104:107]\n" \
"s_cmov_b32 s21, s92\n" \
"v_exp_f32 v132, v132\n" \
"v_mfma_f32_16x16x32_bf16 v[104:107], a[200:203], v[144:147], v[104:107]\n" \
"s_cmovk_i32 s91, 0x4000\n" \
"ds_read_b128 a[196:199], v242 offset:1024\n" \
"ds_read_b128 a[228:231], v242 offset:9216\n" \
"v_mfma_f32_16x16x32_bf16 v[108:111], a[204:207], v[144:147], v[108:111]\n" \
"s_mov_b32 m0, s87\n" \
"v_exp_f32 v133, v133\n" \
"v_mfma_f32_16x16x32_bf16 v[108:111], a[236:239], v[152:155], v[108:111]\n" \
"s_cmp_eq_u32 s88, s17\n" \
"ds_read_b128 a[232:235], v242 offset:10240\n" \
"v_cvt_pk_bf16_f32 v130, v132, v133\n" \
"v_mfma_f32_16x16x32_bf16 v[112:115], a[240:243], v[152:155], v[112:115]\n" \
"s_cmov_b32 s88, s18\n" \
"buffer_load_dwordx4 v248, s[32:35], s93 offen sc1 lds\n" \
"v_mfma_f32_16x16x32_bf16 v[112:115], a[208:211], v[144:147], v[112:115]\n" \
"s_add_u32 s93, s93, 1024\n" \
"v_exp_f32 v134, v134\n" \
"v_mfma_f32_16x16x32_bf16 v[116:119], a[212:215], v[144:147], v[116:119]\n" \
"s_add_u32 s87, s87, 1024\n" \
"v_exp_f32 v135, v135\n" \
"v_mfma_f32_16x16x32_bf16 v[116:119], a[244:247], v[152:155], v[116:119]\n" \
"ds_read_b128 a[200:203], v242 offset:2048\n" \
"v_cvt_pk_bf16_f32 v131, v134, v135\n" \
"v_mfma_f32_16x16x32_bf16 v[120:123], a[248:251], v[152:155], v[120:123]\n" \
"ds_read_b128 a[204:207], v242 offset:3072\n" \
"ds_read_b128 a[236:239], v242 offset:11264\n" \
"v_mfma_f32_16x16x32_bf16 v[120:123], a[216:219], v[144:147], v[120:123]\n" \
"v_exp_f32 v136, v136\n" \
"v_mfma_f32_16x16x32_bf16 v[124:127], a[220:223], v[144:147], v[124:127]\n" \
"ds_read_b128 a[240:243], v242 offset:12288\n" \
"v_mfma_f32_16x16x32_bf16 v[124:127], a[252:255], v[152:155], v[124:127]\n" \
"s_waitcnt vmcnt(14)\n" \
"v_exp_f32 v137, v137\n" \
"v_mfma_f32_16x16x32_bf16 v[144:147], a[128:131], v[176:179], 0\n" \
"s_barrier\n" \
"ds_read_b128 a[208:211], v242 offset:4096\n" \
"v_cvt_pk_bf16_f32 v136, v136, v137\n" \
"v_mfma_f32_16x16x32_bf16 v[144:147], a[132:135], v[180:183], v[144:147]\n" \
"s_mov_b32 m0, s20\n" \
"v_exp_f32 v138, v138\n" \
"v_mfma_f32_16x16x32_bf16 v[144:147], a[136:139], v[184:187], v[144:147]\n" \
"buffer_load_dwordx4 v248, s[32:35], s89 offen offset:0 sc1 lds\n" \
"v_mfma_f32_16x16x32_bf16 v[144:147], a[140:143], v[188:191], v[144:147]\n" \
"v_exp_f32 v139, v139\n" \
"v_mfma_f32_16x16x32_bf16 v[148:151], a[156:159], v[188:191], 0\n" \
"ds_read_b128 a[212:215], v242 offset:5120\n" \
"v_cvt_pk_bf16_f32 v137, v138, v139\n" \
"v_mfma_f32_16x16x32_bf16 v[148:151], a[152:155], v[184:187], v[148:151]\n" \
"v_exp_f32 v140, v140\n" \
"v_mfma_f32_16x16x32_bf16 v[148:151], a[148:151], v[180:183], v[148:151]\n" \
"ds_read_b128 a[244:247], v242 offset:13312\n" \
"ds_read_b128 a[248:251], v242 offset:14336\n" \
"v_mfma_f32_16x16x32_bf16 v[148:151], a[144:147], v[176:179], v[148:151]\n" \
"v_exp_f32 v141, v141\n" \
"v_mfma_f32_16x16x32_bf16 v[152:155], a[160:163], v[176:179], 0\n" \
"buffer_load_dwordx4 v248, s[32:35], s89 offen offset:1024 sc1 lds\n" \
"v_mfma_f32_16x16x32_bf16 v[152:155], a[164:167], v[180:183], v[152:155]\n" \
"v_exp_f32 v142, v142\n" \
"v_mfma_f32_16x16x32_bf16 v[152:155], a[168:171], v[184:187], v[152:155]\n" \
"ds_read_b128 a[216:219], v242 offset:6144\n" \
"v_cvt_pk_bf16_f32 v138, v140, v141\n" \
"v_mfma_f32_16x16x32_bf16 v[152:155], a[172:175], v[188:191], v[152:155]\n" \
"v_exp_f32 v143, v143\n" \
"v_mfma_f32_16x16x32_bf16 v[156:159], a[188:191], v[188:191], 0\n" \
"ds_read_b128 a[128:131], v240 offset:0\n" \
"v_cvt_pk_bf16_f32 v139, v142, v143\n" \
"v_mfma_f32_16x16x32_bf16 v[156:159], a[184:187], v[184:187], v[156:159]\n" \
"ds_read_b128 a[132:135], v240 offset:1024\n" \
"ds_read_b128 a[220:223], v242 offset:7168\n" \
"v_mfma_f32_16x16x32_bf16 v[156:159], a[180:183], v[180:183], v[156:159]\n" \
"v_exp_f32 v144, v144\n" \
"v_mfma_f32_16x16x32_bf16 v[156:159], a[176:179], v[176:179], v[156:159]\n" \
"buffer_load_dwordx4 v248, s[32:35], s89 offen offset:2048 sc1 lds\n" \
"v_mfma_f32_16x16x32_bf16 a[40:43], a[48:51], v[128:131], a[40:43]\n" \
"v_exp_f32 v145, v145\n" \
"v_mfma_f32_16x16x32_bf16 a[40:43], a[48:51], v[136:139], a[40:43]\n" \
"s_waitcnt lgkmcnt(13)\n" \
"ds_read_b128 a[136:139], v240 offset:2048\n" \
"v_cvt_pk_bf16_f32 v144, v144, v145\n" \
"v_mfma_f32_16x16x32_bf16 v[64:67], a[224:227], v[136:139], v[64:67]\n" \
"v_exp_f32 v146, v146\n" \
"v_mfma_f32_16x16x32_bf16 v[64:67], a[192:195], v[128:131], v[64:67]\n" \
"ds_read_b128 a[252:255], v242 offset:15360\n" \
"ds_read_b128 a[140:143], v240 offset:3072\n" \
"v_mfma_f32_16x16x32_bf16 v[68:71], a[196:199], v[128:131], v[68:71]\n" \
"v_exp_f32 v147, v147\n" \
"v_mfma_f32_16x16x32_bf16 v[68:71], a[228:231], v[136:139], v[68:71]\n" \
"s_waitcnt lgkmcnt(12)\n" \
"buffer_load_dwordx4 v248, s[32:35], s89 offen offset:3072 sc1 lds\n" \
"v_mfma_f32_16x16x32_bf16 v[72:75], a[232:235], v[136:139], v[72:75]\n" \
"s_add_u32 s20, s20, 0x4000\n" \
"v_exp_f32 v148, v148\n" \
"v_mfma_f32_16x16x32_bf16 v[72:75], a[200:203], v[128:131], v[72:75]\n" \
"s_and_b32 s20, s20, 0xffff\n" \
"ds_read_b128 a[156:159], v240 offset:7168\n" \
"v_cvt_pk_bf16_f32 v145, v146, v147\n" \
"v_mfma_f32_16x16x32_bf16 v[76:79], a[204:207], v[128:131], v[76:79]\n" \
"s_bitset1_b32 s20, 16\n" \
"v_exp_f32 v149, v149\n" \
"v_mfma_f32_16x16x32_bf16 v[76:79], a[236:239], v[136:139], v[76:79]\n" \
"s_waitcnt lgkmcnt(9)\n" \
"ds_read_b128 a[152:155], v240 offset:6144\n" \
"v_cvt_pk_bf16_f32 v146, v148, v149\n" \
"v_mfma_f32_16x16x32_bf16 v[80:83], a[240:243], v[136:139], v[80:83]\n" \
"v_exp_f32 v150, v150\n" \
"v_mfma_f32_16x16x32_bf16 v[80:83], a[208:211], v[128:131], v[80:83]\n" \
"ds_read_b128 a[148:151], v240 offset:5120\n" \
"ds_read_b128 a[144:147], v240 offset:4096\n" \
"v_mfma_f32_16x16x32_bf16 v[84:87], a[212:215], v[128:131], v[84:87]\n" \
"s_sub_u32 s95, s95, 1\n" \
"v_exp_f32 v151, v151\n" \
"v_mfma_f32_16x16x32_bf16 v[84:87], a[244:247], v[136:139], v[84:87]\n" \
"s_waitcnt lgkmcnt(5)\n" \
"ds_read_b128 a[160:163], v240 offset:8192\n" \
"v_cvt_pk_bf16_f32 v147, v150, v151\n" \
"v_mfma_f32_16x16x32_bf16 v[88:91], a[248:251], v[136:139], v[88:91]\n" \
"ds_read_b128 a[164:167], v240 offset:9216\n" \
"ds_read_b128 a[168:171], v240 offset:10240\n" \
"v_mfma_f32_16x16x32_bf16 v[88:91], a[216:219], v[128:131], v[88:91]\n" \
"v_exp_f32 v152, v152\n" \
"v_mfma_f32_16x16x32_bf16 v[92:95], a[220:223], v[128:131], v[92:95]\n" \
"v_exp_f32 v153, v153\n" \
"v_mfma_f32_16x16x32_bf16 v[92:95], a[252:255], v[136:139], v[92:95]\n" \
"s_waitcnt lgkmcnt(7)\n" \
"ds_read_b128 a[172:175], v240 offset:11264\n" \
"s_cbranch_scc0 .Lloop2_%=\n" \
"s_lshl_b32 s95, s67, 1\n" \
"s_sub_u32 s95, 7, s95\n" \
".p2align 3\n" \
".Lloop3_%=:\n" \
".Lst1_0_%=:\n" \
"s_cbranch_execz .Lst1_4_%=\n" \
  \
"v_mfma_f32_16x16x32_bf16 v[128:131], a[128:131], v[160:163], 0\n" \
"s_add_u32 s86, s86, 0x4000\n" \
"ds_read_b128 a[188:191], v240 offset:15360\n" \
"v_cvt_pk_bf16_f32 v152, v152, v153\n" \
"v_mfma_f32_16x16x32_bf16 v[128:131], a[132:135], v[164:167], v[128:131]\n" \
"s_bitset0_b32 s86, 16\n" \
"v_exp_f32 v154, v154\n" \
"v_mfma_f32_16x16x32_bf16 v[128:131], a[136:139], v[168:171], v[128:131]\n" \
"ds_read_b128 a[184:187], v240 offset:14336\n" \
"v_add_u32 v242, 0x10000, v240\n" \
"v_mfma_f32_16x16x32_bf16 v[128:131], a[140:143], v[172:175], v[128:131]\n" \
"s_waitcnt lgkmcnt(6)\n" \
"v_exp_f32 v155, v155\n" \
"v_mfma_f32_16x16x32_bf16 v[132:135], a[156:159], v[172:175], 0\n" \
"s_mov_b32 m0, s19\n" \
"ds_read_b128 a[180:183], v240 offset:13312\n" \
"v_cvt_pk_bf16_f32 v153, v154, v155\n" \
"v_mfma_f32_16x16x32_bf16 v[132:135], a[152:155], v[168:171], v[132:135]\n" \
"ds_read_b128 a[176:179], v240 offset:12288\n" \
"v_exp_f32 v156, v156\n" \
"v_mfma_f32_16x16x32_bf16 v[132:135], a[148:151], v[164:167], v[132:135]\n" \
"buffer_load_dwordx4 v248, s[32:35], s88 offen offset:0 sc1 lds\n" \
"v_mfma_f32_16x16x32_bf16 v[132:135], a[144:147], v[160:163], v[132:135]\n" \
"s_waitcnt lgkmcnt(4)\n" \
"v_exp_f32 v157, v157\n" \
"v_mfma_f32_16x16x32_bf16 v[136:139], a[160:163], v[160:163], 0\n" \
"v_cvt_pk_bf16_f32 v154, v156, v157\n" \
"v_mfma_f32_16x16x32_bf16 v[136:139], a[164:167], v[164:167], v[136:139]\n" \
"v_exp_f32 v158, v158\n" \
"v_mfma_f32_16x16x32_bf16 v[136:139], a[168:171], v[168:171], v[136:139]\n" \
"buffer_load_dwordx4 v248, s[32:35], s88 offen offset:1024 sc1 lds\n" \
"v_mfma_f32_16x16x32_bf16 v[136:139], a[172:175], v[172:175], v[136:139]\n" \
"s_waitcnt lgkmcnt(0)\n" \
"v_exp_f32 v159, v159\n" \
"v_mfma_f32_16x16x32_bf16 v[140:143], a[188:191], v[172:175], 0\n" \
"v_add_u32 v240, s86, v248\n" \
"v_cvt_pk_bf16_f32 v155, v158, v159\n" \
"v_mfma_f32_16x16x32_bf16 v[140:143], a[184:187], v[168:171], v[140:143]\n" \
"buffer_load_dwordx4 v248, s[32:35], s88 offen offset:2048 sc1 lds\n" \
"v_mfma_f32_16x16x32_bf16 v[140:143], a[180:183], v[164:167], v[140:143]\n" \
"v_exp_f32 v128, v128\n" \
"v_mfma_f32_16x16x32_bf16 v[140:143], a[176:179], v[160:163], v[140:143]\n" \
"buffer_load_dwordx4 v248, s[32:35], s88 offen offset:3072 sc1 lds\n" \
"v_mfma_f32_16x16x32_bf16 a[44:47], a[48:51], v[144:147], a[44:47]\n" \
"s_add_u32 s89, s88, 589824\n" \
"v_exp_f32 v129, v129\n" \
"v_mfma_f32_16x16x32_bf16 a[44:47], a[48:51], v[152:155], a[44:47]\n" \
"s_add_u32 s88, s88, s27\n" \
"s_add_u32 s19, s19, 0x4000\n" \
"v_cvt_pk_bf16_f32 v128, v128, v129\n" \
"v_mfma_f32_16x16x32_bf16 v[96:99], a[224:227], v[152:155], v[96:99]\n" \
"s_bitset0_b32 s19, 16\n" \
"v_exp_f32 v130, v130\n" \
"v_mfma_f32_16x16x32_bf16 v[96:99], a[192:195], v[144:147], v[96:99]\n" \
"s_cmp_eq_u32 s88, s16\n" \
"s_cmov_b32 s88, s21\n" \
"ds_read_b128 a[224:227], v242 offset:8192\n" \
"v_mfma_f32_16x16x32_bf16 v[100:103], a[196:199], v[144:147], v[100:103]\n" \
"s_cmov_b32 s27, s91\n" \
"v_exp_f32 v131, v131\n" \
"v_mfma_f32_16x16x32_bf16 v[100:103], a[228:231], v[152:155], v[100:103]\n" \
"s_cmov_b32 s16, s77\n" \
"ds_read_b128 a[192:195], v242 offset:0\n" \
"v_cvt_pk_bf16_f32 v129, v130, v131\n" \
"v_mfma_f32_16x16x32_bf16 v[104:107], a[232:235], v[152:155], v[104:107]\n" \
"s_cmov_b32 s21, s92\n" \
"v_exp_f32 v132, v132\n" \
"v_mfma_f32_16x16x32_bf16 v[104:107], a[200:203], v[144:147], v[104:107]\n" \
"s_cmovk_i32 s91, 0x4000\n" \
"ds_read_b128 a[196:199], v242 offset:1024\n" \
"ds_read_b128 a[228:231], v242 offset:9216\n" \
"v_mfma_f32_16x16x32_bf16 v[108:111], a[204:207], v[144:147], v[108:111]\n" \
"s_cmp_eq_u32 s88, s17\n" \
"v_exp_f32 v133, v133\n" \
"v_mfma_f32_16x16x32_bf16 v[108:111], a[236:239], v[152:155], v[108:111]\n" \
"s_cmov_b32 s88, s18\n" \
"ds_read_b128 a[232:235], v242 offset:10240\n" \
"v_cvt_pk_bf16_f32 v130, v132, v133\n" \
"v_mfma_f32_16x16x32_bf16 v[112:115], a[240:243], v[152:155], v[112:115]\n" \
"v_exp_f32 v134, v134\n" \
"v_mfma_f32_16x16x32_bf16 v[112:115], a[208:211], v[144:147], v[112:115]\n" \
"ds_read_b128 a[200:203], v242 offset:2048\n" \
"v_mfma_f32_16x16x32_bf16 v[116:119], a[212:215], v[144:147], v[116:119]\n" \
"v_exp_f32 v135, v135\n" \
"v_mfma_f32_16x16x32_bf16 v[116:119], a[244:247], v[152:155], v[116:119]\n" \
"ds_read_b128 a[204:207], v242 offset:3072\n" \
"v_cvt_pk_bf16_f32 v131, v134, v135\n" \
"v_mfma_f32_16x16x32_bf16 v[120:123], a[248:251], v[152:155], v[120:123]\n" \
"ds_read_b128 a[236:239], v242 offset:11264\n" \
"v_mfma_f32_16x16x32_bf16 v[120:123], a[216:219], v[144:147], v[120:123]\n" \
"v_exp_f32 v136, v136\n" \
"v_mfma_f32_16x16x32_bf16 v[124:127], a[220:223], v[144:147], v[124:127]\n" \
"ds_read_b128 a[240:243], v242 offset:12288\n" \
"v_mfma_f32_16x16x32_bf16 v[124:127], a[252:255], v[152:155], v[124:127]\n" \
"s_waitcnt vmcnt(12)\n" \
"v_exp_f32 v137, v137\n" \
"v_mfma_f32_16x16x32_bf16 v[144:147], a[128:131], v[176:179], 0\n" \
"s_barrier\n" \
"ds_read_b128 a[208:211], v242 offset:4096\n" \
"v_cvt_pk_bf16_f32 v136, v136, v137\n" \
"v_mfma_f32_16x16x32_bf16 v[144:147], a[132:135], v[180:183], v[144:147]\n" \
"s_mov_b32 m0, s20\n" \
"v_exp_f32 v138, v138\n" \
"v_mfma_f32_16x16x32_bf16 v[144:147], a[136:139], v[184:187], v[144:147]\n" \
"buffer_load_dwordx4 v248, s[32:35], s89 offen offset:0 sc1 lds\n" \
"v_mfma_f32_16x16x32_bf16 v[144:147], a[140:143], v[188:191], v[144:147]\n" \
"v_exp_f32 v139, v139\n" \
"v_mfma_f32_16x16x32_bf16 v[148:151], a[156:159], v[188:191], 0\n" \
"ds_read_b128 a[212:215], v242 offset:5120\n" \
"v_cvt_pk_bf16_f32 v137, v138, v139\n" \
"v_mfma_f32_16x16x32_bf16 v[148:151], a[152:155], v[184:187], v[148:151]\n" \
"v_exp_f32 v140, v140\n" \
"v_mfma_f32_16x16x32_bf16 v[148:151], a[148:151], v[180:183], v[148:151]\n" \
"ds_read_b128 a[244:247], v242 offset:13312\n" \
"ds_read_b128 a[248:251], v242 offset:14336\n" \
"v_mfma_f32_16x16x32_bf16 v[148:151], a[144:147], v[176:179], v[148:151]\n" \
"v_exp_f32 v141, v141\n" \
"v_mfma_f32_16x16x32_bf16 v[152:155], a[160:163], v[176:179], 0\n" \
"buffer_load_dwordx4 v248, s[32:35], s89 offen offset:1024 sc1 lds\n" \
"v_mfma_f32_16x16x32_bf16 v[152:155], a[164:167], v[180:183], v[152:155]\n" \
"v_exp_f32 v142, v142\n" \
"v_mfma_f32_16x16x32_bf16 v[152:155], a[168:171], v[184:187], v[152:155]\n" \
"ds_read_b128 a[216:219], v242 offset:6144\n" \
"v_cvt_pk_bf16_f32 v138, v140, v141\n" \
"v_mfma_f32_16x16x32_bf16 v[152:155], a[172:175], v[188:191], v[152:155]\n" \
"v_exp_f32 v143, v143\n" \
"v_mfma_f32_16x16x32_bf16 v[156:159], a[188:191], v[188:191], 0\n" \
"ds_read_b128 a[128:131], v240 offset:0\n" \
"v_cvt_pk_bf16_f32 v139, v142, v143\n" \
"v_mfma_f32_16x16x32_bf16 v[156:159], a[184:187], v[184:187], v[156:159]\n" \
"ds_read_b128 a[132:135], v240 offset:1024\n" \
"ds_read_b128 a[220:223], v242 offset:7168\n" \
"v_mfma_f32_16x16x32_bf16 v[156:159], a[180:183], v[180:183], v[156:159]\n" \
"v_exp_f32 v144, v144\n" \
"v_mfma_f32_16x16x32_bf16 v[156:159], a[176:179], v[176:179], v[156:159]\n" \
"buffer_load_dwordx4 v248, s[32:35], s89 offen offset:2048 sc1 lds\n" \
"v_mfma_f32_16x16x32_bf16 a[40:43], a[48:51], v[128:131], a[40:43]\n" \
"v_exp_f32 v145, v145\n" \
"v_mfma_f32_16x16x32_bf16 a[40:43], a[48:51], v[136:139], a[40:43]\n" \
"s_waitcnt lgkmcnt(13)\n" \
"ds_read_b128 a[136:139], v240 offset:2048\n" \
"v_cvt_pk_bf16_f32 v144, v144, v145\n" \
"v_mfma_f32_16x16x32_bf16 v[64:67], a[224:227], v[136:139], v[64:67]\n" \
"v_exp_f32 v146, v146\n" \
"v_mfma_f32_16x16x32_bf16 v[64:67], a[192:195], v[128:131], v[64:67]\n" \
"ds_read_b128 a[252:255], v242 offset:15360\n" \
"ds_read_b128 a[140:143], v240 offset:3072\n" \
"v_mfma_f32_16x16x32_bf16 v[68:71], a[196:199], v[128:131], v[68:71]\n" \
"v_exp_f32 v147, v147\n" \
"v_mfma_f32_16x16x32_bf16 v[68:71], a[228:231], v[136:139], v[68:71]\n" \
"s_waitcnt lgkmcnt(12)\n" \
"buffer_load_dwordx4 v248, s[32:35], s89 offen offset:3072 sc1 lds\n" \
"v_mfma_f32_16x16x32_bf16 v[72:75], a[232:235], v[136:139], v[72:75]\n" \
"s_add_u32 s20, s20, 0x4000\n" \
"v_exp_f32 v148, v148\n" \
"v_mfma_f32_16x16x32_bf16 v[72:75], a[200:203], v[128:131], v[72:75]\n" \
"s_and_b32 s20, s20, 0xffff\n" \
"ds_read_b128 a[156:159], v240 offset:7168\n" \
"v_cvt_pk_bf16_f32 v145, v146, v147\n" \
"v_mfma_f32_16x16x32_bf16 v[76:79], a[204:207], v[128:131], v[76:79]\n" \
"s_bitset1_b32 s20, 16\n" \
"v_exp_f32 v149, v149\n" \
"v_mfma_f32_16x16x32_bf16 v[76:79], a[236:239], v[136:139], v[76:79]\n" \
"s_waitcnt lgkmcnt(9)\n" \
"ds_read_b128 a[152:155], v240 offset:6144\n" \
"v_cvt_pk_bf16_f32 v146, v148, v149\n" \
"v_mfma_f32_16x16x32_bf16 v[80:83], a[240:243], v[136:139], v[80:83]\n" \
"v_exp_f32 v150, v150\n" \
"v_mfma_f32_16x16x32_bf16 v[80:83], a[208:211], v[128:131], v[80:83]\n" \
"ds_read_b128 a[148:151], v240 offset:5120\n" \
"ds_read_b128 a[144:147], v240 offset:4096\n" \
"v_mfma_f32_16x16x32_bf16 v[84:87], a[212:215], v[128:131], v[84:87]\n" \
".Lst1_1_%=:\n" \
"s_cbranch_execz .Lst1_5_%=\n" \
"v_exp_f32 v151, v151\n" \
"v_mfma_f32_16x16x32_bf16 v[84:87], a[244:247], v[136:139], v[84:87]\n" \
"s_waitcnt lgkmcnt(5)\n" \
"ds_read_b128 a[160:163], v240 offset:8192\n" \
"v_cvt_pk_bf16_f32 v147, v150, v151\n" \
"v_mfma_f32_16x16x32_bf16 v[88:91], a[248:251], v[136:139], v[88:91]\n" \
"ds_read_b128 a[164:167], v240 offset:9216\n" \
"ds_read_b128 a[168:171], v240 offset:10240\n" \
"v_mfma_f32_16x16x32_bf16 v[88:91], a[216:219], v[128:131], v[88:91]\n" \
"v_exp_f32 v152, v152\n" \
"v_mfma_f32_16x16x32_bf16 v[92:95], a[220:223], v[128:131], v[92:95]\n" \
"ds_read_b128 a[172:175], v240 offset:11264\n" \
"ds_read_b128 a[188:191], v240 offset:15360\n" \
"v_mfma_f32_16x16x32_bf16 v[92:95], a[252:255], v[136:139], v[92:95]\n" \
"s_waitcnt lgkmcnt(9)\n" \
"v_exp_f32 v153, v153\n" \
"v_mfma_f32_16x16x32_bf16 v[128:131], a[128:131], v[160:163], 0\n" \
"s_add_u32 s86, s86, 0x4000\n" \
"ds_read_b128 a[184:187], v240 offset:14336\n" \
"v_cvt_pk_bf16_f32 v152, v152, v153\n" \
"v_mfma_f32_16x16x32_bf16 v[128:131], a[132:135], v[164:167], v[128:131]\n" \
"s_bitset0_b32 s86, 16\n" \
"v_exp_f32 v154, v154\n" \
"v_mfma_f32_16x16x32_bf16 v[128:131], a[136:139], v[168:171], v[128:131]\n" \
"s_cmp_eq_u32 s88, s24\n" \
"s_cbranch_scc1 .Lcht_%=\n" \
".Lsbht_%=:\n" \
"v_mfma_f32_16x16x32_bf16 v[128:131], a[140:143], v[172:175], v[128:131]\n" \
"s_waitcnt lgkmcnt(6)\n" \
"v_exp_f32 v155, v155\n" \
"v_mfma_f32_16x16x32_bf16 v[132:135], a[156:159], v[172:175], 0\n" \
"s_mov_b32 m0, s19\n" \
"v_add_u32 v242, 0x10000, v240\n" \
"v_cvt_pk_bf16_f32 v153, v154, v155\n" \
"v_mfma_f32_16x16x32_bf16 v[132:135], a[152:155], v[168:171], v[132:135]\n" \
"s_cmp_lg_u32 s76, 0\n" \
"ds_read_b128 a[180:183], v240 offset:13312\n" \
"ds_read_b128 a[176:179], v240 offset:12288\n" \
"v_exp_f32 v156, v156\n" \
"v_mfma_f32_16x16x32_bf16 v[132:135], a[148:151], v[164:167], v[132:135]\n" \
"buffer_load_dwordx4 v248, s[32:35], s88 offen offset:0 sc1 lds\n" \
"v_mfma_f32_16x16x32_bf16 v[132:135], a[144:147], v[160:163], v[132:135]\n" \
"s_waitcnt lgkmcnt(4)\n" \
"v_exp_f32 v157, v157\n" \
"v_mfma_f32_16x16x32_bf16 v[136:139], a[160:163], v[160:163], 0\n" \
"s_cbranch_scc1 .Lant_%=\n" \
".Labt_%=:\n" \
"v_cvt_pk_bf16_f32 v154, v156, v157\n" \
"v_mfma_f32_16x16x32_bf16 v[136:139], a[164:167], v[164:167], v[136:139]\n" \
"v_exp_f32 v158, v158\n" \
"v_mfma_f32_16x16x32_bf16 v[136:139], a[168:171], v[168:171], v[136:139]\n" \
"buffer_load_dwordx4 v248, s[32:35], s88 offen offset:1024 sc1 lds\n" \
"v_mfma_f32_16x16x32_bf16 v[136:139], a[172:175], v[172:175], v[136:139]\n" \
"s_waitcnt lgkmcnt(0)\n" \
"v_exp_f32 v159, v159\n" \
"v_mfma_f32_16x16x32_bf16 v[140:143], a[188:191], v[172:175], 0\n" \
"v_add_u32 v240, s86, v248\n" \
"v_cvt_pk_bf16_f32 v155, v158, v159\n" \
"v_mfma_f32_16x16x32_bf16 v[140:143], a[184:187], v[168:171], v[140:143]\n" \
"buffer_load_dwordx4 v248, s[32:35], s88 offen offset:2048 sc1 lds\n" \
"v_mfma_f32_16x16x32_bf16 v[140:143], a[180:183], v[164:167], v[140:143]\n" \
"v_exp_f32 v128, v128\n" \
"v_mfma_f32_16x16x32_bf16 v[140:143], a[176:179], v[160:163], v[140:143]\n" \
"buffer_load_dwordx4 v248, s[32:35], s88 offen offset:3072 sc1 lds\n" \
"v_mfma_f32_16x16x32_bf16 a[44:47], a[48:51], v[144:147], a[44:47]\n" \
"s_add_u32 s89, s88, 589824\n" \
"v_exp_f32 v129, v129\n" \
"v_mfma_f32_16x16x32_bf16 a[44:47], a[48:51], v[152:155], a[44:47]\n" \
"s_add_u32 s88, s88, s27\n" \
"s_add_u32 s19, s19, 0x4000\n" \
"v_cvt_pk_bf16_f32 v128, v128, v129\n" \
"v_mfma_f32_16x16x32_bf16 v[96:99], a[224:227], v[152:155], v[96:99]\n" \
"s_bitset0_b32 s19, 16\n" \
"v_exp_f32 v130, v130\n" \
"v_mfma_f32_16x16x32_bf16 v[96:99], a[192:195], v[144:147], v[96:99]\n" \
"s_cmp_eq_u32 s88, s16\n" \
"s_cmov_b32 s88, s21\n" \
"ds_read_b128 a[224:227], v242 offset:8192\n" \
"v_mfma_f32_16x16x32_bf16 v[100:103], a[196:199], v[144:147], v[100:103]\n" \
"s_cmov_b32 s27, s91\n" \
"v_exp_f32 v131, v131\n" \
"v_mfma_f32_16x16x32_bf16 v[100:103], a[228:231], v[152:155], v[100:103]\n" \
"s_cmov_b32 s16, s77\n" \
"ds_read_b128 a[192:195], v242 offset:0\n" \
"v_cvt_pk_bf16_f32 v129, v130, v131\n" \
"v_mfma_f32_16x16x32_bf16 v[104:107], a[232:235], v[152:155], v[104:107]\n" \
"s_cmov_b32 s21, s92\n" \
"v_exp_f32 v132, v132\n" \
"v_mfma_f32_16x16x32_bf16 v[104:107], a[200:203], v[144:147], v[104:107]\n" \
"s_cmovk_i32 s91, 0x4000\n" \
"ds_read_b128 a[196:199], v242 offset:1024\n" \
"ds_read_b128 a[228:231], v242 offset:9216\n" \
"v_mfma_f32_16x16x32_bf16 v[108:111], a[204:207], v[144:147], v[108:111]\n" \
"s_cmp_eq_u32 s88, s17\n" \
"v_exp_f32 v133, v133\n" \
"v_mfma_f32_16x16x32_bf16 v[108:111], a[236:239], v[152:155], v[108:111]\n" \
"s_cmov_b32 s88, s18\n" \
"ds_read_b128 a[232:235], v242 offset:10240\n" \
"v_cvt_pk_bf16_f32 v130, v132, v133\n" \
"v_mfma_f32_16x16x32_bf16 v[112:115], a[240:243], v[152:155], v[112:115]\n" \
"v_exp_f32 v134, v134\n" \
"v_mfma_f32_16x16x32_bf16 v[112:115], a[208:211], v[144:147], v[112:115]\n" \
"ds_read_b128 a[200:203], v242 offset:2048\n" \
"v_mfma_f32_16x16x32_bf16 v[116:119], a[212:215], v[144:147], v[116:119]\n" \
"v_exp_f32 v135, v135\n" \
"v_mfma_f32_16x16x32_bf16 v[116:119], a[244:247], v[152:155], v[116:119]\n" \
"ds_read_b128 a[204:207], v242 offset:3072\n" \
"v_cvt_pk_bf16_f32 v131, v134, v135\n" \
"v_mfma_f32_16x16x32_bf16 v[120:123], a[248:251], v[152:155], v[120:123]\n" \
"ds_read_b128 a[236:239], v242 offset:11264\n" \
"v_mfma_f32_16x16x32_bf16 v[120:123], a[216:219], v[144:147], v[120:123]\n" \
"v_exp_f32 v136, v136\n" \
"v_mfma_f32_16x16x32_bf16 v[124:127], a[220:223], v[144:147], v[124:127]\n" \
"ds_read_b128 a[240:243], v242 offset:12288\n" \
"v_mfma_f32_16x16x32_bf16 v[124:127], a[252:255], v[152:155], v[124:127]\n" \
"s_waitcnt vmcnt(12)\n" \
"v_exp_f32 v137, v137\n" \
"v_mfma_f32_16x16x32_bf16 v[144:147], a[128:131], v[176:179], 0\n" \
"s_barrier\n" \
"ds_read_b128 a[208:211], v242 offset:4096\n" \
"v_cvt_pk_bf16_f32 v136, v136, v137\n" \
"v_mfma_f32_16x16x32_bf16 v[144:147], a[132:135], v[180:183], v[144:147]\n" \
"s_mov_b32 m0, s20\n" \
"v_exp_f32 v138, v138\n" \
"v_mfma_f32_16x16x32_bf16 v[144:147], a[136:139], v[184:187], v[144:147]\n" \
"buffer_load_dwordx4 v248, s[32:35], s89 offen offset:0 sc1 lds\n" \
"v_mfma_f32_16x16x32_bf16 v[144:147], a[140:143], v[188:191], v[144:147]\n" \
"v_exp_f32 v139, v139\n" \
"v_mfma_f32_16x16x32_bf16 v[148:151], a[156:159], v[188:191], 0\n" \
"ds_read_b128 a[212:215], v242 offset:5120\n" \
"v_cvt_pk_bf16_f32 v137, v138, v139\n" \
"v_mfma_f32_16x16x32_bf16 v[148:151], a[152:155], v[184:187], v[148:151]\n" \
"v_exp_f32 v140, v140\n" \
"v_mfma_f32_16x16x32_bf16 v[148:151], a[148:151], v[180:183], v[148:151]\n" \
"ds_read_b128 a[244:247], v242 offset:13312\n" \
"ds_read_b128 a[248:251], v242 offset:14336\n" \
"v_mfma_f32_16x16x32_bf16 v[148:151], a[144:147], v[176:179], v[148:151]\n" \
"v_exp_f32 v141, v141\n" \
"v_mfma_f32_16x16x32_bf16 v[152:155], a[160:163], v[176:179], 0\n" \
"buffer_load_dwordx4 v248, s[32:35], s89 offen offset:1024 sc1 lds\n" \
"v_mfma_f32_16x16x32_bf16 v[152:155], a[164:167], v[180:183], v[152:155]\n" \
"v_exp_f32 v142, v142\n" \
"v_mfma_f32_16x16x32_bf16 v[152:155], a[168:171], v[184:187], v[152:155]\n" \
"ds_read_b128 a[216:219], v242 offset:6144\n" \
"v_cvt_pk_bf16_f32 v138, v140, v141\n" \
"v_mfma_f32_16x16x32_bf16 v[152:155], a[172:175], v[188:191], v[152:155]\n" \
"v_exp_f32 v143, v143\n" \
"v_mfma_f32_16x16x32_bf16 v[156:159], a[188:191], v[188:191], 0\n" \
"ds_read_b128 a[128:131], v240 offset:0\n" \
"v_cvt_pk_bf16_f32 v139, v142, v143\n" \
"v_mfma_f32_16x16x32_bf16 v[156:159], a[184:187], v[184:187], v[156:159]\n" \
"ds_read_b128 a[132:135], v240 offset:1024\n" \
"ds_read_b128 a[220:223], v242 offset:7168\n" \
"v_mfma_f32_16x16x32_bf16 v[156:159], a[180:183], v[180:183], v[156:159]\n" \
"v_exp_f32 v144, v144\n" \
"v_mfma_f32_16x16x32_bf16 v[156:159], a[176:179], v[176:179], v[156:159]\n" \
"buffer_load_dwordx4 v248, s[32:35], s89 offen offset:2048 sc1 lds\n" \
"v_mfma_f32_16x16x32_bf16 a[40:43], a[48:51], v[128:131], a[40:43]\n" \
"v_exp_f32 v145, v145\n" \
"v_mfma_f32_16x16x32_bf16 a[40:43], a[48:51], v[136:139], a[40:43]\n" \
"s_waitcnt lgkmcnt(13)\n" \
"ds_read_b128 a[136:139], v240 offset:2048\n" \
"v_cvt_pk_bf16_f32 v144, v144, v145\n" \
"v_mfma_f32_16x16x32_bf16 v[64:67], a[224:227], v[136:139], v[64:67]\n" \
"v_exp_f32 v146, v146\n" \
"v_mfma_f32_16x16x32_bf16 v[64:67], a[192:195], v[128:131], v[64:67]\n" \
"ds_read_b128 a[252:255], v242 offset:15360\n" \
"ds_read_b128 a[140:143], v240 offset:3072\n" \
"v_mfma_f32_16x16x32_bf16 v[68:71], a[196:199], v[128:131], v[68:71]\n" \
"v_exp_f32 v147, v147\n" \
"v_mfma_f32_16x16x32_bf16 v[68:71], a[228:231], v[136:139], v[68:71]\n" \
"s_waitcnt lgkmcnt(12)\n" \
"buffer_load_dwordx4 v248, s[32:35], s89 offen offset:3072 sc1 lds\n" \
"v_mfma_f32_16x16x32_bf16 v[72:75], a[232:235], v[136:139], v[72:75]\n" \
"s_add_u32 s20, s20, 0x4000\n" \
"v_exp_f32 v148, v148\n" \
"v_mfma_f32_16x16x32_bf16 v[72:75], a[200:203], v[128:131], v[72:75]\n" \
"s_and_b32 s20, s20, 0xffff\n" \
"ds_read_b128 a[156:159], v240 offset:7168\n" \
"v_cvt_pk_bf16_f32 v145, v146, v147\n" \
"v_mfma_f32_16x16x32_bf16 v[76:79], a[204:207], v[128:131], v[76:79]\n" \
"s_bitset1_b32 s20, 16\n" \
"v_exp_f32 v149, v149\n" \
"v_mfma_f32_16x16x32_bf16 v[76:79], a[236:239], v[136:139], v[76:79]\n" \
"s_waitcnt lgkmcnt(9)\n" \
"ds_read_b128 a[152:155], v240 offset:6144\n" \
"v_cvt_pk_bf16_f32 v146, v148, v149\n" \
"v_mfma_f32_16x16x32_bf16 v[80:83], a[240:243], v[136:139], v[80:83]\n" \
"v_exp_f32 v150, v150\n" \
"v_mfma_f32_16x16x32_bf16 v[80:83], a[208:211], v[128:131], v[80:83]\n" \
"ds_read_b128 a[148:151], v240 offset:5120\n" \
"ds_read_b128 a[144:147], v240 offset:4096\n" \
"v_mfma_f32_16x16x32_bf16 v[84:87], a[212:215], v[128:131], v[84:87]\n" \
"s_sub_u32 s95, s95, 1\n" \
"v_exp_f32 v151, v151\n" \
"v_mfma_f32_16x16x32_bf16 v[84:87], a[244:247], v[136:139], v[84:87]\n" \
"s_waitcnt lgkmcnt(5)\n" \
"ds_read_b128 a[160:163], v240 offset:8192\n" \
"v_cvt_pk_bf16_f32 v147, v150, v151\n" \
"v_mfma_f32_16x16x32_bf16 v[88:91], a[248:251], v[136:139], v[88:91]\n" \
"ds_read_b128 a[164:167], v240 offset:9216\n" \
"ds_read_b128 a[168:171], v240 offset:10240\n" \
"v_mfma_f32_16x16x32_bf16 v[88:91], a[216:219], v[128:131], v[88:91]\n" \
"v_exp_f32 v152, v152\n" \
"v_mfma_f32_16x16x32_bf16 v[92:95], a[220:223], v[128:131], v[92:95]\n" \
"v_exp_f32 v153, v153\n" \
"v_mfma_f32_16x16x32_bf16 v[92:95], a[252:255], v[136:139], v[92:95]\n" \
"s_waitcnt lgkmcnt(7)\n" \
"ds_read_b128 a[172:175], v240 offset:11264\n" \
"s_cbranch_scc0 .Lloop3_%=\n" \
".Lst1_2_%=:\n" \
"s_cbranch_execz .Lst1_6_%=\n" \
  \
"v_mfma_f32_16x16x32_bf16 v[128:131], a[128:131], v[160:163], 0\n" \
"s_add_u32 s86, s86, 0x4000\n" \
"v_and_b32 v255, 3, v251\n" \
"v_lshlrev_b32 v255, 2, v255\n" \
"v_mfma_f32_16x16x32_bf16 v[128:131], a[132:135], v[164:167], v[128:131]\n" \
"s_bitset0_b32 s86, 16\n" \
"v_exp_f32 v154, v154\n" \
"v_mfma_f32_16x16x32_bf16 v[128:131], a[136:139], v[168:171], v[128:131]\n" \
"buffer_load_dword v247, v255, s[32:35], s31 offen sc1\n" \
"v_add_u32 v242, 0x10000, v240\n" \
"v_mfma_f32_16x16x32_bf16 v[128:131], a[140:143], v[172:175], v[128:131]\n" \
"s_waitcnt lgkmcnt(4)\n" \
"v_exp_f32 v155, v155\n" \
"v_mfma_f32_16x16x32_bf16 v[132:135], a[156:159], v[172:175], 0\n" \
"s_mov_b32 m0, s19\n" \
"v_cvt_pk_bf16_f32 v152, v152, v153\n" \
"v_cvt_pk_bf16_f32 v153, v154, v155\n" \
"v_mfma_f32_16x16x32_bf16 v[132:135], a[152:155], v[168:171], v[132:135]\n" \
"ds_read_b128 a[188:191], v240 offset:15360\n" \
"ds_read_b128 a[184:187], v240 offset:14336\n" \
"ds_read_b128 a[180:183], v240 offset:13312\n" \
"ds_read_b128 a[176:179], v240 offset:12288\n" \
"v_exp_f32 v156, v156\n" \
"v_mfma_f32_16x16x32_bf16 v[132:135], a[148:151], v[164:167], v[132:135]\n" \
"buffer_load_dwordx4 v248, s[32:35], s88 offen offset:0 sc1 lds\n" \
"v_mfma_f32_16x16x32_bf16 v[132:135], a[144:147], v[160:163], v[132:135]\n" \
"s_waitcnt lgkmcnt(4)\n" \
"v_exp_f32 v157, v157\n" \
"v_mfma_f32_16x16x32_bf16 v[136:139], a[160:163], v[160:163], 0\n" \
"v_cvt_pk_bf16_f32 v154, v156, v157\n" \
"v_mfma_f32_16x16x32_bf16 v[136:139], a[164:167], v[164:167], v[136:139]\n" \
"v_exp_f32 v158, v158\n" \
"v_mfma_f32_16x16x32_bf16 v[136:139], a[168:171], v[168:171], v[136:139]\n" \
"buffer_load_dwordx4 v248, s[32:35], s88 offen offset:1024 sc1 lds\n" \
"v_mfma_f32_16x16x32_bf16 v[136:139], a[172:175], v[172:175], v[136:139]\n" \
"s_waitcnt lgkmcnt(0)\n" \
"v_exp_f32 v159, v159\n" \
"v_mfma_f32_16x16x32_bf16 v[140:143], a[188:191], v[172:175], 0\n" \
"v_add_u32 v240, s86, v248\n" \
"v_cvt_pk_bf16_f32 v155, v158, v159\n" \
"v_mfma_f32_16x16x32_bf16 v[140:143], a[184:187], v[168:171], v[140:143]\n" \
"buffer_load_dwordx4 v248, s[32:35], s88 offen offset:2048 sc1 lds\n" \
"v_mfma_f32_16x16x32_bf16 v[140:143], a[180:183], v[164:167], v[140:143]\n" \
"v_exp_f32 v128, v128\n" \
"v_mfma_f32_16x16x32_bf16 v[140:143], a[176:179], v[160:163], v[140:143]\n" \
"buffer_load_dwordx4 v248, s[32:35], s88 offen offset:3072 sc1 lds\n" \
"v_mfma_f32_16x16x32_bf16 a[44:47], a[48:51], v[144:147], a[44:47]\n" \
"s_add_u32 s89, s88, 589824\n" \
"v_exp_f32 v129, v129\n" \
"v_mfma_f32_16x16x32_bf16 a[44:47], a[48:51], v[152:155], a[44:47]\n" \
"s_add_u32 s88, s88, s27\n" \
"s_add_u32 s19, s19, 0x4000\n" \
"v_cvt_pk_bf16_f32 v128, v128, v129\n" \
"v_mfma_f32_16x16x32_bf16 v[96:99], a[224:227], v[152:155], v[96:99]\n" \
"s_bitset0_b32 s19, 16\n" \
"v_exp_f32 v130, v130\n" \
"v_mfma_f32_16x16x32_bf16 v[96:99], a[192:195], v[144:147], v[96:99]\n" \
"s_cmp_eq_u32 s88, s16\n" \
"s_cmov_b32 s88, s21\n" \
"ds_read_b128 a[224:227], v242 offset:8192\n" \
"v_mfma_f32_16x16x32_bf16 v[100:103], a[196:199], v[144:147], v[100:103]\n" \
"s_cmov_b32 s27, s91\n" \
"v_exp_f32 v131, v131\n" \
"v_mfma_f32_16x16x32_bf16 v[100:103], a[228:231], v[152:155], v[100:103]\n" \
"s_cmov_b32 s16, s77\n" \
"ds_read_b128 a[192:195], v242 offset:0\n" \
"v_cvt_pk_bf16_f32 v129, v130, v131\n" \
"v_mfma_f32_16x16x32_bf16 v[104:107], a[232:235], v[152:155], v[104:107]\n" \
"s_cmov_b32 s21, s92\n" \
"v_exp_f32 v132, v132\n" \
"v_mfma_f32_16x16x32_bf16 v[104:107], a[200:203], v[144:147], v[104:107]\n" \
"s_cmovk_i32 s91, 0x4000\n" \
"ds_read_b128 a[196:199], v242 offset:1024\n" \
"ds_read_b128 a[228:231], v242 offset:9216\n" \
"v_mfma_f32_16x16x32_bf16 v[108:111], a[204:207], v[144:147], v[108:111]\n" \
"s_cmp_eq_u32 s88, s17\n" \
"v_exp_f32 v133, v133\n" \
"v_mfma_f32_16x16x32_bf16 v[108:111], a[236:239], v[152:155], v[108:111]\n" \
"s_cmov_b32 s88, s18\n" \
"ds_read_b128 a[232:235], v242 offset:10240\n" \
"v_cvt_pk_bf16_f32 v130, v132, v133\n" \
"v_mfma_f32_16x16x32_bf16 v[112:115], a[240:243], v[152:155], v[112:115]\n" \
"v_exp_f32 v134, v134\n" \
"v_mfma_f32_16x16x32_bf16 v[112:115], a[208:211], v[144:147], v[112:115]\n" \
"ds_read_b128 a[200:203], v242 offset:2048\n" \
"v_mfma_f32_16x16x32_bf16 v[116:119], a[212:215], v[144:147], v[116:119]\n" \
"v_exp_f32 v135, v135\n" \
"v_mfma_f32_16x16x32_bf16 v[116:119], a[244:247], v[152:155], v[116:119]\n" \
"ds_read_b128 a[204:207], v242 offset:3072\n" \
"v_cvt_pk_bf16_f32 v131, v134, v135\n" \
"v_mfma_f32_16x16x32_bf16 v[120:123], a[248:251], v[152:155], v[120:123]\n" \
"s_waitcnt vmcnt(4)\n" \
"ds_read_b128 a[236:239], v242 offset:11264\n" \
"v_cmp_eq_u32 vcc, 0, v247\n" \
"v_mfma_f32_16x16x32_bf16 v[120:123], a[216:219], v[144:147], v[120:123]\n" \
"v_exp_f32 v136, v136\n" \
"v_mfma_f32_16x16x32_bf16 v[124:127], a[220:223], v[144:147], v[124:127]\n" \
"ds_read_b128 a[240:243], v242 offset:12288\n" \
"v_mfma_f32_16x16x32_bf16 v[124:127], a[252:255], v[152:155], v[124:127]\n" \
"s_waitcnt vmcnt(13)\n" \
"v_exp_f32 v137, v137\n" \
"v_mfma_f32_16x16x32_bf16 v[144:147], a[128:131], v[176:179], 0\n" \
"s_barrier\n" \
"s_cbranch_vccnz .Lspl_%=\n" \
".Lsbl_%=:\n" \
"v_mfma_f32_16x16x32_bf16 v[144:147], a[132:135], v[180:183], v[144:147]\n" \
"s_mov_b32 m0, s20\n" \
"v_exp_f32 v138, v138\n" \
"v_mfma_f32_16x16x32_bf16 v[144:147], a[136:139], v[184:187], v[144:147]\n" \
"s_add_u32 s38, s66, 1\n" \
"buffer_load_dwordx4 v248, s[32:35], s89 offen offset:0 sc1 lds\n" \
"v_mfma_f32_16x16x32_bf16 v[144:147], a[140:143], v[188:191], v[144:147]\n" \
"s_and_b32 s38, s38, 3\n" \
"v_exp_f32 v139, v139\n" \
"v_mfma_f32_16x16x32_bf16 v[148:151], a[156:159], v[188:191], 0\n" \
"s_lshl_b32 s38, s38, 13\n" \
"v_cvt_pk_bf16_f32 v136, v136, v137\n" \
"v_cvt_pk_bf16_f32 v137, v138, v139\n" \
"v_mfma_f32_16x16x32_bf16 v[148:151], a[152:155], v[184:187], v[148:151]\n" \
"s_add_u32 s38, s38, s29\n" \
"v_exp_f32 v140, v140\n" \
"v_mfma_f32_16x16x32_bf16 v[148:151], a[148:151], v[180:183], v[148:151]\n" \
"buffer_load_dwordx4 v[32:35], v248, s[32:35], s38 offen offset:0 sc1\n" \
"v_mfma_f32_16x16x32_bf16 v[148:151], a[144:147], v[176:179], v[148:151]\n" \
"v_exp_f32 v141, v141\n" \
"v_mfma_f32_16x16x32_bf16 v[152:155], a[160:163], v[176:179], 0\n" \
"buffer_load_dwordx4 v248, s[32:35], s89 offen offset:1024 sc1 lds\n" \
"v_mfma_f32_16x16x32_bf16 v[152:155], a[164:167], v[180:183], v[152:155]\n" \
"v_exp_f32 v142, v142\n" \
"v_mfma_f32_16x16x32_bf16 v[152:155], a[168:171], v[184:187], v[152:155]\n" \
"ds_read_b128 a[208:211], v242 offset:4096\n" \
"v_cvt_pk_bf16_f32 v138, v140, v141\n" \
"v_mfma_f32_16x16x32_bf16 v[152:155], a[172:175], v[188:191], v[152:155]\n" \
"v_exp_f32 v143, v143\n" \
"v_mfma_f32_16x16x32_bf16 v[156:159], a[188:191], v[188:191], 0\n" \
"buffer_load_dwordx4 v[36:39], v248, s[32:35], s38 offen offset:1024 sc1\n" \
"v_cvt_pk_bf16_f32 v139, v142, v143\n" \
"v_mfma_f32_16x16x32_bf16 v[156:159], a[184:187], v[184:187], v[156:159]\n" \
"ds_read_b128 a[212:215], v242 offset:5120\n" \
"ds_read_b128 a[244:247], v242 offset:13312\n" \
"v_mfma_f32_16x16x32_bf16 v[156:159], a[180:183], v[180:183], v[156:159]\n" \
"v_exp_f32 v144, v144\n" \
"v_mfma_f32_16x16x32_bf16 v[156:159], a[176:179], v[176:179], v[156:159]\n" \
"buffer_load_dwordx4 v248, s[32:35], s89 offen offset:2048 sc1 lds\n" \
"v_mfma_f32_16x16x32_bf16 a[40:43], a[48:51], v[128:131], a[40:43]\n" \
"v_exp_f32 v145, v145\n" \
"v_mfma_f32_16x16x32_bf16 a[40:43], a[48:51], v[136:139], a[40:43]\n" \
"s_waitcnt lgkmcnt(8)\n" \
"ds_read_b128 a[248:251], v242 offset:14336\n" \
"v_cvt_pk_bf16_f32 v144, v144, v145\n" \
"v_mfma_f32_16x16x32_bf16 v[64:67], a[224:227], v[136:139], v[64:67]\n" \
"v_exp_f32 v146, v146\n" \
"v_mfma_f32_16x16x32_bf16 v[64:67], a[192:195], v[128:131], v[64:67]\n" \
"ds_read_b128 a[216:219], v242 offset:6144\n" \
"ds_read_b128 a[128:131], v240 offset:0\n" \
"v_mfma_f32_16x16x32_bf16 v[68:71], a[196:199], v[128:131], v[68:71]\n" \
"v_exp_f32 v147, v147\n" \
"v_mfma_f32_16x16x32_bf16 v[68:71], a[228:231], v[136:139], v[68:71]\n" \
"s_waitcnt lgkmcnt(7)\n" \
"buffer_load_dwordx4 v248, s[32:35], s89 offen offset:3072 sc1 lds\n" \
"v_mfma_f32_16x16x32_bf16 v[72:75], a[232:235], v[136:139], v[72:75]\n" \
"s_add_u32 s20, s20, 0x4000\n" \
"v_exp_f32 v148, v148\n" \
"v_mfma_f32_16x16x32_bf16 v[72:75], a[200:203], v[128:131], v[72:75]\n" \
"s_and_b32 s20, s20, 0xffff\n" \
"buffer_load_dwordx4 v[40:43], v248, s[32:35], s38 offen offset:2048 sc1\n" \
"v_cvt_pk_bf16_f32 v145, v146, v147\n" \
"v_mfma_f32_16x16x32_bf16 v[76:79], a[204:207], v[128:131], v[76:79]\n" \
"s_bitset1_b32 s20, 16\n" \
"v_exp_f32 v149, v149\n" \
"v_mfma_f32_16x16x32_bf16 v[76:79], a[236:239], v[136:139], v[76:79]\n" \
"s_waitcnt lgkmcnt(3)\n" \
"ds_read_b128 a[132:135], v240 offset:1024\n" \
"ds_read_b128 a[220:223], v242 offset:7168\n" \
"v_cvt_pk_bf16_f32 v146, v148, v149\n" \
"v_mfma_f32_16x16x32_bf16 v[80:83], a[240:243], v[136:139], v[80:83]\n" \
"v_exp_f32 v150, v150\n" \
"v_mfma_f32_16x16x32_bf16 v[80:83], a[208:211], v[128:131], v[80:83]\n" \
"ds_read_b128 a[136:139], v240 offset:2048\n" \
"ds_read_b128 a[252:255], v242 offset:15360\n" \
"ds_read_b128 a[140:143], v240 offset:3072\n" \
"v_mfma_f32_16x16x32_bf16 v[84:87], a[212:215], v[128:131], v[84:87]\n" \
"v_exp_f32 v151, v151\n" \
"v_mfma_f32_16x16x32_bf16 v[84:87], a[244:247], v[136:139], v[84:87]\n" \
"s_waitcnt lgkmcnt(1)\n" \
"buffer_load_dwordx4 v[44:47], v248, s[32:35], s38 offen offset:3072 sc1\n" \
"v_cvt_pk_bf16_f32 v147, v150, v151\n" \
"v_mfma_f32_16x16x32_bf16 v[88:91], a[248:251], v[136:139], v[88:91]\n" \
"ds_read_b128 a[156:159], v240 offset:7168\n" \
"ds_read_b128 a[152:155], v240 offset:6144\n" \
"v_mfma_f32_16x16x32_bf16 v[88:91], a[216:219], v[128:131], v[88:91]\n" \
"ds_read_b128 a[148:151], v240 offset:5120\n" \
"ds_read_b128 a[144:147], v240 offset:4096\n" \
"v_exp_f32 v152, v152\n" \
"v_mfma_f32_16x16x32_bf16 v[92:95], a[220:223], v[128:131], v[92:95]\n" \
"ds_read_b128 a[160:163], v240 offset:8192\n" \
"ds_read_b128 a[164:167], v240 offset:9216\n" \
"v_mfma_f32_16x16x32_bf16 v[92:95], a[252:255], v[136:139], v[92:95]\n" \
"s_waitcnt lgkmcnt(6)\n" \
"v_exp_f32 v153, v153\n" \
"v_mfma_f32_16x16x32_bf16 v[128:131], a[128:131], v[160:163], 0\n" \
"s_add_u32 s86, s86, 0x4000\n" \
"ds_read_b128 a[168:171], v240 offset:10240\n" \
"v_cvt_pk_bf16_f32 v152, v152, v153\n" \
"v_mfma_f32_16x16x32_bf16 v[128:131], a[132:135], v[164:167], v[128:131]\n" \
"s_bitset0_b32 s86, 16\n" \
"ds_read_b128 a[172:175], v240 offset:11264\n" \
"v_exp_f32 v154, v154\n" \
"v_mfma_f32_16x16x32_bf16 v[128:131], a[136:139], v[168:171], v[128:131]\n" \
"s_add_u32 s38, s38, 4096\n" \
"buffer_load_dwordx4 v[48:51], v248, s[32:35], s38 offen offset:0 sc1\n" \
"v_add_u32 v242, 0x10000, v240\n" \
"v_mfma_f32_16x16x32_bf16 v[128:131], a[140:143], v[172:175], v[128:131]\n" \
"s_waitcnt lgkmcnt(4)\n" \
"v_exp_f32 v155, v155\n" \
"v_mfma_f32_16x16x32_bf16 v[132:135], a[156:159], v[172:175], 0\n" \
"s_mov_b32 m0, s19\n" \
"ds_read_b128 a[188:191], v240 offset:15360\n" \
"v_cvt_pk_bf16_f32 v153, v154, v155\n" \
"v_mfma_f32_16x16x32_bf16 v[132:135], a[152:155], v[168:171], v[132:135]\n" \
"ds_read_b128 a[184:187], v240 offset:14336\n" \
"ds_read_b128 a[180:183], v240 offset:13312\n" \
"ds_read_b128 a[176:179], v240 offset:12288\n" \
"v_exp_f32 v156, v156\n" \
"v_mfma_f32_16x16x32_bf16 v[132:135], a[148:151], v[164:167], v[132:135]\n" \
"buffer_load_dwordx4 v248, s[32:35], s88 offen offset:0 sc1 lds\n" \
"v_mfma_f32_16x16x32_bf16 v[132:135], a[144:147], v[160:163], v[132:135]\n" \
"s_waitcnt lgkmcnt(4)\n" \
"v_exp_f32 v157, v157\n" \
"v_mfma_f32_16x16x32_bf16 v[136:139], a[160:163], v[160:163], 0\n" \
"v_cvt_pk_bf16_f32 v154, v156, v157\n" \
"v_mfma_f32_16x16x32_bf16 v[136:139], a[164:167], v[164:167], v[136:139]\n" \
"v_exp_f32 v158, v158\n" \
"v_mfma_f32_16x16x32_bf16 v[136:139], a[168:171], v[168:171], v[136:139]\n" \
"buffer_load_dwordx4 v248, s[32:35], s88 offen offset:1024 sc1 lds\n" \
"v_mfma_f32_16x16x32_bf16 v[136:139], a[172:175], v[172:175], v[136:139]\n" \
"s_waitcnt lgkmcnt(0)\n" \
"v_exp_f32 v159, v159\n" \
"v_mfma_f32_16x16x32_bf16 v[140:143], a[188:191], v[172:175], 0\n" \
"buffer_load_dwordx4 v[52:55], v248, s[32:35], s38 offen offset:1024 sc1\n" \
"v_add_u32 v240, s86, v248\n" \
"v_mfma_f32_16x16x32_bf16 v[140:143], a[184:187], v[168:171], v[140:143]\n" \
"buffer_load_dwordx4 v248, s[32:35], s88 offen offset:2048 sc1 lds\n" \
"v_mfma_f32_16x16x32_bf16 v[140:143], a[180:183], v[164:167], v[140:143]\n" \
"v_exp_f32 v128, v128\n" \
"v_mfma_f32_16x16x32_bf16 v[140:143], a[176:179], v[160:163], v[140:143]\n" \
"buffer_load_dwordx4 v248, s[32:35], s88 offen offset:3072 sc1 lds\n" \
"v_cvt_pk_bf16_f32 v155, v158, v159\n" \
"v_mfma_f32_16x16x32_bf16 a[44:47], a[48:51], v[144:147], a[44:47]\n" \
"s_add_u32 s89, s88, 589824\n" \
"v_exp_f32 v129, v129\n" \
"v_mfma_f32_16x16x32_bf16 a[44:47], a[48:51], v[152:155], a[44:47]\n" \
"s_add_u32 s88, s88, s27\n" \
"s_add_u32 s19, s19, 0x4000\n" \
"v_cvt_pk_bf16_f32 v128, v128, v129\n" \
"v_mfma_f32_16x16x32_bf16 v[96:99], a[224:227], v[152:155], v[96:99]\n" \
"s_bitset0_b32 s19, 16\n" \
"v_exp_f32 v130, v130\n" \
"v_mfma_f32_16x16x32_bf16 v[96:99], a[192:195], v[144:147], v[96:99]\n" \
"s_cmp_eq_u32 s88, s16\n" \
"s_cmov_b32 s88, s21\n" \
"buffer_load_dwordx4 v[56:59], v248, s[32:35], s38 offen offset:2048 sc1\n" \
"v_mfma_f32_16x16x32_bf16 v[100:103], a[196:199], v[144:147], v[100:103]\n" \
"s_cmov_b32 s27, s91\n" \
"v_exp_f32 v131, v131\n" \
"v_mfma_f32_16x16x32_bf16 v[100:103], a[228:231], v[152:155], v[100:103]\n" \
"s_cmov_b32 s16, s77\n" \
"ds_read_b128 a[224:227], v242 offset:8192\n" \
"v_cvt_pk_bf16_f32 v129, v130, v131\n" \
"v_mfma_f32_16x16x32_bf16 v[104:107], a[232:235], v[152:155], v[104:107]\n" \
"s_cmov_b32 s21, s92\n" \
"v_exp_f32 v132, v132\n" \
"v_mfma_f32_16x16x32_bf16 v[104:107], a[200:203], v[144:147], v[104:107]\n" \
"s_cmovk_i32 s91, 0x4000\n" \
"ds_read_b128 a[192:195], v242 offset:0\n" \
"ds_read_b128 a[196:199], v242 offset:1024\n" \
"v_mfma_f32_16x16x32_bf16 v[108:111], a[204:207], v[144:147], v[108:111]\n" \
"s_cmp_eq_u32 s88, s17\n" \
"v_exp_f32 v133, v133\n" \
"v_mfma_f32_16x16x32_bf16 v[108:111], a[236:239], v[152:155], v[108:111]\n" \
"s_cmov_b32 s88, s18\n" \
"ds_read_b128 a[228:231], v242 offset:9216\n" \
"v_cvt_pk_bf16_f32 v130, v132, v133\n" \
"v_mfma_f32_16x16x32_bf16 v[112:115], a[240:243], v[152:155], v[112:115]\n" \
"v_exp_f32 v134, v134\n" \
"v_mfma_f32_16x16x32_bf16 v[112:115], a[208:211], v[144:147], v[112:115]\n" \
"buffer_load_dwordx4 v[60:63], v248, s[32:35], s38 offen offset:3072 sc1\n" \
"v_mfma_f32_16x16x32_bf16 v[116:119], a[212:215], v[144:147], v[116:119]\n" \
"v_exp_f32 v135, v135\n" \
"v_mfma_f32_16x16x32_bf16 v[116:119], a[244:247], v[152:155], v[116:119]\n" \
"ds_read_b128 a[232:235], v242 offset:10240\n" \
"v_cvt_pk_bf16_f32 v131, v134, v135\n" \
"v_mfma_f32_16x16x32_bf16 v[120:123], a[248:251], v[152:155], v[120:123]\n" \
"s_add_u32 s38, s66, 2\n" \
"ds_read_b128 a[200:203], v242 offset:2048\n" \
"ds_read_b128 a[204:207], v242 offset:3072\n" \
"v_mfma_f32_16x16x32_bf16 v[120:123], a[216:219], v[144:147], v[120:123]\n" \
"s_and_b32 s38, s38, 3\n" \
"v_exp_f32 v136, v136\n" \
"v_mfma_f32_16x16x32_bf16 v[124:127], a[220:223], v[144:147], v[124:127]\n" \
"s_lshl_b32 s38, s38, 13\n" \
"ds_read_b128 a[236:239], v242 offset:11264\n" \
"ds_read_b128 a[240:243], v242 offset:12288\n" \
"v_mfma_f32_16x16x32_bf16 v[124:127], a[252:255], v[152:155], v[124:127]\n" \
"s_waitcnt vmcnt(21)\n" \
"v_exp_f32 v137, v137\n" \
"v_mfma_f32_16x16x32_bf16 v[144:147], a[128:131], v[176:179], 0\n" \
"s_barrier\n" \
"ds_read_b128 a[208:211], v242 offset:4096\n" \
"v_cvt_pk_bf16_f32 v136, v136, v137\n" \
"v_mfma_f32_16x16x32_bf16 v[144:147], a[132:135], v[180:183], v[144:147]\n" \
"s_mov_b32 m0, s20\n" \
"v_exp_f32 v138, v138\n" \
"v_mfma_f32_16x16x32_bf16 v[144:147], a[136:139], v[184:187], v[144:147]\n" \
"s_add_u32 s38, s38, s29\n" \
"buffer_load_dwordx4 v248, s[32:35], s89 offen offset:0 sc1 lds\n" \
"v_mfma_f32_16x16x32_bf16 v[144:147], a[140:143], v[188:191], v[144:147]\n" \
"buffer_load_dwordx4 a[64:67], v248, s[32:35], s38 offen offset:0 sc1\n" \
"v_exp_f32 v139, v139\n" \
"v_mfma_f32_16x16x32_bf16 v[148:151], a[156:159], v[188:191], 0\n" \
"ds_read_b128 a[212:215], v242 offset:5120\n" \
"v_cvt_pk_bf16_f32 v137, v138, v139\n" \
"v_mfma_f32_16x16x32_bf16 v[148:151], a[152:155], v[184:187], v[148:151]\n" \
"v_exp_f32 v140, v140\n" \
"v_mfma_f32_16x16x32_bf16 v[148:151], a[148:151], v[180:183], v[148:151]\n" \
"ds_read_b128 a[244:247], v242 offset:13312\n" \
"ds_read_b128 a[248:251], v242 offset:14336\n" \
"v_mfma_f32_16x16x32_bf16 v[148:151], a[144:147], v[176:179], v[148:151]\n" \
"v_exp_f32 v141, v141\n" \
"v_mfma_f32_16x16x32_bf16 v[152:155], a[160:163], v[176:179], 0\n" \
"buffer_load_dwordx4 v248, s[32:35], s89 offen offset:1024 sc1 lds\n" \
"v_mfma_f32_16x16x32_bf16 v[152:155], a[164:167], v[180:183], v[152:155]\n" \
"v_exp_f32 v142, v142\n" \
"v_mfma_f32_16x16x32_bf16 v[152:155], a[168:171], v[184:187], v[152:155]\n" \
"buffer_load_dwordx4 a[72:75], v248, s[32:35], s38 offen offset:1024 sc1\n" \
"v_cvt_pk_bf16_f32 v138, v140, v141\n" \
"v_mfma_f32_16x16x32_bf16 v[152:155], a[172:175], v[188:191], v[152:155]\n" \
"v_exp_f32 v143, v143\n" \
"v_mfma_f32_16x16x32_bf16 v[156:159], a[188:191], v[188:191], 0\n" \
"ds_read_b128 a[216:219], v242 offset:6144\n" \
"v_cvt_pk_bf16_f32 v139, v142, v143\n" \
"v_mfma_f32_16x16x32_bf16 v[156:159], a[184:187], v[184:187], v[156:159]\n" \
"ds_read_b128 a[128:131], v240 offset:0\n" \
"ds_read_b128 a[132:135], v240 offset:1024\n" \
"v_mfma_f32_16x16x32_bf16 v[156:159], a[180:183], v[180:183], v[156:159]\n" \
"v_exp_f32 v144, v144\n" \
"v_mfma_f32_16x16x32_bf16 v[156:159], a[176:179], v[176:179], v[156:159]\n" \
"buffer_load_dwordx4 v248, s[32:35], s89 offen offset:2048 sc1 lds\n" \
"v_mfma_f32_16x16x32_bf16 a[40:43], a[48:51], v[128:131], a[40:43]\n" \
"v_exp_f32 v145, v145\n" \
"v_mfma_f32_16x16x32_bf16 a[40:43], a[48:51], v[136:139], a[40:43]\n" \
"s_waitcnt lgkmcnt(12)\n" \
"buffer_load_dwordx4 a[80:83], v248, s[32:35], s38 offen offset:2048 sc1\n" \
"v_cvt_pk_bf16_f32 v144, v144, v145\n" \
"v_mfma_f32_16x16x32_bf16 v[64:67], a[224:227], v[136:139], v[64:67]\n" \
"v_exp_f32 v146, v146\n" \
"v_mfma_f32_16x16x32_bf16 v[64:67], a[192:195], v[128:131], v[64:67]\n" \
"ds_read_b128 a[220:223], v242 offset:7168\n" \
"ds_read_b128 a[136:139], v240 offset:2048\n" \
"v_mfma_f32_16x16x32_bf16 v[68:71], a[196:199], v[128:131], v[68:71]\n" \
"v_exp_f32 v147, v147\n" \
"v_mfma_f32_16x16x32_bf16 v[68:71], a[228:231], v[136:139], v[68:71]\n" \
"s_waitcnt lgkmcnt(10)\n" \
"buffer_load_dwordx4 v248, s[32:35], s89 offen offset:3072 sc1 lds\n" \
"v_mfma_f32_16x16x32_bf16 v[72:75], a[232:235], v[136:139], v[72:75]\n" \
"s_add_u32 s20, s20, 0x4000\n" \
"v_exp_f32 v148, v148\n" \
"v_mfma_f32_16x16x32_bf16 v[72:75], a[200:203], v[128:131], v[72:75]\n" \
"s_and_b32 s20, s20, 0xffff\n" \
"ds_read_b128 a[252:255], v242 offset:15360\n" \
"v_cvt_pk_bf16_f32 v145, v146, v147\n" \
"v_mfma_f32_16x16x32_bf16 v[76:79], a[204:207], v[128:131], v[76:79]\n" \
"s_bitset1_b32 s20, 16\n" \
"v_exp_f32 v149, v149\n" \
"v_mfma_f32_16x16x32_bf16 v[76:79], a[236:239], v[136:139], v[76:79]\n" \
"s_waitcnt lgkmcnt(7)\n" \
"buffer_load_dwordx4 a[88:91], v248, s[32:35], s38 offen offset:3072 sc1\n" \
"v_cvt_pk_bf16_f32 v146, v148, v149\n" \
"v_mfma_f32_16x16x32_bf16 v[80:83], a[240:243], v[136:139], v[80:83]\n" \
"v_exp_f32 v150, v150\n" \
"v_mfma_f32_16x16x32_bf16 v[80:83], a[208:211], v[128:131], v[80:83]\n" \
"ds_read_b128 a[140:143], v240 offset:3072\n" \
"ds_read_b128 a[156:159], v240 offset:7168\n" \
"v_mfma_f32_16x16x32_bf16 v[84:87], a[212:215], v[128:131], v[84:87]\n" \
"v_exp_f32 v151, v151\n" \
"v_mfma_f32_16x16x32_bf16 v[84:87], a[244:247], v[136:139], v[84:87]\n" \
"s_waitcnt lgkmcnt(2)\n" \
"ds_read_b128 a[152:155], v240 offset:6144\n" \
"v_cvt_pk_bf16_f32 v147, v150, v151\n" \
"v_mfma_f32_16x16x32_bf16 v[88:91], a[248:251], v[136:139], v[88:91]\n" \
"ds_read_b128 a[148:151], v240 offset:5120\n" \
"ds_read_b128 a[144:147], v240 offset:4096\n" \
"v_mfma_f32_16x16x32_bf16 v[88:91], a[216:219], v[128:131], v[88:91]\n" \
"v_exp_f32 v152, v152\n" \
"v_mfma_f32_16x16x32_bf16 v[92:95], a[220:223], v[128:131], v[92:95]\n" \
"v_exp_f32 v153, v153\n" \
"v_mfma_f32_16x16x32_bf16 v[92:95], a[252:255], v[136:139], v[92:95]\n" \
"s_waitcnt lgkmcnt(4)\n" \
"ds_read_b128 a[160:163], v240 offset:8192\n" \
"ds_read_b128 a[164:167], v240 offset:9216\n" \
"ds_read_b128 a[168:171], v240 offset:10240\n" \
"ds_read_b128 a[172:175], v240 offset:11264\n" \
".Llast_%=:\n" \
".Lst1_3_%=:\n" \
"s_cbranch_execz .Lst1_7_%=\n" \
  \
"v_mfma_f32_16x16x32_bf16 v[128:131], a[128:131], v[160:163], 0\n" \
".Lst1_4_%=:\n" \
"s_cbranch_execz .Lst1_8_%=\n" \
"s_add_u32 s86, s86, 0x4000\n" \
"ds_read_b128 a[188:191], v240 offset:15360\n" \
"v_cvt_pk_bf16_f32 v152, v152, v153\n" \
"v_mfma_f32_16x16x32_bf16 v[128:131], a[132:135], v[164:167], v[128:131]\n" \
"s_bitset0_b32 s86, 16\n" \
"v_exp_f32 v154, v154\n" \
"v_mfma_f32_16x16x32_bf16 v[128:131], a[136:139], v[168:171], v[128:131]\n" \
"ds_read_b128 a[184:187], v240 offset:14336\n" \
"v_add_u32 v242, 0x10000, v240\n" \
"v_mfma_f32_16x16x32_bf16 v[128:131], a[140:143], v[172:175], v[128:131]\n" \
"s_waitcnt lgkmcnt(6)\n" \
"v_exp_f32 v155, v155\n" \
"v_mfma_f32_16x16x32_bf16 v[132:135], a[156:159], v[172:175], 0\n" \
"s_mov_b32 m0, s19\n" \
"ds_read_b128 a[180:183], v240 offset:13312\n" \
"v_cvt_pk_bf16_f32 v153, v154, v155\n" \
"v_mfma_f32_16x16x32_bf16 v[132:135], a[152:155], v[168:171], v[132:135]\n" \
"ds_read_b128 a[176:179], v240 offset:12288\n" \
"v_exp_f32 v156, v156\n" \
"v_mfma_f32_16x16x32_bf16 v[132:135], a[148:151], v[164:167], v[132:135]\n" \
"buffer_load_dwordx4 v248, s[32:35], s88 offen offset:0 sc1 lds\n" \
"v_mfma_f32_16x16x32_bf16 v[132:135], a[144:147], v[160:163], v[132:135]\n" \
"s_waitcnt lgkmcnt(4)\n" \
"v_exp_f32 v157, v157\n" \
"v_mfma_f32_16x16x32_bf16 v[136:139], a[160:163], v[160:163], 0\n" \
"v_cvt_pk_bf16_f32 v154, v156, v157\n" \
"v_mfma_f32_16x16x32_bf16 v[136:139], a[164:167], v[164:167], v[136:139]\n" \
"v_exp_f32 v158, v158\n" \
"v_mfma_f32_16x16x32_bf16 v[136:139], a[168:171], v[168:171], v[136:139]\n" \
"buffer_load_dwordx4 v248, s[32:35], s88 offen offset:1024 sc1 lds\n" \
"v_mfma_f32_16x16x32_bf16 v[136:139], a[172:175], v[172:175], v[136:139]\n" \
"s_waitcnt lgkmcnt(0)\n" \
"v_exp_f32 v159, v159\n" \
"v_mfma_f32_16x16x32_bf16 v[140:143], a[188:191], v[172:175], 0\n" \
"v_add_u32 v240, s86, v248\n" \
"v_cvt_pk_bf16_f32 v155, v158, v159\n" \
"v_mfma_f32_16x16x32_bf16 v[140:143], a[184:187], v[168:171], v[140:143]\n" \
"buffer_load_dwordx4 v248, s[32:35], s88 offen offset:2048 sc1 lds\n" \
"v_mfma_f32_16x16x32_bf16 v[140:143], a[180:183], v[164:167], v[140:143]\n" \
"v_exp_f32 v128, v128\n" \
"v_mfma_f32_16x16x32_bf16 v[140:143], a[176:179], v[160:163], v[140:143]\n" \
"buffer_load_dwordx4 v248, s[32:35], s88 offen offset:3072 sc1 lds\n" \
"v_mfma_f32_16x16x32_bf16 a[44:47], a[48:51], v[144:147], a[44:47]\n" \
"s_add_u32 s89, s88, 589824\n" \
"v_exp_f32 v129, v129\n" \
"v_mfma_f32_16x16x32_bf16 a[44:47], a[48:51], v[152:155], a[44:47]\n" \
"s_add_u32 s88, s88, s27\n" \
"s_add_u32 s19, s19, 0x4000\n" \
"v_cvt_pk_bf16_f32 v128, v128, v129\n" \
"v_mfma_f32_16x16x32_bf16 v[96:99], a[224:227], v[152:155], v[96:99]\n" \
"s_bitset0_b32 s19, 16\n" \
"v_exp_f32 v130, v130\n" \
"v_mfma_f32_16x16x32_bf16 v[96:99], a[192:195], v[144:147], v[96:99]\n" \
"s_cmp_eq_u32 s88, s16\n" \
"s_cmov_b32 s88, s21\n" \
"ds_read_b128 a[224:227], v242 offset:8192\n" \
"v_mfma_f32_16x16x32_bf16 v[100:103], a[196:199], v[144:147], v[100:103]\n" \
"s_cmov_b32 s27, s91\n" \
"v_exp_f32 v131, v131\n" \
"v_mfma_f32_16x16x32_bf16 v[100:103], a[228:231], v[152:155], v[100:103]\n" \
"s_cmov_b32 s16, s77\n" \
"ds_read_b128 a[192:195], v242 offset:0\n" \
"v_cvt_pk_bf16_f32 v129, v130, v131\n" \
"v_mfma_f32_16x16x32_bf16 v[104:107], a[232:235], v[152:155], v[104:107]\n" \
"s_cmov_b32 s21, s92\n" \
"v_exp_f32 v132, v132\n" \
"v_mfma_f32_16x16x32_bf16 v[104:107], a[200:203], v[144:147], v[104:107]\n" \
"s_cmovk_i32 s91, 0x4000\n" \
"s_add_u32 s38, s38, 4096\n" \
"ds_read_b128 a[196:199], v242 offset:1024\n" \
"ds_read_b128 a[228:231], v242 offset:9216\n" \
"buffer_load_dwordx4 a[96:99], v248, s[32:35], s38 offen offset:0 sc1\n" \
"v_mfma_f32_16x16x32_bf16 v[108:111], a[204:207], v[144:147], v[108:111]\n" \
"s_cmp_eq_u32 s88, s17\n" \
"v_exp_f32 v133, v133\n" \
"v_mfma_f32_16x16x32_bf16 v[108:111], a[236:239], v[152:155], v[108:111]\n" \
"s_cmov_b32 s88, s18\n" \
"ds_read_b128 a[232:235], v242 offset:10240\n" \
"v_cvt_pk_bf16_f32 v130, v132, v133\n" \
"v_mfma_f32_16x16x32_bf16 v[112:115], a[240:243], v[152:155], v[112:115]\n" \
"v_exp_f32 v134, v134\n" \
"v_mfma_f32_16x16x32_bf16 v[112:115], a[208:211], v[144:147], v[112:115]\n" \
"ds_read_b128 a[200:203], v242 offset:2048\n" \
"v_mfma_f32_16x16x32_bf16 v[116:119], a[212:215], v[144:147], v[116:119]\n" \
"v_exp_f32 v135, v135\n" \
"v_mfma_f32_16x16x32_bf16 v[116:119], a[244:247], v[152:155], v[116:119]\n" \
"ds_read_b128 a[204:207], v242 offset:3072\n" \
"v_cvt_pk_bf16_f32 v131, v134, v135\n" \
"v_mfma_f32_16x16x32_bf16 v[120:123], a[248:251], v[152:155], v[120:123]\n" \
"ds_read_b128 a[236:239], v242 offset:11264\n" \
"v_mfma_f32_16x16x32_bf16 v[120:123], a[216:219], v[144:147], v[120:123]\n" \
"v_exp_f32 v136, v136\n" \
"v_mfma_f32_16x16x32_bf16 v[124:127], a[220:223], v[144:147], v[124:127]\n" \
"ds_read_b128 a[240:243], v242 offset:12288\n" \
"v_mfma_f32_16x16x32_bf16 v[124:127], a[252:255], v[152:155], v[124:127]\n" \
"s_waitcnt vmcnt(13)\n" \
"v_exp_f32 v137, v137\n" \
"v_mfma_f32_16x16x32_bf16 v[144:147], a[128:131], v[176:179], 0\n" \
"s_barrier\n" \
"ds_read_b128 a[208:211], v242 offset:4096\n" \
"v_cvt_pk_bf16_f32 v136, v136, v137\n" \
"v_mfma_f32_16x16x32_bf16 v[144:147], a[132:135], v[180:183], v[144:147]\n" \
"s_mov_b32 m0, s20\n" \
"buffer_load_dwordx4 a[104:107], v248, s[32:35], s38 offen offset:1024 sc1\n" \
"v_exp_f32 v138, v138\n" \
"v_mfma_f32_16x16x32_bf16 v[144:147], a[136:139], v[184:187], v[144:147]\n" \
"buffer_load_dwordx4 v248, s[32:35], s89 offen offset:0 sc1 lds\n" \
"v_mfma_f32_16x16x32_bf16 v[144:147], a[140:143], v[188:191], v[144:147]\n" \
"v_exp_f32 v139, v139\n" \
"v_mfma_f32_16x16x32_bf16 v[148:151], a[156:159], v[188:191], 0\n" \
"ds_read_b128 a[212:215], v242 offset:5120\n" \
"v_cvt_pk_bf16_f32 v137, v138, v139\n" \
"v_mfma_f32_16x16x32_bf16 v[148:151], a[152:155], v[184:187], v[148:151]\n" \
"v_exp_f32 v140, v140\n" \
"v_mfma_f32_16x16x32_bf16 v[148:151], a[148:151], v[180:183], v[148:151]\n" \
"ds_read_b128 a[244:247], v242 offset:13312\n" \
"ds_read_b128 a[248:251], v242 offset:14336\n" \
"v_mfma_f32_16x16x32_bf16 v[148:151], a[144:147], v[176:179], v[148:151]\n" \
"v_exp_f32 v141, v141\n" \
"v_mfma_f32_16x16x32_bf16 v[152:155], a[160:163], v[176:179], 0\n" \
"buffer_load_dwordx4 v248, s[32:35], s89 offen offset:1024 sc1 lds\n" \
"v_mfma_f32_16x16x32_bf16 v[152:155], a[164:167], v[180:183], v[152:155]\n" \
"v_exp_f32 v142, v142\n" \
"v_mfma_f32_16x16x32_bf16 v[152:155], a[168:171], v[184:187], v[152:155]\n" \
"ds_read_b128 a[216:219], v242 offset:6144\n" \
"v_cvt_pk_bf16_f32 v138, v140, v141\n" \
"v_mfma_f32_16x16x32_bf16 v[152:155], a[172:175], v[188:191], v[152:155]\n" \
"v_exp_f32 v143, v143\n" \
"v_mfma_f32_16x16x32_bf16 v[156:159], a[188:191], v[188:191], 0\n" \
"ds_read_b128 a[128:131], v240 offset:0\n" \
"v_cvt_pk_bf16_f32 v139, v142, v143\n" \
"v_mfma_f32_16x16x32_bf16 v[156:159], a[184:187], v[184:187], v[156:159]\n" \
"ds_read_b128 a[132:135], v240 offset:1024\n" \
"ds_read_b128 a[220:223], v242 offset:7168\n" \
"v_mfma_f32_16x16x32_bf16 v[156:159], a[180:183], v[180:183], v[156:159]\n" \
"v_exp_f32 v144, v144\n" \
"v_mfma_f32_16x16x32_bf16 v[156:159], a[176:179], v[176:179], v[156:159]\n" \
"buffer_load_dwordx4 v248, s[32:35], s89 offen offset:2048 sc1 lds\n" \
"buffer_load_dwordx4 a[112:115], v248, s[32:35], s38 offen offset:2048 sc1\n" \
"v_mfma_f32_16x16x32_bf16 a[40:43], a[48:51], v[128:131], a[40:43]\n" \
"v_exp_f32 v145, v145\n" \
"v_mfma_f32_16x16x32_bf16 a[40:43], a[48:51], v[136:139], a[40:43]\n" \
"s_waitcnt lgkmcnt(13)\n" \
"ds_read_b128 a[136:139], v240 offset:2048\n" \
"v_cvt_pk_bf16_f32 v144, v144, v145\n" \
"v_mfma_f32_16x16x32_bf16 v[64:67], a[224:227], v[136:139], v[64:67]\n" \
"v_exp_f32 v146, v146\n" \
"v_mfma_f32_16x16x32_bf16 v[64:67], a[192:195], v[128:131], v[64:67]\n" \
"ds_read_b128 a[252:255], v242 offset:15360\n" \
"ds_read_b128 a[140:143], v240 offset:3072\n" \
"v_mfma_f32_16x16x32_bf16 v[68:71], a[196:199], v[128:131], v[68:71]\n" \
"v_exp_f32 v147, v147\n" \
"v_mfma_f32_16x16x32_bf16 v[68:71], a[228:231], v[136:139], v[68:71]\n" \
"s_waitcnt lgkmcnt(12)\n" \
"buffer_load_dwordx4 v248, s[32:35], s89 offen offset:3072 sc1 lds\n" \
"v_mfma_f32_16x16x32_bf16 v[72:75], a[232:235], v[136:139], v[72:75]\n" \
"s_add_u32 s20, s20, 0x4000\n" \
"v_exp_f32 v148, v148\n" \
"v_mfma_f32_16x16x32_bf16 v[72:75], a[200:203], v[128:131], v[72:75]\n" \
"s_and_b32 s20, s20, 0xffff\n" \
"ds_read_b128 a[156:159], v240 offset:7168\n" \
"v_cvt_pk_bf16_f32 v145, v146, v147\n" \
"v_mfma_f32_16x16x32_bf16 v[76:79], a[204:207], v[128:131], v[76:79]\n" \
"s_bitset1_b32 s20, 16\n" \
"v_exp_f32 v149, v149\n" \
"v_mfma_f32_16x16x32_bf16 v[76:79], a[236:239], v[136:139], v[76:79]\n" \
"s_waitcnt lgkmcnt(9)\n" \
"ds_read_b128 a[152:155], v240 offset:6144\n" \
"v_cvt_pk_bf16_f32 v146, v148, v149\n" \
"v_mfma_f32_16x16x32_bf16 v[80:83], a[240:243], v[136:139], v[80:83]\n" \
"v_exp_f32 v150, v150\n" \
"v_mfma_f32_16x16x32_bf16 v[80:83], a[208:211], v[128:131], v[80:83]\n" \
"ds_read_b128 a[148:151], v240 offset:5120\n" \
"ds_read_b128 a[144:147], v240 offset:4096\n" \
"v_mfma_f32_16x16x32_bf16 v[84:87], a[212:215], v[128:131], v[84:87]\n" \
"v_exp_f32 v151, v151\n" \
"v_mfma_f32_16x16x32_bf16 v[84:87], a[244:247], v[136:139], v[84:87]\n" \
"s_waitcnt lgkmcnt(5)\n" \
"ds_read_b128 a[160:163], v240 offset:8192\n" \
"buffer_load_dwordx4 a[116:119], v248, s[32:35], s38 offen offset:3072 sc1\n" \
"v_cvt_pk_bf16_f32 v147, v150, v151\n" \
"v_mfma_f32_16x16x32_bf16 v[88:91], a[248:251], v[136:139], v[88:91]\n" \
"ds_read_b128 a[164:167], v240 offset:9216\n" \
"ds_read_b128 a[168:171], v240 offset:10240\n" \
"v_mfma_f32_16x16x32_bf16 v[88:91], a[216:219], v[128:131], v[88:91]\n" \
"v_exp_f32 v152, v152\n" \
"v_mfma_f32_16x16x32_bf16 v[92:95], a[220:223], v[128:131], v[92:95]\n" \
"ds_read_b128 a[172:175], v240 offset:11264\n" \
"ds_read_b128 a[188:191], v240 offset:15360\n" \
"v_mfma_f32_16x16x32_bf16 v[92:95], a[252:255], v[136:139], v[92:95]\n" \
"s_waitcnt lgkmcnt(9)\n" \
"v_exp_f32 v153, v153\n" \
"v_mfma_f32_16x16x32_bf16 v[128:131], a[128:131], v[160:163], 0\n" \
"ds_read_b128 a[184:187], v240 offset:14336\n" \
"v_cvt_pk_bf16_f32 v152, v152, v153\n" \
"v_mfma_f32_16x16x32_bf16 v[128:131], a[132:135], v[164:167], v[128:131]\n" \
"v_exp_f32 v154, v154\n" \
"v_mfma_f32_16x16x32_bf16 v[128:131], a[136:139], v[168:171], v[128:131]\n" \
"s_add_u32 s38, s66, 3\n" \
"ds_read_b128 a[180:183], v240 offset:13312\n" \
"v_add_u32 v242, 0x10000, v240\n" \
"v_mfma_f32_16x16x32_bf16 v[128:131], a[140:143], v[172:175], v[128:131]\n" \
"s_waitcnt lgkmcnt(7)\n" \
"v_exp_f32 v155, v155\n" \
"v_mfma_f32_16x16x32_bf16 v[132:135], a[156:159], v[172:175], 0\n" \
"s_mov_b32 m0, s19\n" \
"ds_read_b128 a[176:179], v240 offset:12288\n" \
"v_cvt_pk_bf16_f32 v153, v154, v155\n" \
"v_mfma_f32_16x16x32_bf16 v[132:135], a[152:155], v[168:171], v[132:135]\n" \
"s_and_b32 s38, s38, 3\n" \
"v_exp_f32 v156, v156\n" \
"v_mfma_f32_16x16x32_bf16 v[132:135], a[148:151], v[164:167], v[132:135]\n" \
"s_lshl_b32 s38, s38, 13\n" \
"buffer_load_dwordx4 v248, s[32:35], s88 offen offset:0 sc1 lds\n" \
"v_mfma_f32_16x16x32_bf16 v[132:135], a[144:147], v[160:163], v[132:135]\n" \
"s_waitcnt lgkmcnt(4)\n" \
"v_exp_f32 v157, v157\n" \
"v_mfma_f32_16x16x32_bf16 v[136:139], a[160:163], v[160:163], 0\n" \
"s_add_u32 s38, s38, s29\n" \
"buffer_load_dwordx4 a[120:123], v248, s[32:35], s38 offen offset:0 sc1\n" \
"v_cvt_pk_bf16_f32 v154, v156, v157\n" \
"v_mfma_f32_16x16x32_bf16 v[136:139], a[164:167], v[164:167], v[136:139]\n" \
"v_exp_f32 v158, v158\n" \
"v_mfma_f32_16x16x32_bf16 v[136:139], a[168:171], v[168:171], v[136:139]\n" \
"buffer_load_dwordx4 v248, s[32:35], s88 offen offset:1024 sc1 lds\n" \
"v_mfma_f32_16x16x32_bf16 v[136:139], a[172:175], v[172:175], v[136:139]\n" \
"s_waitcnt lgkmcnt(0)\n" \
"v_exp_f32 v159, v159\n" \
"v_mfma_f32_16x16x32_bf16 v[140:143], a[188:191], v[172:175], 0\n" \
"v_cvt_pk_bf16_f32 v155, v158, v159\n" \
"v_mfma_f32_16x16x32_bf16 v[140:143], a[184:187], v[168:171], v[140:143]\n" \
"buffer_load_dwordx4 v248, s[32:35], s88 offen offset:2048 sc1 lds\n" \
"v_mfma_f32_16x16x32_bf16 v[140:143], a[180:183], v[164:167], v[140:143]\n" \
"v_exp_f32 v128, v128\n" \
"v_mfma_f32_16x16x32_bf16 v[140:143], a[176:179], v[160:163], v[140:143]\n" \
"buffer_load_dwordx4 v248, s[32:35], s88 offen offset:3072 sc1 lds\n" \
"v_mfma_f32_16x16x32_bf16 a[44:47], a[48:51], v[144:147], a[44:47]\n" \
"s_add_u32 s89, s88, 589824\n" \
"v_exp_f32 v129, v129\n" \
"v_mfma_f32_16x16x32_bf16 a[44:47], a[48:51], v[152:155], a[44:47]\n" \
"v_cvt_pk_bf16_f32 v128, v128, v129\n" \
"v_lshlrev_b32 v232, 3, v251\n" \
"v_mfma_f32_16x16x32_bf16 v[96:99], a[224:227], v[152:155], v[96:99]\n" \
"s_lshl_b32 s70, %[wid], 9\n" \
"v_exp_f32 v130, v130\n" \
"v_mfma_f32_16x16x32_bf16 v[96:99], a[192:195], v[144:147], v[96:99]\n" \
"s_add_u32 s70, s70, 659456\n" \
"ds_read_b128 a[224:227], v242 offset:8192\n" \
"v_mov_b32 v234, 1\n" \
"v_mfma_f32_16x16x32_bf16 v[100:103], a[196:199], v[144:147], v[100:103]\n" \
"v_exp_f32 v131, v131\n" \
"v_mfma_f32_16x16x32_bf16 v[100:103], a[228:231], v[152:155], v[100:103]\n" \
"ds_read_b128 a[192:195], v242 offset:0\n" \
"v_cvt_pk_bf16_f32 v129, v130, v131\n" \
"v_mfma_f32_16x16x32_bf16 v[104:107], a[232:235], v[152:155], v[104:107]\n" \
"v_exp_f32 v132, v132\n" \
"v_mfma_f32_16x16x32_bf16 v[104:107], a[200:203], v[144:147], v[104:107]\n" \
"ds_read_b128 a[196:199], v242 offset:1024\n" \
"ds_read_b128 a[228:231], v242 offset:9216\n" \
"buffer_load_dwordx4 a[124:127], v248, s[32:35], s38 offen offset:1024 sc1\n" \
"v_mfma_f32_16x16x32_bf16 v[108:111], a[204:207], v[144:147], v[108:111]\n" \
"v_exp_f32 v133, v133\n" \
"v_mfma_f32_16x16x32_bf16 v[108:111], a[236:239], v[152:155], v[108:111]\n" \
"ds_read_b128 a[232:235], v242 offset:10240\n" \
"v_cvt_pk_bf16_f32 v130, v132, v133\n" \
"v_mfma_f32_16x16x32_bf16 v[112:115], a[240:243], v[152:155], v[112:115]\n" \
"v_exp_f32 v134, v134\n" \
"v_mfma_f32_16x16x32_bf16 v[112:115], a[208:211], v[144:147], v[112:115]\n" \
"ds_read_b128 a[200:203], v242 offset:2048\n" \
"v_mov_b32 v235, 1\n" \
"v_mfma_f32_16x16x32_bf16 v[116:119], a[212:215], v[144:147], v[116:119]\n" \
"v_exp_f32 v135, v135\n" \
"v_mfma_f32_16x16x32_bf16 v[116:119], a[244:247], v[152:155], v[116:119]\n" \
"ds_read_b128 a[204:207], v242 offset:3072\n" \
"v_cvt_pk_bf16_f32 v131, v134, v135\n" \
"v_mfma_f32_16x16x32_bf16 v[120:123], a[248:251], v[152:155], v[120:123]\n" \
"ds_read_b128 a[236:239], v242 offset:11264\n" \
"v_mfma_f32_16x16x32_bf16 v[120:123], a[216:219], v[144:147], v[120:123]\n" \
"v_exp_f32 v136, v136\n" \
"v_mfma_f32_16x16x32_bf16 v[124:127], a[220:223], v[144:147], v[124:127]\n" \
"ds_read_b128 a[240:243], v242 offset:12288\n" \
"v_mfma_f32_16x16x32_bf16 v[124:127], a[252:255], v[152:155], v[124:127]\n" \
"s_waitcnt vmcnt(14)\n" \
"v_exp_f32 v137, v137\n" \
"v_mfma_f32_16x16x32_bf16 v[144:147], a[128:131], v[176:179], 0\n" \
"s_barrier\n" \
"ds_read_b128 a[208:211], v242 offset:4096\n" \
"v_cvt_pk_bf16_f32 v136, v136, v137\n" \
"v_mfma_f32_16x16x32_bf16 v[144:147], a[132:135], v[180:183], v[144:147]\n" \
"s_mov_b32 m0, s20\n" \
"v_exp_f32 v138, v138\n" \
"v_mfma_f32_16x16x32_bf16 v[144:147], a[136:139], v[184:187], v[144:147]\n" \
"buffer_load_dwordx4 v248, s[32:35], s89 offen offset:0 sc1 lds\n" \
"v_mfma_f32_16x16x32_bf16 v[144:147], a[140:143], v[188:191], v[144:147]\n" \
"v_exp_f32 v139, v139\n" \
"v_mfma_f32_16x16x32_bf16 v[148:151], a[156:159], v[188:191], 0\n" \
"ds_read_b128 a[212:215], v242 offset:5120\n" \
"v_cvt_pk_bf16_f32 v137, v138, v139\n" \
"v_mfma_f32_16x16x32_bf16 v[148:151], a[152:155], v[184:187], v[148:151]\n" \
"v_exp_f32 v140, v140\n" \
"v_mfma_f32_16x16x32_bf16 v[148:151], a[148:151], v[180:183], v[148:151]\n" \
"ds_read_b128 a[244:247], v242 offset:13312\n" \
"ds_read_b128 a[248:251], v242 offset:14336\n" \
"v_mfma_f32_16x16x32_bf16 v[148:151], a[144:147], v[176:179], v[148:151]\n" \
"v_exp_f32 v141, v141\n" \
"v_mfma_f32_16x16x32_bf16 v[152:155], a[160:163], v[176:179], 0\n" \
"buffer_load_dwordx4 v248, s[32:35], s89 offen offset:1024 sc1 lds\n" \
"v_mfma_f32_16x16x32_bf16 v[152:155], a[164:167], v[180:183], v[152:155]\n" \
"v_exp_f32 v142, v142\n" \
"v_mfma_f32_16x16x32_bf16 v[152:155], a[168:171], v[184:187], v[152:155]\n" \
"ds_read_b128 a[216:219], v242 offset:6144\n" \
"v_cvt_pk_bf16_f32 v138, v140, v141\n" \
"v_mfma_f32_16x16x32_bf16 v[152:155], a[172:175], v[188:191], v[152:155]\n" \
"v_exp_f32 v143, v143\n" \
"v_mfma_f32_16x16x32_bf16 v[156:159], a[188:191], v[188:191], 0\n" \
"v_exp_f32 v144, v144\n" \
"v_mfma_f32_16x16x32_bf16 v[156:159], a[184:187], v[184:187], v[156:159]\n" \
"v_exp_f32 v145, v145\n" \
"v_mfma_f32_16x16x32_bf16 v[156:159], a[180:183], v[180:183], v[156:159]\n" \
"v_exp_f32 v146, v146\n" \
"v_mfma_f32_16x16x32_bf16 v[156:159], a[176:179], v[176:179], v[156:159]\n" \
"v_exp_f32 v147, v147\n" \
"v_cvt_pk_bf16_f32 v139, v142, v143\n" \
"v_mfma_f32_16x16x32_bf16 a[40:43], a[48:51], v[128:131], a[40:43]\n" \
"v_exp_f32 v148, v148\n" \
"v_cvt_pk_bf16_f32 v144, v144, v145\n" \
"v_mfma_f32_16x16x32_bf16 a[40:43], a[48:51], v[136:139], a[40:43]\n" \
"s_waitcnt lgkmcnt(10)\n" \
"v_exp_f32 v149, v149\n" \
"v_cvt_pk_bf16_f32 v145, v146, v147\n" \
"v_mfma_f32_16x16x32_bf16 v[64:67], a[224:227], v[136:139], v[64:67]\n" \
"v_exp_f32 v150, v150\n" \
"v_cvt_pk_bf16_f32 v146, v148, v149\n" \
"v_mfma_f32_16x16x32_bf16 v[64:67], a[192:195], v[128:131], v[64:67]\n" \
"ds_read_b128 a[220:223], v242 offset:7168\n" \
"v_exp_f32 v151, v151\n" \
"v_mfma_f32_16x16x32_bf16 v[68:71], a[196:199], v[128:131], v[68:71]\n" \
"buffer_load_dwordx4 a[128:131], v248, s[32:35], s38 offen offset:2048 sc1\n" \
"v_exp_f32 v152, v152\n" \
"v_cvt_pk_bf16_f32 v147, v150, v151\n" \
"v_mfma_f32_16x16x32_bf16 v[68:71], a[228:231], v[136:139], v[68:71]\n" \
"s_waitcnt lgkmcnt(7)\n" \
"ds_read_b128 a[252:255], v242 offset:15360\n" \
"v_exp_f32 v153, v153\n" \
"v_mfma_f32_16x16x32_bf16 v[72:75], a[232:235], v[136:139], v[72:75]\n" \
"v_exp_f32 v154, v154\n" \
"v_cvt_pk_bf16_f32 v152, v152, v153\n" \
"v_mfma_f32_16x16x32_bf16 v[72:75], a[200:203], v[128:131], v[72:75]\n" \
"buffer_load_dwordx4 a[132:135], v248, s[32:35], s38 offen offset:3072 sc1\n" \
"v_exp_f32 v155, v155\n" \
"v_mfma_f32_16x16x32_bf16 v[76:79], a[204:207], v[128:131], v[76:79]\n" \
"v_exp_f32 v156, v156\n" \
"v_cvt_pk_bf16_f32 v153, v154, v155\n" \
"v_mfma_f32_16x16x32_bf16 v[76:79], a[236:239], v[136:139], v[76:79]\n" \
"s_waitcnt lgkmcnt(4)\n" \
"buffer_load_dwordx4 v248, s[32:35], s89 offen offset:2048 sc1 lds\n" \
"v_accvgpr_read_b32 v224, a40\n" \
"v_mfma_f32_16x16x32_bf16 v[80:83], a[240:243], v[136:139], v[80:83]\n" \
"buffer_load_dwordx2 v[230:231], v232, s[32:35], s70 offen sc1\n" \
"v_exp_f32 v157, v157\n" \
"v_mfma_f32_16x16x32_bf16 v[80:83], a[208:211], v[128:131], v[80:83]\n" \
"v_exp_f32 v158, v158\n" \
"v_cvt_pk_bf16_f32 v154, v156, v157\n" \
"v_mfma_f32_16x16x32_bf16 v[84:87], a[212:215], v[128:131], v[84:87]\n" \
"s_add_u32 s38, s38, 4096\n" \
"buffer_load_dwordx4 v248, s[32:35], s89 offen offset:3072 sc1 lds\n" \
"v_mfma_f32_16x16x32_bf16 v[84:87], a[244:247], v[136:139], v[84:87]\n" \
"s_waitcnt lgkmcnt(0)\n" \
"buffer_load_dwordx4 a[136:139], v248, s[32:35], s38 offen offset:0 sc1\n" \
"v_exp_f32 v159, v159\n" \
"v_mfma_f32_16x16x32_bf16 v[88:91], a[248:251], v[136:139], v[88:91]\n" \
"v_cvt_pk_bf16_f32 v155, v158, v159\n" \
"v_add_u32 v240, s83, v248\n" \
"v_add_u32 v241, 0x10000, v240\n" \
"v_mfma_f32_16x16x32_bf16 v[88:91], a[216:219], v[128:131], v[88:91]\n" \
"v_xor_b32 v244, 0x8000, v240\n" \
"v_xor_b32 v249, 0x8000, v241\n" \
"v_add_u32 v239, 0x20000, v248\n" \
"v_mfma_f32_16x16x32_bf16 v[92:95], a[220:223], v[128:131], v[92:95]\n" \
"s_lshl_b32 s39, s66, 13\n" \
"v_rcp_f32 v224, v224\n" \
"s_nop 0\n" \
"v_mul_f32 v64, v64, v224\n" \
"v_mfma_f32_16x16x32_bf16 v[92:95], a[252:255], v[136:139], v[92:95]\n" \
"s_add_u32 s39, s39, s37\n" \
"v_mul_f32 v65, v65, v224\n" \
"v_mul_f32 v66, v66, v224\n" \
"v_mul_f32 v67, v67, v224\n" \
"v_mfma_f32_16x16x32_bf16 a[44:47], a[48:51], v[144:147], a[44:47]\n" \
"s_add_u32 s19, s39, 4096\n" \
"v_mul_f32 v68, v68, v224\n" \
"v_mul_f32 v69, v69, v224\n" \
"v_mul_f32 v70, v70, v224\n" \
"v_mfma_f32_16x16x32_bf16 a[44:47], a[48:51], v[152:155], a[44:47]\n" \
"buffer_load_dwordx4 a[140:143], v248, s[32:35], s38 offen offset:1024 sc1\n" \
"v_mul_f32 v71, v71, v224\n" \
"v_cvt_pk_bf16_f32 v192, v64, v65\n" \
"v_mfma_f32_16x16x32_bf16 v[96:99], a[224:227], v[152:155], v[96:99]\n" \
"v_cvt_pk_bf16_f32 v193, v66, v67\n" \
"v_cvt_pk_bf16_f32 v194, v68, v69\n" \
"v_cvt_pk_bf16_f32 v195, v70, v71\n" \
"v_mfma_f32_16x16x32_bf16 v[96:99], a[192:195], v[144:147], v[96:99]\n" \
"buffer_store_dwordx4 v[192:195], v248, s[32:35], s39 offen offset:0 sc1\n" \
"v_mul_f32 v72, v72, v224\n" \
"v_mfma_f32_16x16x32_bf16 v[100:103], a[196:199], v[144:147], v[100:103]\n" \
"ds_read_b128 a[0:3], v240 offset:0\n" \
"v_mul_f32 v73, v73, v224\n" \
"v_mul_f32 v74, v74, v224\n" \
"v_mfma_f32_16x16x32_bf16 v[100:103], a[228:231], v[152:155], v[100:103]\n" \
"v_mul_f32 v75, v75, v224\n" \
"v_mul_f32 v76, v76, v224\n" \
"v_mul_f32 v77, v77, v224\n" \
"v_mfma_f32_16x16x32_bf16 v[104:107], a[232:235], v[152:155], v[104:107]\n" \
"v_accvgpr_read_b32 v228, a40\n" \
"v_accvgpr_read_b32 v229, a44\n" \
"v_min_f32 v253, v228, v229\n" \
"v_mfma_f32_16x16x32_bf16 v[104:107], a[200:203], v[144:147], v[104:107]\n" \
"v_max_f32 v252, v228, v229\n" \
"v_cmp_nle_f32 s[98:99], v226, v253\n" \
"v_cmp_nle_f32 vcc, v252, v227\n" \
"v_mfma_f32_16x16x32_bf16 v[108:111], a[204:207], v[144:147], v[108:111]\n" \
"ds_read_b128 a[4:7], v240 offset:8192\n" \
"ds_read_b128 a[8:11], v240 offset:1024\n" \
"v_cmp_u_f32 s[14:15], v228, v229\n" \
"v_mfma_f32_16x16x32_bf16 v[108:111], a[236:239], v[152:155], v[108:111]\n" \
"buffer_load_dwordx4 a[144:147], v248, s[32:35], s38 offen offset:2048 sc1\n" \
"v_mul_f32 v78, v78, v224\n" \
"v_mul_f32 v79, v79, v224\n" \
"v_mfma_f32_16x16x32_bf16 v[112:115], a[240:243], v[152:155], v[112:115]\n" \
"s_or_b64 vcc, vcc, s[98:99]\n" \
"ds_read_b128 a[12:15], v240 offset:9216\n" \
"v_cvt_pk_bf16_f32 v200, v72, v73\n" \
"v_cvt_pk_bf16_f32 v201, v74, v75\n" \
"v_mfma_f32_16x16x32_bf16 v[112:115], a[208:211], v[144:147], v[112:115]\n" \
"s_or_b64 vcc, vcc, s[14:15]\n" \
"v_cvt_pk_bf16_f32 v202, v76, v77\n" \
"v_cvt_pk_bf16_f32 v203, v78, v79\n" \
"v_mul_f32 v80, v80, v224\n" \
"v_mfma_f32_16x16x32_bf16 v[116:119], a[212:215], v[144:147], v[116:119]\n" \
"ds_read_b128 a[16:19], v240 offset:2048\n" \
"v_mul_f32 v81, v81, v224\n" \
"v_mul_f32 v82, v82, v224\n" \
"v_mfma_f32_16x16x32_bf16 v[116:119], a[244:247], v[152:155], v[116:119]\n" \
"buffer_store_dwordx4 v[200:203], v248, s[32:35], s39 offen offset:2048 sc1\n" \
"v_mul_f32 v83, v83, v224\n" \
"v_mfma_f32_16x16x32_bf16 v[120:123], a[248:251], v[152:155], v[120:123]\n" \
"ds_read_b128 a[20:23], v240 offset:10240\n" \
"v_mul_f32 v84, v84, v224\n" \
"v_mul_f32 v85, v85, v224\n" \
"v_mfma_f32_16x16x32_bf16 v[120:123], a[216:219], v[144:147], v[120:123]\n" \
"buffer_load_dwordx4 a[148:151], v248, s[32:35], s38 offen offset:3072 sc1\n" \
"v_mul_f32 v86, v86, v224\n" \
"v_mul_f32 v87, v87, v224\n" \
"v_mfma_f32_16x16x32_bf16 v[124:127], a[220:223], v[144:147], v[124:127]\n" \
"v_cvt_pk_bf16_f32 v208, v80, v81\n" \
"v_cvt_pk_bf16_f32 v209, v82, v83\n" \
"v_cvt_pk_bf16_f32 v210, v84, v85\n" \
"v_mfma_f32_16x16x32_bf16 v[124:127], a[252:255], v[152:155], v[124:127]\n" \
"v_cvt_pk_bf16_f32 v211, v86, v87\n" \
"v_mul_f32 v88, v88, v224\n" \
"v_mul_f32 v89, v89, v224\n" \
"s_cbranch_vccnz .Lrh_%=\n" \
".Lrhb_%=:\n" \
  \
"s_waitcnt lgkmcnt(5)\n" \
"v_mfma_f32_16x16x32_bf16 v[128:131], a[0:3], v[0:3], 0\n" \
"ds_read_b128 a[24:27], v240 offset:3072\n" \
"v_mul_f32 v90, v90, v224\n" \
"v_mul_f32 v91, v91, v224\n" \
"v_mul_f32 v92, v92, v224\n" \
"s_waitcnt lgkmcnt(5)\n" \
"v_mfma_f32_16x16x32_bf16 v[128:131], a[4:7], v[8:11], v[128:131]\n" \
"buffer_store_dwordx4 v[208:211], v248, s[32:35], s19 offen offset:0 sc1\n" \
"v_mul_f32 v93, v93, v224\n" \
"v_mul_f32 v94, v94, v224\n" \
"v_mfma_f32_16x16x32_bf16 v[136:139], a[4:7], v[12:15], 0\n" \
"ds_read_b128 a[28:31], v240 offset:11264\n" \
"v_mul_f32 v95, v95, v224\n" \
"v_cvt_pk_bf16_f32 v216, v88, v89\n" \
"v_cvt_pk_bf16_f32 v217, v90, v91\n" \
"v_mfma_f32_16x16x32_bf16 v[136:139], a[0:3], v[4:7], v[136:139]\n" \
"s_waitcnt lgkmcnt(4)\n" \
"v_cvt_pk_bf16_f32 v218, v92, v93\n" \
"v_cvt_pk_bf16_f32 v219, v94, v95\n" \
"v_accvgpr_read_b32 v225, a44\n" \
"v_mfma_f32_16x16x32_bf16 v[132:135], a[8:11], v[0:3], 0\n" \
"ds_read_b128 a[0:3], v240 offset:4096\n" \
"v_rcp_f32 v225, v225\n" \
"s_nop 0\n" \
"v_mul_f32 v96, v96, v225\n" \
"v_mfma_f32_16x16x32_bf16 v[132:135], a[12:15], v[8:11], v[132:135]\n" \
"buffer_store_dwordx4 v[216:219], v248, s[32:35], s19 offen offset:2048 sc1\n" \
"v_mul_f32 v97, v97, v225\n" \
"v_mul_f32 v98, v98, v225\n" \
"v_mfma_f32_16x16x32_bf16 v[140:143], a[12:15], v[12:15], 0\n" \
"ds_read_b128 a[4:7], v240 offset:12288\n" \
"v_mul_f32 v99, v99, v225\n" \
"v_mul_f32 v100, v100, v225\n" \
"v_mul_f32 v101, v101, v225\n" \
"v_mfma_f32_16x16x32_bf16 v[140:143], a[8:11], v[4:7], v[140:143]\n" \
"s_waitcnt lgkmcnt(4)\n" \
"v_mul_f32 v102, v102, v225\n" \
"v_mul_f32 v103, v103, v225\n" \
"v_cvt_pk_bf16_f32 v196, v96, v97\n" \
"v_cvt_pk_bf16_f32 v197, v98, v99\n" \
"v_mfma_f32_16x16x32_bf16 v[144:147], a[16:19], v[0:3], 0\n" \
"ds_read_b128 a[8:11], v240 offset:5120\n" \
"v_cvt_pk_bf16_f32 v198, v100, v101\n" \
"v_cvt_pk_bf16_f32 v199, v102, v103\n" \
"v_mul_f32 v104, v104, v225\n" \
"v_mfma_f32_16x16x32_bf16 v[144:147], a[20:23], v[8:11], v[144:147]\n" \
"buffer_store_dwordx4 v[196:199], v248, s[32:35], s39 offen offset:1024 sc1\n" \
"v_mul_f32 v105, v105, v225\n" \
"v_mul_f32 v106, v106, v225\n" \
"v_mfma_f32_16x16x32_bf16 v[152:155], a[20:23], v[12:15], 0\n" \
"ds_read_b128 a[12:15], v240 offset:13312\n" \
"v_mul_f32 v107, v107, v225\n" \
"v_mul_f32 v108, v108, v225\n" \
"v_mul_f32 v109, v109, v225\n" \
"v_mfma_f32_16x16x32_bf16 v[152:155], a[16:19], v[4:7], v[152:155]\n" \
"s_waitcnt lgkmcnt(4)\n" \
"v_mul_f32 v110, v110, v225\n" \
"v_mul_f32 v111, v111, v225\n" \
"v_cvt_pk_bf16_f32 v204, v104, v105\n" \
"v_cvt_pk_bf16_f32 v205, v106, v107\n" \
"v_mfma_f32_16x16x32_bf16 v[148:151], a[24:27], v[0:3], 0\n" \
"ds_read_b128 a[16:19], v240 offset:6144\n" \
"v_cvt_pk_bf16_f32 v206, v108, v109\n" \
"v_cvt_pk_bf16_f32 v207, v110, v111\n" \
"v_mul_f32 v112, v112, v225\n" \
"v_mfma_f32_16x16x32_bf16 v[148:151], a[28:31], v[8:11], v[148:151]\n" \
"buffer_store_dwordx4 v[204:207], v248, s[32:35], s39 offen offset:3072 sc1\n" \
"v_mul_f32 v113, v113, v225\n" \
"v_mul_f32 v114, v114, v225\n" \
"v_mfma_f32_16x16x32_bf16 v[156:159], a[28:31], v[12:15], 0\n" \
"ds_read_b128 a[20:23], v240 offset:14336\n" \
"v_mul_f32 v115, v115, v225\n" \
"v_mul_f32 v116, v116, v225\n" \
"v_mul_f32 v117, v117, v225\n" \
"v_mfma_f32_16x16x32_bf16 v[156:159], a[24:27], v[4:7], v[156:159]\n" \
"s_waitcnt lgkmcnt(4)\n" \
"v_mul_f32 v118, v118, v225\n" \
"v_mul_f32 v119, v119, v225\n" \
"v_cvt_pk_bf16_f32 v212, v112, v113\n" \
"v_cvt_pk_bf16_f32 v213, v114, v115\n" \
"v_mfma_f32_16x16x32_bf16 v[160:163], a[0:3], v[0:3], 0\n" \
"ds_read_b128 a[24:27], v240 offset:7168\n" \
"v_cvt_pk_bf16_f32 v214, v116, v117\n" \
"v_cvt_pk_bf16_f32 v215, v118, v119\n" \
"v_mul_f32 v120, v120, v225\n" \
"v_mfma_f32_16x16x32_bf16 v[160:163], a[4:7], v[8:11], v[160:163]\n" \
"buffer_store_dwordx4 v[212:215], v248, s[32:35], s19 offen offset:1024 sc1\n" \
"v_mul_f32 v121, v121, v225\n" \
"v_mul_f32 v122, v122, v225\n" \
"v_mfma_f32_16x16x32_bf16 v[168:171], a[4:7], v[12:15], 0\n" \
"ds_read_b128 a[28:31], v240 offset:15360\n" \
"v_mul_f32 v123, v123, v225\n" \
"v_mul_f32 v124, v124, v225\n" \
"v_mul_f32 v125, v125, v225\n" \
"v_mfma_f32_16x16x32_bf16 v[168:171], a[0:3], v[4:7], v[168:171]\n" \
"s_waitcnt lgkmcnt(4)\n" \
"v_mul_f32 v126, v126, v225\n" \
"v_mul_f32 v127, v127, v225\n" \
"v_cvt_pk_bf16_f32 v220, v120, v121\n" \
"v_cvt_pk_bf16_f32 v221, v122, v123\n" \
"v_mfma_f32_16x16x32_bf16 v[164:167], a[8:11], v[0:3], 0\n" \
"ds_read_b128 a[0:3], v240 offset:16384\n" \
"v_cvt_pk_bf16_f32 v222, v124, v125\n" \
"v_cvt_pk_bf16_f32 v223, v126, v127\n" \
"v_mov_b32 v252, s66\n" \
"v_mfma_f32_16x16x32_bf16 v[164:167], a[12:15], v[8:11], v[164:167]\n" \
"buffer_store_dwordx4 v[220:223], v248, s[32:35], s19 offen offset:3072 sc1\n" \
"v_mfma_f32_16x16x32_bf16 v[172:175], a[12:15], v[12:15], 0\n" \
"s_waitcnt vmcnt(13)\n" \
"ds_read_b128 a[4:7], v240 offset:24576\n" \
"v_cmp_ne_u64 vcc, v[230:231], v[234:235]\n" \
"v_mfma_f32_16x16x32_bf16 v[172:175], a[8:11], v[4:7], v[172:175]\n" \
"s_waitcnt lgkmcnt(4)\n" \
"v_lshlrev_b32 v252, 2, v252\n" \
"v_mov_b32 v253, 1\n" \
"v_mfma_f32_16x16x32_bf16 v[176:179], a[16:19], v[0:3], 0\n" \
"ds_read_b128 a[8:11], v240 offset:17408\n" \
"v_and_b32 v254, 15, v251\n" \
"v_mfma_f32_16x16x32_bf16 v[176:179], a[20:23], v[8:11], v[176:179]\n" \
"s_cbranch_vccnz .Lspg_%=\n" \
".Lsbg_%=:\n" \
"v_lshlrev_b32 v254, 6, v254\n" \
"v_mfma_f32_16x16x32_bf16 v[184:187], a[20:23], v[12:15], 0\n" \
"ds_read_b128 a[12:15], v240 offset:25600\n" \
"v_and_or_b32 v250, v251, 48, v254\n" \
"v_mfma_f32_16x16x32_bf16 v[184:187], a[16:19], v[4:7], v[184:187]\n" \
"s_waitcnt lgkmcnt(4)\n" \
"v_mov_b32 v254, v255\n" \
"v_mfma_f32_16x16x32_bf16 v[180:183], a[24:27], v[0:3], 0\n" \
"ds_read_b128 a[16:19], v240 offset:18432\n" \
"v_mfma_f32_16x16x32_bf16 v[180:183], a[28:31], v[8:11], v[180:183]\n" \
"v_mfma_f32_16x16x32_bf16 v[188:191], a[28:31], v[12:15], 0\n" \
"ds_read_b128 a[20:23], v240 offset:26624\n" \
"v_mfma_f32_16x16x32_bf16 v[188:191], a[24:27], v[4:7], v[188:191]\n" \
"s_waitcnt lgkmcnt(4)\n" \
"v_mfma_f32_16x16x32_bf16 v[128:131], a[0:3], v[16:19], v[128:131]\n" \
"ds_read_b128 a[24:27], v240 offset:19456\n" \
"v_mfma_f32_16x16x32_bf16 v[128:131], a[4:7], v[24:27], v[128:131]\n" \
"v_mfma_f32_16x16x32_bf16 v[136:139], a[4:7], v[28:31], v[136:139]\n" \
"ds_read_b128 a[28:31], v240 offset:27648\n" \
"v_mfma_f32_16x16x32_bf16 v[136:139], a[0:3], v[20:23], v[136:139]\n" \
"s_waitcnt lgkmcnt(4)\n" \
"v_mfma_f32_16x16x32_bf16 v[132:135], a[8:11], v[16:19], v[132:135]\n" \
"ds_read_b128 a[0:3], v240 offset:20480\n" \
"v_mfma_f32_16x16x32_bf16 v[132:135], a[12:15], v[24:27], v[132:135]\n" \
"v_mfma_f32_16x16x32_bf16 v[140:143], a[12:15], v[28:31], v[140:143]\n" \
"ds_read_b128 a[4:7], v240 offset:28672\n" \
"v_mfma_f32_16x16x32_bf16 v[140:143], a[8:11], v[20:23], v[140:143]\n" \
"s_waitcnt lgkmcnt(4)\n" \
"v_mfma_f32_16x16x32_bf16 v[144:147], a[16:19], v[16:19], v[144:147]\n" \
"ds_read_b128 a[8:11], v240 offset:21504\n" \
"v_mfma_f32_16x16x32_bf16 v[144:147], a[20:23], v[24:27], v[144:147]\n" \
".Lst1_5_%=:\n" \
"s_cbranch_execz .Lst1_9_%=\n" \
"v_mfma_f32_16x16x32_bf16 v[152:155], a[20:23], v[28:31], v[152:155]\n" \
"ds_read_b128 a[12:15], v240 offset:29696\n" \
"v_mfma_f32_16x16x32_bf16 v[152:155], a[16:19], v[20:23], v[152:155]\n" \
"s_waitcnt lgkmcnt(4)\n" \
"v_mfma_f32_16x16x32_bf16 v[148:151], a[24:27], v[16:19], v[148:151]\n" \
"ds_read_b128 a[16:19], v240 offset:22528\n" \
"v_mfma_f32_16x16x32_bf16 v[148:151], a[28:31], v[24:27], v[148:151]\n" \
"v_mfma_f32_16x16x32_bf16 v[156:159], a[28:31], v[28:31], v[156:159]\n" \
"ds_read_b128 a[20:23], v240 offset:30720\n" \
"v_mfma_f32_16x16x32_bf16 v[156:159], a[24:27], v[20:23], v[156:159]\n" \
"s_waitcnt lgkmcnt(4)\n" \
"v_mfma_f32_16x16x32_bf16 v[160:163], a[0:3], v[16:19], v[160:163]\n" \
"s_waitcnt vmcnt(12)\n" \
"s_barrier\n" \
"ds_read_b128 a[24:27], v240 offset:23552\n" \
"v_mfma_f32_16x16x32_bf16 v[160:163], a[4:7], v[24:27], v[160:163]\n" \
"s_lshl2_add_u32 s70, s69, %[wid]\n" \
"s_lshl_b32 s70, s70, 12\n" \
"s_add_u32 s70, s70, 77594624\n" \
"v_mfma_f32_16x16x32_bf16 v[168:171], a[4:7], v[28:31], v[168:171]\n" \
"ds_read_b128 a[28:31], v240 offset:31744\n" \
"v_mfma_f32_16x16x32_bf16 v[168:171], a[0:3], v[20:23], v[168:171]\n" \
"s_waitcnt lgkmcnt(4)\n" \
"buffer_load_dwordx4 a[68:71], v248, s[32:35], s70 offen offset:0 sc1\n" \
"v_mfma_f32_16x16x32_bf16 v[164:167], a[8:11], v[16:19], v[164:167]\n" \
"ds_read_b128 a[0:3], v241 offset:0\n" \
"v_mfma_f32_16x16x32_bf16 v[164:167], a[12:15], v[24:27], v[164:167]\n" \
"v_mfma_f32_16x16x32_bf16 v[172:175], a[12:15], v[28:31], v[172:175]\n" \
"ds_read_b128 a[4:7], v241 offset:8192\n" \
"v_mfma_f32_16x16x32_bf16 v[172:175], a[8:11], v[20:23], v[172:175]\n" \
"s_waitcnt lgkmcnt(4)\n" \
"buffer_load_dwordx4 a[76:79], v248, s[32:35], s70 offen offset:1024 sc1\n" \
"v_mfma_f32_16x16x32_bf16 v[176:179], a[16:19], v[16:19], v[176:179]\n" \
"ds_read_b128 a[8:11], v241 offset:1024\n" \
"v_mfma_f32_16x16x32_bf16 v[176:179], a[20:23], v[24:27], v[176:179]\n" \
"v_mfma_f32_16x16x32_bf16 v[184:187], a[20:23], v[28:31], v[184:187]\n" \
"ds_read_b128 a[12:15], v241 offset:9216\n" \
"v_mfma_f32_16x16x32_bf16 v[184:187], a[16:19], v[20:23], v[184:187]\n" \
"s_waitcnt lgkmcnt(4)\n" \
"buffer_load_dwordx4 a[84:87], v248, s[32:35], s70 offen offset:2048 sc1\n" \
"v_mfma_f32_16x16x32_bf16 v[180:183], a[24:27], v[16:19], v[180:183]\n" \
"ds_read_b128 a[16:19], v241 offset:2048\n" \
"v_mfma_f32_16x16x32_bf16 v[180:183], a[28:31], v[24:27], v[180:183]\n" \
"v_mfma_f32_16x16x32_bf16 v[188:191], a[28:31], v[28:31], v[188:191]\n" \
"ds_read_b128 a[20:23], v241 offset:10240\n" \
"v_mfma_f32_16x16x32_bf16 v[188:191], a[24:27], v[20:23], v[188:191]\n" \
"s_waitcnt lgkmcnt(4)\n" \
"buffer_load_dwordx4 a[92:95], v248, s[32:35], s70 offen offset:3072 sc1\n" \
"v_lshlrev_b32 v231, 2, v251\n" \
"v_mfma_f32_16x16x32_bf16 v[128:131], a[0:3], v[32:35], v[128:131]\n" \
"ds_read_b128 a[24:27], v241 offset:3072\n" \
"v_mfma_f32_16x16x32_bf16 v[128:131], a[4:7], v[40:43], v[128:131]\n" \
"v_mfma_f32_16x16x32_bf16 v[136:139], a[4:7], v[44:47], v[136:139]\n" \
"ds_read_b128 a[28:31], v241 offset:11264\n" \
"v_mfma_f32_16x16x32_bf16 v[136:139], a[0:3], v[36:39], v[136:139]\n" \
"s_waitcnt lgkmcnt(4)\n" \
"buffer_load_dword a100, v231, s[60:63], s79 offen offset:0\n" \
"v_mfma_f32_16x16x32_bf16 v[132:135], a[8:11], v[32:35], v[132:135]\n" \
"ds_read_b128 a[0:3], v241 offset:16384\n" \
"v_mfma_f32_16x16x32_bf16 v[132:135], a[12:15], v[40:43], v[132:135]\n" \
"v_mfma_f32_16x16x32_bf16 v[140:143], a[12:15], v[44:47], v[140:143]\n" \
"ds_read_b128 a[4:7], v241 offset:24576\n" \
"v_mfma_f32_16x16x32_bf16 v[140:143], a[8:11], v[36:39], v[140:143]\n" \
"s_waitcnt lgkmcnt(4)\n" \
"buffer_load_dword a101, v231, s[60:63], s79 offen offset:1024\n" \
"v_mfma_f32_16x16x32_bf16 v[144:147], a[16:19], v[32:35], v[144:147]\n" \
"ds_read_b128 a[8:11], v241 offset:17408\n" \
"v_mfma_f32_16x16x32_bf16 v[144:147], a[20:23], v[40:43], v[144:147]\n" \
"v_mfma_f32_16x16x32_bf16 v[152:155], a[20:23], v[44:47], v[152:155]\n" \
"ds_read_b128 a[12:15], v241 offset:25600\n" \
"v_mfma_f32_16x16x32_bf16 v[152:155], a[16:19], v[36:39], v[152:155]\n" \
"s_waitcnt lgkmcnt(4)\n" \
"buffer_load_dword a102, v231, s[60:63], s79 offen offset:2048\n" \
"v_mfma_f32_16x16x32_bf16 v[148:151], a[24:27], v[32:35], v[148:151]\n" \
"ds_read_b128 a[16:19], v241 offset:18432\n" \
"v_mfma_f32_16x16x32_bf16 v[148:151], a[28:31], v[40:43], v[148:151]\n" \
"v_mfma_f32_16x16x32_bf16 v[156:159], a[28:31], v[44:47], v[156:159]\n" \
"ds_read_b128 a[20:23], v241 offset:26624\n" \
"v_mfma_f32_16x16x32_bf16 v[156:159], a[24:27], v[36:39], v[156:159]\n" \
"s_waitcnt lgkmcnt(4)\n" \
"s_sub_u32 s41, s31, 8192\n" \
"buffer_load_dword a103, v231, s[60:63], s79 offen offset:3072\n" \
"v_mfma_f32_16x16x32_bf16 v[128:131], a[0:3], v[48:51], v[128:131]\n" \
"ds_read_b128 a[24:27], v241 offset:19456\n" \
"v_mfma_f32_16x16x32_bf16 v[128:131], a[4:7], v[56:59], v[128:131]\n" \
"s_waitcnt vmcnt(8)\n" \
"buffer_store_dword v253, v252, s[32:35], s41 offen sc1\n" \
"v_mfma_f32_16x16x32_bf16 v[136:139], a[4:7], v[60:63], v[136:139]\n" \
"ds_read_b128 a[28:31], v241 offset:27648\n" \
"v_mfma_f32_16x16x32_bf16 v[136:139], a[0:3], v[52:55], v[136:139]\n" \
"s_waitcnt lgkmcnt(4)\n" \
"v_mfma_f32_16x16x32_bf16 v[132:135], a[8:11], v[48:51], v[132:135]\n" \
"ds_read_b128 a[0:3], v244 offset:0\n" \
"v_mfma_f32_16x16x32_bf16 v[132:135], a[12:15], v[56:59], v[132:135]\n" \
"buffer_load_dword a108, v231, s[60:63], s80 offen offset:0\n" \
"v_mfma_f32_16x16x32_bf16 v[140:143], a[12:15], v[60:63], v[140:143]\n" \
"ds_read_b128 a[4:7], v244 offset:8192\n" \
"v_mfma_f32_16x16x32_bf16 v[140:143], a[8:11], v[52:55], v[140:143]\n" \
"s_waitcnt lgkmcnt(4)\n" \
"v_mfma_f32_16x16x32_bf16 v[144:147], a[16:19], v[48:51], v[144:147]\n" \
"ds_read_b128 a[8:11], v244 offset:1024\n" \
"v_mfma_f32_16x16x32_bf16 v[144:147], a[20:23], v[56:59], v[144:147]\n" \
"buffer_load_dword a109, v231, s[60:63], s80 offen offset:1024\n" \
"v_mfma_f32_16x16x32_bf16 v[152:155], a[20:23], v[60:63], v[152:155]\n" \
"s_lshl_b32 s17, s67, 18\n" \
"s_sub_u32 s17, 1966080, s17\n" \
"ds_read_b128 a[12:15], v244 offset:9216\n" \
"v_mfma_f32_16x16x32_bf16 v[152:155], a[16:19], v[52:55], v[152:155]\n" \
"s_waitcnt lgkmcnt(4)\n" \
"s_sub_u32 s17, s36, s17\n" \
"s_add_u32 s16, s17, 4096\n" \
"v_mfma_f32_16x16x32_bf16 v[148:151], a[24:27], v[48:51], v[148:151]\n" \
"s_add_u32 s21, s36, 4096\n" \
"ds_read_b128 a[16:19], v244 offset:2048\n" \
"v_and_b32 v246, 7, v251\n" \
"v_mfma_f32_16x16x32_bf16 v[148:151], a[28:31], v[56:59], v[148:151]\n" \
"buffer_load_dword a110, v231, s[60:63], s80 offen offset:2048\n" \
"v_lshlrev_b32 v246, 8, v246\n" \
"v_mfma_f32_16x16x32_bf16 v[156:159], a[28:31], v[60:63], v[156:159]\n" \
"ds_read_b128 a[20:23], v244 offset:10240\n" \
"v_lshlrev_b32 v255, 2, v251\n" \
"v_mfma_f32_16x16x32_bf16 v[156:159], a[24:27], v[52:55], v[156:159]\n" \
"s_waitcnt lgkmcnt(4)\n" \
"v_and_b32 v252, 0x7c, v255\n" \
"v_mfma_f32_16x16x32_bf16 v[128:131], a[0:3], a[64:67], v[128:131]\n" \
"ds_read_b128 a[24:27], v244 offset:3072\n" \
"v_mfma_f32_16x16x32_bf16 v[128:131], a[4:7], a[80:83], v[128:131]\n" \
"buffer_load_dword a111, v231, s[60:63], s80 offen offset:3072\n" \
"v_mfma_f32_16x16x32_bf16 v[136:139], a[4:7], a[88:91], v[136:139]\n" \
"ds_read_b128 a[28:31], v244 offset:11264\n" \
"v_mfma_f32_16x16x32_bf16 v[136:139], a[0:3], a[72:75], v[136:139]\n" \
"s_waitcnt lgkmcnt(4)\n" \
"v_mfma_f32_16x16x32_bf16 v[132:135], a[8:11], a[64:67], v[132:135]\n" \
"ds_read_b128 a[0:3], v249 offset:0\n" \
"v_mfma_f32_16x16x32_bf16 v[132:135], a[12:15], a[80:83], v[132:135]\n" \
"v_mfma_f32_16x16x32_bf16 v[140:143], a[12:15], a[88:91], v[140:143]\n" \
"ds_read_b128 a[4:7], v249 offset:8192\n" \
"v_mfma_f32_16x16x32_bf16 v[140:143], a[8:11], a[72:75], v[140:143]\n" \
"s_waitcnt lgkmcnt(4)\n" \
"v_mfma_f32_16x16x32_bf16 v[144:147], a[16:19], a[64:67], v[144:147]\n" \
"ds_read_b128 a[8:11], v249 offset:1024\n" \
"v_mfma_f32_16x16x32_bf16 v[144:147], a[20:23], a[80:83], v[144:147]\n" \
"v_mfma_f32_16x16x32_bf16 v[152:155], a[20:23], a[88:91], v[152:155]\n" \
"ds_read_b128 a[12:15], v249 offset:9216\n" \
"v_mfma_f32_16x16x32_bf16 v[152:155], a[16:19], a[72:75], v[152:155]\n" \
"s_waitcnt lgkmcnt(4)\n" \
"v_mfma_f32_16x16x32_bf16 v[148:151], a[24:27], a[64:67], v[148:151]\n" \
"ds_read_b128 a[16:19], v249 offset:2048\n" \
"v_mfma_f32_16x16x32_bf16 v[148:151], a[28:31], a[80:83], v[148:151]\n" \
"v_mfma_f32_16x16x32_bf16 v[156:159], a[28:31], a[88:91], v[156:159]\n" \
"ds_read_b128 a[20:23], v249 offset:10240\n" \
"v_mfma_f32_16x16x32_bf16 v[156:159], a[24:27], a[72:75], v[156:159]\n" \
"s_waitcnt lgkmcnt(4)\n" \
"v_mfma_f32_16x16x32_bf16 v[128:131], a[0:3], a[96:99], v[128:131]\n" \
"ds_read_b128 a[24:27], v249 offset:3072\n" \
"v_mfma_f32_16x16x32_bf16 v[128:131], a[4:7], a[112:115], v[128:131]\n" \
"v_mfma_f32_16x16x32_bf16 v[136:139], a[4:7], a[116:119], v[136:139]\n" \
"ds_read_b128 a[28:31], v249 offset:11264\n" \
"v_mfma_f32_16x16x32_bf16 v[136:139], a[0:3], a[104:107], v[136:139]\n" \
"s_waitcnt lgkmcnt(4)\n" \
"v_mfma_f32_16x16x32_bf16 v[132:135], a[8:11], a[96:99], v[132:135]\n" \
"ds_read_b128 a[0:3], v239 offset:0\n" \
"v_mfma_f32_16x16x32_bf16 v[132:135], a[12:15], a[112:115], v[132:135]\n" \
"v_mfma_f32_16x16x32_bf16 v[140:143], a[12:15], a[116:119], v[140:143]\n" \
"ds_read_b128 a[4:7], v239 offset:8192\n" \
"v_mfma_f32_16x16x32_bf16 v[140:143], a[8:11], a[104:107], v[140:143]\n" \
"s_waitcnt lgkmcnt(4)\n" \
"v_mfma_f32_16x16x32_bf16 v[144:147], a[16:19], a[96:99], v[144:147]\n" \
"ds_read_b128 a[8:11], v239 offset:1024\n" \
"v_mfma_f32_16x16x32_bf16 v[144:147], a[20:23], a[112:115], v[144:147]\n" \
"v_mfma_f32_16x16x32_bf16 v[152:155], a[20:23], a[116:119], v[152:155]\n" \
"ds_read_b128 a[12:15], v239 offset:9216\n" \
"v_mfma_f32_16x16x32_bf16 v[152:155], a[16:19], a[104:107], v[152:155]\n" \
"s_waitcnt lgkmcnt(4)\n" \
"v_mfma_f32_16x16x32_bf16 v[148:151], a[24:27], a[96:99], v[148:151]\n" \
"ds_read_b128 a[16:19], v239 offset:2048\n" \
"v_mfma_f32_16x16x32_bf16 v[148:151], a[28:31], a[112:115], v[148:151]\n" \
"v_mfma_f32_16x16x32_bf16 v[156:159], a[28:31], a[116:119], v[156:159]\n" \
"ds_read_b128 a[20:23], v239 offset:10240\n" \
"v_mfma_f32_16x16x32_bf16 v[156:159], a[24:27], a[104:107], v[156:159]\n" \
"s_waitcnt lgkmcnt(4)\n" \
"v_mfma_f32_16x16x32_bf16 v[128:131], a[0:3], a[120:123], v[128:131]\n" \
"ds_read_b128 a[24:27], v239 offset:3072\n" \
"v_mfma_f32_16x16x32_bf16 v[128:131], a[4:7], a[128:131], v[128:131]\n" \
"v_mfma_f32_16x16x32_bf16 v[136:139], a[4:7], a[132:135], v[136:139]\n" \
"ds_read_b128 a[28:31], v239 offset:11264\n" \
"v_mfma_f32_16x16x32_bf16 v[136:139], a[0:3], a[124:127], v[136:139]\n" \
"s_waitcnt lgkmcnt(4)\n" \
"v_mfma_f32_16x16x32_bf16 v[132:135], a[8:11], a[120:123], v[132:135]\n" \
"ds_read_b128 a[0:3], v239 offset:16384\n" \
"v_mfma_f32_16x16x32_bf16 v[132:135], a[12:15], a[128:131], v[132:135]\n" \
"v_mfma_f32_16x16x32_bf16 v[140:143], a[12:15], a[132:135], v[140:143]\n" \
"ds_read_b128 a[4:7], v239 offset:24576\n" \
"v_mfma_f32_16x16x32_bf16 v[140:143], a[8:11], a[124:127], v[140:143]\n" \
"s_waitcnt lgkmcnt(4)\n" \
"v_mfma_f32_16x16x32_bf16 v[144:147], a[16:19], a[120:123], v[144:147]\n" \
"ds_read_b128 a[8:11], v239 offset:17408\n" \
"v_mfma_f32_16x16x32_bf16 v[144:147], a[20:23], a[128:131], v[144:147]\n" \
"v_mfma_f32_16x16x32_bf16 v[152:155], a[20:23], a[132:135], v[152:155]\n" \
"ds_read_b128 a[12:15], v239 offset:25600\n" \
"v_mfma_f32_16x16x32_bf16 v[152:155], a[16:19], a[124:127], v[152:155]\n" \
"s_waitcnt lgkmcnt(4)\n" \
"v_mfma_f32_16x16x32_bf16 v[148:151], a[24:27], a[120:123], v[148:151]\n" \
"ds_read_b128 a[16:19], v239 offset:18432\n" \
"v_mfma_f32_16x16x32_bf16 v[148:151], a[28:31], a[128:131], v[148:151]\n" \
"v_mfma_f32_16x16x32_bf16 v[156:159], a[28:31], a[132:135], v[156:159]\n" \
"ds_read_b128 a[20:23], v239 offset:26624\n" \
"v_mfma_f32_16x16x32_bf16 v[156:159], a[24:27], a[124:127], v[156:159]\n" \
"s_waitcnt lgkmcnt(4)\n" \
"v_mfma_f32_16x16x32_bf16 v[128:131], a[0:3], a[136:139], v[128:131]\n" \
"ds_read_b128 a[24:27], v239 offset:19456\n" \
"v_mfma_f32_16x16x32_bf16 v[128:131], a[4:7], a[144:147], v[128:131]\n" \
"v_mfma_f32_16x16x32_bf16 v[136:139], a[4:7], a[148:151], v[136:139]\n" \
"ds_read_b128 a[28:31], v239 offset:27648\n" \
"v_mfma_f32_16x16x32_bf16 v[136:139], a[0:3], a[140:143], v[136:139]\n" \
"s_waitcnt lgkmcnt(4)\n" \
"v_mfma_f32_16x16x32_bf16 v[132:135], a[8:11], a[136:139], v[132:135]\n" \
"s_lshl2_add_u32 s18, s67, %[wid]\n" \
"s_lshl_b32 s18, s18, 11\n" \
"ds_read_b128 a[0:3], v241 offset:4096\n" \
"v_mfma_f32_16x16x32_bf16 v[132:135], a[12:15], a[144:147], v[132:135]\n" \
"s_add_u32 s18, s18, 589824\n" \
"s_lshl2_add_u32 s39, s68, s18\n" \
"s_lshl_b32 s20, s64, 8\n" \
"v_mfma_f32_16x16x32_bf16 v[140:143], a[12:15], a[148:151], v[140:143]\n" \
"s_add_u32 s18, s18, s20\n" \
"ds_read_b128 a[4:7], v241 offset:12288\n" \
"v_mfma_f32_16x16x32_bf16 v[140:143], a[8:11], a[140:143], v[140:143]\n" \
"s_waitcnt lgkmcnt(4)\n" \
"v_mfma_f32_16x16x32_bf16 v[144:147], a[16:19], a[136:139], v[144:147]\n" \
"ds_read_b128 a[8:11], v241 offset:5120\n" \
"v_mfma_f32_16x16x32_bf16 v[144:147], a[20:23], a[144:147], v[144:147]\n" \
"v_mfma_f32_16x16x32_bf16 v[152:155], a[20:23], a[148:151], v[152:155]\n" \
"ds_read_b128 a[12:15], v241 offset:13312\n" \
"v_mfma_f32_16x16x32_bf16 v[152:155], a[16:19], a[140:143], v[152:155]\n" \
"s_waitcnt lgkmcnt(4)\n" \
"v_mfma_f32_16x16x32_bf16 v[148:151], a[24:27], a[136:139], v[148:151]\n" \
"ds_read_b128 a[16:19], v241 offset:20480\n" \
"v_mfma_f32_16x16x32_bf16 v[148:151], a[28:31], a[144:147], v[148:151]\n" \
"v_cvt_pk_bf16_f32 v128, v128, v129\n" \
"v_cvt_pk_bf16_f32 v129, v130, v131\n" \
"v_mfma_f32_16x16x32_bf16 v[156:159], a[28:31], a[148:151], v[156:159]\n" \
"ds_read_b128 a[20:23], v241 offset:28672\n" \
"v_cvt_pk_bf16_f32 v130, v132, v133\n" \
"v_mfma_f32_16x16x32_bf16 v[156:159], a[24:27], a[140:143], v[156:159]\n" \
"s_waitcnt lgkmcnt(4)\n" \
"v_cvt_pk_bf16_f32 v131, v134, v135\n" \
"v_mfma_f32_16x16x32_bf16 v[160:163], a[0:3], v[32:35], v[160:163]\n" \
"ds_read_b128 a[24:27], v241 offset:21504\n" \
"v_mfma_f32_16x16x32_bf16 v[160:163], a[4:7], v[40:43], v[160:163]\n" \
"v_mfma_f32_16x16x32_bf16 v[168:171], a[4:7], v[44:47], v[168:171]\n" \
"ds_read_b128 a[28:31], v241 offset:29696\n" \
"v_mfma_f32_16x16x32_bf16 v[168:171], a[0:3], v[36:39], v[168:171]\n" \
"s_waitcnt lgkmcnt(4)\n" \
"buffer_store_dwordx4 v[128:131], v250, s[32:35], s17 offen offset:0 sc1\n" \
"v_mfma_f32_16x16x32_bf16 v[164:167], a[8:11], v[32:35], v[164:167]\n" \
"ds_read_b128 a[0:3], v244 offset:4096\n" \
"v_mfma_f32_16x16x32_bf16 v[164:167], a[12:15], v[40:43], v[164:167]\n" \
"buffer_load_dword v247, v254, s[32:35], s41 offen sc1\n" \
"v_mfma_f32_16x16x32_bf16 v[172:175], a[12:15], v[44:47], v[172:175]\n" \
"ds_read_b128 a[4:7], v244 offset:12288\n" \
"v_mfma_f32_16x16x32_bf16 v[172:175], a[8:11], v[36:39], v[172:175]\n" \
"s_waitcnt lgkmcnt(4)\n" \
"v_cvt_pk_bf16_f32 v136, v136, v137\n" \
"v_cvt_pk_bf16_f32 v137, v138, v139\n" \
"v_mfma_f32_16x16x32_bf16 v[160:163], a[16:19], v[48:51], v[160:163]\n" \
"ds_read_b128 a[8:11], v244 offset:5120\n" \
"v_cvt_pk_bf16_f32 v138, v140, v141\n" \
"v_mfma_f32_16x16x32_bf16 v[160:163], a[20:23], v[56:59], v[160:163]\n" \
"v_cvt_pk_bf16_f32 v139, v142, v143\n" \
"v_mfma_f32_16x16x32_bf16 v[168:171], a[20:23], v[60:63], v[168:171]\n" \
"ds_read_b128 a[12:15], v244 offset:13312\n" \
"v_mfma_f32_16x16x32_bf16 v[168:171], a[16:19], v[52:55], v[168:171]\n" \
"s_waitcnt lgkmcnt(4)\n" \
"v_mfma_f32_16x16x32_bf16 v[164:167], a[24:27], v[48:51], v[164:167]\n" \
"ds_read_b128 a[16:19], v249 offset:4096\n" \
"v_mfma_f32_16x16x32_bf16 v[164:167], a[28:31], v[56:59], v[164:167]\n" \
"v_mfma_f32_16x16x32_bf16 v[172:175], a[28:31], v[60:63], v[172:175]\n" \
"ds_read_b128 a[20:23], v249 offset:12288\n" \
"v_mfma_f32_16x16x32_bf16 v[172:175], a[24:27], v[52:55], v[172:175]\n" \
"s_waitcnt lgkmcnt(4)\n" \
"buffer_store_dwordx4 v[136:139], v250, s[32:35], s16 offen offset:0 sc1\n" \
"v_mfma_f32_16x16x32_bf16 v[160:163], a[0:3], a[64:67], v[160:163]\n" \
"ds_read_b128 a[24:27], v249 offset:5120\n" \
"v_mfma_f32_16x16x32_bf16 v[160:163], a[4:7], a[80:83], v[160:163]\n" \
"v_mfma_f32_16x16x32_bf16 v[168:171], a[4:7], a[88:91], v[168:171]\n" \
"ds_read_b128 a[28:31], v249 offset:13312\n" \
"v_mfma_f32_16x16x32_bf16 v[168:171], a[0:3], a[72:75], v[168:171]\n" \
"s_waitcnt lgkmcnt(4)\n" \
"v_cvt_pk_bf16_f32 v144, v144, v145\n" \
"v_cvt_pk_bf16_f32 v145, v146, v147\n" \
"v_mfma_f32_16x16x32_bf16 v[164:167], a[8:11], a[64:67], v[164:167]\n" \
"ds_read_b128 a[0:3], v239 offset:4096\n" \
"v_cvt_pk_bf16_f32 v146, v148, v149\n" \
"v_mfma_f32_16x16x32_bf16 v[164:167], a[12:15], a[80:83], v[164:167]\n" \
"v_cvt_pk_bf16_f32 v147, v150, v151\n" \
"v_mfma_f32_16x16x32_bf16 v[172:175], a[12:15], a[88:91], v[172:175]\n" \
"ds_read_b128 a[4:7], v239 offset:12288\n" \
"v_mfma_f32_16x16x32_bf16 v[172:175], a[8:11], a[72:75], v[172:175]\n" \
"s_waitcnt lgkmcnt(4)\n" \
"v_mfma_f32_16x16x32_bf16 v[160:163], a[16:19], a[96:99], v[160:163]\n" \
"ds_read_b128 a[8:11], v239 offset:5120\n" \
"v_mfma_f32_16x16x32_bf16 v[160:163], a[20:23], a[112:115], v[160:163]\n" \
"v_mfma_f32_16x16x32_bf16 v[168:171], a[20:23], a[116:119], v[168:171]\n" \
"ds_read_b128 a[12:15], v239 offset:13312\n" \
"v_mfma_f32_16x16x32_bf16 v[168:171], a[16:19], a[104:107], v[168:171]\n" \
"s_waitcnt lgkmcnt(4)\n" \
"buffer_store_dwordx4 v[144:147], v250, s[32:35], s17 offen offset:1024 sc1\n" \
"v_mfma_f32_16x16x32_bf16 v[164:167], a[24:27], a[96:99], v[164:167]\n" \
"ds_read_b128 a[16:19], v239 offset:20480\n" \
"v_mfma_f32_16x16x32_bf16 v[164:167], a[28:31], a[112:115], v[164:167]\n" \
"v_mfma_f32_16x16x32_bf16 v[172:175], a[28:31], a[116:119], v[172:175]\n" \
"ds_read_b128 a[20:23], v239 offset:28672\n" \
"v_mfma_f32_16x16x32_bf16 v[172:175], a[24:27], a[104:107], v[172:175]\n" \
"s_waitcnt lgkmcnt(4)\n" \
"v_cvt_pk_bf16_f32 v152, v152, v153\n" \
"v_cvt_pk_bf16_f32 v153, v154, v155\n" \
"v_mfma_f32_16x16x32_bf16 v[160:163], a[0:3], a[120:123], v[160:163]\n" \
"ds_read_b128 a[24:27], v239 offset:21504\n" \
"v_cvt_pk_bf16_f32 v154, v156, v157\n" \
"v_mfma_f32_16x16x32_bf16 v[160:163], a[4:7], a[128:131], v[160:163]\n" \
"v_cvt_pk_bf16_f32 v155, v158, v159\n" \
"v_mfma_f32_16x16x32_bf16 v[168:171], a[4:7], a[132:135], v[168:171]\n" \
"ds_read_b128 a[28:31], v239 offset:29696\n" \
"v_mfma_f32_16x16x32_bf16 v[168:171], a[0:3], a[124:127], v[168:171]\n" \
"s_waitcnt lgkmcnt(4)\n" \
"v_mfma_f32_16x16x32_bf16 v[164:167], a[8:11], a[120:123], v[164:167]\n" \
"s_sub_u32 s86, 15, s67\n" \
"s_lshl_b32 s90, s86, 2\n" \
"ds_read_b128 a[0:3], v241 offset:6144\n" \
"v_mfma_f32_16x16x32_bf16 v[164:167], a[12:15], a[128:131], v[164:167]\n" \
"s_add_u32 s90, s90, %[wid]\n" \
"s_lshl_b32 s90, s90, 3\n" \
"s_lshr_b32 s26, s68, 4\n" \
"v_mfma_f32_16x16x32_bf16 v[172:175], a[12:15], a[132:135], v[172:175]\n" \
"s_add_u32 s90, s90, s26\n" \
"s_lshl_b32 s90, s90, 6\n" \
"ds_read_b128 a[4:7], v241 offset:14336\n" \
"v_mfma_f32_16x16x32_bf16 v[172:175], a[8:11], a[124:127], v[172:175]\n" \
"s_waitcnt lgkmcnt(4)\n" \
"buffer_store_dwordx4 v[152:155], v250, s[32:35], s16 offen offset:1024 sc1\n" \
"v_mfma_f32_16x16x32_bf16 v[160:163], a[16:19], a[136:139], v[160:163]\n" \
"s_and_b32 s27, s68, 15\n" \
"s_add_u32 s90, s90, s27\n" \
"ds_read_b128 a[8:11], v241 offset:7168\n" \
"v_mfma_f32_16x16x32_bf16 v[160:163], a[20:23], a[144:147], v[160:163]\n" \
"s_lshl_b32 s90, s90, 6\n" \
"s_add_u32 s90, s90, 59768832\n" \
"s_mul_i32 s26, s26, 7\n" \
"v_mfma_f32_16x16x32_bf16 v[168:171], a[20:23], a[148:151], v[168:171]\n" \
"s_xor_b32 s26, s26, %[wid]\n" \
"s_lshl3_add_u32 s86, s86, s26\n" \
"ds_read_b128 a[12:15], v241 offset:15360\n" \
"v_mfma_f32_16x16x32_bf16 v[168:171], a[16:19], a[140:143], v[168:171]\n" \
"s_waitcnt lgkmcnt(4)\n" \
"s_lshl4_add_u32 s86, s86, s27\n" \
"s_lshl2_add_u32 s74, s67, %[wid]\n" \
"v_mfma_f32_16x16x32_bf16 v[164:167], a[24:27], a[136:139], v[164:167]\n" \
"s_lshl_b32 s74, s74, 7\n" \
"s_add_u32 s74, s74, 94208\n" \
"ds_read_b128 a[16:19], v241 offset:22528\n" \
"v_mfma_f32_16x16x32_bf16 v[164:167], a[28:31], a[144:147], v[164:167]\n" \
"v_mov_b32 v245, 1\n" \
"v_mfma_f32_16x16x32_bf16 v[172:175], a[28:31], a[148:151], v[172:175]\n" \
"ds_read_b128 a[20:23], v241 offset:30720\n" \
"v_mfma_f32_16x16x32_bf16 v[172:175], a[24:27], a[140:143], v[172:175]\n" \
"s_waitcnt lgkmcnt(4)\n" \
"v_mfma_f32_16x16x32_bf16 v[176:179], a[0:3], v[32:35], v[176:179]\n" \
"ds_read_b128 a[24:27], v241 offset:23552\n" \
"v_mfma_f32_16x16x32_bf16 v[176:179], a[4:7], v[40:43], v[176:179]\n" \
"v_mfma_f32_16x16x32_bf16 v[184:187], a[4:7], v[44:47], v[184:187]\n" \
"ds_read_b128 a[28:31], v241 offset:31744\n" \
"v_mfma_f32_16x16x32_bf16 v[184:187], a[0:3], v[36:39], v[184:187]\n" \
"s_waitcnt lgkmcnt(4)\n" \
"v_mfma_f32_16x16x32_bf16 v[180:183], a[8:11], v[32:35], v[180:183]\n" \
"ds_read_b128 a[0:3], v244 offset:6144\n" \
"v_mfma_f32_16x16x32_bf16 v[180:183], a[12:15], v[40:43], v[180:183]\n" \
"v_cvt_pk_bf16_f32 v160, v160, v161\n" \
"v_cvt_pk_bf16_f32 v161, v162, v163\n" \
"v_mfma_f32_16x16x32_bf16 v[188:191], a[12:15], v[44:47], v[188:191]\n" \
"ds_read_b128 a[4:7], v244 offset:14336\n" \
"v_cvt_pk_bf16_f32 v162, v164, v165\n" \
"v_mfma_f32_16x16x32_bf16 v[188:191], a[8:11], v[36:39], v[188:191]\n" \
"s_waitcnt lgkmcnt(4)\n" \
"v_cvt_pk_bf16_f32 v163, v166, v167\n" \
"v_mfma_f32_16x16x32_bf16 v[176:179], a[16:19], v[48:51], v[176:179]\n" \
"s_waitcnt vmcnt(3)\n" \
"ds_read_b128 a[8:11], v244 offset:7168\n" \
"v_cmp_eq_u32 vcc, 0, v247\n" \
"v_mfma_f32_16x16x32_bf16 v[176:179], a[20:23], v[56:59], v[176:179]\n" \
"s_add_u32 s38, s66, 1\n" \
"s_and_b32 s38, s38, 3\n" \
"v_mfma_f32_16x16x32_bf16 v[184:187], a[20:23], v[60:63], v[184:187]\n" \
"s_lshl_b32 s38, s38, 13\n" \
"ds_read_b128 a[12:15], v244 offset:15360\n" \
"v_mfma_f32_16x16x32_bf16 v[184:187], a[16:19], v[52:55], v[184:187]\n" \
"s_waitcnt lgkmcnt(4)\n" \
"s_cbranch_vccnz .LspcA_%=\n" \
".LsbcA_%=:\n" \
"v_mfma_f32_16x16x32_bf16 v[180:183], a[24:27], v[48:51], v[180:183]\n" \
"s_add_u32 s38, s38, s37\n" \
"ds_read_b128 a[16:19], v249 offset:6144\n" \
"v_mfma_f32_16x16x32_bf16 v[180:183], a[28:31], v[56:59], v[180:183]\n" \
"buffer_store_dwordx4 v[160:163], v250, s[32:35], s17 offen offset:2048 sc1\n" \
"v_mfma_f32_16x16x32_bf16 v[188:191], a[28:31], v[60:63], v[188:191]\n" \
"ds_read_b128 a[20:23], v249 offset:14336\n" \
"v_mfma_f32_16x16x32_bf16 v[188:191], a[24:27], v[52:55], v[188:191]\n" \
"s_waitcnt lgkmcnt(4)\n" \
"v_cvt_pk_bf16_f32 v168, v168, v169\n" \
"v_cvt_pk_bf16_f32 v169, v170, v171\n" \
"v_mfma_f32_16x16x32_bf16 v[176:179], a[0:3], a[64:67], v[176:179]\n" \
"ds_read_b128 a[24:27], v249 offset:7168\n" \
"v_cvt_pk_bf16_f32 v170, v172, v173\n" \
"v_mfma_f32_16x16x32_bf16 v[176:179], a[4:7], a[80:83], v[176:179]\n" \
"buffer_load_dwordx4 a[192:195], v248, s[32:35], s38 offen offset:0 sc1\n" \
"v_cvt_pk_bf16_f32 v171, v174, v175\n" \
"v_mfma_f32_16x16x32_bf16 v[184:187], a[4:7], a[88:91], v[184:187]\n" \
"ds_read_b128 a[28:31], v249 offset:15360\n" \
"v_mfma_f32_16x16x32_bf16 v[184:187], a[0:3], a[72:75], v[184:187]\n" \
"s_waitcnt lgkmcnt(4)\n" \
"v_mfma_f32_16x16x32_bf16 v[180:183], a[8:11], a[64:67], v[180:183]\n" \
"ds_read_b128 a[0:3], v239 offset:6144\n" \
"v_mfma_f32_16x16x32_bf16 v[180:183], a[12:15], a[80:83], v[180:183]\n" \
"buffer_load_dwordx4 a[196:199], v248, s[32:35], s38 offen offset:1024 sc1\n" \
"v_mfma_f32_16x16x32_bf16 v[188:191], a[12:15], a[88:91], v[188:191]\n" \
"ds_read_b128 a[4:7], v239 offset:14336\n" \
"v_mfma_f32_16x16x32_bf16 v[188:191], a[8:11], a[72:75], v[188:191]\n" \
"s_waitcnt lgkmcnt(4)\n" \
"buffer_store_dwordx4 v[168:171], v250, s[32:35], s16 offen offset:2048 sc1\n" \
"v_mfma_f32_16x16x32_bf16 v[176:179], a[16:19], a[96:99], v[176:179]\n" \
"ds_read_b128 a[8:11], v239 offset:7168\n" \
"v_mfma_f32_16x16x32_bf16 v[176:179], a[20:23], a[112:115], v[176:179]\n" \
"v_mfma_f32_16x16x32_bf16 v[184:187], a[20:23], a[116:119], v[184:187]\n" \
"ds_read_b128 a[12:15], v239 offset:15360\n" \
"v_mfma_f32_16x16x32_bf16 v[184:187], a[16:19], a[104:107], v[184:187]\n" \
"s_waitcnt lgkmcnt(4)\n" \
"buffer_load_dwordx4 a[200:203], v248, s[32:35], s38 offen offset:2048 sc1\n" \
"v_mfma_f32_16x16x32_bf16 v[180:183], a[24:27], a[96:99], v[180:183]\n" \
"ds_read_b128 a[16:19], v239 offset:22528\n" \
"v_mfma_f32_16x16x32_bf16 v[180:183], a[28:31], a[112:115], v[180:183]\n" \
"v_mfma_f32_16x16x32_bf16 v[188:191], a[28:31], a[116:119], v[188:191]\n" \
"ds_read_b128 a[20:23], v239 offset:30720\n" \
"v_mfma_f32_16x16x32_bf16 v[188:191], a[24:27], a[104:107], v[188:191]\n" \
"s_waitcnt lgkmcnt(4)\n" \
"buffer_load_dwordx4 a[204:207], v248, s[32:35], s38 offen offset:3072 sc1\n" \
"v_mfma_f32_16x16x32_bf16 v[176:179], a[0:3], a[120:123], v[176:179]\n" \
"s_add_u32 s38, s38, 4096\n" \
"ds_read_b128 a[24:27], v239 offset:23552\n" \
"v_mfma_f32_16x16x32_bf16 v[176:179], a[4:7], a[128:131], v[176:179]\n" \
"s_mov_b64 exec, 1\n" \
"buffer_atomic_add v245, off, s[32:35], s74 sc0 sc1\n" \
"s_mov_b64 exec, -1\n" \
"v_mfma_f32_16x16x32_bf16 v[184:187], a[4:7], a[132:135], v[184:187]\n" \
"ds_read_b128 a[28:31], v239 offset:31744\n" \
"v_mfma_f32_16x16x32_bf16 v[184:187], a[0:3], a[124:127], v[184:187]\n" \
"s_waitcnt lgkmcnt(4)\n" \
"v_mfma_f32_16x16x32_bf16 v[180:183], a[8:11], a[120:123], v[180:183]\n" \
"ds_read_b128 a[0:3], v240 offset:0\n" \
"v_mfma_f32_16x16x32_bf16 v[180:183], a[12:15], a[128:131], v[180:183]\n" \
"buffer_load_dwordx4 a[208:211], v248, s[32:35], s38 offen offset:0 sc1\n" \
"v_mfma_f32_16x16x32_bf16 v[188:191], a[12:15], a[132:135], v[188:191]\n" \
"ds_read_b128 a[4:7], v240 offset:8192\n" \
"v_mfma_f32_16x16x32_bf16 v[188:191], a[8:11], a[124:127], v[188:191]\n" \
"s_waitcnt lgkmcnt(4)\n" \
"v_mfma_f32_16x16x32_bf16 v[176:179], a[16:19], a[136:139], v[176:179]\n" \
"ds_read_b128 a[8:11], v240 offset:1024\n" \
"v_mfma_f32_16x16x32_bf16 v[176:179], a[20:23], a[144:147], v[176:179]\n" \
"buffer_load_dwordx4 a[212:215], v248, s[32:35], s38 offen offset:1024 sc1\n" \
"v_mfma_f32_16x16x32_bf16 v[184:187], a[20:23], a[148:151], v[184:187]\n" \
"ds_read_b128 a[12:15], v240 offset:9216\n" \
"v_mfma_f32_16x16x32_bf16 v[184:187], a[16:19], a[140:143], v[184:187]\n" \
"s_waitcnt lgkmcnt(4)\n" \
"v_mfma_f32_16x16x32_bf16 v[180:183], a[24:27], a[136:139], v[180:183]\n" \
"ds_read_b128 a[16:19], v240 offset:2048\n" \
"v_mfma_f32_16x16x32_bf16 v[180:183], a[28:31], a[144:147], v[180:183]\n" \
"buffer_load_dwordx4 a[216:219], v248, s[32:35], s38 offen offset:2048 sc1\n" \
"v_mfma_f32_16x16x32_bf16 v[188:191], a[28:31], a[148:151], v[188:191]\n" \
"ds_read_b128 a[20:23], v240 offset:10240\n" \
"v_mfma_f32_16x16x32_bf16 v[188:191], a[24:27], a[140:143], v[188:191]\n" \
"s_waitcnt lgkmcnt(4)\n" \
"v_mfma_f32_16x16x32_bf16 v[64:67], a[0:3], v[192:195], 0\n" \
"ds_read_b128 a[24:27], v240 offset:3072\n" \
"v_mfma_f32_16x16x32_bf16 v[64:67], a[4:7], v[200:203], v[64:67]\n" \
"buffer_load_dwordx4 a[220:223], v248, s[32:35], s38 offen offset:3072 sc1\n" \
"v_mfma_f32_16x16x32_bf16 v[72:75], a[4:7], v[204:207], 0\n" \
"s_add_u32 s38, s66, 2\n" \
"s_and_b32 s38, s38, 3\n" \
"ds_read_b128 a[28:31], v240 offset:11264\n" \
"v_mfma_f32_16x16x32_bf16 v[72:75], a[0:3], v[196:199], v[72:75]\n" \
"s_waitcnt lgkmcnt(4)\n" \
"s_lshl_b32 s38, s38, 13\n" \
"s_add_u32 s38, s38, s37\n" \
"v_mfma_f32_16x16x32_bf16 v[68:71], a[8:11], v[192:195], 0\n" \
"ds_read_b128 a[0:3], v240 offset:4096\n" \
"v_mfma_f32_16x16x32_bf16 v[68:71], a[12:15], v[200:203], v[68:71]\n" \
"buffer_load_dwordx4 a[224:227], v248, s[32:35], s38 offen offset:0 sc1\n" \
"v_cvt_pk_bf16_f32 v176, v176, v177\n" \
"v_mfma_f32_16x16x32_bf16 v[76:79], a[12:15], v[204:207], 0\n" \
"ds_read_b128 a[4:7], v240 offset:12288\n" \
"v_cvt_pk_bf16_f32 v177, v178, v179\n" \
"v_mfma_f32_16x16x32_bf16 v[76:79], a[8:11], v[196:199], v[76:79]\n" \
"s_waitcnt lgkmcnt(4)\n" \
"v_cvt_pk_bf16_f32 v178, v180, v181\n" \
"v_cvt_pk_bf16_f32 v179, v182, v183\n" \
"v_mfma_f32_16x16x32_bf16 v[80:83], a[16:19], v[192:195], 0\n" \
"ds_read_b128 a[8:11], v240 offset:5120\n" \
"v_mfma_f32_16x16x32_bf16 v[80:83], a[20:23], v[200:203], v[80:83]\n" \
"buffer_load_dwordx4 a[228:231], v248, s[32:35], s38 offen offset:1024 sc1\n" \
"v_mfma_f32_16x16x32_bf16 v[88:91], a[20:23], v[204:207], 0\n" \
"ds_read_b128 a[12:15], v240 offset:13312\n" \
"v_mfma_f32_16x16x32_bf16 v[88:91], a[16:19], v[196:199], v[88:91]\n" \
"s_waitcnt lgkmcnt(4)\n" \
"buffer_store_dwordx4 v[176:179], v250, s[32:35], s17 offen offset:3072 sc1\n" \
"v_mfma_f32_16x16x32_bf16 v[84:87], a[24:27], v[192:195], 0\n" \
"ds_read_b128 a[16:19], v240 offset:6144\n" \
"v_mfma_f32_16x16x32_bf16 v[84:87], a[28:31], v[200:203], v[84:87]\n" \
"v_mfma_f32_16x16x32_bf16 v[92:95], a[28:31], v[204:207], 0\n" \
"ds_read_b128 a[20:23], v240 offset:14336\n" \
"v_mfma_f32_16x16x32_bf16 v[92:95], a[24:27], v[196:199], v[92:95]\n" \
"s_waitcnt lgkmcnt(4)\n" \
"buffer_load_dwordx4 a[232:235], v248, s[32:35], s38 offen offset:2048 sc1\n" \
"v_cvt_pk_bf16_f32 v184, v184, v185\n" \
"v_mfma_f32_16x16x32_bf16 v[96:99], a[0:3], v[192:195], 0\n" \
"ds_read_b128 a[24:27], v240 offset:7168\n" \
"v_cvt_pk_bf16_f32 v185, v186, v187\n" \
"v_mfma_f32_16x16x32_bf16 v[96:99], a[4:7], v[200:203], v[96:99]\n" \
"v_cvt_pk_bf16_f32 v186, v188, v189\n" \
"v_cvt_pk_bf16_f32 v187, v190, v191\n" \
"v_mfma_f32_16x16x32_bf16 v[104:107], a[4:7], v[204:207], 0\n" \
"ds_read_b128 a[28:31], v240 offset:15360\n" \
"v_mfma_f32_16x16x32_bf16 v[104:107], a[0:3], v[196:199], v[104:107]\n" \
"s_waitcnt lgkmcnt(4)\n" \
"buffer_load_dwordx4 a[236:239], v248, s[32:35], s38 offen offset:3072 sc1\n" \
"v_mfma_f32_16x16x32_bf16 v[100:103], a[8:11], v[192:195], 0\n" \
"s_add_u32 s38, s38, 4096\n" \
"ds_read_b128 a[0:3], v240 offset:16384\n" \
"v_mfma_f32_16x16x32_bf16 v[100:103], a[12:15], v[200:203], v[100:103]\n" \
"v_mfma_f32_16x16x32_bf16 v[108:111], a[12:15], v[204:207], 0\n" \
"ds_read_b128 a[4:7], v240 offset:24576\n" \
"v_mfma_f32_16x16x32_bf16 v[108:111], a[8:11], v[196:199], v[108:111]\n" \
"s_waitcnt lgkmcnt(4)\n" \
"buffer_store_dwordx4 v[184:187], v250, s[32:35], s16 offen offset:3072 sc1\n" \
"v_mfma_f32_16x16x32_bf16 v[112:115], a[16:19], v[192:195], 0\n" \
"ds_read_b128 a[8:11], v240 offset:17408\n" \
"v_lshrrev_b32 v188, 2, v251\n" \
"v_mfma_f32_16x16x32_bf16 v[112:115], a[20:23], v[200:203], v[112:115]\n" \
"v_lshlrev_b32 v188, 6, v188\n" \
"v_and_b32 v189, 3, v251\n" \
"v_mfma_f32_16x16x32_bf16 v[120:123], a[20:23], v[204:207], 0\n" \
"ds_read_b128 a[12:15], v240 offset:25600\n" \
"v_lshl_or_b32 v188, v189, 3, v188\n" \
"v_mfma_f32_16x16x32_bf16 v[120:123], a[16:19], v[196:199], v[120:123]\n" \
"s_waitcnt lgkmcnt(4)\n" \
"s_lshl_b32 s19, %[wid], 6\n" \
"buffer_load_dwordx4 a[240:243], v248, s[32:35], s38 offen offset:0 sc1\n" \
"v_mfma_f32_16x16x32_bf16 v[116:119], a[24:27], v[192:195], 0\n" \
"ds_read_b128 a[16:19], v240 offset:18432\n" \
"v_add_u32 v191, s19, v251\n" \
"v_mfma_f32_16x16x32_bf16 v[116:119], a[28:31], v[200:203], v[116:119]\n" \
"v_lshlrev_b32 v190, 3, v191\n" \
"v_mfma_f32_16x16x32_bf16 v[124:127], a[28:31], v[204:207], 0\n" \
"ds_read_b128 a[20:23], v240 offset:26624\n" \
"v_mfma_f32_16x16x32_bf16 v[124:127], a[24:27], v[196:199], v[124:127]\n" \
"s_waitcnt lgkmcnt(4)\n" \
"buffer_load_dwordx4 a[244:247], v248, s[32:35], s38 offen offset:1024 sc1\n" \
"v_mfma_f32_16x16x32_bf16 v[64:67], a[0:3], v[208:211], v[64:67]\n" \
"ds_read_b128 a[24:27], v240 offset:19456\n" \
"v_mfma_f32_16x16x32_bf16 v[64:67], a[4:7], v[216:219], v[64:67]\n" \
"v_mfma_f32_16x16x32_bf16 v[72:75], a[4:7], v[220:223], v[72:75]\n" \
"ds_read_b128 a[28:31], v240 offset:27648\n" \
"v_mfma_f32_16x16x32_bf16 v[72:75], a[0:3], v[212:215], v[72:75]\n" \
".Lst1_6_%=:\n" \
"s_cbranch_execz .Lst1_10_%=\n" \
"s_waitcnt lgkmcnt(4)\n" \
"buffer_load_dwordx4 a[248:251], v248, s[32:35], s38 offen offset:2048 sc1\n" \
"v_mfma_f32_16x16x32_bf16 v[68:71], a[8:11], v[208:211], v[68:71]\n" \
"ds_read_b128 a[0:3], v240 offset:20480\n" \
"v_mfma_f32_16x16x32_bf16 v[68:71], a[12:15], v[216:219], v[68:71]\n" \
"v_mfma_f32_16x16x32_bf16 v[76:79], a[12:15], v[220:223], v[76:79]\n" \
"ds_read_b128 a[4:7], v240 offset:28672\n" \
"v_mfma_f32_16x16x32_bf16 v[76:79], a[8:11], v[212:215], v[76:79]\n" \
"s_waitcnt lgkmcnt(4)\n" \
"buffer_load_dwordx4 a[252:255], v248, s[32:35], s38 offen offset:3072 sc1\n" \
"v_mfma_f32_16x16x32_bf16 v[80:83], a[16:19], v[208:211], v[80:83]\n" \
"s_add_u32 s38, s66, 3\n" \
"s_and_b32 s38, s38, 3\n" \
"ds_read_b128 a[8:11], v240 offset:21504\n" \
"v_mfma_f32_16x16x32_bf16 v[80:83], a[20:23], v[216:219], v[80:83]\n" \
"s_lshl_b32 s38, s38, 13\n" \
"s_add_u32 s38, s38, s37\n" \
"v_mfma_f32_16x16x32_bf16 v[88:91], a[20:23], v[220:223], v[88:91]\n" \
"ds_read_b128 a[12:15], v240 offset:29696\n" \
"v_mfma_f32_16x16x32_bf16 v[88:91], a[16:19], v[212:215], v[88:91]\n" \
"s_waitcnt lgkmcnt(4)\n" \
"buffer_load_dwordx4 a[64:67], v248, s[32:35], s38 offen offset:0 sc1\n" \
"v_mfma_f32_16x16x32_bf16 v[84:87], a[24:27], v[208:211], v[84:87]\n" \
"s_waitcnt vmcnt(15)\n" \
"ds_read_b128 a[16:19], v240 offset:22528\n" \
"v_readfirstlane_b32 s81, v245\n" \
"v_mfma_f32_16x16x32_bf16 v[84:87], a[28:31], v[216:219], v[84:87]\n" \
"v_mfma_f32_16x16x32_bf16 v[92:95], a[28:31], v[220:223], v[92:95]\n" \
"s_mov_b32 s40, s81\n" \
"s_min_u32 s25, s40, 8\n" \
"ds_read_b128 a[20:23], v240 offset:30720\n" \
"v_mfma_f32_16x16x32_bf16 v[92:95], a[24:27], v[212:215], v[92:95]\n" \
"s_waitcnt lgkmcnt(4)\n" \
"s_add_u32 s25, s25, s40\n" \
"buffer_load_dwordx4 a[72:75], v248, s[32:35], s38 offen offset:1024 sc1\n" \
"v_mfma_f32_16x16x32_bf16 v[96:99], a[0:3], v[208:211], v[96:99]\n" \
"s_lshl_b32 s95, s67, 2\n" \
"s_add_u32 s95, s95, %[wid]\n" \
"ds_read_b128 a[24:27], v240 offset:23552\n" \
"v_mfma_f32_16x16x32_bf16 v[96:99], a[4:7], v[216:219], v[96:99]\n" \
"s_lshl_b32 s95, s95, 3\n" \
"s_lshr_b32 s26, s25, 4\n" \
"s_add_u32 s95, s95, s26\n" \
"v_mfma_f32_16x16x32_bf16 v[104:107], a[4:7], v[220:223], v[104:107]\n" \
"s_lshl_b32 s95, s95, 6\n" \
"s_and_b32 s27, s25, 15\n" \
"ds_read_b128 a[28:31], v240 offset:31744\n" \
"v_mfma_f32_16x16x32_bf16 v[104:107], a[0:3], v[212:215], v[104:107]\n" \
"s_waitcnt lgkmcnt(4)\n" \
"s_add_u32 s95, s95, s27\n" \
"buffer_load_dwordx4 a[80:83], v248, s[32:35], s38 offen offset:2048 sc1\n" \
"v_mfma_f32_16x16x32_bf16 v[100:103], a[8:11], v[208:211], v[100:103]\n" \
"s_lshl_b32 s95, s95, 6\n" \
"s_add_u32 s95, s95, 59768832\n" \
"ds_read_b128 a[0:3], v241 offset:0\n" \
"v_mfma_f32_16x16x32_bf16 v[100:103], a[12:15], v[216:219], v[100:103]\n" \
"s_mul_i32 s26, s26, 7\n" \
"s_xor_b32 s26, s26, %[wid]\n" \
"s_lshl3_add_u32 s85, s67, s26\n" \
"v_mfma_f32_16x16x32_bf16 v[108:111], a[12:15], v[220:223], v[108:111]\n" \
"s_lshl4_add_u32 s85, s85, s27\n" \
"s_mov_b32 s87, 1\n" \
"ds_read_b128 a[4:7], v241 offset:8192\n" \
"v_mfma_f32_16x16x32_bf16 v[108:111], a[8:11], v[212:215], v[108:111]\n" \
"s_waitcnt lgkmcnt(4)\n" \
"s_mov_b32 s88, 1\n" \
"buffer_load_dwordx4 a[88:91], v248, s[32:35], s38 offen offset:3072 sc1\n" \
"v_mfma_f32_16x16x32_bf16 v[112:115], a[16:19], v[208:211], v[112:115]\n" \
"s_add_u32 s38, s38, 4096\n" \
"s_mov_b32 s75, 0\n" \
"ds_read_b128 a[8:11], v241 offset:1024\n" \
"v_mfma_f32_16x16x32_bf16 v[112:115], a[20:23], v[216:219], v[112:115]\n" \
"s_cmp_lt_u32 s40, 8\n" \
"s_cselect_b32 s84, 0, 1073741824\n" \
"v_mfma_f32_16x16x32_bf16 v[120:123], a[20:23], v[220:223], v[120:123]\n" \
"ds_read_b128 a[12:15], v241 offset:9216\n" \
"v_mfma_f32_16x16x32_bf16 v[120:123], a[16:19], v[212:215], v[120:123]\n" \
"s_waitcnt lgkmcnt(4)\n" \
"buffer_load_dwordx4 a[96:99], v248, s[32:35], s38 offen offset:0 sc1\n" \
"v_mfma_f32_16x16x32_bf16 v[116:119], a[24:27], v[208:211], v[116:119]\n" \
"ds_read_b128 a[16:19], v241 offset:2048\n" \
"v_mfma_f32_16x16x32_bf16 v[116:119], a[28:31], v[216:219], v[116:119]\n" \
"s_cmp_lt_u32 s40, 24\n" \
"s_cselect_b32 s85, s85, 1048576\n" \
"s_cselect_b32 s74, 0, 0x11111111\n" \
"v_mfma_f32_16x16x32_bf16 v[124:127], a[28:31], v[220:223], v[124:127]\n" \
"ds_read_b128 a[20:23], v241 offset:10240\n" \
"v_mfma_f32_16x16x32_bf16 v[124:127], a[24:27], v[212:215], v[124:127]\n" \
"s_waitcnt lgkmcnt(4)\n" \
"buffer_load_dwordx4 a[104:107], v248, s[32:35], s38 offen offset:1024 sc1\n" \
"v_mfma_f32_16x16x32_bf16 v[64:67], a[0:3], a[192:195], v[64:67]\n" \
"ds_read_b128 a[24:27], v241 offset:3072\n" \
"v_mfma_f32_16x16x32_bf16 v[64:67], a[4:7], a[200:203], v[64:67]\n" \
"v_mfma_f32_16x16x32_bf16 v[72:75], a[4:7], a[204:207], v[72:75]\n" \
"ds_read_b128 a[28:31], v241 offset:11264\n" \
"v_mfma_f32_16x16x32_bf16 v[72:75], a[0:3], a[196:199], v[72:75]\n" \
"s_waitcnt vmcnt(10) lgkmcnt(4)\n" \
"buffer_store_dword v253, v246, s[32:35], s39 offen sc1\n" \
"v_mfma_f32_16x16x32_bf16 v[68:71], a[8:11], a[192:195], v[68:71]\n" \
"ds_read_b128 a[0:3], v241 offset:16384\n" \
"v_mfma_f32_16x16x32_bf16 v[68:71], a[12:15], a[200:203], v[68:71]\n" \
"v_mfma_f32_16x16x32_bf16 v[76:79], a[12:15], a[204:207], v[76:79]\n" \
"ds_read_b128 a[4:7], v241 offset:24576\n" \
"v_lshrrev_b32 v214, 4, v251\n" \
"v_mfma_f32_16x16x32_bf16 v[76:79], a[8:11], a[196:199], v[76:79]\n" \
"s_waitcnt lgkmcnt(4)\n" \
"buffer_load_dwordx4 a[112:115], v248, s[32:35], s38 offen offset:2048 sc1\n" \
"v_lshlrev_b32 v214, 13, v214\n" \
"v_mfma_f32_16x16x32_bf16 v[80:83], a[16:19], a[192:195], v[80:83]\n" \
"ds_read_b128 a[8:11], v241 offset:17408\n" \
"v_and_or_b32 v214, v248, 48, v214\n" \
"v_mfma_f32_16x16x32_bf16 v[80:83], a[20:23], a[200:203], v[80:83]\n" \
"v_bfe_u32 v208, v251, 2, 2\n" \
"v_lshl_or_b32 v214, v208, 10, v214\n" \
"v_mfma_f32_16x16x32_bf16 v[88:91], a[20:23], a[204:207], v[88:91]\n" \
"ds_read_b128 a[12:15], v241 offset:25600\n" \
"v_mov_b32 v208, 0\n" \
"v_mfma_f32_16x16x32_bf16 v[88:91], a[16:19], a[196:199], v[88:91]\n" \
"s_waitcnt lgkmcnt(4)\n" \
"buffer_load_dwordx4 a[116:119], v248, s[32:35], s38 offen offset:3072 sc1\n" \
"v_mov_b32 v209, 0\n" \
"v_mfma_f32_16x16x32_bf16 v[84:87], a[24:27], a[192:195], v[84:87]\n" \
"s_lshl_b32 s38, s86, 10\n" \
"ds_read_b128 a[16:19], v241 offset:18432\n" \
"v_mov_b32 v212, 0\n" \
"v_mfma_f32_16x16x32_bf16 v[84:87], a[28:31], a[200:203], v[84:87]\n" \
"v_mov_b32 v213, 0\n" \
"v_bfe_u32 v210, v251, 2, 2\n" \
"v_mfma_f32_16x16x32_bf16 v[92:95], a[28:31], a[204:207], v[92:95]\n" \
"ds_read_b128 a[20:23], v241 offset:26624\n" \
"v_lshrrev_b32 v211, 4, v251\n" \
"v_mfma_f32_16x16x32_bf16 v[92:95], a[24:27], a[196:199], v[92:95]\n" \
"s_waitcnt lgkmcnt(4)\n" \
"buffer_load_dwordx2 v[128:129], v188, s[44:47], s38 offen offset:0\n" \
"v_cmp_eq_u32 s[24:25], v210, v211\n" \
"v_mfma_f32_16x16x32_bf16 v[64:67], a[0:3], a[208:211], v[64:67]\n" \
"ds_read_b128 a[24:27], v241 offset:19456\n" \
"v_and_b32 v210, 1, v251\n" \
"v_mfma_f32_16x16x32_bf16 v[64:67], a[4:7], a[216:219], v[64:67]\n" \
"v_lshlrev_b32 v210, 4, v210\n" \
"v_mov_b32 v211, 0x3f80\n" \
"v_mfma_f32_16x16x32_bf16 v[72:75], a[4:7], a[220:223], v[72:75]\n" \
"ds_read_b128 a[28:31], v241 offset:27648\n" \
"v_lshlrev_b32 v210, v210, v211\n" \
"v_mfma_f32_16x16x32_bf16 v[72:75], a[0:3], a[212:215], v[72:75]\n" \
"s_waitcnt lgkmcnt(4)\n" \
"buffer_load_dwordx2 v[130:131], v188, s[44:47], s38 offen offset:32\n" \
"v_cndmask_b32 v210, 0, v210, s[24:25]\n" \
"v_mfma_f32_16x16x32_bf16 v[68:71], a[8:11], a[208:211], v[68:71]\n" \
"ds_read_b128 a[0:3], v244 offset:0\n" \
"v_bfe_i32 v215, v251, 1, 1\n" \
"v_mfma_f32_16x16x32_bf16 v[68:71], a[12:15], a[216:219], v[68:71]\n" \
"v_and_b32 v211, v215, v210\n" \
"v_xor_b32 v210, v210, v211\n" \
"v_mfma_f32_16x16x32_bf16 v[76:79], a[12:15], a[220:223], v[76:79]\n" \
"ds_read_b128 a[4:7], v244 offset:8192\n" \
"v_mfma_f32_16x16x32_bf16 v[76:79], a[8:11], a[212:215], v[76:79]\n" \
"s_waitcnt lgkmcnt(4)\n" \
"buffer_load_dwordx4 v[172:175], v248, s[32:35], 0 offen offset:0 sc1\n" \
"v_mfma_f32_16x16x32_bf16 v[80:83], a[16:19], a[208:211], v[80:83]\n" \
"ds_read_b128 a[8:11], v244 offset:1024\n" \
"v_mfma_f32_16x16x32_bf16 v[80:83], a[20:23], a[216:219], v[80:83]\n" \
"v_mfma_f32_16x16x32_bf16 v[88:91], a[20:23], a[220:223], v[88:91]\n" \
"ds_read_b128 a[12:15], v244 offset:9216\n" \
"v_mfma_f32_16x16x32_bf16 v[88:91], a[16:19], a[212:215], v[88:91]\n" \
"s_waitcnt lgkmcnt(4)\n" \
"buffer_load_dwordx4 v[176:179], v248, s[32:35], 0 offen offset:1024 sc1\n" \
"v_mfma_f32_16x16x32_bf16 v[84:87], a[24:27], a[208:211], v[84:87]\n" \
"ds_read_b128 a[16:19], v244 offset:2048\n" \
"v_mfma_f32_16x16x32_bf16 v[84:87], a[28:31], a[216:219], v[84:87]\n" \
"v_mfma_f32_16x16x32_bf16 v[92:95], a[28:31], a[220:223], v[92:95]\n" \
"ds_read_b128 a[20:23], v244 offset:10240\n" \
"v_mfma_f32_16x16x32_bf16 v[92:95], a[24:27], a[212:215], v[92:95]\n" \
"s_waitcnt lgkmcnt(4)\n" \
"buffer_load_dwordx4 v[180:183], v248, s[32:35], 0 offen offset:2048 sc1\n" \
"v_mfma_f32_16x16x32_bf16 v[64:67], a[0:3], a[224:227], v[64:67]\n" \
"ds_read_b128 a[24:27], v244 offset:3072\n" \
"v_mfma_f32_16x16x32_bf16 v[64:67], a[4:7], a[232:235], v[64:67]\n" \
"v_mfma_f32_16x16x32_bf16 v[72:75], a[4:7], a[236:239], v[72:75]\n" \
"ds_read_b128 a[28:31], v244 offset:11264\n" \
"v_mfma_f32_16x16x32_bf16 v[72:75], a[0:3], a[228:231], v[72:75]\n" \
"s_waitcnt lgkmcnt(4)\n" \
"buffer_load_dwordx4 v[184:187], v248, s[32:35], 0 offen offset:3072 sc1\n" \
"v_mfma_f32_16x16x32_bf16 v[68:71], a[8:11], a[224:227], v[68:71]\n" \
"ds_read_b128 a[0:3], v249 offset:0\n" \
"v_mfma_f32_16x16x32_bf16 v[68:71], a[12:15], a[232:235], v[68:71]\n" \
"v_mfma_f32_16x16x32_bf16 v[76:79], a[12:15], a[236:239], v[76:79]\n" \
"ds_read_b128 a[4:7], v249 offset:8192\n" \
"v_mfma_f32_16x16x32_bf16 v[76:79], a[8:11], a[228:231], v[76:79]\n" \
"s_waitcnt lgkmcnt(4)\n" \
"global_load_dwordx2 v[168:169], v188, s[96:97] offset:0\n" \
"v_mfma_f32_16x16x32_bf16 v[80:83], a[16:19], a[224:227], v[80:83]\n" \
"ds_read_b128 a[8:11], v249 offset:1024\n" \
"v_mfma_f32_16x16x32_bf16 v[80:83], a[20:23], a[232:235], v[80:83]\n" \
"v_mfma_f32_16x16x32_bf16 v[88:91], a[20:23], a[236:239], v[88:91]\n" \
"ds_read_b128 a[12:15], v249 offset:9216\n" \
"v_mfma_f32_16x16x32_bf16 v[88:91], a[16:19], a[228:231], v[88:91]\n" \
"s_waitcnt lgkmcnt(4)\n" \
"global_load_dwordx2 v[170:171], v188, s[96:97] offset:32\n" \
"v_mfma_f32_16x16x32_bf16 v[84:87], a[24:27], a[224:227], v[84:87]\n" \
"s_lshl_b32 s38, s85, 10\n" \
"ds_read_b128 a[16:19], v249 offset:2048\n" \
"v_mfma_f32_16x16x32_bf16 v[84:87], a[28:31], a[232:235], v[84:87]\n" \
"v_mfma_f32_16x16x32_bf16 v[92:95], a[28:31], a[236:239], v[92:95]\n" \
"s_waitcnt vmcnt(17)\n" \
"ds_read_b128 a[20:23], v249 offset:10240\n" \
"v_mfma_f32_16x16x32_bf16 v[92:95], a[24:27], a[228:231], v[92:95]\n" \
"s_waitcnt lgkmcnt(4)\n" \
"buffer_load_dwordx2 a[48:49], v188, s[44:47], s38 offen offset:0\n" \
"v_mfma_f32_16x16x32_bf16 v[64:67], a[0:3], a[240:243], v[64:67]\n" \
"ds_read_b128 a[24:27], v249 offset:3072\n" \
"v_mfma_f32_16x16x32_bf16 v[64:67], a[4:7], a[248:251], v[64:67]\n" \
"v_mfma_f32_16x16x32_bf16 v[72:75], a[4:7], a[252:255], v[72:75]\n" \
"ds_read_b128 a[28:31], v249 offset:11264\n" \
"v_mfma_f32_16x16x32_bf16 v[72:75], a[0:3], a[244:247], v[72:75]\n" \
"s_waitcnt lgkmcnt(4)\n" \
"buffer_load_dwordx2 a[50:51], v188, s[44:47], s38 offen offset:32\n" \
"v_mfma_f32_16x16x32_bf16 v[68:71], a[8:11], a[240:243], v[68:71]\n" \
"ds_read_b128 a[0:3], v239 offset:0\n" \
"v_mfma_f32_16x16x32_bf16 v[68:71], a[12:15], a[248:251], v[68:71]\n" \
"buffer_load_dword v245, v252, s[32:35], s18 offen sc1\n" \
"v_mfma_f32_16x16x32_bf16 v[76:79], a[12:15], a[252:255], v[76:79]\n" \
"ds_read_b128 a[4:7], v239 offset:8192\n" \
"v_mfma_f32_16x16x32_bf16 v[76:79], a[8:11], a[244:247], v[76:79]\n" \
"s_waitcnt lgkmcnt(4)\n" \
"v_mfma_f32_16x16x32_bf16 v[80:83], a[16:19], a[240:243], v[80:83]\n" \
"ds_read_b128 a[8:11], v239 offset:1024\n" \
"v_mfma_f32_16x16x32_bf16 v[80:83], a[20:23], a[248:251], v[80:83]\n" \
"buffer_load_dwordx2 a[52:53], v188, s[44:47], s38 offen offset:1024\n" \
"v_mfma_f32_16x16x32_bf16 v[88:91], a[20:23], a[252:255], v[88:91]\n" \
"ds_read_b128 a[12:15], v239 offset:9216\n" \
"v_mfma_f32_16x16x32_bf16 v[88:91], a[16:19], a[244:247], v[88:91]\n" \
"s_waitcnt lgkmcnt(4)\n" \
"v_mfma_f32_16x16x32_bf16 v[84:87], a[24:27], a[240:243], v[84:87]\n" \
"ds_read_b128 a[16:19], v239 offset:2048\n" \
"v_mfma_f32_16x16x32_bf16 v[84:87], a[28:31], a[248:251], v[84:87]\n" \
"buffer_load_dwordx2 a[54:55], v188, s[44:47], s38 offen offset:1056\n" \
"v_mfma_f32_16x16x32_bf16 v[92:95], a[28:31], a[252:255], v[92:95]\n" \
"s_waitcnt vmcnt(18)\n" \
"ds_read_b128 a[20:23], v239 offset:10240\n" \
"v_mfma_f32_16x16x32_bf16 v[92:95], a[24:27], a[244:247], v[92:95]\n" \
"s_waitcnt lgkmcnt(4)\n" \
"v_mfma_f32_16x16x32_bf16 v[64:67], a[0:3], a[64:67], v[64:67]\n" \
"ds_read_b128 a[24:27], v239 offset:3072\n" \
"v_mfma_f32_16x16x32_bf16 v[64:67], a[4:7], a[80:83], v[64:67]\n" \
"v_mfma_f32_16x16x32_bf16 v[72:75], a[4:7], a[88:91], v[72:75]\n" \
"ds_read_b128 a[28:31], v239 offset:11264\n" \
"v_mfma_f32_16x16x32_bf16 v[72:75], a[0:3], a[72:75], v[72:75]\n" \
"s_waitcnt lgkmcnt(4)\n" \
"v_mfma_f32_16x16x32_bf16 v[68:71], a[8:11], a[64:67], v[68:71]\n" \
"ds_read_b128 a[0:3], v239 offset:16384\n" \
"v_mfma_f32_16x16x32_bf16 v[68:71], a[12:15], a[80:83], v[68:71]\n" \
"v_mfma_f32_16x16x32_bf16 v[76:79], a[12:15], a[88:91], v[76:79]\n" \
"ds_read_b128 a[4:7], v239 offset:24576\n" \
"v_mfma_f32_16x16x32_bf16 v[76:79], a[8:11], a[72:75], v[76:79]\n" \
"s_waitcnt lgkmcnt(4)\n" \
"v_mfma_f32_16x16x32_bf16 v[80:83], a[16:19], a[64:67], v[80:83]\n" \
"ds_read_b128 a[8:11], v239 offset:17408\n" \
"v_mfma_f32_16x16x32_bf16 v[80:83], a[20:23], a[80:83], v[80:83]\n" \
"v_mfma_f32_16x16x32_bf16 v[88:91], a[20:23], a[88:91], v[88:91]\n" \
"ds_read_b128 a[12:15], v239 offset:25600\n" \
"v_mfma_f32_16x16x32_bf16 v[88:91], a[16:19], a[72:75], v[88:91]\n" \
"s_waitcnt lgkmcnt(4)\n" \
"v_mfma_f32_16x16x32_bf16 v[84:87], a[24:27], a[64:67], v[84:87]\n" \
"ds_read_b128 a[16:19], v239 offset:18432\n" \
"v_mfma_f32_16x16x32_bf16 v[84:87], a[28:31], a[80:83], v[84:87]\n" \
"v_mfma_f32_16x16x32_bf16 v[92:95], a[28:31], a[88:91], v[92:95]\n" \
"s_waitcnt vmcnt(13)\n" \
"ds_read_b128 a[20:23], v239 offset:26624\n" \
"v_mfma_f32_16x16x32_bf16 v[92:95], a[24:27], a[72:75], v[92:95]\n" \
"s_waitcnt lgkmcnt(4)\n" \
"v_mfma_f32_16x16x32_bf16 v[64:67], a[0:3], a[96:99], v[64:67]\n" \
"ds_read_b128 a[24:27], v239 offset:19456\n" \
"v_mfma_f32_16x16x32_bf16 v[64:67], a[4:7], a[112:115], v[64:67]\n" \
"v_mfma_f32_16x16x32_bf16 v[72:75], a[4:7], a[116:119], v[72:75]\n" \
"ds_read_b128 a[28:31], v239 offset:27648\n" \
"v_mfma_f32_16x16x32_bf16 v[72:75], a[0:3], a[104:107], v[72:75]\n" \
"s_waitcnt lgkmcnt(4)\n" \
"v_mfma_f32_16x16x32_bf16 v[68:71], a[8:11], a[96:99], v[68:71]\n" \
"ds_read_b128 a[0:3], v241 offset:4096\n" \
"v_mfma_f32_16x16x32_bf16 v[68:71], a[12:15], a[112:115], v[68:71]\n" \
"v_mfma_f32_16x16x32_bf16 v[76:79], a[12:15], a[116:119], v[76:79]\n" \
"ds_read_b128 a[4:7], v241 offset:12288\n" \
"v_mfma_f32_16x16x32_bf16 v[76:79], a[8:11], a[104:107], v[76:79]\n" \
"s_waitcnt lgkmcnt(4)\n" \
"v_mfma_f32_16x16x32_bf16 v[80:83], a[16:19], a[96:99], v[80:83]\n" \
"ds_read_b128 a[8:11], v241 offset:5120\n" \
"v_mfma_f32_16x16x32_bf16 v[80:83], a[20:23], a[112:115], v[80:83]\n" \
"v_mfma_f32_16x16x32_bf16 v[88:91], a[20:23], a[116:119], v[88:91]\n" \
"ds_read_b128 a[12:15], v241 offset:13312\n" \
"v_mfma_f32_16x16x32_bf16 v[88:91], a[16:19], a[104:107], v[88:91]\n" \
"s_waitcnt lgkmcnt(4)\n" \
"v_mfma_f32_16x16x32_bf16 v[84:87], a[24:27], a[96:99], v[84:87]\n" \
"ds_read_b128 a[16:19], v241 offset:20480\n" \
"v_mfma_f32_16x16x32_bf16 v[84:87], a[28:31], a[112:115], v[84:87]\n" \
"v_cvt_pk_bf16_f32 v64, v64, v65\n" \
"v_cvt_pk_bf16_f32 v65, v66, v67\n" \
"v_mfma_f32_16x16x32_bf16 v[92:95], a[28:31], a[116:119], v[92:95]\n" \
"ds_read_b128 a[20:23], v241 offset:28672\n" \
"v_cvt_pk_bf16_f32 v66, v68, v69\n" \
"v_mfma_f32_16x16x32_bf16 v[92:95], a[24:27], a[104:107], v[92:95]\n" \
"s_waitcnt lgkmcnt(4)\n" \
"v_cvt_pk_bf16_f32 v67, v70, v71\n" \
"v_mfma_f32_16x16x32_bf16 v[96:99], a[0:3], a[192:195], v[96:99]\n" \
"s_waitcnt vmcnt(2)\n" \
"ds_read_b128 a[24:27], v241 offset:21504\n" \
"v_cmp_ne_u32 vcc, 0, v245\n" \
"v_mfma_f32_16x16x32_bf16 v[96:99], a[4:7], a[200:203], v[96:99]\n" \
"v_mfma_f32_16x16x32_bf16 v[104:107], a[4:7], a[204:207], v[104:107]\n" \
"ds_read_b128 a[28:31], v241 offset:29696\n" \
"v_mfma_f32_16x16x32_bf16 v[104:107], a[0:3], a[196:199], v[104:107]\n" \
"s_waitcnt lgkmcnt(4)\n" \
"s_lshr_b32 s76, vcc_lo, 1\n" \
"s_and_b32 s76, s76, vcc_lo\n" \
"v_mfma_f32_16x16x32_bf16 v[100:103], a[8:11], a[192:195], v[100:103]\n" \
"s_lshr_b32 s77, s76, 2\n" \
"s_and_b32 s76, s76, s77\n" \
"ds_read_b128 a[0:3], v244 offset:4096\n" \
"v_mfma_f32_16x16x32_bf16 v[100:103], a[12:15], a[200:203], v[100:103]\n" \
"s_and_b32 s76, s76, 0x11111111\n" \
"buffer_store_dwordx4 v[64:67], v250, s[32:35], s36 offen offset:0 sc1\n" \
"v_mfma_f32_16x16x32_bf16 v[108:111], a[12:15], a[204:207], v[108:111]\n" \
"s_andn2_b32 s76, s76, s74\n" \
"ds_read_b128 a[4:7], v244 offset:12288\n" \
"v_cvt_pk_bf16_f32 v72, v72, v73\n" \
"v_mfma_f32_16x16x32_bf16 v[108:111], a[8:11], a[196:199], v[108:111]\n" \
"s_waitcnt lgkmcnt(4)\n" \
"s_or_b32 s74, s74, s76\n" \
"v_cvt_pk_bf16_f32 v73, v74, v75\n" \
"v_mfma_f32_16x16x32_bf16 v[96:99], a[16:19], a[208:211], v[96:99]\n" \
"s_add_u32 s22, s95, 0\n" \
"ds_read_b128 a[8:11], v244 offset:5120\n" \
"v_cvt_pk_bf16_f32 v74, v76, v77\n" \
"v_mfma_f32_16x16x32_bf16 v[96:99], a[20:23], a[216:219], v[96:99]\n" \
"s_bitcmp1_b32 s76, 0\n" \
"s_cselect_b32 s22, s22, 1073741824\n" \
"v_cvt_pk_bf16_f32 v75, v78, v79\n" \
"v_mfma_f32_16x16x32_bf16 v[104:107], a[20:23], a[220:223], v[104:107]\n" \
"ds_read_b128 a[12:15], v244 offset:13312\n" \
"v_mfma_f32_16x16x32_bf16 v[104:107], a[16:19], a[212:215], v[104:107]\n" \
"s_waitcnt lgkmcnt(4)\n" \
"buffer_load_dwordx4 a[128:131], v214, s[32:35], s22 offen sc1\n" \
"v_mfma_f32_16x16x32_bf16 v[100:103], a[24:27], a[208:211], v[100:103]\n" \
"s_add_u32 s22, s22, s84\n" \
"ds_read_b128 a[16:19], v249 offset:4096\n" \
"v_mfma_f32_16x16x32_bf16 v[100:103], a[28:31], a[216:219], v[100:103]\n" \
"buffer_store_dwordx4 v[72:75], v250, s[32:35], s21 offen offset:0 sc1\n" \
"v_mfma_f32_16x16x32_bf16 v[108:111], a[28:31], a[220:223], v[108:111]\n" \
"ds_read_b128 a[20:23], v249 offset:12288\n" \
"v_mfma_f32_16x16x32_bf16 v[108:111], a[24:27], a[212:215], v[108:111]\n" \
"s_waitcnt lgkmcnt(4)\n" \
"v_cvt_pk_bf16_f32 v80, v80, v81\n" \
"v_cvt_pk_bf16_f32 v81, v82, v83\n" \
"v_mfma_f32_16x16x32_bf16 v[96:99], a[0:3], a[224:227], v[96:99]\n" \
"ds_read_b128 a[24:27], v249 offset:5120\n" \
"v_cvt_pk_bf16_f32 v82, v84, v85\n" \
"v_mfma_f32_16x16x32_bf16 v[96:99], a[4:7], a[232:235], v[96:99]\n" \
"v_cvt_pk_bf16_f32 v83, v86, v87\n" \
"v_mfma_f32_16x16x32_bf16 v[104:107], a[4:7], a[236:239], v[104:107]\n" \
"ds_read_b128 a[28:31], v249 offset:13312\n" \
"v_mfma_f32_16x16x32_bf16 v[104:107], a[0:3], a[228:231], v[104:107]\n" \
"s_waitcnt lgkmcnt(4)\n" \
"buffer_load_dwordx4 a[160:163], v214, s[32:35], s22 offen offset:64 sc1\n" \
"v_mfma_f32_16x16x32_bf16 v[100:103], a[8:11], a[224:227], v[100:103]\n" \
"s_add_u32 s22, s95, 2097152\n" \
"ds_read_b128 a[0:3], v239 offset:4096\n" \
"v_mfma_f32_16x16x32_bf16 v[100:103], a[12:15], a[232:235], v[100:103]\n" \
"s_bitcmp1_b32 s76, 4\n" \
"s_cselect_b32 s22, s22, 1073741824\n" \
"v_mfma_f32_16x16x32_bf16 v[108:111], a[12:15], a[236:239], v[108:111]\n" \
"ds_read_b128 a[4:7], v239 offset:12288\n" \
"v_mfma_f32_16x16x32_bf16 v[108:111], a[8:11], a[228:231], v[108:111]\n" \
"s_waitcnt lgkmcnt(4)\n" \
"buffer_store_dwordx4 v[80:83], v250, s[32:35], s36 offen offset:1024 sc1\n" \
"v_mfma_f32_16x16x32_bf16 v[96:99], a[16:19], a[240:243], v[96:99]\n" \
"ds_read_b128 a[8:11], v239 offset:5120\n" \
"v_mfma_f32_16x16x32_bf16 v[96:99], a[20:23], a[248:251], v[96:99]\n" \
"v_cvt_pk_bf16_f32 v88, v88, v89\n" \
"v_cvt_pk_bf16_f32 v89, v90, v91\n" \
"v_mfma_f32_16x16x32_bf16 v[104:107], a[20:23], a[252:255], v[104:107]\n" \
"ds_read_b128 a[12:15], v239 offset:13312\n" \
"v_cvt_pk_bf16_f32 v90, v92, v93\n" \
"v_mfma_f32_16x16x32_bf16 v[104:107], a[16:19], a[244:247], v[104:107]\n" \
"s_waitcnt lgkmcnt(4)\n" \
"v_cvt_pk_bf16_f32 v91, v94, v95\n" \
"v_mfma_f32_16x16x32_bf16 v[100:103], a[24:27], a[240:243], v[100:103]\n" \
"ds_read_b128 a[16:19], v239 offset:20480\n" \
"v_mfma_f32_16x16x32_bf16 v[100:103], a[28:31], a[248:251], v[100:103]\n" \
"buffer_load_dwordx4 a[132:135], v214, s[32:35], s22 offen sc1\n" \
"v_mfma_f32_16x16x32_bf16 v[108:111], a[28:31], a[252:255], v[108:111]\n" \
"s_add_u32 s22, s22, s84\n" \
"ds_read_b128 a[20:23], v239 offset:28672\n" \
"v_mfma_f32_16x16x32_bf16 v[108:111], a[24:27], a[244:247], v[108:111]\n" \
"s_waitcnt lgkmcnt(4)\n" \
"v_mfma_f32_16x16x32_bf16 v[96:99], a[0:3], a[64:67], v[96:99]\n" \
"ds_read_b128 a[24:27], v239 offset:21504\n" \
"v_mfma_f32_16x16x32_bf16 v[96:99], a[4:7], a[80:83], v[96:99]\n" \
"buffer_store_dwordx4 v[88:91], v250, s[32:35], s21 offen offset:1024 sc1\n" \
"v_mfma_f32_16x16x32_bf16 v[104:107], a[4:7], a[88:91], v[104:107]\n" \
"ds_read_b128 a[28:31], v239 offset:29696\n" \
"v_mfma_f32_16x16x32_bf16 v[104:107], a[0:3], a[72:75], v[104:107]\n" \
"s_waitcnt lgkmcnt(4)\n" \
"v_mfma_f32_16x16x32_bf16 v[100:103], a[8:11], a[64:67], v[100:103]\n" \
"ds_read_b128 a[0:3], v241 offset:6144\n" \
"v_mfma_f32_16x16x32_bf16 v[100:103], a[12:15], a[80:83], v[100:103]\n" \
"v_mfma_f32_16x16x32_bf16 v[108:111], a[12:15], a[88:91], v[108:111]\n" \
"ds_read_b128 a[4:7], v241 offset:14336\n" \
"v_mfma_f32_16x16x32_bf16 v[108:111], a[8:11], a[72:75], v[108:111]\n" \
"s_waitcnt lgkmcnt(4)\n" \
"buffer_load_dwordx4 a[164:167], v214, s[32:35], s22 offen offset:64 sc1\n" \
"v_mfma_f32_16x16x32_bf16 v[96:99], a[16:19], a[96:99], v[96:99]\n" \
"s_add_u32 s22, s95, 4194304\n" \
"ds_read_b128 a[8:11], v241 offset:7168\n" \
"v_mfma_f32_16x16x32_bf16 v[96:99], a[20:23], a[112:115], v[96:99]\n" \
"s_bitcmp1_b32 s76, 8\n" \
"s_cselect_b32 s22, s22, 1073741824\n" \
"v_mfma_f32_16x16x32_bf16 v[104:107], a[20:23], a[116:119], v[104:107]\n" \
"ds_read_b128 a[12:15], v241 offset:15360\n" \
"v_mfma_f32_16x16x32_bf16 v[104:107], a[16:19], a[104:107], v[104:107]\n" \
"s_waitcnt lgkmcnt(4)\n" \
"v_mfma_f32_16x16x32_bf16 v[100:103], a[24:27], a[96:99], v[100:103]\n" \
"ds_read_b128 a[16:19], v241 offset:22528\n" \
"v_mfma_f32_16x16x32_bf16 v[100:103], a[28:31], a[112:115], v[100:103]\n" \
"buffer_load_dwordx4 a[136:139], v214, s[32:35], s22 offen sc1\n" \
"v_mfma_f32_16x16x32_bf16 v[108:111], a[28:31], a[116:119], v[108:111]\n" \
"s_add_u32 s22, s22, s84\n" \
"ds_read_b128 a[20:23], v241 offset:30720\n" \
"v_lshlrev_b32 v192, 16, v128\n" \
"v_mfma_f32_16x16x32_bf16 v[108:111], a[24:27], a[104:107], v[108:111]\n" \
"s_waitcnt lgkmcnt(4)\n" \
"v_and_b32 v193, 0xffff0000, v128\n" \
"v_lshlrev_b32 v194, 16, v129\n" \
"v_mfma_f32_16x16x32_bf16 v[112:115], a[0:3], a[192:195], v[112:115]\n" \
"ds_read_b128 a[24:27], v241 offset:23552\n" \
"v_and_b32 v195, 0xffff0000, v129\n" \
"v_mfma_f32_16x16x32_bf16 v[112:115], a[4:7], a[200:203], v[112:115]\n" \
"v_lshlrev_b32 v196, 16, v130\n" \
"v_and_b32 v197, 0xffff0000, v130\n" \
"v_mfma_f32_16x16x32_bf16 v[120:123], a[4:7], a[204:207], v[120:123]\n" \
"ds_read_b128 a[28:31], v241 offset:31744\n" \
"v_lshlrev_b32 v198, 16, v131\n" \
"v_mfma_f32_16x16x32_bf16 v[120:123], a[0:3], a[196:199], v[120:123]\n" \
"s_waitcnt lgkmcnt(4)\n" \
"buffer_load_dwordx4 a[168:171], v214, s[32:35], s22 offen offset:64 sc1\n" \
"v_and_b32 v199, 0xffff0000, v131\n" \
"v_mfma_f32_16x16x32_bf16 v[116:119], a[8:11], a[192:195], v[116:119]\n" \
"s_add_u32 s22, s95, 6291456\n" \
"ds_read_b128 a[0:3], v244 offset:6144\n" \
"v_lshlrev_b32 v136, 16, v172\n" \
"v_mfma_f32_16x16x32_bf16 v[116:119], a[12:15], a[200:203], v[116:119]\n" \
"s_bitcmp1_b32 s76, 12\n" \
"s_cselect_b32 s22, s22, 1073741824\n" \
"v_cvt_pk_bf16_f32 v96, v96, v97\n" \
"v_mfma_f32_16x16x32_bf16 v[124:127], a[12:15], a[204:207], v[124:127]\n" \
"ds_read_b128 a[4:7], v244 offset:14336\n" \
"v_cvt_pk_bf16_f32 v97, v98, v99\n" \
"v_mfma_f32_16x16x32_bf16 v[124:127], a[8:11], a[196:199], v[124:127]\n" \
"s_waitcnt lgkmcnt(4)\n" \
"v_cvt_pk_bf16_f32 v98, v100, v101\n" \
"v_cvt_pk_bf16_f32 v99, v102, v103\n" \
"v_mfma_f32_16x16x32_bf16 v[112:115], a[16:19], a[208:211], v[112:115]\n" \
"ds_read_b128 a[8:11], v244 offset:7168\n" \
"v_and_b32 v137, 0xffff0000, v172\n" \
"v_mfma_f32_16x16x32_bf16 v[112:115], a[20:23], a[216:219], v[112:115]\n" \
"buffer_load_dwordx4 a[140:143], v214, s[32:35], s22 offen sc1\n" \
"v_lshlrev_b32 v138, 16, v173\n" \
"v_mfma_f32_16x16x32_bf16 v[120:123], a[20:23], a[220:223], v[120:123]\n" \
"s_add_u32 s22, s22, s84\n" \
"ds_read_b128 a[12:15], v244 offset:15360\n" \
"v_and_b32 v139, 0xffff0000, v173\n" \
"v_mfma_f32_16x16x32_bf16 v[120:123], a[16:19], a[212:215], v[120:123]\n" \
"s_waitcnt lgkmcnt(4)\n" \
"buffer_store_dwordx4 v[96:99], v250, s[32:35], s36 offen offset:2048 sc1\n" \
"v_mfma_f32_16x16x32_bf16 v[116:119], a[24:27], a[208:211], v[116:119]\n" \
"ds_read_b128 a[16:19], v249 offset:6144\n" \
"v_lshlrev_b32 v140, 16, v174\n" \
"v_mfma_f32_16x16x32_bf16 v[116:119], a[28:31], a[216:219], v[116:119]\n" \
"v_cvt_pk_bf16_f32 v104, v104, v105\n" \
"v_cvt_pk_bf16_f32 v105, v106, v107\n" \
"v_mfma_f32_16x16x32_bf16 v[124:127], a[28:31], a[220:223], v[124:127]\n" \
"ds_read_b128 a[20:23], v249 offset:14336\n" \
"v_cvt_pk_bf16_f32 v106, v108, v109\n" \
"v_mfma_f32_16x16x32_bf16 v[124:127], a[24:27], a[212:215], v[124:127]\n" \
"s_waitcnt lgkmcnt(4)\n" \
"v_cvt_pk_bf16_f32 v107, v110, v111\n" \
"v_and_b32 v141, 0xffff0000, v174\n" \
"v_mfma_f32_16x16x32_bf16 v[112:115], a[0:3], a[224:227], v[112:115]\n" \
"ds_read_b128 a[24:27], v249 offset:7168\n" \
"v_lshlrev_b32 v142, 16, v175\n" \
"v_mfma_f32_16x16x32_bf16 v[112:115], a[4:7], a[232:235], v[112:115]\n" \
"buffer_load_dwordx4 a[172:175], v214, s[32:35], s22 offen offset:64 sc1\n" \
"v_and_b32 v143, 0xffff0000, v175\n" \
"v_mfma_f32_16x16x32_bf16 v[120:123], a[4:7], a[236:239], v[120:123]\n" \
"s_add_u32 s22, s95, 8388608\n" \
"ds_read_b128 a[28:31], v249 offset:15360\n" \
"v_lshlrev_b32 v144, 16, v176\n" \
"v_mfma_f32_16x16x32_bf16 v[120:123], a[0:3], a[228:231], v[120:123]\n" \
"s_waitcnt lgkmcnt(4)\n" \
"s_bitcmp1_b32 s76, 16\n" \
"s_cselect_b32 s22, s22, 1073741824\n" \
"v_mfma_f32_16x16x32_bf16 v[116:119], a[8:11], a[224:227], v[116:119]\n" \
"ds_read_b128 a[0:3], v239 offset:6144\n" \
"v_and_b32 v145, 0xffff0000, v176\n" \
"v_mfma_f32_16x16x32_bf16 v[116:119], a[12:15], a[232:235], v[116:119]\n" \
"buffer_store_dwordx4 v[104:107], v250, s[32:35], s21 offen offset:2048 sc1\n" \
"v_mfma_f32_16x16x32_bf16 v[124:127], a[12:15], a[236:239], v[124:127]\n" \
"ds_read_b128 a[4:7], v239 offset:14336\n" \
"v_lshlrev_b32 v146, 16, v177\n" \
"v_mfma_f32_16x16x32_bf16 v[124:127], a[8:11], a[228:231], v[124:127]\n" \
"s_waitcnt lgkmcnt(4)\n" \
"v_and_b32 v147, 0xffff0000, v177\n" \
"v_lshlrev_b32 v148, 16, v178\n" \
"v_mfma_f32_16x16x32_bf16 v[112:115], a[16:19], a[240:243], v[112:115]\n" \
"ds_read_b128 a[8:11], v239 offset:7168\n" \
"v_and_b32 v149, 0xffff0000, v178\n" \
"v_mfma_f32_16x16x32_bf16 v[112:115], a[20:23], a[248:251], v[112:115]\n" \
"v_lshlrev_b32 v150, 16, v179\n" \
"v_and_b32 v151, 0xffff0000, v179\n" \
"v_mfma_f32_16x16x32_bf16 v[120:123], a[20:23], a[252:255], v[120:123]\n" \
"ds_read_b128 a[12:15], v239 offset:15360\n" \
"v_lshlrev_b32 v152, 16, v180\n" \
"v_mfma_f32_16x16x32_bf16 v[120:123], a[16:19], a[244:247], v[120:123]\n" \
"s_waitcnt lgkmcnt(4)\n" \
"buffer_load_dwordx4 a[144:147], v214, s[32:35], s22 offen sc1\n" \
"v_and_b32 v153, 0xffff0000, v180\n" \
"v_mfma_f32_16x16x32_bf16 v[116:119], a[24:27], a[240:243], v[116:119]\n" \
"s_add_u32 s22, s22, s84\n" \
"ds_read_b128 a[16:19], v239 offset:22528\n" \
"v_lshlrev_b32 v154, 16, v181\n" \
"v_mfma_f32_16x16x32_bf16 v[116:119], a[28:31], a[248:251], v[116:119]\n" \
"v_and_b32 v155, 0xffff0000, v181\n" \
"v_lshlrev_b32 v156, 16, v182\n" \
"v_mfma_f32_16x16x32_bf16 v[124:127], a[28:31], a[252:255], v[124:127]\n" \
"ds_read_b128 a[20:23], v239 offset:30720\n" \
"v_and_b32 v157, 0xffff0000, v182\n" \
"v_mfma_f32_16x16x32_bf16 v[124:127], a[24:27], a[244:247], v[124:127]\n" \
"s_waitcnt lgkmcnt(4)\n" \
"v_lshlrev_b32 v158, 16, v183\n" \
"v_and_b32 v159, 0xffff0000, v183\n" \
"v_mfma_f32_16x16x32_bf16 v[112:115], a[0:3], a[64:67], v[112:115]\n" \
"ds_read_b128 a[24:27], v239 offset:23552\n" \
"v_lshlrev_b32 v160, 16, v184\n" \
"v_mfma_f32_16x16x32_bf16 v[112:115], a[4:7], a[80:83], v[112:115]\n" \
"buffer_load_dwordx4 a[176:179], v214, s[32:35], s22 offen offset:64 sc1\n" \
"v_and_b32 v161, 0xffff0000, v184\n" \
"v_mfma_f32_16x16x32_bf16 v[120:123], a[4:7], a[88:91], v[120:123]\n" \
"s_add_u32 s22, s95, 10485760\n" \
"ds_read_b128 a[28:31], v239 offset:31744\n" \
"v_lshlrev_b32 v162, 16, v185\n" \
"v_mfma_f32_16x16x32_bf16 v[120:123], a[0:3], a[72:75], v[120:123]\n" \
"s_waitcnt lgkmcnt(4)\n" \
"s_bitcmp1_b32 s76, 20\n" \
"s_cselect_b32 s22, s22, 1073741824\n" \
"v_mfma_f32_16x16x32_bf16 v[116:119], a[8:11], a[64:67], v[116:119]\n" \
"v_and_b32 v163, 0xffff0000, v185\n" \
"v_lshlrev_b32 v164, 16, v186\n" \
"v_mfma_f32_16x16x32_bf16 v[116:119], a[12:15], a[80:83], v[116:119]\n" \
"v_and_b32 v165, 0xffff0000, v186\n" \
"v_lshlrev_b32 v166, 16, v187\n" \
"v_mfma_f32_16x16x32_bf16 v[124:127], a[12:15], a[88:91], v[124:127]\n" \
"buffer_load_dwordx4 a[148:151], v214, s[32:35], s22 offen sc1\n" \
"v_and_b32 v167, 0xffff0000, v187\n" \
"v_mfma_f32_16x16x32_bf16 v[124:127], a[8:11], a[72:75], v[124:127]\n" \
"s_waitcnt lgkmcnt(2)\n" \
"s_add_u32 s22, s22, s84\n" \
"v_mfma_f32_16x16x32_bf16 v[112:115], a[16:19], a[96:99], v[112:115]\n" \
"v_mfma_f32_16x16x32_bf16 v[112:115], a[20:23], a[112:115], v[112:115]\n" \
"v_mfma_f32_16x16x32_bf16 v[120:123], a[20:23], a[116:119], v[120:123]\n" \
"v_mfma_f32_16x16x32_bf16 v[120:123], a[16:19], a[104:107], v[120:123]\n" \
"s_waitcnt lgkmcnt(0)\n" \
"buffer_load_dwordx4 a[180:183], v214, s[32:35], s22 offen offset:64 sc1\n" \
"v_mfma_f32_16x16x32_bf16 v[116:119], a[24:27], a[96:99], v[116:119]\n" \
"s_add_u32 s22, s95, 12582912\n" \
"s_bitcmp1_b32 s76, 24\n" \
"s_cselect_b32 s22, s22, 1073741824\n" \
"v_mfma_f32_16x16x32_bf16 v[116:119], a[28:31], a[112:115], v[116:119]\n" \
"v_mfma_f32_16x16x32_bf16 v[124:127], a[28:31], a[116:119], v[124:127]\n" \
"v_mfma_f32_16x16x32_bf16 v[124:127], a[24:27], a[104:107], v[124:127]\n" \
"v_cvt_pk_bf16_f32 v112, v112, v113\n" \
"v_cvt_pk_bf16_f32 v113, v114, v115\n" \
"s_nop 3\n" \
"v_cvt_pk_bf16_f32 v114, v116, v117\n" \
"v_cvt_pk_bf16_f32 v115, v118, v119\n" \
"buffer_store_dwordx4 v[112:115], v250, s[32:35], s36 offen offset:3072 sc1\n" \
"v_cvt_pk_bf16_f32 v120, v120, v121\n" \
"v_cvt_pk_bf16_f32 v121, v122, v123\n" \
"v_cvt_pk_bf16_f32 v122, v124, v125\n" \
"v_cvt_pk_bf16_f32 v123, v126, v127\n" \
"buffer_store_dwordx4 v[120:123], v250, s[32:35], s21 offen offset:3072 sc1\n" \
"s_waitcnt vmcnt(0)\n" \
"buffer_store_dword v253, v246, s[32:35], s39 offen offset:128 sc1\n" \
".Lst1_7_%=:\n" \
"s_cbranch_execz .Lst1_11_%=\n" \
"buffer_load_dwordx4 a[152:155], v214, s[32:35], s22 offen sc1\n" \
"s_add_u32 s22, s22, s84\n" \
"buffer_load_dwordx4 a[184:187], v214, s[32:35], s22 offen offset:64 sc1\n" \
"s_add_u32 s22, s95, 14680064\n" \
"s_bitcmp1_b32 s76, 28\n" \
"s_cselect_b32 s22, s22, 1073741824\n" \
"buffer_load_dwordx4 a[156:159], v214, s[32:35], s22 offen sc1\n" \
"s_add_u32 s22, s22, s84\n" \
"buffer_load_dwordx4 a[188:191], v214, s[32:35], s22 offen offset:64 sc1\n" \
"v_lshlrev_b32 v236, 2, v191\n" \
"v_lshrrev_b32 v100, 4, v191\n" \
"v_bfe_u32 v101, v191, 1, 2\n" \
"v_lshlrev_b32 v101, 4, v101\n" \
"v_and_or_b32 v101, v191, 8, v101\n" \
"v_and_or_b32 v101, v236, 4, v101\n" \
"v_and_b32 v102, 3, v100\n" \
"v_lshl_or_b32 v239, v102, 10, v101\n" \
"v_lshrrev_b32 v102, 2, v100\n" \
"v_lshl_or_b32 v239, v102, 13, v239\n" \
"v_lshl_or_b32 v101, v100, 6, v101\n" \
"buffer_load_dword v172, v101, s[32:35], 0 offen offset:0 sc1\n" \
"buffer_load_dword v174, v101, s[32:35], 0 offen offset:1024 sc1\n" \
"buffer_load_dword v176, v101, s[32:35], 0 offen offset:2048 sc1\n" \
"buffer_load_dword v178, v101, s[32:35], 0 offen offset:3072 sc1\n" \
  \
".Ljoin_%=:\n" \
"s_nop 1\n" \
"s_cmp_eq_u32 s74, 0x11111111\n" \
"s_cselect_b32 s19, s74, 0\n" \
"s_cselect_b64 vcc, -1, 0\n" \
"v_mov_b32 v254, v255\n" \
"v_cndmask_b32 v255, v252, v255, vcc\n" \
"buffer_load_dword v245, v255, s[32:35], s18 offen sc1\n" \
"s_xor_b32 s94, s83, 0xc000\n" \
"s_lshl3_add_u32 s24, %[wid], s94\n" \
"v_mov_b32 v232, s24\n" \
"v_mov_b32 v233, s94\n" \
"v_lshlrev_b32 v234, 11, v251\n" \
"v_cmp_gt_u32 vcc, 8, v251\n" \
"v_mov_b32 v235, 1073741824\n" \
"s_lshl2_add_u32 s38, s64, %[wid]\n" \
"v_cndmask_b32 v234, v235, v234, vcc\n" \
"v_lshlrev_b32 v235, 3, v251\n" \
"s_lshl_b32 s38, s38, 9\n" \
"s_add_u32 s38, s38, 557056\n" \
"s_lshl_b64 s[42:43], 1, %[bid]\n" \
"s_lshr_b32 s24, %[bid], 6\n" \
"s_cmp_eq_u32 s24, %[wid]\n" \
"s_cselect_b64 s[42:43], s[42:43], 0\n" \
"s_call_b64 s[98:99], .Lch3_%=\n" \
"s_mov_b32 s82, 0x100000\n" \
"s_cmp_lt_u32 s40, 24\n" \
"s_cbranch_scc0 .Lg1_%=\n" \
".Lg0_%=:\n" \
"s_call_b64 s[98:99], .Lga_%=\n" \
"s_cmp_eq_u32 s74, 0x11111111\n" \
"s_cbranch_scc0 .Lgr0_%=\n" \
"v_accvgpr_read_b32 v120, a48\n" \
"v_accvgpr_read_b32 v121, a49\n" \
"v_accvgpr_read_b32 v122, a50\n" \
"v_accvgpr_read_b32 v123, a51\n" \
"v_accvgpr_read_b32 v124, a52\n" \
"v_accvgpr_read_b32 v125, a53\n" \
"v_accvgpr_read_b32 v126, a54\n" \
"v_accvgpr_read_b32 v127, a55\n" \
"v_lshlrev_b32 v48, 16, v120\n" \
"v_and_b32 v49, 0xffff0000, v120\n" \
"v_lshlrev_b32 v50, 16, v121\n" \
"v_and_b32 v51, 0xffff0000, v121\n" \
"v_lshlrev_b32 v52, 16, v122\n" \
"v_and_b32 v53, 0xffff0000, v122\n" \
"v_lshlrev_b32 v54, 16, v123\n" \
"v_and_b32 v55, 0xffff0000, v123\n" \
"v_lshlrev_b32 v56, 16, v124\n" \
"v_and_b32 v57, 0xffff0000, v124\n" \
"v_lshlrev_b32 v58, 16, v125\n" \
"v_and_b32 v59, 0xffff0000, v125\n" \
"v_lshlrev_b32 v60, 16, v126\n" \
"v_and_b32 v61, 0xffff0000, v126\n" \
"v_lshlrev_b32 v62, 16, v127\n" \
"v_and_b32 v63, 0xffff0000, v127\n" \
"v_mov_b32 v255, v254\n" \
"s_mov_b32 s19, 0x11111111\n" \
"s_waitcnt vmcnt(0)\n" \
"buffer_load_dword v245, v255, s[32:35], s18 offen sc1\n" \
"v_mfma_f32_16x16x32_bf16 v[92:95], v[210:213], a[128:131], 0\n" \
"v_mfma_f32_16x16x32_bf16 v[96:99], v[208:211], a[128:131], 0\n" \
"v_mfma_f32_16x16x32_bf16 v[92:95], v[210:213], a[132:135], v[92:95]\n" \
"v_mfma_f32_16x16x32_bf16 v[96:99], v[208:211], a[132:135], v[96:99]\n" \
"v_mfma_f32_16x16x32_bf16 v[92:95], v[210:213], a[136:139], v[92:95]\n" \
"v_mfma_f32_16x16x32_bf16 v[96:99], v[208:211], a[136:139], v[96:99]\n" \
"v_mfma_f32_16x16x32_bf16 v[92:95], v[210:213], a[140:143], v[92:95]\n" \
"v_mfma_f32_16x16x32_bf16 v[96:99], v[208:211], a[140:143], v[96:99]\n" \
"v_mfma_f32_16x16x32_bf16 v[92:95], v[210:213], a[144:147], v[92:95]\n" \
"v_mfma_f32_16x16x32_bf16 v[96:99], v[208:211], a[144:147], v[96:99]\n" \
"v_mfma_f32_16x16x32_bf16 v[92:95], v[210:213], a[148:151], v[92:95]\n" \
"v_mfma_f32_16x16x32_bf16 v[96:99], v[208:211], a[148:151], v[96:99]\n" \
"v_mfma_f32_16x16x32_bf16 v[92:95], v[210:213], a[152:155], v[92:95]\n" \
"v_mfma_f32_16x16x32_bf16 v[96:99], v[208:211], a[152:155], v[96:99]\n" \
"v_mfma_f32_16x16x32_bf16 v[92:95], v[210:213], a[156:159], v[92:95]\n" \
"v_mfma_f32_16x16x32_bf16 v[96:99], v[208:211], a[156:159], v[96:99]\n" \
"s_nop 6\n" \
"v_cvt_pk_bf16_f32 v100, v92, v93\n" \
"v_cvt_pk_bf16_f32 v101, v94, v95\n" \
"v_cvt_pk_bf16_f32 v102, v96, v97\n" \
"v_cvt_pk_bf16_f32 v103, v98, v99\n" \
"v_lshlrev_b32 v92, 16, v100\n" \
"v_and_b32 v93, 0xffff0000, v100\n" \
"v_lshlrev_b32 v94, 16, v101\n" \
"v_and_b32 v95, 0xffff0000, v101\n" \
"v_lshlrev_b32 v96, 16, v102\n" \
"v_and_b32 v97, 0xffff0000, v102\n" \
"v_lshlrev_b32 v98, 16, v103\n" \
"v_and_b32 v99, 0xffff0000, v103\n" \
"v_pk_mul_f32 v[92:93], v[92:93], v[136:137]\n" \
"v_pk_mul_f32 v[94:95], v[94:95], v[138:139]\n" \
"v_pk_mul_f32 v[96:97], v[96:97], v[140:141]\n" \
"v_pk_mul_f32 v[98:99], v[98:99], v[142:143]\n" \
"v_cvt_pk_bf16_f32 v100, v92, v93\n" \
"v_cvt_pk_bf16_f32 v101, v94, v95\n" \
"v_cvt_pk_bf16_f32 v102, v96, v97\n" \
"v_cvt_pk_bf16_f32 v103, v98, v99\n" \
"v_lshlrev_b32 v92, 16, v100\n" \
"v_and_b32 v93, 0xffff0000, v100\n" \
"v_lshlrev_b32 v94, 16, v101\n" \
"v_and_b32 v95, 0xffff0000, v101\n" \
"v_lshlrev_b32 v96, 16, v102\n" \
"v_and_b32 v97, 0xffff0000, v102\n" \
"v_lshlrev_b32 v98, 16, v103\n" \
"v_and_b32 v99, 0xffff0000, v103\n" \
"v_pk_add_f32 v[92:93], v[92:93], v[48:49]\n" \
"v_pk_add_f32 v[94:95], v[94:95], v[50:51]\n" \
"v_pk_add_f32 v[96:97], v[96:97], v[52:53]\n" \
"v_pk_add_f32 v[98:99], v[98:99], v[54:55]\n" \
"v_cvt_pk_bf16_f32 v100, v92, v93\n" \
"v_cvt_pk_bf16_f32 v101, v94, v95\n" \
"v_cvt_pk_bf16_f32 v102, v96, v97\n" \
"v_cvt_pk_bf16_f32 v103, v98, v99\n" \
"v_lshlrev_b32 v92, 16, v100\n" \
"v_and_b32 v93, 0xffff0000, v100\n" \
"v_lshlrev_b32 v94, 16, v101\n" \
"v_readfirstlane_b32 s78, v92\n" \
"v_and_b32 v95, 0xffff0000, v101\n" \
"v_lshlrev_b32 v96, 16, v102\n" \
"s_mov_b32 s79, s78\n" \
"v_and_b32 v97, 0xffff0000, v102\n" \
"v_lshlrev_b32 v98, 16, v103\n" \
"v_and_b32 v99, 0xffff0000, v103\n" \
"v_pk_add_f32 v[104:105], v[92:93], s[78:79] neg_lo:[0,1] neg_hi:[0,1]\n" \
"v_pk_add_f32 v[106:107], v[94:95], s[78:79] neg_lo:[0,1] neg_hi:[0,1]\n" \
"v_pk_add_f32 v[108:109], v[96:97], s[78:79] neg_lo:[0,1] neg_hi:[0,1]\n" \
"v_pk_add_f32 v[110:111], v[98:99], s[78:79] neg_lo:[0,1] neg_hi:[0,1]\n" \
"v_pk_add_f32 v[112:113], v[104:105], v[106:107]\n" \
"v_pk_mul_f32 v[114:115], v[104:105], v[104:105]\n" \
"v_pk_add_f32 v[116:117], v[108:109], v[110:111]\n" \
"v_pk_mul_f32 v[118:119], v[108:109], v[108:109]\n" \
"v_pk_fma_f32 v[114:115], v[106:107], v[106:107], v[114:115]\n" \
"v_pk_fma_f32 v[118:119], v[110:111], v[110:111], v[118:119]\n" \
"v_pk_add_f32 v[112:113], v[112:113], v[116:117]\n" \
"v_pk_add_f32 v[114:115], v[114:115], v[118:119]\n" \
"v_add_f32 v112, v112, v113\n" \
"v_add_f32 v114, v114, v115\n" \
"s_nop 1\n" \
"v_permlane32_swap_b32 v112, v114\n" \
"v_add_f32 v112, v112, v114\n" \
"s_nop 1\n" \
"v_add_f32_dpp v112, v112, v112 quad_perm:[1,0,3,2] row_mask:0xf bank_mask:0xf\n" \
"s_nop 1\n" \
"v_add_f32_dpp v112, v112, v112 quad_perm:[2,3,0,1] row_mask:0xf bank_mask:0xf\n" \
"s_nop 1\n" \
"v_add_f32_dpp v112, v112, v112 row_half_mirror row_mask:0xf bank_mask:0xf\n" \
"s_nop 1\n" \
"v_add_f32_dpp v112, v112, v112 row_mirror row_mask:0xf bank_mask:0xf\n" \
"s_nop 1\n" \
"v_add_f32_dpp v112, v112, v112 row_bcast:15 row_mask:0xa bank_mask:0xf\n" \
"v_mul_f32 v112, 0x3b000000, v112\n" \
"s_nop 0\n" \
"v_readlane_b32 s80, v112, 31\n" \
"v_readlane_b32 s81, v112, 63\n" \
"v_mov_b32 v113, s78\n" \
"s_nop 0\n" \
"v_mov_b32 v114, s81\n" \
"v_add_f32 v113, s80, v113\n" \
"v_fma_f32 v114, -s80, s80, v114\n" \
"v_max_f32 v115, 0, v114\n" \
"v_cmp_u_f32 s[22:23], v114, v114\n" \
"s_nop 1\n" \
"v_cndmask_b32 v114, v115, v114, s[22:23]\n" \
"v_cvt_pk_bf16_f32 v116, v114, v113\n" \
"v_lshlrev_b32 v114, 16, v116\n" \
"v_and_b32 v112, 0xffff0000, v116\n" \
"v_add_f32 v114, 0x3727c5ac, v114\n" \
"v_cvt_pk_bf16_f32 v114, v114, v114\n" \
"v_pk_add_f32 v[104:105], v[92:93], v[112:113] op_sel_hi:[1,0] neg_lo:[0,1] neg_hi:[0,1]\n" \
"v_lshlrev_b32 v114, 16, v114\n" \
"v_pk_add_f32 v[106:107], v[94:95], v[112:113] op_sel_hi:[1,0] neg_lo:[0,1] neg_hi:[0,1]\n" \
"v_sqrt_f32 v114, v114\n" \
"v_pk_add_f32 v[108:109], v[96:97], v[112:113] op_sel_hi:[1,0] neg_lo:[0,1] neg_hi:[0,1]\n" \
"v_pk_add_f32 v[110:111], v[98:99], v[112:113] op_sel_hi:[1,0] neg_lo:[0,1] neg_hi:[0,1]\n" \
"v_cvt_pk_bf16_f32 v116, v114, v112\n" \
"v_cvt_pk_bf16_f32 v100, v104, v105\n" \
"v_lshlrev_b32 v114, 16, v116\n" \
"v_cvt_pk_bf16_f32 v101, v106, v107\n" \
"v_rcp_f32 v114, v114\n" \
"v_cvt_pk_bf16_f32 v102, v108, v109\n" \
"v_cvt_pk_bf16_f32 v103, v110, v111\n" \
"v_readfirstlane_b32 s72, v116\n" \
"v_lshlrev_b32 v104, 16, v100\n" \
"v_and_b32 v105, 0xffff0000, v100\n" \
"v_lshlrev_b32 v106, 16, v101\n" \
"v_and_b32 v107, 0xffff0000, v101\n" \
"v_lshlrev_b32 v108, 16, v102\n" \
"v_and_b32 v109, 0xffff0000, v102\n" \
"v_lshlrev_b32 v110, 16, v103\n" \
"v_and_b32 v111, 0xffff0000, v103\n" \
"v_pk_mul_f32 v[104:105], v[104:105], v[114:115] op_sel_hi:[1,0]\n" \
"v_pk_mul_f32 v[106:107], v[106:107], v[114:115] op_sel_hi:[1,0]\n" \
"v_pk_mul_f32 v[108:109], v[108:109], v[114:115] op_sel_hi:[1,0]\n" \
"v_pk_mul_f32 v[110:111], v[110:111], v[114:115] op_sel_hi:[1,0]\n" \
"v_cvt_pk_bf16_f32 v100, v104, v105\n" \
"v_cvt_pk_bf16_f32 v101, v106, v107\n" \
"v_cvt_pk_bf16_f32 v102, v108, v109\n" \
"v_cvt_pk_bf16_f32 v103, v110, v111\n" \
"v_lshlrev_b32 v104, 16, v100\n" \
"v_and_b32 v105, 0xffff0000, v100\n" \
"v_lshlrev_b32 v106, 16, v101\n" \
"v_and_b32 v107, 0xffff0000, v101\n" \
"v_lshlrev_b32 v108, 16, v102\n" \
"v_and_b32 v109, 0xffff0000, v102\n" \
"v_lshlrev_b32 v110, 16, v103\n" \
"v_and_b32 v111, 0xffff0000, v103\n" \
"v_pk_mul_f32 v[104:105], v[104:105], v[152:153]\n" \
"v_pk_mul_f32 v[106:107], v[106:107], v[154:155]\n" \
"v_pk_mul_f32 v[108:109], v[108:109], v[156:157]\n" \
"v_pk_mul_f32 v[110:111], v[110:111], v[158:159]\n" \
"v_cvt_pk_bf16_f32 v100, v104, v105\n" \
"v_cvt_pk_bf16_f32 v101, v106, v107\n" \
"v_cvt_pk_bf16_f32 v102, v108, v109\n" \
"v_cvt_pk_bf16_f32 v103, v110, v111\n" \
"v_lshlrev_b32 v104, 16, v100\n" \
"v_and_b32 v105, 0xffff0000, v100\n" \
"v_lshlrev_b32 v106, 16, v101\n" \
"v_and_b32 v107, 0xffff0000, v101\n" \
"v_lshlrev_b32 v108, 16, v102\n" \
"v_and_b32 v109, 0xffff0000, v102\n" \
"v_lshlrev_b32 v110, 16, v103\n" \
"v_and_b32 v111, 0xffff0000, v103\n" \
"v_pk_add_f32 v[104:105], v[104:105], v[144:145]\n" \
"v_pk_add_f32 v[106:107], v[106:107], v[146:147]\n" \
"v_pk_add_f32 v[108:109], v[108:109], v[148:149]\n" \
"v_pk_add_f32 v[110:111], v[110:111], v[150:151]\n" \
"v_cvt_pk_bf16_f32 v220, v104, v105\n" \
"v_cvt_pk_bf16_f32 v221, v106, v107\n" \
"v_cvt_pk_bf16_f32 v222, v108, v109\n" \
"v_cvt_pk_bf16_f32 v223, v110, v111\n" \
"v_dot2_f32_bf16 v112, v220, v168, 0\n" \
"v_dot2_f32_bf16 v113, v221, v169, 0\n" \
"v_dot2_f32_bf16 v112, v222, v170, v112\n" \
"v_dot2_f32_bf16 v113, v223, v171, v113\n" \
"s_nop 2\n" \
"v_add_f32 v112, v112, v113\n" \
"s_nop 1\n" \
"v_add_f32_dpp v112, v112, v112 quad_perm:[1,0,3,2] row_mask:0xf bank_mask:0xf\n" \
"s_nop 1\n" \
"v_add_f32_dpp v112, v112, v112 quad_perm:[2,3,0,1] row_mask:0xf bank_mask:0xf\n" \
"s_nop 1\n" \
"v_add_f32_dpp v112, v112, v112 row_half_mirror row_mask:0xf bank_mask:0xf\n" \
"s_nop 1\n" \
"v_add_f32_dpp v112, v112, v112 row_mirror row_mask:0xf bank_mask:0xf\n" \
"s_nop 1\n" \
"v_add_f32_dpp v112, v112, v112 row_bcast:15 row_mask:0xa bank_mask:0xf\n" \
"s_nop 1\n" \
"v_add_f32_dpp v112, v112, v112 row_bcast:31 row_mask:0xc bank_mask:0xf\n" \
"v_cvt_pk_bf16_f32 v112, v112, v112\n" \
"s_sub_u32 s79, 2047, s85\n" \
"s_lshl_b32 s79, s79, 5\n" \
"s_or_b32 s79, s79, 3\n" \
"v_readlane_b32 s78, v112, 63\n" \
"s_and_b32 s78, s78, 0xffff\n" \
"s_or_b32 s80, s78, 0x8000\n" \
"s_andn2_b32 s81, 0xffff, s78\n" \
"s_bitcmp1_b32 s78, 15\n" \
"s_cselect_b32 s80, s81, s80\n" \
"s_and_b32 s78, s78, 0x7fff\n" \
"s_cmp_gt_u32 s78, 0x7f80\n" \
"s_cselect_b32 s80, 0, s80\n" \
"s_pack_ll_b32_b16 s87, s79, s80\n" \
"s_cmp_eq_u32 s84, 0\n" \
"s_cbranch_scc1 .Lr2_%=\n" \
".Lr2b_%=:\n" \
".Lg1_%=:\n" \
".Lst3_0_%=:\n" \
"s_cbranch_execz .Lst3_4_%=\n" \
"s_call_b64 s[98:99], .Lga_%=\n" \
"s_cmp_eq_u32 s75, 0x11111111\n" \
"s_cbranch_scc0 .Lgr1_%=\n" \
"v_lshlrev_b32 v104, 16, v220\n" \
"v_and_b32 v105, 0xffff0000, v220\n" \
"v_lshlrev_b32 v106, 16, v221\n" \
"v_and_b32 v107, 0xffff0000, v221\n" \
"v_lshlrev_b32 v108, 16, v222\n" \
"v_and_b32 v109, 0xffff0000, v222\n" \
"v_lshlrev_b32 v110, 16, v223\n" \
"v_and_b32 v111, 0xffff0000, v223\n" \
"v_pk_mul_f32 v[104:105], v[104:105], v[160:161]\n" \
"v_pk_mul_f32 v[106:107], v[106:107], v[162:163]\n" \
"v_pk_mul_f32 v[108:109], v[108:109], v[164:165]\n" \
"v_pk_mul_f32 v[110:111], v[110:111], v[166:167]\n" \
"v_cvt_pk_bf16_f32 v100, v104, v105\n" \
"v_cvt_pk_bf16_f32 v101, v106, v107\n" \
"v_cvt_pk_bf16_f32 v102, v108, v109\n" \
"v_cvt_pk_bf16_f32 v103, v110, v111\n" \
"v_lshlrev_b32 v104, 16, v100\n" \
"v_and_b32 v105, 0xffff0000, v100\n" \
"v_lshlrev_b32 v106, 16, v101\n" \
"v_and_b32 v107, 0xffff0000, v101\n" \
"v_lshlrev_b32 v108, 16, v102\n" \
"v_and_b32 v109, 0xffff0000, v102\n" \
"v_lshlrev_b32 v110, 16, v103\n" \
"v_and_b32 v111, 0xffff0000, v103\n" \
"v_pk_add_f32 v[104:105], v[104:105], v[92:93]\n" \
"v_pk_add_f32 v[106:107], v[106:107], v[94:95]\n" \
"v_pk_add_f32 v[108:109], v[108:109], v[96:97]\n" \
"v_pk_add_f32 v[110:111], v[110:111], v[98:99]\n" \
"v_cvt_pk_bf16_f32 v100, v104, v105\n" \
"v_cvt_pk_bf16_f32 v101, v106, v107\n" \
"v_cvt_pk_bf16_f32 v102, v108, v109\n" \
"v_cvt_pk_bf16_f32 v103, v110, v111\n" \
"s_mov_b32 vcc_lo, 0xcccccccc\n" \
"s_mov_b32 vcc_hi, 0xcccccccc\n" \
"v_mov_b32_dpp v104, v102 quad_perm:[0,2,0,2] row_mask:0xf bank_mask:0xf\n" \
"v_mov_b32_dpp v105, v103 quad_perm:[0,2,0,2] row_mask:0xf bank_mask:0xf\n" \
"v_mov_b32_dpp v106, v102 quad_perm:[1,3,1,3] row_mask:0xf bank_mask:0xf\n" \
"v_mov_b32_dpp v107, v103 quad_perm:[1,3,1,3] row_mask:0xf bank_mask:0xf\n" \
"v_cndmask_b32_dpp v224, v100, v104, vcc quad_perm:[0,2,0,2] row_mask:0xf bank_mask:0xf\n" \
"v_cndmask_b32_dpp v225, v101, v105, vcc quad_perm:[0,2,0,2] row_mask:0xf bank_mask:0xf\n" \
"v_cndmask_b32_dpp v226, v100, v106, vcc quad_perm:[1,3,1,3] row_mask:0xf bank_mask:0xf\n" \
"v_cndmask_b32_dpp v227, v101, v107, vcc quad_perm:[1,3,1,3] row_mask:0xf bank_mask:0xf\n" \
"s_waitcnt vmcnt(0)\n" \
"v_mfma_f32_16x16x32_bf16 v[64:67], v[210:213], v[0:3], 0\n" \
"v_mfma_f32_16x16x32_bf16 v[68:71], v[208:211], v[0:3], 0\n" \
"v_mfma_f32_16x16x32_bf16 v[64:67], v[210:213], v[4:7], v[64:67]\n" \
"v_mfma_f32_16x16x32_bf16 v[68:71], v[208:211], v[4:7], v[68:71]\n" \
"v_mfma_f32_16x16x32_bf16 v[64:67], v[210:213], v[8:11], v[64:67]\n" \
"v_mfma_f32_16x16x32_bf16 v[68:71], v[208:211], v[8:11], v[68:71]\n" \
"v_mfma_f32_16x16x32_bf16 v[64:67], v[210:213], v[12:15], v[64:67]\n" \
"v_mfma_f32_16x16x32_bf16 v[68:71], v[208:211], v[12:15], v[68:71]\n" \
"v_mfma_f32_16x16x32_bf16 v[64:67], v[210:213], v[16:19], v[64:67]\n" \
"v_mfma_f32_16x16x32_bf16 v[68:71], v[208:211], v[16:19], v[68:71]\n" \
"v_mfma_f32_16x16x32_bf16 v[64:67], v[210:213], v[20:23], v[64:67]\n" \
"v_mfma_f32_16x16x32_bf16 v[68:71], v[208:211], v[20:23], v[68:71]\n" \
"v_mfma_f32_16x16x32_bf16 v[64:67], v[210:213], v[24:27], v[64:67]\n" \
"v_mfma_f32_16x16x32_bf16 v[68:71], v[208:211], v[24:27], v[68:71]\n" \
"v_mfma_f32_16x16x32_bf16 v[64:67], v[210:213], v[28:31], v[64:67]\n" \
"v_mfma_f32_16x16x32_bf16 v[68:71], v[208:211], v[28:31], v[68:71]\n" \
"s_nop 6\n" \
"v_cvt_pk_bf16_f32 v72, v64, v65\n" \
"v_cvt_pk_bf16_f32 v73, v66, v67\n" \
"v_cvt_pk_bf16_f32 v74, v68, v69\n" \
"v_cvt_pk_bf16_f32 v75, v70, v71\n" \
"v_lshlrev_b32 v64, 16, v72\n" \
"v_and_b32 v65, 0xffff0000, v72\n" \
"v_lshlrev_b32 v66, 16, v73\n" \
"v_and_b32 v67, 0xffff0000, v73\n" \
"v_lshlrev_b32 v68, 16, v74\n" \
"v_and_b32 v69, 0xffff0000, v74\n" \
"v_lshlrev_b32 v70, 16, v75\n" \
"v_and_b32 v71, 0xffff0000, v75\n" \
"v_pk_mul_f32 v[64:65], v[64:65], v[136:137]\n" \
"v_pk_mul_f32 v[66:67], v[66:67], v[138:139]\n" \
"v_pk_mul_f32 v[68:69], v[68:69], v[140:141]\n" \
"v_pk_mul_f32 v[70:71], v[70:71], v[142:143]\n" \
"v_cvt_pk_bf16_f32 v72, v64, v65\n" \
"v_cvt_pk_bf16_f32 v73, v66, v67\n" \
"v_cvt_pk_bf16_f32 v74, v68, v69\n" \
"v_cvt_pk_bf16_f32 v75, v70, v71\n" \
"v_lshlrev_b32 v64, 16, v72\n" \
"v_and_b32 v65, 0xffff0000, v72\n" \
"v_lshlrev_b32 v66, 16, v73\n" \
"v_and_b32 v67, 0xffff0000, v73\n" \
"v_lshlrev_b32 v68, 16, v74\n" \
"v_and_b32 v69, 0xffff0000, v74\n" \
"v_lshlrev_b32 v70, 16, v75\n" \
"v_and_b32 v71, 0xffff0000, v75\n" \
"v_pk_add_f32 v[64:65], v[64:65], v[192:193]\n" \
"v_pk_add_f32 v[66:67], v[66:67], v[194:195]\n" \
"v_pk_add_f32 v[68:69], v[68:69], v[196:197]\n" \
"v_pk_add_f32 v[70:71], v[70:71], v[198:199]\n" \
"v_cvt_pk_bf16_f32 v72, v64, v65\n" \
"v_cvt_pk_bf16_f32 v73, v66, v67\n" \
"v_cvt_pk_bf16_f32 v74, v68, v69\n" \
"v_cvt_pk_bf16_f32 v75, v70, v71\n" \
"v_lshlrev_b32 v64, 16, v72\n" \
"v_and_b32 v65, 0xffff0000, v72\n" \
"v_lshlrev_b32 v66, 16, v73\n" \
"v_readfirstlane_b32 s24, v64\n" \
"v_and_b32 v67, 0xffff0000, v73\n" \
"v_lshlrev_b32 v68, 16, v74\n" \
"s_mov_b32 s25, s24\n" \
"v_and_b32 v69, 0xffff0000, v74\n" \
"v_lshlrev_b32 v70, 16, v75\n" \
"v_and_b32 v71, 0xffff0000, v75\n" \
"v_pk_add_f32 v[76:77], v[64:65], s[24:25] neg_lo:[0,1] neg_hi:[0,1]\n" \
"v_pk_add_f32 v[78:79], v[66:67], s[24:25] neg_lo:[0,1] neg_hi:[0,1]\n" \
"v_pk_add_f32 v[80:81], v[68:69], s[24:25] neg_lo:[0,1] neg_hi:[0,1]\n" \
"v_pk_add_f32 v[82:83], v[70:71], s[24:25] neg_lo:[0,1] neg_hi:[0,1]\n" \
"v_pk_add_f32 v[84:85], v[76:77], v[78:79]\n" \
"v_pk_mul_f32 v[86:87], v[76:77], v[76:77]\n" \
"v_pk_add_f32 v[88:89], v[80:81], v[82:83]\n" \
"v_pk_mul_f32 v[90:91], v[80:81], v[80:81]\n" \
"v_pk_fma_f32 v[86:87], v[78:79], v[78:79], v[86:87]\n" \
"v_pk_fma_f32 v[90:91], v[82:83], v[82:83], v[90:91]\n" \
"v_pk_add_f32 v[84:85], v[84:85], v[88:89]\n" \
"v_pk_add_f32 v[86:87], v[86:87], v[90:91]\n" \
"v_add_f32 v84, v84, v85\n" \
"v_add_f32 v86, v86, v87\n" \
"s_nop 1\n" \
"v_permlane32_swap_b32 v84, v86\n" \
"v_add_f32 v84, v84, v86\n" \
"s_nop 1\n" \
"v_add_f32_dpp v84, v84, v84 quad_perm:[1,0,3,2] row_mask:0xf bank_mask:0xf\n" \
"s_nop 1\n" \
"v_add_f32_dpp v84, v84, v84 quad_perm:[2,3,0,1] row_mask:0xf bank_mask:0xf\n" \
"s_nop 1\n" \
"v_add_f32_dpp v84, v84, v84 row_half_mirror row_mask:0xf bank_mask:0xf\n" \
"s_nop 1\n" \
"v_add_f32_dpp v84, v84, v84 row_mirror row_mask:0xf bank_mask:0xf\n" \
"s_nop 1\n" \
"v_add_f32_dpp v84, v84, v84 row_bcast:15 row_mask:0xa bank_mask:0xf\n" \
"v_mul_f32 v84, 0x3b000000, v84\n" \
"s_nop 0\n" \
"v_readlane_b32 s26, v84, 31\n" \
"v_readlane_b32 s27, v84, 63\n" \
"v_mov_b32 v85, s24\n" \
"s_nop 0\n" \
"v_mov_b32 v86, s27\n" \
"v_add_f32 v85, s26, v85\n" \
"v_fma_f32 v86, -s26, s26, v86\n" \
"v_max_f32 v87, 0, v86\n" \
"v_cmp_u_f32 s[20:21], v86, v86\n" \
"s_nop 1\n" \
"v_cndmask_b32 v86, v87, v86, s[20:21]\n" \
"v_cvt_pk_bf16_f32 v88, v86, v85\n" \
"v_lshlrev_b32 v86, 16, v88\n" \
"v_and_b32 v84, 0xffff0000, v88\n" \
"v_add_f32 v86, 0x3727c5ac, v86\n" \
"v_cvt_pk_bf16_f32 v86, v86, v86\n" \
"v_pk_add_f32 v[76:77], v[64:65], v[84:85] op_sel_hi:[1,0] neg_lo:[0,1] neg_hi:[0,1]\n" \
"v_lshlrev_b32 v86, 16, v86\n" \
"v_pk_add_f32 v[78:79], v[66:67], v[84:85] op_sel_hi:[1,0] neg_lo:[0,1] neg_hi:[0,1]\n" \
"v_sqrt_f32 v86, v86\n" \
"v_pk_add_f32 v[80:81], v[68:69], v[84:85] op_sel_hi:[1,0] neg_lo:[0,1] neg_hi:[0,1]\n" \
"v_pk_add_f32 v[82:83], v[70:71], v[84:85] op_sel_hi:[1,0] neg_lo:[0,1] neg_hi:[0,1]\n" \
"v_cvt_pk_bf16_f32 v88, v86, v84\n" \
"v_cvt_pk_bf16_f32 v72, v76, v77\n" \
"v_lshlrev_b32 v86, 16, v88\n" \
"v_cvt_pk_bf16_f32 v73, v78, v79\n" \
"v_rcp_f32 v86, v86\n" \
"v_cvt_pk_bf16_f32 v74, v80, v81\n" \
"v_cvt_pk_bf16_f32 v75, v82, v83\n" \
"v_readfirstlane_b32 s83, v88\n" \
"v_lshlrev_b32 v76, 16, v72\n" \
"v_and_b32 v77, 0xffff0000, v72\n" \
"v_lshlrev_b32 v78, 16, v73\n" \
"v_and_b32 v79, 0xffff0000, v73\n" \
"v_lshlrev_b32 v80, 16, v74\n" \
"v_and_b32 v81, 0xffff0000, v74\n" \
"v_lshlrev_b32 v82, 16, v75\n" \
"v_and_b32 v83, 0xffff0000, v75\n" \
"v_pk_mul_f32 v[76:77], v[76:77], v[86:87] op_sel_hi:[1,0]\n" \
"v_pk_mul_f32 v[78:79], v[78:79], v[86:87] op_sel_hi:[1,0]\n" \
"v_pk_mul_f32 v[80:81], v[80:81], v[86:87] op_sel_hi:[1,0]\n" \
"v_pk_mul_f32 v[82:83], v[82:83], v[86:87] op_sel_hi:[1,0]\n" \
"v_cvt_pk_bf16_f32 v72, v76, v77\n" \
"v_cvt_pk_bf16_f32 v73, v78, v79\n" \
"v_cvt_pk_bf16_f32 v74, v80, v81\n" \
"v_cvt_pk_bf16_f32 v75, v82, v83\n" \
"v_lshlrev_b32 v76, 16, v72\n" \
"v_and_b32 v77, 0xffff0000, v72\n" \
"v_lshlrev_b32 v78, 16, v73\n" \
"v_and_b32 v79, 0xffff0000, v73\n" \
"v_lshlrev_b32 v80, 16, v74\n" \
"v_and_b32 v81, 0xffff0000, v74\n" \
"v_lshlrev_b32 v82, 16, v75\n" \
"v_and_b32 v83, 0xffff0000, v75\n" \
"v_pk_mul_f32 v[76:77], v[76:77], v[152:153]\n" \
"v_pk_mul_f32 v[78:79], v[78:79], v[154:155]\n" \
"v_pk_mul_f32 v[80:81], v[80:81], v[156:157]\n" \
"v_pk_mul_f32 v[82:83], v[82:83], v[158:159]\n" \
"v_cvt_pk_bf16_f32 v72, v76, v77\n" \
"v_cvt_pk_bf16_f32 v73, v78, v79\n" \
"v_cvt_pk_bf16_f32 v74, v80, v81\n" \
"v_cvt_pk_bf16_f32 v75, v82, v83\n" \
"v_lshlrev_b32 v76, 16, v72\n" \
"v_and_b32 v77, 0xffff0000, v72\n" \
"v_lshlrev_b32 v78, 16, v73\n" \
"v_and_b32 v79, 0xffff0000, v73\n" \
"v_lshlrev_b32 v80, 16, v74\n" \
"v_and_b32 v81, 0xffff0000, v74\n" \
"v_lshlrev_b32 v82, 16, v75\n" \
"v_and_b32 v83, 0xffff0000, v75\n" \
"v_pk_add_f32 v[76:77], v[76:77], v[144:145]\n" \
"v_pk_add_f32 v[78:79], v[78:79], v[146:147]\n" \
"v_pk_add_f32 v[80:81], v[80:81], v[148:149]\n" \
"v_pk_add_f32 v[82:83], v[82:83], v[150:151]\n" \
"v_cvt_pk_bf16_f32 v216, v76, v77\n" \
"v_cvt_pk_bf16_f32 v217, v78, v79\n" \
"v_cvt_pk_bf16_f32 v218, v80, v81\n" \
"v_cvt_pk_bf16_f32 v219, v82, v83\n" \
"v_dot2_f32_bf16 v84, v216, v168, 0\n" \
"v_dot2_f32_bf16 v85, v217, v169, 0\n" \
"v_dot2_f32_bf16 v84, v218, v170, v84\n" \
"v_dot2_f32_bf16 v85, v219, v171, v85\n" \
"s_nop 2\n" \
"v_add_f32 v84, v84, v85\n" \
"s_nop 1\n" \
"v_add_f32_dpp v84, v84, v84 quad_perm:[1,0,3,2] row_mask:0xf bank_mask:0xf\n" \
"s_nop 1\n" \
"v_add_f32_dpp v84, v84, v84 quad_perm:[2,3,0,1] row_mask:0xf bank_mask:0xf\n" \
"s_nop 1\n" \
"v_add_f32_dpp v84, v84, v84 row_half_mirror row_mask:0xf bank_mask:0xf\n" \
"s_nop 1\n" \
"v_add_f32_dpp v84, v84, v84 row_mirror row_mask:0xf bank_mask:0xf\n" \
"s_nop 1\n" \
"v_add_f32_dpp v84, v84, v84 row_bcast:15 row_mask:0xa bank_mask:0xf\n" \
"s_nop 1\n" \
"v_add_f32_dpp v84, v84, v84 row_bcast:31 row_mask:0xc bank_mask:0xf\n" \
"v_cvt_pk_bf16_f32 v84, v84, v84\n" \
"s_sub_u32 s25, 2047, s86\n" \
"s_lshl_b32 s25, s25, 5\n" \
"s_or_b32 s25, s25, 3\n" \
"v_readlane_b32 s24, v84, 63\n" \
"s_and_b32 s24, s24, 0xffff\n" \
"s_or_b32 s26, s24, 0x8000\n" \
"s_andn2_b32 s27, 0xffff, s24\n" \
"s_bitcmp1_b32 s24, 15\n" \
"s_cselect_b32 s26, s27, s26\n" \
"s_and_b32 s24, s24, 0x7fff\n" \
"s_cmp_gt_u32 s24, 0x7f80\n" \
"s_cselect_b32 s26, 0, s26\n" \
"s_pack_ll_b32_b16 s89, s25, s26\n" \
"s_cmp_gt_u32 s87, s88\n" \
"s_cselect_b32 s25, s87, s88\n" \
"s_cselect_b32 s24, s72, s73\n" \
"s_cmp_gt_u32 s89, s25\n" \
"s_cselect_b32 s25, s89, s25\n" \
"s_cselect_b32 s24, s83, s24\n" \
  \
"v_mov_b32 v85, s25\n" \
"v_mov_b32 v84, s24\n" \
"ds_write_b64 v232, v[84:85]\n" \
"s_waitcnt lgkmcnt(0)\n" \
"s_barrier\n" \
"ds_read_b128 v[84:87], v233\n" \
"ds_read_b128 v[88:91], v233 offset:16\n" \
"s_lshl_b32 s26, %[bid], 3\n" \
"s_add_u32 s26, s26, 557056\n" \
"s_cmp_eq_u32 %[wid], 0\n" \
"s_cselect_b32 s26, s26, 1073741824\n" \
"s_waitcnt lgkmcnt(0)\n" \
"v_cmp_gt_u32 vcc, v85, v87\n" \
"v_max_u32 v85, v85, v87\n" \
"s_nop 0\n" \
"v_cndmask_b32 v84, v86, v84, vcc\n" \
"v_cmp_gt_u32 vcc, v89, v91\n" \
"v_max_u32 v89, v89, v91\n" \
"s_nop 0\n" \
"v_cndmask_b32 v88, v90, v88, vcc\n" \
"v_cmp_gt_u32 vcc, v85, v89\n" \
"v_max_u32 v85, v85, v89\n" \
"s_nop 0\n" \
"v_cndmask_b32 v84, v88, v84, vcc\n" \
"buffer_store_dwordx2 v[84:85], v234, s[32:35], s26 offen sc1\n" \
"buffer_load_dwordx2 v[86:87], v235, s[32:35], s38 offen sc1\n" \
"v_readfirstlane_b32 s92, v85\n" \
"v_lshlrev_b32 v76, 16, v216\n" \
"v_and_b32 v77, 0xffff0000, v216\n" \
"v_lshlrev_b32 v78, 16, v217\n" \
"v_and_b32 v79, 0xffff0000, v217\n" \
"v_lshlrev_b32 v80, 16, v218\n" \
"v_and_b32 v81, 0xffff0000, v218\n" \
"v_lshlrev_b32 v82, 16, v219\n" \
"v_and_b32 v83, 0xffff0000, v219\n" \
"v_pk_mul_f32 v[76:77], v[76:77], v[160:161]\n" \
"v_pk_mul_f32 v[78:79], v[78:79], v[162:163]\n" \
"v_pk_mul_f32 v[80:81], v[80:81], v[164:165]\n" \
"v_pk_mul_f32 v[82:83], v[82:83], v[166:167]\n" \
"v_cvt_pk_bf16_f32 v72, v76, v77\n" \
"v_cvt_pk_bf16_f32 v73, v78, v79\n" \
"v_cvt_pk_bf16_f32 v74, v80, v81\n" \
"v_cvt_pk_bf16_f32 v75, v82, v83\n" \
"v_lshlrev_b32 v76, 16, v72\n" \
"v_and_b32 v77, 0xffff0000, v72\n" \
"v_lshlrev_b32 v78, 16, v73\n" \
"v_and_b32 v79, 0xffff0000, v73\n" \
"v_lshlrev_b32 v80, 16, v74\n" \
"v_and_b32 v81, 0xffff0000, v74\n" \
"v_lshlrev_b32 v82, 16, v75\n" \
"v_and_b32 v83, 0xffff0000, v75\n" \
"v_pk_add_f32 v[76:77], v[76:77], v[64:65]\n" \
"v_pk_add_f32 v[78:79], v[78:79], v[66:67]\n" \
"v_pk_add_f32 v[80:81], v[80:81], v[68:69]\n" \
"v_pk_add_f32 v[82:83], v[82:83], v[70:71]\n" \
"v_cvt_pk_bf16_f32 v72, v76, v77\n" \
"v_cvt_pk_bf16_f32 v73, v78, v79\n" \
"v_cvt_pk_bf16_f32 v74, v80, v81\n" \
"v_cvt_pk_bf16_f32 v75, v82, v83\n" \
"s_mov_b32 vcc_lo, 0xcccccccc\n" \
"s_mov_b32 vcc_hi, 0xcccccccc\n" \
"v_mov_b32_dpp v76, v74 quad_perm:[0,2,0,2] row_mask:0xf bank_mask:0xf\n" \
"v_mov_b32_dpp v77, v75 quad_perm:[0,2,0,2] row_mask:0xf bank_mask:0xf\n" \
"v_mov_b32_dpp v78, v74 quad_perm:[1,3,1,3] row_mask:0xf bank_mask:0xf\n" \
"v_mov_b32_dpp v79, v75 quad_perm:[1,3,1,3] row_mask:0xf bank_mask:0xf\n" \
"v_cndmask_b32_dpp v208, v72, v76, vcc quad_perm:[0,2,0,2] row_mask:0xf bank_mask:0xf\n" \
"v_cndmask_b32_dpp v209, v73, v77, vcc quad_perm:[0,2,0,2] row_mask:0xf bank_mask:0xf\n" \
"v_cndmask_b32_dpp v210, v72, v78, vcc quad_perm:[1,3,1,3] row_mask:0xf bank_mask:0xf\n" \
"v_cndmask_b32_dpp v211, v73, v79, vcc quad_perm:[1,3,1,3] row_mask:0xf bank_mask:0xf\n" \
"v_accvgpr_read_b32 v180, a100\n" \
"v_accvgpr_read_b32 v181, a101\n" \
"v_accvgpr_read_b32 v182, a102\n" \
"v_accvgpr_read_b32 v183, a103\n" \
"v_accvgpr_read_b32 v184, a108\n" \
"v_accvgpr_read_b32 v185, a109\n" \
"v_accvgpr_read_b32 v186, a110\n" \
"v_accvgpr_read_b32 v187, a111\n" \
"s_mov_b32 s24, 0x05040100\n" \
"s_mov_b32 s25, 0x07060302\n" \
"v_accvgpr_read_b32 v32, a68\n" \
".Lst3_1_%=:\n" \
"s_cbranch_execz .Lst3_5_%=\n" \
"v_accvgpr_read_b32 v33, a69\n" \
"v_accvgpr_read_b32 v34, a70\n" \
"v_accvgpr_read_b32 v35, a71\n" \
"v_accvgpr_read_b32 v36, a76\n" \
"v_accvgpr_read_b32 v37, a77\n" \
"v_accvgpr_read_b32 v38, a78\n" \
"v_accvgpr_read_b32 v39, a79\n" \
"v_accvgpr_read_b32 v40, a84\n" \
"v_accvgpr_read_b32 v41, a85\n" \
"v_accvgpr_read_b32 v42, a86\n" \
"v_accvgpr_read_b32 v43, a87\n" \
"v_accvgpr_read_b32 v44, a92\n" \
"v_accvgpr_read_b32 v45, a93\n" \
"v_accvgpr_read_b32 v46, a94\n" \
"v_accvgpr_read_b32 v47, a95\n" \
"v_perm_b32 v0, v36, v32, s24\n" \
"v_perm_b32 v1, v36, v32, s25\n" \
"v_perm_b32 v2, v37, v33, s24\n" \
"v_perm_b32 v3, v37, v33, s25\n" \
"v_perm_b32 v4, v38, v34, s24\n" \
"v_perm_b32 v5, v38, v34, s25\n" \
"v_perm_b32 v6, v39, v35, s24\n" \
"v_perm_b32 v7, v39, v35, s25\n" \
"v_perm_b32 v8, v44, v40, s24\n" \
"v_perm_b32 v9, v44, v40, s25\n" \
"v_perm_b32 v10, v45, v41, s24\n" \
"v_perm_b32 v11, v45, v41, s25\n" \
"v_perm_b32 v12, v46, v42, s24\n" \
"v_perm_b32 v13, v46, v42, s25\n" \
"v_perm_b32 v14, v47, v43, s24\n" \
"v_perm_b32 v15, v47, v43, s25\n" \
"v_lshlrev_b32 v16, 16, v180\n" \
"v_and_b32 v17, 0xffff0000, v180\n" \
"v_lshlrev_b32 v18, 16, v181\n" \
"v_and_b32 v19, 0xffff0000, v181\n" \
"v_lshlrev_b32 v20, 16, v182\n" \
"v_and_b32 v21, 0xffff0000, v182\n" \
"v_lshlrev_b32 v22, 16, v183\n" \
"v_and_b32 v23, 0xffff0000, v183\n" \
"v_lshlrev_b32 v24, 16, v184\n" \
"v_and_b32 v25, 0xffff0000, v184\n" \
"v_lshlrev_b32 v26, 16, v185\n" \
"v_and_b32 v27, 0xffff0000, v185\n" \
"v_lshlrev_b32 v28, 16, v186\n" \
"v_and_b32 v29, 0xffff0000, v186\n" \
"v_lshlrev_b32 v30, 16, v187\n" \
"v_and_b32 v31, 0xffff0000, v187\n" \
"v_and_b32 v173, 0xffff0000, v172\n" \
"v_lshlrev_b32 v172, 16, v172\n" \
"v_and_b32 v175, 0xffff0000, v174\n" \
"v_lshlrev_b32 v174, 16, v174\n" \
"v_and_b32 v177, 0xffff0000, v176\n" \
"v_lshlrev_b32 v176, 16, v176\n" \
"v_and_b32 v179, 0xffff0000, v178\n" \
"v_lshlrev_b32 v178, 16, v178\n" \
"v_lshrrev_b32 v100, 4, v191\n" \
"v_lshl_add_u32 v180, v100, 4, s94\n" \
"v_add_u32 v180, 256, v180\n" \
"v_and_b32 v101, 28, v236\n" \
"v_add_u32 v181, s94, v101\n" \
"v_add_u32 v181, 256, v181\n" \
"v_lshrrev_b32 v101, 1, v191\n" \
"v_and_b32 v102, 1, v191\n" \
"v_lshlrev_b32 v102, 4, v102\n" \
"v_lshl_or_b32 v182, v101, 11, v102\n" \
"s_lshl_b32 s24, %[wid], 5\n" \
"s_add_u32 s24, s24, s94\n" \
"s_add_u32 s24, s24, 512\n" \
"v_bfe_u32 v101, v251, 4, 2\n" \
"v_lshl_add_u32 v183, v101, 2, v102\n" \
"v_add_u32 v183, s24, v183\n" \
"v_and_b32 v101, 3, v191\n" \
"s_add_u32 s24, s94, 512\n" \
"v_lshl_add_u32 v238, v101, 3, s24\n" \
"s_lshl3_add_u32 s24, s64, s65\n" \
"v_lshrrev_b32 v101, 2, v191\n" \
"v_cmp_eq_u32 vcc, s24, v101\n" \
"v_mov_b32 v237, 1073741824\n" \
"s_lshl_b32 s24, s24, 5\n" \
"s_add_u32 s37, s24, 98304\n" \
"v_cndmask_b32 v237, v237, v236, vcc\n" \
"s_add_u32 s29, s37, 262144\n" \
"s_cmp_lt_u32 %[wid], 3\n" \
"s_cselect_b32 s29, s29, 1073741824\n" \
"s_lshl_b32 s24, s69, 11\n" \
"s_add_u32 s24, s24, 98304\n" \
"s_cmp_lt_u32 s65, 28\n" \
"s_cselect_b32 s41, s24, 1073741824\n" \
"s_lshl_b32 s85, s85, 10\n" \
"s_lshl_b32 s86, s86, 10\n" \
"s_cmp_eq_u32 s87, s92\n" \
"s_cselect_b32 s24, 1073741824, s85\n" \
"buffer_store_dwordx4 v[224:227], v248, s[48:51], s24 offen offset:0 sc1\n" \
"s_cmp_eq_u32 s88, s92\n" \
"s_cselect_b32 s24, 1073741824, s85\n" \
"s_add_u32 s24, s24, s84\n" \
"buffer_store_dwordx4 v[228:231], v248, s[48:51], s24 offen offset:1024 sc1\n" \
"s_cmp_eq_u32 s89, s92\n" \
"s_cselect_b32 s24, 1073741824, s86\n" \
"buffer_store_dwordx4 v[208:211], v248, s[48:51], s24 offen offset:0 sc1\n" \
"s_mov_b32 s82, 0x100000\n" \
"s_mov_b32 s93, 0\n" \
"s_waitcnt vmcnt(3)\n" \
".Lkp_%=:\n" \
"v_cndmask_b32 v87, v87, v85, s[42:43]\n" \
"v_cndmask_b32 v86, v86, v84, s[42:43]\n" \
"v_cmp_eq_u32 vcc, 0, v87\n" \
"v_mov_b32 v88, v87\n" \
"s_nop 1\n" \
"s_cbranch_vccnz .Lkr_%=\n" \
".Lkb_%=:\n" \
"v_max_u32_dpp v88, v88, v88 quad_perm:[1,0,3,2] row_mask:0xf bank_mask:0xf\n" \
"s_nop 1\n" \
"v_max_u32_dpp v88, v88, v88 quad_perm:[2,3,0,1] row_mask:0xf bank_mask:0xf\n" \
"s_nop 1\n" \
"v_max_u32_dpp v88, v88, v88 row_half_mirror row_mask:0xf bank_mask:0xf\n" \
"s_nop 1\n" \
"v_max_u32_dpp v88, v88, v88 row_mirror row_mask:0xf bank_mask:0xf\n" \
"s_nop 1\n" \
"v_max_u32_dpp v88, v88, v88 row_bcast:15 row_mask:0xa bank_mask:0xf\n" \
"s_nop 1\n" \
"v_max_u32_dpp v88, v88, v88 row_bcast:31 row_mask:0xc bank_mask:0xf\n" \
"s_nop 0\n" \
"v_readlane_b32 s25, v88, 63\n" \
"s_nop 1\n" \
"v_cmp_eq_u32 vcc, s25, v87\n" \
"v_mov_b32 v89, s25\n" \
"s_nop 2\n" \
"s_ff1_i32_b64 s26, vcc\n" \
"s_and_b32 s26, s26, 63\n" \
"v_readlane_b32 s24, v86, s26\n" \
"s_nop 1\n" \
"v_mov_b32 v88, s24\n" \
"ds_write_b64 v232, v[88:89] offset:64\n" \
"s_waitcnt lgkmcnt(0)\n" \
"s_barrier\n" \
"ds_read_b128 v[84:87], v233 offset:64\n" \
"ds_read_b128 v[88:91], v233 offset:80\n" \
"s_waitcnt lgkmcnt(0)\n" \
"v_cmp_gt_u32 vcc, v85, v87\n" \
"v_max_u32 v85, v85, v87\n" \
"s_nop 0\n" \
"v_cndmask_b32 v84, v86, v84, vcc\n" \
"v_cmp_gt_u32 vcc, v89, v91\n" \
"v_max_u32 v89, v89, v91\n" \
"s_nop 0\n" \
"v_cndmask_b32 v88, v90, v88, vcc\n" \
"v_cmp_gt_u32 vcc, v85, v89\n" \
"v_max_u32 v85, v85, v89\n" \
"s_nop 0\n" \
"v_cndmask_b32 v84, v88, v84, vcc\n" \
"v_readfirstlane_b32 s93, v85\n" \
"s_bfe_u32 s30, s93, 0xb0005\n" \
"s_sub_u32 s30, 2047, s30\n" \
"s_mov_b32 s22, 0x18100800\n" \
"s_mov_b32 s23, 0x01091119\n" \
"s_bfe_u32 s24, s30, 0x30004\n" \
"s_lshl_b32 s24, s24, 3\n" \
"s_lshr_b64 s[22:23], s[22:23], s24\n" \
"s_and_b32 s22, s22, 0xff\n" \
"s_lshr_b32 s25, s30, 7\n" \
"s_lshl_b32 s25, s25, 5\n" \
"s_add_u32 s25, s25, s22\n" \
"s_lshl_b32 s25, s25, 6\n" \
"s_and_b32 s24, s30, 15\n" \
"s_add_u32 s25, s25, s24\n" \
"s_lshl_b32 s25, s25, 6\n" \
"s_add_u32 s25, s25, 59768832\n" \
"s_lshl_b32 s30, s30, 10\n" \
  \
"s_add_u32 s24, s25, 0\n" \
"buffer_load_dword v32, v239, s[32:35], s24 offen sc1\n" \
"s_add_u32 s24, s25, 2097152\n" \
"buffer_load_dword v33, v239, s[32:35], s24 offen sc1\n" \
"s_add_u32 s24, s25, 4194304\n" \
"buffer_load_dword v34, v239, s[32:35], s24 offen sc1\n" \
"s_add_u32 s24, s25, 6291456\n" \
"buffer_load_dword v35, v239, s[32:35], s24 offen sc1\n" \
"s_add_u32 s24, s25, 8388608\n" \
"buffer_load_dword v36, v239, s[32:35], s24 offen sc1\n" \
"s_add_u32 s24, s25, 10485760\n" \
"buffer_load_dword v37, v239, s[32:35], s24 offen sc1\n" \
"s_add_u32 s24, s25, 12582912\n" \
"buffer_load_dword v38, v239, s[32:35], s24 offen sc1\n" \
"s_add_u32 s24, s25, 14680064\n" \
"buffer_load_dword v39, v239, s[32:35], s24 offen sc1\n" \
"buffer_load_dword v40, v236, s[44:47], s30 offen\n" \
"s_cmp_eq_u32 s93, s92\n" \
"s_cselect_b32 s92, 0, s92\n" \
"s_cmp_eq_u32 s87, s92\n" \
"s_cselect_b32 s24, s85, 1073741824\n" \
"buffer_store_dwordx4 v[224:227], v248, s[48:51], s24 offen offset:0 sc1\n" \
"s_cmp_eq_u32 s88, s92\n" \
"s_cselect_b32 s24, s85, 1073741824\n" \
"s_add_u32 s24, s24, s84\n" \
"buffer_store_dwordx4 v[228:231], v248, s[48:51], s24 offen offset:1024 sc1\n" \
"s_cmp_eq_u32 s89, s92\n" \
"s_cselect_b32 s24, s86, 1073741824\n" \
"buffer_store_dwordx4 v[208:211], v248, s[48:51], s24 offen offset:0 sc1\n" \
"v_lshlrev_b32 v86, 16, v84\n" \
"v_and_b32 v88, 0xffff0000, v84\n" \
"v_rcp_f32 v86, v86\n" \
"s_lshr_b32 s24, s93, 16\n" \
"s_and_b32 s25, s24, 0x7fff\n" \
"s_andn2_b32 s26, 0xffff, s24\n" \
"s_bitcmp1_b32 s24, 15\n" \
"s_cselect_b32 s24, s25, s26\n" \
"s_lshl_b32 s24, s24, 16\n" \
"v_mov_b32 v90, 0xbfb8aa3b\n" \
"v_mul_f32 v90, s24, v90\n" \
"v_exp_f32 v90, v90\n" \
"s_nop 0\n" \
"v_add_f32 v90, 1.0, v90\n" \
"v_rcp_f32 v90, v90\n" \
"s_nop 0\n" \
"v_cvt_pk_bf16_f32 v90, v90, v90\n" \
"v_lshlrev_b32 v90, 16, v90\n" \
"s_waitcnt vmcnt(3)\n" \
"v_lshlrev_b32 v44, 16, v32\n" \
"v_and_b32 v45, 0xffff0000, v32\n" \
"v_lshlrev_b32 v46, 16, v33\n" \
"v_and_b32 v47, 0xffff0000, v33\n" \
"v_lshlrev_b32 v48, 16, v34\n" \
"v_and_b32 v49, 0xffff0000, v34\n" \
"v_lshlrev_b32 v50, 16, v35\n" \
"v_and_b32 v51, 0xffff0000, v35\n" \
"v_lshlrev_b32 v52, 16, v36\n" \
"v_and_b32 v53, 0xffff0000, v36\n" \
"v_lshlrev_b32 v54, 16, v37\n" \
"v_and_b32 v55, 0xffff0000, v37\n" \
"v_lshlrev_b32 v56, 16, v38\n" \
"v_and_b32 v57, 0xffff0000, v38\n" \
"v_lshlrev_b32 v58, 16, v39\n" \
"v_and_b32 v59, 0xffff0000, v39\n" \
"v_pk_add_f32 v[44:45], v[44:45], v[46:47]\n" \
"v_pk_add_f32 v[48:49], v[48:49], v[50:51]\n" \
"v_pk_add_f32 v[52:53], v[52:53], v[54:55]\n" \
"v_pk_add_f32 v[56:57], v[56:57], v[58:59]\n" \
"v_pk_add_f32 v[44:45], v[44:45], v[48:49]\n" \
"v_pk_add_f32 v[52:53], v[52:53], v[56:57]\n" \
"v_lshlrev_b32 v60, 16, v40\n" \
"v_pk_add_f32 v[44:45], v[44:45], v[52:53]\n" \
"v_and_b32 v61, 0xffff0000, v40\n" \
"v_cvt_pk_bf16_f32 v46, v44, v45\n" \
"v_lshlrev_b32 v44, 16, v46\n" \
"v_and_b32 v45, 0xffff0000, v46\n" \
"v_pk_mul_f32 v[44:45], v[44:45], v[172:173]\n" \
"v_cvt_pk_bf16_f32 v46, v44, v45\n" \
"v_lshlrev_b32 v44, 16, v46\n" \
"v_and_b32 v45, 0xffff0000, v46\n" \
"v_pk_add_f32 v[60:61], v[60:61], v[44:45]\n" \
"v_cvt_pk_bf16_f32 v46, v60, v61\n" \
"v_lshlrev_b32 v60, 16, v46\n" \
"v_and_b32 v61, 0xffff0000, v46\n" \
"v_pk_add_f32 v[44:45], v[60:61], v[88:89] op_sel_hi:[1,0] neg_lo:[0,1] neg_hi:[0,1]\n" \
"v_cvt_pk_bf16_f32 v46, v44, v45\n" \
"v_lshlrev_b32 v44, 16, v46\n" \
"v_and_b32 v45, 0xffff0000, v46\n" \
"v_pk_mul_f32 v[44:45], v[44:45], v[86:87] op_sel_hi:[1,0]\n" \
"v_cvt_pk_bf16_f32 v46, v44, v45\n" \
"v_lshlrev_b32 v44, 16, v46\n" \
"v_and_b32 v45, 0xffff0000, v46\n" \
"v_pk_mul_f32 v[44:45], v[44:45], v[176:177]\n" \
"v_cvt_pk_bf16_f32 v46, v44, v45\n" \
"v_lshlrev_b32 v44, 16, v46\n" \
"v_and_b32 v45, 0xffff0000, v46\n" \
"v_pk_add_f32 v[62:63], v[44:45], v[174:175]\n" \
"v_cvt_pk_bf16_f32 v46, v62, v63\n" \
"v_lshlrev_b32 v62, 16, v46\n" \
"v_and_b32 v63, 0xffff0000, v46\n" \
"v_dot2_f32_bf16 v64, v0, v46, 0\n" \
"v_dot2_f32_bf16 v65, v1, v46, 0\n" \
"v_dot2_f32_bf16 v66, v2, v46, 0\n" \
"v_dot2_f32_bf16 v67, v3, v46, 0\n" \
"v_dot2_f32_bf16 v68, v4, v46, 0\n" \
"v_dot2_f32_bf16 v69, v5, v46, 0\n" \
"v_dot2_f32_bf16 v70, v6, v46, 0\n" \
"v_dot2_f32_bf16 v71, v7, v46, 0\n" \
"v_dot2_f32_bf16 v72, v8, v46, 0\n" \
"v_dot2_f32_bf16 v73, v9, v46, 0\n" \
"v_dot2_f32_bf16 v74, v10, v46, 0\n" \
"v_dot2_f32_bf16 v75, v11, v46, 0\n" \
"v_dot2_f32_bf16 v76, v12, v46, 0\n" \
"v_dot2_f32_bf16 v77, v13, v46, 0\n" \
"v_dot2_f32_bf16 v78, v14, v46, 0\n" \
"v_dot2_f32_bf16 v79, v15, v46, 0\n" \
"s_nop 1\n" \
"v_permlane32_swap_b32 v64, v72\n" \
"v_permlane32_swap_b32 v65, v73\n" \
"v_permlane32_swap_b32 v66, v74\n" \
"v_permlane32_swap_b32 v67, v75\n" \
"v_permlane32_swap_b32 v68, v76\n" \
"v_permlane32_swap_b32 v69, v77\n" \
"v_permlane32_swap_b32 v70, v78\n" \
"v_permlane32_swap_b32 v71, v79\n" \
"v_pk_add_f32 v[64:65], v[64:65], v[72:73]\n" \
"v_pk_add_f32 v[66:67], v[66:67], v[74:75]\n" \
"v_pk_add_f32 v[68:69], v[68:69], v[76:77]\n" \
"v_pk_add_f32 v[70:71], v[70:71], v[78:79]\n" \
"s_nop 0\n" \
"v_permlane16_swap_b32 v64, v68\n" \
"v_permlane16_swap_b32 v65, v69\n" \
"v_permlane16_swap_b32 v66, v70\n" \
"v_permlane16_swap_b32 v67, v71\n" \
"v_pk_add_f32 v[64:65], v[64:65], v[68:69]\n" \
"v_pk_add_f32 v[66:67], v[66:67], v[70:71]\n" \
"s_nop 0\n" \
"v_add_f32_dpp v64, v64, v64 quad_perm:[1,0,3,2] row_mask:0xf bank_mask:0xf\n" \
"v_add_f32_dpp v65, v65, v65 quad_perm:[1,0,3,2] row_mask:0xf bank_mask:0xf\n" \
"v_add_f32_dpp v66, v66, v66 quad_perm:[1,0,3,2] row_mask:0xf bank_mask:0xf\n" \
"v_add_f32_dpp v67, v67, v67 quad_perm:[1,0,3,2] row_mask:0xf bank_mask:0xf\n" \
"v_add_f32_dpp v64, v64, v64 quad_perm:[2,3,0,1] row_mask:0xf bank_mask:0xf\n" \
"v_add_f32_dpp v65, v65, v65 quad_perm:[2,3,0,1] row_mask:0xf bank_mask:0xf\n" \
"v_add_f32_dpp v66, v66, v66 quad_perm:[2,3,0,1] row_mask:0xf bank_mask:0xf\n" \
"v_add_f32_dpp v67, v67, v67 quad_perm:[2,3,0,1] row_mask:0xf bank_mask:0xf\n" \
"v_add_f32_dpp v64, v64, v64 row_half_mirror row_mask:0xf bank_mask:0xf\n" \
"v_add_f32_dpp v65, v65, v65 row_half_mirror row_mask:0xf bank_mask:0xf\n" \
"v_add_f32_dpp v66, v66, v66 row_half_mirror row_mask:0xf bank_mask:0xf\n" \
"v_add_f32_dpp v67, v67, v67 row_half_mirror row_mask:0xf bank_mask:0xf\n" \
"v_add_f32_dpp v64, v64, v64 row_mirror row_mask:0xf bank_mask:0xf\n" \
"v_add_f32_dpp v65, v65, v65 row_mirror row_mask:0xf bank_mask:0xf\n" \
"v_add_f32_dpp v66, v66, v66 row_mirror row_mask:0xf bank_mask:0xf\n" \
"v_add_f32_dpp v67, v67, v67 row_mirror row_mask:0xf bank_mask:0xf\n" \
"ds_write_b128 v180, v[64:67]\n" \
"s_waitcnt lgkmcnt(0)\n" \
"s_barrier\n" \
"ds_read2_b32 v[64:65], v181 offset0:0 offset1:8\n" \
"ds_read2_b32 v[66:67], v181 offset0:16 offset1:24\n" \
"ds_read2_b32 v[68:69], v181 offset0:32 offset1:40\n" \
"ds_read2_b32 v[70:71], v181 offset0:48 offset1:56\n" \
"s_waitcnt lgkmcnt(0)\n" \
"v_pk_add_f32 v[64:65], v[64:65], v[66:67]\n" \
"v_pk_add_f32 v[68:69], v[68:69], v[70:71]\n" \
"v_pk_add_f32 v[64:65], v[64:65], v[68:69]\n" \
"v_cvt_pk_bf16_f32 v66, v64, v65\n" \
"v_lshlrev_b32 v64, 16, v66\n" \
"v_and_b32 v65, 0xffff0000, v66\n" \
"v_mul_f32 v66, 0xbfb8aa3b, v64\n" \
"v_exp_f32 v66, v66\n" \
"s_nop 0\n" \
"v_add_f32 v66, 1.0, v66\n" \
"v_rcp_f32 v66, v66\n" \
"s_nop 0\n" \
"v_mul_f32 v64, v64, v66\n" \
"v_cvt_pk_bf16_f32 v64, v64, v64\n" \
"v_lshlrev_b32 v64, 16, v64\n" \
"v_mul_f32 v64, v64, v65\n" \
"v_cvt_pk_bf16_f32 v64, v64, v64\n" \
"v_lshlrev_b32 v64, 16, v64\n" \
"s_nop 0\n" \
"v_readlane_b32 s16, v64, 0\n" \
"v_readlane_b32 s17, v64, 1\n" \
"v_readlane_b32 s18, v64, 2\n" \
"v_readlane_b32 s19, v64, 3\n" \
"v_readlane_b32 s20, v64, 4\n" \
"v_readlane_b32 s21, v64, 5\n" \
"v_readlane_b32 s22, v64, 6\n" \
"v_readlane_b32 s23, v64, 7\n" \
"v_mul_f32 v66, s16, v16\n" \
"v_mul_f32 v67, s16, v17\n" \
"v_mul_f32 v68, s17, v18\n" \
"v_mul_f32 v69, s17, v19\n" \
"v_fmac_f32 v66, s18, v20\n" \
"v_fmac_f32 v67, s18, v21\n" \
"v_fmac_f32 v68, s19, v22\n" \
"v_fmac_f32 v69, s19, v23\n" \
"v_fmac_f32 v66, s20, v24\n" \
"v_fmac_f32 v67, s20, v25\n" \
"v_fmac_f32 v68, s21, v26\n" \
"v_fmac_f32 v69, s21, v27\n" \
"v_fmac_f32 v66, s22, v28\n" \
"v_fmac_f32 v67, s22, v29\n" \
"v_fmac_f32 v68, s23, v30\n" \
"v_fmac_f32 v69, s23, v31\n" \
"v_pk_add_f32 v[66:67], v[66:67], v[68:69]\n" \
"v_min_u32 v66, -2, v66\n" \
"v_min_u32 v67, -2, v67\n" \
"buffer_store_dwordx2 v[66:67], v190, s[32:35], s41 offen sc1\n" \
  \
"s_cmp_ge_u32 %[bid], 64\n" \
"s_cbranch_scc1 .Lend_%=\n" \
"s_mov_b32 s82, 0x100000\n" \
"s_sleep 14\n" \
".Lfp_%=:\n" \
"buffer_load_dwordx4 v[64:67], v182, s[32:35], s37 offen sc1\n" \
"buffer_load_dwordx4 v[68:71], v182, s[32:35], s29 offen sc1\n" \
"s_waitcnt vmcnt(0)\n" \
"v_max3_u32 v72, v64, v65, v66\n" \
"v_max3_u32 v73, v67, v68, v69\n" \
"v_max3_u32 v72, v72, v70, v71\n" \
"v_max_u32 v72, v72, v73\n" \
"v_cmp_eq_u32 vcc, -1, v72\n" \
"v_pk_add_f32 v[64:65], v[64:65], v[68:69]\n" \
"v_pk_add_f32 v[66:67], v[66:67], v[70:71]\n" \
"s_nop 0\n" \
"s_cbranch_vccnz .Lfr_%=\n" \
".Lfb_%=:\n" \
"v_permlane32_swap_b32 v64, v66\n" \
"v_permlane32_swap_b32 v65, v67\n" \
"v_pk_add_f32 v[64:65], v[64:65], v[66:67]\n" \
"s_nop 1\n" \
"v_permlane16_swap_b32 v64, v65\n" \
"v_add_f32 v64, v64, v65\n" \
"s_nop 1\n" \
"v_add_f32_dpp v64, v64, v64 quad_perm:[2,3,0,1] row_mask:0xf bank_mask:0xf\n" \
"s_nop 1\n" \
"v_add_f32_dpp v64, v64, v64 row_ror:4 row_mask:0xf bank_mask:0xf\n" \
"s_nop 1\n" \
"v_add_f32_dpp v64, v64, v64 row_ror:8 row_mask:0xf bank_mask:0xf\n" \
"ds_write_b32 v183, v64\n" \
"s_waitcnt lgkmcnt(0)\n" \
"s_barrier\n" \
"ds_read2_b64 v[64:67], v238 offset0:0 offset1:4\n" \
"ds_read2_b64 v[68:71], v238 offset0:8 offset1:12\n" \
"s_waitcnt lgkmcnt(0)\n" \
"v_pk_add_f32 v[64:65], v[64:65], v[66:67]\n" \
"v_pk_add_f32 v[68:69], v[68:69], v[70:71]\n" \
"v_pk_add_f32 v[64:65], v[64:65], v[68:69]\n" \
"v_cvt_pk_bf16_f32 v46, v64, v65\n" \
"v_lshlrev_b32 v64, 16, v46\n" \
"v_and_b32 v65, 0xffff0000, v46\n" \
"v_pk_mul_f32 v[64:65], v[64:65], v[90:91] op_sel_hi:[1,0]\n" \
"v_cvt_pk_bf16_f32 v46, v64, v65\n" \
"v_lshlrev_b32 v64, 16, v46\n" \
"v_and_b32 v65, 0xffff0000, v46\n" \
"v_pk_add_f32 v[64:65], v[64:65], v[62:63]\n" \
"v_cvt_pk_bf16_f32 v46, v64, v65\n" \
"v_lshlrev_b32 v64, 16, v46\n" \
"v_and_b32 v65, 0xffff0000, v46\n" \
"v_pk_mul_f32 v[64:65], v[64:65], v[178:179]\n" \
"v_cvt_pk_bf16_f32 v46, v64, v65\n" \
"v_lshlrev_b32 v64, 16, v46\n" \
"v_and_b32 v65, 0xffff0000, v46\n" \
"v_pk_add_f32 v[64:65], v[64:65], v[60:61]\n" \
"v_cvt_pk_bf16_f32 v64, v64, v65\n" \
"buffer_store_dword v64, v237, s[48:51], s30 offen sc1\n" \
"s_branch .Lend_%=\n" \
".Lrare_%=:\n" \
".Lspq_%=:\n" \
"s_mov_b32 s82, 0x100000\n" \
".Lsqq_%=:\n" \
"s_sleep 6\n" \
"buffer_load_dwordx2 v[204:205], v253, s[32:35], s81 offen sc1\n" \
"s_waitcnt vmcnt(0)\n" \
"v_cmp_ne_u64 vcc, v[204:205], v[202:203]\n" \
"s_cbranch_vccz .Lsbq_%=\n" \
"s_sub_u32 s82, s82, 1\n" \
"s_cbranch_scc0 .Lsqq_%=\n" \
"s_branch .Lsbq_%=\n" \
".Lsph_%=:\n" \
"s_mov_b32 s82, 0x100000\n" \
".Lsqh_%=:\n" \
"s_sleep 6\n" \
"buffer_load_dword v247, v252, s[32:35], s81 offen sc1\n" \
"s_waitcnt vmcnt(0)\n" \
"v_cmp_eq_u32 vcc, 0, v247\n" \
"s_cbranch_vccz .Lsbh_%=\n" \
"s_sub_u32 s82, s82, 1\n" \
"s_cbranch_scc0 .Lsqh_%=\n" \
"s_branch .Lsbh_%=\n" \
".Lspv_%=:\n" \
"s_mov_b32 s82, 0x100000\n" \
".Lsqv_%=:\n" \
"s_sleep 6\n" \
"buffer_load_dword v253, v252, s[32:35], s81 offen sc1\n" \
"s_waitcnt vmcnt(0)\n" \
"v_cmp_eq_u32 vcc, 0, v253\n" \
"s_cbranch_vccz .Lsbv_%=\n" \
"s_sub_u32 s82, s82, 1\n" \
"s_cbranch_scc0 .Lsqv_%=\n" \
"s_branch .Lsbv_%=\n" \
".Lchd_%=:\n" \
".Lst1_8_%=:\n" \
"s_cbranch_execz .Lst1_12_%=\n" \
"v_cmp_eq_u32 vcc, 0, v247\n" \
"s_sub_u32 s23, s18, 262144\n" \
"s_cmp_eq_u32 s24, s23\n" \
"s_cbranch_scc1 .Ldhd_%=\n" \
"s_mov_b32 s24, s23\n" \
"buffer_load_dword v247, v244, s[32:35], s26 offen sc1\n" \
"s_branch .Lsbhd_%=\n" \
".Ldhd_%=:\n" \
"s_cbranch_vccz .Lsbhd_%=\n" \
".Lsphd_%=:\n" \
"s_mov_b32 s82, 0x100000\n" \
".Lsqhd_%=:\n" \
"s_sleep 6\n" \
"buffer_load_dword v247, v244, s[32:35], s26 offen sc1\n" \
"s_waitcnt vmcnt(0)\n" \
"v_cmp_eq_u32 vcc, 0, v247\n" \
"s_cbranch_vccz .Lsbhd_%=\n" \
"s_sub_u32 s82, s82, 1\n" \
"s_cbranch_scc0 .Lsqhd_%=\n" \
"s_branch .Lsbhd_%=\n" \
".Land_%=:\n" \
".Lst1_9_%=:\n" \
"s_cbranch_execz .Lst1_13_%=\n" \
"s_waitcnt vmcnt(4)\n" \
"buffer_store_dword a56, off, s[32:35], s25 sc1\n" \
"buffer_store_dword a56, off, s[32:35], s78 sc1\n" \
"s_mov_b32 s78, 1073741824\n" \
"s_mov_b32 s76, 0\n" \
"s_branch .Labd_%=\n" \
".Lchtx_%=:\n" \
".Lst1_10_%=:\n" \
"s_cbranch_execz .Lst1_14_%=\n" \
"v_cmp_eq_u32 vcc, 0, v247\n" \
"s_sub_u32 s23, s18, 262144\n" \
"s_cmp_eq_u32 s24, s23\n" \
"s_cbranch_scc1 .Ldhtx_%=\n" \
"s_mov_b32 s24, s23\n" \
"buffer_load_dword v247, v244, s[32:35], s26 offen sc1\n" \
"s_branch .Lsbhtx_%=\n" \
".Ldhtx_%=:\n" \
"s_cbranch_vccz .Lsbhtx_%=\n" \
".Lsphtx_%=:\n" \
"s_mov_b32 s82, 0x100000\n" \
".Lsqhtx_%=:\n" \
"s_sleep 6\n" \
"buffer_load_dword v247, v244, s[32:35], s26 offen sc1\n" \
"s_waitcnt vmcnt(0)\n" \
"v_cmp_eq_u32 vcc, 0, v247\n" \
"s_cbranch_vccz .Lsbhtx_%=\n" \
"s_sub_u32 s82, s82, 1\n" \
"s_cbranch_scc0 .Lsqhtx_%=\n" \
"s_branch .Lsbhtx_%=\n" \
".Lantx_%=:\n" \
".Lst1_11_%=:\n" \
"s_cbranch_execz .Lstb1_%=\n" \
"s_waitcnt vmcnt(10)\n" \
"buffer_store_dword a56, off, s[32:35], s25 sc1\n" \
"buffer_store_dword a56, off, s[32:35], s78 sc1\n" \
"s_mov_b32 s78, 1073741824\n" \
"s_mov_b32 s76, 0\n" \
"s_branch .Labtx_%=\n" \
".Lcht_%=:\n" \
".Lst1_12_%=:\n" \
"s_cbranch_execz .Lstb1_%=\n" \
"v_cmp_eq_u32 vcc, 0, v247\n" \
"s_sub_u32 s23, s18, 262144\n" \
"s_cmp_eq_u32 s24, s23\n" \
"s_cbranch_scc1 .Ldht_%=\n" \
"s_mov_b32 s24, s23\n" \
"buffer_load_dword v247, v244, s[32:35], s26 offen sc1\n" \
"s_branch .Lsbht_%=\n" \
".Ldht_%=:\n" \
"s_cbranch_vccz .Lsbht_%=\n" \
".Lspht_%=:\n" \
"s_mov_b32 s82, 0x100000\n" \
".Lsqht_%=:\n" \
"s_sleep 6\n" \
"buffer_load_dword v247, v244, s[32:35], s26 offen sc1\n" \
"s_waitcnt vmcnt(0)\n" \
"v_cmp_eq_u32 vcc, 0, v247\n" \
"s_cbranch_vccz .Lsbht_%=\n" \
"s_sub_u32 s82, s82, 1\n" \
"s_cbranch_scc0 .Lsqht_%=\n" \
"s_branch .Lsbht_%=\n" \
".Lant_%=:\n" \
".Lst1_13_%=:\n" \
"s_cbranch_execz .Lstb1_%=\n" \
"s_waitcnt vmcnt(9)\n" \
"buffer_store_dword a56, off, s[32:35], s25 sc1\n" \
"buffer_store_dword a56, off, s[32:35], s78 sc1\n" \
"s_mov_b32 s78, 1073741824\n" \
"s_mov_b32 s76, 0\n" \
"s_branch .Labt_%=\n" \
".Lspl_%=:\n" \
"s_mov_b32 s82, 0x100000\n" \
".Lsql_%=:\n" \
"s_sleep 6\n" \
"buffer_load_dword v247, v255, s[32:35], s31 offen sc1\n" \
"s_waitcnt vmcnt(0)\n" \
"v_cmp_eq_u32 vcc, 0, v247\n" \
"s_cbranch_vccz .Lsbl_%=\n" \
"s_sub_u32 s82, s82, 1\n" \
"s_cbranch_scc0 .Lsql_%=\n" \
"s_branch .Lsbl_%=\n" \
".Lrh_%=:\n" \
"s_call_b64 s[98:99], .Lredo2_%=\n" \
"v_accvgpr_read_b32 v224, a40\n" \
"v_rcp_f32 v224, v224\n" \
"s_nop 0\n" \
"v_mul_f32 v64, v64, v224\n" \
"v_mul_f32 v65, v65, v224\n" \
"v_mul_f32 v66, v66, v224\n" \
"v_mul_f32 v67, v67, v224\n" \
"v_mul_f32 v68, v68, v224\n" \
"v_mul_f32 v69, v69, v224\n" \
"v_mul_f32 v70, v70, v224\n" \
"v_mul_f32 v71, v71, v224\n" \
"v_cvt_pk_bf16_f32 v192, v64, v65\n" \
"v_cvt_pk_bf16_f32 v193, v66, v67\n" \
"v_cvt_pk_bf16_f32 v194, v68, v69\n" \
"v_cvt_pk_bf16_f32 v195, v70, v71\n" \
"v_mul_f32 v72, v72, v224\n" \
"v_mul_f32 v73, v73, v224\n" \
"v_mul_f32 v74, v74, v224\n" \
"v_mul_f32 v75, v75, v224\n" \
"v_mul_f32 v76, v76, v224\n" \
"v_mul_f32 v77, v77, v224\n" \
"v_mul_f32 v78, v78, v224\n" \
"v_mul_f32 v79, v79, v224\n" \
"v_cvt_pk_bf16_f32 v200, v72, v73\n" \
"v_cvt_pk_bf16_f32 v201, v74, v75\n" \
"v_cvt_pk_bf16_f32 v202, v76, v77\n" \
"v_cvt_pk_bf16_f32 v203, v78, v79\n" \
"v_mul_f32 v80, v80, v224\n" \
"v_mul_f32 v81, v81, v224\n" \
"v_mul_f32 v82, v82, v224\n" \
"v_mul_f32 v83, v83, v224\n" \
"v_mul_f32 v84, v84, v224\n" \
"v_mul_f32 v85, v85, v224\n" \
"v_mul_f32 v86, v86, v224\n" \
"v_mul_f32 v87, v87, v224\n" \
"v_cvt_pk_bf16_f32 v208, v80, v81\n" \
"v_cvt_pk_bf16_f32 v209, v82, v83\n" \
"v_cvt_pk_bf16_f32 v210, v84, v85\n" \
"v_cvt_pk_bf16_f32 v211, v86, v87\n" \
"v_mul_f32 v88, v88, v224\n" \
"v_mul_f32 v89, v89, v224\n" \
"buffer_store_dwordx4 v[192:195], v248, s[32:35], s39 offen offset:0 sc1\n" \
"buffer_store_dwordx4 v[200:203], v248, s[32:35], s39 offen offset:2048 sc1\n" \
"s_add_u32 s38, s66, 3\n" \
"s_and_b32 s38, s38, 3\n" \
"s_lshl_b32 s38, s38, 13\n" \
"s_add_u32 s38, s38, s29\n" \
"buffer_load_dwordx4 a[120:123], v248, s[32:35], s38 offen offset:0 sc1\n" \
"buffer_load_dwordx4 a[124:127], v248, s[32:35], s38 offen offset:1024 sc1\n" \
"buffer_load_dwordx4 a[128:131], v248, s[32:35], s38 offen offset:2048 sc1\n" \
"buffer_load_dwordx4 a[132:135], v248, s[32:35], s38 offen offset:3072 sc1\n" \
"s_add_u32 s38, s38, 4096\n" \
"buffer_load_dwordx4 a[136:139], v248, s[32:35], s38 offen offset:0 sc1\n" \
"buffer_load_dwordx4 a[140:143], v248, s[32:35], s38 offen offset:1024 sc1\n" \
"buffer_load_dwordx4 a[144:147], v248, s[32:35], s38 offen offset:2048 sc1\n" \
"buffer_load_dwordx4 a[148:151], v248, s[32:35], s38 offen offset:3072 sc1\n" \
"s_waitcnt vmcnt(0)\n" \
"s_branch .Lrhb_%=\n" \
".LspcA_%=:\n" \
"s_mov_b32 s82, 0x100000\n" \
".LsqcA_%=:\n" \
"s_sleep 6\n" \
"buffer_load_dword v247, v254, s[32:35], s41 offen sc1\n" \
"s_waitcnt vmcnt(0)\n" \
"v_cmp_eq_u32 vcc, 0, v247\n" \
"s_cbranch_vccz .LsbcA_%=\n" \
"s_sub_u32 s82, s82, 1\n" \
"s_cbranch_scc0 .LsqcA_%=\n" \
"s_branch .LsbcA_%=\n" \
".Lspg_%=:\n" \
"s_mov_b32 s82, 0x100000\n" \
".Lsqg_%=:\n" \
"s_sleep 6\n" \
"buffer_load_dwordx2 v[230:231], v232, s[32:35], s70 offen sc1\n" \
"s_waitcnt vmcnt(0)\n" \
"v_cmp_ne_u64 vcc, v[230:231], v[234:235]\n" \
"s_cbranch_vccz .Lsbg_%=\n" \
"s_sub_u32 s82, s82, 1\n" \
"s_cbranch_scc0 .Lsqg_%=\n" \
"s_branch .Lsbg_%=\n" \
".Lch3_%=:\n" \
".Lst1_14_%=:\n" \
"s_cbranch_execz .Lstb1_%=\n" \
"s_mov_b64 exec, 0\n" \
"s_cmp_eq_u32 %[wid], 0\n" \
"s_cbranch_scc1 .Lst3_0_%=\n" \
"s_cmp_eq_u32 %[wid], 1\n" \
"s_cbranch_scc1 .Lst3_1_%=\n" \
"s_cmp_eq_u32 %[wid], 2\n" \
"s_cbranch_scc1 .Lst3_2_%=\n" \
"s_branch .Lst3_3_%=\n" \
".Lstb3_%=:\n" \
"s_mov_b64 exec, -1\n" \
"s_setpc_b64 s[98:99]\n" \
".Lga_%=:\n" \
".Lst3_2_%=:\n" \
"s_cbranch_execz .Lstb3_%=\n" \
"s_waitcnt vmcnt(0)\n" \
"v_cmp_ne_u32 vcc, 0, v245\n" \
"s_mov_b32 s26, 0x11111111\n" \
"s_mov_b32 s27, s19\n" \
"s_lshr_b64 s[76:77], vcc, 1\n" \
"s_and_b64 s[76:77], s[76:77], vcc\n" \
"s_lshr_b64 s[24:25], s[76:77], 2\n" \
"s_and_b64 s[76:77], s[76:77], s[24:25]\n" \
"s_and_b64 s[76:77], s[76:77], s[26:27]\n" \
"s_sub_u32 s82, s82, 1\n" \
"s_cselect_b64 s[76:77], s[26:27], s[76:77]\n" \
"s_not_b64 vcc, vcc\n" \
"s_andn2_b64 s[76:77], s[76:77], s[74:75]\n" \
"s_cbranch_scc0 .Lgx_%=\n" \
"s_or_b64 s[74:75], s[74:75], s[76:77]\n" \
"s_cmp_eq_u32 s76, 0\n" \
"s_cbranch_scc1 .Lgs08_%=\n" \
"s_bitcmp1_b32 s76, 0\n" \
"s_cbranch_scc0 .Lgs00_%=\n" \
"s_add_u32 s24, s95, 0\n" \
"buffer_load_dwordx4 a[128:131], v214, s[32:35], s24 offen sc1\n" \
"s_add_u32 s24, s24, s84\n" \
"buffer_load_dwordx4 a[160:163], v214, s[32:35], s24 offen offset:64 sc1\n" \
".Lgs00_%=:\n" \
"s_bitcmp1_b32 s76, 4\n" \
"s_cbranch_scc0 .Lgs01_%=\n" \
"s_add_u32 s24, s95, 2097152\n" \
"buffer_load_dwordx4 a[132:135], v214, s[32:35], s24 offen sc1\n" \
"s_add_u32 s24, s24, s84\n" \
"buffer_load_dwordx4 a[164:167], v214, s[32:35], s24 offen offset:64 sc1\n" \
".Lgs01_%=:\n" \
"s_bitcmp1_b32 s76, 8\n" \
"s_cbranch_scc0 .Lgs02_%=\n" \
"s_add_u32 s24, s95, 4194304\n" \
"buffer_load_dwordx4 a[136:139], v214, s[32:35], s24 offen sc1\n" \
"s_add_u32 s24, s24, s84\n" \
"buffer_load_dwordx4 a[168:171], v214, s[32:35], s24 offen offset:64 sc1\n" \
".Lgs02_%=:\n" \
"s_bitcmp1_b32 s76, 12\n" \
"s_cbranch_scc0 .Lgs03_%=\n" \
"s_add_u32 s24, s95, 6291456\n" \
"buffer_load_dwordx4 a[140:143], v214, s[32:35], s24 offen sc1\n" \
"s_add_u32 s24, s24, s84\n" \
"buffer_load_dwordx4 a[172:175], v214, s[32:35], s24 offen offset:64 sc1\n" \
".Lgs03_%=:\n" \
"s_bitcmp1_b32 s76, 16\n" \
"s_cbranch_scc0 .Lgs04_%=\n" \
"s_add_u32 s24, s95, 8388608\n" \
"buffer_load_dwordx4 a[144:147], v214, s[32:35], s24 offen sc1\n" \
"s_add_u32 s24, s24, s84\n" \
"buffer_load_dwordx4 a[176:179], v214, s[32:35], s24 offen offset:64 sc1\n" \
".Lgs04_%=:\n" \
"s_bitcmp1_b32 s76, 20\n" \
"s_cbranch_scc0 .Lgs05_%=\n" \
"s_add_u32 s24, s95, 10485760\n" \
"buffer_load_dwordx4 a[148:151], v214, s[32:35], s24 offen sc1\n" \
"s_add_u32 s24, s24, s84\n" \
"buffer_load_dwordx4 a[180:183], v214, s[32:35], s24 offen offset:64 sc1\n" \
".Lgs05_%=:\n" \
"s_bitcmp1_b32 s76, 24\n" \
"s_cbranch_scc0 .Lgs06_%=\n" \
"s_add_u32 s24, s95, 12582912\n" \
"buffer_load_dwordx4 a[152:155], v214, s[32:35], s24 offen sc1\n" \
"s_add_u32 s24, s24, s84\n" \
"buffer_load_dwordx4 a[184:187], v214, s[32:35], s24 offen offset:64 sc1\n" \
".Lgs06_%=:\n" \
"s_bitcmp1_b32 s76, 28\n" \
"s_cbranch_scc0 .Lgs07_%=\n" \
"s_add_u32 s24, s95, 14680064\n" \
"buffer_load_dwordx4 a[156:159], v214, s[32:35], s24 offen sc1\n" \
"s_add_u32 s24, s24, s84\n" \
"buffer_load_dwordx4 a[188:191], v214, s[32:35], s24 offen offset:64 sc1\n" \
".Lgs07_%=:\n" \
".Lgs08_%=:\n" \
"s_cmp_eq_u32 s77, 0\n" \
"s_cbranch_scc1 .Lgs18_%=\n" \
"s_bitcmp1_b32 s77, 0\n" \
"s_cbranch_scc0 .Lgs10_%=\n" \
"s_add_u32 s24, s90, 0\n" \
"buffer_load_dwordx4 v[0:3], v214, s[32:35], s24 offen sc1\n" \
".Lgs10_%=:\n" \
"s_bitcmp1_b32 s77, 4\n" \
"s_cbranch_scc0 .Lgs11_%=\n" \
"s_add_u32 s24, s90, 2097152\n" \
"buffer_load_dwordx4 v[4:7], v214, s[32:35], s24 offen sc1\n" \
".Lgs11_%=:\n" \
"s_bitcmp1_b32 s77, 8\n" \
"s_cbranch_scc0 .Lgs12_%=\n" \
"s_add_u32 s24, s90, 4194304\n" \
"buffer_load_dwordx4 v[8:11], v214, s[32:35], s24 offen sc1\n" \
".Lgs12_%=:\n" \
"s_bitcmp1_b32 s77, 12\n" \
"s_cbranch_scc0 .Lgs13_%=\n" \
"s_add_u32 s24, s90, 6291456\n" \
"buffer_load_dwordx4 v[12:15], v214, s[32:35], s24 offen sc1\n" \
".Lgs13_%=:\n" \
"s_bitcmp1_b32 s77, 16\n" \
"s_cbranch_scc0 .Lgs14_%=\n" \
"s_add_u32 s24, s90, 8388608\n" \
"buffer_load_dwordx4 v[16:19], v214, s[32:35], s24 offen sc1\n" \
".Lgs14_%=:\n" \
"s_bitcmp1_b32 s77, 20\n" \
"s_cbranch_scc0 .Lgs15_%=\n" \
"s_add_u32 s24, s90, 10485760\n" \
"buffer_load_dwordx4 v[20:23], v214, s[32:35], s24 offen sc1\n" \
".Lgs15_%=:\n" \
"s_bitcmp1_b32 s77, 24\n" \
"s_cbranch_scc0 .Lgs16_%=\n" \
"s_add_u32 s24, s90, 12582912\n" \
"buffer_load_dwordx4 v[24:27], v214, s[32:35], s24 offen sc1\n" \
".Lgs16_%=:\n" \
"s_bitcmp1_b32 s77, 28\n" \
"s_cbranch_scc0 .Lgs17_%=\n" \
"s_add_u32 s24, s90, 14680064\n" \
"buffer_load_dwordx4 v[28:31], v214, s[32:35], s24 offen sc1\n" \
".Lgs17_%=:\n" \
".Lgs18_%=:\n" \
".Lgx_%=:\n" \
"s_setpc_b64 s[98:99]\n" \
".Lgr0_%=:\n" \
"s_mov_b32 vcc_hi, 0\n" \
"s_bcnt1_i32_b64 s27, vcc\n" \
"s_cmp_lt_u32 s27, 8\n" \
"s_cbranch_scc1 .Lnap1_%=\n" \
"s_sleep 10\n" \
".Lnap1_%=:\n" \
"s_sleep 2\n" \
"buffer_load_dword v245, v255, s[32:35], s18 offen sc1\n" \
"s_branch .Lg0_%=\n" \
".Lr2_%=:\n" \
"v_mfma_f32_16x16x32_bf16 v[64:67], v[210:213], a[160:163], 0\n" \
"v_mfma_f32_16x16x32_bf16 v[68:71], v[208:211], a[160:163], 0\n" \
"v_mfma_f32_16x16x32_bf16 v[64:67], v[210:213], a[164:167], v[64:67]\n" \
"v_mfma_f32_16x16x32_bf16 v[68:71], v[208:211], a[164:167], v[68:71]\n" \
"v_mfma_f32_16x16x32_bf16 v[64:67], v[210:213], a[168:171], v[64:67]\n" \
"v_mfma_f32_16x16x32_bf16 v[68:71], v[208:211], a[168:171], v[68:71]\n" \
"v_mfma_f32_16x16x32_bf16 v[64:67], v[210:213], a[172:175], v[64:67]\n" \
"v_mfma_f32_16x16x32_bf16 v[68:71], v[208:211], a[172:175], v[68:71]\n" \
"v_mfma_f32_16x16x32_bf16 v[64:67], v[210:213], a[176:179], v[64:67]\n" \
"v_mfma_f32_16x16x32_bf16 v[68:71], v[208:211], a[176:179], v[68:71]\n" \
"v_mfma_f32_16x16x32_bf16 v[64:67], v[210:213], a[180:183], v[64:67]\n" \
"v_mfma_f32_16x16x32_bf16 v[68:71], v[208:211], a[180:183], v[68:71]\n" \
"v_mfma_f32_16x16x32_bf16 v[64:67], v[210:213], a[184:187], v[64:67]\n" \
"v_mfma_f32_16x16x32_bf16 v[68:71], v[208:211], a[184:187], v[68:71]\n" \
"v_mfma_f32_16x16x32_bf16 v[64:67], v[210:213], a[188:191], v[64:67]\n" \
"v_mfma_f32_16x16x32_bf16 v[68:71], v[208:211], a[188:191], v[68:71]\n" \
"s_nop 6\n" \
"v_cvt_pk_bf16_f32 v72, v64, v65\n" \
"v_cvt_pk_bf16_f32 v73, v66, v67\n" \
"v_cvt_pk_bf16_f32 v74, v68, v69\n" \
"v_cvt_pk_bf16_f32 v75, v70, v71\n" \
"v_lshlrev_b32 v64, 16, v72\n" \
"v_and_b32 v65, 0xffff0000, v72\n" \
"v_lshlrev_b32 v66, 16, v73\n" \
"v_and_b32 v67, 0xffff0000, v73\n" \
"v_lshlrev_b32 v68, 16, v74\n" \
"v_and_b32 v69, 0xffff0000, v74\n" \
"v_lshlrev_b32 v70, 16, v75\n" \
"v_and_b32 v71, 0xffff0000, v75\n" \
"v_pk_mul_f32 v[64:65], v[64:65], v[136:137]\n" \
"v_pk_mul_f32 v[66:67], v[66:67], v[138:139]\n" \
"v_pk_mul_f32 v[68:69], v[68:69], v[140:141]\n" \
"v_pk_mul_f32 v[70:71], v[70:71], v[142:143]\n" \
"v_cvt_pk_bf16_f32 v72, v64, v65\n" \
"v_cvt_pk_bf16_f32 v73, v66, v67\n" \
"v_cvt_pk_bf16_f32 v74, v68, v69\n" \
"v_cvt_pk_bf16_f32 v75, v70, v71\n" \
"v_lshlrev_b32 v64, 16, v72\n" \
"v_and_b32 v65, 0xffff0000, v72\n" \
"v_lshlrev_b32 v66, 16, v73\n" \
"v_and_b32 v67, 0xffff0000, v73\n" \
"v_lshlrev_b32 v68, 16, v74\n" \
"v_and_b32 v69, 0xffff0000, v74\n" \
"v_lshlrev_b32 v70, 16, v75\n" \
"v_and_b32 v71, 0xffff0000, v75\n" \
"v_pk_add_f32 v[64:65], v[64:65], v[56:57]\n" \
"v_pk_add_f32 v[66:67], v[66:67], v[58:59]\n" \
"v_pk_add_f32 v[68:69], v[68:69], v[60:61]\n" \
"v_pk_add_f32 v[70:71], v[70:71], v[62:63]\n" \
"v_cvt_pk_bf16_f32 v72, v64, v65\n" \
"v_cvt_pk_bf16_f32 v73, v66, v67\n" \
"v_cvt_pk_bf16_f32 v74, v68, v69\n" \
"v_cvt_pk_bf16_f32 v75, v70, v71\n" \
"v_lshlrev_b32 v64, 16, v72\n" \
"v_and_b32 v65, 0xffff0000, v72\n" \
"v_lshlrev_b32 v66, 16, v73\n" \
"v_readfirstlane_b32 s24, v64\n" \
"v_and_b32 v67, 0xffff0000, v73\n" \
"v_lshlrev_b32 v68, 16, v74\n" \
"s_mov_b32 s25, s24\n" \
"v_and_b32 v69, 0xffff0000, v74\n" \
"v_lshlrev_b32 v70, 16, v75\n" \
"v_and_b32 v71, 0xffff0000, v75\n" \
"v_pk_add_f32 v[76:77], v[64:65], s[24:25] neg_lo:[0,1] neg_hi:[0,1]\n" \
"v_pk_add_f32 v[78:79], v[66:67], s[24:25] neg_lo:[0,1] neg_hi:[0,1]\n" \
"v_pk_add_f32 v[80:81], v[68:69], s[24:25] neg_lo:[0,1] neg_hi:[0,1]\n" \
"v_pk_add_f32 v[82:83], v[70:71], s[24:25] neg_lo:[0,1] neg_hi:[0,1]\n" \
"v_pk_add_f32 v[84:85], v[76:77], v[78:79]\n" \
"v_pk_mul_f32 v[86:87], v[76:77], v[76:77]\n" \
"v_pk_add_f32 v[88:89], v[80:81], v[82:83]\n" \
"v_pk_mul_f32 v[90:91], v[80:81], v[80:81]\n" \
"v_pk_fma_f32 v[86:87], v[78:79], v[78:79], v[86:87]\n" \
"v_pk_fma_f32 v[90:91], v[82:83], v[82:83], v[90:91]\n" \
"v_pk_add_f32 v[84:85], v[84:85], v[88:89]\n" \
"v_pk_add_f32 v[86:87], v[86:87], v[90:91]\n" \
"v_add_f32 v84, v84, v85\n" \
"v_add_f32 v86, v86, v87\n" \
"s_nop 1\n" \
"v_permlane32_swap_b32 v84, v86\n" \
"v_add_f32 v84, v84, v86\n" \
"s_nop 1\n" \
"v_add_f32_dpp v84, v84, v84 quad_perm:[1,0,3,2] row_mask:0xf bank_mask:0xf\n" \
"s_nop 1\n" \
"v_add_f32_dpp v84, v84, v84 quad_perm:[2,3,0,1] row_mask:0xf bank_mask:0xf\n" \
"s_nop 1\n" \
"v_add_f32_dpp v84, v84, v84 row_half_mirror row_mask:0xf bank_mask:0xf\n" \
"s_nop 1\n" \
"v_add_f32_dpp v84, v84, v84 row_mirror row_mask:0xf bank_mask:0xf\n" \
"s_nop 1\n" \
"v_add_f32_dpp v84, v84, v84 row_bcast:15 row_mask:0xa bank_mask:0xf\n" \
"v_mul_f32 v84, 0x3b000000, v84\n" \
"s_nop 0\n" \
"v_readlane_b32 s26, v84, 31\n" \
"v_readlane_b32 s27, v84, 63\n" \
"v_mov_b32 v85, s24\n" \
"s_nop 0\n" \
"v_mov_b32 v86, s27\n" \
"v_add_f32 v85, s26, v85\n" \
"v_fma_f32 v86, -s26, s26, v86\n" \
"v_max_f32 v87, 0, v86\n" \
"v_cmp_u_f32 s[20:21], v86, v86\n" \
"s_nop 1\n" \
"v_cndmask_b32 v86, v87, v86, s[20:21]\n" \
"v_cvt_pk_bf16_f32 v88, v86, v85\n" \
"v_lshlrev_b32 v86, 16, v88\n" \
"v_and_b32 v84, 0xffff0000, v88\n" \
"v_add_f32 v86, 0x3727c5ac, v86\n" \
"v_cvt_pk_bf16_f32 v86, v86, v86\n" \
"v_pk_add_f32 v[76:77], v[64:65], v[84:85] op_sel_hi:[1,0] neg_lo:[0,1] neg_hi:[0,1]\n" \
"v_lshlrev_b32 v86, 16, v86\n" \
"v_pk_add_f32 v[78:79], v[66:67], v[84:85] op_sel_hi:[1,0] neg_lo:[0,1] neg_hi:[0,1]\n" \
"v_sqrt_f32 v86, v86\n" \
"v_pk_add_f32 v[80:81], v[68:69], v[84:85] op_sel_hi:[1,0] neg_lo:[0,1] neg_hi:[0,1]\n" \
"v_pk_add_f32 v[82:83], v[70:71], v[84:85] op_sel_hi:[1,0] neg_lo:[0,1] neg_hi:[0,1]\n" \
"v_cvt_pk_bf16_f32 v88, v86, v84\n" \
"v_cvt_pk_bf16_f32 v72, v76, v77\n" \
"v_lshlrev_b32 v86, 16, v88\n" \
"v_cvt_pk_bf16_f32 v73, v78, v79\n" \
"v_rcp_f32 v86, v86\n" \
"v_cvt_pk_bf16_f32 v74, v80, v81\n" \
"v_cvt_pk_bf16_f32 v75, v82, v83\n" \
"v_readfirstlane_b32 s73, v88\n" \
"v_lshlrev_b32 v76, 16, v72\n" \
"v_and_b32 v77, 0xffff0000, v72\n" \
"v_lshlrev_b32 v78, 16, v73\n" \
"v_and_b32 v79, 0xffff0000, v73\n" \
"v_lshlrev_b32 v80, 16, v74\n" \
"v_and_b32 v81, 0xffff0000, v74\n" \
"v_lshlrev_b32 v82, 16, v75\n" \
"v_and_b32 v83, 0xffff0000, v75\n" \
"v_pk_mul_f32 v[76:77], v[76:77], v[86:87] op_sel_hi:[1,0]\n" \
"v_pk_mul_f32 v[78:79], v[78:79], v[86:87] op_sel_hi:[1,0]\n" \
"v_pk_mul_f32 v[80:81], v[80:81], v[86:87] op_sel_hi:[1,0]\n" \
"v_pk_mul_f32 v[82:83], v[82:83], v[86:87] op_sel_hi:[1,0]\n" \
"v_cvt_pk_bf16_f32 v72, v76, v77\n" \
"v_cvt_pk_bf16_f32 v73, v78, v79\n" \
"v_cvt_pk_bf16_f32 v74, v80, v81\n" \
"v_cvt_pk_bf16_f32 v75, v82, v83\n" \
"v_lshlrev_b32 v76, 16, v72\n" \
"v_and_b32 v77, 0xffff0000, v72\n" \
"v_lshlrev_b32 v78, 16, v73\n" \
"v_and_b32 v79, 0xffff0000, v73\n" \
"v_lshlrev_b32 v80, 16, v74\n" \
"v_and_b32 v81, 0xffff0000, v74\n" \
"v_lshlrev_b32 v82, 16, v75\n" \
"v_and_b32 v83, 0xffff0000, v75\n" \
"v_pk_mul_f32 v[76:77], v[76:77], v[152:153]\n" \
"v_pk_mul_f32 v[78:79], v[78:79], v[154:155]\n" \
"v_pk_mul_f32 v[80:81], v[80:81], v[156:157]\n" \
"v_pk_mul_f32 v[82:83], v[82:83], v[158:159]\n" \
"v_cvt_pk_bf16_f32 v72, v76, v77\n" \
"v_cvt_pk_bf16_f32 v73, v78, v79\n" \
"v_cvt_pk_bf16_f32 v74, v80, v81\n" \
"v_cvt_pk_bf16_f32 v75, v82, v83\n" \
"v_lshlrev_b32 v76, 16, v72\n" \
"v_and_b32 v77, 0xffff0000, v72\n" \
"v_lshlrev_b32 v78, 16, v73\n" \
"v_and_b32 v79, 0xffff0000, v73\n" \
"v_lshlrev_b32 v80, 16, v74\n" \
"v_and_b32 v81, 0xffff0000, v74\n" \
"v_lshlrev_b32 v82, 16, v75\n" \
"v_and_b32 v83, 0xffff0000, v75\n" \
"v_pk_add_f32 v[76:77], v[76:77], v[144:145]\n" \
"v_pk_add_f32 v[78:79], v[78:79], v[146:147]\n" \
"v_pk_add_f32 v[80:81], v[80:81], v[148:149]\n" \
"v_pk_add_f32 v[82:83], v[82:83], v[150:151]\n" \
"v_cvt_pk_bf16_f32 v216, v76, v77\n" \
"v_cvt_pk_bf16_f32 v217, v78, v79\n" \
"v_cvt_pk_bf16_f32 v218, v80, v81\n" \
"v_cvt_pk_bf16_f32 v219, v82, v83\n" \
"v_dot2_f32_bf16 v84, v216, v168, 0\n" \
"v_dot2_f32_bf16 v85, v217, v169, 0\n" \
"v_dot2_f32_bf16 v84, v218, v170, v84\n" \
"v_dot2_f32_bf16 v85, v219, v171, v85\n" \
"s_nop 2\n" \
"v_add_f32 v84, v84, v85\n" \
"s_nop 1\n" \
"v_add_f32_dpp v84, v84, v84 quad_perm:[1,0,3,2] row_mask:0xf bank_mask:0xf\n" \
"s_nop 1\n" \
"v_add_f32_dpp v84, v84, v84 quad_perm:[2,3,0,1] row_mask:0xf bank_mask:0xf\n" \
"s_nop 1\n" \
"v_add_f32_dpp v84, v84, v84 row_half_mirror row_mask:0xf bank_mask:0xf\n" \
"s_nop 1\n" \
"v_add_f32_dpp v84, v84, v84 row_mirror row_mask:0xf bank_mask:0xf\n" \
"s_nop 1\n" \
"v_add_f32_dpp v84, v84, v84 row_bcast:15 row_mask:0xa bank_mask:0xf\n" \
"s_nop 1\n" \
"v_add_f32_dpp v84, v84, v84 row_bcast:31 row_mask:0xc bank_mask:0xf\n" \
"v_cvt_pk_bf16_f32 v84, v84, v84\n" \
"s_sub_u32 s25, 2046, s85\n" \
"s_lshl_b32 s25, s25, 5\n" \
"s_or_b32 s25, s25, 3\n" \
"v_readlane_b32 s24, v84, 63\n" \
"s_and_b32 s24, s24, 0xffff\n" \
"s_or_b32 s26, s24, 0x8000\n" \
"s_andn2_b32 s27, 0xffff, s24\n" \
"s_bitcmp1_b32 s24, 15\n" \
"s_cselect_b32 s26, s27, s26\n" \
"s_and_b32 s24, s24, 0x7fff\n" \
"s_cmp_gt_u32 s24, 0x7f80\n" \
"s_cselect_b32 s26, 0, s26\n" \
"s_pack_ll_b32_b16 s88, s25, s26\n" \
"v_lshlrev_b32 v76, 16, v216\n" \
"v_and_b32 v77, 0xffff0000, v216\n" \
"v_lshlrev_b32 v78, 16, v217\n" \
"v_and_b32 v79, 0xffff0000, v217\n" \
"v_lshlrev_b32 v80, 16, v218\n" \
"v_and_b32 v81, 0xffff0000, v218\n" \
"v_lshlrev_b32 v82, 16, v219\n" \
"v_and_b32 v83, 0xffff0000, v219\n" \
"v_pk_mul_f32 v[76:77], v[76:77], v[160:161]\n" \
"v_pk_mul_f32 v[78:79], v[78:79], v[162:163]\n" \
"v_pk_mul_f32 v[80:81], v[80:81], v[164:165]\n" \
"v_pk_mul_f32 v[82:83], v[82:83], v[166:167]\n" \
"v_cvt_pk_bf16_f32 v72, v76, v77\n" \
"v_cvt_pk_bf16_f32 v73, v78, v79\n" \
"v_cvt_pk_bf16_f32 v74, v80, v81\n" \
"v_cvt_pk_bf16_f32 v75, v82, v83\n" \
"v_lshlrev_b32 v76, 16, v72\n" \
"v_and_b32 v77, 0xffff0000, v72\n" \
"v_lshlrev_b32 v78, 16, v73\n" \
"v_and_b32 v79, 0xffff0000, v73\n" \
"v_lshlrev_b32 v80, 16, v74\n" \
"v_and_b32 v81, 0xffff0000, v74\n" \
"v_lshlrev_b32 v82, 16, v75\n" \
"v_and_b32 v83, 0xffff0000, v75\n" \
"v_pk_add_f32 v[76:77], v[76:77], v[64:65]\n" \
"v_pk_add_f32 v[78:79], v[78:79], v[66:67]\n" \
"v_pk_add_f32 v[80:81], v[80:81], v[68:69]\n" \
"v_pk_add_f32 v[82:83], v[82:83], v[70:71]\n" \
"v_cvt_pk_bf16_f32 v72, v76, v77\n" \
"v_cvt_pk_bf16_f32 v73, v78, v79\n" \
"v_cvt_pk_bf16_f32 v74, v80, v81\n" \
"v_cvt_pk_bf16_f32 v75, v82, v83\n" \
"s_mov_b32 vcc_lo, 0xcccccccc\n" \
"s_mov_b32 vcc_hi, 0xcccccccc\n" \
"v_mov_b32_dpp v76, v74 quad_perm:[0,2,0,2] row_mask:0xf bank_mask:0xf\n" \
"v_mov_b32_dpp v77, v75 quad_perm:[0,2,0,2] row_mask:0xf bank_mask:0xf\n" \
"v_mov_b32_dpp v78, v74 quad_perm:[1,3,1,3] row_mask:0xf bank_mask:0xf\n" \
"v_mov_b32_dpp v79, v75 quad_perm:[1,3,1,3] row_mask:0xf bank_mask:0xf\n" \
"v_cndmask_b32_dpp v228, v72, v76, vcc quad_perm:[0,2,0,2] row_mask:0xf bank_mask:0xf\n" \
"v_cndmask_b32_dpp v229, v73, v77, vcc quad_perm:[0,2,0,2] row_mask:0xf bank_mask:0xf\n" \
"v_cndmask_b32_dpp v230, v72, v78, vcc quad_perm:[1,3,1,3] row_mask:0xf bank_mask:0xf\n" \
"v_cndmask_b32_dpp v231, v73, v79, vcc quad_perm:[1,3,1,3] row_mask:0xf bank_mask:0xf\n" \
"s_branch .Lr2b_%=\n" \
".Lgr1_%=:\n" \
"s_mov_b32 vcc_lo, 0\n" \
"s_bcnt1_i32_b64 s27, vcc\n" \
"s_cmp_lt_u32 s27, 8\n" \
"s_cbranch_scc1 .Lnap2_%=\n" \
"s_sleep 10\n" \
".Lnap2_%=:\n" \
"s_sleep 2\n" \
"buffer_load_dword v245, v255, s[32:35], s18 offen sc1\n" \
"s_branch .Lg1_%=\n" \
".Lkr_%=:\n" \
".Lst3_3_%=:\n" \
"s_cbranch_execz .Lstb3_%=\n" \
"s_sub_u32 s82, s82, 1\n" \
"s_cbranch_scc1 .Lkb_%=\n" \
"s_cmp_lt_u32 s65, 28\n" \
"s_cbranch_scc1 .Lkn_%=\n" \
"v_max_u32_dpp v88, v88, v88 quad_perm:[1,0,3,2] row_mask:0xf bank_mask:0xf\n" \
"s_nop 1\n" \
"v_max_u32_dpp v88, v88, v88 quad_perm:[2,3,0,1] row_mask:0xf bank_mask:0xf\n" \
"s_nop 1\n" \
"v_max_u32_dpp v88, v88, v88 row_half_mirror row_mask:0xf bank_mask:0xf\n" \
"s_nop 1\n" \
"v_max_u32_dpp v88, v88, v88 row_mirror row_mask:0xf bank_mask:0xf\n" \
"s_nop 1\n" \
"v_max_u32_dpp v88, v88, v88 row_bcast:15 row_mask:0xa bank_mask:0xf\n" \
"s_nop 1\n" \
"v_max_u32_dpp v88, v88, v88 row_bcast:31 row_mask:0xc bank_mask:0xf\n" \
"s_nop 0\n" \
"v_readlane_b32 s25, v88, 63\n" \
"v_lshrrev_b32 v89, 4, v251\n" \
"v_and_b32 v90, 15, v251\n" \
"s_cmp_eq_u32 s25, s93\n" \
"s_cbranch_scc1 .Lkn_%=\n" \
"s_mov_b32 s93, s25\n" \
"s_bfe_u32 s25, s25, 0xb0005\n" \
"s_sub_u32 s25, 2047, s25\n" \
"s_mov_b32 s22, 0x18100800\n" \
"s_mov_b32 s23, 0x01091119\n" \
"s_bfe_u32 s24, s25, 0x30004\n" \
"s_lshl_b32 s24, s24, 3\n" \
"s_lshr_b64 s[22:23], s[22:23], s24\n" \
"s_and_b32 s22, s22, 0xff\n" \
"s_lshr_b32 s26, s25, 7\n" \
"s_lshl_b32 s26, s26, 5\n" \
"s_add_u32 s26, s26, s22\n" \
"s_lshl_b32 s26, s26, 6\n" \
"s_and_b32 s24, s25, 15\n" \
"s_add_u32 s26, s26, s24\n" \
"s_lshl_b32 s26, s26, 6\n" \
"s_add_u32 s26, s26, 59768832\n" \
"v_lshlrev_b32 v89, 21, v89\n" \
"v_and_b32 v91, 3, v251\n" \
"v_lshl_or_b32 v89, v91, 10, v89\n" \
"v_lshrrev_b32 v91, 2, v90\n" \
"v_lshl_or_b32 v89, v91, 13, v89\n" \
"v_lshlrev_b32 v90, 6, v90\n" \
"s_lshl_b32 s25, s25, 10\n" \
"s_add_u32 s24, s26, 8388608\n" \
"buffer_load_dword v91, v89, s[32:35], s26 offen sc1\n" \
"buffer_load_dword v91, v89, s[32:35], s24 offen sc1\n" \
"buffer_load_dword v91, v90, s[44:47], s25 offen\n" \
".Lkn_%=:\n" \
"s_bcnt1_i32_b64 s27, vcc\n" \
"s_cmp_lt_u32 s27, 6\n" \
"s_cbranch_scc1 .Lnap3_%=\n" \
"s_sleep 20\n" \
".Lnap3_%=:\n" \
"s_sleep 2\n" \
"buffer_load_dwordx2 v[86:87], v235, s[32:35], s38 offen sc1\n" \
"s_waitcnt vmcnt(0)\n" \
"s_branch .Lkp_%=\n" \
".Lfr_%=:\n" \
".Lst3_4_%=:\n" \
"s_cbranch_execz .Lstb3_%=\n" \
"s_sub_u32 s82, s82, 1\n" \
"s_cbranch_scc1 .Lfb_%=\n" \
"s_sleep 2\n" \
"s_branch .Lfp_%=\n" \
  \
".Lredo0_%=:\n" \
"s_nop 7\n" \
"v_and_b32 v196, 15, v251\n" \
"v_lshrrev_b32 v197, 4, v251\n" \
"v_lshlrev_b32 v197, 2, v197\n" \
"v_sub_u32 v196, v196, v197\n" \
"v_mov_b32 v199, 0xff800000\n" \
"s_mov_b32 s14, s67\n" \
"s_xor_b32 s15, %[wid], 0\n" \
"s_lshl3_add_u32 s15, s14, s15\n" \
"s_lshl_b32 s15, s15, 4\n" \
"v_add_u32 v194, s15, v196\n" \
"v_mov_b32 v192, 0xff7fffff\n" \
"s_xor_b32 s15, %[wid], 7\n" \
"s_lshl3_add_u32 s15, s14, s15\n" \
"s_lshl_b32 s15, s15, 4\n" \
"v_add_u32 v195, s15, v196\n" \
"v_mov_b32 v193, 0xff7fffff\n" \
"v_mov_b32 v0, 0\n" \
"v_mov_b32 v1, 0\n" \
"v_mov_b32 v2, 0\n" \
"v_mov_b32 v3, 0\n" \
"v_mov_b32 v4, 0\n" \
"v_mov_b32 v5, 0\n" \
"v_mov_b32 v6, 0\n" \
"v_mov_b32 v7, 0\n" \
"v_mov_b32 v8, 0\n" \
"v_mov_b32 v9, 0\n" \
"v_mov_b32 v10, 0\n" \
"v_mov_b32 v11, 0\n" \
"v_mov_b32 v12, 0\n" \
"v_mov_b32 v13, 0\n" \
"v_mov_b32 v14, 0\n" \
"v_mov_b32 v15, 0\n" \
"v_mov_b32 v16, 0\n" \
"v_mov_b32 v17, 0\n" \
"v_mov_b32 v18, 0\n" \
"v_mov_b32 v19, 0\n" \
"v_mov_b32 v20, 0\n" \
"v_mov_b32 v21, 0\n" \
"v_mov_b32 v22, 0\n" \
"v_mov_b32 v23, 0\n" \
"v_mov_b32 v24, 0\n" \
"v_mov_b32 v25, 0\n" \
"v_mov_b32 v26, 0\n" \
"v_mov_b32 v27, 0\n" \
"v_mov_b32 v28, 0\n" \
"v_mov_b32 v29, 0\n" \
"v_mov_b32 v30, 0\n" \
"v_mov_b32 v31, 0\n" \
"v_mov_b32 v32, 0\n" \
"v_mov_b32 v33, 0\n" \
"v_mov_b32 v34, 0\n" \
"v_mov_b32 v35, 0\n" \
"v_mov_b32 v36, 0\n" \
"v_mov_b32 v37, 0\n" \
"v_mov_b32 v38, 0\n" \
"v_mov_b32 v39, 0\n" \
"v_mov_b32 v40, 0\n" \
"v_mov_b32 v41, 0\n" \
"v_mov_b32 v42, 0\n" \
"v_mov_b32 v43, 0\n" \
"v_mov_b32 v44, 0\n" \
"v_mov_b32 v45, 0\n" \
"v_mov_b32 v46, 0\n" \
"v_mov_b32 v47, 0\n" \
"v_mov_b32 v48, 0\n" \
"v_mov_b32 v49, 0\n" \
"v_mov_b32 v50, 0\n" \
"v_mov_b32 v51, 0\n" \
"v_mov_b32 v52, 0\n" \
"v_mov_b32 v53, 0\n" \
"v_mov_b32 v54, 0\n" \
"v_mov_b32 v55, 0\n" \
"v_mov_b32 v56, 0\n" \
"v_mov_b32 v57, 0\n" \
"v_mov_b32 v58, 0\n" \
"v_mov_b32 v59, 0\n" \
"v_mov_b32 v60, 0\n" \
"v_mov_b32 v61, 0\n" \
"v_mov_b32 v62, 0\n" \
"v_mov_b32 v63, 0\n" \
"v_accvgpr_write_b32 a32, 0\n" \
"v_accvgpr_write_b32 a33, 0\n" \
"v_accvgpr_write_b32 a34, 0\n" \
"v_accvgpr_write_b32 a35, 0\n" \
"v_accvgpr_write_b32 a36, 0\n" \
"v_accvgpr_write_b32 a37, 0\n" \
"v_accvgpr_write_b32 a38, 0\n" \
"v_accvgpr_write_b32 a39, 0\n" \
"s_lshl1_add_u32 s14, s14, 1\n" \
"s_mov_b32 s15, s73\n" \
".Lslr0_%=:\n" \
"s_add_u32 s82, s15, 0\n" \
"buffer_load_dwordx4 a[64:67], v248, s[32:35], s82 offen offset:0 sc1\n" \
"buffer_load_dwordx4 a[68:71], v248, s[32:35], s82 offen offset:1024 sc1\n" \
"buffer_load_dwordx4 a[72:75], v248, s[32:35], s82 offen offset:2048 sc1\n" \
"buffer_load_dwordx4 a[76:79], v248, s[32:35], s82 offen offset:3072 sc1\n" \
"s_add_u32 s82, s15, 4096\n" \
"buffer_load_dwordx4 a[80:83], v248, s[32:35], s82 offen offset:0 sc1\n" \
"buffer_load_dwordx4 a[84:87], v248, s[32:35], s82 offen offset:1024 sc1\n" \
"buffer_load_dwordx4 a[88:91], v248, s[32:35], s82 offen offset:2048 sc1\n" \
"buffer_load_dwordx4 a[92:95], v248, s[32:35], s82 offen offset:3072 sc1\n" \
"s_add_u32 s82, s15, 8192\n" \
"buffer_load_dwordx4 a[96:99], v248, s[32:35], s82 offen offset:0 sc1\n" \
"buffer_load_dwordx4 a[100:103], v248, s[32:35], s82 offen offset:1024 sc1\n" \
"buffer_load_dwordx4 a[104:107], v248, s[32:35], s82 offen offset:2048 sc1\n" \
"buffer_load_dwordx4 a[108:111], v248, s[32:35], s82 offen offset:3072 sc1\n" \
"s_add_u32 s82, s15, 12288\n" \
"buffer_load_dwordx4 a[112:115], v248, s[32:35], s82 offen offset:0 sc1\n" \
"buffer_load_dwordx4 a[116:119], v248, s[32:35], s82 offen offset:1024 sc1\n" \
"buffer_load_dwordx4 a[120:123], v248, s[32:35], s82 offen offset:2048 sc1\n" \
"buffer_load_dwordx4 a[124:127], v248, s[32:35], s82 offen offset:3072 sc1\n" \
"s_waitcnt vmcnt(0)\n" \
"v_mfma_f32_16x16x32_bf16 v[128:131], a[64:67], a[0:3], 0\n" \
"v_mfma_f32_16x16x32_bf16 v[128:131], a[68:71], a[4:7], v[128:131]\n" \
"v_mfma_f32_16x16x32_bf16 v[128:131], a[72:75], a[8:11], v[128:131]\n" \
"v_mfma_f32_16x16x32_bf16 v[128:131], a[76:79], a[12:15], v[128:131]\n" \
"v_mfma_f32_16x16x32_bf16 v[132:135], a[92:95], a[12:15], 0\n" \
"v_mfma_f32_16x16x32_bf16 v[132:135], a[88:91], a[8:11], v[132:135]\n" \
"v_mfma_f32_16x16x32_bf16 v[132:135], a[84:87], a[4:7], v[132:135]\n" \
"v_mfma_f32_16x16x32_bf16 v[132:135], a[80:83], a[0:3], v[132:135]\n" \
"v_mfma_f32_16x16x32_bf16 v[136:139], a[96:99], a[0:3], 0\n" \
"v_mfma_f32_16x16x32_bf16 v[136:139], a[100:103], a[4:7], v[136:139]\n" \
"v_mfma_f32_16x16x32_bf16 v[136:139], a[104:107], a[8:11], v[136:139]\n" \
"v_mfma_f32_16x16x32_bf16 v[136:139], a[108:111], a[12:15], v[136:139]\n" \
"v_mfma_f32_16x16x32_bf16 v[140:143], a[124:127], a[12:15], 0\n" \
"v_mfma_f32_16x16x32_bf16 v[140:143], a[120:123], a[8:11], v[140:143]\n" \
"v_mfma_f32_16x16x32_bf16 v[140:143], a[116:119], a[4:7], v[140:143]\n" \
"v_mfma_f32_16x16x32_bf16 v[140:143], a[112:115], a[0:3], v[140:143]\n" \
"v_cmp_gt_i32 vcc, 0, v194\n" \
"s_nop 1\n" \
"v_cndmask_b32 v128, v128, v199, vcc\n" \
"v_cmp_gt_i32 vcc, 1, v194\n" \
"s_nop 1\n" \
"v_cndmask_b32 v129, v129, v199, vcc\n" \
"v_cmp_gt_i32 vcc, 2, v194\n" \
"s_nop 1\n" \
"v_cndmask_b32 v130, v130, v199, vcc\n" \
"v_cmp_gt_i32 vcc, 3, v194\n" \
"s_nop 1\n" \
"v_cndmask_b32 v131, v131, v199, vcc\n" \
"v_cmp_gt_i32 vcc, 16, v194\n" \
"s_nop 1\n" \
"v_cndmask_b32 v132, v132, v199, vcc\n" \
"v_cmp_gt_i32 vcc, 17, v194\n" \
"s_nop 1\n" \
"v_cndmask_b32 v133, v133, v199, vcc\n" \
"v_cmp_gt_i32 vcc, 18, v194\n" \
"s_nop 1\n" \
"v_cndmask_b32 v134, v134, v199, vcc\n" \
"v_cmp_gt_i32 vcc, 19, v194\n" \
"s_nop 1\n" \
"v_cndmask_b32 v135, v135, v199, vcc\n" \
"v_cmp_gt_i32 vcc, 32, v194\n" \
"s_nop 1\n" \
"v_cndmask_b32 v136, v136, v199, vcc\n" \
"v_cmp_gt_i32 vcc, 33, v194\n" \
"s_nop 1\n" \
"v_cndmask_b32 v137, v137, v199, vcc\n" \
"v_cmp_gt_i32 vcc, 34, v194\n" \
"s_nop 1\n" \
"v_cndmask_b32 v138, v138, v199, vcc\n" \
"v_cmp_gt_i32 vcc, 35, v194\n" \
"s_nop 1\n" \
"v_cndmask_b32 v139, v139, v199, vcc\n" \
"v_cmp_gt_i32 vcc, 48, v194\n" \
"s_nop 1\n" \
"v_cndmask_b32 v140, v140, v199, vcc\n" \
"v_cmp_gt_i32 vcc, 49, v194\n" \
"s_nop 1\n" \
"v_cndmask_b32 v141, v141, v199, vcc\n" \
"v_cmp_gt_i32 vcc, 50, v194\n" \
"s_nop 1\n" \
"v_cndmask_b32 v142, v142, v199, vcc\n" \
"v_cmp_gt_i32 vcc, 51, v194\n" \
"s_nop 1\n" \
"v_cndmask_b32 v143, v143, v199, vcc\n" \
"v_max3_f32 v196, v192, v128, v129\n" \
"v_max3_f32 v196, v196, v130, v131\n" \
"v_max3_f32 v196, v196, v132, v133\n" \
"v_max3_f32 v196, v196, v134, v135\n" \
"v_max3_f32 v196, v196, v136, v137\n" \
"v_max3_f32 v196, v196, v138, v139\n" \
"v_max3_f32 v196, v196, v140, v141\n" \
"v_max3_f32 v196, v196, v142, v143\n" \
"v_mov_b32 v197, v196\n" \
"s_nop 1\n" \
"v_permlane32_swap_b32 v196, v197\n" \
"v_max_f32 v196, v196, v197\n" \
"v_mov_b32 v197, v196\n" \
"s_nop 1\n" \
"v_permlane16_swap_b32 v196, v197\n" \
"v_max_f32 v196, v196, v197\n" \
"v_sub_f32 v198, v192, v196\n" \
"v_mov_b32 v192, v196\n" \
"v_exp_f32 v198, v198\n" \
"v_subrev_u32 v194, 64, v194\n" \
"s_add_u32 s82, s15, 589824\n" \
"buffer_load_dwordx4 a[64:67], v248, s[32:35], s82 offen offset:0 sc1\n" \
"buffer_load_dwordx4 a[68:71], v248, s[32:35], s82 offen offset:1024 sc1\n" \
"buffer_load_dwordx4 a[72:75], v248, s[32:35], s82 offen offset:2048 sc1\n" \
"buffer_load_dwordx4 a[76:79], v248, s[32:35], s82 offen offset:3072 sc1\n" \
"s_add_u32 s82, s15, 593920\n" \
"buffer_load_dwordx4 a[80:83], v248, s[32:35], s82 offen offset:0 sc1\n" \
"buffer_load_dwordx4 a[84:87], v248, s[32:35], s82 offen offset:1024 sc1\n" \
"buffer_load_dwordx4 a[88:91], v248, s[32:35], s82 offen offset:2048 sc1\n" \
"buffer_load_dwordx4 a[92:95], v248, s[32:35], s82 offen offset:3072 sc1\n" \
"s_add_u32 s82, s15, 598016\n" \
"buffer_load_dwordx4 a[96:99], v248, s[32:35], s82 offen offset:0 sc1\n" \
"buffer_load_dwordx4 a[100:103], v248, s[32:35], s82 offen offset:1024 sc1\n" \
"buffer_load_dwordx4 a[104:107], v248, s[32:35], s82 offen offset:2048 sc1\n" \
"buffer_load_dwordx4 a[108:111], v248, s[32:35], s82 offen offset:3072 sc1\n" \
"s_add_u32 s82, s15, 602112\n" \
"buffer_load_dwordx4 a[112:115], v248, s[32:35], s82 offen offset:0 sc1\n" \
"buffer_load_dwordx4 a[116:119], v248, s[32:35], s82 offen offset:1024 sc1\n" \
"buffer_load_dwordx4 a[120:123], v248, s[32:35], s82 offen offset:2048 sc1\n" \
"buffer_load_dwordx4 a[124:127], v248, s[32:35], s82 offen offset:3072 sc1\n" \
"s_waitcnt vmcnt(0)\n" \
"v_sub_f32 v128, v128, v196\n" \
"v_sub_f32 v129, v129, v196\n" \
"v_sub_f32 v130, v130, v196\n" \
"v_sub_f32 v131, v131, v196\n" \
"v_sub_f32 v132, v132, v196\n" \
"v_sub_f32 v133, v133, v196\n" \
"v_sub_f32 v134, v134, v196\n" \
"v_sub_f32 v135, v135, v196\n" \
"v_sub_f32 v136, v136, v196\n" \
"v_sub_f32 v137, v137, v196\n" \
"v_sub_f32 v138, v138, v196\n" \
"v_sub_f32 v139, v139, v196\n" \
"v_sub_f32 v140, v140, v196\n" \
"v_sub_f32 v141, v141, v196\n" \
"v_sub_f32 v142, v142, v196\n" \
"v_sub_f32 v143, v143, v196\n" \
"v_exp_f32 v128, v128\n" \
"v_exp_f32 v129, v129\n" \
"v_exp_f32 v130, v130\n" \
"v_exp_f32 v131, v131\n" \
"v_exp_f32 v132, v132\n" \
"v_exp_f32 v133, v133\n" \
"v_exp_f32 v134, v134\n" \
"v_exp_f32 v135, v135\n" \
"v_exp_f32 v136, v136\n" \
"v_exp_f32 v137, v137\n" \
"v_exp_f32 v138, v138\n" \
"v_exp_f32 v139, v139\n" \
"v_exp_f32 v140, v140\n" \
"v_exp_f32 v141, v141\n" \
"v_exp_f32 v142, v142\n" \
"v_exp_f32 v143, v143\n" \
"v_cvt_pk_bf16_f32 v128, v128, v129\n" \
"v_cvt_pk_bf16_f32 v129, v130, v131\n" \
"v_cvt_pk_bf16_f32 v130, v132, v133\n" \
"v_cvt_pk_bf16_f32 v131, v134, v135\n" \
"v_cvt_pk_bf16_f32 v136, v136, v137\n" \
"v_cvt_pk_bf16_f32 v137, v138, v139\n" \
"v_cvt_pk_bf16_f32 v138, v140, v141\n" \
"v_cvt_pk_bf16_f32 v139, v142, v143\n" \
"v_mul_f32 v0, v198, v0\n" \
"v_mul_f32 v1, v198, v1\n" \
"v_mul_f32 v2, v198, v2\n" \
"v_mul_f32 v3, v198, v3\n" \
"v_mul_f32 v4, v198, v4\n" \
"v_mul_f32 v5, v198, v5\n" \
"v_mul_f32 v6, v198, v6\n" \
"v_mul_f32 v7, v198, v7\n" \
"v_mul_f32 v8, v198, v8\n" \
"v_mul_f32 v9, v198, v9\n" \
"v_mul_f32 v10, v198, v10\n" \
"v_mul_f32 v11, v198, v11\n" \
"v_mul_f32 v12, v198, v12\n" \
"v_mul_f32 v13, v198, v13\n" \
"v_mul_f32 v14, v198, v14\n" \
"v_mul_f32 v15, v198, v15\n" \
"v_mul_f32 v16, v198, v16\n" \
"v_mul_f32 v17, v198, v17\n" \
"v_mul_f32 v18, v198, v18\n" \
"v_mul_f32 v19, v198, v19\n" \
"v_mul_f32 v20, v198, v20\n" \
"v_mul_f32 v21, v198, v21\n" \
"v_mul_f32 v22, v198, v22\n" \
"v_mul_f32 v23, v198, v23\n" \
"v_mul_f32 v24, v198, v24\n" \
"v_mul_f32 v25, v198, v25\n" \
"v_mul_f32 v26, v198, v26\n" \
"v_mul_f32 v27, v198, v27\n" \
"v_mul_f32 v28, v198, v28\n" \
"v_mul_f32 v29, v198, v29\n" \
"v_mul_f32 v30, v198, v30\n" \
"v_mul_f32 v31, v198, v31\n" \
"v_accvgpr_read_b32 v197, a32\n" \
"v_mul_f32 v197, v198, v197\n" \
"v_accvgpr_write_b32 a32, v197\n" \
"v_accvgpr_read_b32 v197, a33\n" \
"v_mul_f32 v197, v198, v197\n" \
"v_accvgpr_write_b32 a33, v197\n" \
"v_accvgpr_read_b32 v197, a34\n" \
"v_mul_f32 v197, v198, v197\n" \
"v_accvgpr_write_b32 a34, v197\n" \
"v_accvgpr_read_b32 v197, a35\n" \
"v_mul_f32 v197, v198, v197\n" \
"v_accvgpr_write_b32 a35, v197\n" \
"s_nop 1\n" \
"v_mfma_f32_16x16x32_bf16 a[32:35], a[48:51], v[128:131], a[32:35]\n" \
"v_mfma_f32_16x16x32_bf16 a[32:35], a[48:51], v[136:139], a[32:35]\n" \
"v_mfma_f32_16x16x32_bf16 v[0:3], a[96:99], v[136:139], v[0:3]\n" \
"v_mfma_f32_16x16x32_bf16 v[0:3], a[64:67], v[128:131], v[0:3]\n" \
"v_mfma_f32_16x16x32_bf16 v[4:7], a[68:71], v[128:131], v[4:7]\n" \
"v_mfma_f32_16x16x32_bf16 v[4:7], a[100:103], v[136:139], v[4:7]\n" \
"v_mfma_f32_16x16x32_bf16 v[8:11], a[104:107], v[136:139], v[8:11]\n" \
"v_mfma_f32_16x16x32_bf16 v[8:11], a[72:75], v[128:131], v[8:11]\n" \
"v_mfma_f32_16x16x32_bf16 v[12:15], a[76:79], v[128:131], v[12:15]\n" \
"v_mfma_f32_16x16x32_bf16 v[12:15], a[108:111], v[136:139], v[12:15]\n" \
"v_mfma_f32_16x16x32_bf16 v[16:19], a[112:115], v[136:139], v[16:19]\n" \
"v_mfma_f32_16x16x32_bf16 v[16:19], a[80:83], v[128:131], v[16:19]\n" \
"v_mfma_f32_16x16x32_bf16 v[20:23], a[84:87], v[128:131], v[20:23]\n" \
"v_mfma_f32_16x16x32_bf16 v[20:23], a[116:119], v[136:139], v[20:23]\n" \
"v_mfma_f32_16x16x32_bf16 v[24:27], a[120:123], v[136:139], v[24:27]\n" \
"v_mfma_f32_16x16x32_bf16 v[24:27], a[88:91], v[128:131], v[24:27]\n" \
"v_mfma_f32_16x16x32_bf16 v[28:31], a[92:95], v[128:131], v[28:31]\n" \
"v_mfma_f32_16x16x32_bf16 v[28:31], a[124:127], v[136:139], v[28:31]\n" \
"s_nop 7\n" \
"s_add_u32 s82, s15, 0\n" \
"buffer_load_dwordx4 a[64:67], v248, s[32:35], s82 offen offset:0 sc1\n" \
"buffer_load_dwordx4 a[68:71], v248, s[32:35], s82 offen offset:1024 sc1\n" \
"buffer_load_dwordx4 a[72:75], v248, s[32:35], s82 offen offset:2048 sc1\n" \
"buffer_load_dwordx4 a[76:79], v248, s[32:35], s82 offen offset:3072 sc1\n" \
"s_add_u32 s82, s15, 4096\n" \
"buffer_load_dwordx4 a[80:83], v248, s[32:35], s82 offen offset:0 sc1\n" \
"buffer_load_dwordx4 a[84:87], v248, s[32:35], s82 offen offset:1024 sc1\n" \
"buffer_load_dwordx4 a[88:91], v248, s[32:35], s82 offen offset:2048 sc1\n" \
"buffer_load_dwordx4 a[92:95], v248, s[32:35], s82 offen offset:3072 sc1\n" \
"s_add_u32 s82, s15, 8192\n" \
"buffer_load_dwordx4 a[96:99], v248, s[32:35], s82 offen offset:0 sc1\n" \
"buffer_load_dwordx4 a[100:103], v248, s[32:35], s82 offen offset:1024 sc1\n" \
"buffer_load_dwordx4 a[104:107], v248, s[32:35], s82 offen offset:2048 sc1\n" \
"buffer_load_dwordx4 a[108:111], v248, s[32:35], s82 offen offset:3072 sc1\n" \
"s_add_u32 s82, s15, 12288\n" \
"buffer_load_dwordx4 a[112:115], v248, s[32:35], s82 offen offset:0 sc1\n" \
"buffer_load_dwordx4 a[116:119], v248, s[32:35], s82 offen offset:1024 sc1\n" \
"buffer_load_dwordx4 a[120:123], v248, s[32:35], s82 offen offset:2048 sc1\n" \
"buffer_load_dwordx4 a[124:127], v248, s[32:35], s82 offen offset:3072 sc1\n" \
"s_waitcnt vmcnt(0)\n" \
"v_mfma_f32_16x16x32_bf16 v[128:131], a[64:67], a[16:19], 0\n" \
"v_mfma_f32_16x16x32_bf16 v[128:131], a[68:71], a[20:23], v[128:131]\n" \
"v_mfma_f32_16x16x32_bf16 v[128:131], a[72:75], a[24:27], v[128:131]\n" \
"v_mfma_f32_16x16x32_bf16 v[128:131], a[76:79], a[28:31], v[128:131]\n" \
"v_mfma_f32_16x16x32_bf16 v[132:135], a[92:95], a[28:31], 0\n" \
"v_mfma_f32_16x16x32_bf16 v[132:135], a[88:91], a[24:27], v[132:135]\n" \
"v_mfma_f32_16x16x32_bf16 v[132:135], a[84:87], a[20:23], v[132:135]\n" \
"v_mfma_f32_16x16x32_bf16 v[132:135], a[80:83], a[16:19], v[132:135]\n" \
"v_mfma_f32_16x16x32_bf16 v[136:139], a[96:99], a[16:19], 0\n" \
"v_mfma_f32_16x16x32_bf16 v[136:139], a[100:103], a[20:23], v[136:139]\n" \
"v_mfma_f32_16x16x32_bf16 v[136:139], a[104:107], a[24:27], v[136:139]\n" \
"v_mfma_f32_16x16x32_bf16 v[136:139], a[108:111], a[28:31], v[136:139]\n" \
"v_mfma_f32_16x16x32_bf16 v[140:143], a[124:127], a[28:31], 0\n" \
"v_mfma_f32_16x16x32_bf16 v[140:143], a[120:123], a[24:27], v[140:143]\n" \
"v_mfma_f32_16x16x32_bf16 v[140:143], a[116:119], a[20:23], v[140:143]\n" \
"v_mfma_f32_16x16x32_bf16 v[140:143], a[112:115], a[16:19], v[140:143]\n" \
"v_cmp_gt_i32 vcc, 0, v195\n" \
"s_nop 1\n" \
"v_cndmask_b32 v128, v128, v199, vcc\n" \
"v_cmp_gt_i32 vcc, 1, v195\n" \
"s_nop 1\n" \
"v_cndmask_b32 v129, v129, v199, vcc\n" \
"v_cmp_gt_i32 vcc, 2, v195\n" \
"s_nop 1\n" \
"v_cndmask_b32 v130, v130, v199, vcc\n" \
"v_cmp_gt_i32 vcc, 3, v195\n" \
"s_nop 1\n" \
"v_cndmask_b32 v131, v131, v199, vcc\n" \
"v_cmp_gt_i32 vcc, 16, v195\n" \
"s_nop 1\n" \
"v_cndmask_b32 v132, v132, v199, vcc\n" \
"v_cmp_gt_i32 vcc, 17, v195\n" \
"s_nop 1\n" \
"v_cndmask_b32 v133, v133, v199, vcc\n" \
"v_cmp_gt_i32 vcc, 18, v195\n" \
"s_nop 1\n" \
"v_cndmask_b32 v134, v134, v199, vcc\n" \
"v_cmp_gt_i32 vcc, 19, v195\n" \
"s_nop 1\n" \
"v_cndmask_b32 v135, v135, v199, vcc\n" \
"v_cmp_gt_i32 vcc, 32, v195\n" \
"s_nop 1\n" \
"v_cndmask_b32 v136, v136, v199, vcc\n" \
"v_cmp_gt_i32 vcc, 33, v195\n" \
"s_nop 1\n" \
"v_cndmask_b32 v137, v137, v199, vcc\n" \
"v_cmp_gt_i32 vcc, 34, v195\n" \
"s_nop 1\n" \
"v_cndmask_b32 v138, v138, v199, vcc\n" \
"v_cmp_gt_i32 vcc, 35, v195\n" \
"s_nop 1\n" \
"v_cndmask_b32 v139, v139, v199, vcc\n" \
"v_cmp_gt_i32 vcc, 48, v195\n" \
"s_nop 1\n" \
"v_cndmask_b32 v140, v140, v199, vcc\n" \
"v_cmp_gt_i32 vcc, 49, v195\n" \
"s_nop 1\n" \
"v_cndmask_b32 v141, v141, v199, vcc\n" \
"v_cmp_gt_i32 vcc, 50, v195\n" \
"s_nop 1\n" \
"v_cndmask_b32 v142, v142, v199, vcc\n" \
"v_cmp_gt_i32 vcc, 51, v195\n" \
"s_nop 1\n" \
"v_cndmask_b32 v143, v143, v199, vcc\n" \
"v_max3_f32 v196, v193, v128, v129\n" \
"v_max3_f32 v196, v196, v130, v131\n" \
"v_max3_f32 v196, v196, v132, v133\n" \
"v_max3_f32 v196, v196, v134, v135\n" \
"v_max3_f32 v196, v196, v136, v137\n" \
"v_max3_f32 v196, v196, v138, v139\n" \
"v_max3_f32 v196, v196, v140, v141\n" \
"v_max3_f32 v196, v196, v142, v143\n" \
"v_mov_b32 v197, v196\n" \
"s_nop 1\n" \
"v_permlane32_swap_b32 v196, v197\n" \
"v_max_f32 v196, v196, v197\n" \
"v_mov_b32 v197, v196\n" \
"s_nop 1\n" \
"v_permlane16_swap_b32 v196, v197\n" \
"v_max_f32 v196, v196, v197\n" \
"v_sub_f32 v198, v193, v196\n" \
"v_mov_b32 v193, v196\n" \
"v_exp_f32 v198, v198\n" \
"v_subrev_u32 v195, 64, v195\n" \
"s_add_u32 s82, s15, 589824\n" \
"buffer_load_dwordx4 a[64:67], v248, s[32:35], s82 offen offset:0 sc1\n" \
"buffer_load_dwordx4 a[68:71], v248, s[32:35], s82 offen offset:1024 sc1\n" \
"buffer_load_dwordx4 a[72:75], v248, s[32:35], s82 offen offset:2048 sc1\n" \
"buffer_load_dwordx4 a[76:79], v248, s[32:35], s82 offen offset:3072 sc1\n" \
"s_add_u32 s82, s15, 593920\n" \
"buffer_load_dwordx4 a[80:83], v248, s[32:35], s82 offen offset:0 sc1\n" \
"buffer_load_dwordx4 a[84:87], v248, s[32:35], s82 offen offset:1024 sc1\n" \
"buffer_load_dwordx4 a[88:91], v248, s[32:35], s82 offen offset:2048 sc1\n" \
"buffer_load_dwordx4 a[92:95], v248, s[32:35], s82 offen offset:3072 sc1\n" \
"s_add_u32 s82, s15, 598016\n" \
"buffer_load_dwordx4 a[96:99], v248, s[32:35], s82 offen offset:0 sc1\n" \
"buffer_load_dwordx4 a[100:103], v248, s[32:35], s82 offen offset:1024 sc1\n" \
"buffer_load_dwordx4 a[104:107], v248, s[32:35], s82 offen offset:2048 sc1\n" \
"buffer_load_dwordx4 a[108:111], v248, s[32:35], s82 offen offset:3072 sc1\n" \
"s_add_u32 s82, s15, 602112\n" \
"buffer_load_dwordx4 a[112:115], v248, s[32:35], s82 offen offset:0 sc1\n" \
"buffer_load_dwordx4 a[116:119], v248, s[32:35], s82 offen offset:1024 sc1\n" \
"buffer_load_dwordx4 a[120:123], v248, s[32:35], s82 offen offset:2048 sc1\n" \
"buffer_load_dwordx4 a[124:127], v248, s[32:35], s82 offen offset:3072 sc1\n" \
"s_waitcnt vmcnt(0)\n" \
"v_sub_f32 v128, v128, v196\n" \
"v_sub_f32 v129, v129, v196\n" \
"v_sub_f32 v130, v130, v196\n" \
"v_sub_f32 v131, v131, v196\n" \
"v_sub_f32 v132, v132, v196\n" \
"v_sub_f32 v133, v133, v196\n" \
"v_sub_f32 v134, v134, v196\n" \
"v_sub_f32 v135, v135, v196\n" \
"v_sub_f32 v136, v136, v196\n" \
"v_sub_f32 v137, v137, v196\n" \
"v_sub_f32 v138, v138, v196\n" \
"v_sub_f32 v139, v139, v196\n" \
"v_sub_f32 v140, v140, v196\n" \
"v_sub_f32 v141, v141, v196\n" \
"v_sub_f32 v142, v142, v196\n" \
"v_sub_f32 v143, v143, v196\n" \
"v_exp_f32 v128, v128\n" \
"v_exp_f32 v129, v129\n" \
"v_exp_f32 v130, v130\n" \
"v_exp_f32 v131, v131\n" \
"v_exp_f32 v132, v132\n" \
"v_exp_f32 v133, v133\n" \
"v_exp_f32 v134, v134\n" \
"v_exp_f32 v135, v135\n" \
"v_exp_f32 v136, v136\n" \
"v_exp_f32 v137, v137\n" \
"v_exp_f32 v138, v138\n" \
"v_exp_f32 v139, v139\n" \
"v_exp_f32 v140, v140\n" \
"v_exp_f32 v141, v141\n" \
"v_exp_f32 v142, v142\n" \
"v_exp_f32 v143, v143\n" \
"v_cvt_pk_bf16_f32 v128, v128, v129\n" \
"v_cvt_pk_bf16_f32 v129, v130, v131\n" \
"v_cvt_pk_bf16_f32 v130, v132, v133\n" \
"v_cvt_pk_bf16_f32 v131, v134, v135\n" \
"v_cvt_pk_bf16_f32 v136, v136, v137\n" \
"v_cvt_pk_bf16_f32 v137, v138, v139\n" \
"v_cvt_pk_bf16_f32 v138, v140, v141\n" \
"v_cvt_pk_bf16_f32 v139, v142, v143\n" \
"v_mul_f32 v32, v198, v32\n" \
"v_mul_f32 v33, v198, v33\n" \
"v_mul_f32 v34, v198, v34\n" \
"v_mul_f32 v35, v198, v35\n" \
"v_mul_f32 v36, v198, v36\n" \
"v_mul_f32 v37, v198, v37\n" \
"v_mul_f32 v38, v198, v38\n" \
"v_mul_f32 v39, v198, v39\n" \
"v_mul_f32 v40, v198, v40\n" \
"v_mul_f32 v41, v198, v41\n" \
"v_mul_f32 v42, v198, v42\n" \
"v_mul_f32 v43, v198, v43\n" \
"v_mul_f32 v44, v198, v44\n" \
"v_mul_f32 v45, v198, v45\n" \
"v_mul_f32 v46, v198, v46\n" \
"v_mul_f32 v47, v198, v47\n" \
"v_mul_f32 v48, v198, v48\n" \
"v_mul_f32 v49, v198, v49\n" \
"v_mul_f32 v50, v198, v50\n" \
"v_mul_f32 v51, v198, v51\n" \
"v_mul_f32 v52, v198, v52\n" \
"v_mul_f32 v53, v198, v53\n" \
"v_mul_f32 v54, v198, v54\n" \
"v_mul_f32 v55, v198, v55\n" \
"v_mul_f32 v56, v198, v56\n" \
"v_mul_f32 v57, v198, v57\n" \
"v_mul_f32 v58, v198, v58\n" \
"v_mul_f32 v59, v198, v59\n" \
"v_mul_f32 v60, v198, v60\n" \
"v_mul_f32 v61, v198, v61\n" \
"v_mul_f32 v62, v198, v62\n" \
"v_mul_f32 v63, v198, v63\n" \
"v_accvgpr_read_b32 v197, a36\n" \
"v_mul_f32 v197, v198, v197\n" \
"v_accvgpr_write_b32 a36, v197\n" \
"v_accvgpr_read_b32 v197, a37\n" \
"v_mul_f32 v197, v198, v197\n" \
"v_accvgpr_write_b32 a37, v197\n" \
"v_accvgpr_read_b32 v197, a38\n" \
"v_mul_f32 v197, v198, v197\n" \
"v_accvgpr_write_b32 a38, v197\n" \
"v_accvgpr_read_b32 v197, a39\n" \
"v_mul_f32 v197, v198, v197\n" \
"v_accvgpr_write_b32 a39, v197\n" \
"s_nop 1\n" \
"v_mfma_f32_16x16x32_bf16 a[36:39], a[48:51], v[128:131], a[36:39]\n" \
"v_mfma_f32_16x16x32_bf16 a[36:39], a[48:51], v[136:139], a[36:39]\n" \
"v_mfma_f32_16x16x32_bf16 v[32:35], a[96:99], v[136:139], v[32:35]\n" \
"v_mfma_f32_16x16x32_bf16 v[32:35], a[64:67], v[128:131], v[32:35]\n" \
"v_mfma_f32_16x16x32_bf16 v[36:39], a[68:71], v[128:131], v[36:39]\n" \
"v_mfma_f32_16x16x32_bf16 v[36:39], a[100:103], v[136:139], v[36:39]\n" \
"v_mfma_f32_16x16x32_bf16 v[40:43], a[104:107], v[136:139], v[40:43]\n" \
"v_mfma_f32_16x16x32_bf16 v[40:43], a[72:75], v[128:131], v[40:43]\n" \
"v_mfma_f32_16x16x32_bf16 v[44:47], a[76:79], v[128:131], v[44:47]\n" \
"v_mfma_f32_16x16x32_bf16 v[44:47], a[108:111], v[136:139], v[44:47]\n" \
"v_mfma_f32_16x16x32_bf16 v[48:51], a[112:115], v[136:139], v[48:51]\n" \
"v_mfma_f32_16x16x32_bf16 v[48:51], a[80:83], v[128:131], v[48:51]\n" \
"v_mfma_f32_16x16x32_bf16 v[52:55], a[84:87], v[128:131], v[52:55]\n" \
"v_mfma_f32_16x16x32_bf16 v[52:55], a[116:119], v[136:139], v[52:55]\n" \
"v_mfma_f32_16x16x32_bf16 v[56:59], a[120:123], v[136:139], v[56:59]\n" \
"v_mfma_f32_16x16x32_bf16 v[56:59], a[88:91], v[128:131], v[56:59]\n" \
"v_mfma_f32_16x16x32_bf16 v[60:63], a[92:95], v[128:131], v[60:63]\n" \
"v_mfma_f32_16x16x32_bf16 v[60:63], a[124:127], v[136:139], v[60:63]\n" \
"s_nop 7\n" \
"s_add_u32 s15, s15, 0x4000\n" \
"s_sub_u32 s14, s14, 1\n" \
"s_cbranch_scc0 .Lslr0_%=\n" \
"s_nop 7\n" \
"s_setpc_b64 s[98:99]\n" \
  \
".Lredo2_%=:\n" \
"s_nop 7\n" \
"v_and_b32 v196, 31, v251\n" \
"v_lshlrev_b32 v196, 2, v196\n" \
"s_branch .Lspr2_%=\n" \
".Lsbr2_%=:\n" \
"v_and_b32 v196, 15, v251\n" \
"v_lshrrev_b32 v197, 4, v251\n" \
"v_lshlrev_b32 v197, 2, v197\n" \
"v_sub_u32 v196, v196, v197\n" \
"v_mov_b32 v199, 0xff800000\n" \
"s_sub_u32 s14, 15, s67\n" \
"s_xor_b32 s15, %[wid], 0\n" \
"s_lshl3_add_u32 s15, s14, s15\n" \
"s_lshl_b32 s15, s15, 4\n" \
"v_add_u32 v194, s15, v196\n" \
"v_mov_b32 v192, 0xff7fffff\n" \
"s_xor_b32 s15, %[wid], 7\n" \
"s_lshl3_add_u32 s15, s14, s15\n" \
"s_lshl_b32 s15, s15, 4\n" \
"v_add_u32 v195, s15, v196\n" \
"v_mov_b32 v193, 0xff7fffff\n" \
"v_mov_b32 v64, 0\n" \
"v_mov_b32 v65, 0\n" \
"v_mov_b32 v66, 0\n" \
"v_mov_b32 v67, 0\n" \
"v_mov_b32 v68, 0\n" \
"v_mov_b32 v69, 0\n" \
"v_mov_b32 v70, 0\n" \
"v_mov_b32 v71, 0\n" \
"v_mov_b32 v72, 0\n" \
"v_mov_b32 v73, 0\n" \
"v_mov_b32 v74, 0\n" \
"v_mov_b32 v75, 0\n" \
"v_mov_b32 v76, 0\n" \
"v_mov_b32 v77, 0\n" \
"v_mov_b32 v78, 0\n" \
"v_mov_b32 v79, 0\n" \
"v_mov_b32 v80, 0\n" \
"v_mov_b32 v81, 0\n" \
"v_mov_b32 v82, 0\n" \
"v_mov_b32 v83, 0\n" \
"v_mov_b32 v84, 0\n" \
"v_mov_b32 v85, 0\n" \
"v_mov_b32 v86, 0\n" \
"v_mov_b32 v87, 0\n" \
"v_mov_b32 v88, 0\n" \
"v_mov_b32 v89, 0\n" \
"v_mov_b32 v90, 0\n" \
"v_mov_b32 v91, 0\n" \
"v_mov_b32 v92, 0\n" \
"v_mov_b32 v93, 0\n" \
"v_mov_b32 v94, 0\n" \
"v_mov_b32 v95, 0\n" \
"v_mov_b32 v96, 0\n" \
"v_mov_b32 v97, 0\n" \
"v_mov_b32 v98, 0\n" \
"v_mov_b32 v99, 0\n" \
"v_mov_b32 v100, 0\n" \
"v_mov_b32 v101, 0\n" \
"v_mov_b32 v102, 0\n" \
"v_mov_b32 v103, 0\n" \
"v_mov_b32 v104, 0\n" \
"v_mov_b32 v105, 0\n" \
"v_mov_b32 v106, 0\n" \
"v_mov_b32 v107, 0\n" \
"v_mov_b32 v108, 0\n" \
"v_mov_b32 v109, 0\n" \
"v_mov_b32 v110, 0\n" \
"v_mov_b32 v111, 0\n" \
"v_mov_b32 v112, 0\n" \
"v_mov_b32 v113, 0\n" \
"v_mov_b32 v114, 0\n" \
"v_mov_b32 v115, 0\n" \
"v_mov_b32 v116, 0\n" \
"v_mov_b32 v117, 0\n" \
"v_mov_b32 v118, 0\n" \
"v_mov_b32 v119, 0\n" \
"v_mov_b32 v120, 0\n" \
"v_mov_b32 v121, 0\n" \
"v_mov_b32 v122, 0\n" \
"v_mov_b32 v123, 0\n" \
"v_mov_b32 v124, 0\n" \
"v_mov_b32 v125, 0\n" \
"v_mov_b32 v126, 0\n" \
"v_mov_b32 v127, 0\n" \
"v_accvgpr_write_b32 a40, 0\n" \
"v_accvgpr_write_b32 a41, 0\n" \
"v_accvgpr_write_b32 a42, 0\n" \
"v_accvgpr_write_b32 a43, 0\n" \
"v_accvgpr_write_b32 a44, 0\n" \
"v_accvgpr_write_b32 a45, 0\n" \
"v_accvgpr_write_b32 a46, 0\n" \
"v_accvgpr_write_b32 a47, 0\n" \
"s_lshl1_add_u32 s14, s14, 1\n" \
"s_mov_b32 s15, s73\n" \
".Lslr2_%=:\n" \
"s_add_u32 s82, s15, 0\n" \
"buffer_load_dwordx4 a[128:131], v248, s[32:35], s82 offen offset:0 sc1\n" \
"buffer_load_dwordx4 a[132:135], v248, s[32:35], s82 offen offset:1024 sc1\n" \
"buffer_load_dwordx4 a[136:139], v248, s[32:35], s82 offen offset:2048 sc1\n" \
"buffer_load_dwordx4 a[140:143], v248, s[32:35], s82 offen offset:3072 sc1\n" \
"s_add_u32 s82, s15, 4096\n" \
"buffer_load_dwordx4 a[144:147], v248, s[32:35], s82 offen offset:0 sc1\n" \
"buffer_load_dwordx4 a[148:151], v248, s[32:35], s82 offen offset:1024 sc1\n" \
"buffer_load_dwordx4 a[152:155], v248, s[32:35], s82 offen offset:2048 sc1\n" \
"buffer_load_dwordx4 a[156:159], v248, s[32:35], s82 offen offset:3072 sc1\n" \
"s_add_u32 s82, s15, 8192\n" \
"buffer_load_dwordx4 a[160:163], v248, s[32:35], s82 offen offset:0 sc1\n" \
"buffer_load_dwordx4 a[164:167], v248, s[32:35], s82 offen offset:1024 sc1\n" \
"buffer_load_dwordx4 a[168:171], v248, s[32:35], s82 offen offset:2048 sc1\n" \
"buffer_load_dwordx4 a[172:175], v248, s[32:35], s82 offen offset:3072 sc1\n" \
"s_add_u32 s82, s15, 12288\n" \
"buffer_load_dwordx4 a[176:179], v248, s[32:35], s82 offen offset:0 sc1\n" \
"buffer_load_dwordx4 a[180:183], v248, s[32:35], s82 offen offset:1024 sc1\n" \
"buffer_load_dwordx4 a[184:187], v248, s[32:35], s82 offen offset:2048 sc1\n" \
"buffer_load_dwordx4 a[188:191], v248, s[32:35], s82 offen offset:3072 sc1\n" \
"s_waitcnt vmcnt(0)\n" \
"v_mfma_f32_16x16x32_bf16 v[128:131], a[128:131], v[160:163], 0\n" \
"v_mfma_f32_16x16x32_bf16 v[128:131], a[132:135], v[164:167], v[128:131]\n" \
"v_mfma_f32_16x16x32_bf16 v[128:131], a[136:139], v[168:171], v[128:131]\n" \
"v_mfma_f32_16x16x32_bf16 v[128:131], a[140:143], v[172:175], v[128:131]\n" \
"v_mfma_f32_16x16x32_bf16 v[132:135], a[156:159], v[172:175], 0\n" \
"v_mfma_f32_16x16x32_bf16 v[132:135], a[152:155], v[168:171], v[132:135]\n" \
"v_mfma_f32_16x16x32_bf16 v[132:135], a[148:151], v[164:167], v[132:135]\n" \
"v_mfma_f32_16x16x32_bf16 v[132:135], a[144:147], v[160:163], v[132:135]\n" \
"v_mfma_f32_16x16x32_bf16 v[136:139], a[160:163], v[160:163], 0\n" \
"v_mfma_f32_16x16x32_bf16 v[136:139], a[164:167], v[164:167], v[136:139]\n" \
"v_mfma_f32_16x16x32_bf16 v[136:139], a[168:171], v[168:171], v[136:139]\n" \
"v_mfma_f32_16x16x32_bf16 v[136:139], a[172:175], v[172:175], v[136:139]\n" \
"v_mfma_f32_16x16x32_bf16 v[140:143], a[188:191], v[172:175], 0\n" \
"v_mfma_f32_16x16x32_bf16 v[140:143], a[184:187], v[168:171], v[140:143]\n" \
"v_mfma_f32_16x16x32_bf16 v[140:143], a[180:183], v[164:167], v[140:143]\n" \
"v_mfma_f32_16x16x32_bf16 v[140:143], a[176:179], v[160:163], v[140:143]\n" \
"v_cmp_gt_i32 vcc, 0, v194\n" \
"s_nop 1\n" \
"v_cndmask_b32 v128, v128, v199, vcc\n" \
"v_cmp_gt_i32 vcc, 1, v194\n" \
"s_nop 1\n" \
"v_cndmask_b32 v129, v129, v199, vcc\n" \
"v_cmp_gt_i32 vcc, 2, v194\n" \
"s_nop 1\n" \
"v_cndmask_b32 v130, v130, v199, vcc\n" \
"v_cmp_gt_i32 vcc, 3, v194\n" \
"s_nop 1\n" \
"v_cndmask_b32 v131, v131, v199, vcc\n" \
"v_cmp_gt_i32 vcc, 16, v194\n" \
"s_nop 1\n" \
"v_cndmask_b32 v132, v132, v199, vcc\n" \
"v_cmp_gt_i32 vcc, 17, v194\n" \
"s_nop 1\n" \
"v_cndmask_b32 v133, v133, v199, vcc\n" \
"v_cmp_gt_i32 vcc, 18, v194\n" \
"s_nop 1\n" \
"v_cndmask_b32 v134, v134, v199, vcc\n" \
"v_cmp_gt_i32 vcc, 19, v194\n" \
"s_nop 1\n" \
"v_cndmask_b32 v135, v135, v199, vcc\n" \
"v_cmp_gt_i32 vcc, 32, v194\n" \
"s_nop 1\n" \
"v_cndmask_b32 v136, v136, v199, vcc\n" \
"v_cmp_gt_i32 vcc, 33, v194\n" \
"s_nop 1\n" \
"v_cndmask_b32 v137, v137, v199, vcc\n" \
"v_cmp_gt_i32 vcc, 34, v194\n" \
"s_nop 1\n" \
"v_cndmask_b32 v138, v138, v199, vcc\n" \
"v_cmp_gt_i32 vcc, 35, v194\n" \
"s_nop 1\n" \
"v_cndmask_b32 v139, v139, v199, vcc\n" \
"v_cmp_gt_i32 vcc, 48, v194\n" \
"s_nop 1\n" \
"v_cndmask_b32 v140, v140, v199, vcc\n" \
"v_cmp_gt_i32 vcc, 49, v194\n" \
"s_nop 1\n" \
"v_cndmask_b32 v141, v141, v199, vcc\n" \
"v_cmp_gt_i32 vcc, 50, v194\n" \
"s_nop 1\n" \
"v_cndmask_b32 v142, v142, v199, vcc\n" \
"v_cmp_gt_i32 vcc, 51, v194\n" \
"s_nop 1\n" \
"v_cndmask_b32 v143, v143, v199, vcc\n" \
"v_max3_f32 v196, v192, v128, v129\n" \
"v_max3_f32 v196, v196, v130, v131\n" \
"v_max3_f32 v196, v196, v132, v133\n" \
"v_max3_f32 v196, v196, v134, v135\n" \
"v_max3_f32 v196, v196, v136, v137\n" \
"v_max3_f32 v196, v196, v138, v139\n" \
"v_max3_f32 v196, v196, v140, v141\n" \
"v_max3_f32 v196, v196, v142, v143\n" \
"v_mov_b32 v197, v196\n" \
"s_nop 1\n" \
"v_permlane32_swap_b32 v196, v197\n" \
"v_max_f32 v196, v196, v197\n" \
"v_mov_b32 v197, v196\n" \
"s_nop 1\n" \
"v_permlane16_swap_b32 v196, v197\n" \
"v_max_f32 v196, v196, v197\n" \
"v_sub_f32 v198, v192, v196\n" \
"v_mov_b32 v192, v196\n" \
"v_exp_f32 v198, v198\n" \
"v_subrev_u32 v194, 64, v194\n" \
"s_add_u32 s82, s15, 589824\n" \
"buffer_load_dwordx4 a[192:195], v248, s[32:35], s82 offen offset:0 sc1\n" \
"buffer_load_dwordx4 a[196:199], v248, s[32:35], s82 offen offset:1024 sc1\n" \
"buffer_load_dwordx4 a[200:203], v248, s[32:35], s82 offen offset:2048 sc1\n" \
"buffer_load_dwordx4 a[204:207], v248, s[32:35], s82 offen offset:3072 sc1\n" \
"s_add_u32 s82, s15, 593920\n" \
"buffer_load_dwordx4 a[208:211], v248, s[32:35], s82 offen offset:0 sc1\n" \
"buffer_load_dwordx4 a[212:215], v248, s[32:35], s82 offen offset:1024 sc1\n" \
"buffer_load_dwordx4 a[216:219], v248, s[32:35], s82 offen offset:2048 sc1\n" \
"buffer_load_dwordx4 a[220:223], v248, s[32:35], s82 offen offset:3072 sc1\n" \
"s_add_u32 s82, s15, 598016\n" \
"buffer_load_dwordx4 a[224:227], v248, s[32:35], s82 offen offset:0 sc1\n" \
"buffer_load_dwordx4 a[228:231], v248, s[32:35], s82 offen offset:1024 sc1\n" \
"buffer_load_dwordx4 a[232:235], v248, s[32:35], s82 offen offset:2048 sc1\n" \
"buffer_load_dwordx4 a[236:239], v248, s[32:35], s82 offen offset:3072 sc1\n" \
"s_add_u32 s82, s15, 602112\n" \
"buffer_load_dwordx4 a[240:243], v248, s[32:35], s82 offen offset:0 sc1\n" \
"buffer_load_dwordx4 a[244:247], v248, s[32:35], s82 offen offset:1024 sc1\n" \
"buffer_load_dwordx4 a[248:251], v248, s[32:35], s82 offen offset:2048 sc1\n" \
"buffer_load_dwordx4 a[252:255], v248, s[32:35], s82 offen offset:3072 sc1\n" \
"s_waitcnt vmcnt(0)\n" \
"v_sub_f32 v128, v128, v196\n" \
"v_sub_f32 v129, v129, v196\n" \
"v_sub_f32 v130, v130, v196\n" \
"v_sub_f32 v131, v131, v196\n" \
"v_sub_f32 v132, v132, v196\n" \
"v_sub_f32 v133, v133, v196\n" \
"v_sub_f32 v134, v134, v196\n" \
"v_sub_f32 v135, v135, v196\n" \
"v_sub_f32 v136, v136, v196\n" \
"v_sub_f32 v137, v137, v196\n" \
"v_sub_f32 v138, v138, v196\n" \
"v_sub_f32 v139, v139, v196\n" \
"v_sub_f32 v140, v140, v196\n" \
"v_sub_f32 v141, v141, v196\n" \
"v_sub_f32 v142, v142, v196\n" \
"v_sub_f32 v143, v143, v196\n" \
"v_exp_f32 v128, v128\n" \
"v_exp_f32 v129, v129\n" \
"v_exp_f32 v130, v130\n" \
"v_exp_f32 v131, v131\n" \
"v_exp_f32 v132, v132\n" \
"v_exp_f32 v133, v133\n" \
"v_exp_f32 v134, v134\n" \
"v_exp_f32 v135, v135\n" \
"v_exp_f32 v136, v136\n" \
"v_exp_f32 v137, v137\n" \
"v_exp_f32 v138, v138\n" \
"v_exp_f32 v139, v139\n" \
"v_exp_f32 v140, v140\n" \
"v_exp_f32 v141, v141\n" \
"v_exp_f32 v142, v142\n" \
"v_exp_f32 v143, v143\n" \
"v_cvt_pk_bf16_f32 v128, v128, v129\n" \
"v_cvt_pk_bf16_f32 v129, v130, v131\n" \
"v_cvt_pk_bf16_f32 v130, v132, v133\n" \
"v_cvt_pk_bf16_f32 v131, v134, v135\n" \
"v_cvt_pk_bf16_f32 v136, v136, v137\n" \
"v_cvt_pk_bf16_f32 v137, v138, v139\n" \
"v_cvt_pk_bf16_f32 v138, v140, v141\n" \
"v_cvt_pk_bf16_f32 v139, v142, v143\n" \
"v_mul_f32 v64, v198, v64\n" \
"v_mul_f32 v65, v198, v65\n" \
"v_mul_f32 v66, v198, v66\n" \
"v_mul_f32 v67, v198, v67\n" \
"v_mul_f32 v68, v198, v68\n" \
"v_mul_f32 v69, v198, v69\n" \
"v_mul_f32 v70, v198, v70\n" \
"v_mul_f32 v71, v198, v71\n" \
"v_mul_f32 v72, v198, v72\n" \
"v_mul_f32 v73, v198, v73\n" \
"v_mul_f32 v74, v198, v74\n" \
"v_mul_f32 v75, v198, v75\n" \
"v_mul_f32 v76, v198, v76\n" \
"v_mul_f32 v77, v198, v77\n" \
"v_mul_f32 v78, v198, v78\n" \
"v_mul_f32 v79, v198, v79\n" \
"v_mul_f32 v80, v198, v80\n" \
"v_mul_f32 v81, v198, v81\n" \
"v_mul_f32 v82, v198, v82\n" \
"v_mul_f32 v83, v198, v83\n" \
"v_mul_f32 v84, v198, v84\n" \
"v_mul_f32 v85, v198, v85\n" \
"v_mul_f32 v86, v198, v86\n" \
"v_mul_f32 v87, v198, v87\n" \
"v_mul_f32 v88, v198, v88\n" \
"v_mul_f32 v89, v198, v89\n" \
"v_mul_f32 v90, v198, v90\n" \
"v_mul_f32 v91, v198, v91\n" \
"v_mul_f32 v92, v198, v92\n" \
"v_mul_f32 v93, v198, v93\n" \
"v_mul_f32 v94, v198, v94\n" \
"v_mul_f32 v95, v198, v95\n" \
"v_accvgpr_read_b32 v197, a40\n" \
"v_mul_f32 v197, v198, v197\n" \
"v_accvgpr_write_b32 a40, v197\n" \
"v_accvgpr_read_b32 v197, a41\n" \
"v_mul_f32 v197, v198, v197\n" \
"v_accvgpr_write_b32 a41, v197\n" \
"v_accvgpr_read_b32 v197, a42\n" \
"v_mul_f32 v197, v198, v197\n" \
"v_accvgpr_write_b32 a42, v197\n" \
"v_accvgpr_read_b32 v197, a43\n" \
"v_mul_f32 v197, v198, v197\n" \
"v_accvgpr_write_b32 a43, v197\n" \
"s_nop 1\n" \
"v_mfma_f32_16x16x32_bf16 a[40:43], a[48:51], v[128:131], a[40:43]\n" \
"v_mfma_f32_16x16x32_bf16 a[40:43], a[48:51], v[136:139], a[40:43]\n" \
"v_mfma_f32_16x16x32_bf16 v[64:67], a[224:227], v[136:139], v[64:67]\n" \
"v_mfma_f32_16x16x32_bf16 v[64:67], a[192:195], v[128:131], v[64:67]\n" \
"v_mfma_f32_16x16x32_bf16 v[68:71], a[196:199], v[128:131], v[68:71]\n" \
"v_mfma_f32_16x16x32_bf16 v[68:71], a[228:231], v[136:139], v[68:71]\n" \
"v_mfma_f32_16x16x32_bf16 v[72:75], a[232:235], v[136:139], v[72:75]\n" \
"v_mfma_f32_16x16x32_bf16 v[72:75], a[200:203], v[128:131], v[72:75]\n" \
"v_mfma_f32_16x16x32_bf16 v[76:79], a[204:207], v[128:131], v[76:79]\n" \
"v_mfma_f32_16x16x32_bf16 v[76:79], a[236:239], v[136:139], v[76:79]\n" \
"v_mfma_f32_16x16x32_bf16 v[80:83], a[240:243], v[136:139], v[80:83]\n" \
"v_mfma_f32_16x16x32_bf16 v[80:83], a[208:211], v[128:131], v[80:83]\n" \
"v_mfma_f32_16x16x32_bf16 v[84:87], a[212:215], v[128:131], v[84:87]\n" \
"v_mfma_f32_16x16x32_bf16 v[84:87], a[244:247], v[136:139], v[84:87]\n" \
"v_mfma_f32_16x16x32_bf16 v[88:91], a[248:251], v[136:139], v[88:91]\n" \
"v_mfma_f32_16x16x32_bf16 v[88:91], a[216:219], v[128:131], v[88:91]\n" \
"v_mfma_f32_16x16x32_bf16 v[92:95], a[220:223], v[128:131], v[92:95]\n" \
"v_mfma_f32_16x16x32_bf16 v[92:95], a[252:255], v[136:139], v[92:95]\n" \
"s_nop 7\n" \
"v_mfma_f32_16x16x32_bf16 v[128:131], a[128:131], v[176:179], 0\n" \
"v_mfma_f32_16x16x32_bf16 v[128:131], a[132:135], v[180:183], v[128:131]\n" \
"v_mfma_f32_16x16x32_bf16 v[128:131], a[136:139], v[184:187], v[128:131]\n" \
"v_mfma_f32_16x16x32_bf16 v[128:131], a[140:143], v[188:191], v[128:131]\n" \
"v_mfma_f32_16x16x32_bf16 v[132:135], a[156:159], v[188:191], 0\n" \
"v_mfma_f32_16x16x32_bf16 v[132:135], a[152:155], v[184:187], v[132:135]\n" \
"v_mfma_f32_16x16x32_bf16 v[132:135], a[148:151], v[180:183], v[132:135]\n" \
"v_mfma_f32_16x16x32_bf16 v[132:135], a[144:147], v[176:179], v[132:135]\n" \
"v_mfma_f32_16x16x32_bf16 v[136:139], a[160:163], v[176:179], 0\n" \
"v_mfma_f32_16x16x32_bf16 v[136:139], a[164:167], v[180:183], v[136:139]\n" \
"v_mfma_f32_16x16x32_bf16 v[136:139], a[168:171], v[184:187], v[136:139]\n" \
"v_mfma_f32_16x16x32_bf16 v[136:139], a[172:175], v[188:191], v[136:139]\n" \
"v_mfma_f32_16x16x32_bf16 v[140:143], a[188:191], v[188:191], 0\n" \
"v_mfma_f32_16x16x32_bf16 v[140:143], a[184:187], v[184:187], v[140:143]\n" \
"v_mfma_f32_16x16x32_bf16 v[140:143], a[180:183], v[180:183], v[140:143]\n" \
"v_mfma_f32_16x16x32_bf16 v[140:143], a[176:179], v[176:179], v[140:143]\n" \
"v_cmp_gt_i32 vcc, 0, v195\n" \
"s_nop 1\n" \
"v_cndmask_b32 v128, v128, v199, vcc\n" \
"v_cmp_gt_i32 vcc, 1, v195\n" \
"s_nop 1\n" \
"v_cndmask_b32 v129, v129, v199, vcc\n" \
"v_cmp_gt_i32 vcc, 2, v195\n" \
"s_nop 1\n" \
"v_cndmask_b32 v130, v130, v199, vcc\n" \
"v_cmp_gt_i32 vcc, 3, v195\n" \
"s_nop 1\n" \
"v_cndmask_b32 v131, v131, v199, vcc\n" \
"v_cmp_gt_i32 vcc, 16, v195\n" \
"s_nop 1\n" \
"v_cndmask_b32 v132, v132, v199, vcc\n" \
"v_cmp_gt_i32 vcc, 17, v195\n" \
"s_nop 1\n" \
"v_cndmask_b32 v133, v133, v199, vcc\n" \
"v_cmp_gt_i32 vcc, 18, v195\n" \
"s_nop 1\n" \
"v_cndmask_b32 v134, v134, v199, vcc\n" \
"v_cmp_gt_i32 vcc, 19, v195\n" \
"s_nop 1\n" \
"v_cndmask_b32 v135, v135, v199, vcc\n" \
"v_cmp_gt_i32 vcc, 32, v195\n" \
"s_nop 1\n" \
"v_cndmask_b32 v136, v136, v199, vcc\n" \
"v_cmp_gt_i32 vcc, 33, v195\n" \
"s_nop 1\n" \
"v_cndmask_b32 v137, v137, v199, vcc\n" \
"v_cmp_gt_i32 vcc, 34, v195\n" \
"s_nop 1\n" \
"v_cndmask_b32 v138, v138, v199, vcc\n" \
"v_cmp_gt_i32 vcc, 35, v195\n" \
"s_nop 1\n" \
"v_cndmask_b32 v139, v139, v199, vcc\n" \
"v_cmp_gt_i32 vcc, 48, v195\n" \
"s_nop 1\n" \
"v_cndmask_b32 v140, v140, v199, vcc\n" \
"v_cmp_gt_i32 vcc, 49, v195\n" \
"s_nop 1\n" \
"v_cndmask_b32 v141, v141, v199, vcc\n" \
"v_cmp_gt_i32 vcc, 50, v195\n" \
"s_nop 1\n" \
"v_cndmask_b32 v142, v142, v199, vcc\n" \
"v_cmp_gt_i32 vcc, 51, v195\n" \
"s_nop 1\n" \
"v_cndmask_b32 v143, v143, v199, vcc\n" \
"v_max3_f32 v196, v193, v128, v129\n" \
"v_max3_f32 v196, v196, v130, v131\n" \
"v_max3_f32 v196, v196, v132, v133\n" \
"v_max3_f32 v196, v196, v134, v135\n" \
"v_max3_f32 v196, v196, v136, v137\n" \
"v_max3_f32 v196, v196, v138, v139\n" \
"v_max3_f32 v196, v196, v140, v141\n" \
"v_max3_f32 v196, v196, v142, v143\n" \
"v_mov_b32 v197, v196\n" \
"s_nop 1\n" \
"v_permlane32_swap_b32 v196, v197\n" \
"v_max_f32 v196, v196, v197\n" \
"v_mov_b32 v197, v196\n" \
"s_nop 1\n" \
"v_permlane16_swap_b32 v196, v197\n" \
"v_max_f32 v196, v196, v197\n" \
"v_sub_f32 v198, v193, v196\n" \
"v_mov_b32 v193, v196\n" \
"v_exp_f32 v198, v198\n" \
"v_subrev_u32 v195, 64, v195\n" \
"v_sub_f32 v128, v128, v196\n" \
"v_sub_f32 v129, v129, v196\n" \
"v_sub_f32 v130, v130, v196\n" \
"v_sub_f32 v131, v131, v196\n" \
"v_sub_f32 v132, v132, v196\n" \
"v_sub_f32 v133, v133, v196\n" \
"v_sub_f32 v134, v134, v196\n" \
"v_sub_f32 v135, v135, v196\n" \
"v_sub_f32 v136, v136, v196\n" \
"v_sub_f32 v137, v137, v196\n" \
"v_sub_f32 v138, v138, v196\n" \
"v_sub_f32 v139, v139, v196\n" \
"v_sub_f32 v140, v140, v196\n" \
"v_sub_f32 v141, v141, v196\n" \
"v_sub_f32 v142, v142, v196\n" \
"v_sub_f32 v143, v143, v196\n" \
"v_exp_f32 v128, v128\n" \
"v_exp_f32 v129, v129\n" \
"v_exp_f32 v130, v130\n" \
"v_exp_f32 v131, v131\n" \
"v_exp_f32 v132, v132\n" \
"v_exp_f32 v133, v133\n" \
"v_exp_f32 v134, v134\n" \
"v_exp_f32 v135, v135\n" \
"v_exp_f32 v136, v136\n" \
"v_exp_f32 v137, v137\n" \
"v_exp_f32 v138, v138\n" \
"v_exp_f32 v139, v139\n" \
"v_exp_f32 v140, v140\n" \
"v_exp_f32 v141, v141\n" \
"v_exp_f32 v142, v142\n" \
"v_exp_f32 v143, v143\n" \
"v_cvt_pk_bf16_f32 v128, v128, v129\n" \
"v_cvt_pk_bf16_f32 v129, v130, v131\n" \
"v_cvt_pk_bf16_f32 v130, v132, v133\n" \
"v_cvt_pk_bf16_f32 v131, v134, v135\n" \
"v_cvt_pk_bf16_f32 v136, v136, v137\n" \
"v_cvt_pk_bf16_f32 v137, v138, v139\n" \
"v_cvt_pk_bf16_f32 v138, v140, v141\n" \
"v_cvt_pk_bf16_f32 v139, v142, v143\n" \
"v_mul_f32 v96, v198, v96\n" \
"v_mul_f32 v97, v198, v97\n" \
"v_mul_f32 v98, v198, v98\n" \
"v_mul_f32 v99, v198, v99\n" \
"v_mul_f32 v100, v198, v100\n" \
"v_mul_f32 v101, v198, v101\n" \
"v_mul_f32 v102, v198, v102\n" \
"v_mul_f32 v103, v198, v103\n" \
"v_mul_f32 v104, v198, v104\n" \
"v_mul_f32 v105, v198, v105\n" \
"v_mul_f32 v106, v198, v106\n" \
"v_mul_f32 v107, v198, v107\n" \
"v_mul_f32 v108, v198, v108\n" \
"v_mul_f32 v109, v198, v109\n" \
"v_mul_f32 v110, v198, v110\n" \
"v_mul_f32 v111, v198, v111\n" \
"v_mul_f32 v112, v198, v112\n" \
"v_mul_f32 v113, v198, v113\n" \
"v_mul_f32 v114, v198, v114\n" \
"v_mul_f32 v115, v198, v115\n" \
"v_mul_f32 v116, v198, v116\n" \
"v_mul_f32 v117, v198, v117\n" \
"v_mul_f32 v118, v198, v118\n" \
"v_mul_f32 v119, v198, v119\n" \
"v_mul_f32 v120, v198, v120\n" \
"v_mul_f32 v121, v198, v121\n" \
"v_mul_f32 v122, v198, v122\n" \
"v_mul_f32 v123, v198, v123\n" \
"v_mul_f32 v124, v198, v124\n" \
"v_mul_f32 v125, v198, v125\n" \
"v_mul_f32 v126, v198, v126\n" \
"v_mul_f32 v127, v198, v127\n" \
"v_accvgpr_read_b32 v197, a44\n" \
"v_mul_f32 v197, v198, v197\n" \
"v_accvgpr_write_b32 a44, v197\n" \
"v_accvgpr_read_b32 v197, a45\n" \
"v_mul_f32 v197, v198, v197\n" \
"v_accvgpr_write_b32 a45, v197\n" \
"v_accvgpr_read_b32 v197, a46\n" \
"v_mul_f32 v197, v198, v197\n" \
"v_accvgpr_write_b32 a46, v197\n" \
"v_accvgpr_read_b32 v197, a47\n" \
"v_mul_f32 v197, v198, v197\n" \
"v_accvgpr_write_b32 a47, v197\n" \
"s_nop 1\n" \
"v_mfma_f32_16x16x32_bf16 a[44:47], a[48:51], v[128:131], a[44:47]\n" \
"v_mfma_f32_16x16x32_bf16 a[44:47], a[48:51], v[136:139], a[44:47]\n" \
"v_mfma_f32_16x16x32_bf16 v[96:99], a[224:227], v[136:139], v[96:99]\n" \
"v_mfma_f32_16x16x32_bf16 v[96:99], a[192:195], v[128:131], v[96:99]\n" \
"v_mfma_f32_16x16x32_bf16 v[100:103], a[196:199], v[128:131], v[100:103]\n" \
"v_mfma_f32_16x16x32_bf16 v[100:103], a[228:231], v[136:139], v[100:103]\n" \
"v_mfma_f32_16x16x32_bf16 v[104:107], a[232:235], v[136:139], v[104:107]\n" \
"v_mfma_f32_16x16x32_bf16 v[104:107], a[200:203], v[128:131], v[104:107]\n" \
"v_mfma_f32_16x16x32_bf16 v[108:111], a[204:207], v[128:131], v[108:111]\n" \
"v_mfma_f32_16x16x32_bf16 v[108:111], a[236:239], v[136:139], v[108:111]\n" \
"v_mfma_f32_16x16x32_bf16 v[112:115], a[240:243], v[136:139], v[112:115]\n" \
"v_mfma_f32_16x16x32_bf16 v[112:115], a[208:211], v[128:131], v[112:115]\n" \
"v_mfma_f32_16x16x32_bf16 v[116:119], a[212:215], v[128:131], v[116:119]\n" \
"v_mfma_f32_16x16x32_bf16 v[116:119], a[244:247], v[136:139], v[116:119]\n" \
"v_mfma_f32_16x16x32_bf16 v[120:123], a[248:251], v[136:139], v[120:123]\n" \
"v_mfma_f32_16x16x32_bf16 v[120:123], a[216:219], v[128:131], v[120:123]\n" \
"v_mfma_f32_16x16x32_bf16 v[124:127], a[220:223], v[128:131], v[124:127]\n" \
"v_mfma_f32_16x16x32_bf16 v[124:127], a[252:255], v[136:139], v[124:127]\n" \
"s_nop 7\n" \
"s_add_u32 s15, s15, 0x4000\n" \
"s_sub_u32 s14, s14, 1\n" \
"s_cbranch_scc0 .Lslr2_%=\n" \
"s_nop 7\n" \
"s_setpc_b64 s[98:99]\n" \
".Lspr2_%=:\n" \
"s_mov_b32 s82, 0x100000\n" \
".Lsqr2_%=:\n" \
"s_sleep 6\n" \
"buffer_load_dword v197, v196, s[32:35], s26 offen sc1\n" \
"s_waitcnt vmcnt(0)\n" \
"v_cmp_eq_u32 vcc, 0, v197\n" \
"s_cbranch_vccz .Lsbr2_%=\n" \
"s_sub_u32 s82, s82, 1\n" \
"s_cbranch_scc0 .Lsqr2_%=\n" \
"s_branch .Lsbr2_%=\n" \
".Lend_%=:\n" \
".Lst3_5_%=:\n" \
"s_cbranch_execz .Lstb3_%=\n"
struct Desc { const void* p; unsigned bytes, flags; };
struct Args { Desc ckv, w_uk, w_uv, w_q, ws, w_mod, cvec, x, w_o, wg, wu, wd, out; const void* w_r; };
constexpr size_t WS_DBG = (size_t)56 << 20; constexpr unsigned WS_BYTES = 80u << 20, LDS_BYTES = 163840;
typedef const __attribute__((address_space(4))) Args* kargs_t;
typedef unsigned u32x16 __attribute__((ext_vector_type(16)));
#define VCLOB_LOW "v0","v1","v2","v3","v4","v5","v6","v7","v8","v9","v10","v11","v12","v13","v14","v15","v16","v17","v18","v19","v20","v21","v22","v23","v24","v25","v26","v27","v28","v29","v30","v31","v32","v33","v34","v35","v36","v37","v38","v39","v40","v41","v42","v43","v44","v45","v46","v47","v48","v49","v50","v51","v52","v53","v54","v55","v56","v57","v58","v59","v60","v61","v62","v63","v64","v65","v66","v67","v68","v69","v70","v71","v72","v73","v74","v75","v76","v77","v78","v79","v80","v81","v82","v83","v84","v85","v86","v87","v88","v89","v90","v91","v92","v93","v94","v95","v96","v97","v98","v99","v100","v101","v102","v103","v104","v105","v106","v107","v108","v109","v110","v111","v112","v113","v114","v115","v116","v117","v118","v119","v120","v121","v122","v123","v124","v125","v126","v127","v128","v129","v130","v131","v132","v133","v134","v135","v136","v137","v138","v139","v140","v141","v142","v143","v144","v145","v146","v147","v148","v149","v150","v151","v152","v153","v154","v155","v156","v157","v158","v159","v160","v161","v162","v163","v164","v165","v166","v167","v168","v169","v170","v171","v172","v173","v174","v175","v176","v177","v178","v179","v180","v181","v182","v183","v184","v185","v186","v187","v188","v189","v190","v191","v192","v193","v194","v195","v196","v197","v198","v199","v200","v201","v202","v203","v204","v205","v206","v207","v208","v209","v210","v211","v212","v213","v214","v215","v216","v217","v218","v219","v220","v221","v222","v223","v224","v225","v226","v227","v228","v229","v230","v231","v232","v233","v234","v235","v236","v237","v238","v239"
#define SCLOB "s14","s15","s16","s17","s18","s19","s20","s21","s22","s23","s24","s25","s26","s27","s28","s29","s30","s31","s32","s33","s34","s35","s36","s37","s38","s39","s40","s41","s42","s43","s44","s45","s46","s47","s48","s49","s50","s51","s52","s53","s54","s55","s56","s57","s58","s59","s60","s61","s62","s63","s64","s65","s66","s67","s68","s69","s70","s71","s72","s73","s74","s75","s76","s77","s78","s79","s80","s81","s82","s83","s84","s85","s86","s87","s88","s89","s90","s91","s92","s93","s94","s95","s96","s97","s98","s99","s100","s101"
__global__ __launch_bounds__(256, 1) __attribute__((aligned(4096))) void k_all(const Args a) {
    unsigned wid = __builtin_amdgcn_readfirstlane(threadIdx.x >> 6);
    u32x16 RB;
    asm volatile(MAIN_ASM : "={v[240:255]}"(RB)
                 : [ka] "s"(__builtin_amdgcn_kernarg_segment_ptr()), [bid] "s"(blockIdx.x), [wid] "s"(wid) : "memory", "vcc", "scc", VCLOB_LOW, SCLOB, ACLOB_ALL);
    asm volatile(".Lat_code_e:");
}
extern "C" void kernel_launch(void* const* d_in, const int* in_sizes, int n_in, void* d_out, int out_size, void* d_ws, size_t ws_size, hipStream_t stream) {
    Args a;
    auto D = [](const void* p, unsigned bytes) { return Desc{p, bytes, 0x00020000u}; };
    a.ckv = D(d_in[1], SEQ * DM * 2); a.w_uk = D(d_in[3], DM * HD * 2); a.w_uv = D(d_in[4], DM * HD * 2); a.w_q = D(d_in[2], DM * HD * 2);
    a.ws = D(d_ws, WS_BYTES); a.w_mod = D(d_in[12], DM * 6 * DM * 2); a.cvec = D(d_in[11], DM * 2); a.x = D(d_in[0], SEQ * DM * 2);
    a.w_o = D(d_in[5], HD * DM * 2); a.wg = D(d_in[8], DM * FF * 2); a.wu = D(d_in[9], DM * FF * 2); a.wd = D(d_in[10], FF * DM * 2);
    a.out = D(d_out, SEQ * DM * 2); a.w_r = d_in[7];
    (void)hipFuncSetAttribute((const void*)k_all, hipFuncAttributeMaxDynamicSharedMemorySize, LDS_BYTES);
    k_all<<<256, 256, LDS_BYTES, stream>>>(a);
}
```

```cpp
#include <hip/hip_runtime.h>
#include <stdint.h>
constexpr int SEQ = 2048, DM = 512, HD = 4096, FF = 1792;
#define ACLOB_ALL "a0","a1","a2","a3","a4","a5","a6","a7","a8","a9","a10","a11","a12","a13","a14","a15","a16","a17","a18","a19","a20","a21","a22","a23","a24","a25","a26","a27","a28","a29","a30","a31","a32","a33","a34","a35","a36","a37","a38","a39","a40","a41","a42","a43","a44","a45","a46","a47","a48","a49","a50","a51","a52","a53","a54","a55","a56","a57","a58","a59","a60","a61","a62","a63","a64","a65","a66","a67","a68","a69","a70","a71","a72","a73","a74","a75","a76","a77","a78","a79","a80","a81","a82","a83","a84","a85","a86","a87","a88","a89","a90","a91","a92","a93","a94","a95","a96","a97","a98","a99","a100","a101","a102","a103","a104","a105","a106","a107","a108","a109","a110","a111","a112","a113","a114","a115","a116","a117","a118","a119","a120","a121","a122","a123","a124","a125","a126","a127","a128","a129","a130","a131","a132","a133","a134","a135","a136","a137","a138","a139","a140","a141","a142","a143","a144","a145","a146","a147","a148","a149","a150","a151","a152","a153","a154","a155","a156","a157","a158","a159","a160","a161","a162","a163","a164","a165","a166","a167","a168","a169","a170","a171","a172","a173","a174","a175","a176","a177","a178","a179","a180","a181","a182","a183","a184","a185","a186","a187","a188","a189","a190","a191","a192","a193","a194","a195","a196","a197","a198","a199","a200","a201","a202","a203","a204","a205","a206","a207","a208","a209","a210","a211","a212","a213","a214","a215","a216","a217","a218","a219","a220","a221","a222","a223","a224","a225","a226","a227","a228","a229","a230","a231","a232","a233","a234","a235","a236","a237","a238","a239","a240","a241","a242","a243","a244","a245","a246","a247","a248","a249","a250","a251","a252","a253","a254","a255"
#define MAIN_ASM \
  \
".p2align 4\n" \
"s_load_dwordx16 s[16:31], %[ka], 0\n" \
"s_load_dwordx16 s[32:47], %[ka], 64\n" \
"s_load_dwordx16 s[48:63], %[ka], 128\n" \
"v_mbcnt_lo_u32_b32 v251, -1, 0\n" \
"v_mbcnt_hi_u32_b32 v251, -1, v251\n" \
"s_and_b32 s64, %[bid], 7\n" \
"s_lshr_b32 s65, %[bid], 3\n" \
"s_and_b32 s66, s65, 3\n" \
"s_lshr_b32 s67, %[bid], 5\n" \
"s_lshl2_add_u32 s68, s64, s66\n" \
"v_lshlrev_b32 v248, 4, v251\n" \
"v_lshlrev_b32 v0, 7, v251\n" \
"s_getpc_b64 s[96:97]\n" \
".Lpf0_%=:\n" \
"s_and_b32 s97, s97, 0xffff\n" \
"s_mov_b32 s98, .Lloop2_%=-.Lpf0_%=\n" \
"s_mov_b32 s99, 0x20000\n" \
"s_lshl2_add_u32 s74, s66, %[wid]\n" \
"s_lshl_b32 s74, s74, 13\n" \
"buffer_load_dword v245, v0, s[96:99], s74 offen\n" \
"v_bfe_u32 v1, v251, 2, 2\n" \
"v_bfe_u32 v2, v251, 4, 1\n" \
"v_lshl_add_u32 v1, v2, 3, v1\n" \
"v_lshlrev_b32 v1, 13, v1\n" \
"v_lshrrev_b32 v3, 5, v251\n" \
"v_lshl_or_b32 v1, v3, 6, v1\n" \
"v_and_b32 v4, 3, v251\n" \
"v_lshlrev_b32 v5, 1, v2\n" \
"v_xor_b32 v4, v4, v5\n" \
"v_lshl_or_b32 v246, v4, 4, v1\n" \
"v_lshrrev_b32 v6, 3, v251\n" \
"v_and_b32 v7, 7, v251\n" \
"v_lshrrev_b32 v8, 4, v251\n" \
"v_xor_b32 v9, v7, v8\n" \
"v_lshlrev_b32 v9, 4, v9\n" \
"v_lshl_or_b32 v249, v6, 10, v9\n" \
"v_xor_b32 v250, 64, v249\n" \
"s_sub_u32 s76, %[bid], 192\n" \
"s_min_u32 s76, s76, %[bid]\n" \
"s_mul_i32 s77, s76, 1366\n" \
"s_lshr_b32 s77, s77, 16\n" \
"s_mul_i32 s78, s77, 48\n" \
"s_sub_u32 s76, s76, s78\n" \
"s_lshl2_add_u32 s78, s77, %[wid]\n" \
"s_lshl_b32 s78, s78, 5\n" \
"v_lshl_add_u32 v10, v6, 2, s78\n" \
"v_lshlrev_b32 v11, 1, v10\n" \
"v_mul_u32_u24 v12, 0x1800, v10\n" \
"v_lshl_add_u32 v12, v7, 4, v12\n" \
"s_lshl_b32 s74, s76, 7\n" \
"s_add_u32 s74, s74, 3072\n" \
"v_add_u32 v12, s74, v12\n" \
"v_add_u32 v13, 0x3000, v12\n" \
"v_mov_b32 v14, 720896\n" \
"s_mul_i32 s81, s77, 12288\n" \
"s_lshl_b32 s74, s76, 8\n" \
"s_add_u32 s81, s81, s74\n" \
"s_add_u32 s81, s81, 917504\n" \
"s_lshl_b32 s74, %[wid], 8\n" \
"s_or_b32 s74, s74, %[bid]\n" \
"s_cmp_lt_u32 s74, 192\n" \
"s_cselect_b32 s81, s81, 1073741824\n" \
"s_lshr_b32 s74, %[wid], 1\n" \
"s_lshl_b32 s69, s74, 17\n" \
"s_and_b32 s74, %[wid], 1\n" \
"s_lshl_b32 s74, s74, 15\n" \
"s_add_u32 s69, s69, s74\n" \
"s_lshl_b32 s74, s68, 8\n" \
"s_add_u32 s69, s69, s74\n" \
"s_lshl_b32 s70, %[wid], 11\n" \
"s_add_u32 s70, s70, 65536\n" \
"s_lshl2_add_u32 s71, s67, %[wid]\n" \
"s_lshl_b32 s71, s71, 15\n" \
"s_lshl_b32 s72, %[wid], 14\n" \
"s_lshl_b32 s74, s67, 18\n" \
"s_sub_u32 s94, s71, s74\n" \
"s_add_u32 s94, s94, 1933312\n" \
"s_sub_u32 s74, 15, s67\n" \
"s_cmp_lt_u32 s68, 16\n" \
"s_cselect_b32 s74, s67, s74\n" \
"s_lshl_b32 s74, s74, 4\n" \
"s_bfe_u32 s76, s68, 0x20002\n" \
"s_sub_u32 s78, 7, s76\n" \
"s_bitcmp1_b32 s68, 1\n" \
"s_cselect_b32 s76, s78, s76\n" \
"s_lshl1_add_u32 s74, s76, s74\n" \
"s_and_b32 s76, s68, 1\n" \
"s_add_u32 s74, s74, s76\n" \
"s_lshl_b32 s78, s74, 3\n" \
"s_lshl1_add_u32 s74, %[wid], s78\n" \
"s_lshl_b32 s75, s74, 10\n" \
"s_waitcnt lgkmcnt(0)\n" \
"global_load_dwordx2 v[16:17], v11, s[40:41]\n" \
"global_load_dwordx4 v[20:23], v12, s[36:37] offset:-3072 nt\n" \
"global_load_dwordx4 v[24:27], v12, s[36:37] offset:3072 nt\n" \
"global_load_dwordx4 v[28:31], v13, s[36:37] offset:-3072 nt\n" \
"global_load_dwordx4 v[32:35], v13, s[36:37] offset:3072 nt\n" \
"s_add_u32 m0, s70, 0\n" \
"s_add_u32 s74, s69, 0\n" \
"buffer_load_dwordx4 v246, s[20:23], s74 offen lds\n" \
"s_add_u32 m0, s70, 1024\n" \
"s_add_u32 s74, s69, 128\n" \
"buffer_load_dwordx4 v246, s[20:23], s74 offen lds\n" \
"s_add_u32 m0, s72, 0\n" \
"s_add_u32 s74, s71, 0\n" \
"buffer_load_dwordx4 v249, s[16:19], s74 offen lds\n" \
"s_add_u32 m0, s72, 1024\n" \
"s_add_u32 s74, s71, 8192\n" \
"buffer_load_dwordx4 v250, s[16:19], s74 offen lds\n" \
"s_add_u32 m0, s72, 2048\n" \
"s_add_u32 s74, s71, 16384\n" \
"buffer_load_dwordx4 v249, s[16:19], s74 offen lds\n" \
"s_add_u32 m0, s72, 3072\n" \
"s_add_u32 s74, s71, 24576\n" \
"buffer_load_dwordx4 v250, s[16:19], s74 offen lds\n" \
"s_add_u32 m0, s72, 4096\n" \
"s_add_u32 s74, s94, 32768\n" \
"buffer_load_dwordx4 v249, s[16:19], s74 offen lds\n" \
"s_add_u32 m0, s72, 5120\n" \
"s_add_u32 s74, s94, 40960\n" \
"buffer_load_dwordx4 v250, s[16:19], s74 offen lds\n" \
"s_add_u32 m0, s72, 6144\n" \
"s_add_u32 s74, s94, 49152\n" \
"buffer_load_dwordx4 v249, s[16:19], s74 offen lds\n" \
"s_add_u32 m0, s72, 7168\n" \
"s_add_u32 s74, s94, 57344\n" \
"buffer_load_dwordx4 v250, s[16:19], s74 offen lds\n" \
"s_add_u32 m0, s70, 8192\n" \
"s_add_u32 s74, s69, 262144\n" \
"buffer_load_dwordx4 v246, s[20:23], s74 offen lds\n" \
"s_add_u32 m0, s70, 9216\n" \
"s_add_u32 s74, s69, 262272\n" \
"buffer_load_dwordx4 v246, s[20:23], s74 offen lds\n" \
"s_mov_b64 exec, 0\n" \
"s_cmp_eq_u32 %[wid], 0\n" \
"s_cbranch_scc1 .Lst0_0_%=\n" \
"s_cmp_eq_u32 %[wid], 1\n" \
"s_cbranch_scc1 .Lst0_1_%=\n" \
"s_cmp_eq_u32 %[wid], 2\n" \
"s_cbranch_scc1 .Lst0_2_%=\n" \
"s_branch .Lst0_3_%=\n" \
".Lstb0_%=:\n" \
"s_mov_b64 exec, -1\n" \
"v_mov_b32 v224, -1\n" \
"v_mov_b32 v225, -1\n" \
"s_min_u32 s74, %[bid], 223\n" \
"s_lshl_b32 s74, s74, 11\n" \
"s_lshl_b32 s76, %[wid], 9\n" \
"s_add_u32 s74, s74, s76\n" \
"s_add_u32 s74, s74, 98304\n" \
"v_lshl_add_u32 v228, v251, 3, s74\n" \
"s_lshl_b32 s74, %[wid], 8\n" \
"s_or_b32 s74, s74, %[bid]\n" \
"s_lshl_b32 s76, %[bid], 7\n" \
"s_add_u32 s76, s76, 65536\n" \
"s_cmp_lt_u32 s74, 256\n" \
"s_cselect_b32 s77, s76, 1073741824\n" \
"s_add_u32 s76, s76, 491392\n" \
"s_cmp_lt_u32 s74, 128\n" \
"s_cselect_b32 s76, s76, 1073741824\n" \
"v_mov_b32 v37, s77\n" \
"v_mov_b32 v38, s76\n" \
"v_cmp_gt_u32 vcc, 32, v251\n" \
"v_mov_b32 v230, 0\n" \
"s_lshl_b32 s77, %[wid], 16\n" \
"v_cndmask_b32 v37, v38, v37, vcc\n" \
"s_lshl_b32 s76, %[bid], 8\n" \
"s_add_u32 s76, s76, s77\n" \
"s_add_u32 s76, s76, 524288\n" \
"v_mov_b32 v38, s76\n" \
"s_sub_u32 s77, %[wid], 1\n" \
"s_cmp_lt_u32 s77, 2\n" \
"s_cselect_b64 vcc, -1, 0\n" \
"v_cndmask_b32 v37, v37, v38, vcc\n" \
"v_lshl_add_u32 v229, v251, 2, v37\n" \
"v_lshlrev_b32 v40, 12, v3\n" \
"v_lshl_or_b32 v40, v2, 8, v40\n" \
"v_bfe_u32 v41, v251, 2, 2\n" \
"v_lshl_or_b32 v40, v41, 6, v40\n" \
"v_lshl_or_b32 v40, v2, 5, v40\n" \
"v_and_b32 v41, 3, v251\n" \
"v_lshl_or_b32 v40, v41, 3, v40\n" \
"v_add_u32 v240, 65536, v40\n" \
"v_xor_b32 v241, 32, v240\n" \
"v_and_b32 v42, 15, v251\n" \
"v_bfe_u32 v43, v251, 1, 3\n" \
"v_xor_b32 v43, v43, v8\n" \
"v_lshlrev_b32 v43, 4, v43\n" \
"v_lshl_or_b32 v43, v42, 7, v43\n" \
"v_add_u32 v242, s72, v43\n" \
"v_xor_b32 v244, 64, v242\n" \
"s_mov_b32 s76, 0x05040100\n" \
"s_mov_b32 s77, 0x07060302\n" \
"s_add_u32 m0, s70, 16384\n" \
"s_add_u32 s74, s69, 0\n" \
"buffer_load_dwordx4 v246, s[24:27], s74 offen lds\n" \
"s_add_u32 m0, s70, 17408\n" \
"s_add_u32 s74, s69, 128\n" \
"buffer_load_dwordx4 v246, s[24:27], s74 offen lds\n" \
"s_waitcnt vmcnt(14)\n" \
"v_lshlrev_b32 v44, 16, v16\n" \
"v_and_b32 v45, 0xffff0000, v16\n" \
"v_lshlrev_b32 v46, 16, v17\n" \
"v_and_b32 v47, 0xffff0000, v17\n" \
"v_mul_f32 v48, 0xbfb8aa3b, v44\n" \
"v_mul_f32 v49, 0xbfb8aa3b, v45\n" \
"v_mul_f32 v50, 0xbfb8aa3b, v46\n" \
"v_mul_f32 v51, 0xbfb8aa3b, v47\n" \
"v_exp_f32 v48, v48\n" \
"v_exp_f32 v49, v49\n" \
"v_exp_f32 v50, v50\n" \
"v_exp_f32 v51, v51\n" \
"v_add_f32 v48, 1.0, v48\n" \
"v_add_f32 v49, 1.0, v49\n" \
"v_add_f32 v50, 1.0, v50\n" \
"v_add_f32 v51, 1.0, v51\n" \
"v_rcp_f32 v48, v48\n" \
"v_rcp_f32 v49, v49\n" \
"v_rcp_f32 v50, v50\n" \
"v_rcp_f32 v51, v51\n" \
"v_mul_f32 v44, v44, v48\n" \
"v_mul_f32 v45, v45, v49\n" \
"v_mul_f32 v46, v46, v50\n" \
"v_mul_f32 v47, v47, v51\n" \
"v_cvt_pk_bf16_f32 v44, v44, v45\n" \
"v_cvt_pk_bf16_f32 v45, v46, v47\n" \
"s_add_u32 m0, s70, 24576\n" \
"s_add_u32 s74, s69, 262144\n" \
"buffer_load_dwordx4 v246, s[24:27], s74 offen lds\n" \
"v_perm_b32 v64, v24, v20, s76\n" \
"v_perm_b32 v65, v24, v20, s77\n" \
"v_perm_b32 v66, v25, v21, s76\n" \
"v_perm_b32 v67, v25, v21, s77\n" \
"v_perm_b32 v68, v26, v22, s76\n" \
"v_perm_b32 v69, v26, v22, s77\n" \
"v_perm_b32 v70, v27, v23, s76\n" \
"v_perm_b32 v71, v27, v23, s77\n" \
"v_dot2_f32_bf16 v52, v64, v44, 0\n" \
"v_dot2_f32_bf16 v53, v65, v44, 0\n" \
"v_dot2_f32_bf16 v54, v66, v44, 0\n" \
"v_dot2_f32_bf16 v55, v67, v44, 0\n" \
"v_dot2_f32_bf16 v56, v68, v44, 0\n" \
"v_dot2_f32_bf16 v57, v69, v44, 0\n" \
"v_dot2_f32_bf16 v58, v70, v44, 0\n" \
"v_dot2_f32_bf16 v59, v71, v44, 0\n" \
"s_add_u32 m0, s70, 25600\n" \
"s_add_u32 s74, s69, 262272\n" \
"buffer_load_dwordx4 v246, s[24:27], s74 offen lds\n" \
"v_perm_b32 v64, v32, v28, s76\n" \
"v_perm_b32 v65, v32, v28, s77\n" \
"v_perm_b32 v66, v33, v29, s76\n" \
"v_perm_b32 v67, v33, v29, s77\n" \
"v_perm_b32 v68, v34, v30, s76\n" \
"v_perm_b32 v69, v34, v30, s77\n" \
"v_perm_b32 v70, v35, v31, s76\n" \
"v_perm_b32 v71, v35, v31, s77\n" \
"v_dot2_f32_bf16 v52, v64, v45, v52\n" \
"v_dot2_f32_bf16 v53, v65, v45, v53\n" \
"v_dot2_f32_bf16 v54, v66, v45, v54\n" \
"v_dot2_f32_bf16 v55, v67, v45, v55\n" \
"v_dot2_f32_bf16 v56, v68, v45, v56\n" \
"v_dot2_f32_bf16 v57, v69, v45, v57\n" \
"v_dot2_f32_bf16 v58, v70, v45, v58\n" \
"v_dot2_f32_bf16 v59, v71, v45, v59\n" \
"buffer_load_dwordx2 v[202:203], v14, s[32:35], 0 offen sc1\n" \
"s_nop 2\n" \
"v_permlane32_swap_b32 v52, v56\n" \
"v_permlane32_swap_b32 v53, v57\n" \
"v_permlane32_swap_b32 v54, v58\n" \
"v_permlane32_swap_b32 v55, v59\n" \
"v_add_f32 v52, v52, v56\n" \
"v_add_f32 v53, v53, v57\n" \
"v_add_f32 v54, v54, v58\n" \
"v_add_f32 v55, v55, v59\n" \
"s_nop 0\n" \
"v_permlane16_swap_b32 v52, v54\n" \
"v_permlane16_swap_b32 v53, v55\n" \
"v_add_f32 v52, v52, v54\n" \
"v_add_f32 v53, v53, v55\n" \
"s_lshl_b32 s74, %[wid], 8\n" \
"s_add_u32 s74, s74, 147456\n" \
"v_lshl_add_u32 v60, v7, 5, s74\n" \
"v_lshl_add_u32 v60, v8, 3, v60\n" \
"v_lshlrev_b32 v231, 2, v251\n" \
"v_add_u32 v62, 147456, v231\n" \
"v_add_f32_dpp v52, v52, v52 row_ror:8 row_mask:0xf bank_mask:0xf\n" \
"v_add_f32_dpp v53, v53, v53 row_ror:8 row_mask:0xf bank_mask:0xf\n" \
"ds_write_b64 v60, v[52:53]\n" \
"buffer_load_dwordx4 v[128:131], v248, s[44:47], s75 offen\n" \
"buffer_load_dwordx4 v[132:135], v248, s[44:47], s75 offen offset:1024\n" \
"s_waitcnt vmcnt(7) lgkmcnt(0)\n" \
"s_barrier\n" \
"ds_read2st64_b32 v[232:233], v62 offset1:1\n" \
"ds_read2st64_b32 v[234:235], v62 offset0:2 offset1:3\n" \
"ds_read_b64_tr_b16 a[192:193], v240 offset:0\n" \
"ds_read_b64_tr_b16 a[194:195], v240 offset:2048\n" \
"ds_read_b64_tr_b16 a[196:197], v240 offset:8192\n" \
"ds_read_b64_tr_b16 a[198:199], v240 offset:10240\n" \
"ds_read_b64_tr_b16 a[200:201], v241 offset:0\n" \
"ds_read_b64_tr_b16 a[202:203], v241 offset:2048\n" \
"ds_read_b64_tr_b16 a[204:205], v241 offset:8192\n" \
"ds_read_b64_tr_b16 a[206:207], v241 offset:10240\n" \
"ds_read_b64_tr_b16 a[208:209], v240 offset:512\n" \
"ds_read_b64_tr_b16 a[210:211], v240 offset:2560\n" \
"ds_read_b64_tr_b16 a[212:213], v240 offset:8704\n" \
"ds_read_b64_tr_b16 a[214:215], v240 offset:10752\n" \
"ds_read_b128 a[128:131], v242 offset:0\n" \
"ds_read_b128 a[132:135], v242 offset:2048\n" \
"ds_read_b128 a[136:139], v242 offset:4096\n" \
"ds_read_b128 a[140:143], v242 offset:6144\n" \
"ds_read_b128 a[144:147], v244 offset:0\n" \
"ds_read_b128 a[148:151], v244 offset:2048\n" \
"ds_read_b128 a[152:155], v244 offset:4096\n" \
"ds_read_b128 a[156:159], v244 offset:6144\n" \
"s_waitcnt lgkmcnt(0)\n" \
  \
"v_mfma_f32_16x16x32_bf16 v[0:3], a[192:195], a[128:131], 0\n" \
"s_add_u32 m0, s72, 8192\n" \
"ds_read_b64_tr_b16 a[216:217], v241 offset:512\n" \
"v_add_f32 v232, v232, v233\n" \
"v_mfma_f32_16x16x32_bf16 v[0:3], a[196:199], a[144:147], v[0:3]\n" \
"s_add_u32 s74, s71, 128\n" \
"buffer_load_dwordx4 v249, s[16:19], s74 offen lds\n" \
"v_mfma_f32_16x16x32_bf16 v[32:35], a[196:199], a[148:151], 0\n" \
"s_add_u32 m0, s72, 9216\n" \
"ds_read_b64_tr_b16 a[218:219], v241 offset:2560\n" \
"v_add_f32 v234, v234, v235\n" \
"v_mfma_f32_16x16x32_bf16 v[32:35], a[192:195], a[132:135], v[32:35]\n" \
"s_add_u32 s74, s71, 8320\n" \
"v_add_f32 v232, v232, v234\n" \
"v_mfma_f32_16x16x32_bf16 v[64:67], a[192:195], a[136:139], 0\n" \
"buffer_load_dwordx4 v250, s[16:19], s74 offen lds\n" \
"v_mfma_f32_16x16x32_bf16 v[64:67], a[196:199], a[152:155], v[64:67]\n" \
"s_add_u32 m0, s72, 10240\n" \
"buffer_store_dword v232, v231, s[32:35], s81 offen sc1\n" \
"v_mov_b32 v239, 1\n" \
"v_mfma_f32_16x16x32_bf16 v[96:99], a[196:199], a[156:159], 0\n" \
"s_add_u32 s74, s71, 16512\n" \
"buffer_store_dwordx2 v[224:225], v228, s[32:35], 0 offen sc1\n" \
"v_mfma_f32_16x16x32_bf16 v[96:99], a[192:195], a[140:143], v[96:99]\n" \
"ds_read_b64_tr_b16 a[220:221], v241 offset:8704\n" \
"ds_read_b64_tr_b16 a[222:223], v241 offset:10752\n" \
"v_mfma_f32_16x16x32_bf16 v[4:7], a[200:203], a[128:131], 0\n" \
"buffer_store_dword v230, v229, s[32:35], 0 offen sc1\n" \
"v_bfe_u32 v210, v251, 1, 3\n" \
"v_mfma_f32_16x16x32_bf16 v[4:7], a[204:207], a[144:147], v[4:7]\n" \
"ds_read_b64_tr_b16 a[192:193], v240 offset:1024\n" \
"v_lshlrev_b32 v210, 13, v210\n" \
"v_mfma_f32_16x16x32_bf16 v[36:39], a[204:207], a[148:151], 0\n" \
"ds_read_b64_tr_b16 a[194:195], v240 offset:3072\n" \
"v_and_b32 v208, 1, v251\n" \
"v_mfma_f32_16x16x32_bf16 v[36:39], a[200:203], a[132:135], v[36:39]\n" \
"buffer_load_dwordx4 v249, s[16:19], s74 offen lds\n" \
"v_mfma_f32_16x16x32_bf16 v[68:71], a[200:203], a[136:139], 0\n" \
"s_add_u32 m0, s72, 11264\n" \
"ds_read_b64_tr_b16 a[196:197], v240 offset:9216\n" \
"v_lshl_or_b32 v208, v208, 7, v210\n" \
"v_mfma_f32_16x16x32_bf16 v[68:71], a[204:207], a[152:155], v[68:71]\n" \
"s_add_u32 s74, s71, 24704\n" \
"v_and_b32 v209, 15, v251\n" \
"v_lshlrev_b32 v209, 7, v209\n" \
"v_mfma_f32_16x16x32_bf16 v[100:103], a[204:207], a[156:159], 0\n" \
"buffer_load_dwordx4 v250, s[16:19], s74 offen lds\n" \
"v_mfma_f32_16x16x32_bf16 v[100:103], a[200:203], a[140:143], v[100:103]\n" \
"s_add_u32 m0, s72, 12288\n" \
"ds_read_b64_tr_b16 a[198:199], v240 offset:11264\n" \
"v_and_b32 v211, 0x7fff, v208\n" \
"v_mfma_f32_16x16x32_bf16 v[8:11], a[208:211], a[128:131], 0\n" \
"s_add_u32 s74, s94, 32896\n" \
"ds_read_b64_tr_b16 a[200:201], v241 offset:1024\n" \
"v_mfma_f32_16x16x32_bf16 v[8:11], a[212:215], a[144:147], v[8:11]\n" \
"buffer_load_dwordx4 v249, s[16:19], s74 offen lds\n" \
"v_mfma_f32_16x16x32_bf16 v[40:43], a[212:215], a[148:151], 0\n" \
"s_add_u32 m0, s72, 13312\n" \
"s_add_u32 s74, s94, 41088\n" \
"ds_read_b64_tr_b16 a[202:203], v241 offset:3072\n" \
"v_mfma_f32_16x16x32_bf16 v[40:43], a[208:211], a[132:135], v[40:43]\n" \
"v_mfma_f32_16x16x32_bf16 v[72:75], a[208:211], a[136:139], 0\n" \
"buffer_load_dwordx4 v250, s[16:19], s74 offen lds\n" \
"v_mfma_f32_16x16x32_bf16 v[72:75], a[212:215], a[152:155], v[72:75]\n" \
"s_add_u32 m0, s72, 14336\n" \
"s_add_u32 s74, s94, 49280\n" \
"ds_read_b64_tr_b16 a[204:205], v241 offset:9216\n" \
"v_mfma_f32_16x16x32_bf16 v[104:107], a[212:215], a[156:159], 0\n" \
"ds_read_b64_tr_b16 a[206:207], v241 offset:11264\n" \
"v_mfma_f32_16x16x32_bf16 v[104:107], a[208:211], a[140:143], v[104:107]\n" \
"s_waitcnt lgkmcnt(8)\n" \
"buffer_load_dwordx4 v249, s[16:19], s74 offen lds\n" \
"v_mfma_f32_16x16x32_bf16 v[12:15], a[216:219], a[128:131], 0\n" \
"s_add_u32 m0, s72, 15360\n" \
"s_add_u32 s74, s94, 57472\n" \
"ds_read_b64_tr_b16 a[208:209], v240 offset:1536\n" \
"v_mfma_f32_16x16x32_bf16 v[12:15], a[220:223], a[144:147], v[12:15]\n" \
"v_mfma_f32_16x16x32_bf16 v[44:47], a[220:223], a[148:151], 0\n" \
"buffer_load_dwordx4 v250, s[16:19], s74 offen lds\n" \
"v_mfma_f32_16x16x32_bf16 v[44:47], a[216:219], a[132:135], v[44:47]\n" \
"s_add_u32 m0, s70, 32768\n" \
"s_add_u32 s74, s69, 524288\n" \
"ds_read_b64_tr_b16 a[210:211], v240 offset:3584\n" \
"v_mfma_f32_16x16x32_bf16 v[76:79], a[216:219], a[136:139], 0\n" \
"ds_read_b64_tr_b16 a[212:213], v240 offset:9728\n" \
"v_mfma_f32_16x16x32_bf16 v[76:79], a[220:223], a[152:155], v[76:79]\n" \
"buffer_load_dwordx4 v246, s[20:23], s74 offen lds\n" \
"v_mfma_f32_16x16x32_bf16 v[108:111], a[220:223], a[156:159], 0\n" \
"s_add_u32 m0, s70, 33792\n" \
"s_add_u32 s74, s69, 524416\n" \
"ds_read_b64_tr_b16 a[214:215], v240 offset:11776\n" \
"v_mfma_f32_16x16x32_bf16 v[108:111], a[216:219], a[140:143], v[108:111]\n" \
"s_waitcnt lgkmcnt(8)\n" \
"v_mfma_f32_16x16x32_bf16 v[16:19], a[192:195], a[128:131], 0\n" \
"buffer_load_dwordx4 v246, s[20:23], s74 offen lds\n" \
"v_mfma_f32_16x16x32_bf16 v[16:19], a[196:199], a[144:147], v[16:19]\n" \
"s_add_u32 m0, s70, 40960\n" \
"s_add_u32 s74, s69, 786432\n" \
"ds_read_b64_tr_b16 a[216:217], v241 offset:1536\n" \
"v_mfma_f32_16x16x32_bf16 v[48:51], a[196:199], a[148:151], 0\n" \
"ds_read_b64_tr_b16 a[218:219], v241 offset:3584\n" \
"v_mfma_f32_16x16x32_bf16 v[48:51], a[192:195], a[132:135], v[48:51]\n" \
"buffer_load_dwordx4 v246, s[20:23], s74 offen lds\n" \
"v_mfma_f32_16x16x32_bf16 v[80:83], a[192:195], a[136:139], 0\n" \
"s_add_u32 m0, s70, 41984\n" \
"ds_read_b64_tr_b16 a[220:221], v241 offset:9728\n" \
"ds_read_b64_tr_b16 a[222:223], v241 offset:11776\n" \
"v_mfma_f32_16x16x32_bf16 v[80:83], a[196:199], a[152:155], v[80:83]\n" \
"s_add_u32 s74, s69, 786560\n" \
"v_mfma_f32_16x16x32_bf16 v[112:115], a[196:199], a[156:159], 0\n" \
"buffer_load_dwordx4 v246, s[20:23], s74 offen lds\n" \
"v_mfma_f32_16x16x32_bf16 v[112:115], a[192:195], a[140:143], v[112:115]\n" \
"s_waitcnt lgkmcnt(8)\n" \
"s_add_u32 m0, s70, 49152\n" \
"s_add_u32 s74, s69, 524288\n" \
"v_mfma_f32_16x16x32_bf16 v[20:23], a[200:203], a[128:131], 0\n" \
"s_mov_b32 s77, 0x7f4a7c15\n" \
"v_mov_b32 v253, 0x9e3779b9\n" \
"v_mfma_f32_16x16x32_bf16 v[20:23], a[204:207], a[144:147], v[20:23]\n" \
"buffer_load_dwordx4 v246, s[24:27], s74 offen lds\n" \
"v_mfma_f32_16x16x32_bf16 v[52:55], a[204:207], a[148:151], 0\n" \
"s_waitcnt vmcnt(18)\n" \
"s_add_u32 m0, s70, 50176\n" \
"v_add_co_u32 v202, vcc, s77, v202\n" \
"v_mfma_f32_16x16x32_bf16 v[52:55], a[200:203], a[132:135], v[52:55]\n" \
"s_waitcnt vmcnt(16)\n" \
"s_add_u32 s74, s69, 524416\n" \
"v_lshlrev_b32 v136, 16, v128\n" \
"v_mfma_f32_16x16x32_bf16 v[84:87], a[200:203], a[136:139], 0\n" \
"s_waitcnt vmcnt(11) lgkmcnt(0)\n" \
"s_barrier\n" \
"v_addc_co_u32 v203, vcc, v253, v203, vcc\n" \
"v_mfma_f32_16x16x32_bf16 v[84:87], a[204:207], a[152:155], v[84:87]\n" \
"s_lshl_b32 s85, %[bid], 3\n" \
"buffer_load_dwordx4 v246, s[24:27], s74 offen lds\n" \
"v_mfma_f32_16x16x32_bf16 v[116:119], a[204:207], a[156:159], 0\n" \
"s_add_u32 m0, s70, 57344\n" \
"ds_read_b64_tr_b16 a[192:193], v240 offset:16384\n" \
"ds_read_b64_tr_b16 a[194:195], v240 offset:18432\n" \
"v_mfma_f32_16x16x32_bf16 v[116:119], a[200:203], a[140:143], v[116:119]\n" \
"s_add_u32 s74, s69, 786432\n" \
"ds_read_b64_tr_b16 a[196:197], v240 offset:24576\n" \
"ds_read_b64_tr_b16 a[198:199], v240 offset:26624\n" \
"v_mfma_f32_16x16x32_bf16 v[24:27], a[208:211], a[128:131], 0\n" \
"s_add_u32 s85, s85, 573440\n" \
"buffer_load_dwordx4 v246, s[24:27], s74 offen lds\n" \
"v_mfma_f32_16x16x32_bf16 v[24:27], a[212:215], a[144:147], v[24:27]\n" \
"s_add_u32 m0, s70, 58368\n" \
"s_cmp_eq_u32 %[wid], 0\n" \
"s_cselect_b32 s85, s85, 1073741824\n" \
"v_mfma_f32_16x16x32_bf16 v[56:59], a[212:215], a[148:151], 0\n" \
"s_add_u32 s74, s69, 786560\n" \
"ds_read_b64_tr_b16 a[200:201], v241 offset:16384\n" \
"ds_read_b64_tr_b16 a[202:203], v241 offset:18432\n" \
"v_mfma_f32_16x16x32_bf16 v[56:59], a[208:211], a[132:135], v[56:59]\n" \
"buffer_load_dwordx4 v246, s[24:27], s74 offen lds\n" \
"v_mfma_f32_16x16x32_bf16 v[88:91], a[208:211], a[136:139], 0\n" \
"s_add_u32 m0, s72, 0\n" \
"ds_read_b64_tr_b16 a[204:205], v241 offset:24576\n" \
"v_and_b32 v252, 7, v251\n" \
"v_mfma_f32_16x16x32_bf16 v[88:91], a[212:215], a[152:155], v[88:91]\n" \
"s_add_u32 s74, s71, 256\n" \
"v_lshlrev_b32 v252, 11, v252\n" \
"v_and_b32 v137, 0xffff0000, v128\n" \
"v_mfma_f32_16x16x32_bf16 v[120:123], a[212:215], a[156:159], 0\n" \
"buffer_load_dwordx4 v249, s[16:19], s74 offen lds\n" \
"v_mfma_f32_16x16x32_bf16 v[120:123], a[208:211], a[140:143], v[120:123]\n" \
"s_add_u32 m0, s72, 1024\n" \
"buffer_store_dwordx2 v[202:203], v252, s[32:35], s85 offen sc1\n" \
"v_lshrrev_b32 v253, 1, v248\n" \
"v_mfma_f32_16x16x32_bf16 v[28:31], a[216:219], a[128:131], 0\n" \
"s_add_u32 s74, s71, 8448\n" \
"ds_read_b64_tr_b16 a[206:207], v241 offset:26624\n" \
"ds_read_b64_tr_b16 a[208:209], v240 offset:16896\n" \
"v_mfma_f32_16x16x32_bf16 v[28:31], a[220:223], a[144:147], v[28:31]\n" \
"v_lshlrev_b32 v138, 16, v129\n" \
"v_and_b32 v139, 0xffff0000, v129\n" \
"v_mfma_f32_16x16x32_bf16 v[60:63], a[220:223], a[148:151], 0\n" \
"buffer_load_dwordx4 v250, s[16:19], s74 offen lds\n" \
"v_mfma_f32_16x16x32_bf16 v[60:63], a[216:219], a[132:135], v[60:63]\n" \
"s_add_u32 m0, s72, 2048\n" \
"ds_read_b64_tr_b16 a[210:211], v240 offset:18944\n" \
"v_lshlrev_b32 v140, 16, v130\n" \
"v_mfma_f32_16x16x32_bf16 v[92:95], a[216:219], a[136:139], 0\n" \
"s_add_u32 s74, s71, 16640\n" \
"ds_read_b64_tr_b16 a[212:213], v240 offset:25088\n" \
"v_and_b32 v141, 0xffff0000, v130\n" \
"v_mfma_f32_16x16x32_bf16 v[92:95], a[220:223], a[152:155], v[92:95]\n" \
"buffer_load_dwordx4 v249, s[16:19], s74 offen lds\n" \
"v_mfma_f32_16x16x32_bf16 v[124:127], a[220:223], a[156:159], 0\n" \
"s_add_u32 m0, s72, 3072\n" \
"ds_read_b64_tr_b16 a[214:215], v240 offset:27136\n" \
"v_lshlrev_b32 v142, 16, v131\n" \
"v_mfma_f32_16x16x32_bf16 v[124:127], a[216:219], a[140:143], v[124:127]\n" \
"s_waitcnt lgkmcnt(8)\n" \
"v_and_b32 v143, 0xffff0000, v131\n" \
"v_lshlrev_b32 v144, 16, v132\n" \
"v_mfma_f32_16x16x32_bf16 a[0:3], a[128:131], a[192:195], 0\n" \
"s_add_u32 s74, s71, 24832\n" \
"buffer_load_dwordx4 v250, s[16:19], s74 offen lds\n" \
"v_mfma_f32_16x16x32_bf16 a[0:3], a[144:147], a[196:199], a[0:3]\n" \
"s_add_u32 m0, s72, 4096\n" \
"ds_read_b64_tr_b16 a[216:217], v241 offset:16896\n" \
"v_and_b32 v145, 0xffff0000, v132\n" \
"v_mfma_f32_16x16x32_bf16 a[4:7], a[148:151], a[196:199], 0\n" \
"s_add_u32 s74, s94, 33024\n" \
"ds_read_b64_tr_b16 a[218:219], v241 offset:18944\n" \
"v_lshlrev_b32 v146, 16, v133\n" \
"v_mfma_f32_16x16x32_bf16 a[4:7], a[132:135], a[192:195], a[4:7]\n" \
"buffer_load_dwordx4 v249, s[16:19], s74 offen lds\n" \
"v_mfma_f32_16x16x32_bf16 a[64:67], a[136:139], a[192:195], 0\n" \
"s_add_u32 m0, s70, 0\n" \
"ds_read_b64_tr_b16 a[220:221], v241 offset:25088\n" \
"v_and_b32 v147, 0xffff0000, v133\n" \
"v_mfma_f32_16x16x32_bf16 a[64:67], a[152:155], a[196:199], a[64:67]\n" \
"s_add_u32 s74, s69, 1048576\n" \
"v_lshlrev_b32 v148, 16, v134\n" \
"v_and_b32 v149, 0xffff0000, v134\n" \
"v_mfma_f32_16x16x32_bf16 a[68:71], a[156:159], a[196:199], 0\n" \
"buffer_load_dwordx4 v246, s[20:23], s74 offen lds\n" \
"v_mfma_f32_16x16x32_bf16 a[68:71], a[140:143], a[192:195], a[68:71]\n" \
"s_waitcnt lgkmcnt(7)\n" \
"ds_read_b64_tr_b16 a[222:223], v241 offset:27136\n" \
"v_lshlrev_b32 v150, 16, v135\n" \
"v_mfma_f32_16x16x32_bf16 a[8:11], a[128:131], a[200:203], 0\n" \
"s_add_u32 m0, s72, 5120\n" \
"ds_read_b64_tr_b16 a[192:193], v240 offset:17408\n" \
"v_and_b32 v151, 0xffff0000, v135\n" \
"v_mfma_f32_16x16x32_bf16 a[8:11], a[144:147], a[204:207], a[8:11]\n" \
"s_add_u32 s74, s94, 41216\n" \
"buffer_load_dwordx4 v250, s[16:19], s74 offen lds\n" \
"v_mfma_f32_16x16x32_bf16 a[12:15], a[148:151], a[204:207], 0\n" \
"s_add_u32 m0, s72, 6144\n" \
"ds_read_b64_tr_b16 a[194:195], v240 offset:19456\n" \
"v_add_f32 v152, v136, v137\n" \
"v_mfma_f32_16x16x32_bf16 a[12:15], a[132:135], a[200:203], a[12:15]\n" \
"s_add_u32 s74, s94, 49408\n" \
"v_add_f32 v154, v138, v139\n" \
"v_add_f32 v153, v144, v145\n" \
"v_mfma_f32_16x16x32_bf16 a[72:75], a[136:139], a[200:203], 0\n" \
"buffer_load_dwordx4 v249, s[16:19], s74 offen lds\n" \
"v_mfma_f32_16x16x32_bf16 a[72:75], a[152:155], a[204:207], a[72:75]\n" \
"s_add_u32 m0, s70, 1024\n" \
"ds_read_b64_tr_b16 a[196:197], v240 offset:25600\n" \
"v_add_f32 v155, v146, v147\n" \
"v_mfma_f32_16x16x32_bf16 a[76:79], a[156:159], a[204:207], 0\n" \
"s_add_u32 s74, s69, 1048704\n" \
"ds_read_b64_tr_b16 a[198:199], v240 offset:27648\n" \
"v_add_f32 v152, v152, v140\n" \
"v_mfma_f32_16x16x32_bf16 a[76:79], a[140:143], a[200:203], a[76:79]\n" \
"s_waitcnt lgkmcnt(8)\n" \
"buffer_load_dwordx4 v246, s[20:23], s74 offen lds\n" \
"v_mfma_f32_16x16x32_bf16 a[16:19], a[128:131], a[208:211], 0\n" \
"s_add_u32 m0, s72, 7168\n" \
"ds_read_b64_tr_b16 a[200:201], v241 offset:17408\n" \
"v_add_f32 v154, v154, v141\n" \
"v_mfma_f32_16x16x32_bf16 a[16:19], a[144:147], a[212:215], a[16:19]\n" \
"s_add_u32 s74, s94, 57600\n" \
"v_add_f32 v153, v153, v148\n" \
"v_add_f32 v155, v155, v149\n" \
"v_mfma_f32_16x16x32_bf16 a[20:23], a[148:151], a[212:215], 0\n" \
"buffer_load_dwordx4 v250, s[16:19], s74 offen lds\n" \
"v_mfma_f32_16x16x32_bf16 a[20:23], a[132:135], a[208:211], a[20:23]\n" \
"s_add_u32 m0, s70, 8192\n" \
"ds_read_b64_tr_b16 a[202:203], v241 offset:19456\n" \
"v_add_f32 v152, v152, v142\n" \
"v_mfma_f32_16x16x32_bf16 a[80:83], a[136:139], a[208:211], 0\n" \
"s_add_u32 s74, s69, 1310720\n" \
"ds_read_b64_tr_b16 a[204:205], v241 offset:25600\n" \
"v_add_f32 v154, v154, v143\n" \
"v_mfma_f32_16x16x32_bf16 a[80:83], a[152:155], a[212:215], a[80:83]\n" \
"buffer_load_dwordx4 v246, s[20:23], s74 offen lds\n" \
"v_mfma_f32_16x16x32_bf16 a[84:87], a[156:159], a[212:215], 0\n" \
"s_add_u32 m0, s70, 9216\n" \
"ds_read_b64_tr_b16 a[206:207], v241 offset:27648\n" \
"v_add_f32 v153, v153, v150\n" \
"v_mfma_f32_16x16x32_bf16 a[84:87], a[140:143], a[208:211], a[84:87]\n" \
"s_waitcnt lgkmcnt(8)\n" \
"v_add_f32 v155, v155, v151\n" \
"v_add_f32 v152, v152, v154\n" \
"v_mfma_f32_16x16x32_bf16 a[24:27], a[128:131], a[216:219], 0\n" \
"s_add_u32 s74, s69, 1310848\n" \
"buffer_load_dwordx4 v246, s[20:23], s74 offen lds\n" \
"v_mfma_f32_16x16x32_bf16 a[24:27], a[144:147], a[220:223], a[24:27]\n" \
"ds_read_b64_tr_b16 a[208:209], v240 offset:17920\n" \
"v_add_f32 v153, v153, v155\n" \
"v_mfma_f32_16x16x32_bf16 a[28:31], a[148:151], a[220:223], 0\n" \
"ds_read_b64_tr_b16 a[210:211], v240 offset:19968\n" \
"v_add_f32_dpp v152, v152, v152 quad_perm:[1,0,3,2] row_mask:0xf bank_mask:0xf\n" \
"v_mfma_f32_16x16x32_bf16 a[28:31], a[132:135], a[216:219], a[28:31]\n" \
"v_add_f32_dpp v153, v153, v153 quad_perm:[1,0,3,2] row_mask:0xf bank_mask:0xf\n" \
"v_mfma_f32_16x16x32_bf16 a[88:91], a[136:139], a[216:219], 0\n" \
"ds_read_b64_tr_b16 a[212:213], v240 offset:26112\n" \
"v_add_f32_dpp v152, v152, v152 quad_perm:[2,3,0,1] row_mask:0xf bank_mask:0xf\n" \
"v_mfma_f32_16x16x32_bf16 a[88:91], a[152:155], a[220:223], a[88:91]\n" \
".Lst0_0_%=:\n" \
"s_cbranch_execz .Lst0_4_%=\n" \
"v_add_f32_dpp v153, v153, v153 quad_perm:[2,3,0,1] row_mask:0xf bank_mask:0xf\n" \
"v_mfma_f32_16x16x32_bf16 a[92:95], a[156:159], a[220:223], 0\n" \
"ds_read_b64_tr_b16 a[214:215], v240 offset:28160\n" \
"v_add_f32_dpp v152, v152, v152 row_half_mirror row_mask:0xf bank_mask:0xf\n" \
"v_mfma_f32_16x16x32_bf16 a[92:95], a[140:143], a[216:219], a[92:95]\n" \
"s_waitcnt lgkmcnt(8)\n" \
"v_add_f32_dpp v153, v153, v153 row_half_mirror row_mask:0xf bank_mask:0xf\n" \
"v_mfma_f32_16x16x32_bf16 a[32:35], a[128:131], a[192:195], 0\n" \
"s_waitcnt vmcnt(21)\n" \
"ds_read_b64_tr_b16 a[216:217], v241 offset:17920\n" \
"ds_read_b128 a[160:163], v242 offset:8192\n" \
"v_mfma_f32_16x16x32_bf16 a[32:35], a[144:147], a[196:199], a[32:35]\n" \
"v_add_f32_dpp v152, v152, v152 row_mirror row_mask:0xf bank_mask:0xf\n" \
"v_add_f32_dpp v153, v153, v153 row_mirror row_mask:0xf bank_mask:0xf\n" \
"v_mfma_f32_16x16x32_bf16 a[36:39], a[148:151], a[196:199], 0\n" \
"ds_read_b64_tr_b16 a[218:219], v241 offset:19968\n" \
"ds_read_b128 a[164:167], v242 offset:10240\n" \
"v_mfma_f32_16x16x32_bf16 a[36:39], a[132:135], a[192:195], a[36:39]\n" \
"ds_read_b64_tr_b16 a[220:221], v241 offset:26112\n" \
"v_mov_b32 v154, v152\n" \
"v_mfma_f32_16x16x32_bf16 a[96:99], a[136:139], a[192:195], 0\n" \
"ds_read_b64_tr_b16 a[222:223], v241 offset:28160\n" \
"ds_read_b128 a[168:171], v242 offset:12288\n" \
"v_mfma_f32_16x16x32_bf16 a[96:99], a[152:155], a[196:199], a[96:99]\n" \
"v_mov_b32 v155, v153\n" \
"v_mfma_f32_16x16x32_bf16 a[100:103], a[156:159], a[196:199], 0\n" \
"ds_read_b128 a[172:175], v242 offset:14336\n" \
"v_mfma_f32_16x16x32_bf16 a[100:103], a[140:143], a[192:195], a[100:103]\n" \
"s_waitcnt lgkmcnt(12)\n" \
"v_permlane16_swap_b32 v152, v154\n" \
"v_mfma_f32_16x16x32_bf16 a[40:43], a[128:131], a[200:203], 0\n" \
"v_permlane16_swap_b32 v153, v155\n" \
"v_mfma_f32_16x16x32_bf16 a[40:43], a[144:147], a[204:207], a[40:43]\n" \
"v_add_f32 v152, v152, v154\n" \
"v_add_f32 v153, v153, v155\n" \
"v_mfma_f32_16x16x32_bf16 a[44:47], a[148:151], a[204:207], 0\n" \
"v_mov_b32 v154, v152\n" \
"v_mov_b32 v155, v153\n" \
"v_mfma_f32_16x16x32_bf16 a[44:47], a[132:135], a[200:203], a[44:47]\n" \
"v_mfma_f32_16x16x32_bf16 a[104:107], a[136:139], a[200:203], 0\n" \
"s_waitcnt vmcnt(17) lgkmcnt(2)\n" \
"s_barrier\n" \
"ds_read_b128 a[176:179], v244 offset:8192\n" \
"v_mfma_f32_16x16x32_bf16 a[104:107], a[152:155], a[204:207], a[104:107]\n" \
"s_add_u32 m0, s70, 16384\n" \
"ds_read_b64_tr_b16 a[192:193], v240 offset:32768\n" \
"ds_read_b64_tr_b16 a[194:195], v240 offset:34816\n" \
"v_mfma_f32_16x16x32_bf16 a[108:111], a[156:159], a[204:207], 0\n" \
"s_add_u32 s74, s69, 1048576\n" \
"buffer_load_dwordx4 v246, s[24:27], s74 offen lds\n" \
"v_mfma_f32_16x16x32_bf16 a[108:111], a[140:143], a[200:203], a[108:111]\n" \
"ds_read_b64_tr_b16 a[196:197], v240 offset:40960\n" \
"ds_read_b64_tr_b16 a[198:199], v240 offset:43008\n" \
"v_mfma_f32_16x16x32_bf16 a[48:51], a[128:131], a[208:211], 0\n" \
"ds_read_b64_tr_b16 a[200:201], v241 offset:32768\n" \
"ds_read_b128 a[180:183], v244 offset:10240\n" \
"v_mfma_f32_16x16x32_bf16 a[48:51], a[144:147], a[212:215], a[48:51]\n" \
"ds_read_b128 a[184:187], v244 offset:12288\n" \
"v_mfma_f32_16x16x32_bf16 a[52:55], a[148:151], a[212:215], 0\n" \
"ds_read_b64_tr_b16 a[202:203], v241 offset:34816\n" \
"ds_read_b128 a[188:191], v244 offset:14336\n" \
"v_mfma_f32_16x16x32_bf16 a[52:55], a[132:135], a[208:211], a[52:55]\n" \
"v_permlane32_swap_b32 v152, v154\n" \
"v_mfma_f32_16x16x32_bf16 a[112:115], a[136:139], a[208:211], 0\n" \
"ds_read_b64_tr_b16 a[204:205], v241 offset:40960\n" \
"v_mfma_f32_16x16x32_bf16 a[112:115], a[152:155], a[212:215], a[112:115]\n" \
"v_permlane32_swap_b32 v153, v155\n" \
"v_mfma_f32_16x16x32_bf16 a[116:119], a[156:159], a[212:215], 0\n" \
"ds_read_b64_tr_b16 a[206:207], v241 offset:43008\n" \
"v_add_f32 v152, v152, v154\n" \
"v_mfma_f32_16x16x32_bf16 a[116:119], a[140:143], a[208:211], a[116:119]\n" \
"v_add_f32 v153, v153, v155\n" \
"v_mul_f32 v152, 0x3b000000, v152\n" \
"v_mfma_f32_16x16x32_bf16 a[56:59], a[128:131], a[216:219], 0\n" \
"ds_read_b64_tr_b16 a[208:209], v240 offset:33280\n" \
"v_mul_f32 v153, 0x3b000000, v153\n" \
"v_mfma_f32_16x16x32_bf16 a[56:59], a[144:147], a[220:223], a[56:59]\n" \
"v_sub_f32 v254, v136, v152\n" \
"v_sub_f32 v255, v144, v153\n" \
"v_mfma_f32_16x16x32_bf16 a[60:63], a[148:151], a[220:223], 0\n" \
"s_add_u32 m0, s70, 17408\n" \
"ds_read_b64_tr_b16 a[210:211], v240 offset:35328\n" \
"v_mul_f32 v156, v254, v254\n" \
"v_mfma_f32_16x16x32_bf16 a[60:63], a[132:135], a[216:219], a[60:63]\n" \
"s_add_u32 s74, s69, 1048704\n" \
"v_mul_f32 v157, v255, v255\n" \
"v_sub_f32 v254, v137, v152\n" \
"v_mfma_f32_16x16x32_bf16 a[120:123], a[136:139], a[216:219], 0\n" \
"buffer_load_dwordx4 v246, s[24:27], s74 offen lds\n" \
"v_mfma_f32_16x16x32_bf16 a[120:123], a[152:155], a[220:223], a[120:123]\n" \
"ds_read_b64_tr_b16 a[212:213], v240 offset:41472\n" \
"v_sub_f32 v255, v145, v153\n" \
"v_mfma_f32_16x16x32_bf16 a[124:127], a[156:159], a[220:223], 0\n" \
"s_waitcnt lgkmcnt(5)\n" \
"ds_read_b64_tr_b16 a[214:215], v240 offset:43520\n" \
"v_mfma_f32_16x16x32_bf16 a[124:127], a[140:143], a[216:219], a[124:127]\n" \
"s_add_u32 m0, s72, 8192\n" \
"v_mul_f32 v158, v254, v254\n" \
"v_mul_f32 v159, v255, v255\n" \
"v_mfma_f32_16x16x32_bf16 v[0:3], a[192:195], a[160:163], v[0:3]\n" \
"s_add_u32 s74, s71, 384\n" \
"buffer_load_dwordx4 v249, s[16:19], s74 offen lds\n" \
"v_mfma_f32_16x16x32_bf16 v[0:3], a[196:199], a[176:179], v[0:3]\n" \
"ds_read_b64_tr_b16 a[216:217], v241 offset:33280\n" \
"v_sub_f32 v254, v138, v152\n" \
"v_mfma_f32_16x16x32_bf16 v[32:35], a[196:199], a[180:183], v[32:35]\n" \
"ds_read_b64_tr_b16 a[218:219], v241 offset:35328\n" \
"v_sub_f32 v255, v146, v153\n" \
"v_mfma_f32_16x16x32_bf16 v[32:35], a[192:195], a[164:167], v[32:35]\n" \
"v_fmac_f32 v156, v254, v254\n" \
"v_fmac_f32 v157, v255, v255\n" \
"v_mfma_f32_16x16x32_bf16 v[64:67], a[192:195], a[168:171], v[64:67]\n" \
"s_add_u32 m0, s72, 9216\n" \
"ds_read_b64_tr_b16 a[220:221], v241 offset:41472\n" \
"v_sub_f32 v254, v139, v152\n" \
"v_mfma_f32_16x16x32_bf16 v[64:67], a[196:199], a[184:187], v[64:67]\n" \
"s_add_u32 s74, s71, 8576\n" \
"v_sub_f32 v255, v147, v153\n" \
"v_fmac_f32 v158, v254, v254\n" \
"v_mfma_f32_16x16x32_bf16 v[96:99], a[196:199], a[188:191], v[96:99]\n" \
"buffer_load_dwordx4 v250, s[16:19], s74 offen lds\n" \
"v_mfma_f32_16x16x32_bf16 v[96:99], a[192:195], a[172:175], v[96:99]\n" \
"s_waitcnt lgkmcnt(7)\n" \
"ds_read_b64_tr_b16 a[222:223], v241 offset:43520\n" \
"v_fmac_f32 v159, v255, v255\n" \
"v_mfma_f32_16x16x32_bf16 v[4:7], a[200:203], a[160:163], v[4:7]\n" \
"s_add_u32 m0, s70, 24576\n" \
"ds_read_b64_tr_b16 a[192:193], v240 offset:33792\n" \
"v_sub_f32 v254, v140, v152\n" \
"v_mfma_f32_16x16x32_bf16 v[4:7], a[204:207], a[176:179], v[4:7]\n" \
"s_add_u32 s74, s69, 1310720\n" \
"v_sub_f32 v255, v148, v153\n" \
"v_fmac_f32 v156, v254, v254\n" \
"v_mfma_f32_16x16x32_bf16 v[36:39], a[204:207], a[180:183], v[36:39]\n" \
"buffer_load_dwordx4 v246, s[24:27], s74 offen lds\n" \
"v_mfma_f32_16x16x32_bf16 v[36:39], a[200:203], a[164:167], v[36:39]\n" \
"s_add_u32 m0, s72, 10240\n" \
"ds_read_b64_tr_b16 a[194:195], v240 offset:35840\n" \
"v_fmac_f32 v157, v255, v255\n" \
"v_mfma_f32_16x16x32_bf16 v[68:71], a[200:203], a[168:171], v[68:71]\n" \
"s_add_u32 s74, s71, 16768\n" \
"ds_read_b64_tr_b16 a[196:197], v240 offset:41984\n" \
"v_sub_f32 v254, v141, v152\n" \
"v_mfma_f32_16x16x32_bf16 v[68:71], a[204:207], a[184:187], v[68:71]\n" \
"buffer_load_dwordx4 v249, s[16:19], s74 offen lds\n" \
"v_mfma_f32_16x16x32_bf16 v[100:103], a[204:207], a[188:191], v[100:103]\n" \
"ds_read_b64_tr_b16 a[198:199], v240 offset:44032\n" \
"v_sub_f32 v255, v149, v153\n" \
"v_mfma_f32_16x16x32_bf16 v[100:103], a[200:203], a[172:175], v[100:103]\n" \
"s_waitcnt lgkmcnt(8)\n" \
"v_fmac_f32 v158, v254, v254\n" \
"v_fmac_f32 v159, v255, v255\n" \
"v_mfma_f32_16x16x32_bf16 v[8:11], a[208:211], a[160:163], v[8:11]\n" \
"s_add_u32 m0, s72, 11264\n" \
"ds_read_b64_tr_b16 a[200:201], v241 offset:33792\n" \
"v_sub_f32 v254, v142, v152\n" \
"v_mfma_f32_16x16x32_bf16 v[8:11], a[212:215], a[176:179], v[8:11]\n" \
"s_add_u32 s74, s71, 24960\n" \
"v_sub_f32 v255, v150, v153\n" \
"v_fmac_f32 v156, v254, v254\n" \
"v_mfma_f32_16x16x32_bf16 v[40:43], a[212:215], a[180:183], v[40:43]\n" \
"s_lshl2_add_u32 s85, s67, %[wid]\n" \
"buffer_load_dwordx4 v250, s[16:19], s74 offen lds\n" \
"v_mfma_f32_16x16x32_bf16 v[40:43], a[208:211], a[164:167], v[40:43]\n" \
"s_lshl_b32 s85, s85, 16\n" \
"ds_read_b64_tr_b16 a[202:203], v241 offset:35840\n" \
"v_fmac_f32 v157, v255, v255\n" \
"v_mfma_f32_16x16x32_bf16 v[72:75], a[208:211], a[168:171], v[72:75]\n" \
"s_lshl_b32 s86, s68, 8\n" \
"ds_read_b64_tr_b16 a[204:205], v241 offset:41984\n" \
"v_sub_f32 v254, v143, v152\n" \
"v_mfma_f32_16x16x32_bf16 v[72:75], a[212:215], a[184:187], v[72:75]\n" \
"s_add_u32 s85, s85, s86\n" \
"v_sub_f32 v255, v151, v153\n" \
"v_fmac_f32 v158, v254, v254\n" \
"v_mfma_f32_16x16x32_bf16 v[104:107], a[212:215], a[188:191], v[104:107]\n" \
"s_add_u32 m0, s70, 25600\n" \
"ds_read_b64_tr_b16 a[206:207], v241 offset:44032\n" \
"v_fmac_f32 v159, v255, v255\n" \
"v_mfma_f32_16x16x32_bf16 v[104:107], a[208:211], a[172:175], v[104:107]\n" \
"s_waitcnt lgkmcnt(8)\n" \
"v_add_f32 v156, v156, v158\n" \
"v_add_f32 v157, v157, v159\n" \
"v_mfma_f32_16x16x32_bf16 v[12:15], a[216:219], a[160:163], v[12:15]\n" \
"s_add_u32 s74, s69, 1310848\n" \
"buffer_load_dwordx4 v246, s[24:27], s74 offen lds\n" \
"v_mfma_f32_16x16x32_bf16 v[12:15], a[220:223], a[176:179], v[12:15]\n" \
"s_add_u32 m0, s72, 12288\n" \
"ds_read_b64_tr_b16 a[208:209], v240 offset:34304\n" \
"v_add_f32_dpp v156, v156, v156 quad_perm:[1,0,3,2] row_mask:0xf bank_mask:0xf\n" \
"v_mfma_f32_16x16x32_bf16 v[44:47], a[220:223], a[180:183], v[44:47]\n" \
"s_add_u32 s74, s94, 33152\n" \
"ds_read_b64_tr_b16 a[210:211], v240 offset:36352\n" \
"v_add_f32_dpp v157, v157, v157 quad_perm:[1,0,3,2] row_mask:0xf bank_mask:0xf\n" \
"v_mfma_f32_16x16x32_bf16 v[44:47], a[216:219], a[164:167], v[44:47]\n" \
"s_add_u32 s85, s85, 2097152\n" \
"buffer_load_dwordx4 v249, s[16:19], s74 offen lds\n" \
"v_mfma_f32_16x16x32_bf16 v[76:79], a[216:219], a[168:171], v[76:79]\n" \
"s_add_u32 m0, s72, 13312\n" \
"buffer_load_dword v245, v208, s[20:23], s85 offen\n" \
"v_add_f32_dpp v156, v156, v156 quad_perm:[2,3,0,1] row_mask:0xf bank_mask:0xf\n" \
"v_mfma_f32_16x16x32_bf16 v[76:79], a[220:223], a[184:187], v[76:79]\n" \
"s_add_u32 s74, s94, 41344\n" \
"ds_read_b64_tr_b16 a[212:213], v240 offset:42496\n" \
"v_add_f32_dpp v157, v157, v157 quad_perm:[2,3,0,1] row_mask:0xf bank_mask:0xf\n" \
"v_mfma_f32_16x16x32_bf16 v[108:111], a[220:223], a[188:191], v[108:111]\n" \
"ds_read_b64_tr_b16 a[214:215], v240 offset:44544\n" \
"v_add_f32_dpp v156, v156, v156 row_half_mirror row_mask:0xf bank_mask:0xf\n" \
"v_mfma_f32_16x16x32_bf16 v[108:111], a[216:219], a[172:175], v[108:111]\n" \
"s_waitcnt lgkmcnt(8)\n" \
"buffer_load_dwordx4 v250, s[16:19], s74 offen lds\n" \
"v_mfma_f32_16x16x32_bf16 v[16:19], a[192:195], a[160:163], v[16:19]\n" \
"ds_read_b64_tr_b16 a[216:217], v241 offset:34304\n" \
"v_add_f32_dpp v157, v157, v157 row_half_mirror row_mask:0xf bank_mask:0xf\n" \
"v_mfma_f32_16x16x32_bf16 v[16:19], a[196:199], a[176:179], v[16:19]\n" \
"v_add_f32_dpp v156, v156, v156 row_mirror row_mask:0xf bank_mask:0xf\n" \
"v_mfma_f32_16x16x32_bf16 v[48:51], a[196:199], a[180:183], v[48:51]\n" \
"s_add_u32 m0, s72, 14336\n" \
"ds_read_b64_tr_b16 a[218:219], v241 offset:36352\n" \
"v_add_f32_dpp v157, v157, v157 row_mirror row_mask:0xf bank_mask:0xf\n" \
"v_mfma_f32_16x16x32_bf16 v[48:51], a[192:195], a[164:167], v[48:51]\n" \
"s_add_u32 s74, s94, 49536\n" \
"ds_read_b64_tr_b16 a[220:221], v241 offset:42496\n" \
"v_mov_b32 v158, v156\n" \
"v_mfma_f32_16x16x32_bf16 v[80:83], a[192:195], a[168:171], v[80:83]\n" \
"buffer_load_dwordx4 v249, s[16:19], s74 offen lds\n" \
"v_mfma_f32_16x16x32_bf16 v[80:83], a[196:199], a[184:187], v[80:83]\n" \
"ds_read_b64_tr_b16 a[222:223], v241 offset:44544\n" \
"v_mov_b32 v159, v157\n" \
"v_mfma_f32_16x16x32_bf16 v[112:115], a[196:199], a[188:191], v[112:115]\n" \
"v_permlane16_swap_b32 v156, v158\n" \
"v_mfma_f32_16x16x32_bf16 v[112:115], a[192:195], a[172:175], v[112:115]\n" \
"s_waitcnt lgkmcnt(8)\n" \
"v_permlane16_swap_b32 v157, v159\n" \
"v_mfma_f32_16x16x32_bf16 v[20:23], a[200:203], a[160:163], v[20:23]\n" \
"s_add_u32 m0, s72, 15360\n" \
"v_add_f32 v156, v156, v158\n" \
"v_add_f32 v157, v157, v159\n" \
"v_mfma_f32_16x16x32_bf16 v[20:23], a[204:207], a[176:179], v[20:23]\n" \
"s_add_u32 s74, s94, 57728\n" \
"v_mov_b32 v158, v156\n" \
"v_mov_b32 v159, v157\n" \
"v_mfma_f32_16x16x32_bf16 v[52:55], a[204:207], a[180:183], v[52:55]\n" \
"buffer_load_dwordx4 v250, s[16:19], s74 offen lds\n" \
"v_mfma_f32_16x16x32_bf16 v[52:55], a[200:203], a[164:167], v[52:55]\n" \
"v_permlane32_swap_b32 v156, v158\n" \
"v_mfma_f32_16x16x32_bf16 v[84:87], a[200:203], a[168:171], v[84:87]\n" \
"s_waitcnt vmcnt(26) lgkmcnt(0)\n" \
"s_barrier\n" \
"s_add_u32 m0, s70, 32768\n" \
"v_mfma_f32_16x16x32_bf16 v[84:87], a[204:207], a[184:187], v[84:87]\n" \
"s_add_u32 s74, s69, 1572864\n" \
"ds_read_b64_tr_b16 a[192:193], v240 offset:49152\n" \
"ds_read_b64_tr_b16 a[194:195], v240 offset:51200\n" \
"v_mfma_f32_16x16x32_bf16 v[116:119], a[204:207], a[188:191], v[116:119]\n" \
"buffer_load_dwordx4 v246, s[20:23], s74 offen lds\n" \
"v_mfma_f32_16x16x32_bf16 v[116:119], a[200:203], a[172:175], v[116:119]\n" \
"ds_read_b64_tr_b16 a[196:197], v240 offset:57344\n" \
"ds_read_b64_tr_b16 a[198:199], v240 offset:59392\n" \
"v_mfma_f32_16x16x32_bf16 v[24:27], a[208:211], a[160:163], v[24:27]\n" \
"ds_read_b64_tr_b16 a[200:201], v241 offset:49152\n" \
"v_mfma_f32_16x16x32_bf16 v[24:27], a[212:215], a[176:179], v[24:27]\n" \
"v_permlane32_swap_b32 v157, v159\n" \
"v_mfma_f32_16x16x32_bf16 v[56:59], a[212:215], a[180:183], v[56:59]\n" \
"ds_read_b64_tr_b16 a[202:203], v241 offset:51200\n" \
"v_add_f32 v156, v156, v158\n" \
"v_mfma_f32_16x16x32_bf16 v[56:59], a[208:211], a[164:167], v[56:59]\n" \
"v_add_f32 v157, v157, v159\n" \
"v_mul_f32 v156, 0x3b000000, v156\n" \
"v_mfma_f32_16x16x32_bf16 v[88:91], a[208:211], a[168:171], v[88:91]\n" \
"ds_read_b64_tr_b16 a[204:205], v241 offset:57344\n" \
"v_mul_f32 v157, 0x3b000000, v157\n" \
"v_mfma_f32_16x16x32_bf16 v[88:91], a[212:215], a[184:187], v[88:91]\n" \
"v_cvt_pk_bf16_f32 v254, v156, v157\n" \
"v_cvt_pk_bf16_f32 v255, v152, v153\n" \
"v_mfma_f32_16x16x32_bf16 v[120:123], a[212:215], a[188:191], v[120:123]\n" \
"ds_read_b64_tr_b16 a[206:207], v241 offset:59392\n" \
"v_lshlrev_b32 v156, 16, v254\n" \
"v_mfma_f32_16x16x32_bf16 v[120:123], a[208:211], a[172:175], v[120:123]\n" \
"v_and_b32 v157, 0xffff0000, v254\n" \
"v_lshlrev_b32 v152, 16, v255\n" \
"v_mfma_f32_16x16x32_bf16 v[28:31], a[216:219], a[160:163], v[28:31]\n" \
"ds_read_b64_tr_b16 a[208:209], v240 offset:49664\n" \
"v_and_b32 v153, 0xffff0000, v255\n" \
"v_mfma_f32_16x16x32_bf16 v[28:31], a[220:223], a[176:179], v[28:31]\n" \
"v_add_f32 v156, 0x3727c5ac, v156\n" \
"v_add_f32 v157, 0x3727c5ac, v157\n" \
"v_mfma_f32_16x16x32_bf16 v[60:63], a[220:223], a[180:183], v[60:63]\n" \
"s_add_u32 m0, s70, 33792\n" \
"ds_read_b64_tr_b16 a[210:211], v240 offset:51712\n" \
"v_cvt_pk_bf16_f32 v254, v156, v157\n" \
"v_mfma_f32_16x16x32_bf16 v[60:63], a[216:219], a[164:167], v[60:63]\n" \
"s_add_u32 s74, s69, 1572992\n" \
"v_lshlrev_b32 v156, 16, v254\n" \
"v_and_b32 v157, 0xffff0000, v254\n" \
"v_mfma_f32_16x16x32_bf16 v[92:95], a[216:219], a[168:171], v[92:95]\n" \
"buffer_load_dwordx4 v246, s[20:23], s74 offen lds\n" \
"v_mfma_f32_16x16x32_bf16 v[92:95], a[220:223], a[184:187], v[92:95]\n" \
"ds_read_b64_tr_b16 a[212:213], v240 offset:57856\n" \
"v_mfma_f32_16x16x32_bf16 v[124:127], a[220:223], a[188:191], v[124:127]\n" \
"s_waitcnt vmcnt(17)\n" \
"ds_read_b64_tr_b16 a[214:215], v240 offset:59904\n" \
"ds_read_b128 a[224:227], v242 offset:0\n" \
"v_mfma_f32_16x16x32_bf16 v[124:127], a[216:219], a[172:175], v[124:127]\n" \
"s_waitcnt lgkmcnt(9)\n" \
"v_sqrt_f32 v156, v156\n" \
"v_mfma_f32_16x16x32_bf16 a[0:3], a[160:163], a[192:195], a[0:3]\n" \
"ds_read_b64_tr_b16 a[216:217], v241 offset:49664\n" \
"ds_read_b128 a[228:231], v242 offset:2048\n" \
"v_mfma_f32_16x16x32_bf16 a[0:3], a[176:179], a[196:199], a[0:3]\n" \
"v_sqrt_f32 v157, v157\n" \
"v_mfma_f32_16x16x32_bf16 a[4:7], a[180:183], a[196:199], a[4:7]\n" \
"ds_read_b64_tr_b16 a[218:219], v241 offset:51712\n" \
"ds_read_b128 a[232:235], v242 offset:4096\n" \
"v_mfma_f32_16x16x32_bf16 a[4:7], a[164:167], a[192:195], a[4:7]\n" \
"v_cvt_pk_bf16_f32 v254, v156, v157\n" \
"v_lshlrev_b32 v156, 16, v254\n" \
"v_mfma_f32_16x16x32_bf16 a[64:67], a[168:171], a[192:195], a[64:67]\n" \
"ds_read_b64_tr_b16 a[220:221], v241 offset:57856\n" \
"ds_read_b128 a[236:239], v242 offset:6144\n" \
"v_mfma_f32_16x16x32_bf16 a[64:67], a[184:187], a[196:199], a[64:67]\n" \
"v_and_b32 v157, 0xffff0000, v254\n" \
"v_mfma_f32_16x16x32_bf16 a[68:71], a[188:191], a[196:199], a[68:71]\n" \
"ds_read_b64_tr_b16 a[222:223], v241 offset:59904\n" \
"v_mfma_f32_16x16x32_bf16 a[68:71], a[172:175], a[192:195], a[68:71]\n" \
"s_waitcnt lgkmcnt(12)\n" \
"v_rcp_f32 v156, v156\n" \
"v_mfma_f32_16x16x32_bf16 a[8:11], a[160:163], a[200:203], a[8:11]\n" \
"s_add_u32 m0, s70, 40960\n" \
"ds_read_b64_tr_b16 a[192:193], v240 offset:50176\n" \
"v_mfma_f32_16x16x32_bf16 a[8:11], a[176:179], a[204:207], a[8:11]\n" \
"s_add_u32 s74, s69, 1835008\n" \
"v_rcp_f32 v157, v157\n" \
"v_mfma_f32_16x16x32_bf16 a[12:15], a[180:183], a[204:207], a[12:15]\n" \
"buffer_load_dwordx4 v246, s[20:23], s74 offen lds\n" \
"v_mfma_f32_16x16x32_bf16 a[12:15], a[164:167], a[200:203], a[12:15]\n" \
"ds_read_b64_tr_b16 a[194:195], v240 offset:52224\n" \
"ds_read_b128 a[240:243], v244 offset:0\n" \
"v_mfma_f32_16x16x32_bf16 a[72:75], a[168:171], a[200:203], a[72:75]\n" \
"ds_read_b64_tr_b16 a[196:197], v240 offset:58368\n" \
"ds_read_b128 a[244:247], v244 offset:2048\n" \
"v_mfma_f32_16x16x32_bf16 a[72:75], a[184:187], a[204:207], a[72:75]\n" \
"v_sub_f32 v136, v136, v152\n" \
"v_sub_f32 v137, v137, v152\n" \
"v_mfma_f32_16x16x32_bf16 a[76:79], a[188:191], a[204:207], a[76:79]\n" \
"ds_read_b64_tr_b16 a[198:199], v240 offset:60416\n" \
"ds_read_b128 a[248:251], v244 offset:4096\n" \
"v_mfma_f32_16x16x32_bf16 a[76:79], a[172:175], a[200:203], a[76:79]\n" \
"s_waitcnt lgkmcnt(15)\n" \
"v_cvt_pk_bf16_f32 v128, v136, v137\n" \
"v_lshlrev_b32 v136, 16, v128\n" \
"v_mfma_f32_16x16x32_bf16 a[16:19], a[160:163], a[208:211], a[16:19]\n" \
"s_lshl2_add_u32 s81, s64, %[wid]\n" \
"ds_read_b64_tr_b16 a[200:201], v241 offset:50176\n" \
"ds_read_b128 a[252:255], v244 offset:6144\n" \
"v_mfma_f32_16x16x32_bf16 a[16:19], a[176:179], a[212:215], a[16:19]\n" \
"s_lshl_b32 s81, s81, 9\n" \
"v_and_b32 v137, 0xffff0000, v128\n" \
"v_mul_f32 v136, v136, v156\n" \
"v_mfma_f32_16x16x32_bf16 a[20:23], a[180:183], a[212:215], a[20:23]\n" \
"s_add_u32 s81, s81, 573440\n" \
"buffer_load_dwordx2 v[204:205], v253, s[32:35], s81 offen sc1\n" \
"v_mul_f32 v137, v137, v156\n" \
"v_mfma_f32_16x16x32_bf16 a[20:23], a[164:167], a[208:211], a[20:23]\n" \
"ds_read_b64_tr_b16 a[202:203], v241 offset:52224\n" \
"v_cvt_pk_bf16_f32 v128, v136, v137\n" \
"v_mfma_f32_16x16x32_bf16 a[80:83], a[168:171], a[208:211], a[80:83]\n" \
"ds_read_b64_tr_b16 a[204:205], v241 offset:58368\n" \
"v_sub_f32 v138, v138, v152\n" \
"v_mfma_f32_16x16x32_bf16 a[80:83], a[184:187], a[212:215], a[80:83]\n" \
"v_sub_f32 v139, v139, v152\n" \
"v_cvt_pk_bf16_f32 v129, v138, v139\n" \
"v_mfma_f32_16x16x32_bf16 a[84:87], a[188:191], a[212:215], a[84:87]\n" \
"s_add_u32 m0, s70, 41984\n" \
"ds_read_b64_tr_b16 a[206:207], v241 offset:60416\n" \
"v_lshlrev_b32 v138, 16, v129\n" \
"v_mfma_f32_16x16x32_bf16 a[84:87], a[172:175], a[208:211], a[84:87]\n" \
"s_waitcnt lgkmcnt(12)\n" \
"v_and_b32 v139, 0xffff0000, v129\n" \
"v_mul_f32 v138, v138, v156\n" \
"v_mfma_f32_16x16x32_bf16 a[24:27], a[160:163], a[216:219], a[24:27]\n" \
"s_add_u32 s74, s69, 1835136\n" \
"buffer_load_dwordx4 v246, s[20:23], s74 offen lds\n" \
"v_mfma_f32_16x16x32_bf16 a[24:27], a[176:179], a[220:223], a[24:27]\n" \
"ds_read_b64_tr_b16 a[208:209], v240 offset:50688\n" \
"v_mul_f32 v139, v139, v156\n" \
"v_mfma_f32_16x16x32_bf16 a[28:31], a[180:183], a[220:223], a[28:31]\n" \
"ds_read_b64_tr_b16 a[210:211], v240 offset:52736\n" \
"v_cvt_pk_bf16_f32 v129, v138, v139\n" \
"v_mfma_f32_16x16x32_bf16 a[28:31], a[164:167], a[216:219], a[28:31]\n" \
"v_sub_f32 v140, v140, v152\n" \
"v_sub_f32 v141, v141, v152\n" \
"v_mfma_f32_16x16x32_bf16 a[88:91], a[168:171], a[216:219], a[88:91]\n" \
"s_add_u32 m0, s72, 0\n" \
"ds_read_b64_tr_b16 a[212:213], v240 offset:58880\n" \
"v_cvt_pk_bf16_f32 v130, v140, v141\n" \
"v_mfma_f32_16x16x32_bf16 a[88:91], a[184:187], a[220:223], a[88:91]\n" \
"s_add_u32 s74, s71, 512\n" \
"v_lshlrev_b32 v140, 16, v130\n" \
"v_and_b32 v141, 0xffff0000, v130\n" \
"v_mfma_f32_16x16x32_bf16 a[92:95], a[188:191], a[220:223], a[92:95]\n" \
"s_waitcnt lgkmcnt(6)\n" \
"buffer_load_dwordx4 v249, s[16:19], s74 offen lds\n" \
"v_mfma_f32_16x16x32_bf16 a[92:95], a[172:175], a[216:219], a[92:95]\n" \
"ds_read_b64_tr_b16 a[214:215], v240 offset:60928\n" \
"v_mul_f32 v140, v140, v156\n" \
"v_mfma_f32_16x16x32_bf16 a[32:35], a[160:163], a[192:195], a[32:35]\n" \
"ds_read_b64_tr_b16 a[216:217], v241 offset:50688\n" \
"v_mul_f32 v141, v141, v156\n" \
"v_mfma_f32_16x16x32_bf16 a[32:35], a[176:179], a[196:199], a[32:35]\n" \
"s_lshl2_add_u32 s85, s67, %[wid]\n" \
"v_cvt_pk_bf16_f32 v130, v140, v141\n" \
"v_sub_f32 v142, v142, v152\n" \
"v_mfma_f32_16x16x32_bf16 a[36:39], a[180:183], a[196:199], a[36:39]\n" \
"s_add_u32 m0, s72, 1024\n" \
"ds_read_b64_tr_b16 a[218:219], v241 offset:52736\n" \
"v_sub_f32 v143, v143, v152\n" \
"v_mfma_f32_16x16x32_bf16 a[36:39], a[164:167], a[192:195], a[36:39]\n" \
"s_add_u32 s74, s71, 8704\n" \
"ds_read_b64_tr_b16 a[220:221], v241 offset:58880\n" \
"v_cvt_pk_bf16_f32 v131, v142, v143\n" \
"v_mfma_f32_16x16x32_bf16 a[96:99], a[168:171], a[192:195], a[96:99]\n" \
"s_lshl_b32 s85, s85, 16\n" \
"buffer_load_dwordx4 v250, s[16:19], s74 offen lds\n" \
"v_mfma_f32_16x16x32_bf16 a[96:99], a[184:187], a[196:199], a[96:99]\n" \
"s_lshl_b32 s86, s68, 8\n" \
"ds_read_b64_tr_b16 a[222:223], v241 offset:60928\n" \
"v_lshlrev_b32 v142, 16, v131\n" \
"v_mfma_f32_16x16x32_bf16 a[100:103], a[188:191], a[196:199], a[100:103]\n" \
"s_add_u32 s85, s85, s86\n" \
"v_and_b32 v143, 0xffff0000, v131\n" \
"v_mul_f32 v142, v142, v156\n" \
"v_mfma_f32_16x16x32_bf16 a[100:103], a[172:175], a[192:195], a[100:103]\n" \
"s_waitcnt lgkmcnt(8)\n" \
"v_mul_f32 v143, v143, v156\n" \
"v_cvt_pk_bf16_f32 v131, v142, v143\n" \
"v_mfma_f32_16x16x32_bf16 a[40:43], a[160:163], a[200:203], a[40:43]\n" \
"s_add_u32 m0, s72, 2048\n" \
"v_sub_f32 v144, v144, v153\n" \
"v_sub_f32 v145, v145, v153\n" \
"v_mfma_f32_16x16x32_bf16 a[40:43], a[176:179], a[204:207], a[40:43]\n" \
"s_add_u32 s74, s71, 16896\n" \
"v_cvt_pk_bf16_f32 v132, v144, v145\n" \
"v_lshlrev_b32 v144, 16, v132\n" \
"v_mfma_f32_16x16x32_bf16 a[44:47], a[180:183], a[204:207], a[44:47]\n" \
"s_add_u32 s85, s85, 2097152\n" \
"buffer_load_dwordx4 v249, s[16:19], s74 offen lds\n" \
"v_mfma_f32_16x16x32_bf16 a[44:47], a[164:167], a[200:203], a[44:47]\n" \
"buffer_load_dword v245, v208, s[24:27], s85 offen\n" \
"v_and_b32 v145, 0xffff0000, v132\n" \
"v_mfma_f32_16x16x32_bf16 a[104:107], a[168:171], a[200:203], a[104:107]\n" \
"s_waitcnt vmcnt(22) lgkmcnt(0)\n" \
"s_barrier\n" \
"v_mul_f32 v144, v144, v157\n" \
"v_mfma_f32_16x16x32_bf16 a[104:107], a[184:187], a[204:207], a[104:107]\n" \
"s_add_u32 m0, s70, 49152\n" \
"ds_read_b64_tr_b16 a[192:193], v240 offset:0\n" \
"ds_read_b64_tr_b16 a[194:195], v240 offset:2048\n" \
"v_mfma_f32_16x16x32_bf16 a[108:111], a[188:191], a[204:207], a[108:111]\n" \
"s_add_u32 s74, s69, 1572864\n" \
"buffer_load_dwordx4 v246, s[24:27], s74 offen lds\n" \
"v_mfma_f32_16x16x32_bf16 a[108:111], a[172:175], a[200:203], a[108:111]\n" \
"s_add_u32 m0, s72, 3072\n" \
"ds_read_b64_tr_b16 a[196:197], v240 offset:8192\n" \
"ds_read_b64_tr_b16 a[198:199], v240 offset:10240\n" \
"v_mfma_f32_16x16x32_bf16 a[48:51], a[160:163], a[208:211], a[48:51]\n" \
"s_add_u32 s74, s71, 25088\n" \
"ds_read_b64_tr_b16 a[200:201], v241 offset:0\n" \
"v_mul_f32 v145, v145, v157\n" \
"v_mfma_f32_16x16x32_bf16 a[48:51], a[176:179], a[212:215], a[48:51]\n" \
"buffer_load_dwordx4 v250, s[16:19], s74 offen lds\n" \
"v_mfma_f32_16x16x32_bf16 a[52:55], a[180:183], a[212:215], a[52:55]\n" \
"ds_read_b64_tr_b16 a[202:203], v241 offset:2048\n" \
"v_cvt_pk_bf16_f32 v132, v144, v145\n" \
"v_mfma_f32_16x16x32_bf16 a[52:55], a[164:167], a[208:211], a[52:55]\n" \
"v_sub_f32 v146, v146, v153\n" \
"v_sub_f32 v147, v147, v153\n" \
"v_mfma_f32_16x16x32_bf16 a[112:115], a[168:171], a[208:211], a[112:115]\n" \
"s_add_u32 m0, s72, 4096\n" \
"ds_read_b64_tr_b16 a[204:205], v241 offset:8192\n" \
"v_cvt_pk_bf16_f32 v133, v146, v147\n" \
"v_mfma_f32_16x16x32_bf16 a[112:115], a[184:187], a[212:215], a[112:115]\n" \
"s_add_u32 s74, s94, 33280\n" \
"v_lshlrev_b32 v146, 16, v133\n" \
"v_and_b32 v147, 0xffff0000, v133\n" \
"v_mfma_f32_16x16x32_bf16 a[116:119], a[188:191], a[212:215], a[116:119]\n" \
"buffer_load_dwordx4 v249, s[16:19], s74 offen lds\n" \
"v_mfma_f32_16x16x32_bf16 a[116:119], a[172:175], a[208:211], a[116:119]\n" \
"ds_read_b64_tr_b16 a[206:207], v241 offset:10240\n" \
"v_mul_f32 v146, v146, v157\n" \
"v_mfma_f32_16x16x32_bf16 a[56:59], a[160:163], a[216:219], a[56:59]\n" \
"ds_read_b64_tr_b16 a[208:209], v240 offset:512\n" \
"v_mul_f32 v147, v147, v157\n" \
"v_mfma_f32_16x16x32_bf16 a[56:59], a[176:179], a[220:223], a[56:59]\n" \
"v_cvt_pk_bf16_f32 v133, v146, v147\n" \
"v_sub_f32 v148, v148, v153\n" \
"v_mfma_f32_16x16x32_bf16 a[60:63], a[180:183], a[220:223], a[60:63]\n" \
"s_add_u32 m0, s70, 50176\n" \
"ds_read_b64_tr_b16 a[210:211], v240 offset:2560\n" \
"v_sub_f32 v149, v149, v153\n" \
"v_mfma_f32_16x16x32_bf16 a[60:63], a[164:167], a[216:219], a[60:63]\n" \
"s_add_u32 s74, s69, 1572992\n" \
"v_cvt_pk_bf16_f32 v134, v148, v149\n" \
"v_lshlrev_b32 v148, 16, v134\n" \
"v_mfma_f32_16x16x32_bf16 a[120:123], a[168:171], a[216:219], a[120:123]\n" \
"buffer_load_dwordx4 v246, s[24:27], s74 offen lds\n" \
"v_mfma_f32_16x16x32_bf16 a[120:123], a[184:187], a[220:223], a[120:123]\n" \
"s_add_u32 m0, s72, 5120\n" \
"ds_read_b64_tr_b16 a[212:213], v240 offset:8704\n" \
"v_and_b32 v149, 0xffff0000, v134\n" \
"v_mfma_f32_16x16x32_bf16 a[124:127], a[188:191], a[220:223], a[124:127]\n" \
"s_add_u32 s74, s94, 41472\n" \
"ds_read_b64_tr_b16 a[214:215], v240 offset:10752\n" \
"v_mul_f32 v148, v148, v157\n" \
"v_mfma_f32_16x16x32_bf16 a[124:127], a[172:175], a[216:219], a[124:127]\n" \
"s_waitcnt lgkmcnt(8)\n" \
"buffer_load_dwordx4 v250, s[16:19], s74 offen lds\n" \
"v_mfma_f32_16x16x32_bf16 v[0:3], a[192:195], a[224:227], v[0:3]\n" \
"s_add_u32 m0, s72, 6144\n" \
"ds_read_b64_tr_b16 a[216:217], v241 offset:512\n" \
"v_mul_f32 v149, v149, v157\n" \
"v_mfma_f32_16x16x32_bf16 v[0:3], a[196:199], a[240:243], v[0:3]\n" \
"s_add_u32 s74, s94, 49664\n" \
"v_cvt_pk_bf16_f32 v134, v148, v149\n" \
"v_sub_f32 v150, v150, v153\n" \
"v_mfma_f32_16x16x32_bf16 v[32:35], a[196:199], a[244:247], v[32:35]\n" \
"buffer_load_dwordx4 v249, s[16:19], s74 offen lds\n" \
"v_mfma_f32_16x16x32_bf16 v[32:35], a[192:195], a[228:231], v[32:35]\n" \
"ds_read_b64_tr_b16 a[218:219], v241 offset:2560\n" \
"v_sub_f32 v151, v151, v153\n" \
"v_mfma_f32_16x16x32_bf16 v[64:67], a[192:195], a[232:235], v[64:67]\n" \
"ds_read_b64_tr_b16 a[220:221], v241 offset:8704\n" \
"v_cvt_pk_bf16_f32 v135, v150, v151\n" \
"v_mfma_f32_16x16x32_bf16 v[64:67], a[196:199], a[248:251], v[64:67]\n" \
"v_lshlrev_b32 v150, 16, v135\n" \
"v_and_b32 v151, 0xffff0000, v135\n" \
"v_mfma_f32_16x16x32_bf16 v[96:99], a[196:199], a[252:255], v[96:99]\n" \
"s_add_u32 m0, s72, 7168\n" \
"ds_read_b64_tr_b16 a[222:223], v241 offset:10752\n" \
"v_mul_f32 v150, v150, v157\n" \
"v_mfma_f32_16x16x32_bf16 v[96:99], a[192:195], a[236:239], v[96:99]\n" \
"s_waitcnt lgkmcnt(8)\n" \
"v_mul_f32 v151, v151, v157\n" \
"v_cvt_pk_bf16_f32 v135, v150, v151\n" \
"v_mfma_f32_16x16x32_bf16 v[4:7], a[200:203], a[224:227], v[4:7]\n" \
"s_add_u32 s74, s94, 57856\n" \
"buffer_load_dwordx4 v250, s[16:19], s74 offen lds\n" \
"v_mfma_f32_16x16x32_bf16 v[4:7], a[204:207], a[240:243], v[4:7]\n" \
"s_add_u32 m0, s70, 57344\n" \
"s_add_u32 s74, s69, 1835008\n" \
"ds_read_b64_tr_b16 a[192:193], v240 offset:1024\n" \
"v_mfma_f32_16x16x32_bf16 v[36:39], a[204:207], a[244:247], v[36:39]\n" \
"s_mul_i32 s76, %[wid], 2080\n" \
"s_add_u32 s76, s76, 149504\n" \
"ds_read_b64_tr_b16 a[194:195], v240 offset:3072\n" \
"v_mfma_f32_16x16x32_bf16 v[36:39], a[200:203], a[228:231], v[36:39]\n" \
"buffer_load_dwordx4 v246, s[24:27], s74 offen lds\n" \
"v_mfma_f32_16x16x32_bf16 v[68:71], a[200:203], a[232:235], v[68:71]\n" \
"ds_read_b64_tr_b16 a[196:197], v240 offset:9216\n" \
"v_add_u32 v254, s76, v248\n" \
"v_mfma_f32_16x16x32_bf16 v[68:71], a[204:207], a[248:251], v[68:71]\n" \
"ds_write_b128 v254, v[128:131]\n" \
"v_mfma_f32_16x16x32_bf16 v[100:103], a[204:207], a[252:255], v[100:103]\n" \
"ds_read_b64_tr_b16 a[198:199], v240 offset:11264\n" \
"v_mfma_f32_16x16x32_bf16 v[100:103], a[200:203], a[236:239], v[100:103]\n" \
"s_waitcnt lgkmcnt(9)\n" \
"ds_write_b128 v254, v[132:135] offset:1040\n" \
"v_and_b32 v254, 7, v251\n" \
"v_mfma_f32_16x16x32_bf16 v[8:11], a[208:211], a[224:227], v[8:11]\n" \
"ds_read_b64_tr_b16 a[200:201], v241 offset:1024\n" \
"v_mul_u32_u24 v254, 1040, v254\n" \
"v_mfma_f32_16x16x32_bf16 v[8:11], a[212:215], a[240:243], v[8:11]\n" \
"v_lshrrev_b32 v255, 3, v251\n" \
"v_lshl_add_u32 v254, v255, 4, v254\n" \
"v_mfma_f32_16x16x32_bf16 v[40:43], a[212:215], a[244:247], v[40:43]\n" \
"s_lshl_b32 s76, %[wid], 7\n" \
"s_add_u32 s76, s76, 149504\n" \
"ds_read_b64_tr_b16 a[202:203], v241 offset:3072\n" \
"v_mfma_f32_16x16x32_bf16 v[40:43], a[208:211], a[228:231], v[40:43]\n" \
"v_add_u32 v254, s76, v254\n" \
"v_mfma_f32_16x16x32_bf16 v[72:75], a[208:211], a[232:235], v[72:75]\n" \
"ds_read_b64_tr_b16 a[204:205], v241 offset:9216\n" \
"v_mfma_f32_16x16x32_bf16 v[72:75], a[212:215], a[248:251], v[72:75]\n" \
"v_mfma_f32_16x16x32_bf16 v[104:107], a[212:215], a[252:255], v[104:107]\n" \
"s_add_u32 m0, s70, 58368\n" \
"ds_read_b64_tr_b16 a[206:207], v241 offset:11264\n" \
"v_mfma_f32_16x16x32_bf16 v[104:107], a[208:211], a[236:239], v[104:107]\n" \
".Lst0_1_%=:\n" \
"s_cbranch_execz .Lst0_5_%=\n" \
"s_waitcnt lgkmcnt(10)\n" \
"s_add_u32 s74, s69, 1835136\n" \
"v_mfma_f32_16x16x32_bf16 v[12:15], a[216:219], a[224:227], v[12:15]\n" \
"buffer_load_dwordx4 v246, s[24:27], s74 offen lds\n" \
"v_mfma_f32_16x16x32_bf16 v[12:15], a[220:223], a[240:243], v[12:15]\n" \
"ds_read_b64_tr_b16 a[208:209], v240 offset:1536\n" \
"v_mfma_f32_16x16x32_bf16 v[44:47], a[220:223], a[244:247], v[44:47]\n" \
"ds_read_b64_tr_b16 a[210:211], v240 offset:3584\n" \
"v_mfma_f32_16x16x32_bf16 v[44:47], a[216:219], a[228:231], v[44:47]\n" \
"v_mfma_f32_16x16x32_bf16 v[76:79], a[216:219], a[232:235], v[76:79]\n" \
"ds_read_b64_tr_b16 a[212:213], v240 offset:9728\n" \
"v_mfma_f32_16x16x32_bf16 v[76:79], a[220:223], a[248:251], v[76:79]\n" \
"v_mfma_f32_16x16x32_bf16 v[108:111], a[220:223], a[252:255], v[108:111]\n" \
"ds_read_b64_tr_b16 a[214:215], v240 offset:11776\n" \
"v_mfma_f32_16x16x32_bf16 v[108:111], a[216:219], a[236:239], v[108:111]\n" \
"s_waitcnt lgkmcnt(9)\n" \
"v_mfma_f32_16x16x32_bf16 v[16:19], a[192:195], a[224:227], v[16:19]\n" \
"s_waitcnt vmcnt(14)\n" \
"ds_read_b64_tr_b16 a[216:217], v241 offset:1536\n" \
"v_cmp_ne_u64 vcc, v[204:205], v[202:203]\n" \
"v_mfma_f32_16x16x32_bf16 v[16:19], a[196:199], a[240:243], v[16:19]\n" \
"v_mfma_f32_16x16x32_bf16 v[48:51], a[196:199], a[244:247], v[48:51]\n" \
"ds_read_b64_tr_b16 a[218:219], v241 offset:3584\n" \
"v_mfma_f32_16x16x32_bf16 v[48:51], a[192:195], a[228:231], v[48:51]\n" \
"s_cbranch_vccnz .Lspq_%=\n" \
".Lsbq_%=:\n" \
"ds_read_b64_tr_b16 a[220:221], v241 offset:9728\n" \
"v_mfma_f32_16x16x32_bf16 v[80:83], a[192:195], a[232:235], v[80:83]\n" \
"ds_read_b64_tr_b16 a[222:223], v241 offset:11776\n" \
"v_mfma_f32_16x16x32_bf16 v[80:83], a[196:199], a[248:251], v[80:83]\n" \
"v_mfma_f32_16x16x32_bf16 v[112:115], a[196:199], a[252:255], v[112:115]\n" \
"v_mfma_f32_16x16x32_bf16 v[112:115], a[192:195], a[236:239], v[112:115]\n" \
"s_waitcnt lgkmcnt(8)\n" \
"v_mfma_f32_16x16x32_bf16 v[20:23], a[200:203], a[224:227], v[20:23]\n" \
"v_mfma_f32_16x16x32_bf16 v[20:23], a[204:207], a[240:243], v[20:23]\n" \
"v_mfma_f32_16x16x32_bf16 v[52:55], a[204:207], a[244:247], v[52:55]\n" \
"v_mfma_f32_16x16x32_bf16 v[52:55], a[200:203], a[228:231], v[52:55]\n" \
"v_mfma_f32_16x16x32_bf16 v[84:87], a[200:203], a[232:235], v[84:87]\n" \
"s_waitcnt lgkmcnt(0)\n" \
"s_barrier\n" \
"ds_read_b128 a[128:131], v242 offset:8192\n" \
"v_mfma_f32_16x16x32_bf16 v[84:87], a[204:207], a[248:251], v[84:87]\n" \
"s_add_u32 m0, s70, 0\n" \
"ds_read_b64_tr_b16 a[192:193], v240 offset:16384\n" \
"ds_read_b64_tr_b16 a[194:195], v240 offset:18432\n" \
"v_mfma_f32_16x16x32_bf16 v[116:119], a[204:207], a[252:255], v[116:119]\n" \
"s_add_u32 s74, s69, 2097152\n" \
"buffer_load_dwordx4 v246, s[20:23], s74 offen lds\n" \
"v_mfma_f32_16x16x32_bf16 v[116:119], a[200:203], a[236:239], v[116:119]\n" \
"s_lshl_b32 s85, %[wid], 10\n" \
"ds_read_b64_tr_b16 a[196:197], v240 offset:24576\n" \
"ds_read_b64_tr_b16 a[198:199], v240 offset:26624\n" \
"v_mfma_f32_16x16x32_bf16 v[24:27], a[208:211], a[224:227], v[24:27]\n" \
"s_add_u32 s85, s85, 917504\n" \
"buffer_load_dwordx4 v[168:171], v248, s[32:35], s85 offen sc1\n" \
"v_and_b32 v252, 3, v251\n" \
"v_mfma_f32_16x16x32_bf16 v[24:27], a[212:215], a[240:243], v[24:27]\n" \
"s_lshl_b32 s85, %[wid], 10\n" \
"ds_read_b64_tr_b16 a[200:201], v241 offset:16384\n" \
"ds_read_b128 a[132:135], v242 offset:10240\n" \
"v_mfma_f32_16x16x32_bf16 v[56:59], a[212:215], a[244:247], v[56:59]\n" \
"s_add_u32 s85, s85, 929792\n" \
"buffer_load_dwordx4 v[172:175], v248, s[32:35], s85 offen sc1\n" \
"v_mul_u32_u24 v252, 0x3000, v252\n" \
"v_mfma_f32_16x16x32_bf16 v[56:59], a[208:211], a[228:231], v[56:59]\n" \
"s_lshl_b32 s85, %[wid], 10\n" \
"ds_read_b64_tr_b16 a[202:203], v241 offset:18432\n" \
"ds_read_b128 a[136:139], v242 offset:12288\n" \
"v_mfma_f32_16x16x32_bf16 v[88:91], a[208:211], a[232:235], v[88:91]\n" \
"s_add_u32 s85, s85, 942080\n" \
"buffer_load_dwordx4 v[176:179], v248, s[32:35], s85 offen sc1\n" \
"v_and_b32 v253, 28, v251\n" \
"v_mfma_f32_16x16x32_bf16 v[88:91], a[212:215], a[248:251], v[88:91]\n" \
"s_lshl_b32 s85, %[wid], 10\n" \
"ds_read_b64_tr_b16 a[204:205], v241 offset:24576\n" \
"ds_read_b128 a[140:143], v242 offset:14336\n" \
"v_mfma_f32_16x16x32_bf16 v[120:123], a[212:215], a[252:255], v[120:123]\n" \
"s_add_u32 s85, s85, 954368\n" \
"buffer_load_dwordx4 v[180:183], v248, s[32:35], s85 offen sc1\n" \
"v_add_u32 v252, v252, v253\n" \
"v_mfma_f32_16x16x32_bf16 v[120:123], a[208:211], a[236:239], v[120:123]\n" \
"s_lshl_b32 s85, %[bid], 5\n" \
"s_add_u32 s85, s85, 921600\n" \
"ds_read_b64_tr_b16 a[206:207], v241 offset:26624\n" \
"v_mfma_f32_16x16x32_bf16 v[28:31], a[216:219], a[224:227], v[28:31]\n" \
"s_cmp_lt_u32 %[wid], 2\n" \
"s_cselect_b32 s83, 0, 1.0\n" \
"buffer_load_dword v200, v252, s[32:35], s85 offen sc1\n" \
"v_mfma_f32_16x16x32_bf16 v[28:31], a[220:223], a[240:243], v[28:31]\n" \
"ds_read_b64_tr_b16 a[208:209], v240 offset:16896\n" \
"ds_read_b128 a[144:147], v244 offset:8192\n" \
"v_mfma_f32_16x16x32_bf16 v[60:63], a[220:223], a[244:247], v[60:63]\n" \
"s_add_u32 m0, s70, 1024\n" \
"ds_read_b64_tr_b16 a[210:211], v240 offset:18944\n" \
"ds_read_b128 a[148:151], v244 offset:10240\n" \
"v_mfma_f32_16x16x32_bf16 v[60:63], a[216:219], a[228:231], v[60:63]\n" \
"s_add_u32 s74, s69, 2097280\n" \
"v_mfma_f32_16x16x32_bf16 v[92:95], a[216:219], a[232:235], v[92:95]\n" \
"buffer_load_dwordx4 v246, s[20:23], s74 offen lds\n" \
"v_mfma_f32_16x16x32_bf16 v[92:95], a[220:223], a[248:251], v[92:95]\n" \
"ds_read_b64_tr_b16 a[212:213], v240 offset:25088\n" \
"ds_read_b128 a[152:155], v244 offset:12288\n" \
"v_mfma_f32_16x16x32_bf16 v[124:127], a[220:223], a[252:255], v[124:127]\n" \
"ds_read_b64_tr_b16 a[214:215], v240 offset:27136\n" \
"ds_read_b128 a[156:159], v244 offset:14336\n" \
"v_mfma_f32_16x16x32_bf16 v[124:127], a[216:219], a[236:239], v[124:127]\n" \
"s_waitcnt lgkmcnt(15)\n" \
"v_mfma_f32_16x16x32_bf16 a[0:3], a[224:227], a[192:195], a[0:3]\n" \
"ds_read_b64_tr_b16 a[216:217], v241 offset:16896\n" \
"v_mfma_f32_16x16x32_bf16 a[0:3], a[240:243], a[196:199], a[0:3]\n" \
"v_mfma_f32_16x16x32_bf16 a[4:7], a[244:247], a[196:199], a[4:7]\n" \
"ds_read_b64_tr_b16 a[218:219], v241 offset:18944\n" \
"v_mfma_f32_16x16x32_bf16 a[4:7], a[228:231], a[192:195], a[4:7]\n" \
"v_mfma_f32_16x16x32_bf16 a[64:67], a[232:235], a[192:195], a[64:67]\n" \
"ds_read_b64_tr_b16 a[220:221], v241 offset:25088\n" \
"v_mfma_f32_16x16x32_bf16 a[64:67], a[248:251], a[196:199], a[64:67]\n" \
"v_mfma_f32_16x16x32_bf16 a[68:71], a[252:255], a[196:199], a[68:71]\n" \
"ds_read_b64_tr_b16 a[222:223], v241 offset:27136\n" \
"v_mfma_f32_16x16x32_bf16 a[68:71], a[236:239], a[192:195], a[68:71]\n" \
"s_waitcnt lgkmcnt(12)\n" \
"v_mfma_f32_16x16x32_bf16 a[8:11], a[224:227], a[200:203], a[8:11]\n" \
"s_add_u32 m0, s70, 8192\n" \
"ds_read_b64_tr_b16 a[192:193], v240 offset:17408\n" \
"v_mfma_f32_16x16x32_bf16 a[8:11], a[240:243], a[204:207], a[8:11]\n" \
"s_add_u32 s74, s69, 2359296\n" \
"v_mfma_f32_16x16x32_bf16 a[12:15], a[244:247], a[204:207], a[12:15]\n" \
"s_lshl_b32 s89, %[wid], 10\n" \
"buffer_load_dwordx4 v246, s[20:23], s74 offen lds\n" \
"v_mfma_f32_16x16x32_bf16 a[12:15], a[228:231], a[200:203], a[12:15]\n" \
"s_add_u32 m0, s72, 8192\n" \
"s_add_u32 s74, s71, 640\n" \
"ds_read_b64_tr_b16 a[194:195], v240 offset:19456\n" \
"v_mfma_f32_16x16x32_bf16 a[72:75], a[232:235], a[200:203], a[72:75]\n" \
"s_add_u32 s89, s89, 159744\n" \
"ds_read_b64_tr_b16 a[196:197], v240 offset:25600\n" \
"v_add_u32 v236, s89, v248\n" \
"v_mfma_f32_16x16x32_bf16 a[72:75], a[248:251], a[204:207], a[72:75]\n" \
"s_waitcnt lgkmcnt(7)\n" \
"buffer_load_dwordx4 v249, s[16:19], s74 offen lds\n" \
"v_mfma_f32_16x16x32_bf16 a[76:79], a[252:255], a[204:207], a[76:79]\n" \
"s_mov_b32 s77, 0x80b583ec\n" \
"ds_read_b64_tr_b16 a[198:199], v240 offset:27648\n" \
"v_lshrrev_b32 v237, 3, v251\n" \
"v_mfma_f32_16x16x32_bf16 a[76:79], a[236:239], a[200:203], a[76:79]\n" \
"v_lshlrev_b32 v237, 5, v237\n" \
"v_mov_b32 v253, 0x61c88646\n" \
"v_mfma_f32_16x16x32_bf16 a[16:19], a[224:227], a[208:211], a[16:19]\n" \
"s_add_u32 m0, s72, 9216\n" \
"ds_read_b64_tr_b16 a[200:201], v241 offset:17408\n" \
"v_add_co_u32 v202, vcc, s77, v202\n" \
"v_mfma_f32_16x16x32_bf16 a[16:19], a[240:243], a[212:215], a[16:19]\n" \
"s_add_u32 s74, s71, 8832\n" \
"s_lshl_b32 s89, %[wid], 8\n" \
"s_add_u32 s89, s89, 159744\n" \
"v_mfma_f32_16x16x32_bf16 a[20:23], a[244:247], a[212:215], a[20:23]\n" \
"buffer_load_dwordx4 v250, s[16:19], s74 offen lds\n" \
"v_mfma_f32_16x16x32_bf16 a[20:23], a[228:231], a[208:211], a[20:23]\n" \
"s_or_b32 s85, %[bid], %[wid]\n" \
"ds_read_b64_tr_b16 a[202:203], v241 offset:19456\n" \
"v_add_u32 v237, s89, v237\n" \
"v_mfma_f32_16x16x32_bf16 a[80:83], a[232:235], a[208:211], a[80:83]\n" \
"s_cmp_eq_u32 s85, 0\n" \
"s_cselect_b32 s85, 720896, -1\n" \
"ds_read_b64_tr_b16 a[204:205], v241 offset:25600\n" \
"v_mfma_f32_16x16x32_bf16 a[80:83], a[248:251], a[212:215], a[80:83]\n" \
"s_lshr_b32 s93, %[bid], 6\n" \
"v_addc_co_u32 v203, vcc, v253, v203, vcc\n" \
"v_mov_b32 v253, 0\n" \
"v_mfma_f32_16x16x32_bf16 a[84:87], a[252:255], a[212:215], a[84:87]\n" \
"s_add_u32 m0, s70, 9216\n" \
"buffer_store_dwordx2 v[202:203], v253, s[32:35], s85 offen sc1\n" \
"v_mfma_f32_16x16x32_bf16 a[84:87], a[236:239], a[208:211], a[84:87]\n" \
"s_waitcnt lgkmcnt(7)\n" \
"s_add_u32 s74, s69, 2359424\n" \
"ds_read_b64_tr_b16 a[206:207], v241 offset:27648\n" \
"v_mfma_f32_16x16x32_bf16 a[24:27], a[224:227], a[216:219], a[24:27]\n" \
"s_cmp_eq_u32 s93, 2\n" \
"s_cselect_b32 s93, 1.0, 0\n" \
"ds_read_b64_tr_b16 a[208:209], v240 offset:17920\n" \
"v_mfma_f32_16x16x32_bf16 a[24:27], a[240:243], a[220:223], a[24:27]\n" \
"buffer_load_dwordx4 v246, s[20:23], s74 offen lds\n" \
"v_mfma_f32_16x16x32_bf16 a[28:31], a[244:247], a[220:223], a[28:31]\n" \
"s_add_u32 m0, s72, 10240\n" \
"s_add_u32 s74, s71, 17024\n" \
"ds_read_b64_tr_b16 a[210:211], v240 offset:19968\n" \
"v_mfma_f32_16x16x32_bf16 a[28:31], a[228:231], a[216:219], a[28:31]\n" \
"v_mfma_f32_16x16x32_bf16 a[88:91], a[232:235], a[216:219], a[88:91]\n" \
"buffer_load_dwordx4 v249, s[16:19], s74 offen lds\n" \
"v_mfma_f32_16x16x32_bf16 a[88:91], a[248:251], a[220:223], a[88:91]\n" \
"s_add_u32 m0, s72, 11264\n" \
"s_add_u32 s74, s71, 25216\n" \
"ds_read_b64_tr_b16 a[212:213], v240 offset:26112\n" \
"v_mfma_f32_16x16x32_bf16 a[92:95], a[252:255], a[220:223], a[92:95]\n" \
"ds_read_b64_tr_b16 a[214:215], v240 offset:28160\n" \
"v_mfma_f32_16x16x32_bf16 a[92:95], a[236:239], a[216:219], a[92:95]\n" \
"s_waitcnt lgkmcnt(8)\n" \
"buffer_load_dwordx4 v250, s[16:19], s74 offen lds\n" \
"v_mfma_f32_16x16x32_bf16 a[32:35], a[224:227], a[192:195], a[32:35]\n" \
"ds_read_b64_tr_b16 a[216:217], v241 offset:17920\n" \
"v_mfma_f32_16x16x32_bf16 a[32:35], a[240:243], a[196:199], a[32:35]\n" \
"v_mfma_f32_16x16x32_bf16 a[36:39], a[244:247], a[196:199], a[36:39]\n" \
"s_add_u32 m0, s72, 12288\n" \
"ds_read_b64_tr_b16 a[218:219], v241 offset:19968\n" \
"v_mfma_f32_16x16x32_bf16 a[36:39], a[228:231], a[192:195], a[36:39]\n" \
"s_add_u32 s74, s94, 33408\n" \
"ds_read_b64_tr_b16 a[220:221], v241 offset:26112\n" \
"v_mfma_f32_16x16x32_bf16 a[96:99], a[232:235], a[192:195], a[96:99]\n" \
"buffer_load_dwordx4 v249, s[16:19], s74 offen lds\n" \
"v_mfma_f32_16x16x32_bf16 a[96:99], a[248:251], a[196:199], a[96:99]\n" \
"ds_read_b64_tr_b16 a[222:223], v241 offset:28160\n" \
"v_mfma_f32_16x16x32_bf16 a[100:103], a[252:255], a[196:199], a[100:103]\n" \
"v_mfma_f32_16x16x32_bf16 a[100:103], a[236:239], a[192:195], a[100:103]\n" \
"s_waitcnt lgkmcnt(8)\n" \
"v_mfma_f32_16x16x32_bf16 a[40:43], a[224:227], a[200:203], a[40:43]\n" \
"s_add_u32 m0, s72, 13312\n" \
"v_mfma_f32_16x16x32_bf16 a[40:43], a[240:243], a[204:207], a[40:43]\n" \
"s_add_u32 s74, s94, 41600\n" \
"v_mfma_f32_16x16x32_bf16 a[44:47], a[244:247], a[204:207], a[44:47]\n" \
"buffer_load_dwordx4 v250, s[16:19], s74 offen lds\n" \
"v_mfma_f32_16x16x32_bf16 a[44:47], a[228:231], a[200:203], a[44:47]\n" \
"v_mfma_f32_16x16x32_bf16 a[104:107], a[232:235], a[200:203], a[104:107]\n" \
"s_waitcnt vmcnt(29) lgkmcnt(0)\n" \
"s_barrier\n" \
"s_add_u32 m0, s70, 16384\n" \
"v_mfma_f32_16x16x32_bf16 a[104:107], a[248:251], a[204:207], a[104:107]\n" \
"s_add_u32 s74, s69, 2621440\n" \
"ds_read_b64_tr_b16 a[192:193], v240 offset:32768\n" \
"ds_read_b64_tr_b16 a[194:195], v240 offset:34816\n" \
"v_mfma_f32_16x16x32_bf16 a[108:111], a[252:255], a[204:207], a[108:111]\n" \
"buffer_load_dwordx4 v246, s[20:23], s74 offen lds\n" \
"v_mfma_f32_16x16x32_bf16 a[108:111], a[236:239], a[200:203], a[108:111]\n" \
"s_add_u32 m0, s72, 14336\n" \
"ds_read_b64_tr_b16 a[196:197], v240 offset:40960\n" \
"ds_read_b64_tr_b16 a[198:199], v240 offset:43008\n" \
"v_mfma_f32_16x16x32_bf16 a[48:51], a[224:227], a[208:211], a[48:51]\n" \
"s_add_u32 s74, s94, 49792\n" \
"ds_read_b64_tr_b16 a[200:201], v241 offset:32768\n" \
"ds_read_b128 v[160:163], v254\n" \
"v_mfma_f32_16x16x32_bf16 a[48:51], a[240:243], a[212:215], a[48:51]\n" \
"s_lshl2_add_u32 s85, s65, %[wid]\n" \
"buffer_load_dwordx4 v249, s[16:19], s74 offen lds\n" \
"v_mfma_f32_16x16x32_bf16 a[52:55], a[244:247], a[212:215], a[52:55]\n" \
"s_lshl_b32 s85, s85, 15\n" \
"ds_read_b64_tr_b16 a[202:203], v241 offset:34816\n" \
"ds_read_b128 v[164:167], v254 offset:512\n" \
"v_mfma_f32_16x16x32_bf16 a[52:55], a[228:231], a[208:211], a[52:55]\n" \
"s_waitcnt vmcnt(13)\n" \
"s_lshl_b32 s86, s64, 10\n" \
"v_add_f32 v168, v168, v172\n" \
"v_mfma_f32_16x16x32_bf16 a[112:115], a[232:235], a[208:211], a[112:115]\n" \
"s_add_u32 m0, s72, 15360\n" \
"ds_read_b64_tr_b16 a[204:205], v241 offset:40960\n" \
"v_add_f32 v176, v176, v180\n" \
"v_mfma_f32_16x16x32_bf16 a[112:115], a[248:251], a[212:215], a[112:115]\n" \
"s_add_u32 s74, s94, 57984\n" \
"v_add_f32 v169, v169, v173\n" \
"v_add_f32 v177, v177, v181\n" \
"v_mfma_f32_16x16x32_bf16 a[116:119], a[252:255], a[212:215], a[116:119]\n" \
"s_add_u32 s85, s85, s86\n" \
"buffer_load_dwordx4 v250, s[16:19], s74 offen lds\n" \
"v_mfma_f32_16x16x32_bf16 a[116:119], a[236:239], a[208:211], a[116:119]\n" \
"buffer_load_dword v245, v211, s[28:31], s85 offen offset:256\n" \
"v_add_f32 v170, v170, v174\n" \
"v_mfma_f32_16x16x32_bf16 a[56:59], a[224:227], a[216:219], a[56:59]\n" \
"s_getpc_b64 s[96:97]\n" \
".Lpf1_%=:\n" \
"s_add_u32 s96, s96, .Lat_b_%=-.Lpf1_%=\n" \
"s_addc_u32 s97, s97, 0\n" \
"v_mfma_f32_16x16x32_bf16 a[56:59], a[240:243], a[220:223], a[56:59]\n" \
"s_and_b32 s97, s97, 0xffff\n" \
"ds_read_b64_tr_b16 a[206:207], v241 offset:43008\n" \
"ds_read_b64_tr_b16 a[208:209], v240 offset:33280\n" \
"v_mfma_f32_16x16x32_bf16 a[60:63], a[244:247], a[220:223], a[60:63]\n" \
"s_add_u32 m0, s70, 17408\n" \
"ds_read_b64_tr_b16 a[210:211], v240 offset:35328\n" \
"v_add_f32 v178, v178, v182\n" \
"v_mfma_f32_16x16x32_bf16 a[60:63], a[228:231], a[216:219], a[60:63]\n" \
"s_add_u32 s74, s69, 2621568\n" \
"v_add_f32 v171, v171, v175\n" \
"v_add_f32 v179, v179, v183\n" \
"v_mfma_f32_16x16x32_bf16 a[120:123], a[232:235], a[216:219], a[120:123]\n" \
"s_mov_b32 s98, .Lat_code_e-.Lat_b_%=\n" \
"buffer_load_dwordx4 v246, s[20:23], s74 offen lds\n" \
"v_mfma_f32_16x16x32_bf16 a[120:123], a[248:251], a[220:223], a[120:123]\n" \
"s_lshl2_add_u32 s85, s65, %[wid]\n" \
"ds_read_b64_tr_b16 a[212:213], v240 offset:41472\n" \
"v_add_f32 v168, v168, v176\n" \
"v_mfma_f32_16x16x32_bf16 a[124:127], a[252:255], a[220:223], a[124:127]\n" \
"s_and_b32 s85, s85, 31\n" \
"ds_read_b64_tr_b16 a[214:215], v240 offset:43520\n" \
"v_add_f32 v169, v169, v177\n" \
"v_mfma_f32_16x16x32_bf16 a[124:127], a[236:239], a[216:219], a[124:127]\n" \
"s_waitcnt lgkmcnt(10)\n" \
"v_add_f32 v170, v170, v178\n" \
"v_add_f32 v171, v171, v179\n" \
"v_mfma_f32_16x16x32_bf16 v[0:3], a[192:195], a[128:131], v[0:3]\n" \
"s_lshl_b32 s85, s85, 11\n" \
"buffer_load_dword v245, v209, s[96:99], s85 offen\n" \
"v_cvt_pk_bf16_f32 v172, v168, v169\n" \
"v_mfma_f32_16x16x32_bf16 v[0:3], a[196:199], a[144:147], v[0:3]\n" \
"ds_read_b64_tr_b16 a[216:217], v241 offset:33280\n" \
"v_cvt_pk_bf16_f32 v173, v170, v171\n" \
"v_mfma_f32_16x16x32_bf16 v[32:35], a[196:199], a[148:151], v[32:35]\n" \
"ds_read_b64_tr_b16 a[218:219], v241 offset:35328\n" \
"v_lshlrev_b32 v168, 16, v172\n" \
"v_mfma_f32_16x16x32_bf16 v[32:35], a[192:195], a[132:135], v[32:35]\n" \
"v_and_b32 v169, 0xffff0000, v172\n" \
"v_lshlrev_b32 v170, 16, v173\n" \
"v_mfma_f32_16x16x32_bf16 v[64:67], a[192:195], a[136:139], v[64:67]\n" \
"ds_read_b64_tr_b16 a[220:221], v241 offset:41472\n" \
"v_and_b32 v171, 0xffff0000, v173\n" \
"v_mfma_f32_16x16x32_bf16 v[64:67], a[196:199], a[152:155], v[64:67]\n" \
"v_add_f32 v168, s83, v168\n" \
"v_add_f32 v169, s83, v169\n" \
"v_mfma_f32_16x16x32_bf16 v[96:99], a[196:199], a[156:159], v[96:99]\n" \
"ds_read_b64_tr_b16 a[222:223], v241 offset:43520\n" \
"v_add_f32 v170, s83, v170\n" \
"v_mfma_f32_16x16x32_bf16 v[96:99], a[192:195], a[140:143], v[96:99]\n" \
"s_waitcnt lgkmcnt(8)\n" \
"v_add_f32 v171, s83, v171\n" \
"v_cvt_pk_bf16_f32 v172, v168, v169\n" \
"v_mfma_f32_16x16x32_bf16 v[4:7], a[200:203], a[128:131], v[4:7]\n" \
"s_add_u32 m0, s70, 24576\n" \
"ds_read_b64_tr_b16 a[192:193], v240 offset:33792\n" \
"v_cvt_pk_bf16_f32 v173, v170, v171\n" \
"v_mfma_f32_16x16x32_bf16 v[4:7], a[204:207], a[144:147], v[4:7]\n" \
"s_add_u32 s74, s69, 2883584\n" \
"v_lshlrev_b32 v168, 16, v172\n" \
"v_and_b32 v169, 0xffff0000, v172\n" \
"v_mfma_f32_16x16x32_bf16 v[36:39], a[204:207], a[148:151], v[36:39]\n" \
"buffer_load_dwordx4 v246, s[20:23], s74 offen lds\n" \
"v_mfma_f32_16x16x32_bf16 v[36:39], a[200:203], a[132:135], v[36:39]\n" \
"ds_read_b64_tr_b16 a[194:195], v240 offset:35840\n" \
"v_lshlrev_b32 v170, 16, v173\n" \
"v_mfma_f32_16x16x32_bf16 v[68:71], a[200:203], a[136:139], v[68:71]\n" \
"ds_read_b64_tr_b16 a[196:197], v240 offset:41984\n" \
"v_and_b32 v171, 0xffff0000, v173\n" \
"v_mfma_f32_16x16x32_bf16 v[68:71], a[204:207], a[152:155], v[68:71]\n" \
"s_waitcnt vmcnt(17)\n" \
"ds_write_b128 v236, v[168:171]\n" \
"v_add_f32_dpp v200, v200, v200 quad_perm:[1,0,3,2] row_mask:0xf bank_mask:0xf\n" \
"v_mfma_f32_16x16x32_bf16 v[100:103], a[204:207], a[156:159], v[100:103]\n" \
"ds_read_b64_tr_b16 a[198:199], v240 offset:44032\n" \
"v_lshrrev_b32 v152, 5, v251\n" \
"v_mfma_f32_16x16x32_bf16 v[100:103], a[200:203], a[140:143], v[100:103]\n" \
"s_waitcnt lgkmcnt(9)\n" \
"v_add_f32_dpp v200, v200, v200 quad_perm:[2,3,0,1] row_mask:0xf bank_mask:0xf\n" \
"v_cvt_pk_bf16_f32 v200, v200, v200\n" \
"v_mfma_f32_16x16x32_bf16 v[8:11], a[208:211], a[128:131], v[8:11]\n" \
"s_lshl3_add_u32 s79, s67, %[wid]\n" \
"ds_read_b64_tr_b16 a[200:201], v241 offset:33792\n" \
"ds_read_b128 a[160:163], v242 offset:0\n" \
"v_mfma_f32_16x16x32_bf16 v[8:11], a[212:215], a[144:147], v[8:11]\n" \
"s_lshl_b32 s79, s79, 14\n" \
"v_lshlrev_b32 v200, 16, v200\n" \
"v_and_b32 v252, 0x23, v251\n" \
"v_mfma_f32_16x16x32_bf16 v[40:43], a[212:215], a[148:151], v[40:43]\n" \
"s_add_u32 s79, s79, 1048576\n" \
"ds_read_b64_tr_b16 a[202:203], v241 offset:35840\n" \
"ds_read_b128 a[164:167], v242 offset:2048\n" \
"v_mfma_f32_16x16x32_bf16 v[40:43], a[208:211], a[132:135], v[40:43]\n" \
"s_lshl_b32 s90, %[wid], 15\n" \
"v_cmp_eq_u32 vcc, 0, v252\n" \
"v_add_f32 v200, s93, v200\n" \
"v_mfma_f32_16x16x32_bf16 v[72:75], a[208:211], a[136:139], v[72:75]\n" \
"s_sub_u32 s80, s79, s90\n" \
"ds_read_b64_tr_b16 a[204:205], v241 offset:41984\n" \
"ds_read_b128 a[168:171], v242 offset:4096\n" \
"v_mfma_f32_16x16x32_bf16 v[72:75], a[212:215], a[152:155], v[72:75]\n" \
"s_add_u32 s80, s80, 114688\n" \
"v_cvt_pk_bf16_f32 v200, v200, v200\n" \
"v_bfe_u32 v201, v251, 1, 3\n" \
"v_mfma_f32_16x16x32_bf16 v[104:107], a[212:215], a[156:159], v[104:107]\n" \
"s_add_u32 m0, s70, 25600\n" \
"ds_read_b64_tr_b16 a[206:207], v241 offset:44032\n" \
"ds_read_b128 a[172:175], v242 offset:6144\n" \
"v_mfma_f32_16x16x32_bf16 v[104:107], a[208:211], a[140:143], v[104:107]\n" \
"s_waitcnt lgkmcnt(13)\n" \
"v_and_or_b32 v201, v251, 16, v201\n" \
"v_mov_b32 v252, 1073741824\n" \
"v_mfma_f32_16x16x32_bf16 v[12:15], a[216:219], a[128:131], v[12:15]\n" \
"s_add_u32 s74, s69, 2883712\n" \
"buffer_load_dwordx4 v246, s[20:23], s74 offen lds\n" \
"v_mfma_f32_16x16x32_bf16 v[12:15], a[220:223], a[144:147], v[12:15]\n" \
"s_lshl_b32 s90, s67, 18\n" \
"ds_read_b64_tr_b16 a[208:209], v240 offset:34304\n" \
"v_cndmask_b32 v252, v252, v201, vcc\n" \
"v_mfma_f32_16x16x32_bf16 v[44:47], a[220:223], a[148:151], v[44:47]\n" \
"s_sub_u32 s90, 1966080, s90\n" \
"ds_read_b64_tr_b16 a[210:211], v240 offset:36352\n" \
"v_lshlrev_b32 v152, 10, v152\n" \
"v_mfma_f32_16x16x32_bf16 v[44:47], a[216:219], a[132:135], v[44:47]\n" \
"s_add_u32 s36, s79, s90\n" \
"v_bfe_u32 v255, v251, 3, 2\n" \
"v_lshl_or_b32 v152, v255, 8, v152\n" \
"v_mfma_f32_16x16x32_bf16 v[76:79], a[216:219], a[136:139], v[76:79]\n" \
"s_add_u32 s37, s80, s90\n" \
"ds_read_b64_tr_b16 a[212:213], v240 offset:42496\n" \
"ds_read_b128 a[176:179], v244 offset:0\n" \
"v_mfma_f32_16x16x32_bf16 v[76:79], a[220:223], a[152:155], v[76:79]\n" \
"s_mul_i32 s73, s68, 1179648\n" \
"v_and_b32 v255, 7, v251\n" \
"v_lshl_or_b32 v152, v255, 4, v152\n" \
"v_mfma_f32_16x16x32_bf16 v[108:111], a[220:223], a[156:159], v[108:111]\n" \
"s_add_u32 s73, s73, 3145728\n" \
"ds_read_b64_tr_b16 a[214:215], v240 offset:44544\n" \
"ds_read_b128 a[180:183], v244 offset:2048\n" \
"v_mfma_f32_16x16x32_bf16 v[108:111], a[216:219], a[140:143], v[108:111]\n" \
"s_waitcnt lgkmcnt(14)\n" \
"v_lshlrev_b32 v136, 16, v160\n" \
"v_and_b32 v137, 0xffff0000, v160\n" \
"v_mfma_f32_16x16x32_bf16 v[16:19], a[192:195], a[128:131], v[16:19]\n" \
"s_lshl1_add_u32 s87, s66, s67\n" \
"ds_read_b64_tr_b16 a[216:217], v241 offset:34304\n" \
"ds_read_b128 a[184:187], v244 offset:4096\n" \
"v_mfma_f32_16x16x32_bf16 v[16:19], a[196:199], a[144:147], v[16:19]\n" \
"s_and_b32 s87, s87, 7\n" \
"v_lshlrev_b32 v138, 16, v161\n" \
"v_and_b32 v139, 0xffff0000, v161\n" \
"v_mfma_f32_16x16x32_bf16 v[48:51], a[196:199], a[148:151], v[48:51]\n" \
"s_lshl3_add_u32 s87, s64, s87\n" \
"ds_read_b64_tr_b16 a[218:219], v241 offset:36352\n" \
"ds_read_b128 a[188:191], v244 offset:6144\n" \
"v_mfma_f32_16x16x32_bf16 v[48:51], a[192:195], a[132:135], v[48:51]\n" \
"s_lshr_b32 s90, %[wid], 1\n" \
"ds_read_b64_tr_b16 a[220:221], v241 offset:42496\n" \
"v_lshlrev_b32 v140, 16, v162\n" \
"v_mfma_f32_16x16x32_bf16 v[80:83], a[192:195], a[136:139], v[80:83]\n" \
"s_lshl1_add_u32 s87, s87, s90\n" \
"ds_read_b64_tr_b16 a[222:223], v241 offset:44544\n" \
"v_and_b32 v141, 0xffff0000, v162\n" \
"v_mfma_f32_16x16x32_bf16 v[80:83], a[196:199], a[152:155], v[80:83]\n" \
"s_lshl_b32 s87, s87, 15\n" \
"v_lshlrev_b32 v142, 16, v163\n" \
"v_and_b32 v143, 0xffff0000, v163\n" \
"v_mfma_f32_16x16x32_bf16 v[112:115], a[196:199], a[156:159], v[112:115]\n" \
"s_and_b32 s90, %[wid], 1\n" \
"v_lshlrev_b32 v144, 16, v164\n" \
"v_and_b32 v145, 0xffff0000, v164\n" \
"v_mfma_f32_16x16x32_bf16 v[112:115], a[192:195], a[140:143], v[112:115]\n" \
"s_waitcnt lgkmcnt(13)\n" \
"v_lshlrev_b32 v146, 16, v165\n" \
"v_and_b32 v147, 0xffff0000, v165\n" \
"v_mfma_f32_16x16x32_bf16 v[20:23], a[200:203], a[128:131], v[20:23]\n" \
"s_lshl1_add_u32 s90, s66, s90\n" \
"v_lshlrev_b32 v148, 16, v166\n" \
"v_and_b32 v149, 0xffff0000, v166\n" \
"v_mfma_f32_16x16x32_bf16 v[20:23], a[204:207], a[144:147], v[20:23]\n" \
"s_lshl_b32 s90, s90, 7\n" \
"v_lshlrev_b32 v150, 16, v167\n" \
"v_and_b32 v151, 0xffff0000, v167\n" \
"v_mfma_f32_16x16x32_bf16 v[52:55], a[204:207], a[148:151], v[52:55]\n" \
"s_add_u32 s87, s87, s90\n" \
"s_lshl_b32 s90, %[wid], 12\n" \
"s_add_u32 s91, s90, 131072\n" \
"v_mfma_f32_16x16x32_bf16 v[52:55], a[200:203], a[132:135], v[52:55]\n" \
"v_mfma_f32_16x16x32_bf16 v[84:87], a[200:203], a[136:139], v[84:87]\n" \
"s_waitcnt lgkmcnt(0)\n" \
"s_barrier\n" \
"s_add_u32 m0, s70, 32768\n" \
"v_mfma_f32_16x16x32_bf16 v[84:87], a[204:207], a[152:155], v[84:87]\n" \
"s_add_u32 s74, s69, 3145728\n" \
"ds_read_b128 v[184:187], v237 offset:2048\n" \
"ds_read_b128 v[188:191], v237 offset:2064\n" \
"v_mfma_f32_16x16x32_bf16 v[116:119], a[204:207], a[156:159], v[116:119]\n" \
"s_lshr_b32 s85, %[bid], 2\n" \
"buffer_load_dwordx4 v246, s[20:23], s74 offen lds\n" \
"v_mfma_f32_16x16x32_bf16 v[116:119], a[200:203], a[140:143], v[116:119]\n" \
"s_add_u32 m0, s72, 0\n" \
"ds_read_b128 v[168:171], v237 offset:0\n" \
"ds_read_b128 v[172:175], v237 offset:16\n" \
"v_mfma_f32_16x16x32_bf16 v[24:27], a[208:211], a[128:131], v[24:27]\n" \
"s_add_u32 s74, s71, 768\n" \
"ds_read_b128 v[192:195], v237 offset:3072\n" \
"ds_read_b128 v[196:199], v237 offset:3088\n" \
"v_mfma_f32_16x16x32_bf16 v[24:27], a[212:215], a[144:147], v[24:27]\n" \
"s_lshl_b32 s85, s85, 6\n" \
"buffer_load_dwordx4 v249, s[16:19], s74 offen lds\n" \
"v_mfma_f32_16x16x32_bf16 v[56:59], a[212:215], a[148:151], v[56:59]\n" \
"s_and_b32 s86, %[bid], 2\n" \
"ds_read_b128 v[176:179], v237 offset:1024\n" \
"ds_read_b128 v[180:183], v237 offset:1040\n" \
"v_mfma_f32_16x16x32_bf16 v[56:59], a[208:211], a[132:135], v[56:59]\n" \
"s_lshl2_add_u32 s85, s86, s85\n" \
"ds_read_b64_tr_b16 a[192:193], v240 offset:49152\n" \
"ds_read_b64_tr_b16 a[194:195], v240 offset:51200\n" \
"v_mfma_f32_16x16x32_bf16 v[88:91], a[208:211], a[136:139], v[88:91]\n" \
"s_add_u32 m0, s72, 1024\n" \
"ds_read_b64_tr_b16 a[196:197], v240 offset:57344\n" \
"ds_read_b64_tr_b16 a[198:199], v240 offset:59392\n" \
"v_mfma_f32_16x16x32_bf16 v[88:91], a[212:215], a[152:155], v[88:91]\n" \
"s_add_u32 s74, s71, 8960\n" \
"ds_read_b64_tr_b16 a[200:201], v241 offset:49152\n" \
"ds_read_b64_tr_b16 a[202:203], v241 offset:51200\n" \
"v_mfma_f32_16x16x32_bf16 v[120:123], a[212:215], a[156:159], v[120:123]\n" \
"s_and_b32 s86, %[bid], 1\n" \
"buffer_load_dwordx4 v250, s[16:19], s74 offen lds\n" \
"v_mfma_f32_16x16x32_bf16 v[120:123], a[208:211], a[140:143], v[120:123]\n" \
"s_lshl_b32 s86, s86, 5\n" \
"ds_read_b64_tr_b16 a[204:205], v241 offset:57344\n" \
"ds_read_b64_tr_b16 a[206:207], v241 offset:59392\n" \
"v_mfma_f32_16x16x32_bf16 v[28:31], a[216:219], a[128:131], v[28:31]\n" \
"s_add_u32 s85, s85, s86\n" \
"s_cmp_eq_u32 %[wid], 0\n" \
"s_cselect_b32 s85, s85, 1073741824\n" \
"v_mfma_f32_16x16x32_bf16 v[28:31], a[220:223], a[144:147], v[28:31]\n" \
"s_mov_b32 s84, 1040356334\n" \
"buffer_store_short v200, v252, s[32:35], s85 offen sc1\n" \
"v_and_b32 v212, 0x70, v246\n" \
"v_mfma_f32_16x16x32_bf16 v[60:63], a[220:223], a[148:151], v[60:63]\n" \
"s_add_u32 m0, s70, 33792\n" \
"ds_read_b64_tr_b16 a[208:209], v240 offset:49664\n" \
"ds_read_b64_tr_b16 a[210:211], v240 offset:51712\n" \
"v_mfma_f32_16x16x32_bf16 v[60:63], a[216:219], a[132:135], v[60:63]\n" \
"s_add_u32 s74, s69, 3145856\n" \
"v_bfe_u32 v213, v251, 2, 3\n" \
"v_lshl_or_b32 v212, v213, 10, v212\n" \
"v_mfma_f32_16x16x32_bf16 v[92:95], a[216:219], a[136:139], v[92:95]\n" \
"buffer_load_dwordx4 v246, s[20:23], s74 offen lds\n" \
"v_mfma_f32_16x16x32_bf16 v[92:95], a[220:223], a[152:155], v[92:95]\n" \
"s_add_u32 m0, s72, 2048\n" \
"ds_read_b64_tr_b16 a[212:213], v240 offset:57856\n" \
"v_lshrrev_b32 v213, 5, v251\n" \
"v_mfma_f32_16x16x32_bf16 v[124:127], a[220:223], a[156:159], v[124:127]\n" \
"s_add_u32 s74, s71, 17152\n" \
"ds_read_b64_tr_b16 a[214:215], v240 offset:59904\n" \
"v_lshlrev_b32 v213, 11, v213\n" \
"v_mfma_f32_16x16x32_bf16 v[124:127], a[216:219], a[140:143], v[124:127]\n" \
"s_waitcnt lgkmcnt(8)\n" \
"buffer_load_dwordx4 v249, s[16:19], s74 offen lds\n" \
"v_mfma_f32_16x16x32_bf16 a[0:3], a[128:131], a[192:195], a[0:3]\n" \
"s_add_u32 m0, s72, 3072\n" \
"ds_read_b64_tr_b16 a[216:217], v241 offset:49664\n" \
"v_and_b32 v214, 0x1ff, v240\n" \
"v_mfma_f32_16x16x32_bf16 a[0:3], a[144:147], a[196:199], a[0:3]\n" \
"s_add_u32 s74, s71, 25344\n" \
"v_add3_u32 v213, v213, v214, s91\n" \
"v_xor_b32 v214, 32, v213\n" \
"v_mfma_f32_16x16x32_bf16 a[4:7], a[148:151], a[196:199], a[4:7]\n" \
"buffer_load_dwordx4 v250, s[16:19], s74 offen lds\n" \
"v_mfma_f32_16x16x32_bf16 a[4:7], a[132:135], a[192:195], a[4:7]\n" \
"ds_read_b64_tr_b16 a[218:219], v241 offset:51712\n" \
"v_mul_f32 v136, v136, v184\n" \
"v_mfma_f32_16x16x32_bf16 a[64:67], a[136:139], a[192:195], a[64:67]\n" \
"ds_read_b64_tr_b16 a[220:221], v241 offset:57856\n" \
"v_mul_f32 v138, v138, v186\n" \
"v_mfma_f32_16x16x32_bf16 a[64:67], a[152:155], a[196:199], a[64:67]\n" \
"v_mul_f32 v137, v137, v185\n" \
"v_mul_f32 v139, v139, v187\n" \
"v_mfma_f32_16x16x32_bf16 a[68:71], a[156:159], a[196:199], a[68:71]\n" \
"s_add_u32 m0, s72, 4096\n" \
"ds_read_b64_tr_b16 a[222:223], v241 offset:59904\n" \
"v_cvt_pk_bf16_f32 v160, v136, v137\n" \
"v_mfma_f32_16x16x32_bf16 a[68:71], a[140:143], a[192:195], a[68:71]\n" \
"s_waitcnt lgkmcnt(8)\n" \
"v_cvt_pk_bf16_f32 v161, v138, v139\n" \
"v_lshlrev_b32 v136, 16, v160\n" \
"v_mfma_f32_16x16x32_bf16 a[8:11], a[128:131], a[200:203], a[8:11]\n" \
"s_add_u32 s74, s94, 33536\n" \
"buffer_load_dwordx4 v249, s[16:19], s74 offen lds\n" \
"v_mfma_f32_16x16x32_bf16 a[8:11], a[144:147], a[204:207], a[8:11]\n" \
"s_add_u32 m0, s70, 40960\n" \
"ds_read_b64_tr_b16 a[192:193], v240 offset:50176\n" \
"v_lshlrev_b32 v138, 16, v161\n" \
"v_mfma_f32_16x16x32_bf16 a[12:15], a[148:151], a[204:207], a[12:15]\n" \
"s_add_u32 s74, s69, 3407872\n" \
"ds_read_b64_tr_b16 a[194:195], v240 offset:52224\n" \
"v_and_b32 v137, 0xffff0000, v160\n" \
"v_mfma_f32_16x16x32_bf16 a[12:15], a[132:135], a[200:203], a[12:15]\n" \
"buffer_load_dwordx4 v246, s[20:23], s74 offen lds\n" \
"v_mfma_f32_16x16x32_bf16 a[72:75], a[136:139], a[200:203], a[72:75]\n" \
"s_add_u32 m0, s72, 5120\n" \
"ds_read_b64_tr_b16 a[196:197], v240 offset:58368\n" \
"v_and_b32 v139, 0xffff0000, v161\n" \
"v_mfma_f32_16x16x32_bf16 a[72:75], a[152:155], a[204:207], a[72:75]\n" \
"s_add_u32 s74, s94, 41728\n" \
"v_add_f32 v136, v136, v168\n" \
"v_add_f32 v138, v138, v170\n" \
"v_mfma_f32_16x16x32_bf16 a[76:79], a[156:159], a[204:207], a[76:79]\n" \
"s_lshr_b32 s85, s78, 4\n" \
"buffer_load_dwordx4 v250, s[16:19], s74 offen lds\n" \
"v_mfma_f32_16x16x32_bf16 a[76:79], a[140:143], a[200:203], a[76:79]\n" \
".Lst0_2_%=:\n" \
"s_cbranch_execz .Lst0_6_%=\n" \
"s_waitcnt lgkmcnt(7)\n" \
"ds_read_b64_tr_b16 a[198:199], v240 offset:60416\n" \
"v_add_f32 v137, v137, v169\n" \
"v_mfma_f32_16x16x32_bf16 a[16:19], a[128:131], a[208:211], a[16:19]\n" \
"s_lshl_b32 s85, s85, 14\n" \
"ds_read_b64_tr_b16 a[200:201], v241 offset:50176\n" \
"v_add_f32 v139, v139, v171\n" \
"v_mfma_f32_16x16x32_bf16 a[16:19], a[144:147], a[212:215], a[16:19]\n" \
"s_and_b32 s86, s78, 8\n" \
"v_cvt_pk_bf16_f32 v160, v136, v137\n" \
"v_cvt_pk_bf16_f32 v161, v138, v139\n" \
"v_mfma_f32_16x16x32_bf16 a[20:23], a[148:151], a[212:215], a[20:23]\n" \
"s_add_u32 m0, s72, 6144\n" \
"ds_read_b64_tr_b16 a[202:203], v241 offset:52224\n" \
"v_mul_f32 v140, v140, v188\n" \
"v_mfma_f32_16x16x32_bf16 a[20:23], a[132:135], a[208:211], a[20:23]\n" \
"s_add_u32 s74, s94, 49920\n" \
"v_mul_f32 v142, v142, v190\n" \
"v_mul_f32 v141, v141, v189\n" \
"v_mfma_f32_16x16x32_bf16 a[80:83], a[136:139], a[208:211], a[80:83]\n" \
"s_lshl4_add_u32 s85, s86, s85\n" \
"buffer_load_dwordx4 v249, s[16:19], s74 offen lds\n" \
"v_mfma_f32_16x16x32_bf16 a[80:83], a[152:155], a[212:215], a[80:83]\n" \
"s_add_u32 m0, s91, 0\n" \
"ds_read_b64_tr_b16 a[204:205], v241 offset:58368\n" \
"v_mul_f32 v143, v143, v191\n" \
"v_mfma_f32_16x16x32_bf16 a[84:87], a[156:159], a[212:215], a[84:87]\n" \
"s_add_u32 s74, s87, 0\n" \
"ds_read_b64_tr_b16 a[206:207], v241 offset:60416\n" \
"v_cvt_pk_bf16_f32 v162, v140, v141\n" \
"v_mfma_f32_16x16x32_bf16 a[84:87], a[140:143], a[208:211], a[84:87]\n" \
"s_waitcnt lgkmcnt(8)\n" \
"buffer_load_dwordx4 v212, s[48:51], s74 offen nt lds\n" \
"v_mfma_f32_16x16x32_bf16 a[24:27], a[128:131], a[216:219], a[24:27]\n" \
"s_add_u32 m0, s70, 41984\n" \
"ds_read_b64_tr_b16 a[208:209], v240 offset:50688\n" \
"v_cvt_pk_bf16_f32 v163, v142, v143\n" \
"v_mfma_f32_16x16x32_bf16 a[24:27], a[144:147], a[220:223], a[24:27]\n" \
"s_add_u32 s74, s69, 3408000\n" \
"v_lshlrev_b32 v140, 16, v162\n" \
"v_lshlrev_b32 v142, 16, v163\n" \
"v_mfma_f32_16x16x32_bf16 a[28:31], a[148:151], a[220:223], a[28:31]\n" \
"s_lshl_b32 s86, %[wid], 11\n" \
"buffer_load_dwordx4 v246, s[20:23], s74 offen lds\n" \
"v_mfma_f32_16x16x32_bf16 a[28:31], a[132:135], a[216:219], a[28:31]\n" \
"s_add_u32 m0, s72, 7168\n" \
"ds_read_b64_tr_b16 a[210:211], v240 offset:52736\n" \
"v_and_b32 v141, 0xffff0000, v162\n" \
"v_mfma_f32_16x16x32_bf16 a[88:91], a[136:139], a[216:219], a[88:91]\n" \
"s_add_u32 s74, s94, 58112\n" \
"ds_read_b64_tr_b16 a[212:213], v240 offset:58880\n" \
"v_and_b32 v143, 0xffff0000, v163\n" \
"v_mfma_f32_16x16x32_bf16 a[88:91], a[152:155], a[220:223], a[88:91]\n" \
"s_add_u32 s85, s85, s86\n" \
"buffer_load_dwordx4 v250, s[16:19], s74 offen lds\n" \
"v_mfma_f32_16x16x32_bf16 a[92:95], a[156:159], a[220:223], a[92:95]\n" \
"s_add_u32 m0, s91, 1024\n" \
"ds_read_b64_tr_b16 a[214:215], v240 offset:60928\n" \
"v_add_f32 v140, v140, v172\n" \
"v_mfma_f32_16x16x32_bf16 a[92:95], a[140:143], a[216:219], a[92:95]\n" \
"s_waitcnt lgkmcnt(8)\n" \
"v_add_f32 v142, v142, v174\n" \
"v_add_f32 v141, v141, v173\n" \
"v_mfma_f32_16x16x32_bf16 a[32:35], a[128:131], a[192:195], a[32:35]\n" \
"s_add_u32 s74, s87, 16384\n" \
"buffer_load_dwordx4 v212, s[48:51], s74 offen nt lds\n" \
"v_mfma_f32_16x16x32_bf16 a[32:35], a[144:147], a[196:199], a[32:35]\n" \
"s_add_u32 m0, s91, 2048\n" \
"ds_read_b64_tr_b16 a[216:217], v241 offset:50688\n" \
"v_add_f32 v143, v143, v175\n" \
"v_mfma_f32_16x16x32_bf16 a[36:39], a[148:151], a[196:199], a[36:39]\n" \
"s_add_u32 s74, s87, 8192\n" \
"ds_read_b64_tr_b16 a[218:219], v241 offset:52736\n" \
"v_cvt_pk_bf16_f32 v162, v140, v141\n" \
"v_mfma_f32_16x16x32_bf16 a[36:39], a[132:135], a[192:195], a[36:39]\n" \
"s_add_u32 s85, s85, 1048576\n" \
"buffer_load_dwordx4 v212, s[48:51], s74 offen nt lds\n" \
"v_mfma_f32_16x16x32_bf16 a[96:99], a[136:139], a[192:195], a[96:99]\n" \
"ds_read_b64_tr_b16 a[220:221], v241 offset:58880\n" \
"ds_read_b64_tr_b16 a[222:223], v241 offset:60928\n" \
"v_mfma_f32_16x16x32_bf16 a[96:99], a[152:155], a[196:199], a[96:99]\n" \
"s_waitcnt vmcnt(21)\n" \
"ds_read_b128 a[224:227], v242 offset:8192\n" \
"ds_read_b128 a[228:231], v242 offset:10240\n" \
"v_mfma_f32_16x16x32_bf16 a[100:103], a[156:159], a[196:199], a[100:103]\n" \
"s_add_u32 m0, s91, 3072\n" \
"ds_read_b128 a[232:235], v242 offset:12288\n" \
"ds_read_b128 a[236:239], v242 offset:14336\n" \
"v_mfma_f32_16x16x32_bf16 a[100:103], a[140:143], a[192:195], a[100:103]\n" \
"s_waitcnt lgkmcnt(12)\n" \
"v_cvt_pk_bf16_f32 v163, v142, v143\n" \
"v_mul_f32 v144, v144, v192\n" \
"v_mfma_f32_16x16x32_bf16 a[40:43], a[128:131], a[200:203], a[40:43]\n" \
"s_add_u32 s74, s87, 24576\n" \
"buffer_load_dwordx4 v212, s[48:51], s74 offen nt lds\n" \
"v_mfma_f32_16x16x32_bf16 a[40:43], a[144:147], a[204:207], a[40:43]\n" \
"v_mul_f32 v146, v146, v194\n" \
"v_mul_f32 v145, v145, v193\n" \
"v_mfma_f32_16x16x32_bf16 a[44:47], a[148:151], a[204:207], a[44:47]\n" \
"v_mul_f32 v147, v147, v195\n" \
"v_cvt_pk_bf16_f32 v164, v144, v145\n" \
"v_mfma_f32_16x16x32_bf16 a[44:47], a[132:135], a[200:203], a[44:47]\n" \
"buffer_store_dwordx4 v[160:163], v152, s[32:35], s85 offen sc1\n" \
"v_mfma_f32_16x16x32_bf16 a[104:107], a[136:139], a[200:203], a[104:107]\n" \
"s_waitcnt lgkmcnt(4)\n" \
"s_barrier\n" \
"ds_read_b128 a[240:243], v244 offset:8192\n" \
"v_mfma_f32_16x16x32_bf16 a[104:107], a[152:155], a[204:207], a[104:107]\n" \
"s_add_u32 m0, s70, 49152\n" \
"ds_read_b64_tr_b16 a[192:193], v240 offset:0\n" \
"ds_read_b64_tr_b16 a[194:195], v240 offset:2048\n" \
"v_mfma_f32_16x16x32_bf16 a[108:111], a[156:159], a[204:207], a[108:111]\n" \
"s_add_u32 s74, s69, 3670016\n" \
"buffer_load_dwordx4 v246, s[20:23], s74 offen lds\n" \
"v_mfma_f32_16x16x32_bf16 a[108:111], a[140:143], a[200:203], a[108:111]\n" \
"s_add_u32 s85, s85, 8192\n" \
"ds_read_b64_tr_b16 a[196:197], v240 offset:8192\n" \
"ds_read_b64_tr_b16 a[198:199], v240 offset:10240\n" \
"v_mfma_f32_16x16x32_bf16 a[48:51], a[128:131], a[208:211], a[48:51]\n" \
"ds_read_b64_tr_b16 a[200:201], v241 offset:0\n" \
"ds_read_b128 a[244:247], v244 offset:10240\n" \
"v_mfma_f32_16x16x32_bf16 a[48:51], a[144:147], a[212:215], a[48:51]\n" \
"ds_read_b128 a[248:251], v244 offset:12288\n" \
"v_cvt_pk_bf16_f32 v165, v146, v147\n" \
"v_mfma_f32_16x16x32_bf16 a[52:55], a[148:151], a[212:215], a[52:55]\n" \
"ds_read_b64_tr_b16 a[202:203], v241 offset:2048\n" \
"ds_read_b128 a[252:255], v244 offset:14336\n" \
"v_mfma_f32_16x16x32_bf16 a[52:55], a[132:135], a[208:211], a[52:55]\n" \
"v_lshlrev_b32 v144, 16, v164\n" \
"v_lshlrev_b32 v146, 16, v165\n" \
"v_mfma_f32_16x16x32_bf16 a[112:115], a[136:139], a[208:211], a[112:115]\n" \
"ds_read_b64_tr_b16 a[204:205], v241 offset:8192\n" \
"v_and_b32 v145, 0xffff0000, v164\n" \
"v_mfma_f32_16x16x32_bf16 a[112:115], a[152:155], a[212:215], a[112:115]\n" \
"v_and_b32 v147, 0xffff0000, v165\n" \
"v_add_f32 v144, v144, v176\n" \
"v_mfma_f32_16x16x32_bf16 a[116:119], a[156:159], a[212:215], a[116:119]\n" \
"ds_read_b64_tr_b16 a[206:207], v241 offset:10240\n" \
"v_add_f32 v146, v146, v178\n" \
"v_mfma_f32_16x16x32_bf16 a[116:119], a[140:143], a[208:211], a[116:119]\n" \
"v_add_f32 v145, v145, v177\n" \
"v_add_f32 v147, v147, v179\n" \
"v_mfma_f32_16x16x32_bf16 a[56:59], a[128:131], a[216:219], a[56:59]\n" \
"ds_read_b64_tr_b16 a[208:209], v240 offset:512\n" \
"v_cvt_pk_bf16_f32 v164, v144, v145\n" \
"v_mfma_f32_16x16x32_bf16 a[56:59], a[144:147], a[220:223], a[56:59]\n" \
"v_cvt_pk_bf16_f32 v165, v146, v147\n" \
"v_mul_f32 v148, v148, v196\n" \
"v_mfma_f32_16x16x32_bf16 a[60:63], a[148:151], a[220:223], a[60:63]\n" \
"s_add_u32 m0, s70, 50176\n" \
"ds_read_b64_tr_b16 a[210:211], v240 offset:2560\n" \
"v_mul_f32 v150, v150, v198\n" \
"v_mfma_f32_16x16x32_bf16 a[60:63], a[132:135], a[216:219], a[60:63]\n" \
"s_add_u32 s74, s69, 3670144\n" \
"v_mul_f32 v149, v149, v197\n" \
"v_mul_f32 v151, v151, v199\n" \
"v_mfma_f32_16x16x32_bf16 a[120:123], a[136:139], a[216:219], a[120:123]\n" \
"buffer_load_dwordx4 v246, s[20:23], s74 offen lds\n" \
"v_mfma_f32_16x16x32_bf16 a[120:123], a[152:155], a[220:223], a[120:123]\n" \
"ds_read_b64_tr_b16 a[212:213], v240 offset:8704\n" \
"v_cvt_pk_bf16_f32 v166, v148, v149\n" \
"v_mfma_f32_16x16x32_bf16 a[124:127], a[156:159], a[220:223], a[124:127]\n" \
"s_add_u32 m0, s72, 8192\n" \
"ds_read_b64_tr_b16 a[214:215], v240 offset:10752\n" \
"v_cvt_pk_bf16_f32 v167, v150, v151\n" \
"v_mfma_f32_16x16x32_bf16 a[124:127], a[140:143], a[216:219], a[124:127]\n" \
"s_waitcnt lgkmcnt(11)\n" \
"v_lshlrev_b32 v148, 16, v166\n" \
"v_lshlrev_b32 v150, 16, v167\n" \
  \
"v_mfma_f32_16x16x32_bf16 v[0:3], a[192:195], a[160:163], v[0:3]\n" \
"s_add_u32 s74, s71, 896\n" \
"ds_read_b64_tr_b16 a[216:217], v241 offset:512\n" \
"v_and_b32 v149, 0xffff0000, v166\n" \
"v_mfma_f32_16x16x32_bf16 v[0:3], a[196:199], a[176:179], v[0:3]\n" \
"s_waitcnt lgkmcnt(7)\n" \
"buffer_load_dwordx4 v249, s[16:19], s74 offen lds\n" \
"v_mfma_f32_16x16x32_bf16 v[32:35], a[196:199], a[180:183], v[32:35]\n" \
"ds_read_b64_tr_b16 a[218:219], v241 offset:2560\n" \
"v_and_b32 v151, 0xffff0000, v167\n" \
"v_mfma_f32_16x16x32_bf16 v[32:35], a[192:195], a[164:167], v[32:35]\n" \
"v_add_f32 v148, v148, v180\n" \
"v_add_f32 v150, v150, v182\n" \
"v_mfma_f32_16x16x32_bf16 v[64:67], a[192:195], a[168:171], v[64:67]\n" \
"s_add_u32 m0, s72, 9216\n" \
"ds_read_b64_tr_b16 a[220:221], v241 offset:8704\n" \
"v_add_f32 v149, v149, v181\n" \
"v_mfma_f32_16x16x32_bf16 v[64:67], a[196:199], a[184:187], v[64:67]\n" \
"s_add_u32 s74, s71, 9088\n" \
"v_add_f32 v151, v151, v183\n" \
"v_cvt_pk_bf16_f32 v166, v148, v149\n" \
"v_mfma_f32_16x16x32_bf16 v[96:99], a[196:199], a[188:191], v[96:99]\n" \
"buffer_load_dwordx4 v250, s[16:19], s74 offen lds\n" \
"v_mfma_f32_16x16x32_bf16 v[96:99], a[192:195], a[172:175], v[96:99]\n" \
"s_waitcnt lgkmcnt(7)\n" \
"ds_read_b64_tr_b16 a[222:223], v241 offset:10752\n" \
"v_cvt_pk_bf16_f32 v167, v150, v151\n" \
"v_mfma_f32_16x16x32_bf16 v[4:7], a[200:203], a[160:163], v[4:7]\n" \
"s_add_u32 m0, s70, 57344\n" \
"ds_read_b64_tr_b16 a[192:193], v240 offset:1024\n" \
"v_mfma_f32_16x16x32_bf16 v[4:7], a[204:207], a[176:179], v[4:7]\n" \
"s_add_u32 s74, s69, 3932160\n" \
"buffer_store_dwordx4 v[164:167], v152, s[32:35], s85 offen sc1\n" \
"v_mfma_f32_16x16x32_bf16 v[36:39], a[204:207], a[180:183], v[36:39]\n" \
"ds_read_b64_tr_b16 a[194:195], v240 offset:3072\n" \
"v_mfma_f32_16x16x32_bf16 v[36:39], a[200:203], a[164:167], v[36:39]\n" \
"v_mfma_f32_16x16x32_bf16 v[68:71], a[200:203], a[168:171], v[68:71]\n" \
"buffer_load_dwordx4 v246, s[20:23], s74 offen lds\n" \
"v_mfma_f32_16x16x32_bf16 v[68:71], a[204:207], a[184:187], v[68:71]\n" \
"s_add_u32 m0, s72, 10240\n" \
"s_add_u32 s74, s71, 17280\n" \
"ds_read_b64_tr_b16 a[196:197], v240 offset:9216\n" \
"v_mfma_f32_16x16x32_bf16 v[100:103], a[204:207], a[188:191], v[100:103]\n" \
"ds_read_b64_tr_b16 a[198:199], v240 offset:11264\n" \
"v_mfma_f32_16x16x32_bf16 v[100:103], a[200:203], a[172:175], v[100:103]\n" \
"s_waitcnt lgkmcnt(8)\n" \
"buffer_load_dwordx4 v249, s[16:19], s74 offen lds\n" \
"v_mfma_f32_16x16x32_bf16 v[8:11], a[208:211], a[160:163], v[8:11]\n" \
"s_add_u32 m0, s72, 11264\n" \
"ds_read_b64_tr_b16 a[200:201], v241 offset:1024\n" \
"v_mfma_f32_16x16x32_bf16 v[8:11], a[212:215], a[176:179], v[8:11]\n" \
"s_add_u32 s74, s71, 25472\n" \
"v_mfma_f32_16x16x32_bf16 v[40:43], a[212:215], a[180:183], v[40:43]\n" \
"buffer_load_dwordx4 v250, s[16:19], s74 offen lds\n" \
"v_mfma_f32_16x16x32_bf16 v[40:43], a[208:211], a[164:167], v[40:43]\n" \
"ds_read_b64_tr_b16 a[202:203], v241 offset:3072\n" \
"v_mfma_f32_16x16x32_bf16 v[72:75], a[208:211], a[168:171], v[72:75]\n" \
"ds_read_b64_tr_b16 a[204:205], v241 offset:9216\n" \
"v_mfma_f32_16x16x32_bf16 v[72:75], a[212:215], a[184:187], v[72:75]\n" \
"v_mfma_f32_16x16x32_bf16 v[104:107], a[212:215], a[188:191], v[104:107]\n" \
"s_add_u32 m0, s70, 58368\n" \
"ds_read_b64_tr_b16 a[206:207], v241 offset:11264\n" \
"v_mfma_f32_16x16x32_bf16 v[104:107], a[208:211], a[172:175], v[104:107]\n" \
"s_waitcnt lgkmcnt(8)\n" \
"s_add_u32 s74, s69, 3932288\n" \
"v_mfma_f32_16x16x32_bf16 v[12:15], a[216:219], a[160:163], v[12:15]\n" \
"buffer_load_dwordx4 v246, s[20:23], s74 offen lds\n" \
"v_mfma_f32_16x16x32_bf16 v[12:15], a[220:223], a[176:179], v[12:15]\n" \
"s_add_u32 m0, s72, 12288\n" \
"s_add_u32 s74, s94, 33664\n" \
"ds_read_b64_tr_b16 a[208:209], v240 offset:1536\n" \
"v_mfma_f32_16x16x32_bf16 v[44:47], a[220:223], a[180:183], v[44:47]\n" \
"ds_read_b64_tr_b16 a[210:211], v240 offset:3584\n" \
"v_mfma_f32_16x16x32_bf16 v[44:47], a[216:219], a[164:167], v[44:47]\n" \
"buffer_load_dwordx4 v249, s[16:19], s74 offen lds\n" \
"v_mfma_f32_16x16x32_bf16 v[76:79], a[216:219], a[168:171], v[76:79]\n" \
"s_add_u32 m0, s72, 13312\n" \
"ds_read_b64_tr_b16 a[212:213], v240 offset:9728\n" \
"v_mfma_f32_16x16x32_bf16 v[76:79], a[220:223], a[184:187], v[76:79]\n" \
"s_add_u32 s74, s94, 41856\n" \
"v_mfma_f32_16x16x32_bf16 v[108:111], a[220:223], a[188:191], v[108:111]\n" \
"buffer_load_dwordx4 v250, s[16:19], s74 offen lds\n" \
"v_mfma_f32_16x16x32_bf16 v[108:111], a[216:219], a[172:175], v[108:111]\n" \
"s_waitcnt lgkmcnt(7)\n" \
"ds_read_b64_tr_b16 a[214:215], v240 offset:11776\n" \
"v_mfma_f32_16x16x32_bf16 v[16:19], a[192:195], a[160:163], v[16:19]\n" \
"ds_read_b64_tr_b16 a[216:217], v241 offset:1536\n" \
"v_mfma_f32_16x16x32_bf16 v[16:19], a[196:199], a[176:179], v[16:19]\n" \
"v_mfma_f32_16x16x32_bf16 v[48:51], a[196:199], a[180:183], v[48:51]\n" \
"s_add_u32 m0, s72, 14336\n" \
"ds_read_b64_tr_b16 a[218:219], v241 offset:3584\n" \
"v_mfma_f32_16x16x32_bf16 v[48:51], a[192:195], a[164:167], v[48:51]\n" \
"s_add_u32 s74, s94, 50048\n" \
"ds_read_b64_tr_b16 a[220:221], v241 offset:9728\n" \
"v_mfma_f32_16x16x32_bf16 v[80:83], a[192:195], a[168:171], v[80:83]\n" \
"buffer_load_dwordx4 v249, s[16:19], s74 offen lds\n" \
"v_mfma_f32_16x16x32_bf16 v[80:83], a[196:199], a[184:187], v[80:83]\n" \
"ds_read_b64_tr_b16 a[222:223], v241 offset:11776\n" \
"v_mfma_f32_16x16x32_bf16 v[112:115], a[196:199], a[188:191], v[112:115]\n" \
"v_mfma_f32_16x16x32_bf16 v[112:115], a[192:195], a[172:175], v[112:115]\n" \
"s_waitcnt lgkmcnt(8)\n" \
"v_mfma_f32_16x16x32_bf16 v[20:23], a[200:203], a[160:163], v[20:23]\n" \
"s_add_u32 m0, s72, 15360\n" \
"v_mfma_f32_16x16x32_bf16 v[20:23], a[204:207], a[176:179], v[20:23]\n" \
"s_add_u32 s74, s94, 58240\n" \
"v_mfma_f32_16x16x32_bf16 v[52:55], a[204:207], a[180:183], v[52:55]\n" \
"buffer_load_dwordx4 v250, s[16:19], s74 offen lds\n" \
"v_mfma_f32_16x16x32_bf16 v[52:55], a[200:203], a[164:167], v[52:55]\n" \
"v_mfma_f32_16x16x32_bf16 v[84:87], a[200:203], a[168:171], v[84:87]\n" \
"s_waitcnt vmcnt(31) lgkmcnt(0)\n" \
"s_barrier\n" \
"s_add_u32 m0, s70, 0\n" \
"v_mfma_f32_16x16x32_bf16 v[84:87], a[204:207], a[184:187], v[84:87]\n" \
"s_add_u32 s74, s69, 3670016\n" \
"ds_read_b64_tr_b16 a[192:193], v240 offset:16384\n" \
"ds_read_b64_tr_b16 a[194:195], v240 offset:18432\n" \
"v_mfma_f32_16x16x32_bf16 v[116:119], a[204:207], a[188:191], v[116:119]\n" \
"buffer_load_dwordx4 v246, s[24:27], s74 offen lds\n" \
"v_mfma_f32_16x16x32_bf16 v[116:119], a[200:203], a[172:175], v[116:119]\n" \
"ds_read_b64_tr_b16 a[196:197], v240 offset:24576\n" \
"ds_read_b64_tr_b16 a[198:199], v240 offset:26624\n" \
"v_mfma_f32_16x16x32_bf16 v[24:27], a[208:211], a[160:163], v[24:27]\n" \
"s_waitcnt vmcnt(18)\n" \
"ds_read_b64_tr_b16 a[200:201], v241 offset:16384\n" \
"ds_read_b128 a[128:131], v242 offset:0\n" \
"v_mfma_f32_16x16x32_bf16 v[24:27], a[212:215], a[176:179], v[24:27]\n" \
"v_mfma_f32_16x16x32_bf16 v[56:59], a[212:215], a[180:183], v[56:59]\n" \
"ds_read_b64_tr_b16 a[202:203], v241 offset:18432\n" \
"ds_read_b128 a[132:135], v242 offset:2048\n" \
"v_mfma_f32_16x16x32_bf16 v[56:59], a[208:211], a[164:167], v[56:59]\n" \
"v_mfma_f32_16x16x32_bf16 v[88:91], a[208:211], a[168:171], v[88:91]\n" \
"ds_read_b64_tr_b16 a[204:205], v241 offset:24576\n" \
"ds_read_b128 a[136:139], v242 offset:4096\n" \
"v_mfma_f32_16x16x32_bf16 v[88:91], a[212:215], a[184:187], v[88:91]\n" \
"v_mfma_f32_16x16x32_bf16 v[120:123], a[212:215], a[188:191], v[120:123]\n" \
"ds_read_b64_tr_b16 a[206:207], v241 offset:26624\n" \
"ds_read_b128 a[140:143], v242 offset:6144\n" \
"v_mfma_f32_16x16x32_bf16 v[120:123], a[208:211], a[172:175], v[120:123]\n" \
"v_mfma_f32_16x16x32_bf16 v[28:31], a[216:219], a[160:163], v[28:31]\n" \
"ds_read_b64_tr_b16 a[208:209], v240 offset:16896\n" \
"v_mfma_f32_16x16x32_bf16 v[28:31], a[220:223], a[176:179], v[28:31]\n" \
"v_mfma_f32_16x16x32_bf16 v[60:63], a[220:223], a[180:183], v[60:63]\n" \
"s_add_u32 m0, s70, 1024\n" \
"ds_read_b64_tr_b16 a[210:211], v240 offset:18944\n" \
"v_mfma_f32_16x16x32_bf16 v[60:63], a[216:219], a[164:167], v[60:63]\n" \
"s_add_u32 s74, s69, 3670144\n" \
"v_mfma_f32_16x16x32_bf16 v[92:95], a[216:219], a[168:171], v[92:95]\n" \
"buffer_load_dwordx4 v246, s[24:27], s74 offen lds\n" \
"v_mfma_f32_16x16x32_bf16 v[92:95], a[220:223], a[184:187], v[92:95]\n" \
"ds_read_b64_tr_b16 a[212:213], v240 offset:25088\n" \
"ds_read_b128 a[144:147], v244 offset:0\n" \
"v_mfma_f32_16x16x32_bf16 v[124:127], a[220:223], a[188:191], v[124:127]\n" \
"ds_read_b64_tr_b16 a[214:215], v240 offset:27136\n" \
"ds_read_b128 a[148:151], v244 offset:2048\n" \
"v_mfma_f32_16x16x32_bf16 v[124:127], a[216:219], a[172:175], v[124:127]\n" \
"s_waitcnt lgkmcnt(14)\n" \
"v_mfma_f32_16x16x32_bf16 v[0:3], a[192:195], a[224:227], v[0:3]\n" \
"ds_read_b64_tr_b16 a[216:217], v241 offset:16896\n" \
"ds_read_b128 a[152:155], v244 offset:4096\n" \
"v_mfma_f32_16x16x32_bf16 v[0:3], a[196:199], a[240:243], v[0:3]\n" \
"v_mfma_f32_16x16x32_bf16 v[32:35], a[196:199], a[244:247], v[32:35]\n" \
"ds_read_b64_tr_b16 a[218:219], v241 offset:18944\n" \
"ds_read_b128 a[156:159], v244 offset:6144\n" \
"v_mfma_f32_16x16x32_bf16 v[32:35], a[192:195], a[228:231], v[32:35]\n" \
"v_mfma_f32_16x16x32_bf16 v[64:67], a[192:195], a[232:235], v[64:67]\n" \
"ds_read_b64_tr_b16 a[220:221], v241 offset:25088\n" \
"v_mfma_f32_16x16x32_bf16 v[64:67], a[196:199], a[248:251], v[64:67]\n" \
"v_mfma_f32_16x16x32_bf16 v[96:99], a[196:199], a[252:255], v[96:99]\n" \
"ds_read_b64_tr_b16 a[222:223], v241 offset:27136\n" \
"v_mfma_f32_16x16x32_bf16 v[96:99], a[192:195], a[236:239], v[96:99]\n" \
"s_waitcnt lgkmcnt(13)\n" \
"s_lshl_b32 s85, s67, 5\n" \
"v_mfma_f32_16x16x32_bf16 v[4:7], a[200:203], a[224:227], v[4:7]\n" \
"s_add_u32 m0, s70, 8192\n" \
"s_add_u32 s85, s85, s68\n" \
"ds_read_b64_tr_b16 a[192:193], v240 offset:17408\n" \
"v_mfma_f32_16x16x32_bf16 v[4:7], a[204:207], a[240:243], v[4:7]\n" \
"s_add_u32 s74, s69, 3932160\n" \
"s_lshl2_add_u32 s85, s85, %[wid]\n" \
"v_mfma_f32_16x16x32_bf16 v[36:39], a[204:207], a[244:247], v[36:39]\n" \
"s_lshl_b32 s85, s85, 2\n" \
"buffer_load_dwordx4 v246, s[24:27], s74 offen lds\n" \
"v_mfma_f32_16x16x32_bf16 v[36:39], a[200:203], a[228:231], v[36:39]\n" \
"s_add_u32 s85, s85, 90112\n" \
"ds_read_b64_tr_b16 a[194:195], v240 offset:19456\n" \
"v_mfma_f32_16x16x32_bf16 v[68:71], a[200:203], a[232:235], v[68:71]\n" \
"s_waitcnt vmcnt(11)\n" \
"buffer_store_dword v239, off, s[32:35], s85 sc1\n" \
"v_mfma_f32_16x16x32_bf16 v[68:71], a[204:207], a[248:251], v[68:71]\n" \
"ds_read_b64_tr_b16 a[196:197], v240 offset:25600\n" \
"v_mfma_f32_16x16x32_bf16 v[100:103], a[204:207], a[252:255], v[100:103]\n" \
"ds_read_b64_tr_b16 a[198:199], v240 offset:27648\n" \
"v_mfma_f32_16x16x32_bf16 v[100:103], a[200:203], a[236:239], v[100:103]\n" \
"s_waitcnt lgkmcnt(11)\n" \
"v_mfma_f32_16x16x32_bf16 v[8:11], a[208:211], a[224:227], v[8:11]\n" \
"ds_read_b64_tr_b16 a[200:201], v241 offset:17408\n" \
"v_mfma_f32_16x16x32_bf16 v[8:11], a[212:215], a[240:243], v[8:11]\n" \
"v_mfma_f32_16x16x32_bf16 v[40:43], a[212:215], a[244:247], v[40:43]\n" \
"ds_read_b64_tr_b16 a[202:203], v241 offset:19456\n" \
"v_mfma_f32_16x16x32_bf16 v[40:43], a[208:211], a[228:231], v[40:43]\n" \
"v_mfma_f32_16x16x32_bf16 v[72:75], a[208:211], a[232:235], v[72:75]\n" \
"ds_read_b64_tr_b16 a[204:205], v241 offset:25600\n" \
"v_mfma_f32_16x16x32_bf16 v[72:75], a[212:215], a[248:251], v[72:75]\n" \
"v_mfma_f32_16x16x32_bf16 v[104:107], a[212:215], a[252:255], v[104:107]\n" \
"s_add_u32 m0, s70, 9216\n" \
"ds_read_b64_tr_b16 a[206:207], v241 offset:27648\n" \
"v_mfma_f32_16x16x32_bf16 v[104:107], a[208:211], a[236:239], v[104:107]\n" \
"s_waitcnt lgkmcnt(8)\n" \
"s_add_u32 s74, s69, 3932288\n" \
"v_mfma_f32_16x16x32_bf16 v[12:15], a[216:219], a[224:227], v[12:15]\n" \
"buffer_load_dwordx4 v246, s[24:27], s74 offen lds\n" \
"v_mfma_f32_16x16x32_bf16 v[12:15], a[220:223], a[240:243], v[12:15]\n" \
"ds_read_b64_tr_b16 a[208:209], v240 offset:17920\n" \
"v_mfma_f32_16x16x32_bf16 v[44:47], a[220:223], a[244:247], v[44:47]\n" \
"ds_read_b64_tr_b16 a[210:211], v240 offset:19968\n" \
"v_mfma_f32_16x16x32_bf16 v[44:47], a[216:219], a[228:231], v[44:47]\n" \
"v_mfma_f32_16x16x32_bf16 v[76:79], a[216:219], a[232:235], v[76:79]\n" \
"ds_read_b64_tr_b16 a[212:213], v240 offset:26112\n" \
"v_mfma_f32_16x16x32_bf16 v[76:79], a[220:223], a[248:251], v[76:79]\n" \
"v_mfma_f32_16x16x32_bf16 v[108:111], a[220:223], a[252:255], v[108:111]\n" \
"ds_read_b64_tr_b16 a[214:215], v240 offset:28160\n" \
"v_mfma_f32_16x16x32_bf16 v[108:111], a[216:219], a[236:239], v[108:111]\n" \
"s_waitcnt lgkmcnt(8)\n" \
"v_mfma_f32_16x16x32_bf16 v[16:19], a[192:195], a[224:227], v[16:19]\n" \
"ds_read_b64_tr_b16 a[216:217], v241 offset:17920\n" \
"v_mfma_f32_16x16x32_bf16 v[16:19], a[196:199], a[240:243], v[16:19]\n" \
"v_mfma_f32_16x16x32_bf16 v[48:51], a[196:199], a[244:247], v[48:51]\n" \
"ds_read_b64_tr_b16 a[218:219], v241 offset:19968\n" \
"v_mfma_f32_16x16x32_bf16 v[48:51], a[192:195], a[228:231], v[48:51]\n" \
"ds_read_b64_tr_b16 a[220:221], v241 offset:26112\n" \
"v_mfma_f32_16x16x32_bf16 v[80:83], a[192:195], a[232:235], v[80:83]\n" \
"ds_read_b64_tr_b16 a[222:223], v241 offset:28160\n" \
"v_mfma_f32_16x16x32_bf16 v[80:83], a[196:199], a[248:251], v[80:83]\n" \
"v_mfma_f32_16x16x32_bf16 v[112:115], a[196:199], a[252:255], v[112:115]\n" \
"v_mfma_f32_16x16x32_bf16 v[112:115], a[192:195], a[236:239], v[112:115]\n" \
"s_waitcnt lgkmcnt(8)\n" \
"v_mfma_f32_16x16x32_bf16 v[20:23], a[200:203], a[224:227], v[20:23]\n" \
"v_mfma_f32_16x16x32_bf16 v[20:23], a[204:207], a[240:243], v[20:23]\n" \
"v_mfma_f32_16x16x32_bf16 v[52:55], a[204:207], a[244:247], v[52:55]\n" \
"v_mfma_f32_16x16x32_bf16 v[52:55], a[200:203], a[228:231], v[52:55]\n" \
"v_mfma_f32_16x16x32_bf16 v[84:87], a[200:203], a[232:235], v[84:87]\n" \
"s_waitcnt lgkmcnt(0)\n" \
"s_barrier\n" \
"ds_read_b64_tr_b16 v[128:129], v213 offset:0\n" \
"v_mfma_f32_16x16x32_bf16 v[84:87], a[204:207], a[248:251], v[84:87]\n" \
"s_add_u32 m0, s70, 16384\n" \
"ds_read_b64_tr_b16 a[192:193], v240 offset:32768\n" \
"ds_read_b64_tr_b16 a[194:195], v240 offset:34816\n" \
"v_mfma_f32_16x16x32_bf16 v[116:119], a[204:207], a[252:255], v[116:119]\n" \
"s_add_u32 s74, s69, 2097152\n" \
"buffer_load_dwordx4 v246, s[24:27], s74 offen lds\n" \
"v_mfma_f32_16x16x32_bf16 v[116:119], a[200:203], a[236:239], v[116:119]\n" \
"ds_read_b64_tr_b16 a[196:197], v240 offset:40960\n" \
"ds_read_b64_tr_b16 a[198:199], v240 offset:43008\n" \
"v_mfma_f32_16x16x32_bf16 v[24:27], a[208:211], a[224:227], v[24:27]\n" \
"ds_read_b64_tr_b16 a[200:201], v241 offset:32768\n" \
"ds_read_b64_tr_b16 v[130:131], v213 offset:1024\n" \
"v_mfma_f32_16x16x32_bf16 v[24:27], a[212:215], a[240:243], v[24:27]\n" \
"ds_read_b64_tr_b16 v[132:133], v214 offset:0\n" \
"v_mfma_f32_16x16x32_bf16 v[56:59], a[212:215], a[244:247], v[56:59]\n" \
"ds_read_b64_tr_b16 a[202:203], v241 offset:34816\n" \
"ds_read_b64_tr_b16 v[134:135], v214 offset:1024\n" \
"v_mfma_f32_16x16x32_bf16 v[56:59], a[208:211], a[228:231], v[56:59]\n" \
"v_mfma_f32_16x16x32_bf16 v[88:91], a[208:211], a[232:235], v[88:91]\n" \
"ds_read_b64_tr_b16 a[204:205], v241 offset:40960\n" \
"ds_read_b64_tr_b16 v[136:137], v213 offset:512\n" \
"v_mfma_f32_16x16x32_bf16 v[88:91], a[212:215], a[248:251], v[88:91]\n" \
"v_mfma_f32_16x16x32_bf16 v[120:123], a[212:215], a[252:255], v[120:123]\n" \
"ds_read_b64_tr_b16 a[206:207], v241 offset:43008\n" \
"ds_read_b64_tr_b16 v[138:139], v213 offset:1536\n" \
"v_mfma_f32_16x16x32_bf16 v[120:123], a[208:211], a[236:239], v[120:123]\n" \
"v_mfma_f32_16x16x32_bf16 v[28:31], a[216:219], a[224:227], v[28:31]\n" \
"ds_read_b64_tr_b16 a[208:209], v240 offset:33280\n" \
"ds_read_b64_tr_b16 v[140:141], v214 offset:512\n" \
"v_mfma_f32_16x16x32_bf16 v[28:31], a[220:223], a[240:243], v[28:31]\n" \
"v_mfma_f32_16x16x32_bf16 v[60:63], a[220:223], a[244:247], v[60:63]\n" \
"s_add_u32 m0, s70, 17408\n" \
"ds_read_b64_tr_b16 a[210:211], v240 offset:35328\n" \
"ds_read_b64_tr_b16 v[142:143], v214 offset:1536\n" \
"v_mfma_f32_16x16x32_bf16 v[60:63], a[216:219], a[228:231], v[60:63]\n" \
"s_add_u32 s74, s69, 2097280\n" \
"s_lshl2_add_u32 s85, s67, %[wid]\n" \
"v_mfma_f32_16x16x32_bf16 v[92:95], a[216:219], a[232:235], v[92:95]\n" \
"s_lshl_b32 s85, s85, 16\n" \
"buffer_load_dwordx4 v246, s[24:27], s74 offen lds\n" \
"v_mfma_f32_16x16x32_bf16 v[92:95], a[220:223], a[248:251], v[92:95]\n" \
"s_lshl_b32 s86, s68, 8\n" \
"ds_read_b64_tr_b16 a[212:213], v240 offset:41472\n" \
"v_mfma_f32_16x16x32_bf16 v[124:127], a[220:223], a[252:255], v[124:127]\n" \
"s_add_u32 s85, s85, s86\n" \
"ds_read_b64_tr_b16 a[214:215], v240 offset:43520\n" \
"v_mfma_f32_16x16x32_bf16 v[124:127], a[216:219], a[236:239], v[124:127]\n" \
"s_waitcnt lgkmcnt(15)\n" \
"s_add_u32 s85, s85, 0\n" \
"v_mfma_f32_16x16x32_bf16 v[0:3], a[192:195], a[128:131], v[0:3]\n" \
"buffer_load_dword v245, v208, s[28:31], s85 offen\n" \
"v_mfma_f32_16x16x32_bf16 v[0:3], a[196:199], a[144:147], v[0:3]\n" \
"s_lshl2_add_u32 s85, s67, %[wid]\n" \
"s_lshl_b32 s85, s85, 16\n" \
"ds_read_b64_tr_b16 a[216:217], v241 offset:33280\n" \
"v_mfma_f32_16x16x32_bf16 v[32:35], a[196:199], a[148:151], v[32:35]\n" \
"s_lshl_b32 s86, s68, 8\n" \
"s_add_u32 s85, s85, s86\n" \
"ds_read_b64_tr_b16 a[218:219], v241 offset:35328\n" \
"v_mfma_f32_16x16x32_bf16 v[32:35], a[192:195], a[132:135], v[32:35]\n" \
"s_add_u32 s85, s85, 2097152\n" \
"v_mfma_f32_16x16x32_bf16 v[64:67], a[192:195], a[136:139], v[64:67]\n" \
"buffer_load_dword v245, v208, s[28:31], s85 offen\n" \
"v_mfma_f32_16x16x32_bf16 v[64:67], a[196:199], a[152:155], v[64:67]\n" \
"s_mov_b32 s38, 0x45442120\n" \
"s_mov_b32 s39, 0x47234622\n" \
"ds_read_b64_tr_b16 a[220:221], v241 offset:41472\n" \
"v_mfma_f32_16x16x32_bf16 v[96:99], a[196:199], a[156:159], v[96:99]\n" \
"s_lshl_b32 s85, s67, 3\n" \
"s_lshr_b64 s[38:39], s[38:39], s85\n" \
"ds_read_b64_tr_b16 a[222:223], v241 offset:43520\n" \
"v_mfma_f32_16x16x32_bf16 v[96:99], a[192:195], a[140:143], v[96:99]\n" \
"s_waitcnt lgkmcnt(11)\n" \
"s_and_b32 s85, s38, 0xff\n" \
"s_lshl_b32 s85, s85, 14\n" \
"v_mfma_f32_16x16x32_bf16 v[4:7], a[200:203], a[128:131], v[4:7]\n" \
"s_add_u32 m0, s70, 24576\n" \
"s_add_u32 s85, s85, s73\n" \
"ds_read_b64_tr_b16 a[192:193], v240 offset:33792\n" \
"v_mfma_f32_16x16x32_bf16 v[4:7], a[204:207], a[144:147], v[4:7]\n" \
"s_add_u32 s74, s69, 2359296\n" \
"s_add_u32 s92, s85, s90\n" \
"v_mfma_f32_16x16x32_bf16 v[36:39], a[204:207], a[148:151], v[36:39]\n" \
"buffer_load_dwordx4 v246, s[24:27], s74 offen lds\n" \
"v_mfma_f32_16x16x32_bf16 v[36:39], a[200:203], a[132:135], v[36:39]\n" \
"s_waitcnt vmcnt(10)\n" \
"ds_read_b64_tr_b16 a[194:195], v240 offset:35840\n" \
"ds_read_b128 v[192:195], v242 offset:8192\n" \
"v_mfma_f32_16x16x32_bf16 v[68:71], a[200:203], a[136:139], v[68:71]\n" \
"ds_read_b64_tr_b16 a[196:197], v240 offset:41984\n" \
"ds_read_b128 v[196:199], v242 offset:10240\n" \
"v_mfma_f32_16x16x32_bf16 v[68:71], a[204:207], a[152:155], v[68:71]\n" \
"s_waitcnt lgkmcnt(11)\n" \
"buffer_store_dwordx4 v[128:131], v248, s[32:35], s92 offen offset:0 sc1\n" \
"v_mfma_f32_16x16x32_bf16 v[100:103], a[204:207], a[156:159], v[100:103]\n" \
"ds_read_b64_tr_b16 a[198:199], v240 offset:44032\n" \
"ds_read_b128 v[200:203], v242 offset:12288\n" \
"v_mfma_f32_16x16x32_bf16 v[100:103], a[200:203], a[140:143], v[100:103]\n" \
"s_waitcnt lgkmcnt(11)\n" \
"v_mfma_f32_16x16x32_bf16 v[8:11], a[208:211], a[128:131], v[8:11]\n" \
"ds_read_b64_tr_b16 a[200:201], v241 offset:33792\n" \
"ds_read_b128 v[204:207], v242 offset:14336\n" \
"v_mfma_f32_16x16x32_bf16 v[8:11], a[212:215], a[144:147], v[8:11]\n" \
"v_mfma_f32_16x16x32_bf16 v[40:43], a[212:215], a[148:151], v[40:43]\n" \
"ds_read_b64_tr_b16 a[202:203], v241 offset:35840\n" \
"v_mfma_f32_16x16x32_bf16 v[40:43], a[208:211], a[132:135], v[40:43]\n" \
"v_mfma_f32_16x16x32_bf16 v[72:75], a[208:211], a[136:139], v[72:75]\n" \
"buffer_store_dwordx4 v[132:135], v248, s[32:35], s92 offen offset:1024 sc1\n" \
"v_mfma_f32_16x16x32_bf16 v[72:75], a[212:215], a[152:155], v[72:75]\n" \
"ds_read_b64_tr_b16 a[204:205], v241 offset:41984\n" \
"ds_read_b128 v[208:211], v244 offset:8192\n" \
"v_mfma_f32_16x16x32_bf16 v[104:107], a[212:215], a[156:159], v[104:107]\n" \
"s_add_u32 m0, s70, 25600\n" \
"ds_read_b64_tr_b16 a[206:207], v241 offset:44032\n" \
"ds_read_b128 v[212:215], v244 offset:10240\n" \
"v_mfma_f32_16x16x32_bf16 v[104:107], a[208:211], a[140:143], v[104:107]\n" \
"s_waitcnt lgkmcnt(14)\n" \
"s_add_u32 s74, s69, 2359424\n" \
"v_mfma_f32_16x16x32_bf16 v[12:15], a[216:219], a[128:131], v[12:15]\n" \
"buffer_load_dwordx4 v246, s[24:27], s74 offen lds\n" \
"v_mfma_f32_16x16x32_bf16 v[12:15], a[220:223], a[144:147], v[12:15]\n" \
"ds_read_b64_tr_b16 a[208:209], v240 offset:34304\n" \
"ds_read_b128 v[216:219], v244 offset:12288\n" \
"v_mfma_f32_16x16x32_bf16 v[44:47], a[220:223], a[148:151], v[44:47]\n" \
"ds_read_b64_tr_b16 a[210:211], v240 offset:36352\n" \
"ds_read_b128 v[220:223], v244 offset:14336\n" \
"v_mfma_f32_16x16x32_bf16 v[44:47], a[216:219], a[132:135], v[44:47]\n" \
"v_mfma_f32_16x16x32_bf16 v[76:79], a[216:219], a[136:139], v[76:79]\n" \
"ds_read_b64_tr_b16 a[212:213], v240 offset:42496\n" \
"v_mfma_f32_16x16x32_bf16 v[76:79], a[220:223], a[152:155], v[76:79]\n" \
"v_mfma_f32_16x16x32_bf16 v[108:111], a[220:223], a[156:159], v[108:111]\n" \
"buffer_store_dwordx4 v[136:139], v248, s[32:35], s92 offen offset:2048 sc1\n" \
"v_mfma_f32_16x16x32_bf16 v[108:111], a[216:219], a[140:143], v[108:111]\n" \
"s_waitcnt lgkmcnt(13)\n" \
"ds_read_b64_tr_b16 a[214:215], v240 offset:44544\n" \
"v_mfma_f32_16x16x32_bf16 v[16:19], a[192:195], a[128:131], v[16:19]\n" \
"ds_read_b64_tr_b16 a[216:217], v241 offset:34304\n" \
"v_mfma_f32_16x16x32_bf16 v[16:19], a[196:199], a[144:147], v[16:19]\n" \
"v_mfma_f32_16x16x32_bf16 v[48:51], a[196:199], a[148:151], v[48:51]\n" \
"ds_read_b64_tr_b16 a[218:219], v241 offset:36352\n" \
"v_mfma_f32_16x16x32_bf16 v[48:51], a[192:195], a[132:135], v[48:51]\n" \
"ds_read_b64_tr_b16 a[220:221], v241 offset:42496\n" \
"v_mfma_f32_16x16x32_bf16 v[80:83], a[192:195], a[136:139], v[80:83]\n" \
".Lst0_3_%=:\n" \
"s_cbranch_execz .Lst0_7_%=\n" \
"ds_read_b64_tr_b16 a[222:223], v241 offset:44544\n" \
"v_mfma_f32_16x16x32_bf16 v[80:83], a[196:199], a[152:155], v[80:83]\n" \
"v_mfma_f32_16x16x32_bf16 v[112:115], a[196:199], a[156:159], v[112:115]\n" \
"v_mfma_f32_16x16x32_bf16 v[112:115], a[192:195], a[140:143], v[112:115]\n" \
"s_waitcnt lgkmcnt(11)\n" \
"v_mfma_f32_16x16x32_bf16 v[20:23], a[200:203], a[128:131], v[20:23]\n" \
"buffer_store_dwordx4 v[140:143], v248, s[32:35], s92 offen offset:3072 sc1\n" \
"v_mfma_f32_16x16x32_bf16 v[20:23], a[204:207], a[144:147], v[20:23]\n" \
"v_mfma_f32_16x16x32_bf16 v[52:55], a[204:207], a[148:151], v[52:55]\n" \
"v_mfma_f32_16x16x32_bf16 v[52:55], a[200:203], a[132:135], v[52:55]\n" \
"v_mfma_f32_16x16x32_bf16 v[84:87], a[200:203], a[136:139], v[84:87]\n" \
"s_waitcnt lgkmcnt(0)\n" \
"s_barrier\n" \
"s_add_u32 m0, s70, 32768\n" \
"v_mfma_f32_16x16x32_bf16 v[84:87], a[204:207], a[152:155], v[84:87]\n" \
"s_add_u32 s74, s69, 2621440\n" \
"ds_read_b64_tr_b16 a[192:193], v240 offset:49152\n" \
"ds_read_b64_tr_b16 a[194:195], v240 offset:51200\n" \
"v_mfma_f32_16x16x32_bf16 v[116:119], a[204:207], a[156:159], v[116:119]\n" \
"buffer_load_dwordx4 v246, s[24:27], s74 offen lds\n" \
"v_mfma_f32_16x16x32_bf16 v[116:119], a[200:203], a[140:143], v[116:119]\n" \
"ds_read_b64_tr_b16 a[196:197], v240 offset:57344\n" \
"ds_read_b64_tr_b16 a[198:199], v240 offset:59392\n" \
"v_mfma_f32_16x16x32_bf16 v[24:27], a[208:211], a[128:131], v[24:27]\n" \
"ds_read_b64_tr_b16 a[200:201], v241 offset:49152\n" \
"v_mfma_f32_16x16x32_bf16 v[24:27], a[212:215], a[144:147], v[24:27]\n" \
"v_mfma_f32_16x16x32_bf16 v[56:59], a[212:215], a[148:151], v[56:59]\n" \
"ds_read_b64_tr_b16 a[202:203], v241 offset:51200\n" \
"v_mfma_f32_16x16x32_bf16 v[56:59], a[208:211], a[132:135], v[56:59]\n" \
"v_mfma_f32_16x16x32_bf16 v[88:91], a[208:211], a[136:139], v[88:91]\n" \
"ds_read_b64_tr_b16 a[204:205], v241 offset:57344\n" \
"v_mfma_f32_16x16x32_bf16 v[88:91], a[212:215], a[152:155], v[88:91]\n" \
"v_mfma_f32_16x16x32_bf16 v[120:123], a[212:215], a[156:159], v[120:123]\n" \
"ds_read_b64_tr_b16 a[206:207], v241 offset:59392\n" \
"v_mfma_f32_16x16x32_bf16 v[120:123], a[208:211], a[140:143], v[120:123]\n" \
"v_mfma_f32_16x16x32_bf16 v[28:31], a[216:219], a[128:131], v[28:31]\n" \
"ds_read_b64_tr_b16 a[208:209], v240 offset:49664\n" \
"v_mfma_f32_16x16x32_bf16 v[28:31], a[220:223], a[144:147], v[28:31]\n" \
"v_mfma_f32_16x16x32_bf16 v[60:63], a[220:223], a[148:151], v[60:63]\n" \
"s_add_u32 m0, s70, 33792\n" \
"ds_read_b64_tr_b16 a[210:211], v240 offset:51712\n" \
"v_mfma_f32_16x16x32_bf16 v[60:63], a[216:219], a[132:135], v[60:63]\n" \
"s_add_u32 s74, s69, 2621568\n" \
"v_mfma_f32_16x16x32_bf16 v[92:95], a[216:219], a[136:139], v[92:95]\n" \
"s_lshr_b32 s71, s71, 2\n" \
"buffer_load_dwordx4 v246, s[24:27], s74 offen lds\n" \
"v_mfma_f32_16x16x32_bf16 v[92:95], a[220:223], a[152:155], v[92:95]\n" \
"s_add_u32 s71, s71, s73\n" \
"s_add_u32 s94, s94, 32768\n" \
"ds_read_b64_tr_b16 a[212:213], v240 offset:57856\n" \
"v_mfma_f32_16x16x32_bf16 v[124:127], a[220:223], a[156:159], v[124:127]\n" \
"s_lshr_b32 s94, s94, 2\n" \
"s_add_u32 s94, s94, s73\n" \
"ds_read_b64_tr_b16 a[214:215], v240 offset:59904\n" \
"v_mfma_f32_16x16x32_bf16 v[124:127], a[216:219], a[140:143], v[124:127]\n" \
"s_waitcnt lgkmcnt(8)\n" \
"s_lshl_b32 s72, %[wid], 13\n" \
"v_mfma_f32_16x16x32_bf16 v[0:3], a[192:195], v[192:195], v[0:3]\n" \
"ds_read_b64_tr_b16 a[216:217], v241 offset:49664\n" \
"v_mfma_f32_16x16x32_bf16 v[0:3], a[196:199], v[208:211], v[0:3]\n" \
"v_mfma_f32_16x16x32_bf16 v[32:35], a[196:199], v[212:215], v[32:35]\n" \
"ds_read_b64_tr_b16 a[218:219], v241 offset:51712\n" \
"v_add_u32 v244, s72, v248\n" \
"v_mfma_f32_16x16x32_bf16 v[32:35], a[192:195], v[196:199], v[32:35]\n" \
"v_add_u32 v249, 0x10000, v244\n" \
"v_mov_b32 v250, 0\n" \
"v_mfma_f32_16x16x32_bf16 v[64:67], a[192:195], v[200:203], v[64:67]\n" \
"ds_read_b64_tr_b16 a[220:221], v241 offset:57856\n" \
"v_mfma_f32_16x16x32_bf16 v[64:67], a[196:199], v[216:219], v[64:67]\n" \
"v_mfma_f32_16x16x32_bf16 v[96:99], a[196:199], v[220:223], v[96:99]\n" \
"ds_read_b64_tr_b16 a[222:223], v241 offset:59904\n" \
"v_mfma_f32_16x16x32_bf16 v[96:99], a[192:195], v[204:207], v[96:99]\n" \
"s_waitcnt lgkmcnt(8)\n" \
"v_mfma_f32_16x16x32_bf16 v[4:7], a[200:203], v[192:195], v[4:7]\n" \
"s_add_u32 m0, s70, 40960\n" \
"ds_read_b64_tr_b16 a[192:193], v240 offset:50176\n" \
"v_mfma_f32_16x16x32_bf16 v[4:7], a[204:207], v[208:211], v[4:7]\n" \
"s_add_u32 s74, s69, 2883584\n" \
"v_mfma_f32_16x16x32_bf16 v[36:39], a[204:207], v[212:215], v[36:39]\n" \
"buffer_load_dwordx4 v246, s[24:27], s74 offen lds\n" \
"v_mfma_f32_16x16x32_bf16 v[36:39], a[200:203], v[196:199], v[36:39]\n" \
"ds_read_b64_tr_b16 a[194:195], v240 offset:52224\n" \
"v_mfma_f32_16x16x32_bf16 v[68:71], a[200:203], v[200:203], v[68:71]\n" \
"ds_read_b64_tr_b16 a[196:197], v240 offset:58368\n" \
"v_mfma_f32_16x16x32_bf16 v[68:71], a[204:207], v[216:219], v[68:71]\n" \
"v_mfma_f32_16x16x32_bf16 v[100:103], a[204:207], v[220:223], v[100:103]\n" \
"ds_read_b64_tr_b16 a[198:199], v240 offset:60416\n" \
"v_mfma_f32_16x16x32_bf16 v[100:103], a[200:203], v[204:207], v[100:103]\n" \
"s_waitcnt lgkmcnt(8)\n" \
"v_mfma_f32_16x16x32_bf16 v[8:11], a[208:211], v[192:195], v[8:11]\n" \
"ds_read_b64_tr_b16 a[200:201], v241 offset:50176\n" \
"v_mfma_f32_16x16x32_bf16 v[8:11], a[212:215], v[208:211], v[8:11]\n" \
"v_mfma_f32_16x16x32_bf16 v[40:43], a[212:215], v[212:215], v[40:43]\n" \
"ds_read_b64_tr_b16 a[202:203], v241 offset:52224\n" \
"v_mfma_f32_16x16x32_bf16 v[40:43], a[208:211], v[196:199], v[40:43]\n" \
"v_mfma_f32_16x16x32_bf16 v[72:75], a[208:211], v[200:203], v[72:75]\n" \
"ds_read_b64_tr_b16 a[204:205], v241 offset:58368\n" \
"v_mfma_f32_16x16x32_bf16 v[72:75], a[212:215], v[216:219], v[72:75]\n" \
"v_mfma_f32_16x16x32_bf16 v[104:107], a[212:215], v[220:223], v[104:107]\n" \
"s_add_u32 m0, s70, 41984\n" \
"ds_read_b64_tr_b16 a[206:207], v241 offset:60416\n" \
"v_mfma_f32_16x16x32_bf16 v[104:107], a[208:211], v[204:207], v[104:107]\n" \
"s_waitcnt lgkmcnt(8)\n" \
"s_add_u32 s74, s69, 2883712\n" \
"v_mfma_f32_16x16x32_bf16 v[12:15], a[216:219], v[192:195], v[12:15]\n" \
"buffer_load_dwordx4 v246, s[24:27], s74 offen lds\n" \
"v_mfma_f32_16x16x32_bf16 v[12:15], a[220:223], v[208:211], v[12:15]\n" \
"ds_read_b64_tr_b16 a[208:209], v240 offset:50688\n" \
"v_mfma_f32_16x16x32_bf16 v[44:47], a[220:223], v[212:215], v[44:47]\n" \
"ds_read_b64_tr_b16 a[210:211], v240 offset:52736\n" \
"v_mfma_f32_16x16x32_bf16 v[44:47], a[216:219], v[196:199], v[44:47]\n" \
"v_mfma_f32_16x16x32_bf16 v[76:79], a[216:219], v[200:203], v[76:79]\n" \
"ds_read_b64_tr_b16 a[212:213], v240 offset:58880\n" \
"v_mfma_f32_16x16x32_bf16 v[76:79], a[220:223], v[216:219], v[76:79]\n" \
"v_mfma_f32_16x16x32_bf16 v[108:111], a[220:223], v[220:223], v[108:111]\n" \
"ds_read_b64_tr_b16 a[214:215], v240 offset:60928\n" \
"v_mfma_f32_16x16x32_bf16 v[108:111], a[216:219], v[204:207], v[108:111]\n" \
"s_waitcnt lgkmcnt(8)\n" \
"v_mfma_f32_16x16x32_bf16 v[16:19], a[192:195], v[192:195], v[16:19]\n" \
"ds_read_b64_tr_b16 a[216:217], v241 offset:50688\n" \
"v_mfma_f32_16x16x32_bf16 v[16:19], a[196:199], v[208:211], v[16:19]\n" \
"v_mfma_f32_16x16x32_bf16 v[48:51], a[196:199], v[212:215], v[48:51]\n" \
"ds_read_b64_tr_b16 a[218:219], v241 offset:52736\n" \
"v_mfma_f32_16x16x32_bf16 v[48:51], a[192:195], v[196:199], v[48:51]\n" \
"ds_read_b64_tr_b16 a[220:221], v241 offset:58880\n" \
"v_mfma_f32_16x16x32_bf16 v[80:83], a[192:195], v[200:203], v[80:83]\n" \
"ds_read_b64_tr_b16 a[222:223], v241 offset:60928\n" \
"v_mul_f32 v0, s84, v0\n" \
"v_mfma_f32_16x16x32_bf16 v[80:83], a[196:199], v[216:219], v[80:83]\n" \
"v_mul_f32 v1, s84, v1\n" \
"v_mul_f32 v2, s84, v2\n" \
"v_mfma_f32_16x16x32_bf16 v[112:115], a[196:199], v[220:223], v[112:115]\n" \
"v_mul_f32 v3, s84, v3\n" \
"v_mul_f32 v4, s84, v4\n" \
"v_mfma_f32_16x16x32_bf16 v[112:115], a[192:195], v[204:207], v[112:115]\n" \
"s_waitcnt lgkmcnt(8)\n" \
"v_mul_f32 v5, s84, v5\n" \
"v_mul_f32 v6, s84, v6\n" \
"v_mfma_f32_16x16x32_bf16 v[20:23], a[200:203], v[192:195], v[20:23]\n" \
"v_mul_f32 v7, s84, v7\n" \
"v_cvt_pk_bf16_f32 v128, v0, v1\n" \
"v_mfma_f32_16x16x32_bf16 v[20:23], a[204:207], v[208:211], v[20:23]\n" \
"v_cvt_pk_bf16_f32 v129, v2, v3\n" \
"v_cvt_pk_bf16_f32 v130, v4, v5\n" \
"v_mfma_f32_16x16x32_bf16 v[52:55], a[204:207], v[212:215], v[52:55]\n" \
"v_cvt_pk_bf16_f32 v131, v6, v7\n" \
"v_mfma_f32_16x16x32_bf16 v[52:55], a[200:203], v[196:199], v[52:55]\n" \
"ds_write_b128 v244, v[128:131] offset:0\n" \
"v_mfma_f32_16x16x32_bf16 v[84:87], a[200:203], v[200:203], v[84:87]\n" \
"s_waitcnt vmcnt(14) lgkmcnt(1)\n" \
"s_barrier\n" \
"s_add_u32 m0, s70, 49152\n" \
"v_mfma_f32_16x16x32_bf16 v[84:87], a[204:207], v[216:219], v[84:87]\n" \
"s_add_u32 s74, s69, 3145728\n" \
"ds_read_b64_tr_b16 a[192:193], v240 offset:0\n" \
"ds_read_b64_tr_b16 a[194:195], v240 offset:2048\n" \
"v_mfma_f32_16x16x32_bf16 v[116:119], a[204:207], v[220:223], v[116:119]\n" \
"buffer_load_dwordx4 v246, s[24:27], s74 offen lds\n" \
"v_mfma_f32_16x16x32_bf16 v[116:119], a[200:203], v[204:207], v[116:119]\n" \
"ds_read_b64_tr_b16 a[196:197], v240 offset:8192\n" \
"ds_read_b64_tr_b16 a[198:199], v240 offset:10240\n" \
"v_mfma_f32_16x16x32_bf16 v[24:27], a[208:211], v[192:195], v[24:27]\n" \
"ds_read_b64_tr_b16 a[200:201], v241 offset:0\n" \
"v_mfma_f32_16x16x32_bf16 v[24:27], a[212:215], v[208:211], v[24:27]\n" \
"v_mfma_f32_16x16x32_bf16 v[56:59], a[212:215], v[212:215], v[56:59]\n" \
"ds_read_b64_tr_b16 a[202:203], v241 offset:2048\n" \
"v_mul_f32 v8, s84, v8\n" \
"v_mfma_f32_16x16x32_bf16 v[56:59], a[208:211], v[196:199], v[56:59]\n" \
"v_mul_f32 v9, s84, v9\n" \
"v_mul_f32 v10, s84, v10\n" \
"v_mfma_f32_16x16x32_bf16 v[88:91], a[208:211], v[200:203], v[88:91]\n" \
"ds_read_b64_tr_b16 a[204:205], v241 offset:8192\n" \
"v_and_b32 v252, 15, v251\n" \
"v_mfma_f32_16x16x32_bf16 v[88:91], a[212:215], v[216:219], v[88:91]\n" \
"v_lshlrev_b32 v252, 2, v252\n" \
"v_and_b32 v253, 16, v251\n" \
"v_mfma_f32_16x16x32_bf16 v[120:123], a[212:215], v[220:223], v[120:123]\n" \
"ds_read_b64_tr_b16 a[206:207], v241 offset:10240\n" \
"v_lshl_or_b32 v252, v253, 4, v252\n" \
"v_mfma_f32_16x16x32_bf16 v[120:123], a[208:211], v[204:207], v[120:123]\n" \
"v_mul_f32 v11, s84, v11\n" \
"v_mul_f32 v12, s84, v12\n" \
"v_mfma_f32_16x16x32_bf16 v[28:31], a[216:219], v[192:195], v[28:31]\n" \
"ds_read_b64_tr_b16 a[208:209], v240 offset:512\n" \
"v_mul_f32 v13, s84, v13\n" \
"v_mfma_f32_16x16x32_bf16 v[28:31], a[220:223], v[208:211], v[28:31]\n" \
"v_mul_f32 v14, s84, v14\n" \
"v_mul_f32 v15, s84, v15\n" \
"v_mfma_f32_16x16x32_bf16 v[60:63], a[220:223], v[212:215], v[60:63]\n" \
"s_add_u32 m0, s70, 50176\n" \
"ds_read_b64_tr_b16 a[210:211], v240 offset:2560\n" \
"v_cvt_pk_bf16_f32 v132, v8, v9\n" \
"v_mfma_f32_16x16x32_bf16 v[60:63], a[216:219], v[196:199], v[60:63]\n" \
"s_add_u32 s74, s69, 3145856\n" \
"v_cvt_pk_bf16_f32 v133, v10, v11\n" \
"v_cvt_pk_bf16_f32 v134, v12, v13\n" \
"v_mfma_f32_16x16x32_bf16 v[92:95], a[216:219], v[200:203], v[92:95]\n" \
"buffer_load_dwordx4 v246, s[24:27], s74 offen lds\n" \
"v_mfma_f32_16x16x32_bf16 v[92:95], a[220:223], v[216:219], v[92:95]\n" \
"ds_read_b64_tr_b16 a[212:213], v240 offset:8704\n" \
"v_cvt_pk_bf16_f32 v135, v14, v15\n" \
"v_mfma_f32_16x16x32_bf16 v[124:127], a[220:223], v[220:223], v[124:127]\n" \
"ds_read_b64_tr_b16 a[214:215], v240 offset:10752\n" \
"v_mfma_f32_16x16x32_bf16 v[124:127], a[216:219], v[204:207], v[124:127]\n" \
"s_waitcnt lgkmcnt(8)\n" \
"ds_write_b128 v244, v[132:135] offset:1024\n" \
  \
"v_mfma_f32_16x16x32_bf16 a[0:3], v[192:195], a[192:195], a[0:3]\n" \
"ds_read_b64_tr_b16 a[216:217], v241 offset:512\n" \
"v_mul_f32 v16, s84, v16\n" \
"v_mfma_f32_16x16x32_bf16 a[0:3], v[208:211], a[196:199], a[0:3]\n" \
"v_mul_f32 v17, s84, v17\n" \
"v_mul_f32 v18, s84, v18\n" \
"v_mfma_f32_16x16x32_bf16 a[4:7], v[212:215], a[196:199], a[4:7]\n" \
"ds_read_b64_tr_b16 a[218:219], v241 offset:2560\n" \
"v_mul_f32 v19, s84, v19\n" \
"v_mfma_f32_16x16x32_bf16 a[4:7], v[196:199], a[192:195], a[4:7]\n" \
"v_mul_f32 v20, s84, v20\n" \
"v_mul_f32 v21, s84, v21\n" \
"v_mfma_f32_16x16x32_bf16 a[64:67], v[200:203], a[192:195], a[64:67]\n" \
"ds_read_b64_tr_b16 a[220:221], v241 offset:8704\n" \
"v_mul_f32 v22, s84, v22\n" \
"v_mfma_f32_16x16x32_bf16 a[64:67], v[216:219], a[196:199], a[64:67]\n" \
"v_mul_f32 v23, s84, v23\n" \
"v_cvt_pk_bf16_f32 v136, v16, v17\n" \
"v_mfma_f32_16x16x32_bf16 a[68:71], v[220:223], a[196:199], a[68:71]\n" \
"ds_read_b64_tr_b16 a[222:223], v241 offset:10752\n" \
"v_cvt_pk_bf16_f32 v137, v18, v19\n" \
"v_mfma_f32_16x16x32_bf16 a[68:71], v[204:207], a[192:195], a[68:71]\n" \
"s_waitcnt lgkmcnt(9)\n" \
"v_cvt_pk_bf16_f32 v138, v20, v21\n" \
"v_cvt_pk_bf16_f32 v139, v22, v23\n" \
"v_mfma_f32_16x16x32_bf16 a[8:11], v[192:195], a[200:203], a[8:11]\n" \
"s_add_u32 m0, s70, 57344\n" \
"ds_read_b64_tr_b16 a[192:193], v240 offset:1024\n" \
"v_mfma_f32_16x16x32_bf16 a[8:11], v[208:211], a[204:207], a[8:11]\n" \
"s_add_u32 s74, s69, 3407872\n" \
"ds_write_b128 v244, v[136:139] offset:2048\n" \
"v_mfma_f32_16x16x32_bf16 a[12:15], v[212:215], a[204:207], a[12:15]\n" \
"buffer_load_dwordx4 v246, s[24:27], s74 offen lds\n" \
"v_mfma_f32_16x16x32_bf16 a[12:15], v[196:199], a[200:203], a[12:15]\n" \
"ds_read_b64_tr_b16 a[194:195], v240 offset:3072\n" \
"v_mfma_f32_16x16x32_bf16 a[72:75], v[200:203], a[200:203], a[72:75]\n" \
"ds_read_b64_tr_b16 a[196:197], v240 offset:9216\n" \
"v_mfma_f32_16x16x32_bf16 a[72:75], v[216:219], a[204:207], a[72:75]\n" \
"v_mfma_f32_16x16x32_bf16 a[76:79], v[220:223], a[204:207], a[76:79]\n" \
"ds_read_b64_tr_b16 a[198:199], v240 offset:11264\n" \
"v_mul_f32 v24, s84, v24\n" \
"v_mfma_f32_16x16x32_bf16 a[76:79], v[204:207], a[200:203], a[76:79]\n" \
"s_waitcnt lgkmcnt(10)\n" \
"v_mul_f32 v25, s84, v25\n" \
"v_mul_f32 v26, s84, v26\n" \
"v_mfma_f32_16x16x32_bf16 a[16:19], v[192:195], a[208:211], a[16:19]\n" \
"ds_read_b64_tr_b16 a[200:201], v241 offset:1024\n" \
"v_mul_f32 v27, s84, v27\n" \
"v_mfma_f32_16x16x32_bf16 a[16:19], v[208:211], a[212:215], a[16:19]\n" \
"v_mul_f32 v28, s84, v28\n" \
"v_mul_f32 v29, s84, v29\n" \
"v_mfma_f32_16x16x32_bf16 a[20:23], v[212:215], a[212:215], a[20:23]\n" \
"ds_read_b64_tr_b16 a[202:203], v241 offset:3072\n" \
"v_mul_f32 v30, s84, v30\n" \
"v_mfma_f32_16x16x32_bf16 a[20:23], v[196:199], a[208:211], a[20:23]\n" \
"v_mul_f32 v31, s84, v31\n" \
"v_cvt_pk_bf16_f32 v140, v24, v25\n" \
"v_mfma_f32_16x16x32_bf16 a[80:83], v[200:203], a[208:211], a[80:83]\n" \
"ds_read_b64_tr_b16 a[204:205], v241 offset:9216\n" \
"v_cvt_pk_bf16_f32 v141, v26, v27\n" \
"v_mfma_f32_16x16x32_bf16 a[80:83], v[216:219], a[212:215], a[80:83]\n" \
"s_add_u32 s85, s71, 0\n" \
"v_cvt_pk_bf16_f32 v142, v28, v29\n" \
"v_cvt_pk_bf16_f32 v143, v30, v31\n" \
"v_mfma_f32_16x16x32_bf16 a[84:87], v[220:223], a[212:215], a[84:87]\n" \
"s_add_u32 m0, s70, 58368\n" \
"buffer_store_dwordx4 v[128:131], v248, s[32:35], s85 offen offset:0 sc1\n" \
"v_mfma_f32_16x16x32_bf16 a[84:87], v[204:207], a[208:211], a[84:87]\n" \
"s_waitcnt lgkmcnt(8)\n" \
"s_add_u32 s74, s69, 3408000\n" \
"ds_read_b64_tr_b16 a[206:207], v241 offset:11264\n" \
"v_mfma_f32_16x16x32_bf16 a[24:27], v[192:195], a[216:219], a[24:27]\n" \
"s_lshl_b32 s81, s67, 3\n" \
"ds_read_b64_tr_b16 a[208:209], v240 offset:1536\n" \
"v_mfma_f32_16x16x32_bf16 a[24:27], v[208:211], a[220:223], a[24:27]\n" \
"s_add_u32 s81, s81, %[wid]\n" \
"buffer_load_dwordx4 v246, s[24:27], s74 offen lds\n" \
"v_mfma_f32_16x16x32_bf16 a[28:31], v[212:215], a[220:223], a[28:31]\n" \
"s_lshl_b32 s81, s81, 6\n" \
"ds_read_b64_tr_b16 a[210:211], v240 offset:3584\n" \
"v_mfma_f32_16x16x32_bf16 a[28:31], v[196:199], a[216:219], a[28:31]\n" \
"s_add_u32 s81, s81, 90112\n" \
"ds_write_b128 v244, v[140:143] offset:3072\n" \
"v_mfma_f32_16x16x32_bf16 a[88:91], v[200:203], a[216:219], a[88:91]\n" \
"buffer_load_dword v247, v252, s[32:35], s81 offen sc1\n" \
"v_mul_f32 v32, s84, v32\n" \
"v_mfma_f32_16x16x32_bf16 a[88:91], v[216:219], a[220:223], a[88:91]\n" \
"ds_read_b64_tr_b16 a[212:213], v240 offset:9728\n" \
"v_mul_f32 v33, s84, v33\n" \
"v_mfma_f32_16x16x32_bf16 a[92:95], v[220:223], a[220:223], a[92:95]\n" \
"ds_read_b64_tr_b16 a[214:215], v240 offset:11776\n" \
"v_mul_f32 v34, s84, v34\n" \
"v_mfma_f32_16x16x32_bf16 a[92:95], v[204:207], a[216:219], a[92:95]\n" \
"s_waitcnt lgkmcnt(9)\n" \
"v_mul_f32 v35, s84, v35\n" \
"v_mul_f32 v36, s84, v36\n" \
"v_mfma_f32_16x16x32_bf16 a[32:35], v[192:195], a[192:195], a[32:35]\n" \
"ds_read_b64_tr_b16 a[216:217], v241 offset:1536\n" \
"v_mul_f32 v37, s84, v37\n" \
"v_mfma_f32_16x16x32_bf16 a[32:35], v[208:211], a[196:199], a[32:35]\n" \
"v_mul_f32 v38, s84, v38\n" \
"v_mul_f32 v39, s84, v39\n" \
"v_mfma_f32_16x16x32_bf16 a[36:39], v[212:215], a[196:199], a[36:39]\n" \
"ds_read_b64_tr_b16 a[218:219], v241 offset:3584\n" \
"v_cvt_pk_bf16_f32 v144, v32, v33\n" \
"v_mfma_f32_16x16x32_bf16 a[36:39], v[196:199], a[192:195], a[36:39]\n" \
"ds_read_b64_tr_b16 a[220:221], v241 offset:9728\n" \
"v_cvt_pk_bf16_f32 v145, v34, v35\n" \
"v_mfma_f32_16x16x32_bf16 a[96:99], v[200:203], a[192:195], a[96:99]\n" \
"ds_read_b64_tr_b16 a[222:223], v241 offset:11776\n" \
"v_cvt_pk_bf16_f32 v146, v36, v37\n" \
"v_mfma_f32_16x16x32_bf16 a[96:99], v[216:219], a[196:199], a[96:99]\n" \
"v_cvt_pk_bf16_f32 v147, v38, v39\n" \
"v_mfma_f32_16x16x32_bf16 a[100:103], v[220:223], a[196:199], a[100:103]\n" \
"ds_write_b128 v244, v[144:147] offset:4096\n" \
"v_mfma_f32_16x16x32_bf16 a[100:103], v[204:207], a[192:195], a[100:103]\n" \
"s_waitcnt lgkmcnt(10)\n" \
"v_mfma_f32_16x16x32_bf16 a[40:43], v[192:195], a[200:203], a[40:43]\n" \
"v_mfma_f32_16x16x32_bf16 a[40:43], v[208:211], a[204:207], a[40:43]\n" \
"v_mfma_f32_16x16x32_bf16 a[44:47], v[212:215], a[204:207], a[44:47]\n" \
"v_mul_f32 v40, s84, v40\n" \
"v_mul_f32 v41, s84, v41\n" \
"v_mfma_f32_16x16x32_bf16 a[44:47], v[196:199], a[200:203], a[44:47]\n" \
"v_mul_f32 v42, s84, v42\n" \
"v_mul_f32 v43, s84, v43\n" \
"v_mfma_f32_16x16x32_bf16 a[104:107], v[200:203], a[200:203], a[104:107]\n" \
"s_waitcnt vmcnt(12) lgkmcnt(1)\n" \
"s_barrier\n" \
"v_mul_f32 v44, s84, v44\n" \
"v_mfma_f32_16x16x32_bf16 a[104:107], v[216:219], a[204:207], a[104:107]\n" \
"s_add_u32 m0, s70, 65536\n" \
"ds_read_b64_tr_b16 a[192:193], v240 offset:16384\n" \
"ds_read_b64_tr_b16 a[194:195], v240 offset:18432\n" \
"v_mfma_f32_16x16x32_bf16 a[108:111], v[220:223], a[204:207], a[108:111]\n" \
"s_add_u32 s74, s69, 0\n" \
"buffer_load_dwordx4 v246, s[28:31], s74 offen lds\n" \
"v_mfma_f32_16x16x32_bf16 a[108:111], v[204:207], a[200:203], a[108:111]\n" \
"ds_read_b64_tr_b16 a[196:197], v240 offset:24576\n" \
"ds_read_b64_tr_b16 a[198:199], v240 offset:26624\n" \
"v_mfma_f32_16x16x32_bf16 a[48:51], v[192:195], a[208:211], a[48:51]\n" \
"ds_read_b64_tr_b16 a[200:201], v241 offset:16384\n" \
"v_mul_f32 v45, s84, v45\n" \
"v_mfma_f32_16x16x32_bf16 a[48:51], v[208:211], a[212:215], a[48:51]\n" \
"s_add_u32 s85, s71, 0\n" \
"v_mul_f32 v46, s84, v46\n" \
"v_mul_f32 v47, s84, v47\n" \
"v_mfma_f32_16x16x32_bf16 a[52:55], v[212:215], a[212:215], a[52:55]\n" \
"buffer_store_dwordx4 v[132:135], v248, s[32:35], s85 offen offset:1024 sc1\n" \
"v_mfma_f32_16x16x32_bf16 a[52:55], v[196:199], a[208:211], a[52:55]\n" \
"ds_read_b64_tr_b16 a[202:203], v241 offset:18432\n" \
"v_cvt_pk_bf16_f32 v148, v40, v41\n" \
"v_mfma_f32_16x16x32_bf16 a[112:115], v[200:203], a[208:211], a[112:115]\n" \
"ds_read_b64_tr_b16 a[204:205], v241 offset:24576\n" \
"v_cvt_pk_bf16_f32 v149, v42, v43\n" \
"v_mfma_f32_16x16x32_bf16 a[112:115], v[216:219], a[212:215], a[112:115]\n" \
"v_cvt_pk_bf16_f32 v150, v44, v45\n" \
"v_cvt_pk_bf16_f32 v151, v46, v47\n" \
"v_mfma_f32_16x16x32_bf16 a[116:119], v[220:223], a[212:215], a[116:119]\n" \
"ds_read_b64_tr_b16 a[206:207], v241 offset:26624\n" \
"v_mfma_f32_16x16x32_bf16 a[116:119], v[204:207], a[208:211], a[116:119]\n" \
"ds_write_b128 v244, v[148:151] offset:5120\n" \
"v_mfma_f32_16x16x32_bf16 a[56:59], v[192:195], a[216:219], a[56:59]\n" \
"ds_read_b64_tr_b16 a[208:209], v240 offset:16896\n" \
"v_mul_f32 v48, s84, v48\n" \
"v_mfma_f32_16x16x32_bf16 a[56:59], v[208:211], a[220:223], a[56:59]\n" \
"v_mul_f32 v49, s84, v49\n" \
"v_mul_f32 v50, s84, v50\n" \
"v_mfma_f32_16x16x32_bf16 a[60:63], v[212:215], a[220:223], a[60:63]\n" \
"s_add_u32 m0, s70, 66560\n" \
"ds_read_b64_tr_b16 a[210:211], v240 offset:18944\n" \
"v_mul_f32 v51, s84, v51\n" \
"v_mfma_f32_16x16x32_bf16 a[60:63], v[196:199], a[216:219], a[60:63]\n" \
"s_add_u32 s74, s69, 128\n" \
"v_mul_f32 v52, s84, v52\n" \
"v_mul_f32 v53, s84, v53\n" \
"v_mfma_f32_16x16x32_bf16 a[120:123], v[200:203], a[216:219], a[120:123]\n" \
"buffer_load_dwordx4 v246, s[28:31], s74 offen lds\n" \
"v_mfma_f32_16x16x32_bf16 a[120:123], v[216:219], a[220:223], a[120:123]\n" \
"ds_read_b64_tr_b16 a[212:213], v240 offset:25088\n" \
"v_mul_f32 v54, s84, v54\n" \
"v_mfma_f32_16x16x32_bf16 a[124:127], v[220:223], a[220:223], a[124:127]\n" \
"ds_read_b64_tr_b16 a[214:215], v240 offset:27136\n" \
"v_mul_f32 v55, s84, v55\n" \
"v_mfma_f32_16x16x32_bf16 a[124:127], v[204:207], a[216:219], a[124:127]\n" \
"s_waitcnt lgkmcnt(9)\n" \
"v_cvt_pk_bf16_f32 v152, v48, v49\n" \
"v_cvt_pk_bf16_f32 v153, v50, v51\n" \
"v_mfma_f32_16x16x32_bf16 a[0:3], a[160:163], a[192:195], a[0:3]\n" \
"ds_read_b64_tr_b16 a[216:217], v241 offset:16896\n" \
"v_cvt_pk_bf16_f32 v154, v52, v53\n" \
"v_mfma_f32_16x16x32_bf16 a[0:3], a[176:179], a[196:199], a[0:3]\n" \
"v_cvt_pk_bf16_f32 v155, v54, v55\n" \
"v_mfma_f32_16x16x32_bf16 a[4:7], a[180:183], a[196:199], a[4:7]\n" \
"ds_read_b64_tr_b16 a[218:219], v241 offset:18944\n" \
"v_mfma_f32_16x16x32_bf16 a[4:7], a[164:167], a[192:195], a[4:7]\n" \
"ds_write_b128 v244, v[152:155] offset:6144\n" \
"v_mfma_f32_16x16x32_bf16 a[64:67], a[168:171], a[192:195], a[64:67]\n" \
"ds_read_b64_tr_b16 a[220:221], v241 offset:25088\n" \
"v_mfma_f32_16x16x32_bf16 a[64:67], a[184:187], a[196:199], a[64:67]\n" \
"v_mfma_f32_16x16x32_bf16 a[68:71], a[188:191], a[196:199], a[68:71]\n" \
"ds_read_b64_tr_b16 a[222:223], v241 offset:27136\n" \
"v_mul_f32 v56, s84, v56\n" \
"v_mfma_f32_16x16x32_bf16 a[68:71], a[172:175], a[192:195], a[68:71]\n" \
"s_waitcnt lgkmcnt(10)\n" \
"v_mul_f32 v57, s84, v57\n" \
"v_mul_f32 v58, s84, v58\n" \
"v_mfma_f32_16x16x32_bf16 a[8:11], a[160:163], a[200:203], a[8:11]\n" \
"s_add_u32 m0, s70, 73728\n" \
"ds_read_b64_tr_b16 a[192:193], v240 offset:17408\n" \
"v_mul_f32 v59, s84, v59\n" \
"v_mfma_f32_16x16x32_bf16 a[8:11], a[176:179], a[204:207], a[8:11]\n" \
"s_add_u32 s74, s69, 262144\n" \
"v_mul_f32 v60, s84, v60\n" \
"v_mul_f32 v61, s84, v61\n" \
"v_mfma_f32_16x16x32_bf16 a[12:15], a[180:183], a[204:207], a[12:15]\n" \
"buffer_load_dwordx4 v246, s[28:31], s74 offen lds\n" \
"v_mfma_f32_16x16x32_bf16 a[12:15], a[164:167], a[200:203], a[12:15]\n" \
"ds_read_b64_tr_b16 a[194:195], v240 offset:19456\n" \
"v_mul_f32 v62, s84, v62\n" \
"v_mfma_f32_16x16x32_bf16 a[72:75], a[168:171], a[200:203], a[72:75]\n" \
"ds_read_b64_tr_b16 a[196:197], v240 offset:25600\n" \
"v_mul_f32 v63, s84, v63\n" \
"v_mfma_f32_16x16x32_bf16 a[72:75], a[184:187], a[204:207], a[72:75]\n" \
"s_add_u32 s85, s71, 0\n" \
"v_cvt_pk_bf16_f32 v156, v56, v57\n" \
"v_cvt_pk_bf16_f32 v157, v58, v59\n" \
"v_mfma_f32_16x16x32_bf16 a[76:79], a[188:191], a[204:207], a[76:79]\n" \
"buffer_store_dwordx4 v[136:139], v248, s[32:35], s85 offen offset:2048 sc1\n" \
"v_mfma_f32_16x16x32_bf16 a[76:79], a[172:175], a[200:203], a[76:79]\n" \
"s_waitcnt lgkmcnt(8)\n" \
"ds_read_b64_tr_b16 a[198:199], v240 offset:27648\n" \
"v_cvt_pk_bf16_f32 v158, v60, v61\n" \
"v_mfma_f32_16x16x32_bf16 a[16:19], a[160:163], a[208:211], a[16:19]\n" \
"ds_read_b64_tr_b16 a[200:201], v241 offset:17408\n" \
"v_cvt_pk_bf16_f32 v159, v62, v63\n" \
"v_mfma_f32_16x16x32_bf16 a[16:19], a[176:179], a[212:215], a[16:19]\n" \
"ds_write_b128 v244, v[156:159] offset:7168\n" \
"v_mfma_f32_16x16x32_bf16 a[20:23], a[180:183], a[212:215], a[20:23]\n" \
"ds_read_b64_tr_b16 a[202:203], v241 offset:19456\n" \
"v_mfma_f32_16x16x32_bf16 a[20:23], a[164:167], a[208:211], a[20:23]\n" \
"v_mfma_f32_16x16x32_bf16 a[80:83], a[168:171], a[208:211], a[80:83]\n" \
"s_waitcnt vmcnt(5)\n" \
"ds_read_b64_tr_b16 a[204:205], v241 offset:25600\n" \
"v_cmp_eq_u32 vcc, 0, v247\n" \
"v_mfma_f32_16x16x32_bf16 a[80:83], a[184:187], a[212:215], a[80:83]\n" \
"v_mul_f32 v64, s84, v64\n" \
"v_mul_f32 v65, s84, v65\n" \
"v_mfma_f32_16x16x32_bf16 a[84:87], a[188:191], a[212:215], a[84:87]\n" \
"s_add_u32 m0, s70, 74752\n" \
"ds_read_b64_tr_b16 a[206:207], v241 offset:27648\n" \
"v_mul_f32 v66, s84, v66\n" \
"v_mfma_f32_16x16x32_bf16 a[84:87], a[172:175], a[208:211], a[84:87]\n" \
"s_waitcnt lgkmcnt(9)\n" \
"s_cbranch_vccnz .Lsph_%=\n" \
".Lsbh_%=:\n" \
"v_mfma_f32_16x16x32_bf16 a[24:27], a[160:163], a[216:219], a[24:27]\n" \
"s_add_u32 s74, s69, 262272\n" \
"buffer_load_dwordx4 v246, s[28:31], s74 offen lds\n" \
"v_mfma_f32_16x16x32_bf16 a[24:27], a[176:179], a[220:223], a[24:27]\n" \
"ds_read_b64_tr_b16 a[208:209], v240 offset:17920\n" \
"v_mul_f32 v67, s84, v67\n" \
"v_mfma_f32_16x16x32_bf16 a[28:31], a[180:183], a[220:223], a[28:31]\n" \
"buffer_load_dwordx4 v[192:195], v248, s[32:35], s79 offen offset:0 sc1\n" \
"v_mul_f32 v68, s84, v68\n" \
"v_mfma_f32_16x16x32_bf16 a[28:31], a[164:167], a[216:219], a[28:31]\n" \
"ds_read_b64_tr_b16 a[210:211], v240 offset:19968\n" \
"v_mul_f32 v69, s84, v69\n" \
"v_mfma_f32_16x16x32_bf16 a[88:91], a[168:171], a[216:219], a[88:91]\n" \
"ds_read_b64_tr_b16 a[212:213], v240 offset:26112\n" \
"v_mul_f32 v70, s84, v70\n" \
"v_mfma_f32_16x16x32_bf16 a[88:91], a[184:187], a[220:223], a[88:91]\n" \
"v_mul_f32 v71, s84, v71\n" \
"v_cvt_pk_bf16_f32 v160, v64, v65\n" \
"v_mfma_f32_16x16x32_bf16 a[92:95], a[188:191], a[220:223], a[92:95]\n" \
"ds_read_b64_tr_b16 a[214:215], v240 offset:28160\n" \
"v_cvt_pk_bf16_f32 v161, v66, v67\n" \
"v_mfma_f32_16x16x32_bf16 a[92:95], a[172:175], a[216:219], a[92:95]\n" \
"s_waitcnt lgkmcnt(9)\n" \
"v_cvt_pk_bf16_f32 v162, v68, v69\n" \
"v_cvt_pk_bf16_f32 v163, v70, v71\n" \
"v_mfma_f32_16x16x32_bf16 a[32:35], a[160:163], a[192:195], a[32:35]\n" \
"ds_read_b64_tr_b16 a[216:217], v241 offset:17920\n" \
"v_xor_b32 v244, 0x4000, v244\n" \
"v_mfma_f32_16x16x32_bf16 a[32:35], a[176:179], a[196:199], a[32:35]\n" \
"ds_write_b128 v244, v[160:163] offset:32768\n" \
"v_mfma_f32_16x16x32_bf16 a[36:39], a[180:183], a[196:199], a[36:39]\n" \
"buffer_load_dwordx4 v[196:199], v248, s[32:35], s80 offen offset:0 sc1\n" \
"v_mul_f32 v72, s84, v72\n" \
"v_mfma_f32_16x16x32_bf16 a[36:39], a[164:167], a[192:195], a[36:39]\n" \
"ds_read_b64_tr_b16 a[218:219], v241 offset:19968\n" \
"ds_read_b64_tr_b16 a[220:221], v241 offset:26112\n" \
"v_mfma_f32_16x16x32_bf16 a[96:99], a[168:171], a[192:195], a[96:99]\n" \
"ds_read_b64_tr_b16 a[222:223], v241 offset:28160\n" \
"v_mul_f32 v73, s84, v73\n" \
"v_mfma_f32_16x16x32_bf16 a[96:99], a[184:187], a[196:199], a[96:99]\n" \
"v_mul_f32 v74, s84, v74\n" \
"v_mul_f32 v75, s84, v75\n" \
"v_mfma_f32_16x16x32_bf16 a[100:103], a[188:191], a[196:199], a[100:103]\n" \
"v_mul_f32 v76, s84, v76\n" \
"v_mul_f32 v77, s84, v77\n" \
"v_mfma_f32_16x16x32_bf16 a[100:103], a[172:175], a[192:195], a[100:103]\n" \
"s_waitcnt lgkmcnt(9)\n" \
"v_mul_f32 v78, s84, v78\n" \
"v_mul_f32 v79, s84, v79\n" \
"v_mfma_f32_16x16x32_bf16 a[40:43], a[160:163], a[200:203], a[40:43]\n" \
"v_cvt_pk_bf16_f32 v164, v72, v73\n" \
"v_cvt_pk_bf16_f32 v165, v74, v75\n" \
"v_mfma_f32_16x16x32_bf16 a[40:43], a[176:179], a[204:207], a[40:43]\n" \
"s_add_u32 s85, s71, 0\n" \
"v_cvt_pk_bf16_f32 v166, v76, v77\n" \
"v_cvt_pk_bf16_f32 v167, v78, v79\n" \
"v_mfma_f32_16x16x32_bf16 a[44:47], a[180:183], a[204:207], a[44:47]\n" \
"buffer_load_dwordx4 v[200:203], v248, s[32:35], s36 offen offset:0 sc1\n" \
"v_mfma_f32_16x16x32_bf16 a[44:47], a[164:167], a[200:203], a[44:47]\n" \
"ds_write_b128 v244, v[164:167] offset:33792\n" \
"v_mfma_f32_16x16x32_bf16 a[104:107], a[168:171], a[200:203], a[104:107]\n" \
"s_waitcnt lgkmcnt(1)\n" \
"s_barrier\n" \
"s_add_u32 m0, s70, 81920\n" \
"v_mfma_f32_16x16x32_bf16 a[104:107], a[184:187], a[204:207], a[104:107]\n" \
"s_add_u32 s74, s69, 524288\n" \
"buffer_store_dwordx4 v[140:143], v248, s[32:35], s85 offen offset:3072 sc1\n" \
"v_mfma_f32_16x16x32_bf16 a[108:111], a[188:191], a[204:207], a[108:111]\n" \
"ds_read_b64_tr_b16 a[192:193], v240 offset:32768\n" \
"ds_read_b64_tr_b16 a[194:195], v240 offset:34816\n" \
"v_mfma_f32_16x16x32_bf16 a[108:111], a[172:175], a[200:203], a[108:111]\n" \
"ds_read_b64_tr_b16 a[196:197], v240 offset:40960\n" \
"ds_read_b64_tr_b16 a[198:199], v240 offset:43008\n" \
"v_mfma_f32_16x16x32_bf16 a[48:51], a[160:163], a[208:211], a[48:51]\n" \
"buffer_load_dwordx4 v246, s[28:31], s74 offen lds\n" \
"v_mfma_f32_16x16x32_bf16 a[48:51], a[176:179], a[212:215], a[48:51]\n" \
"ds_read_b64_tr_b16 a[200:201], v241 offset:32768\n" \
"v_mul_f32 v80, s84, v80\n" \
"v_mfma_f32_16x16x32_bf16 a[52:55], a[180:183], a[212:215], a[52:55]\n" \
"buffer_load_dwordx4 v[204:207], v248, s[32:35], s37 offen offset:0 sc1\n" \
"v_mul_f32 v81, s84, v81\n" \
"v_mfma_f32_16x16x32_bf16 a[52:55], a[164:167], a[208:211], a[52:55]\n" \
"ds_read_b64_tr_b16 a[202:203], v241 offset:34816\n" \
"v_mul_f32 v82, s84, v82\n" \
"v_mfma_f32_16x16x32_bf16 a[112:115], a[168:171], a[208:211], a[112:115]\n" \
"ds_read_b64_tr_b16 a[204:205], v241 offset:40960\n" \
"v_mul_f32 v83, s84, v83\n" \
"v_mfma_f32_16x16x32_bf16 a[112:115], a[184:187], a[212:215], a[112:115]\n" \
"v_mul_f32 v84, s84, v84\n" \
"v_mul_f32 v85, s84, v85\n" \
"v_mfma_f32_16x16x32_bf16 a[116:119], a[188:191], a[212:215], a[116:119]\n" \
"ds_read_b64_tr_b16 a[206:207], v241 offset:43008\n" \
"v_mul_f32 v86, s84, v86\n" \
"v_mfma_f32_16x16x32_bf16 a[116:119], a[172:175], a[208:211], a[116:119]\n" \
"v_mul_f32 v87, s84, v87\n" \
"v_cvt_pk_bf16_f32 v168, v80, v81\n" \
"v_mfma_f32_16x16x32_bf16 a[56:59], a[160:163], a[216:219], a[56:59]\n" \
"ds_read_b64_tr_b16 a[208:209], v240 offset:33280\n" \
"v_cvt_pk_bf16_f32 v169, v82, v83\n" \
"v_mfma_f32_16x16x32_bf16 a[56:59], a[176:179], a[220:223], a[56:59]\n" \
"v_cvt_pk_bf16_f32 v170, v84, v85\n" \
"v_cvt_pk_bf16_f32 v171, v86, v87\n" \
"v_mfma_f32_16x16x32_bf16 a[60:63], a[180:183], a[220:223], a[60:63]\n" \
"s_add_u32 m0, s70, 82944\n" \
"ds_read_b64_tr_b16 a[210:211], v240 offset:35328\n" \
"v_mfma_f32_16x16x32_bf16 a[60:63], a[164:167], a[216:219], a[60:63]\n" \
"s_add_u32 s74, s69, 524416\n" \
"ds_write_b128 v244, v[168:171] offset:34816\n" \
"v_mfma_f32_16x16x32_bf16 a[120:123], a[168:171], a[216:219], a[120:123]\n" \
"buffer_load_dwordx4 v246, s[28:31], s74 offen lds\n" \
"v_mfma_f32_16x16x32_bf16 a[120:123], a[184:187], a[220:223], a[120:123]\n" \
"ds_read_b64_tr_b16 a[212:213], v240 offset:41472\n" \
"v_mfma_f32_16x16x32_bf16 a[124:127], a[188:191], a[220:223], a[124:127]\n" \
"ds_read_b64_tr_b16 a[214:215], v240 offset:43520\n" \
"v_mul_f32 v88, s84, v88\n" \
"v_mfma_f32_16x16x32_bf16 a[124:127], a[172:175], a[216:219], a[124:127]\n" \
"s_waitcnt lgkmcnt(9)\n" \
"v_mul_f32 v89, s84, v89\n" \
"v_mul_f32 v90, s84, v90\n" \
"v_mfma_f32_16x16x32_bf16 a[0:3], a[224:227], a[192:195], a[0:3]\n" \
"ds_read_b64_tr_b16 a[216:217], v241 offset:33280\n" \
"v_mul_f32 v91, s84, v91\n" \
"v_mfma_f32_16x16x32_bf16 a[0:3], a[240:243], a[196:199], a[0:3]\n" \
".Lst0_4_%=:\n" \
"s_cbranch_execz .Lst0_8_%=\n" \
"buffer_load_dwordx4 a[160:163], v248, s[32:35], s79 offen offset:1024 sc1\n" \
"v_mul_f32 v92, s84, v92\n" \
"v_mfma_f32_16x16x32_bf16 a[4:7], a[244:247], a[196:199], a[4:7]\n" \
"ds_read_b64_tr_b16 a[218:219], v241 offset:35328\n" \
"v_mul_f32 v93, s84, v93\n" \
"v_mfma_f32_16x16x32_bf16 a[4:7], a[228:231], a[192:195], a[4:7]\n" \
"v_mul_f32 v94, s84, v94\n" \
"v_mul_f32 v95, s84, v95\n" \
"v_mfma_f32_16x16x32_bf16 a[64:67], a[232:235], a[192:195], a[64:67]\n" \
"ds_read_b64_tr_b16 a[220:221], v241 offset:41472\n" \
"v_cvt_pk_bf16_f32 v172, v88, v89\n" \
"v_mfma_f32_16x16x32_bf16 a[64:67], a[248:251], a[196:199], a[64:67]\n" \
"s_add_u32 s85, s71, 4096\n" \
"v_cvt_pk_bf16_f32 v173, v90, v91\n" \
"v_cvt_pk_bf16_f32 v174, v92, v93\n" \
"v_mfma_f32_16x16x32_bf16 a[68:71], a[252:255], a[196:199], a[68:71]\n" \
"buffer_store_dwordx4 v[144:147], v248, s[32:35], s85 offen offset:0 sc1\n" \
"v_mfma_f32_16x16x32_bf16 a[68:71], a[236:239], a[192:195], a[68:71]\n" \
"s_waitcnt lgkmcnt(8)\n" \
"ds_read_b64_tr_b16 a[222:223], v241 offset:43520\n" \
"v_cvt_pk_bf16_f32 v175, v94, v95\n" \
"v_mfma_f32_16x16x32_bf16 a[8:11], a[224:227], a[200:203], a[8:11]\n" \
"ds_read_b64_tr_b16 a[192:193], v240 offset:33792\n" \
"v_mfma_f32_16x16x32_bf16 a[8:11], a[240:243], a[204:207], a[8:11]\n" \
"buffer_load_dwordx4 a[164:167], v248, s[32:35], s80 offen offset:1024 sc1\n" \
"v_mfma_f32_16x16x32_bf16 a[12:15], a[244:247], a[204:207], a[12:15]\n" \
"s_add_u32 m0, s70, 90112\n" \
"s_add_u32 s74, s69, 786432\n" \
"ds_read_b64_tr_b16 a[194:195], v240 offset:35840\n" \
"v_mfma_f32_16x16x32_bf16 a[12:15], a[228:231], a[200:203], a[12:15]\n" \
"ds_write_b128 v244, v[172:175] offset:35840\n" \
"v_mfma_f32_16x16x32_bf16 a[72:75], a[232:235], a[200:203], a[72:75]\n" \
"buffer_load_dwordx4 v246, s[28:31], s74 offen lds\n" \
"v_mfma_f32_16x16x32_bf16 a[72:75], a[248:251], a[204:207], a[72:75]\n" \
"ds_read_b64_tr_b16 a[196:197], v240 offset:41984\n" \
"v_mul_f32 v96, s84, v96\n" \
"v_mfma_f32_16x16x32_bf16 a[76:79], a[252:255], a[204:207], a[76:79]\n" \
"ds_read_b64_tr_b16 a[198:199], v240 offset:44032\n" \
"v_mul_f32 v97, s84, v97\n" \
"v_mfma_f32_16x16x32_bf16 a[76:79], a[236:239], a[200:203], a[76:79]\n" \
"s_waitcnt lgkmcnt(9)\n" \
"v_mul_f32 v98, s84, v98\n" \
"v_mul_f32 v99, s84, v99\n" \
"v_mfma_f32_16x16x32_bf16 a[16:19], a[224:227], a[208:211], a[16:19]\n" \
"ds_read_b64_tr_b16 a[200:201], v241 offset:33792\n" \
"v_mul_f32 v100, s84, v100\n" \
"v_mfma_f32_16x16x32_bf16 a[16:19], a[240:243], a[212:215], a[16:19]\n" \
"buffer_load_dwordx4 a[168:171], v248, s[32:35], s36 offen offset:1024 sc1\n" \
"v_mul_f32 v101, s84, v101\n" \
"v_mfma_f32_16x16x32_bf16 a[20:23], a[244:247], a[212:215], a[20:23]\n" \
"ds_read_b64_tr_b16 a[202:203], v241 offset:35840\n" \
"v_mul_f32 v102, s84, v102\n" \
"v_mfma_f32_16x16x32_bf16 a[20:23], a[228:231], a[208:211], a[20:23]\n" \
"v_mul_f32 v103, s84, v103\n" \
"v_cvt_pk_bf16_f32 v176, v96, v97\n" \
"v_mfma_f32_16x16x32_bf16 a[80:83], a[232:235], a[208:211], a[80:83]\n" \
"ds_read_b64_tr_b16 a[204:205], v241 offset:41984\n" \
"v_cvt_pk_bf16_f32 v177, v98, v99\n" \
"v_mfma_f32_16x16x32_bf16 a[80:83], a[248:251], a[212:215], a[80:83]\n" \
"v_cvt_pk_bf16_f32 v178, v100, v101\n" \
"v_cvt_pk_bf16_f32 v179, v102, v103\n" \
"v_mfma_f32_16x16x32_bf16 a[84:87], a[252:255], a[212:215], a[84:87]\n" \
"s_add_u32 m0, s70, 91136\n" \
"ds_read_b64_tr_b16 a[206:207], v241 offset:44032\n" \
"v_mfma_f32_16x16x32_bf16 a[84:87], a[236:239], a[208:211], a[84:87]\n" \
"s_waitcnt lgkmcnt(9)\n" \
"s_add_u32 s74, s69, 786560\n" \
"ds_write_b128 v244, v[176:179] offset:36864\n" \
"v_mfma_f32_16x16x32_bf16 a[24:27], a[224:227], a[216:219], a[24:27]\n" \
"buffer_load_dwordx4 v246, s[28:31], s74 offen lds\n" \
"v_mfma_f32_16x16x32_bf16 a[24:27], a[240:243], a[220:223], a[24:27]\n" \
"buffer_load_dwordx4 a[172:175], v248, s[32:35], s37 offen offset:1024 sc1\n" \
"v_mfma_f32_16x16x32_bf16 a[28:31], a[244:247], a[220:223], a[28:31]\n" \
"buffer_load_dwordx4 a[176:179], v248, s[32:35], s79 offen offset:2048 sc1\n" \
"v_mul_f32 v104, s84, v104\n" \
"v_mfma_f32_16x16x32_bf16 a[28:31], a[228:231], a[216:219], a[28:31]\n" \
"ds_read_b64_tr_b16 a[208:209], v240 offset:34304\n" \
"ds_read_b64_tr_b16 a[210:211], v240 offset:36352\n" \
"v_mfma_f32_16x16x32_bf16 a[88:91], a[232:235], a[216:219], a[88:91]\n" \
"ds_read_b64_tr_b16 a[212:213], v240 offset:42496\n" \
"v_mul_f32 v105, s84, v105\n" \
"v_mfma_f32_16x16x32_bf16 a[88:91], a[248:251], a[220:223], a[88:91]\n" \
"v_mul_f32 v106, s84, v106\n" \
"v_mul_f32 v107, s84, v107\n" \
"v_mfma_f32_16x16x32_bf16 a[92:95], a[252:255], a[220:223], a[92:95]\n" \
"ds_read_b64_tr_b16 a[214:215], v240 offset:44544\n" \
"v_mul_f32 v108, s84, v108\n" \
"v_mfma_f32_16x16x32_bf16 a[92:95], a[236:239], a[216:219], a[92:95]\n" \
"s_waitcnt lgkmcnt(9)\n" \
"v_mul_f32 v109, s84, v109\n" \
"v_mul_f32 v110, s84, v110\n" \
"v_mfma_f32_16x16x32_bf16 a[32:35], a[224:227], a[192:195], a[32:35]\n" \
"ds_read_b64_tr_b16 a[216:217], v241 offset:34304\n" \
"v_mul_f32 v111, s84, v111\n" \
"v_mfma_f32_16x16x32_bf16 a[32:35], a[240:243], a[196:199], a[32:35]\n" \
"s_add_u32 s85, s71, 4096\n" \
"v_cvt_pk_bf16_f32 v180, v104, v105\n" \
"v_cvt_pk_bf16_f32 v181, v106, v107\n" \
"v_mfma_f32_16x16x32_bf16 a[36:39], a[244:247], a[196:199], a[36:39]\n" \
"buffer_load_dwordx4 a[180:183], v248, s[32:35], s80 offen offset:2048 sc1\n" \
"v_cvt_pk_bf16_f32 v182, v108, v109\n" \
"v_mfma_f32_16x16x32_bf16 a[36:39], a[228:231], a[192:195], a[36:39]\n" \
"ds_read_b64_tr_b16 a[218:219], v241 offset:36352\n" \
"ds_read_b64_tr_b16 a[220:221], v241 offset:42496\n" \
"v_mfma_f32_16x16x32_bf16 a[96:99], a[232:235], a[192:195], a[96:99]\n" \
"ds_read_b64_tr_b16 a[222:223], v241 offset:44544\n" \
"v_cvt_pk_bf16_f32 v183, v110, v111\n" \
"v_mfma_f32_16x16x32_bf16 a[96:99], a[248:251], a[196:199], a[96:99]\n" \
"buffer_store_dwordx4 v[148:151], v248, s[32:35], s85 offen offset:1024 sc1\n" \
"v_mfma_f32_16x16x32_bf16 a[100:103], a[252:255], a[196:199], a[100:103]\n" \
"ds_write_b128 v244, v[180:183] offset:37888\n" \
"v_mfma_f32_16x16x32_bf16 a[100:103], a[236:239], a[192:195], a[100:103]\n" \
"s_waitcnt lgkmcnt(10)\n" \
"v_mfma_f32_16x16x32_bf16 a[40:43], a[224:227], a[200:203], a[40:43]\n" \
"v_mul_f32 v112, s84, v112\n" \
"v_mul_f32 v113, s84, v113\n" \
"v_mfma_f32_16x16x32_bf16 a[40:43], a[240:243], a[204:207], a[40:43]\n" \
"v_mul_f32 v114, s84, v114\n" \
"v_mul_f32 v115, s84, v115\n" \
"v_mfma_f32_16x16x32_bf16 a[44:47], a[244:247], a[204:207], a[44:47]\n" \
"buffer_load_dwordx4 a[184:187], v248, s[32:35], s36 offen offset:2048 sc1\n" \
"v_mul_f32 v116, s84, v116\n" \
"v_mfma_f32_16x16x32_bf16 a[44:47], a[228:231], a[200:203], a[44:47]\n" \
"v_mul_f32 v117, s84, v117\n" \
"v_mul_f32 v118, s84, v118\n" \
"v_mfma_f32_16x16x32_bf16 a[104:107], a[232:235], a[200:203], a[104:107]\n" \
"s_waitcnt lgkmcnt(1)\n" \
"s_barrier\n" \
"v_mul_f32 v119, s84, v119\n" \
"v_mfma_f32_16x16x32_bf16 a[104:107], a[248:251], a[204:207], a[104:107]\n" \
"s_add_u32 m0, s70, 32768\n" \
"ds_read_b64_tr_b16 a[192:193], v240 offset:49152\n" \
"ds_read_b64_tr_b16 a[194:195], v240 offset:51200\n" \
"v_mfma_f32_16x16x32_bf16 a[108:111], a[252:255], a[204:207], a[108:111]\n" \
"s_add_u32 s74, s69, 1048576\n" \
"buffer_load_dwordx4 v246, s[28:31], s74 offen lds\n" \
"v_mfma_f32_16x16x32_bf16 a[108:111], a[236:239], a[200:203], a[108:111]\n" \
"ds_read_b64_tr_b16 a[196:197], v240 offset:57344\n" \
"ds_read_b64_tr_b16 a[198:199], v240 offset:59392\n" \
"v_mfma_f32_16x16x32_bf16 a[48:51], a[224:227], a[208:211], a[48:51]\n" \
"ds_read_b64_tr_b16 a[200:201], v241 offset:49152\n" \
"v_cvt_pk_bf16_f32 v184, v112, v113\n" \
"v_mfma_f32_16x16x32_bf16 a[48:51], a[240:243], a[212:215], a[48:51]\n" \
"v_cvt_pk_bf16_f32 v185, v114, v115\n" \
"v_cvt_pk_bf16_f32 v186, v116, v117\n" \
"v_mfma_f32_16x16x32_bf16 a[52:55], a[244:247], a[212:215], a[52:55]\n" \
"buffer_load_dwordx4 a[188:191], v248, s[32:35], s37 offen offset:2048 sc1\n" \
"v_cvt_pk_bf16_f32 v187, v118, v119\n" \
"v_mfma_f32_16x16x32_bf16 a[52:55], a[228:231], a[208:211], a[52:55]\n" \
"ds_read_b64_tr_b16 a[202:203], v241 offset:51200\n" \
"v_mfma_f32_16x16x32_bf16 a[112:115], a[232:235], a[208:211], a[112:115]\n" \
"ds_read_b64_tr_b16 a[204:205], v241 offset:57344\n" \
"v_mfma_f32_16x16x32_bf16 a[112:115], a[248:251], a[212:215], a[112:115]\n" \
"ds_write_b128 v244, v[184:187] offset:38912\n" \
"v_mfma_f32_16x16x32_bf16 a[116:119], a[252:255], a[212:215], a[116:119]\n" \
"ds_read_b64_tr_b16 a[206:207], v241 offset:59392\n" \
"v_mul_f32 v120, s84, v120\n" \
"v_mfma_f32_16x16x32_bf16 a[116:119], a[236:239], a[208:211], a[116:119]\n" \
"v_mul_f32 v121, s84, v121\n" \
"v_mul_f32 v122, s84, v122\n" \
"v_mfma_f32_16x16x32_bf16 a[56:59], a[224:227], a[216:219], a[56:59]\n" \
"ds_read_b64_tr_b16 a[208:209], v240 offset:49664\n" \
"v_mul_f32 v123, s84, v123\n" \
"v_mfma_f32_16x16x32_bf16 a[56:59], a[240:243], a[220:223], a[56:59]\n" \
"v_mul_f32 v124, s84, v124\n" \
"v_mul_f32 v125, s84, v125\n" \
"v_mfma_f32_16x16x32_bf16 a[60:63], a[244:247], a[220:223], a[60:63]\n" \
"s_add_u32 m0, s70, 33792\n" \
"ds_read_b64_tr_b16 a[210:211], v240 offset:51712\n" \
"v_mul_f32 v126, s84, v126\n" \
"v_mfma_f32_16x16x32_bf16 a[60:63], a[228:231], a[216:219], a[60:63]\n" \
"s_add_u32 s74, s69, 1048704\n" \
"v_mul_f32 v127, s84, v127\n" \
"v_cvt_pk_bf16_f32 v188, v120, v121\n" \
"v_mfma_f32_16x16x32_bf16 a[120:123], a[232:235], a[216:219], a[120:123]\n" \
"buffer_load_dwordx4 v246, s[28:31], s74 offen lds\n" \
"v_mfma_f32_16x16x32_bf16 a[120:123], a[248:251], a[220:223], a[120:123]\n" \
"s_add_u32 s85, s71, 4096\n" \
"ds_read_b64_tr_b16 a[212:213], v240 offset:57856\n" \
"v_cvt_pk_bf16_f32 v189, v122, v123\n" \
"v_mfma_f32_16x16x32_bf16 a[124:127], a[252:255], a[220:223], a[124:127]\n" \
"ds_read_b64_tr_b16 a[214:215], v240 offset:59904\n" \
"v_cvt_pk_bf16_f32 v190, v124, v125\n" \
"v_mfma_f32_16x16x32_bf16 a[124:127], a[236:239], a[216:219], a[124:127]\n" \
"s_waitcnt lgkmcnt(9)\n" \
"buffer_store_dwordx4 v[152:155], v248, s[32:35], s85 offen offset:2048 sc1\n" \
"v_mfma_f32_16x16x32_bf16 a[0:3], a[128:131], a[192:195], a[0:3]\n" \
"ds_read_b64_tr_b16 a[216:217], v241 offset:49664\n" \
"v_cvt_pk_bf16_f32 v191, v126, v127\n" \
"v_mfma_f32_16x16x32_bf16 a[0:3], a[144:147], a[196:199], a[0:3]\n" \
"buffer_load_dwordx4 a[224:227], v248, s[32:35], s79 offen offset:3072 sc1\n" \
"v_mfma_f32_16x16x32_bf16 a[4:7], a[148:151], a[196:199], a[4:7]\n" \
"ds_read_b64_tr_b16 a[218:219], v241 offset:51712\n" \
"v_mfma_f32_16x16x32_bf16 a[4:7], a[132:135], a[192:195], a[4:7]\n" \
"ds_write_b128 v244, v[188:191] offset:39936\n" \
"v_mfma_f32_16x16x32_bf16 a[64:67], a[136:139], a[192:195], a[64:67]\n" \
"ds_read_b64_tr_b16 a[220:221], v241 offset:57856\n" \
"v_mfma_f32_16x16x32_bf16 a[64:67], a[152:155], a[196:199], a[64:67]\n" \
"v_mfma_f32_16x16x32_bf16 a[68:71], a[156:159], a[196:199], a[68:71]\n" \
"ds_read_b64_tr_b16 a[222:223], v241 offset:59904\n" \
"v_mfma_f32_16x16x32_bf16 a[68:71], a[140:143], a[192:195], a[68:71]\n" \
"s_waitcnt lgkmcnt(9)\n" \
"v_mfma_f32_16x16x32_bf16 a[8:11], a[128:131], a[200:203], a[8:11]\n" \
"ds_read_b64_tr_b16 a[192:193], v240 offset:50176\n" \
"v_mfma_f32_16x16x32_bf16 a[8:11], a[144:147], a[204:207], a[8:11]\n" \
"buffer_load_dwordx4 a[228:231], v248, s[32:35], s80 offen offset:3072 sc1\n" \
"v_mfma_f32_16x16x32_bf16 a[12:15], a[148:151], a[204:207], a[12:15]\n" \
"s_add_u32 m0, s70, 40960\n" \
"s_add_u32 s74, s69, 1310720\n" \
"ds_read_b64_tr_b16 a[194:195], v240 offset:52224\n" \
"v_mfma_f32_16x16x32_bf16 a[12:15], a[132:135], a[200:203], a[12:15]\n" \
"v_mfma_f32_16x16x32_bf16 a[72:75], a[136:139], a[200:203], a[72:75]\n" \
"buffer_load_dwordx4 v246, s[28:31], s74 offen lds\n" \
"v_mfma_f32_16x16x32_bf16 a[72:75], a[152:155], a[204:207], a[72:75]\n" \
"ds_read_b64_tr_b16 a[196:197], v240 offset:58368\n" \
"v_mfma_f32_16x16x32_bf16 a[76:79], a[156:159], a[204:207], a[76:79]\n" \
"ds_read_b64_tr_b16 a[198:199], v240 offset:60416\n" \
"v_mfma_f32_16x16x32_bf16 a[76:79], a[140:143], a[200:203], a[76:79]\n" \
"s_waitcnt lgkmcnt(9)\n" \
"v_mfma_f32_16x16x32_bf16 a[16:19], a[128:131], a[208:211], a[16:19]\n" \
"ds_read_b64_tr_b16 a[200:201], v241 offset:50176\n" \
"v_mfma_f32_16x16x32_bf16 a[16:19], a[144:147], a[212:215], a[16:19]\n" \
"buffer_load_dwordx4 a[232:235], v248, s[32:35], s36 offen offset:3072 sc1\n" \
"v_mfma_f32_16x16x32_bf16 a[20:23], a[148:151], a[212:215], a[20:23]\n" \
"ds_read_b64_tr_b16 a[202:203], v241 offset:52224\n" \
"v_mfma_f32_16x16x32_bf16 a[20:23], a[132:135], a[208:211], a[20:23]\n" \
"v_mfma_f32_16x16x32_bf16 a[80:83], a[136:139], a[208:211], a[80:83]\n" \
"ds_read_b64_tr_b16 a[204:205], v241 offset:58368\n" \
"v_mfma_f32_16x16x32_bf16 a[80:83], a[152:155], a[212:215], a[80:83]\n" \
"v_mfma_f32_16x16x32_bf16 a[84:87], a[156:159], a[212:215], a[84:87]\n" \
"s_add_u32 m0, s70, 41984\n" \
"ds_read_b64_tr_b16 a[206:207], v241 offset:60416\n" \
"v_mfma_f32_16x16x32_bf16 a[84:87], a[140:143], a[208:211], a[84:87]\n" \
"s_waitcnt lgkmcnt(8)\n" \
"s_add_u32 s74, s69, 1310848\n" \
"v_mfma_f32_16x16x32_bf16 a[24:27], a[128:131], a[216:219], a[24:27]\n" \
"buffer_load_dwordx4 v246, s[28:31], s74 offen lds\n" \
"v_mfma_f32_16x16x32_bf16 a[24:27], a[144:147], a[220:223], a[24:27]\n" \
"s_add_u32 s85, s71, 4096\n" \
"buffer_load_dwordx4 a[236:239], v248, s[32:35], s37 offen offset:3072 sc1\n" \
"v_mfma_f32_16x16x32_bf16 a[28:31], a[148:151], a[220:223], a[28:31]\n" \
"s_add_u32 s79, s79, 4096\n" \
"buffer_load_dwordx4 a[240:243], v248, s[32:35], s79 offen offset:0 sc1\n" \
"v_mfma_f32_16x16x32_bf16 a[28:31], a[132:135], a[216:219], a[28:31]\n" \
"ds_read_b64_tr_b16 a[208:209], v240 offset:50688\n" \
"ds_read_b64_tr_b16 a[210:211], v240 offset:52736\n" \
"v_mfma_f32_16x16x32_bf16 a[88:91], a[136:139], a[216:219], a[88:91]\n" \
"ds_read_b64_tr_b16 a[212:213], v240 offset:58880\n" \
"v_mfma_f32_16x16x32_bf16 a[88:91], a[152:155], a[220:223], a[88:91]\n" \
"buffer_store_dwordx4 v[156:159], v248, s[32:35], s85 offen offset:3072 sc1\n" \
"v_mfma_f32_16x16x32_bf16 a[92:95], a[156:159], a[220:223], a[92:95]\n" \
"ds_read_b64_tr_b16 a[214:215], v240 offset:60928\n" \
"v_mfma_f32_16x16x32_bf16 a[92:95], a[140:143], a[216:219], a[92:95]\n" \
"s_waitcnt lgkmcnt(8)\n" \
"v_mfma_f32_16x16x32_bf16 a[32:35], a[128:131], a[192:195], a[32:35]\n" \
"ds_read_b64_tr_b16 a[216:217], v241 offset:50688\n" \
"v_mfma_f32_16x16x32_bf16 a[32:35], a[144:147], a[196:199], a[32:35]\n" \
"s_add_u32 s80, s80, 4096\n" \
"v_mfma_f32_16x16x32_bf16 a[36:39], a[148:151], a[196:199], a[36:39]\n" \
"buffer_load_dwordx4 a[244:247], v248, s[32:35], s80 offen offset:0 sc1\n" \
"v_mfma_f32_16x16x32_bf16 a[36:39], a[132:135], a[192:195], a[36:39]\n" \
"ds_read_b64_tr_b16 a[218:219], v241 offset:52736\n" \
"ds_read_b64_tr_b16 a[220:221], v241 offset:58880\n" \
"v_mfma_f32_16x16x32_bf16 a[96:99], a[136:139], a[192:195], a[96:99]\n" \
"ds_read_b64_tr_b16 a[222:223], v241 offset:60928\n" \
"v_xor_b32 v240, 0x30000, v240\n" \
"v_mfma_f32_16x16x32_bf16 a[96:99], a[152:155], a[196:199], a[96:99]\n" \
"v_xor_b32 v241, 0x30000, v241\n" \
"v_mfma_f32_16x16x32_bf16 a[100:103], a[156:159], a[196:199], a[100:103]\n" \
"v_mfma_f32_16x16x32_bf16 a[100:103], a[140:143], a[192:195], a[100:103]\n" \
"s_waitcnt lgkmcnt(8)\n" \
"v_mfma_f32_16x16x32_bf16 a[40:43], a[128:131], a[200:203], a[40:43]\n" \
"v_mfma_f32_16x16x32_bf16 a[40:43], a[144:147], a[204:207], a[40:43]\n" \
"s_add_u32 s36, s36, 4096\n" \
"v_mfma_f32_16x16x32_bf16 a[44:47], a[148:151], a[204:207], a[44:47]\n" \
"buffer_load_dwordx4 a[248:251], v248, s[32:35], s36 offen offset:0 sc1\n" \
"v_mfma_f32_16x16x32_bf16 a[44:47], a[132:135], a[200:203], a[44:47]\n" \
"v_mfma_f32_16x16x32_bf16 a[104:107], a[136:139], a[200:203], a[104:107]\n" \
"s_waitcnt vmcnt(32) lgkmcnt(0)\n" \
"s_barrier\n" \
"s_add_u32 m0, s70, 49152\n" \
"v_mfma_f32_16x16x32_bf16 a[104:107], a[152:155], a[204:207], a[104:107]\n" \
"s_add_u32 s74, s69, 1572864\n" \
"ds_read_b64_tr_b16 a[192:193], v240 offset:0\n" \
"ds_read_b64_tr_b16 a[194:195], v240 offset:2048\n" \
"v_mfma_f32_16x16x32_bf16 a[108:111], a[156:159], a[204:207], a[108:111]\n" \
"buffer_load_dwordx4 v246, s[28:31], s74 offen lds\n" \
"v_mfma_f32_16x16x32_bf16 a[108:111], a[140:143], a[200:203], a[108:111]\n" \
"ds_read_b64_tr_b16 a[196:197], v240 offset:8192\n" \
"ds_read_b64_tr_b16 a[198:199], v240 offset:10240\n" \
"v_mfma_f32_16x16x32_bf16 a[48:51], a[128:131], a[208:211], a[48:51]\n" \
"ds_read_b64_tr_b16 a[200:201], v241 offset:0\n" \
"v_mfma_f32_16x16x32_bf16 a[48:51], a[144:147], a[212:215], a[48:51]\n" \
"s_add_u32 s37, s37, 4096\n" \
"v_mfma_f32_16x16x32_bf16 a[52:55], a[148:151], a[212:215], a[52:55]\n" \
"buffer_load_dwordx4 a[252:255], v248, s[32:35], s37 offen offset:0 sc1\n" \
"v_mfma_f32_16x16x32_bf16 a[52:55], a[132:135], a[208:211], a[52:55]\n" \
"ds_read_b64_tr_b16 a[202:203], v241 offset:2048\n" \
"v_mfma_f32_16x16x32_bf16 a[112:115], a[136:139], a[208:211], a[112:115]\n" \
"ds_read_b64_tr_b16 a[204:205], v241 offset:8192\n" \
"v_mfma_f32_16x16x32_bf16 a[112:115], a[152:155], a[212:215], a[112:115]\n" \
"s_add_u32 s85, s94, 0\n" \
"v_mfma_f32_16x16x32_bf16 a[116:119], a[156:159], a[212:215], a[116:119]\n" \
"buffer_store_dwordx4 v[160:163], v248, s[32:35], s85 offen offset:0 sc1\n" \
"v_mfma_f32_16x16x32_bf16 a[116:119], a[140:143], a[208:211], a[116:119]\n" \
"ds_read_b64_tr_b16 a[206:207], v241 offset:10240\n" \
"v_mfma_f32_16x16x32_bf16 a[56:59], a[128:131], a[216:219], a[56:59]\n" \
"ds_read_b64_tr_b16 a[208:209], v240 offset:512\n" \
"v_mfma_f32_16x16x32_bf16 a[56:59], a[144:147], a[220:223], a[56:59]\n" \
"v_mfma_f32_16x16x32_bf16 a[60:63], a[148:151], a[220:223], a[60:63]\n" \
"s_add_u32 m0, s70, 50176\n" \
"ds_read_b64_tr_b16 a[210:211], v240 offset:2560\n" \
"v_mfma_f32_16x16x32_bf16 a[60:63], a[132:135], a[216:219], a[60:63]\n" \
"s_add_u32 s74, s69, 1572992\n" \
"v_mfma_f32_16x16x32_bf16 a[120:123], a[136:139], a[216:219], a[120:123]\n" \
"buffer_load_dwordx4 v246, s[28:31], s74 offen lds\n" \
"v_mfma_f32_16x16x32_bf16 a[120:123], a[152:155], a[220:223], a[120:123]\n" \
"ds_read_b64_tr_b16 a[212:213], v240 offset:8704\n" \
"v_accvgpr_read_b32 v144, a0\n" \
"v_mfma_f32_16x16x32_bf16 a[124:127], a[156:159], a[220:223], a[124:127]\n" \
"s_waitcnt vmcnt(22)\n" \
"ds_read_b64_tr_b16 a[214:215], v240 offset:10752\n" \
"v_mfma_f32_16x16x32_bf16 a[124:127], a[140:143], a[216:219], a[124:127]\n" \
"s_waitcnt lgkmcnt(8)\n" \
"v_accvgpr_read_b32 v145, a1\n" \
"v_accvgpr_read_b32 v146, a2\n" \
  \
"v_mfma_f32_16x16x32_bf16 v[0:3], a[192:195], v[192:195], 0\n" \
"ds_read_b64_tr_b16 a[216:217], v241 offset:512\n" \
"v_accvgpr_read_b32 v147, a3\n" \
"v_mfma_f32_16x16x32_bf16 v[0:3], a[196:199], a[160:163], v[0:3]\n" \
"buffer_load_dwordx4 a[128:131], v248, s[32:35], s79 offen offset:1024 sc1\n" \
"v_accvgpr_read_b32 v148, a4\n" \
"v_mfma_f32_16x16x32_bf16 v[32:35], a[196:199], a[164:167], 0\n" \
"ds_read_b64_tr_b16 a[218:219], v241 offset:2560\n" \
"v_accvgpr_read_b32 v149, a5\n" \
"v_mfma_f32_16x16x32_bf16 v[32:35], a[192:195], v[196:199], v[32:35]\n" \
"v_accvgpr_read_b32 v150, a6\n" \
"v_accvgpr_read_b32 v151, a7\n" \
"v_mfma_f32_16x16x32_bf16 v[64:67], a[192:195], v[200:203], 0\n" \
"ds_read_b64_tr_b16 a[220:221], v241 offset:8704\n" \
"v_max3_f32 v250, |v144|, |v145|, v250\n" \
"v_mfma_f32_16x16x32_bf16 v[64:67], a[196:199], a[168:171], v[64:67]\n" \
"v_max3_f32 v250, |v146|, |v147|, v250\n" \
"v_max3_f32 v250, |v148|, |v149|, v250\n" \
"v_mfma_f32_16x16x32_bf16 v[96:99], a[196:199], a[172:175], 0\n" \
"ds_read_b64_tr_b16 a[222:223], v241 offset:10752\n" \
"v_max3_f32 v250, |v150|, |v151|, v250\n" \
"v_mfma_f32_16x16x32_bf16 v[96:99], a[192:195], v[204:207], v[96:99]\n" \
"s_waitcnt lgkmcnt(8)\n" \
"v_cvt_pk_bf16_f32 v144, v144, v145\n" \
"v_cvt_pk_bf16_f32 v145, v146, v147\n" \
"v_mfma_f32_16x16x32_bf16 v[4:7], a[200:203], v[192:195], 0\n" \
"ds_read_b64_tr_b16 a[192:193], v240 offset:1024\n" \
"v_cvt_pk_bf16_f32 v146, v148, v149\n" \
"v_mfma_f32_16x16x32_bf16 v[4:7], a[204:207], a[160:163], v[4:7]\n" \
"buffer_load_dwordx4 a[132:135], v248, s[32:35], s80 offen offset:1024 sc1\n" \
"v_cvt_pk_bf16_f32 v147, v150, v151\n" \
"v_mfma_f32_16x16x32_bf16 v[36:39], a[204:207], a[164:167], 0\n" \
"s_add_u32 m0, s70, 57344\n" \
"s_add_u32 s74, s69, 1835008\n" \
"ds_read_b64_tr_b16 a[194:195], v240 offset:3072\n" \
"v_mfma_f32_16x16x32_bf16 v[36:39], a[200:203], v[196:199], v[36:39]\n" \
"ds_write_b128 v249, v[144:147] offset:0\n" \
"v_accvgpr_read_b32 v152, a64\n" \
"v_mfma_f32_16x16x32_bf16 v[68:71], a[200:203], v[200:203], 0\n" \
"buffer_load_dwordx4 v246, s[28:31], s74 offen lds\n" \
"v_mfma_f32_16x16x32_bf16 v[68:71], a[204:207], a[168:171], v[68:71]\n" \
"ds_read_b64_tr_b16 a[196:197], v240 offset:9216\n" \
"v_accvgpr_read_b32 v153, a65\n" \
"v_mfma_f32_16x16x32_bf16 v[100:103], a[204:207], a[172:175], 0\n" \
"ds_read_b64_tr_b16 a[198:199], v240 offset:11264\n" \
"v_accvgpr_read_b32 v154, a66\n" \
"v_mfma_f32_16x16x32_bf16 v[100:103], a[200:203], v[204:207], v[100:103]\n" \
"s_waitcnt lgkmcnt(9)\n" \
"v_accvgpr_read_b32 v155, a67\n" \
"v_accvgpr_read_b32 v156, a68\n" \
"v_mfma_f32_16x16x32_bf16 v[8:11], a[208:211], v[192:195], 0\n" \
"s_add_u32 s85, s71, 589824\n" \
"buffer_store_dwordx4 v[144:147], v248, s[32:35], s85 offen offset:0 sc1\n" \
"v_mfma_f32_16x16x32_bf16 v[8:11], a[212:215], a[160:163], v[8:11]\n" \
"buffer_load_dwordx4 a[136:139], v248, s[32:35], s36 offen offset:1024 sc1\n" \
"v_accvgpr_read_b32 v157, a69\n" \
"v_mfma_f32_16x16x32_bf16 v[40:43], a[212:215], a[164:167], 0\n" \
"ds_read_b64_tr_b16 a[200:201], v241 offset:1024\n" \
"ds_read_b64_tr_b16 a[202:203], v241 offset:3072\n" \
"v_mfma_f32_16x16x32_bf16 v[40:43], a[208:211], v[196:199], v[40:43]\n" \
"v_accvgpr_read_b32 v158, a70\n" \
"v_accvgpr_read_b32 v159, a71\n" \
"v_mfma_f32_16x16x32_bf16 v[72:75], a[208:211], v[200:203], 0\n" \
"ds_read_b64_tr_b16 a[204:205], v241 offset:9216\n" \
"v_max3_f32 v250, |v152|, |v153|, v250\n" \
"v_mfma_f32_16x16x32_bf16 v[72:75], a[212:215], a[168:171], v[72:75]\n" \
"v_max3_f32 v250, |v154|, |v155|, v250\n" \
"v_max3_f32 v250, |v156|, |v157|, v250\n" \
"v_mfma_f32_16x16x32_bf16 v[104:107], a[212:215], a[172:175], 0\n" \
"s_add_u32 m0, s70, 58368\n" \
"ds_read_b64_tr_b16 a[206:207], v241 offset:11264\n" \
"v_max3_f32 v250, |v158|, |v159|, v250\n" \
"v_mfma_f32_16x16x32_bf16 v[104:107], a[208:211], v[204:207], v[104:107]\n" \
"s_waitcnt lgkmcnt(9)\n" \
"v_cvt_pk_bf16_f32 v152, v152, v153\n" \
"v_cvt_pk_bf16_f32 v153, v154, v155\n" \
"v_mfma_f32_16x16x32_bf16 v[12:15], a[216:219], v[192:195], 0\n" \
"s_add_u32 s74, s69, 1835136\n" \
"buffer_load_dwordx4 v246, s[28:31], s74 offen lds\n" \
"v_mfma_f32_16x16x32_bf16 v[12:15], a[220:223], a[160:163], v[12:15]\n" \
"buffer_load_dwordx4 a[140:143], v248, s[32:35], s37 offen offset:1024 sc1\n" \
"v_cvt_pk_bf16_f32 v154, v156, v157\n" \
"v_mfma_f32_16x16x32_bf16 v[44:47], a[220:223], a[164:167], 0\n" \
"buffer_load_dwordx4 a[144:147], v248, s[32:35], s79 offen offset:2048 sc1\n" \
"v_cvt_pk_bf16_f32 v155, v158, v159\n" \
"v_mfma_f32_16x16x32_bf16 v[44:47], a[216:219], v[196:199], v[44:47]\n" \
"ds_read_b64_tr_b16 a[208:209], v240 offset:1536\n" \
"ds_read_b64_tr_b16 a[210:211], v240 offset:3584\n" \
"v_mfma_f32_16x16x32_bf16 v[76:79], a[216:219], v[200:203], 0\n" \
"ds_read_b64_tr_b16 a[212:213], v240 offset:9728\n" \
"v_accvgpr_write_b32 a64, v152\n" \
"v_mfma_f32_16x16x32_bf16 v[76:79], a[220:223], a[168:171], v[76:79]\n" \
"v_accvgpr_write_b32 a65, v153\n" \
"v_accvgpr_write_b32 a66, v154\n" \
"v_mfma_f32_16x16x32_bf16 v[108:111], a[220:223], a[172:175], 0\n" \
"ds_read_b64_tr_b16 a[214:215], v240 offset:11776\n" \
"v_accvgpr_write_b32 a67, v155\n" \
"v_mfma_f32_16x16x32_bf16 v[108:111], a[216:219], v[204:207], v[108:111]\n" \
"s_waitcnt lgkmcnt(8)\n" \
"v_mfma_f32_16x16x32_bf16 v[16:19], a[192:195], v[192:195], 0\n" \
"ds_read_b64_tr_b16 a[216:217], v241 offset:1536\n" \
"v_mfma_f32_16x16x32_bf16 v[16:19], a[196:199], a[160:163], v[16:19]\n" \
"v_mfma_f32_16x16x32_bf16 v[48:51], a[196:199], a[164:167], 0\n" \
"buffer_load_dwordx4 a[148:151], v248, s[32:35], s80 offen offset:2048 sc1\n" \
"v_mfma_f32_16x16x32_bf16 v[48:51], a[192:195], v[196:199], v[48:51]\n" \
"s_add_u32 s85, s94, 0\n" \
"ds_read_b64_tr_b16 a[218:219], v241 offset:3584\n" \
"ds_read_b64_tr_b16 a[220:221], v241 offset:9728\n" \
"v_mfma_f32_16x16x32_bf16 v[80:83], a[192:195], v[200:203], 0\n" \
"ds_read_b64_tr_b16 a[222:223], v241 offset:11776\n" \
"v_mfma_f32_16x16x32_bf16 v[80:83], a[196:199], a[168:171], v[80:83]\n" \
"buffer_store_dwordx4 v[164:167], v248, s[32:35], s85 offen offset:1024 sc1\n" \
"v_mfma_f32_16x16x32_bf16 v[112:115], a[196:199], a[172:175], 0\n" \
"v_mfma_f32_16x16x32_bf16 v[112:115], a[192:195], v[204:207], v[112:115]\n" \
"s_waitcnt lgkmcnt(8)\n" \
"v_mfma_f32_16x16x32_bf16 v[20:23], a[200:203], v[192:195], 0\n" \
"v_mfma_f32_16x16x32_bf16 v[20:23], a[204:207], a[160:163], v[20:23]\n" \
"v_mfma_f32_16x16x32_bf16 v[52:55], a[204:207], a[164:167], 0\n" \
"buffer_load_dwordx4 a[152:155], v248, s[32:35], s36 offen offset:2048 sc1\n" \
"v_mfma_f32_16x16x32_bf16 v[52:55], a[200:203], v[196:199], v[52:55]\n" \
"v_mfma_f32_16x16x32_bf16 v[84:87], a[200:203], v[200:203], 0\n" \
"s_waitcnt lgkmcnt(0)\n" \
"s_barrier\n" \
"s_add_u32 m0, s70, 65536\n" \
"v_mfma_f32_16x16x32_bf16 v[84:87], a[204:207], a[168:171], v[84:87]\n" \
"s_add_u32 s74, s69, 2097152\n" \
"ds_read_b64_tr_b16 a[192:193], v240 offset:16384\n" \
"ds_read_b64_tr_b16 a[194:195], v240 offset:18432\n" \
"v_mfma_f32_16x16x32_bf16 v[116:119], a[204:207], a[172:175], 0\n" \
"buffer_load_dwordx4 v246, s[28:31], s74 offen lds\n" \
"v_mfma_f32_16x16x32_bf16 v[116:119], a[200:203], v[204:207], v[116:119]\n" \
"s_lshl_b32 s85, s68, 4\n" \
"ds_read_b64_tr_b16 a[196:197], v240 offset:24576\n" \
"ds_read_b64_tr_b16 a[198:199], v240 offset:26624\n" \
"v_mfma_f32_16x16x32_bf16 v[24:27], a[208:211], v[192:195], 0\n" \
"s_add_u32 s85, s85, s67\n" \
"ds_read_b64_tr_b16 a[200:201], v241 offset:16384\n" \
"v_accvgpr_read_b32 v144, a8\n" \
"v_mfma_f32_16x16x32_bf16 v[24:27], a[212:215], a[160:163], v[24:27]\n" \
"s_lshl2_add_u32 s85, s85, %[wid]\n" \
"v_accvgpr_read_b32 v145, a9\n" \
"v_accvgpr_read_b32 v146, a10\n" \
"v_mfma_f32_16x16x32_bf16 v[56:59], a[212:215], a[164:167], 0\n" \
"s_lshl_b32 s85, s85, 2\n" \
"buffer_load_dwordx4 a[156:159], v248, s[32:35], s37 offen offset:2048 sc1\n" \
"v_accvgpr_read_b32 v147, a11\n" \
"v_mfma_f32_16x16x32_bf16 v[56:59], a[208:211], v[196:199], v[56:59]\n" \
"s_add_u32 s85, s85, 81920\n" \
"ds_read_b64_tr_b16 a[202:203], v241 offset:18432\n" \
"v_accvgpr_read_b32 v148, a12\n" \
"v_mfma_f32_16x16x32_bf16 v[88:91], a[208:211], v[200:203], 0\n" \
"s_waitcnt vmcnt(19)\n" \
"buffer_store_dword v239, off, s[32:35], s85 sc1\n" \
"v_accvgpr_read_b32 v149, a13\n" \
"v_mfma_f32_16x16x32_bf16 v[88:91], a[212:215], a[168:171], v[88:91]\n" \
"ds_read_b64_tr_b16 a[204:205], v241 offset:24576\n" \
"v_accvgpr_read_b32 v150, a14\n" \
"v_mfma_f32_16x16x32_bf16 v[120:123], a[212:215], a[172:175], 0\n" \
"ds_read_b64_tr_b16 a[206:207], v241 offset:26624\n" \
"v_accvgpr_read_b32 v151, a15\n" \
"v_mfma_f32_16x16x32_bf16 v[120:123], a[208:211], v[204:207], v[120:123]\n" \
"v_max3_f32 v250, |v144|, |v145|, v250\n" \
"v_max3_f32 v250, |v146|, |v147|, v250\n" \
"v_mfma_f32_16x16x32_bf16 v[28:31], a[216:219], v[192:195], 0\n" \
"ds_read_b64_tr_b16 a[208:209], v240 offset:16896\n" \
"v_max3_f32 v250, |v148|, |v149|, v250\n" \
"v_mfma_f32_16x16x32_bf16 v[28:31], a[220:223], a[160:163], v[28:31]\n" \
"v_max3_f32 v250, |v150|, |v151|, v250\n" \
"v_cvt_pk_bf16_f32 v144, v144, v145\n" \
"v_mfma_f32_16x16x32_bf16 v[60:63], a[220:223], a[164:167], 0\n" \
"s_add_u32 m0, s70, 66560\n" \
"ds_read_b64_tr_b16 a[210:211], v240 offset:18944\n" \
"v_cvt_pk_bf16_f32 v145, v146, v147\n" \
"v_mfma_f32_16x16x32_bf16 v[60:63], a[216:219], v[196:199], v[60:63]\n" \
"s_add_u32 s74, s69, 2097280\n" \
"v_cvt_pk_bf16_f32 v146, v148, v149\n" \
"v_cvt_pk_bf16_f32 v147, v150, v151\n" \
"v_mfma_f32_16x16x32_bf16 v[92:95], a[216:219], v[200:203], 0\n" \
"buffer_load_dwordx4 v246, s[28:31], s74 offen lds\n" \
"v_mfma_f32_16x16x32_bf16 v[92:95], a[220:223], a[168:171], v[92:95]\n" \
"ds_read_b64_tr_b16 a[212:213], v240 offset:25088\n" \
"v_mfma_f32_16x16x32_bf16 v[124:127], a[220:223], a[172:175], 0\n" \
"ds_read_b64_tr_b16 a[214:215], v240 offset:27136\n" \
"v_mfma_f32_16x16x32_bf16 v[124:127], a[216:219], v[204:207], v[124:127]\n" \
"s_waitcnt lgkmcnt(8)\n" \
"ds_write_b128 v249, v[144:147] offset:1024\n" \
"v_accvgpr_read_b32 v152, a72\n" \
"v_mfma_f32_16x16x32_bf16 v[0:3], a[192:195], a[176:179], v[0:3]\n" \
"ds_read_b64_tr_b16 a[216:217], v241 offset:16896\n" \
"v_accvgpr_read_b32 v153, a73\n" \
"v_mfma_f32_16x16x32_bf16 v[0:3], a[196:199], a[224:227], v[0:3]\n" \
"s_add_u32 s85, s71, 589824\n" \
"buffer_load_dwordx4 v[192:195], v248, s[32:35], s79 offen offset:3072 sc1\n" \
"v_accvgpr_read_b32 v154, a74\n" \
"v_mfma_f32_16x16x32_bf16 v[32:35], a[196:199], a[228:231], v[32:35]\n" \
"ds_read_b64_tr_b16 a[218:219], v241 offset:18944\n" \
"v_accvgpr_read_b32 v155, a75\n" \
"v_mfma_f32_16x16x32_bf16 v[32:35], a[192:195], a[180:183], v[32:35]\n" \
"v_accvgpr_read_b32 v156, a76\n" \
"v_accvgpr_read_b32 v157, a77\n" \
"v_mfma_f32_16x16x32_bf16 v[64:67], a[192:195], a[184:187], v[64:67]\n" \
"buffer_store_dwordx4 v[144:147], v248, s[32:35], s85 offen offset:1024 sc1\n" \
"v_mfma_f32_16x16x32_bf16 v[64:67], a[196:199], a[232:235], v[64:67]\n" \
"ds_read_b64_tr_b16 a[220:221], v241 offset:25088\n" \
"v_accvgpr_read_b32 v158, a78\n" \
"v_mfma_f32_16x16x32_bf16 v[96:99], a[196:199], a[236:239], v[96:99]\n" \
"ds_read_b64_tr_b16 a[222:223], v241 offset:27136\n" \
"v_accvgpr_read_b32 v159, a79\n" \
"v_mfma_f32_16x16x32_bf16 v[96:99], a[192:195], a[188:191], v[96:99]\n" \
"s_waitcnt lgkmcnt(9)\n" \
"v_max3_f32 v250, |v152|, |v153|, v250\n" \
"v_max3_f32 v250, |v154|, |v155|, v250\n" \
"v_mfma_f32_16x16x32_bf16 v[4:7], a[200:203], a[176:179], v[4:7]\n" \
"ds_read_b64_tr_b16 a[192:193], v240 offset:17408\n" \
"v_max3_f32 v250, |v156|, |v157|, v250\n" \
"v_mfma_f32_16x16x32_bf16 v[4:7], a[204:207], a[224:227], v[4:7]\n" \
"buffer_load_dwordx4 v[196:199], v248, s[32:35], s80 offen offset:3072 sc1\n" \
"v_max3_f32 v250, |v158|, |v159|, v250\n" \
"v_mfma_f32_16x16x32_bf16 v[36:39], a[204:207], a[228:231], v[36:39]\n" \
".Lst0_5_%=:\n" \
"s_cbranch_execz .Lst0_9_%=\n" \
"s_add_u32 m0, s70, 73728\n" \
"ds_read_b64_tr_b16 a[194:195], v240 offset:19456\n" \
"v_cvt_pk_bf16_f32 v152, v152, v153\n" \
"v_mfma_f32_16x16x32_bf16 v[36:39], a[200:203], a[180:183], v[36:39]\n" \
"s_add_u32 s74, s69, 2359296\n" \
"v_cvt_pk_bf16_f32 v153, v154, v155\n" \
"v_cvt_pk_bf16_f32 v154, v156, v157\n" \
"v_mfma_f32_16x16x32_bf16 v[68:71], a[200:203], a[184:187], v[68:71]\n" \
"buffer_load_dwordx4 v246, s[28:31], s74 offen lds\n" \
"v_mfma_f32_16x16x32_bf16 v[68:71], a[204:207], a[232:235], v[68:71]\n" \
"ds_read_b64_tr_b16 a[196:197], v240 offset:25600\n" \
"v_cvt_pk_bf16_f32 v155, v158, v159\n" \
"v_mfma_f32_16x16x32_bf16 v[100:103], a[204:207], a[236:239], v[100:103]\n" \
"ds_read_b64_tr_b16 a[198:199], v240 offset:27648\n" \
"v_accvgpr_write_b32 a72, v152\n" \
"v_mfma_f32_16x16x32_bf16 v[100:103], a[200:203], a[188:191], v[100:103]\n" \
"s_waitcnt lgkmcnt(9)\n" \
"v_accvgpr_write_b32 a73, v153\n" \
"v_accvgpr_write_b32 a74, v154\n" \
"v_mfma_f32_16x16x32_bf16 v[8:11], a[208:211], a[176:179], v[8:11]\n" \
"ds_read_b64_tr_b16 a[200:201], v241 offset:17408\n" \
"v_accvgpr_write_b32 a75, v155\n" \
"v_mfma_f32_16x16x32_bf16 v[8:11], a[212:215], a[224:227], v[8:11]\n" \
"buffer_load_dwordx4 v[200:203], v248, s[32:35], s36 offen offset:3072 sc1\n" \
"v_mfma_f32_16x16x32_bf16 v[40:43], a[212:215], a[228:231], v[40:43]\n" \
"ds_read_b64_tr_b16 a[202:203], v241 offset:19456\n" \
"v_mfma_f32_16x16x32_bf16 v[40:43], a[208:211], a[180:183], v[40:43]\n" \
"v_mfma_f32_16x16x32_bf16 v[72:75], a[208:211], a[184:187], v[72:75]\n" \
"ds_read_b64_tr_b16 a[204:205], v241 offset:25600\n" \
"v_mfma_f32_16x16x32_bf16 v[72:75], a[212:215], a[232:235], v[72:75]\n" \
"v_mfma_f32_16x16x32_bf16 v[104:107], a[212:215], a[236:239], v[104:107]\n" \
"s_add_u32 m0, s70, 74752\n" \
"ds_read_b64_tr_b16 a[206:207], v241 offset:27648\n" \
"v_mfma_f32_16x16x32_bf16 v[104:107], a[208:211], a[188:191], v[104:107]\n" \
"s_waitcnt lgkmcnt(8)\n" \
"s_add_u32 s74, s69, 2359424\n" \
"v_mfma_f32_16x16x32_bf16 v[12:15], a[216:219], a[176:179], v[12:15]\n" \
"buffer_load_dwordx4 v246, s[28:31], s74 offen lds\n" \
"v_mfma_f32_16x16x32_bf16 v[12:15], a[220:223], a[224:227], v[12:15]\n" \
"buffer_load_dwordx4 v[204:207], v248, s[32:35], s37 offen offset:3072 sc1\n" \
"v_mfma_f32_16x16x32_bf16 v[44:47], a[220:223], a[228:231], v[44:47]\n" \
"s_add_u32 s79, s79, 4096\n" \
"buffer_load_dwordx4 a[160:163], v248, s[32:35], s79 offen offset:0 sc1\n" \
"v_mfma_f32_16x16x32_bf16 v[44:47], a[216:219], a[180:183], v[44:47]\n" \
"ds_read_b64_tr_b16 a[208:209], v240 offset:17920\n" \
"ds_read_b64_tr_b16 a[210:211], v240 offset:19968\n" \
"v_mfma_f32_16x16x32_bf16 v[76:79], a[216:219], a[184:187], v[76:79]\n" \
"ds_read_b64_tr_b16 a[212:213], v240 offset:26112\n" \
"v_mfma_f32_16x16x32_bf16 v[76:79], a[220:223], a[232:235], v[76:79]\n" \
"v_mfma_f32_16x16x32_bf16 v[108:111], a[220:223], a[236:239], v[108:111]\n" \
"ds_read_b64_tr_b16 a[214:215], v240 offset:28160\n" \
"v_mfma_f32_16x16x32_bf16 v[108:111], a[216:219], a[188:191], v[108:111]\n" \
"s_waitcnt lgkmcnt(8)\n" \
"s_add_u32 s85, s94, 0\n" \
"v_mfma_f32_16x16x32_bf16 v[16:19], a[192:195], a[176:179], v[16:19]\n" \
"buffer_store_dwordx4 v[168:171], v248, s[32:35], s85 offen offset:2048 sc1\n" \
"v_mfma_f32_16x16x32_bf16 v[16:19], a[196:199], a[224:227], v[16:19]\n" \
"s_add_u32 s80, s80, 4096\n" \
"ds_read_b64_tr_b16 a[216:217], v241 offset:17920\n" \
"v_accvgpr_read_b32 v144, a16\n" \
"v_mfma_f32_16x16x32_bf16 v[48:51], a[196:199], a[228:231], v[48:51]\n" \
"buffer_load_dwordx4 a[164:167], v248, s[32:35], s80 offen offset:0 sc1\n" \
"v_accvgpr_read_b32 v145, a17\n" \
"v_mfma_f32_16x16x32_bf16 v[48:51], a[192:195], a[180:183], v[48:51]\n" \
"ds_read_b64_tr_b16 a[218:219], v241 offset:19968\n" \
"ds_read_b64_tr_b16 a[220:221], v241 offset:26112\n" \
"v_mfma_f32_16x16x32_bf16 v[80:83], a[192:195], a[184:187], v[80:83]\n" \
"ds_read_b64_tr_b16 a[222:223], v241 offset:28160\n" \
"v_xor_b32 v240, 0x30000, v240\n" \
"v_mfma_f32_16x16x32_bf16 v[80:83], a[196:199], a[232:235], v[80:83]\n" \
"v_xor_b32 v241, 0x30000, v241\n" \
"v_accvgpr_read_b32 v146, a18\n" \
"v_mfma_f32_16x16x32_bf16 v[112:115], a[196:199], a[236:239], v[112:115]\n" \
"v_accvgpr_read_b32 v147, a19\n" \
"v_accvgpr_read_b32 v148, a20\n" \
"v_mfma_f32_16x16x32_bf16 v[112:115], a[192:195], a[188:191], v[112:115]\n" \
"s_waitcnt lgkmcnt(8)\n" \
"v_accvgpr_read_b32 v149, a21\n" \
"v_accvgpr_read_b32 v150, a22\n" \
"v_mfma_f32_16x16x32_bf16 v[20:23], a[200:203], a[176:179], v[20:23]\n" \
"v_accvgpr_read_b32 v151, a23\n" \
"v_max3_f32 v250, |v144|, |v145|, v250\n" \
"v_mfma_f32_16x16x32_bf16 v[20:23], a[204:207], a[224:227], v[20:23]\n" \
"s_add_u32 s36, s36, 4096\n" \
"v_max3_f32 v250, |v146|, |v147|, v250\n" \
"v_max3_f32 v250, |v148|, |v149|, v250\n" \
"v_mfma_f32_16x16x32_bf16 v[52:55], a[204:207], a[228:231], v[52:55]\n" \
"buffer_load_dwordx4 a[168:171], v248, s[32:35], s36 offen offset:0 sc1\n" \
"v_max3_f32 v250, |v150|, |v151|, v250\n" \
"v_mfma_f32_16x16x32_bf16 v[52:55], a[200:203], a[180:183], v[52:55]\n" \
"v_cvt_pk_bf16_f32 v144, v144, v145\n" \
"v_cvt_pk_bf16_f32 v145, v146, v147\n" \
"v_mfma_f32_16x16x32_bf16 v[84:87], a[200:203], a[184:187], v[84:87]\n" \
"s_waitcnt lgkmcnt(0)\n" \
"s_barrier\n" \
"v_cvt_pk_bf16_f32 v146, v148, v149\n" \
"v_mfma_f32_16x16x32_bf16 v[84:87], a[204:207], a[232:235], v[84:87]\n" \
"s_add_u32 m0, s70, 81920\n" \
"ds_read_b64_tr_b16 a[192:193], v240 offset:32768\n" \
"ds_read_b64_tr_b16 a[194:195], v240 offset:34816\n" \
"v_mfma_f32_16x16x32_bf16 v[116:119], a[204:207], a[236:239], v[116:119]\n" \
"s_add_u32 s74, s69, 2621440\n" \
"buffer_load_dwordx4 v246, s[28:31], s74 offen lds\n" \
"v_mfma_f32_16x16x32_bf16 v[116:119], a[200:203], a[188:191], v[116:119]\n" \
"ds_read_b64_tr_b16 a[196:197], v240 offset:40960\n" \
"ds_read_b64_tr_b16 a[198:199], v240 offset:43008\n" \
"v_mfma_f32_16x16x32_bf16 v[24:27], a[208:211], a[176:179], v[24:27]\n" \
"ds_read_b64_tr_b16 a[200:201], v241 offset:32768\n" \
"v_cvt_pk_bf16_f32 v147, v150, v151\n" \
"v_mfma_f32_16x16x32_bf16 v[24:27], a[212:215], a[224:227], v[24:27]\n" \
"s_add_u32 s37, s37, 4096\n" \
"ds_write_b128 v249, v[144:147] offset:2048\n" \
"v_accvgpr_read_b32 v152, a80\n" \
"v_mfma_f32_16x16x32_bf16 v[56:59], a[212:215], a[228:231], v[56:59]\n" \
"buffer_load_dwordx4 a[172:175], v248, s[32:35], s37 offen offset:0 sc1\n" \
"v_accvgpr_read_b32 v153, a81\n" \
"v_mfma_f32_16x16x32_bf16 v[56:59], a[208:211], a[180:183], v[56:59]\n" \
"s_add_u32 s85, s71, 589824\n" \
"ds_read_b64_tr_b16 a[202:203], v241 offset:34816\n" \
"v_accvgpr_read_b32 v154, a82\n" \
"v_mfma_f32_16x16x32_bf16 v[88:91], a[208:211], a[184:187], v[88:91]\n" \
"ds_read_b64_tr_b16 a[204:205], v241 offset:40960\n" \
"v_accvgpr_read_b32 v155, a83\n" \
"v_mfma_f32_16x16x32_bf16 v[88:91], a[212:215], a[232:235], v[88:91]\n" \
"buffer_store_dwordx4 v[144:147], v248, s[32:35], s85 offen offset:2048 sc1\n" \
"v_mfma_f32_16x16x32_bf16 v[120:123], a[212:215], a[236:239], v[120:123]\n" \
"ds_read_b64_tr_b16 a[206:207], v241 offset:43008\n" \
"v_accvgpr_read_b32 v156, a84\n" \
"v_mfma_f32_16x16x32_bf16 v[120:123], a[208:211], a[188:191], v[120:123]\n" \
"v_accvgpr_read_b32 v157, a85\n" \
"v_accvgpr_read_b32 v158, a86\n" \
"v_mfma_f32_16x16x32_bf16 v[28:31], a[216:219], a[176:179], v[28:31]\n" \
"ds_read_b64_tr_b16 a[208:209], v240 offset:33280\n" \
"v_accvgpr_read_b32 v159, a87\n" \
"v_mfma_f32_16x16x32_bf16 v[28:31], a[220:223], a[224:227], v[28:31]\n" \
"v_max3_f32 v250, |v152|, |v153|, v250\n" \
"v_max3_f32 v250, |v154|, |v155|, v250\n" \
"v_mfma_f32_16x16x32_bf16 v[60:63], a[220:223], a[228:231], v[60:63]\n" \
"s_add_u32 m0, s70, 82944\n" \
"ds_read_b64_tr_b16 a[210:211], v240 offset:35328\n" \
"v_max3_f32 v250, |v156|, |v157|, v250\n" \
"v_mfma_f32_16x16x32_bf16 v[60:63], a[216:219], a[180:183], v[60:63]\n" \
"s_add_u32 s74, s69, 2621568\n" \
"v_max3_f32 v250, |v158|, |v159|, v250\n" \
"v_cvt_pk_bf16_f32 v152, v152, v153\n" \
"v_mfma_f32_16x16x32_bf16 v[92:95], a[216:219], a[184:187], v[92:95]\n" \
"buffer_load_dwordx4 v246, s[28:31], s74 offen lds\n" \
"v_mfma_f32_16x16x32_bf16 v[92:95], a[220:223], a[232:235], v[92:95]\n" \
"ds_read_b64_tr_b16 a[212:213], v240 offset:41472\n" \
"v_cvt_pk_bf16_f32 v153, v154, v155\n" \
"v_mfma_f32_16x16x32_bf16 v[124:127], a[220:223], a[236:239], v[124:127]\n" \
"s_waitcnt vmcnt(23)\n" \
"ds_read_b64_tr_b16 a[214:215], v240 offset:43520\n" \
"v_mfma_f32_16x16x32_bf16 v[124:127], a[216:219], a[188:191], v[124:127]\n" \
"s_waitcnt lgkmcnt(9)\n" \
"v_cvt_pk_bf16_f32 v154, v156, v157\n" \
"v_cvt_pk_bf16_f32 v155, v158, v159\n" \
"v_mfma_f32_16x16x32_bf16 v[0:3], a[192:195], a[240:243], v[0:3]\n" \
"ds_read_b64_tr_b16 a[216:217], v241 offset:33280\n" \
"v_accvgpr_write_b32 a80, v152\n" \
"v_mfma_f32_16x16x32_bf16 v[0:3], a[196:199], a[128:131], v[0:3]\n" \
"buffer_load_dwordx4 a[176:179], v248, s[32:35], s79 offen offset:1024 sc1\n" \
"v_accvgpr_write_b32 a81, v153\n" \
"v_mfma_f32_16x16x32_bf16 v[32:35], a[196:199], a[132:135], v[32:35]\n" \
"ds_read_b64_tr_b16 a[218:219], v241 offset:35328\n" \
"v_accvgpr_write_b32 a82, v154\n" \
"v_mfma_f32_16x16x32_bf16 v[32:35], a[192:195], a[244:247], v[32:35]\n" \
"v_accvgpr_write_b32 a83, v155\n" \
"v_mfma_f32_16x16x32_bf16 v[64:67], a[192:195], a[248:251], v[64:67]\n" \
"ds_read_b64_tr_b16 a[220:221], v241 offset:41472\n" \
"v_mfma_f32_16x16x32_bf16 v[64:67], a[196:199], a[136:139], v[64:67]\n" \
"v_mfma_f32_16x16x32_bf16 v[96:99], a[196:199], a[140:143], v[96:99]\n" \
"ds_read_b64_tr_b16 a[222:223], v241 offset:43520\n" \
"v_mfma_f32_16x16x32_bf16 v[96:99], a[192:195], a[252:255], v[96:99]\n" \
"s_waitcnt lgkmcnt(8)\n" \
"v_mfma_f32_16x16x32_bf16 v[4:7], a[200:203], a[240:243], v[4:7]\n" \
"ds_read_b64_tr_b16 a[192:193], v240 offset:33792\n" \
"v_mfma_f32_16x16x32_bf16 v[4:7], a[204:207], a[128:131], v[4:7]\n" \
"buffer_load_dwordx4 a[180:183], v248, s[32:35], s80 offen offset:1024 sc1\n" \
"v_mfma_f32_16x16x32_bf16 v[36:39], a[204:207], a[132:135], v[36:39]\n" \
"s_add_u32 m0, s70, 90112\n" \
"s_add_u32 s74, s69, 2883584\n" \
"ds_read_b64_tr_b16 a[194:195], v240 offset:35840\n" \
"v_mfma_f32_16x16x32_bf16 v[36:39], a[200:203], a[244:247], v[36:39]\n" \
"v_mfma_f32_16x16x32_bf16 v[68:71], a[200:203], a[248:251], v[68:71]\n" \
"buffer_load_dwordx4 v246, s[28:31], s74 offen lds\n" \
"v_mfma_f32_16x16x32_bf16 v[68:71], a[204:207], a[136:139], v[68:71]\n" \
"ds_read_b64_tr_b16 a[196:197], v240 offset:41984\n" \
"v_mfma_f32_16x16x32_bf16 v[100:103], a[204:207], a[140:143], v[100:103]\n" \
"ds_read_b64_tr_b16 a[198:199], v240 offset:44032\n" \
"v_mfma_f32_16x16x32_bf16 v[100:103], a[200:203], a[252:255], v[100:103]\n" \
"s_waitcnt lgkmcnt(8)\n" \
"v_mfma_f32_16x16x32_bf16 v[8:11], a[208:211], a[240:243], v[8:11]\n" \
"ds_read_b64_tr_b16 a[200:201], v241 offset:33792\n" \
"v_mfma_f32_16x16x32_bf16 v[8:11], a[212:215], a[128:131], v[8:11]\n" \
"buffer_load_dwordx4 a[184:187], v248, s[32:35], s36 offen offset:1024 sc1\n" \
"v_mfma_f32_16x16x32_bf16 v[40:43], a[212:215], a[132:135], v[40:43]\n" \
"ds_read_b64_tr_b16 a[202:203], v241 offset:35840\n" \
"v_accvgpr_read_b32 v144, a24\n" \
"v_mfma_f32_16x16x32_bf16 v[40:43], a[208:211], a[244:247], v[40:43]\n" \
"v_accvgpr_read_b32 v145, a25\n" \
"v_accvgpr_read_b32 v146, a26\n" \
"v_mfma_f32_16x16x32_bf16 v[72:75], a[208:211], a[248:251], v[72:75]\n" \
"ds_read_b64_tr_b16 a[204:205], v241 offset:41984\n" \
"v_accvgpr_read_b32 v147, a27\n" \
"v_mfma_f32_16x16x32_bf16 v[72:75], a[212:215], a[136:139], v[72:75]\n" \
"v_accvgpr_read_b32 v148, a28\n" \
"v_accvgpr_read_b32 v149, a29\n" \
"v_mfma_f32_16x16x32_bf16 v[104:107], a[212:215], a[140:143], v[104:107]\n" \
"s_add_u32 m0, s70, 91136\n" \
"ds_read_b64_tr_b16 a[206:207], v241 offset:44032\n" \
"v_accvgpr_read_b32 v150, a30\n" \
"v_mfma_f32_16x16x32_bf16 v[104:107], a[208:211], a[252:255], v[104:107]\n" \
"s_waitcnt lgkmcnt(8)\n" \
"v_accvgpr_read_b32 v151, a31\n" \
"v_max3_f32 v250, |v144|, |v145|, v250\n" \
"v_mfma_f32_16x16x32_bf16 v[12:15], a[216:219], a[240:243], v[12:15]\n" \
"s_add_u32 s74, s69, 2883712\n" \
"buffer_load_dwordx4 v246, s[28:31], s74 offen lds\n" \
"v_mfma_f32_16x16x32_bf16 v[12:15], a[220:223], a[128:131], v[12:15]\n" \
"buffer_load_dwordx4 a[188:191], v248, s[32:35], s37 offen offset:1024 sc1\n" \
"v_max3_f32 v250, |v146|, |v147|, v250\n" \
"v_mfma_f32_16x16x32_bf16 v[44:47], a[220:223], a[132:135], v[44:47]\n" \
"buffer_load_dwordx4 a[0:3], v248, s[32:35], s79 offen offset:2048 sc1\n" \
"v_max3_f32 v250, |v148|, |v149|, v250\n" \
"v_mfma_f32_16x16x32_bf16 v[44:47], a[216:219], a[244:247], v[44:47]\n" \
"s_add_u32 s85, s94, 0\n" \
"ds_read_b64_tr_b16 a[208:209], v240 offset:34304\n" \
"ds_read_b64_tr_b16 a[210:211], v240 offset:36352\n" \
"v_mfma_f32_16x16x32_bf16 v[76:79], a[216:219], a[248:251], v[76:79]\n" \
"ds_read_b64_tr_b16 a[212:213], v240 offset:42496\n" \
"v_max3_f32 v250, |v150|, |v151|, v250\n" \
"v_mfma_f32_16x16x32_bf16 v[76:79], a[220:223], a[136:139], v[76:79]\n" \
"buffer_store_dwordx4 v[172:175], v248, s[32:35], s85 offen offset:3072 sc1\n" \
"v_mfma_f32_16x16x32_bf16 v[108:111], a[220:223], a[140:143], v[108:111]\n" \
"ds_read_b64_tr_b16 a[214:215], v240 offset:44544\n" \
"v_cvt_pk_bf16_f32 v144, v144, v145\n" \
"v_mfma_f32_16x16x32_bf16 v[108:111], a[216:219], a[252:255], v[108:111]\n" \
"s_waitcnt lgkmcnt(8)\n" \
"v_cvt_pk_bf16_f32 v145, v146, v147\n" \
"v_cvt_pk_bf16_f32 v146, v148, v149\n" \
"v_mfma_f32_16x16x32_bf16 v[16:19], a[192:195], a[240:243], v[16:19]\n" \
"ds_read_b64_tr_b16 a[216:217], v241 offset:34304\n" \
"v_cvt_pk_bf16_f32 v147, v150, v151\n" \
"v_mfma_f32_16x16x32_bf16 v[16:19], a[196:199], a[128:131], v[16:19]\n" \
"ds_write_b128 v249, v[144:147] offset:3072\n" \
"v_accvgpr_read_b32 v152, a88\n" \
"v_mfma_f32_16x16x32_bf16 v[48:51], a[196:199], a[132:135], v[48:51]\n" \
"buffer_load_dwordx4 a[4:7], v248, s[32:35], s80 offen offset:2048 sc1\n" \
"v_accvgpr_read_b32 v153, a89\n" \
"v_mfma_f32_16x16x32_bf16 v[48:51], a[192:195], a[244:247], v[48:51]\n" \
"ds_read_b64_tr_b16 a[218:219], v241 offset:36352\n" \
"ds_read_b64_tr_b16 a[220:221], v241 offset:42496\n" \
"v_mfma_f32_16x16x32_bf16 v[80:83], a[192:195], a[248:251], v[80:83]\n" \
"ds_read_b64_tr_b16 a[222:223], v241 offset:44544\n" \
"v_accvgpr_read_b32 v154, a90\n" \
"v_mfma_f32_16x16x32_bf16 v[80:83], a[196:199], a[136:139], v[80:83]\n" \
"s_add_u32 s85, s71, 589824\n" \
"v_accvgpr_read_b32 v155, a91\n" \
"v_accvgpr_read_b32 v156, a92\n" \
"v_mfma_f32_16x16x32_bf16 v[112:115], a[196:199], a[140:143], v[112:115]\n" \
"buffer_store_dwordx4 v[144:147], v248, s[32:35], s85 offen offset:3072 sc1\n" \
"v_mfma_f32_16x16x32_bf16 v[112:115], a[192:195], a[252:255], v[112:115]\n" \
"s_waitcnt lgkmcnt(9)\n" \
"v_accvgpr_read_b32 v157, a93\n" \
"v_accvgpr_read_b32 v158, a94\n" \
"v_mfma_f32_16x16x32_bf16 v[20:23], a[200:203], a[240:243], v[20:23]\n" \
"v_accvgpr_read_b32 v159, a95\n" \
"v_max3_f32 v250, |v152|, |v153|, v250\n" \
"v_mfma_f32_16x16x32_bf16 v[20:23], a[204:207], a[128:131], v[20:23]\n" \
"v_max3_f32 v250, |v154|, |v155|, v250\n" \
"v_max3_f32 v250, |v156|, |v157|, v250\n" \
"v_mfma_f32_16x16x32_bf16 v[52:55], a[204:207], a[132:135], v[52:55]\n" \
"buffer_load_dwordx4 a[8:11], v248, s[32:35], s36 offen offset:2048 sc1\n" \
"v_max3_f32 v250, |v158|, |v159|, v250\n" \
"v_mfma_f32_16x16x32_bf16 v[52:55], a[200:203], a[244:247], v[52:55]\n" \
"v_cvt_pk_bf16_f32 v152, v152, v153\n" \
"v_cvt_pk_bf16_f32 v153, v154, v155\n" \
"v_mfma_f32_16x16x32_bf16 v[84:87], a[200:203], a[248:251], v[84:87]\n" \
"s_waitcnt lgkmcnt(0)\n" \
"s_barrier\n" \
"v_cvt_pk_bf16_f32 v154, v156, v157\n" \
"v_mfma_f32_16x16x32_bf16 v[84:87], a[204:207], a[136:139], v[84:87]\n" \
"s_add_u32 m0, s70, 32768\n" \
"ds_read_b64_tr_b16 a[192:193], v240 offset:49152\n" \
"ds_read_b64_tr_b16 a[194:195], v240 offset:51200\n" \
"v_mfma_f32_16x16x32_bf16 v[116:119], a[204:207], a[140:143], v[116:119]\n" \
"s_add_u32 s74, s69, 3145728\n" \
"buffer_load_dwordx4 v246, s[28:31], s74 offen lds\n" \
"v_mfma_f32_16x16x32_bf16 v[116:119], a[200:203], a[252:255], v[116:119]\n" \
"ds_read_b64_tr_b16 a[196:197], v240 offset:57344\n" \
"ds_read_b64_tr_b16 a[198:199], v240 offset:59392\n" \
"v_mfma_f32_16x16x32_bf16 v[24:27], a[208:211], a[240:243], v[24:27]\n" \
"ds_read_b64_tr_b16 a[200:201], v241 offset:49152\n" \
"v_cvt_pk_bf16_f32 v155, v158, v159\n" \
"v_mfma_f32_16x16x32_bf16 v[24:27], a[212:215], a[128:131], v[24:27]\n" \
"v_accvgpr_write_b32 a88, v152\n" \
"v_accvgpr_write_b32 a89, v153\n" \
"v_mfma_f32_16x16x32_bf16 v[56:59], a[212:215], a[132:135], v[56:59]\n" \
"buffer_load_dwordx4 a[12:15], v248, s[32:35], s37 offen offset:2048 sc1\n" \
"v_accvgpr_write_b32 a90, v154\n" \
"v_mfma_f32_16x16x32_bf16 v[56:59], a[208:211], a[244:247], v[56:59]\n" \
"ds_read_b64_tr_b16 a[202:203], v241 offset:51200\n" \
"v_accvgpr_write_b32 a91, v155\n" \
"v_mfma_f32_16x16x32_bf16 v[88:91], a[208:211], a[248:251], v[88:91]\n" \
"ds_read_b64_tr_b16 a[204:205], v241 offset:57344\n" \
"v_mfma_f32_16x16x32_bf16 v[88:91], a[212:215], a[136:139], v[88:91]\n" \
"v_mfma_f32_16x16x32_bf16 v[120:123], a[212:215], a[140:143], v[120:123]\n" \
"ds_read_b64_tr_b16 a[206:207], v241 offset:59392\n" \
"v_mfma_f32_16x16x32_bf16 v[120:123], a[208:211], a[252:255], v[120:123]\n" \
"v_mfma_f32_16x16x32_bf16 v[28:31], a[216:219], a[240:243], v[28:31]\n" \
"ds_read_b64_tr_b16 a[208:209], v240 offset:49664\n" \
"v_mfma_f32_16x16x32_bf16 v[28:31], a[220:223], a[128:131], v[28:31]\n" \
"v_mfma_f32_16x16x32_bf16 v[60:63], a[220:223], a[132:135], v[60:63]\n" \
"buffer_load_dwordx4 a[16:19], v248, s[32:35], s79 offen offset:3072 sc1\n" \
"v_mfma_f32_16x16x32_bf16 v[60:63], a[216:219], a[244:247], v[60:63]\n" \
"s_add_u32 m0, s70, 33792\n" \
"s_add_u32 s74, s69, 3145856\n" \
"ds_read_b64_tr_b16 a[210:211], v240 offset:51712\n" \
"v_mfma_f32_16x16x32_bf16 v[92:95], a[216:219], a[248:251], v[92:95]\n" \
"ds_read_b64_tr_b16 a[212:213], v240 offset:57856\n" \
"v_mfma_f32_16x16x32_bf16 v[92:95], a[220:223], a[136:139], v[92:95]\n" \
"buffer_load_dwordx4 v246, s[28:31], s74 offen lds\n" \
"v_mfma_f32_16x16x32_bf16 v[124:127], a[220:223], a[140:143], v[124:127]\n" \
"s_waitcnt vmcnt(23)\n" \
"ds_read_b64_tr_b16 a[214:215], v240 offset:59904\n" \
"v_mfma_f32_16x16x32_bf16 v[124:127], a[216:219], a[252:255], v[124:127]\n" \
"s_waitcnt lgkmcnt(8)\n" \
"v_mfma_f32_16x16x32_bf16 v[0:3], a[192:195], a[144:147], v[0:3]\n" \
"ds_read_b64_tr_b16 a[216:217], v241 offset:49664\n" \
"v_mfma_f32_16x16x32_bf16 v[0:3], a[196:199], v[192:195], v[0:3]\n" \
"v_mfma_f32_16x16x32_bf16 v[32:35], a[196:199], v[196:199], v[32:35]\n" \
"buffer_load_dwordx4 a[20:23], v248, s[32:35], s80 offen offset:3072 sc1\n" \
"v_mfma_f32_16x16x32_bf16 v[32:35], a[192:195], a[148:151], v[32:35]\n" \
"ds_read_b64_tr_b16 a[218:219], v241 offset:51712\n" \
"v_mfma_f32_16x16x32_bf16 v[64:67], a[192:195], a[152:155], v[64:67]\n" \
"ds_read_b64_tr_b16 a[220:221], v241 offset:57856\n" \
"v_mfma_f32_16x16x32_bf16 v[64:67], a[196:199], v[200:203], v[64:67]\n" \
"v_mfma_f32_16x16x32_bf16 v[96:99], a[196:199], v[204:207], v[96:99]\n" \
"ds_read_b64_tr_b16 a[222:223], v241 offset:59904\n" \
"v_mfma_f32_16x16x32_bf16 v[96:99], a[192:195], a[156:159], v[96:99]\n" \
"s_waitcnt lgkmcnt(8)\n" \
"v_mfma_f32_16x16x32_bf16 v[4:7], a[200:203], a[144:147], v[4:7]\n" \
"s_add_u32 m0, s70, 40960\n" \
"ds_read_b64_tr_b16 a[192:193], v240 offset:50176\n" \
"v_mfma_f32_16x16x32_bf16 v[4:7], a[204:207], v[192:195], v[4:7]\n" \
"s_add_u32 s74, s69, 3407872\n" \
"v_mfma_f32_16x16x32_bf16 v[36:39], a[204:207], v[196:199], v[36:39]\n" \
"buffer_load_dwordx4 v246, s[28:31], s74 offen lds\n" \
"v_mfma_f32_16x16x32_bf16 v[36:39], a[200:203], a[148:151], v[36:39]\n" \
"buffer_load_dwordx4 a[24:27], v248, s[32:35], s36 offen offset:3072 sc1\n" \
"v_mfma_f32_16x16x32_bf16 v[68:71], a[200:203], a[152:155], v[68:71]\n" \
"ds_read_b64_tr_b16 a[194:195], v240 offset:52224\n" \
"ds_read_b64_tr_b16 a[196:197], v240 offset:58368\n" \
"v_mfma_f32_16x16x32_bf16 v[68:71], a[204:207], v[200:203], v[68:71]\n" \
"v_mfma_f32_16x16x32_bf16 v[100:103], a[204:207], v[204:207], v[100:103]\n" \
"ds_read_b64_tr_b16 a[198:199], v240 offset:60416\n" \
"v_mfma_f32_16x16x32_bf16 v[100:103], a[200:203], a[156:159], v[100:103]\n" \
"s_waitcnt lgkmcnt(8)\n" \
"v_mfma_f32_16x16x32_bf16 v[8:11], a[208:211], a[144:147], v[8:11]\n" \
"ds_read_b64_tr_b16 a[200:201], v241 offset:50176\n" \
"v_mfma_f32_16x16x32_bf16 v[8:11], a[212:215], v[192:195], v[8:11]\n" \
"v_mfma_f32_16x16x32_bf16 v[40:43], a[212:215], v[196:199], v[40:43]\n" \
"buffer_load_dwordx4 a[28:31], v248, s[32:35], s37 offen offset:3072 sc1\n" \
"v_mfma_f32_16x16x32_bf16 v[40:43], a[208:211], a[148:151], v[40:43]\n" \
"ds_read_b64_tr_b16 a[202:203], v241 offset:52224\n" \
"v_mfma_f32_16x16x32_bf16 v[72:75], a[208:211], a[152:155], v[72:75]\n" \
"ds_read_b64_tr_b16 a[204:205], v241 offset:58368\n" \
"v_mfma_f32_16x16x32_bf16 v[72:75], a[212:215], v[200:203], v[72:75]\n" \
"v_mfma_f32_16x16x32_bf16 v[104:107], a[212:215], v[204:207], v[104:107]\n" \
"s_add_u32 m0, s70, 41984\n" \
"ds_read_b64_tr_b16 a[206:207], v241 offset:60416\n" \
"v_mfma_f32_16x16x32_bf16 v[104:107], a[208:211], a[156:159], v[104:107]\n" \
"s_waitcnt lgkmcnt(8)\n" \
"s_add_u32 s74, s69, 3408000\n" \
"s_add_u32 s85, s94, 4096\n" \
"v_mfma_f32_16x16x32_bf16 v[12:15], a[216:219], a[144:147], v[12:15]\n" \
"buffer_load_dwordx4 v246, s[28:31], s74 offen lds\n" \
"v_mfma_f32_16x16x32_bf16 v[12:15], a[220:223], v[192:195], v[12:15]\n" \
"s_add_u32 s79, s79, 4096\n" \
"ds_read_b64_tr_b16 a[208:209], v240 offset:50688\n" \
"v_mfma_f32_16x16x32_bf16 v[44:47], a[220:223], v[196:199], v[44:47]\n" \
"buffer_load_dwordx4 a[128:131], v248, s[32:35], s79 offen offset:0 sc1\n" \
"v_mfma_f32_16x16x32_bf16 v[44:47], a[216:219], a[148:151], v[44:47]\n" \
"ds_read_b64_tr_b16 a[210:211], v240 offset:52736\n" \
"v_mfma_f32_16x16x32_bf16 v[76:79], a[216:219], a[152:155], v[76:79]\n" \
"ds_read_b64_tr_b16 a[212:213], v240 offset:58880\n" \
"v_mfma_f32_16x16x32_bf16 v[76:79], a[220:223], v[200:203], v[76:79]\n" \
"buffer_store_dwordx4 v[176:179], v248, s[32:35], s85 offen offset:0 sc1\n" \
"v_mfma_f32_16x16x32_bf16 v[108:111], a[220:223], v[204:207], v[108:111]\n" \
"ds_read_b64_tr_b16 a[214:215], v240 offset:60928\n" \
"v_mfma_f32_16x16x32_bf16 v[108:111], a[216:219], a[156:159], v[108:111]\n" \
"s_waitcnt lgkmcnt(8)\n" \
"v_mfma_f32_16x16x32_bf16 v[16:19], a[192:195], a[144:147], v[16:19]\n" \
"ds_read_b64_tr_b16 a[216:217], v241 offset:50688\n" \
"v_mfma_f32_16x16x32_bf16 v[16:19], a[196:199], v[192:195], v[16:19]\n" \
"s_add_u32 s80, s80, 4096\n" \
"v_mfma_f32_16x16x32_bf16 v[48:51], a[196:199], v[196:199], v[48:51]\n" \
"buffer_load_dwordx4 a[132:135], v248, s[32:35], s80 offen offset:0 sc1\n" \
"v_accvgpr_read_b32 v144, a32\n" \
"v_mfma_f32_16x16x32_bf16 v[48:51], a[192:195], a[148:151], v[48:51]\n" \
"ds_read_b64_tr_b16 a[218:219], v241 offset:52736\n" \
"ds_read_b64_tr_b16 a[220:221], v241 offset:58880\n" \
"v_mfma_f32_16x16x32_bf16 v[80:83], a[192:195], a[152:155], v[80:83]\n" \
"ds_read_b64_tr_b16 a[222:223], v241 offset:60928\n" \
"v_xor_b32 v240, 0x30000, v240\n" \
"v_mfma_f32_16x16x32_bf16 v[80:83], a[196:199], v[200:203], v[80:83]\n" \
"v_xor_b32 v241, 0x30000, v241\n" \
"v_accvgpr_read_b32 v145, a33\n" \
"v_mfma_f32_16x16x32_bf16 v[112:115], a[196:199], v[204:207], v[112:115]\n" \
"v_accvgpr_read_b32 v146, a34\n" \
"v_accvgpr_read_b32 v147, a35\n" \
"v_mfma_f32_16x16x32_bf16 v[112:115], a[192:195], a[156:159], v[112:115]\n" \
"s_waitcnt lgkmcnt(8)\n" \
"v_accvgpr_read_b32 v148, a36\n" \
"v_accvgpr_read_b32 v149, a37\n" \
"v_mfma_f32_16x16x32_bf16 v[20:23], a[200:203], a[144:147], v[20:23]\n" \
"v_accvgpr_read_b32 v150, a38\n" \
"v_accvgpr_read_b32 v151, a39\n" \
"v_mfma_f32_16x16x32_bf16 v[20:23], a[204:207], v[192:195], v[20:23]\n" \
"s_add_u32 s36, s36, 4096\n" \
"v_max3_f32 v250, |v144|, |v145|, v250\n" \
"v_max3_f32 v250, |v146|, |v147|, v250\n" \
"v_mfma_f32_16x16x32_bf16 v[52:55], a[204:207], v[196:199], v[52:55]\n" \
"buffer_load_dwordx4 a[136:139], v248, s[32:35], s36 offen offset:0 sc1\n" \
"v_max3_f32 v250, |v148|, |v149|, v250\n" \
"v_mfma_f32_16x16x32_bf16 v[52:55], a[200:203], a[148:151], v[52:55]\n" \
"v_max3_f32 v250, |v150|, |v151|, v250\n" \
"v_cvt_pk_bf16_f32 v144, v144, v145\n" \
"v_mfma_f32_16x16x32_bf16 v[84:87], a[200:203], a[152:155], v[84:87]\n" \
"s_waitcnt lgkmcnt(0)\n" \
"s_barrier\n" \
"v_cvt_pk_bf16_f32 v145, v146, v147\n" \
"v_mfma_f32_16x16x32_bf16 v[84:87], a[204:207], v[200:203], v[84:87]\n" \
"s_add_u32 m0, s70, 49152\n" \
"ds_read_b64_tr_b16 a[192:193], v240 offset:0\n" \
"ds_read_b64_tr_b16 a[194:195], v240 offset:2048\n" \
"v_mfma_f32_16x16x32_bf16 v[116:119], a[204:207], v[204:207], v[116:119]\n" \
"s_add_u32 s74, s69, 3670016\n" \
"buffer_load_dwordx4 v246, s[28:31], s74 offen lds\n" \
"v_mfma_f32_16x16x32_bf16 v[116:119], a[200:203], a[156:159], v[116:119]\n" \
"ds_read_b64_tr_b16 a[196:197], v240 offset:8192\n" \
"ds_read_b64_tr_b16 a[198:199], v240 offset:10240\n" \
"v_mfma_f32_16x16x32_bf16 v[24:27], a[208:211], a[144:147], v[24:27]\n" \
"ds_read_b64_tr_b16 a[200:201], v241 offset:0\n" \
"v_cvt_pk_bf16_f32 v146, v148, v149\n" \
"v_mfma_f32_16x16x32_bf16 v[24:27], a[212:215], v[192:195], v[24:27]\n" \
"s_add_u32 s37, s37, 4096\n" \
"v_cvt_pk_bf16_f32 v147, v150, v151\n" \
"v_mfma_f32_16x16x32_bf16 v[56:59], a[212:215], v[196:199], v[56:59]\n" \
"buffer_load_dwordx4 a[140:143], v248, s[32:35], s37 offen offset:0 sc1\n" \
"v_mfma_f32_16x16x32_bf16 v[56:59], a[208:211], a[148:151], v[56:59]\n" \
"ds_read_b64_tr_b16 a[202:203], v241 offset:2048\n" \
"v_mfma_f32_16x16x32_bf16 v[88:91], a[208:211], a[152:155], v[88:91]\n" \
"ds_read_b64_tr_b16 a[204:205], v241 offset:8192\n" \
"v_mfma_f32_16x16x32_bf16 v[88:91], a[212:215], v[200:203], v[88:91]\n" \
"s_add_u32 s85, s71, 593920\n" \
"ds_write_b128 v249, v[144:147] offset:4096\n" \
"v_accvgpr_read_b32 v152, a96\n" \
"v_mfma_f32_16x16x32_bf16 v[120:123], a[212:215], v[204:207], v[120:123]\n" \
"buffer_store_dwordx4 v[144:147], v248, s[32:35], s85 offen offset:0 sc1\n" \
"v_mfma_f32_16x16x32_bf16 v[120:123], a[208:211], a[156:159], v[120:123]\n" \
"ds_read_b64_tr_b16 a[206:207], v241 offset:10240\n" \
"v_accvgpr_read_b32 v153, a97\n" \
"v_mfma_f32_16x16x32_bf16 v[28:31], a[216:219], a[144:147], v[28:31]\n" \
"ds_read_b64_tr_b16 a[208:209], v240 offset:512\n" \
"v_accvgpr_read_b32 v154, a98\n" \
"v_mfma_f32_16x16x32_bf16 v[28:31], a[220:223], v[192:195], v[28:31]\n" \
"v_accvgpr_read_b32 v155, a99\n" \
"v_accvgpr_read_b32 v156, a100\n" \
"v_mfma_f32_16x16x32_bf16 v[60:63], a[220:223], v[196:199], v[60:63]\n" \
"s_add_u32 m0, s70, 50176\n" \
"ds_read_b64_tr_b16 a[210:211], v240 offset:2560\n" \
"v_accvgpr_read_b32 v157, a101\n" \
"v_mfma_f32_16x16x32_bf16 v[60:63], a[216:219], a[148:151], v[60:63]\n" \
"s_add_u32 s74, s69, 3670144\n" \
"v_accvgpr_read_b32 v158, a102\n" \
"v_accvgpr_read_b32 v159, a103\n" \
"v_mfma_f32_16x16x32_bf16 v[92:95], a[216:219], a[152:155], v[92:95]\n" \
"buffer_load_dwordx4 v246, s[28:31], s74 offen lds\n" \
"v_mfma_f32_16x16x32_bf16 v[92:95], a[220:223], v[200:203], v[92:95]\n" \
"ds_read_b64_tr_b16 a[212:213], v240 offset:8704\n" \
"v_max3_f32 v250, |v152|, |v153|, v250\n" \
"v_mfma_f32_16x16x32_bf16 v[124:127], a[220:223], v[204:207], v[124:127]\n" \
"s_waitcnt vmcnt(22)\n" \
"ds_read_b64_tr_b16 a[214:215], v240 offset:10752\n" \
"v_mfma_f32_16x16x32_bf16 v[124:127], a[216:219], a[156:159], v[124:127]\n" \
"s_waitcnt lgkmcnt(9)\n" \
"v_max3_f32 v250, |v154|, |v155|, v250\n" \
"v_max3_f32 v250, |v156|, |v157|, v250\n" \
"v_mfma_f32_16x16x32_bf16 v[0:3], a[192:195], a[160:163], v[0:3]\n" \
"ds_read_b64_tr_b16 a[216:217], v241 offset:512\n" \
"v_max3_f32 v250, |v158|, |v159|, v250\n" \
"v_mfma_f32_16x16x32_bf16 v[0:3], a[196:199], a[176:179], v[0:3]\n" \
"buffer_load_dwordx4 a[144:147], v248, s[32:35], s79 offen offset:1024 sc1\n" \
"v_cvt_pk_bf16_f32 v152, v152, v153\n" \
"v_mfma_f32_16x16x32_bf16 v[32:35], a[196:199], a[180:183], v[32:35]\n" \
"ds_read_b64_tr_b16 a[218:219], v241 offset:2560\n" \
"v_cvt_pk_bf16_f32 v153, v154, v155\n" \
"v_mfma_f32_16x16x32_bf16 v[32:35], a[192:195], a[164:167], v[32:35]\n" \
"v_cvt_pk_bf16_f32 v154, v156, v157\n" \
"v_cvt_pk_bf16_f32 v155, v158, v159\n" \
"v_mfma_f32_16x16x32_bf16 v[64:67], a[192:195], a[168:171], v[64:67]\n" \
"ds_read_b64_tr_b16 a[220:221], v241 offset:8704\n" \
"v_accvgpr_write_b32 a96, v152\n" \
"v_mfma_f32_16x16x32_bf16 v[64:67], a[196:199], a[184:187], v[64:67]\n" \
"v_accvgpr_write_b32 a97, v153\n" \
"v_accvgpr_write_b32 a98, v154\n" \
"v_mfma_f32_16x16x32_bf16 v[96:99], a[196:199], a[188:191], v[96:99]\n" \
"ds_read_b64_tr_b16 a[222:223], v241 offset:10752\n" \
"v_accvgpr_write_b32 a99, v155\n" \
"v_mfma_f32_16x16x32_bf16 v[96:99], a[192:195], a[172:175], v[96:99]\n" \
"s_waitcnt lgkmcnt(8)\n" \
"v_mfma_f32_16x16x32_bf16 v[4:7], a[200:203], a[160:163], v[4:7]\n" \
"ds_read_b64_tr_b16 a[192:193], v240 offset:1024\n" \
"v_mfma_f32_16x16x32_bf16 v[4:7], a[204:207], a[176:179], v[4:7]\n" \
"buffer_load_dwordx4 a[148:151], v248, s[32:35], s80 offen offset:1024 sc1\n" \
"v_mfma_f32_16x16x32_bf16 v[36:39], a[204:207], a[180:183], v[36:39]\n" \
"s_add_u32 m0, s70, 57344\n" \
"s_add_u32 s74, s69, 3932160\n" \
"ds_read_b64_tr_b16 a[194:195], v240 offset:3072\n" \
"v_mfma_f32_16x16x32_bf16 v[36:39], a[200:203], a[164:167], v[36:39]\n" \
"v_mfma_f32_16x16x32_bf16 v[68:71], a[200:203], a[168:171], v[68:71]\n" \
"buffer_load_dwordx4 v246, s[28:31], s74 offen lds\n" \
"v_mfma_f32_16x16x32_bf16 v[68:71], a[204:207], a[184:187], v[68:71]\n" \
".Lst0_6_%=:\n" \
"s_cbranch_execz .Lst0_10_%=\n" \
"ds_read_b64_tr_b16 a[196:197], v240 offset:9216\n" \
"v_mfma_f32_16x16x32_bf16 v[100:103], a[204:207], a[188:191], v[100:103]\n" \
"ds_read_b64_tr_b16 a[198:199], v240 offset:11264\n" \
"v_mfma_f32_16x16x32_bf16 v[100:103], a[200:203], a[172:175], v[100:103]\n" \
"s_waitcnt lgkmcnt(8)\n" \
"v_mfma_f32_16x16x32_bf16 v[8:11], a[208:211], a[160:163], v[8:11]\n" \
"ds_read_b64_tr_b16 a[200:201], v241 offset:1024\n" \
"v_mfma_f32_16x16x32_bf16 v[8:11], a[212:215], a[176:179], v[8:11]\n" \
"buffer_load_dwordx4 a[152:155], v248, s[32:35], s36 offen offset:1024 sc1\n" \
"v_mfma_f32_16x16x32_bf16 v[40:43], a[212:215], a[180:183], v[40:43]\n" \
"ds_read_b64_tr_b16 a[202:203], v241 offset:3072\n" \
"v_mfma_f32_16x16x32_bf16 v[40:43], a[208:211], a[164:167], v[40:43]\n" \
"s_add_u32 s85, s94, 4096\n" \
"v_mfma_f32_16x16x32_bf16 v[72:75], a[208:211], a[168:171], v[72:75]\n" \
"s_and_b32 s91, %[wid], 1\n" \
"buffer_store_dwordx4 v[180:183], v248, s[32:35], s85 offen offset:1024 sc1\n" \
"v_mfma_f32_16x16x32_bf16 v[72:75], a[212:215], a[184:187], v[72:75]\n" \
"s_lshl_b32 s91, s91, 4\n" \
"ds_read_b64_tr_b16 a[204:205], v241 offset:9216\n" \
"v_and_b32 v254, 15, v251\n" \
"v_mfma_f32_16x16x32_bf16 v[104:107], a[212:215], a[188:191], v[104:107]\n" \
"s_add_u32 m0, s70, 58368\n" \
"ds_read_b64_tr_b16 a[206:207], v241 offset:11264\n" \
"v_lshrrev_b32 v255, 4, v251\n" \
"v_mfma_f32_16x16x32_bf16 v[104:107], a[208:211], a[172:175], v[104:107]\n" \
"s_waitcnt lgkmcnt(8)\n" \
"v_lshlrev_b32 v255, 2, v255\n" \
"v_sub_u32 v254, v254, v255\n" \
"v_mfma_f32_16x16x32_bf16 v[12:15], a[216:219], a[160:163], v[12:15]\n" \
"s_add_u32 s74, s69, 3932288\n" \
"buffer_load_dwordx4 v246, s[28:31], s74 offen lds\n" \
"v_mfma_f32_16x16x32_bf16 v[12:15], a[220:223], a[176:179], v[12:15]\n" \
"buffer_load_dwordx4 a[156:159], v248, s[32:35], s37 offen offset:1024 sc1\n" \
"v_add_u32 v254, s91, v254\n" \
"v_mfma_f32_16x16x32_bf16 v[44:47], a[220:223], a[180:183], v[44:47]\n" \
"buffer_load_dwordx4 a[224:227], v248, s[32:35], s79 offen offset:2048 sc1\n" \
"v_mov_b32 v255, 0xff000000\n" \
"v_mfma_f32_16x16x32_bf16 v[44:47], a[216:219], a[164:167], v[44:47]\n" \
"ds_read_b64_tr_b16 a[208:209], v240 offset:1536\n" \
"ds_read_b64_tr_b16 a[210:211], v240 offset:3584\n" \
"v_mfma_f32_16x16x32_bf16 v[76:79], a[216:219], a[168:171], v[76:79]\n" \
"ds_read_b64_tr_b16 a[212:213], v240 offset:9728\n" \
"v_cmp_gt_i32 s[96:97], 0, v254\n" \
"v_mfma_f32_16x16x32_bf16 v[76:79], a[220:223], a[184:187], v[76:79]\n" \
"v_cmp_gt_i32 s[98:99], 1, v254\n" \
"v_mfma_f32_16x16x32_bf16 v[108:111], a[220:223], a[188:191], v[108:111]\n" \
"ds_read_b64_tr_b16 a[214:215], v240 offset:11776\n" \
"v_cndmask_b32 v224, 0, v255, s[96:97]\n" \
"v_mfma_f32_16x16x32_bf16 v[108:111], a[216:219], a[172:175], v[108:111]\n" \
"s_waitcnt lgkmcnt(8)\n" \
"v_cndmask_b32 v225, 0, v255, s[98:99]\n" \
"v_cmp_gt_i32 s[96:97], 2, v254\n" \
"v_mfma_f32_16x16x32_bf16 v[16:19], a[192:195], a[160:163], v[16:19]\n" \
"ds_read_b64_tr_b16 a[216:217], v241 offset:1536\n" \
"v_cmp_gt_i32 s[98:99], 3, v254\n" \
"v_mfma_f32_16x16x32_bf16 v[16:19], a[196:199], a[176:179], v[16:19]\n" \
"v_cndmask_b32 v226, 0, v255, s[96:97]\n" \
"v_accvgpr_read_b32 v144, a40\n" \
"v_mfma_f32_16x16x32_bf16 v[48:51], a[196:199], a[180:183], v[48:51]\n" \
"buffer_load_dwordx4 a[228:231], v248, s[32:35], s80 offen offset:2048 sc1\n" \
"v_cndmask_b32 v227, 0, v255, s[98:99]\n" \
"v_mfma_f32_16x16x32_bf16 v[48:51], a[192:195], a[164:167], v[48:51]\n" \
"ds_read_b64_tr_b16 a[218:219], v241 offset:3584\n" \
"ds_read_b64_tr_b16 a[220:221], v241 offset:9728\n" \
"v_mfma_f32_16x16x32_bf16 v[80:83], a[192:195], a[168:171], v[80:83]\n" \
"ds_read_b64_tr_b16 a[222:223], v241 offset:11776\n" \
"v_cmp_gt_i32 s[96:97], 16, v254\n" \
"v_mfma_f32_16x16x32_bf16 v[80:83], a[196:199], a[184:187], v[80:83]\n" \
"v_cmp_gt_i32 s[98:99], 17, v254\n" \
"v_accvgpr_read_b32 v145, a41\n" \
"v_mfma_f32_16x16x32_bf16 v[112:115], a[196:199], a[188:191], v[112:115]\n" \
"v_cndmask_b32 v228, 0, v255, s[96:97]\n" \
"v_accvgpr_read_b32 v146, a42\n" \
"v_mfma_f32_16x16x32_bf16 v[112:115], a[192:195], a[172:175], v[112:115]\n" \
"s_waitcnt lgkmcnt(8)\n" \
"v_cndmask_b32 v229, 0, v255, s[98:99]\n" \
"v_cmp_gt_i32 s[96:97], 18, v254\n" \
"v_mfma_f32_16x16x32_bf16 v[20:23], a[200:203], a[160:163], v[20:23]\n" \
"v_cmp_gt_i32 s[98:99], 19, v254\n" \
"v_accvgpr_read_b32 v147, a43\n" \
"v_mfma_f32_16x16x32_bf16 v[20:23], a[204:207], a[176:179], v[20:23]\n" \
"v_cndmask_b32 v230, 0, v255, s[96:97]\n" \
"v_accvgpr_read_b32 v148, a44\n" \
"v_mfma_f32_16x16x32_bf16 v[52:55], a[204:207], a[180:183], v[52:55]\n" \
"buffer_load_dwordx4 a[232:235], v248, s[32:35], s36 offen offset:2048 sc1\n" \
"v_cndmask_b32 v231, 0, v255, s[98:99]\n" \
"v_mfma_f32_16x16x32_bf16 v[52:55], a[200:203], a[164:167], v[52:55]\n" \
"s_lshl_b32 s91, s91, 1\n" \
"v_accvgpr_read_b32 v149, a45\n" \
"v_accvgpr_read_b32 v150, a46\n" \
"v_mfma_f32_16x16x32_bf16 v[84:87], a[200:203], a[168:171], v[84:87]\n" \
"s_waitcnt lgkmcnt(0)\n" \
"s_barrier\n" \
"v_accvgpr_read_b32 v151, a47\n" \
"v_mfma_f32_16x16x32_bf16 v[84:87], a[204:207], a[184:187], v[84:87]\n" \
"s_sub_u32 s91, 16, s91\n" \
"ds_read_b64_tr_b16 a[192:193], v240 offset:16384\n" \
"ds_read_b64_tr_b16 a[194:195], v240 offset:18432\n" \
"v_mfma_f32_16x16x32_bf16 v[116:119], a[204:207], a[188:191], v[116:119]\n" \
"ds_read_b64_tr_b16 a[196:197], v240 offset:24576\n" \
"ds_read_b64_tr_b16 a[198:199], v240 offset:26624\n" \
"v_mfma_f32_16x16x32_bf16 v[116:119], a[200:203], a[172:175], v[116:119]\n" \
"v_add_u32 v254, s91, v254\n" \
"v_cmp_gt_i32 s[96:97], 0, v254\n" \
"v_mfma_f32_16x16x32_bf16 v[24:27], a[208:211], a[160:163], v[24:27]\n" \
"ds_read_b64_tr_b16 a[200:201], v241 offset:16384\n" \
"v_cmp_gt_i32 s[98:99], 1, v254\n" \
"v_mfma_f32_16x16x32_bf16 v[24:27], a[212:215], a[176:179], v[24:27]\n" \
"s_add_u32 s85, s71, 593920\n" \
"v_cndmask_b32 v232, 0, v255, s[96:97]\n" \
"v_max3_f32 v250, |v144|, |v145|, v250\n" \
"v_mfma_f32_16x16x32_bf16 v[56:59], a[212:215], a[180:183], v[56:59]\n" \
"buffer_load_dwordx4 a[236:239], v248, s[32:35], s37 offen offset:2048 sc1\n" \
"v_cndmask_b32 v233, 0, v255, s[98:99]\n" \
"v_mfma_f32_16x16x32_bf16 v[56:59], a[208:211], a[164:167], v[56:59]\n" \
"ds_read_b64_tr_b16 a[202:203], v241 offset:18432\n" \
"v_cmp_gt_i32 s[96:97], 2, v254\n" \
"v_mfma_f32_16x16x32_bf16 v[88:91], a[208:211], a[168:171], v[88:91]\n" \
"ds_read_b64_tr_b16 a[204:205], v241 offset:24576\n" \
"v_cmp_gt_i32 s[98:99], 3, v254\n" \
"v_mfma_f32_16x16x32_bf16 v[88:91], a[212:215], a[184:187], v[88:91]\n" \
"v_cndmask_b32 v234, 0, v255, s[96:97]\n" \
"v_max3_f32 v250, |v146|, |v147|, v250\n" \
"v_mfma_f32_16x16x32_bf16 v[120:123], a[212:215], a[188:191], v[120:123]\n" \
"ds_read_b64_tr_b16 a[206:207], v241 offset:26624\n" \
"v_cndmask_b32 v235, 0, v255, s[98:99]\n" \
"v_mfma_f32_16x16x32_bf16 v[120:123], a[208:211], a[172:175], v[120:123]\n" \
"v_cmp_gt_i32 s[96:97], 16, v254\n" \
"v_cmp_gt_i32 s[98:99], 17, v254\n" \
"v_mfma_f32_16x16x32_bf16 v[28:31], a[216:219], a[160:163], v[28:31]\n" \
"ds_read_b64_tr_b16 a[208:209], v240 offset:16896\n" \
"v_max3_f32 v250, |v148|, |v149|, v250\n" \
"v_mfma_f32_16x16x32_bf16 v[28:31], a[220:223], a[176:179], v[28:31]\n" \
"v_cndmask_b32 v236, 0, v255, s[96:97]\n" \
"v_cndmask_b32 v237, 0, v255, s[98:99]\n" \
"v_mfma_f32_16x16x32_bf16 v[60:63], a[220:223], a[180:183], v[60:63]\n" \
"buffer_load_dwordx4 a[240:243], v248, s[32:35], s79 offen offset:3072 sc1\n" \
"v_cmp_gt_i32 s[96:97], 18, v254\n" \
"v_mfma_f32_16x16x32_bf16 v[60:63], a[216:219], a[164:167], v[60:63]\n" \
"ds_read_b64_tr_b16 a[210:211], v240 offset:18944\n" \
"v_cmp_gt_i32 s[98:99], 19, v254\n" \
"v_mfma_f32_16x16x32_bf16 v[92:95], a[216:219], a[168:171], v[92:95]\n" \
"ds_read_b64_tr_b16 a[212:213], v240 offset:25088\n" \
"v_cndmask_b32 v238, 0, v255, s[96:97]\n" \
"v_mfma_f32_16x16x32_bf16 v[92:95], a[220:223], a[184:187], v[92:95]\n" \
"v_cndmask_b32 v239, 0, v255, s[98:99]\n" \
"v_max3_f32 v250, |v150|, |v151|, v250\n" \
"v_mfma_f32_16x16x32_bf16 v[124:127], a[220:223], a[188:191], v[124:127]\n" \
"s_waitcnt vmcnt(21)\n" \
"ds_read_b64_tr_b16 a[214:215], v240 offset:27136\n" \
"v_mfma_f32_16x16x32_bf16 v[124:127], a[216:219], a[172:175], v[124:127]\n" \
"s_waitcnt lgkmcnt(8)\n" \
"v_cvt_pk_bf16_f32 v144, v144, v145\n" \
"v_cvt_pk_bf16_f32 v145, v146, v147\n" \
"v_mfma_f32_16x16x32_bf16 v[0:3], a[192:195], a[0:3], v[0:3]\n" \
"ds_read_b64_tr_b16 a[216:217], v241 offset:16896\n" \
"v_cvt_pk_bf16_f32 v146, v148, v149\n" \
"v_mfma_f32_16x16x32_bf16 v[0:3], a[196:199], a[16:19], v[0:3]\n" \
"ds_read_b128 a[160:163], v248 offset:8192\n" \
"v_cvt_pk_bf16_f32 v147, v150, v151\n" \
"v_mfma_f32_16x16x32_bf16 v[32:35], a[196:199], a[20:23], v[32:35]\n" \
"buffer_load_dwordx4 a[244:247], v248, s[32:35], s80 offen offset:3072 sc1\n" \
"v_mfma_f32_16x16x32_bf16 v[32:35], a[192:195], a[4:7], v[32:35]\n" \
"ds_read_b64_tr_b16 a[218:219], v241 offset:18944\n" \
"v_mfma_f32_16x16x32_bf16 v[64:67], a[192:195], a[8:11], v[64:67]\n" \
"ds_read_b64_tr_b16 a[220:221], v241 offset:25088\n" \
"ds_read_b128 a[164:167], v248 offset:9216\n" \
"v_mfma_f32_16x16x32_bf16 v[64:67], a[196:199], a[24:27], v[64:67]\n" \
"buffer_store_dwordx4 v[144:147], v248, s[32:35], s85 offen offset:1024 sc1\n" \
"v_mfma_f32_16x16x32_bf16 v[96:99], a[196:199], a[28:31], v[96:99]\n" \
"ds_read_b64_tr_b16 a[222:223], v241 offset:27136\n" \
"v_mfma_f32_16x16x32_bf16 v[96:99], a[192:195], a[12:15], v[96:99]\n" \
"s_waitcnt lgkmcnt(10)\n" \
"ds_read_b128 a[168:171], v248 offset:10240\n" \
"v_mfma_f32_16x16x32_bf16 v[4:7], a[200:203], a[0:3], v[4:7]\n" \
"ds_read_b64_tr_b16 a[192:193], v240 offset:17408\n" \
"v_mfma_f32_16x16x32_bf16 v[4:7], a[204:207], a[16:19], v[4:7]\n" \
"ds_write_b128 v249, v[144:147] offset:5120\n" \
"v_accvgpr_read_b32 v152, a104\n" \
"v_mfma_f32_16x16x32_bf16 v[36:39], a[204:207], a[20:23], v[36:39]\n" \
"buffer_load_dwordx4 a[248:251], v248, s[32:35], s36 offen offset:3072 sc1\n" \
"v_accvgpr_read_b32 v153, a105\n" \
"v_mfma_f32_16x16x32_bf16 v[36:39], a[200:203], a[4:7], v[36:39]\n" \
"ds_read_b64_tr_b16 a[194:195], v240 offset:19456\n" \
"ds_read_b128 a[172:175], v248 offset:11264\n" \
"v_mfma_f32_16x16x32_bf16 v[68:71], a[200:203], a[8:11], v[68:71]\n" \
"ds_read_b64_tr_b16 a[196:197], v240 offset:25600\n" \
"v_accvgpr_read_b32 v154, a106\n" \
"v_mfma_f32_16x16x32_bf16 v[68:71], a[204:207], a[24:27], v[68:71]\n" \
"ds_read_b128 a[188:191], v248 offset:15360\n" \
"v_accvgpr_read_b32 v155, a107\n" \
"v_mfma_f32_16x16x32_bf16 v[100:103], a[204:207], a[28:31], v[100:103]\n" \
"ds_read_b64_tr_b16 a[198:199], v240 offset:27648\n" \
"v_accvgpr_read_b32 v156, a108\n" \
"v_mfma_f32_16x16x32_bf16 v[100:103], a[200:203], a[12:15], v[100:103]\n" \
"s_waitcnt lgkmcnt(14)\n" \
"v_accvgpr_read_b32 v157, a109\n" \
"v_accvgpr_read_b32 v158, a110\n" \
"v_mfma_f32_16x16x32_bf16 v[8:11], a[208:211], a[0:3], v[8:11]\n" \
"s_add_u32 s85, s94, 4096\n" \
"buffer_store_dwordx4 v[184:187], v248, s[32:35], s85 offen offset:2048 sc1\n" \
"v_mfma_f32_16x16x32_bf16 v[8:11], a[212:215], a[16:19], v[8:11]\n" \
"ds_read_b64_tr_b16 a[200:201], v241 offset:17408\n" \
"ds_read_b128 a[184:187], v248 offset:14336\n" \
"v_mfma_f32_16x16x32_bf16 v[40:43], a[212:215], a[20:23], v[40:43]\n" \
"buffer_load_dwordx4 a[252:255], v248, s[32:35], s37 offen offset:3072 sc1\n" \
"v_accvgpr_read_b32 v159, a111\n" \
"v_mfma_f32_16x16x32_bf16 v[40:43], a[208:211], a[4:7], v[40:43]\n" \
"ds_read_b64_tr_b16 a[202:203], v241 offset:19456\n" \
"ds_read_b128 a[180:183], v248 offset:13312\n" \
"v_mfma_f32_16x16x32_bf16 v[72:75], a[208:211], a[8:11], v[72:75]\n" \
"ds_read_b64_tr_b16 a[204:205], v241 offset:25600\n" \
"v_max3_f32 v250, |v152|, |v153|, v250\n" \
"v_mfma_f32_16x16x32_bf16 v[72:75], a[212:215], a[24:27], v[72:75]\n" \
"v_max3_f32 v250, |v154|, |v155|, v250\n" \
"v_max3_f32 v250, |v156|, |v157|, v250\n" \
"v_mfma_f32_16x16x32_bf16 v[104:107], a[212:215], a[28:31], v[104:107]\n" \
"ds_read_b64_tr_b16 a[206:207], v241 offset:27648\n" \
"ds_read_b128 a[176:179], v248 offset:12288\n" \
"v_mfma_f32_16x16x32_bf16 v[104:107], a[208:211], a[12:15], v[104:107]\n" \
"s_waitcnt lgkmcnt(15)\n" \
"v_max3_f32 v250, |v158|, |v159|, v250\n" \
"v_cvt_pk_bf16_f32 v152, v152, v153\n" \
"v_mfma_f32_16x16x32_bf16 v[12:15], a[216:219], a[0:3], v[12:15]\n" \
"ds_read_b64_tr_b16 a[208:209], v240 offset:17920\n" \
"v_cvt_pk_bf16_f32 v153, v154, v155\n" \
"v_mfma_f32_16x16x32_bf16 v[12:15], a[220:223], a[16:19], v[12:15]\n" \
"v_cvt_pk_bf16_f32 v154, v156, v157\n" \
"v_cvt_pk_bf16_f32 v155, v158, v159\n" \
"v_mfma_f32_16x16x32_bf16 v[44:47], a[220:223], a[20:23], v[44:47]\n" \
"ds_read_b64_tr_b16 a[210:211], v240 offset:19968\n" \
"v_accvgpr_write_b32 a104, v152\n" \
"v_mfma_f32_16x16x32_bf16 v[44:47], a[216:219], a[4:7], v[44:47]\n" \
"v_accvgpr_write_b32 a105, v153\n" \
"v_accvgpr_write_b32 a106, v154\n" \
"v_mfma_f32_16x16x32_bf16 v[76:79], a[216:219], a[8:11], v[76:79]\n" \
"ds_read_b64_tr_b16 a[212:213], v240 offset:26112\n" \
"v_accvgpr_write_b32 a107, v155\n" \
"v_mfma_f32_16x16x32_bf16 v[76:79], a[220:223], a[24:27], v[76:79]\n" \
"v_accvgpr_read_b32 v144, a48\n" \
"v_accvgpr_read_b32 v145, a49\n" \
"v_mfma_f32_16x16x32_bf16 v[108:111], a[220:223], a[28:31], v[108:111]\n" \
"ds_read_b64_tr_b16 a[214:215], v240 offset:28160\n" \
"v_accvgpr_read_b32 v146, a50\n" \
"v_mfma_f32_16x16x32_bf16 v[108:111], a[216:219], a[12:15], v[108:111]\n" \
"s_waitcnt lgkmcnt(11)\n" \
"v_accvgpr_read_b32 v147, a51\n" \
"v_accvgpr_read_b32 v148, a52\n" \
"v_mfma_f32_16x16x32_bf16 v[16:19], a[192:195], a[0:3], v[16:19]\n" \
"ds_read_b64_tr_b16 a[216:217], v241 offset:17920\n" \
"v_accvgpr_read_b32 v149, a53\n" \
"v_mfma_f32_16x16x32_bf16 v[16:19], a[196:199], a[16:19], v[16:19]\n" \
"v_accvgpr_read_b32 v150, a54\n" \
"v_accvgpr_read_b32 v151, a55\n" \
"v_mfma_f32_16x16x32_bf16 v[48:51], a[196:199], a[20:23], v[48:51]\n" \
"ds_read_b64_tr_b16 a[218:219], v241 offset:19968\n" \
"v_max3_f32 v250, |v144|, |v145|, v250\n" \
"v_mfma_f32_16x16x32_bf16 v[48:51], a[192:195], a[4:7], v[48:51]\n" \
"ds_read_b64_tr_b16 a[220:221], v241 offset:26112\n" \
"v_max3_f32 v250, |v146|, |v147|, v250\n" \
"v_mfma_f32_16x16x32_bf16 v[80:83], a[192:195], a[8:11], v[80:83]\n" \
"ds_read_b64_tr_b16 a[222:223], v241 offset:28160\n" \
"v_xor_b32 v240, 0x30000, v240\n" \
"v_mfma_f32_16x16x32_bf16 v[80:83], a[196:199], a[24:27], v[80:83]\n" \
"v_xor_b32 v241, 0x30000, v241\n" \
"v_max3_f32 v250, |v148|, |v149|, v250\n" \
"v_mfma_f32_16x16x32_bf16 v[112:115], a[196:199], a[28:31], v[112:115]\n" \
"v_max3_f32 v250, |v150|, |v151|, v250\n" \
"v_cvt_pk_bf16_f32 v144, v144, v145\n" \
"v_mfma_f32_16x16x32_bf16 v[112:115], a[192:195], a[12:15], v[112:115]\n" \
"s_waitcnt lgkmcnt(9)\n" \
"v_cvt_pk_bf16_f32 v145, v146, v147\n" \
"v_cvt_pk_bf16_f32 v146, v148, v149\n" \
"v_mfma_f32_16x16x32_bf16 v[20:23], a[200:203], a[0:3], v[20:23]\n" \
"v_cvt_pk_bf16_f32 v147, v150, v151\n" \
"v_mfma_f32_16x16x32_bf16 v[20:23], a[204:207], a[16:19], v[20:23]\n" \
"ds_write_b128 v249, v[144:147] offset:6144\n" \
"v_accvgpr_read_b32 v152, a112\n" \
"v_mfma_f32_16x16x32_bf16 v[52:55], a[204:207], a[20:23], v[52:55]\n" \
"v_accvgpr_read_b32 v153, a113\n" \
"v_accvgpr_read_b32 v154, a114\n" \
"v_mfma_f32_16x16x32_bf16 v[52:55], a[200:203], a[4:7], v[52:55]\n" \
"v_accvgpr_read_b32 v155, a115\n" \
"v_accvgpr_read_b32 v156, a116\n" \
"v_mfma_f32_16x16x32_bf16 v[84:87], a[200:203], a[8:11], v[84:87]\n" \
"s_waitcnt vmcnt(25) lgkmcnt(1)\n" \
"s_barrier\n" \
"v_accvgpr_read_b32 v157, a117\n" \
"v_mfma_f32_16x16x32_bf16 v[84:87], a[204:207], a[24:27], v[84:87]\n" \
"s_add_u32 s85, s71, 593920\n" \
"ds_read_b64_tr_b16 a[192:193], v240 offset:32768\n" \
"ds_read_b64_tr_b16 a[194:195], v240 offset:34816\n" \
"v_mfma_f32_16x16x32_bf16 v[116:119], a[204:207], a[28:31], v[116:119]\n" \
"buffer_store_dwordx4 v[144:147], v248, s[32:35], s85 offen offset:2048 sc1\n" \
"v_mfma_f32_16x16x32_bf16 v[116:119], a[200:203], a[12:15], v[116:119]\n" \
"ds_read_b64_tr_b16 a[196:197], v240 offset:40960\n" \
"ds_read_b64_tr_b16 a[198:199], v240 offset:43008\n" \
"v_mfma_f32_16x16x32_bf16 v[24:27], a[208:211], a[0:3], v[24:27]\n" \
"ds_read_b64_tr_b16 a[200:201], v241 offset:32768\n" \
"v_accvgpr_read_b32 v158, a118\n" \
"v_mfma_f32_16x16x32_bf16 v[24:27], a[212:215], a[16:19], v[24:27]\n" \
"v_accvgpr_read_b32 v159, a119\n" \
"v_max3_f32 v250, |v152|, |v153|, v250\n" \
"v_mfma_f32_16x16x32_bf16 v[56:59], a[212:215], a[20:23], v[56:59]\n" \
"ds_read_b64_tr_b16 a[202:203], v241 offset:34816\n" \
"v_max3_f32 v250, |v154|, |v155|, v250\n" \
"v_mfma_f32_16x16x32_bf16 v[56:59], a[208:211], a[4:7], v[56:59]\n" \
"v_max3_f32 v250, |v156|, |v157|, v250\n" \
"v_max3_f32 v250, |v158|, |v159|, v250\n" \
"v_mfma_f32_16x16x32_bf16 v[88:91], a[208:211], a[8:11], v[88:91]\n" \
"ds_read_b64_tr_b16 a[204:205], v241 offset:40960\n" \
"v_cvt_pk_bf16_f32 v152, v152, v153\n" \
"v_mfma_f32_16x16x32_bf16 v[88:91], a[212:215], a[24:27], v[88:91]\n" \
"v_cvt_pk_bf16_f32 v153, v154, v155\n" \
"v_cvt_pk_bf16_f32 v154, v156, v157\n" \
"v_mfma_f32_16x16x32_bf16 v[120:123], a[212:215], a[28:31], v[120:123]\n" \
"ds_read_b64_tr_b16 a[206:207], v241 offset:43008\n" \
"v_cvt_pk_bf16_f32 v155, v158, v159\n" \
"v_mfma_f32_16x16x32_bf16 v[120:123], a[208:211], a[12:15], v[120:123]\n" \
"v_accvgpr_write_b32 a112, v152\n" \
"v_accvgpr_write_b32 a113, v153\n" \
"v_mfma_f32_16x16x32_bf16 v[28:31], a[216:219], a[0:3], v[28:31]\n" \
"s_lshl2_add_u32 s76, s67, %[wid]\n" \
"ds_read_b64_tr_b16 a[208:209], v240 offset:33280\n" \
"v_accvgpr_write_b32 a114, v154\n" \
"v_mfma_f32_16x16x32_bf16 v[28:31], a[220:223], a[16:19], v[28:31]\n" \
"s_sub_u32 s77, 15, s67\n" \
"s_lshl_b32 s17, s77, 15\n" \
"v_accvgpr_write_b32 a115, v155\n" \
"v_mfma_f32_16x16x32_bf16 v[60:63], a[220:223], a[20:23], v[60:63]\n" \
"s_lshl2_add_u32 s77, s77, %[wid]\n" \
"s_lshl_b32 s28, %[wid], 12\n" \
"ds_read_b64_tr_b16 a[210:211], v240 offset:35328\n" \
"v_mfma_f32_16x16x32_bf16 v[60:63], a[216:219], a[4:7], v[60:63]\n" \
"s_add_u32 s88, s73, s28\n" \
"s_add_u32 s17, s17, s88\n" \
"s_add_u32 s18, s88, 524288\n" \
"v_mfma_f32_16x16x32_bf16 v[92:95], a[216:219], a[8:11], v[92:95]\n" \
"s_add_u32 s24, s88, 229376\n" \
"s_lshl_b32 s16, s67, 15\n" \
"ds_read_b64_tr_b16 a[212:213], v240 offset:41472\n" \
"v_mfma_f32_16x16x32_bf16 v[92:95], a[220:223], a[24:27], v[92:95]\n" \
"s_add_u32 s16, s16, s88\n" \
"s_add_u32 s21, s16, 0x8000\n" \
"v_mfma_f32_16x16x32_bf16 v[124:127], a[220:223], a[28:31], v[124:127]\n" \
"s_waitcnt vmcnt(11)\n" \
"ds_read_b64_tr_b16 a[214:215], v240 offset:43520\n" \
"v_mfma_f32_16x16x32_bf16 v[124:127], a[216:219], a[12:15], v[124:127]\n" \
"s_waitcnt lgkmcnt(8)\n" \
"s_cmp_eq_u32 s88, s16\n" \
"s_cmov_b32 s88, s21\n" \
"v_mfma_f32_16x16x32_bf16 v[0:3], a[192:195], a[128:131], v[0:3]\n" \
"s_mov_b32 s19, s28\n" \
"s_add_u32 s20, s28, 0x10000\n" \
"ds_read_b64_tr_b16 a[216:217], v241 offset:33280\n" \
"v_mfma_f32_16x16x32_bf16 v[0:3], a[196:199], a[144:147], v[0:3]\n" \
"s_lshl_b32 s36, s64, 6\n" \
"s_add_u32 s36, s36, s77\n" \
"s_lshl2_add_u32 s36, s36, s66\n" \
"v_mfma_f32_16x16x32_bf16 v[32:35], a[196:199], a[148:151], v[32:35]\n" \
"s_lshl_b32 s36, s36, 13\n" \
"s_add_u32 s36, s36, 59768832\n" \
"ds_read_b64_tr_b16 a[218:219], v241 offset:35328\n" \
"v_mfma_f32_16x16x32_bf16 v[32:35], a[192:195], a[132:135], v[32:35]\n" \
"s_lshr_b32 s83, %[wid], 1\n" \
"s_mul_i32 s93, s83, 573440\n" \
"s_lshl_b32 s87, %[wid], 13\n" \
"v_mfma_f32_16x16x32_bf16 v[64:67], a[192:195], a[136:139], v[64:67]\n" \
"s_add_u32 s93, s93, s87\n" \
"s_add_u32 s93, s93, s73\n" \
"ds_read_b64_tr_b16 a[220:221], v241 offset:41472\n" \
"v_mfma_f32_16x16x32_bf16 v[64:67], a[196:199], a[152:155], v[64:67]\n" \
"s_add_u32 s93, s93, 573440\n" \
"s_add_u32 s87, s87, 0x20000\n" \
"s_mov_b32 s25, 1073741824\n" \
"v_mfma_f32_16x16x32_bf16 v[96:99], a[196:199], a[156:159], v[96:99]\n" \
"s_lshl_b32 s26, s68, 7\n" \
"s_add_u32 s26, s26, 655360\n" \
"ds_read_b64_tr_b16 a[222:223], v241 offset:43520\n" \
"v_mfma_f32_16x16x32_bf16 v[96:99], a[192:195], a[140:143], v[96:99]\n" \
"s_waitcnt lgkmcnt(8)\n" \
"s_lshl_b32 s81, s68, 8\n" \
"s_add_u32 s81, s81, 81920\n" \
"v_mfma_f32_16x16x32_bf16 v[4:7], a[200:203], a[128:131], v[4:7]\n" \
"s_lshl_b32 s84, s64, 15\n" \
"s_add_u32 s84, s84, 41943040\n" \
"ds_read_b64_tr_b16 a[192:193], v240 offset:33792\n" \
"v_mfma_f32_16x16x32_bf16 v[4:7], a[204:207], a[144:147], v[4:7]\n" \
"s_lshl_b32 s29, s76, 18\n" \
"s_add_u32 s29, s29, s84\n" \
"s_lshl_b32 s37, s77, 18\n" \
"v_mfma_f32_16x16x32_bf16 v[36:39], a[204:207], a[148:151], v[36:39]\n" \
"s_add_u32 s37, s37, s84\n" \
"s_lshl_b32 s83, s66, 13\n" \
"ds_read_b64_tr_b16 a[194:195], v240 offset:35840\n" \
"v_mfma_f32_16x16x32_bf16 v[36:39], a[200:203], a[132:135], v[36:39]\n" \
"s_add_u32 s85, s94, 4096\n" \
"s_add_u32 s30, s29, s83\n" \
"s_lshl3_add_u32 s83, s67, s64\n" \
"v_mfma_f32_16x16x32_bf16 v[68:71], a[200:203], a[136:139], v[68:71]\n" \
"s_lshl3_add_u32 s83, s83, %[wid]\n" \
"buffer_store_dwordx4 v[188:191], v248, s[32:35], s85 offen offset:3072 sc1\n" \
"v_mfma_f32_16x16x32_bf16 v[68:71], a[204:207], a[152:155], v[68:71]\n" \
"s_lshl_b32 s83, s83, 4\n" \
"s_add_u32 s31, s83, 73728\n" \
"ds_read_b64_tr_b16 a[196:197], v240 offset:41984\n" \
"v_mfma_f32_16x16x32_bf16 v[100:103], a[204:207], a[156:159], v[100:103]\n" \
"s_mul_i32 s69, s64, 28\n" \
"ds_read_b64_tr_b16 a[198:199], v240 offset:44032\n" \
"v_mfma_f32_16x16x32_bf16 v[100:103], a[200:203], a[140:143], v[100:103]\n" \
"s_waitcnt lgkmcnt(8)\n" \
"s_cmp_lt_u32 s65, 28\n" \
"s_cselect_b32 s83, s65, 0\n" \
"v_mfma_f32_16x16x32_bf16 v[8:11], a[208:211], a[128:131], v[8:11]\n" \
"s_add_u32 s69, s69, s83\n" \
"s_lshl_b32 s79, s69, 13\n" \
"ds_read_b64_tr_b16 a[200:201], v241 offset:33792\n" \
"v_mfma_f32_16x16x32_bf16 v[8:11], a[212:215], a[144:147], v[8:11]\n" \
"s_lshl_b32 s83, %[wid], 8\n" \
"s_add_u32 s79, s79, s83\n" \
"s_add_u32 s80, s79, 4096\n" \
"v_mfma_f32_16x16x32_bf16 v[40:43], a[212:215], a[148:151], v[40:43]\n" \
"s_mov_b64 s[42:43], s[54:55]\n" \
"ds_read_b64_tr_b16 a[202:203], v241 offset:35840\n" \
"v_mfma_f32_16x16x32_bf16 v[40:43], a[208:211], a[132:135], v[40:43]\n" \
"s_bitcmp1_b32 %[bid], 6\n" \
"s_cselect_b64 s[40:41], s[56:57], s[52:53]\n" \
"s_cselect_b32 s39, 2048, 0\n" \
"v_mfma_f32_16x16x32_bf16 v[72:75], a[208:211], a[136:139], v[72:75]\n" \
"s_bfe_u32 s83, %[bid], 0x40002\n" \
"s_and_b32 s84, %[bid], 3\n" \
"ds_read_b64_tr_b16 a[204:205], v241 offset:41984\n" \
"v_mfma_f32_16x16x32_bf16 v[72:75], a[212:215], a[152:155], v[72:75]\n" \
"s_lshl_b32 s22, %[wid], 3\n" \
"s_and_b32 s22, s22, 16\n" \
"s_and_b32 s23, %[wid], 1\n" \
"v_mfma_f32_16x16x32_bf16 v[104:107], a[212:215], a[156:159], v[104:107]\n" \
"s_add_u32 s38, s22, s23\n" \
"ds_read_b64_tr_b16 a[206:207], v241 offset:44032\n" \
"v_accvgpr_read_b32 v144, a56\n" \
"v_mfma_f32_16x16x32_bf16 v[104:107], a[208:211], a[140:143], v[104:107]\n" \
"s_waitcnt lgkmcnt(8)\n" \
"v_accvgpr_read_b32 v145, a57\n" \
"v_accvgpr_read_b32 v146, a58\n" \
"v_mfma_f32_16x16x32_bf16 v[12:15], a[216:219], a[128:131], v[12:15]\n" \
"s_lshl_b32 s72, s83, 5\n" \
"ds_read_b64_tr_b16 a[208:209], v240 offset:34304\n" \
"v_accvgpr_read_b32 v147, a59\n" \
"v_mfma_f32_16x16x32_bf16 v[12:15], a[220:223], a[144:147], v[12:15]\n" \
"s_add_u32 s38, s38, s72\n" \
"v_accvgpr_read_b32 v148, a60\n" \
"v_accvgpr_read_b32 v149, a61\n" \
"v_mfma_f32_16x16x32_bf16 v[44:47], a[220:223], a[148:151], v[44:47]\n" \
"s_mul_i32 s38, s38, 3584\n" \
"ds_read_b64_tr_b16 a[210:211], v240 offset:36352\n" \
"v_accvgpr_read_b32 v150, a62\n" \
"v_mfma_f32_16x16x32_bf16 v[44:47], a[216:219], a[132:135], v[44:47]\n" \
"s_lshl_b32 s72, s84, 7\n" \
"v_accvgpr_read_b32 v151, a63\n" \
"v_max3_f32 v250, |v144|, |v145|, v250\n" \
"v_mfma_f32_16x16x32_bf16 v[76:79], a[216:219], a[136:139], v[76:79]\n" \
"s_add_u32 s38, s38, s72\n" \
"ds_read_b64_tr_b16 a[212:213], v240 offset:42496\n" \
"v_max3_f32 v250, |v146|, |v147|, v250\n" \
"v_mfma_f32_16x16x32_bf16 v[76:79], a[220:223], a[152:155], v[76:79]\n" \
"s_lshl3_add_u32 s39, s22, s39\n" \
"v_max3_f32 v250, |v148|, |v149|, v250\n" \
"v_max3_f32 v250, |v150|, |v151|, v250\n" \
"v_mfma_f32_16x16x32_bf16 v[108:111], a[220:223], a[156:159], v[108:111]\n" \
"s_lshl_b32 s23, s23, 10\n" \
"ds_read_b64_tr_b16 a[214:215], v240 offset:44544\n" \
"v_cvt_pk_bf16_f32 v144, v144, v145\n" \
"v_mfma_f32_16x16x32_bf16 v[108:111], a[216:219], a[140:143], v[108:111]\n" \
"s_waitcnt lgkmcnt(8)\n" \
"v_cvt_pk_bf16_f32 v145, v146, v147\n" \
"v_cvt_pk_bf16_f32 v146, v148, v149\n" \
"v_mfma_f32_16x16x32_bf16 v[16:19], a[192:195], a[128:131], v[16:19]\n" \
"s_add_u32 s39, s39, s23\n" \
"ds_read_b64_tr_b16 a[216:217], v241 offset:34304\n" \
"v_cvt_pk_bf16_f32 v147, v150, v151\n" \
"v_mfma_f32_16x16x32_bf16 v[16:19], a[196:199], a[144:147], v[16:19]\n" \
"s_lshl_b32 s22, s84, 17\n" \
"s_add_u32 s39, s39, s22\n" \
"ds_write_b128 v249, v[144:147] offset:7168\n" \
"v_mfma_f32_16x16x32_bf16 v[48:51], a[196:199], a[148:151], v[48:51]\n" \
"s_lshr_b32 s22, s83, 2\n" \
"ds_read_b64_tr_b16 a[218:219], v241 offset:36352\n" \
"v_mov_b32 v254, 0x3f803f80\n" \
"v_mfma_f32_16x16x32_bf16 v[48:51], a[192:195], a[132:135], v[48:51]\n" \
"s_lshl_b32 s22, s22, 12\n" \
"ds_read_b64_tr_b16 a[220:221], v241 offset:42496\n" \
"v_accvgpr_write_b32 a48, v254\n" \
"v_mfma_f32_16x16x32_bf16 v[80:83], a[192:195], a[136:139], v[80:83]\n" \
"s_add_u32 s39, s39, s22\n" \
"ds_read_b64_tr_b16 a[222:223], v241 offset:44544\n" \
"v_accvgpr_write_b32 a49, v254\n" \
"v_mfma_f32_16x16x32_bf16 v[80:83], a[196:199], a[152:155], v[80:83]\n" \
"s_and_b32 s22, s83, 3\n" \
"v_accvgpr_write_b32 a50, v254\n" \
"v_accvgpr_write_b32 a51, v254\n" \
"v_mfma_f32_16x16x32_bf16 v[112:115], a[196:199], a[156:159], v[112:115]\n" \
"s_lshl_b32 s22, s22, 8\n" \
"v_accvgpr_write_b32 a56, 1\n" \
"v_accvgpr_read_b32 v152, a120\n" \
"v_mfma_f32_16x16x32_bf16 v[112:115], a[192:195], a[140:143], v[112:115]\n" \
"s_waitcnt lgkmcnt(9)\n" \
"v_accvgpr_read_b32 v153, a121\n" \
"v_accvgpr_read_b32 v154, a122\n" \
"v_mfma_f32_16x16x32_bf16 v[20:23], a[200:203], a[128:131], v[20:23]\n" \
"s_add_u32 s39, s39, s22\n" \
"v_accvgpr_read_b32 v155, a123\n" \
"v_accvgpr_read_b32 v156, a124\n" \
"v_mfma_f32_16x16x32_bf16 v[20:23], a[204:207], a[144:147], v[20:23]\n" \
"s_add_u32 s85, s71, 593920\n" \
"v_accvgpr_read_b32 v157, a125\n" \
"v_accvgpr_read_b32 v158, a126\n" \
"v_mfma_f32_16x16x32_bf16 v[52:55], a[204:207], a[148:151], v[52:55]\n" \
"s_add_u32 s39, s39, 77070336\n" \
"buffer_store_dwordx4 v[144:147], v248, s[32:35], s85 offen offset:3072 sc1\n" \
"v_mfma_f32_16x16x32_bf16 v[52:55], a[200:203], a[132:135], v[52:55]\n" \
"s_and_b32 s83, %[bid], 127\n" \
"v_accvgpr_read_b32 v159, a127\n" \
"v_max3_f32 v250, |v152|, |v153|, v250\n" \
"v_mfma_f32_16x16x32_bf16 v[84:87], a[200:203], a[136:139], v[84:87]\n" \
"s_waitcnt lgkmcnt(0)\n" \
"s_barrier\n" \
"v_max3_f32 v250, |v154|, |v155|, v250\n" \
"v_mfma_f32_16x16x32_bf16 v[84:87], a[204:207], a[152:155], v[84:87]\n" \
"s_lshl_b32 s78, %[wid], 7\n" \
"ds_read_b64_tr_b16 a[192:193], v240 offset:49152\n" \
"ds_read_b64_tr_b16 a[194:195], v240 offset:51200\n" \
"v_mfma_f32_16x16x32_bf16 v[116:119], a[204:207], a[156:159], v[116:119]\n" \
"s_add_u32 s78, s78, s83\n" \
"ds_read_b64_tr_b16 a[196:197], v240 offset:57344\n" \
"ds_read_b64_tr_b16 a[198:199], v240 offset:59392\n" \
"v_mfma_f32_16x16x32_bf16 v[116:119], a[200:203], a[140:143], v[116:119]\n" \
"s_lshl_b32 s78, s78, 2\n" \
"v_max3_f32 v250, |v156|, |v157|, v250\n" \
"v_max3_f32 v250, |v158|, |v159|, v250\n" \
"v_mfma_f32_16x16x32_bf16 v[24:27], a[208:211], a[128:131], v[24:27]\n" \
"s_add_u32 s78, s78, 659456\n" \
"ds_read_b64_tr_b16 a[200:201], v241 offset:49152\n" \
"v_cvt_pk_bf16_f32 v152, v152, v153\n" \
"v_mfma_f32_16x16x32_bf16 v[24:27], a[212:215], a[144:147], v[24:27]\n" \
"s_cmp_lt_u32 s67, 4\n" \
"s_cselect_b32 s78, 1073741824, s78\n" \
"v_cvt_pk_bf16_f32 v153, v154, v155\n" \
"v_mfma_f32_16x16x32_bf16 v[56:59], a[212:215], a[148:151], v[56:59]\n" \
"s_lshl2_add_u32 s72, s83, %[wid]\n" \
"ds_read_b64_tr_b16 a[202:203], v241 offset:51200\n" \
"v_cvt_pk_bf16_f32 v154, v156, v157\n" \
"v_mfma_f32_16x16x32_bf16 v[56:59], a[208:211], a[132:135], v[56:59]\n" \
"s_mul_i32 s72, s72, 3584\n" \
"v_cvt_pk_bf16_f32 v155, v158, v159\n" \
"v_accvgpr_write_b32 a120, v152\n" \
"v_mfma_f32_16x16x32_bf16 v[88:91], a[208:211], a[136:139], v[88:91]\n" \
"s_mov_b32 s76, 0\n" \
"ds_read_b64_tr_b16 a[204:205], v241 offset:57344\n" \
"v_and_b32 v244, 31, v251\n" \
"v_mfma_f32_16x16x32_bf16 v[88:91], a[212:215], a[152:155], v[88:91]\n" \
"s_lshl_b32 s84, s67, 1\n" \
"v_lshlrev_b32 v244, 2, v244\n" \
"v_accvgpr_write_b32 a121, v153\n" \
"v_mfma_f32_16x16x32_bf16 v[120:123], a[212:215], a[156:159], v[120:123]\n" \
"s_sub_u32 s84, s84, 11\n" \
"ds_read_b64_tr_b16 a[206:207], v241 offset:59392\n" \
"v_accvgpr_write_b32 a122, v154\n" \
"v_mfma_f32_16x16x32_bf16 v[120:123], a[208:211], a[140:143], v[120:123]\n" \
".Lst0_7_%=:\n" \
"s_cbranch_execz .Lst0_11_%=\n" \
"s_max_i32 s84, s84, 0\n" \
"v_accvgpr_write_b32 a123, v155\n" \
"v_max_u32_dpp v250, v250, v250 quad_perm:[1,0,3,2] row_mask:0xf bank_mask:0xf\n" \
"v_mfma_f32_16x16x32_bf16 v[28:31], a[216:219], a[128:131], v[28:31]\n" \
"s_add_u32 s83, s84, s67\n" \
"ds_read_b64_tr_b16 a[208:209], v240 offset:49664\n" \
"v_lshlrev_b32 v252, 2, v251\n" \
"v_mfma_f32_16x16x32_bf16 v[28:31], a[220:223], a[144:147], v[28:31]\n" \
"s_and_b32 s83, s83, 1\n" \
"v_max_u32_dpp v250, v250, v250 quad_perm:[2,3,0,1] row_mask:0xf bank_mask:0xf\n" \
"v_add_u32 v242, 0x10000, v248\n" \
"v_mfma_f32_16x16x32_bf16 v[60:63], a[220:223], a[148:151], v[60:63]\n" \
"s_lshl_b32 s83, s83, 15\n" \
"ds_read_b64_tr_b16 a[210:211], v240 offset:51712\n" \
"v_xor_b32 v249, 0x4000, v249\n" \
"v_mfma_f32_16x16x32_bf16 v[60:63], a[216:219], a[132:135], v[60:63]\n" \
"s_movk_i32 s27, 0x4000\n" \
"s_mov_b32 s92, s21\n" \
"v_max_u32_dpp v250, v250, v250 row_half_mirror row_mask:0xf bank_mask:0xf\n" \
"v_mfma_f32_16x16x32_bf16 v[92:95], a[216:219], a[136:139], v[92:95]\n" \
"s_sub_u32 s22, s18, 524288\n" \
"s_lshl_b32 s77, s84, 16\n" \
"ds_read_b64_tr_b16 a[212:213], v240 offset:57856\n" \
"v_mfma_f32_16x16x32_bf16 v[92:95], a[220:223], a[152:155], v[92:95]\n" \
"s_add_u32 s77, s77, s22\n" \
"v_max_u32_dpp v250, v250, v250 row_mirror row_mask:0xf bank_mask:0xf\n" \
"v_mov_b32 v152, v250\n" \
"v_mfma_f32_16x16x32_bf16 v[124:127], a[220:223], a[156:159], v[124:127]\n" \
"s_waitcnt vmcnt(3)\n" \
"ds_read_b64_tr_b16 a[214:215], v240 offset:59904\n" \
"v_mfma_f32_16x16x32_bf16 v[124:127], a[216:219], a[140:143], v[124:127]\n" \
"s_waitcnt lgkmcnt(8)\n" \
"v_permlane16_swap_b32 v250, v152\n" \
"v_mfma_f32_16x16x32_bf16 v[0:3], a[192:195], a[224:227], v[0:3]\n" \
"s_movk_i32 s91, 0x4000\n" \
"ds_read_b64_tr_b16 a[216:217], v241 offset:49664\n" \
"v_max_u32 v250, v250, v152\n" \
"v_mfma_f32_16x16x32_bf16 v[0:3], a[196:199], a[240:243], v[0:3]\n" \
"ds_read_b64_tr_b16 a[218:219], v241 offset:51712\n" \
"ds_read_b64_tr_b16 a[220:221], v241 offset:57856\n" \
"v_mfma_f32_16x16x32_bf16 v[32:35], a[196:199], a[244:247], v[32:35]\n" \
"ds_read_b64_tr_b16 a[222:223], v241 offset:59904\n" \
"ds_read_b128 a[128:131], v248 offset:0\n" \
"v_mfma_f32_16x16x32_bf16 v[32:35], a[192:195], a[228:231], v[32:35]\n" \
"s_waitcnt lgkmcnt(9)\n" \
"v_mov_b32 v152, v250\n" \
"v_mfma_f32_16x16x32_bf16 v[4:7], a[200:203], a[224:227], v[4:7]\n" \
"ds_read_b64_tr_b16 a[192:193], v240 offset:50176\n" \
"ds_read_b128 a[132:135], v248 offset:1024\n" \
"v_mfma_f32_16x16x32_bf16 v[4:7], a[204:207], a[240:243], v[4:7]\n" \
"ds_read_b64_tr_b16 a[194:195], v240 offset:52224\n" \
"ds_read_b64_tr_b16 a[196:197], v240 offset:58368\n" \
"v_mfma_f32_16x16x32_bf16 v[36:39], a[204:207], a[244:247], v[36:39]\n" \
"ds_read_b64_tr_b16 a[198:199], v240 offset:60416\n" \
"v_mfma_f32_16x16x32_bf16 v[36:39], a[200:203], a[228:231], v[36:39]\n" \
"s_waitcnt lgkmcnt(10)\n" \
"ds_read_b128 a[136:139], v248 offset:2048\n" \
"v_mfma_f32_16x16x32_bf16 v[8:11], a[208:211], a[224:227], v[8:11]\n" \
"ds_read_b64_tr_b16 a[200:201], v241 offset:50176\n" \
"v_mfma_f32_16x16x32_bf16 v[8:11], a[212:215], a[240:243], v[8:11]\n" \
"ds_read_b64_tr_b16 a[202:203], v241 offset:52224\n" \
"ds_read_b64_tr_b16 a[204:205], v241 offset:58368\n" \
"v_mfma_f32_16x16x32_bf16 v[40:43], a[212:215], a[244:247], v[40:43]\n" \
"ds_read_b64_tr_b16 a[206:207], v241 offset:60416\n" \
"ds_read_b128 a[140:143], v248 offset:3072\n" \
"v_mfma_f32_16x16x32_bf16 v[40:43], a[208:211], a[228:231], v[40:43]\n" \
"s_waitcnt lgkmcnt(12)\n" \
"v_permlane32_swap_b32 v250, v152\n" \
"v_mfma_f32_16x16x32_bf16 v[12:15], a[216:219], a[224:227], v[12:15]\n" \
"ds_read_b64_tr_b16 a[208:209], v240 offset:50688\n" \
"v_max_u32 v250, v250, v152\n" \
"v_mfma_f32_16x16x32_bf16 v[12:15], a[220:223], a[240:243], v[12:15]\n" \
"ds_read_b64_tr_b16 a[210:211], v240 offset:52736\n" \
"ds_read_b64_tr_b16 a[212:213], v240 offset:58880\n" \
"v_mfma_f32_16x16x32_bf16 v[44:47], a[220:223], a[244:247], v[44:47]\n" \
"ds_read_b64_tr_b16 a[214:215], v240 offset:60928\n" \
"ds_read_b128 a[156:159], v248 offset:7168\n" \
"v_mfma_f32_16x16x32_bf16 v[44:47], a[216:219], a[228:231], v[44:47]\n" \
"s_waitcnt lgkmcnt(11)\n" \
"v_max_u32 v250, 1, v250\n" \
"v_cvt_pk_bf16_f32 v192, v0, v1\n" \
"v_mfma_f32_16x16x32_bf16 v[16:19], a[192:195], a[224:227], v[16:19]\n" \
"ds_read_b64_tr_b16 a[216:217], v241 offset:50688\n" \
"ds_read_b128 a[152:155], v248 offset:6144\n" \
"v_mfma_f32_16x16x32_bf16 v[16:19], a[196:199], a[240:243], v[16:19]\n" \
"ds_read_b64_tr_b16 a[218:219], v241 offset:52736\n" \
"ds_read_b64_tr_b16 a[220:221], v241 offset:58880\n" \
"v_mfma_f32_16x16x32_bf16 v[48:51], a[196:199], a[244:247], v[48:51]\n" \
"ds_read_b64_tr_b16 a[222:223], v241 offset:60928\n" \
"v_cvt_pk_bf16_f32 v193, v2, v3\n" \
"v_mfma_f32_16x16x32_bf16 v[48:51], a[192:195], a[228:231], v[48:51]\n" \
"s_waitcnt lgkmcnt(11)\n" \
"ds_read_b128 a[148:151], v248 offset:5120\n" \
"v_cvt_pk_bf16_f32 v194, v4, v5\n" \
"v_mfma_f32_16x16x32_bf16 v[20:23], a[200:203], a[224:227], v[20:23]\n" \
"ds_read_b64_tr_b16 a[192:193], v240 offset:49152\n" \
"v_cvt_pk_bf16_f32 v195, v6, v7\n" \
"v_mfma_f32_16x16x32_bf16 v[20:23], a[204:207], a[240:243], v[20:23]\n" \
"ds_read_b64_tr_b16 a[194:195], v240 offset:51200\n" \
"ds_read_b64_tr_b16 a[196:197], v240 offset:57344\n" \
"v_mfma_f32_16x16x32_bf16 v[52:55], a[204:207], a[244:247], v[52:55]\n" \
"ds_read_b64_tr_b16 a[198:199], v240 offset:59392\n" \
"ds_read_b128 a[144:147], v248 offset:4096\n" \
"v_mfma_f32_16x16x32_bf16 v[52:55], a[200:203], a[228:231], v[52:55]\n" \
"s_waitcnt lgkmcnt(12)\n" \
"v_cvt_pk_bf16_f32 v208, v32, v33\n" \
"v_cvt_pk_bf16_f32 v209, v34, v35\n" \
"v_mfma_f32_16x16x32_bf16 v[24:27], a[208:211], a[224:227], v[24:27]\n" \
"ds_read_b64_tr_b16 a[200:201], v241 offset:49152\n" \
"v_cvt_pk_bf16_f32 v210, v36, v37\n" \
"v_mfma_f32_16x16x32_bf16 v[24:27], a[212:215], a[240:243], v[24:27]\n" \
"ds_read_b64_tr_b16 a[202:203], v241 offset:51200\n" \
"ds_read_b64_tr_b16 a[204:205], v241 offset:57344\n" \
"v_mfma_f32_16x16x32_bf16 v[56:59], a[212:215], a[244:247], v[56:59]\n" \
"ds_read_b64_tr_b16 a[206:207], v241 offset:59392\n" \
"v_cvt_pk_bf16_f32 v211, v38, v39\n" \
"v_mfma_f32_16x16x32_bf16 v[56:59], a[208:211], a[228:231], v[56:59]\n" \
"s_waitcnt lgkmcnt(10)\n" \
"v_cvt_pk_bf16_f32 v196, v8, v9\n" \
"v_cvt_pk_bf16_f32 v197, v10, v11\n" \
"v_mfma_f32_16x16x32_bf16 v[28:31], a[216:219], a[224:227], v[28:31]\n" \
"ds_read_b64_tr_b16 a[208:209], v240 offset:49664\n" \
"v_cvt_pk_bf16_f32 v198, v12, v13\n" \
"v_mfma_f32_16x16x32_bf16 v[28:31], a[220:223], a[240:243], v[28:31]\n" \
"ds_read_b64_tr_b16 a[210:211], v240 offset:51712\n" \
"ds_read_b64_tr_b16 a[212:213], v240 offset:57856\n" \
"v_mfma_f32_16x16x32_bf16 v[60:63], a[220:223], a[244:247], v[60:63]\n" \
"ds_read_b64_tr_b16 a[214:215], v240 offset:59904\n" \
"v_cvt_pk_bf16_f32 v199, v14, v15\n" \
"v_mfma_f32_16x16x32_bf16 v[60:63], a[216:219], a[228:231], v[60:63]\n" \
"s_waitcnt lgkmcnt(9)\n" \
"v_cvt_pk_bf16_f32 v212, v40, v41\n" \
"v_cvt_pk_bf16_f32 v213, v42, v43\n" \
"v_mfma_f32_16x16x32_bf16 v[64:67], a[192:195], a[232:235], v[64:67]\n" \
"ds_read_b64_tr_b16 a[216:217], v241 offset:49664\n" \
"v_cvt_pk_bf16_f32 v214, v44, v45\n" \
"v_mfma_f32_16x16x32_bf16 v[64:67], a[196:199], a[248:251], v[64:67]\n" \
"ds_read_b64_tr_b16 a[218:219], v241 offset:51712\n" \
"ds_read_b64_tr_b16 a[220:221], v241 offset:57856\n" \
"v_mfma_f32_16x16x32_bf16 v[96:99], a[196:199], a[252:255], v[96:99]\n" \
"ds_read_b64_tr_b16 a[222:223], v241 offset:59904\n" \
"v_cvt_pk_bf16_f32 v215, v46, v47\n" \
"v_mfma_f32_16x16x32_bf16 v[96:99], a[192:195], a[236:239], v[96:99]\n" \
"s_waitcnt lgkmcnt(8)\n" \
"v_cvt_pk_bf16_f32 v200, v16, v17\n" \
"v_cvt_pk_bf16_f32 v201, v18, v19\n" \
"v_mfma_f32_16x16x32_bf16 v[68:71], a[200:203], a[232:235], v[68:71]\n" \
"ds_read_b64_tr_b16 a[192:193], v240 offset:50176\n" \
"v_cvt_pk_bf16_f32 v202, v20, v21\n" \
"v_mfma_f32_16x16x32_bf16 v[68:71], a[204:207], a[248:251], v[68:71]\n" \
"ds_read_b64_tr_b16 a[194:195], v240 offset:52224\n" \
"ds_read_b64_tr_b16 a[196:197], v240 offset:58368\n" \
"v_mfma_f32_16x16x32_bf16 v[100:103], a[204:207], a[252:255], v[100:103]\n" \
"ds_read_b64_tr_b16 a[198:199], v240 offset:60416\n" \
"v_cvt_pk_bf16_f32 v203, v22, v23\n" \
"v_mfma_f32_16x16x32_bf16 v[100:103], a[200:203], a[236:239], v[100:103]\n" \
"s_waitcnt lgkmcnt(8)\n" \
"v_cvt_pk_bf16_f32 v216, v48, v49\n" \
"v_cvt_pk_bf16_f32 v217, v50, v51\n" \
"v_mfma_f32_16x16x32_bf16 v[72:75], a[208:211], a[232:235], v[72:75]\n" \
"ds_read_b64_tr_b16 a[200:201], v241 offset:50176\n" \
"v_cvt_pk_bf16_f32 v218, v52, v53\n" \
"v_mfma_f32_16x16x32_bf16 v[72:75], a[212:215], a[248:251], v[72:75]\n" \
"ds_read_b64_tr_b16 a[202:203], v241 offset:52224\n" \
"ds_read_b64_tr_b16 a[204:205], v241 offset:58368\n" \
"v_mfma_f32_16x16x32_bf16 v[104:107], a[212:215], a[252:255], v[104:107]\n" \
"ds_read_b64_tr_b16 a[206:207], v241 offset:60416\n" \
"v_cvt_pk_bf16_f32 v219, v54, v55\n" \
"v_mfma_f32_16x16x32_bf16 v[104:107], a[208:211], a[236:239], v[104:107]\n" \
"s_waitcnt lgkmcnt(8)\n" \
"v_cvt_pk_bf16_f32 v204, v24, v25\n" \
"v_cvt_pk_bf16_f32 v205, v26, v27\n" \
"v_mfma_f32_16x16x32_bf16 v[76:79], a[216:219], a[232:235], v[76:79]\n" \
"ds_read_b64_tr_b16 a[208:209], v240 offset:50688\n" \
"v_cvt_pk_bf16_f32 v206, v28, v29\n" \
"v_mfma_f32_16x16x32_bf16 v[76:79], a[220:223], a[248:251], v[76:79]\n" \
"ds_read_b64_tr_b16 a[210:211], v240 offset:52736\n" \
"ds_read_b64_tr_b16 a[212:213], v240 offset:58880\n" \
"v_mfma_f32_16x16x32_bf16 v[108:111], a[220:223], a[252:255], v[108:111]\n" \
"ds_read_b64_tr_b16 a[214:215], v240 offset:60928\n" \
"v_cvt_pk_bf16_f32 v207, v30, v31\n" \
"v_mfma_f32_16x16x32_bf16 v[108:111], a[216:219], a[236:239], v[108:111]\n" \
"s_waitcnt lgkmcnt(8)\n" \
"v_cvt_pk_bf16_f32 v220, v56, v57\n" \
"v_cvt_pk_bf16_f32 v221, v58, v59\n" \
"v_mfma_f32_16x16x32_bf16 v[80:83], a[192:195], a[232:235], v[80:83]\n" \
"ds_read_b64_tr_b16 a[216:217], v241 offset:50688\n" \
"v_cvt_pk_bf16_f32 v222, v60, v61\n" \
"v_mfma_f32_16x16x32_bf16 v[80:83], a[196:199], a[248:251], v[80:83]\n" \
"ds_read_b64_tr_b16 a[218:219], v241 offset:52736\n" \
"ds_read_b64_tr_b16 a[220:221], v241 offset:58880\n" \
"v_mfma_f32_16x16x32_bf16 v[112:115], a[196:199], a[252:255], v[112:115]\n" \
"ds_read_b64_tr_b16 a[222:223], v241 offset:60928\n" \
"v_cvt_pk_bf16_f32 v223, v62, v63\n" \
"v_mfma_f32_16x16x32_bf16 v[112:115], a[192:195], a[236:239], v[112:115]\n" \
"s_waitcnt lgkmcnt(8)\n" \
"v_cvt_pk_bf16_f32 v160, v64, v65\n" \
"v_cvt_pk_bf16_f32 v161, v66, v67\n" \
"v_mfma_f32_16x16x32_bf16 v[84:87], a[200:203], a[232:235], v[84:87]\n" \
"s_cmp_lg_u32 s84, 0\n" \
"s_cmov_b32 s21, s22\n" \
"s_cmov_b32 s91, 0x8000\n" \
"v_mfma_f32_16x16x32_bf16 v[84:87], a[204:207], a[248:251], v[84:87]\n" \
"s_lshl_b32 s85, s68, 4\n" \
"v_cvt_pk_bf16_f32 v162, v68, v69\n" \
"v_cvt_pk_bf16_f32 v163, v70, v71\n" \
"v_mfma_f32_16x16x32_bf16 v[116:119], a[204:207], a[252:255], v[116:119]\n" \
"s_add_u32 s85, s85, s67\n" \
"v_cvt_pk_bf16_f32 v164, v72, v73\n" \
"v_cvt_pk_bf16_f32 v165, v74, v75\n" \
"v_mfma_f32_16x16x32_bf16 v[116:119], a[200:203], a[236:239], v[116:119]\n" \
"s_waitcnt lgkmcnt(4)\n" \
"v_cvt_pk_bf16_f32 v166, v76, v77\n" \
"v_cvt_pk_bf16_f32 v167, v78, v79\n" \
"v_mfma_f32_16x16x32_bf16 v[88:91], a[208:211], a[232:235], v[88:91]\n" \
"s_lshl2_add_u32 s85, s85, %[wid]\n" \
"s_lshl_b32 s85, s85, 2\n" \
"v_mfma_f32_16x16x32_bf16 v[88:91], a[212:215], a[248:251], v[88:91]\n" \
"s_add_u32 s85, s85, 82048\n" \
"v_mfma_f32_16x16x32_bf16 v[120:123], a[212:215], a[252:255], v[120:123]\n" \
"s_waitcnt vmcnt(0)\n" \
"buffer_store_dword v250, off, s[32:35], s85 sc1\n" \
"v_cvt_pk_bf16_f32 v168, v80, v81\n" \
"v_mfma_f32_16x16x32_bf16 v[120:123], a[208:211], a[236:239], v[120:123]\n" \
"s_waitcnt lgkmcnt(0)\n" \
"v_cvt_pk_bf16_f32 v169, v82, v83\n" \
"v_cvt_pk_bf16_f32 v170, v84, v85\n" \
"v_mfma_f32_16x16x32_bf16 v[92:95], a[216:219], a[232:235], v[92:95]\n" \
"v_cvt_pk_bf16_f32 v171, v86, v87\n" \
"v_mfma_f32_16x16x32_bf16 v[92:95], a[220:223], a[248:251], v[92:95]\n" \
"v_mfma_f32_16x16x32_bf16 v[124:127], a[220:223], a[252:255], v[124:127]\n" \
"v_mfma_f32_16x16x32_bf16 v[124:127], a[216:219], a[236:239], v[124:127]\n" \
  \
".Lat_b_%=:\n" \
"s_cmp_ge_u32 %[wid], 2\n" \
"s_cbranch_scc1 .Ldhi_%=\n" \
"v_mfma_f32_16x16x32_bf16 v[128:131], a[128:131], v[192:195], v[224:227]\n" \
"v_mfma_f32_16x16x32_bf16 v[128:131], a[132:135], v[196:199], v[128:131]\n" \
"s_barrier\n" \
"v_mfma_f32_16x16x32_bf16 v[128:131], a[136:139], v[200:203], v[128:131]\n" \
"ds_read_b128 a[192:195], v242 offset:0\n" \
"v_cvt_pk_bf16_f32 v172, v88, v89\n" \
"v_mfma_f32_16x16x32_bf16 v[128:131], a[140:143], v[204:207], v[128:131]\n" \
"ds_read_b128 a[196:199], v242 offset:1024\n" \
"v_cvt_pk_bf16_f32 v173, v90, v91\n" \
"v_mfma_f32_16x16x32_bf16 v[132:135], a[156:159], v[204:207], v[228:231]\n" \
"ds_read_b128 a[200:203], v242 offset:2048\n" \
"v_cvt_pk_bf16_f32 v174, v92, v93\n" \
"v_mfma_f32_16x16x32_bf16 v[132:135], a[152:155], v[200:203], v[132:135]\n" \
"ds_read_b128 a[204:207], v242 offset:3072\n" \
"v_cvt_pk_bf16_f32 v175, v94, v95\n" \
"v_mfma_f32_16x16x32_bf16 v[132:135], a[148:151], v[196:199], v[132:135]\n" \
"v_exp_f32 v128, v128\n" \
"v_mfma_f32_16x16x32_bf16 v[132:135], a[144:147], v[192:195], v[132:135]\n" \
"s_add_u32 s85, s94, 589824\n" \
"ds_read_b128 a[208:211], v242 offset:4096\n" \
"ds_read_b128 a[212:215], v242 offset:5120\n" \
"v_mfma_f32_16x16x32_bf16 v[144:147], a[128:131], v[208:211], 0\n" \
"v_exp_f32 v129, v129\n" \
"v_mfma_f32_16x16x32_bf16 v[144:147], a[132:135], v[212:215], v[144:147]\n" \
"buffer_store_dwordx4 a[64:67], v248, s[32:35], s85 offen offset:0 sc1\n" \
"v_mfma_f32_16x16x32_bf16 v[144:147], a[136:139], v[216:219], v[144:147]\n" \
"v_exp_f32 v130, v130\n" \
"v_mfma_f32_16x16x32_bf16 v[144:147], a[140:143], v[220:223], v[144:147]\n" \
"ds_read_b128 a[216:219], v242 offset:6144\n" \
"v_cvt_pk_bf16_f32 v128, v128, v129\n" \
"v_mfma_f32_16x16x32_bf16 v[148:151], a[156:159], v[220:223], 0\n" \
"v_exp_f32 v131, v131\n" \
"v_mfma_f32_16x16x32_bf16 v[148:151], a[152:155], v[216:219], v[148:151]\n" \
"v_exp_f32 v132, v132\n" \
"v_mfma_f32_16x16x32_bf16 v[148:151], a[148:151], v[212:215], v[148:151]\n" \
"ds_read_b128 a[220:223], v242 offset:7168\n" \
"v_cvt_pk_bf16_f32 v129, v130, v131\n" \
"v_mfma_f32_16x16x32_bf16 v[148:151], a[144:147], v[208:211], v[148:151]\n" \
"v_exp_f32 v133, v133\n" \
"v_mfma_f32_16x16x32_bf16 v[152:155], a[160:163], v[208:211], 0\n" \
"ds_read_b128 a[224:227], v242 offset:8192\n" \
"v_cvt_pk_bf16_f32 v130, v132, v133\n" \
"v_mfma_f32_16x16x32_bf16 v[152:155], a[164:167], v[212:215], v[152:155]\n" \
"v_exp_f32 v134, v134\n" \
"v_mfma_f32_16x16x32_bf16 v[152:155], a[168:171], v[216:219], v[152:155]\n" \
"ds_read_b128 a[228:231], v242 offset:9216\n" \
"ds_read_b128 a[232:235], v242 offset:10240\n" \
"v_mfma_f32_16x16x32_bf16 v[152:155], a[172:175], v[220:223], v[152:155]\n" \
"v_exp_f32 v135, v135\n" \
"v_mfma_f32_16x16x32_bf16 v[156:159], a[188:191], v[220:223], 0\n" \
"v_exp_f32 v144, v144\n" \
"v_mfma_f32_16x16x32_bf16 v[156:159], a[184:187], v[216:219], v[156:159]\n" \
"v_exp_f32 v145, v145\n" \
"v_mfma_f32_16x16x32_bf16 v[156:159], a[180:183], v[212:215], v[156:159]\n" \
"v_exp_f32 v146, v146\n" \
"v_cvt_pk_bf16_f32 v131, v134, v135\n" \
"v_mfma_f32_16x16x32_bf16 v[156:159], a[176:179], v[208:211], v[156:159]\n" \
"v_exp_f32 v147, v147\n" \
"v_mfma_f32_16x16x32_bf16 a[32:35], a[48:51], v[128:131], 0\n" \
"s_waitcnt lgkmcnt(7)\n" \
"v_exp_f32 v148, v148\n" \
"v_mfma_f32_16x16x32_bf16 v[0:3], a[192:195], v[128:131], 0\n" \
"s_add_u32 s85, s94, 589824\n" \
"v_exp_f32 v149, v149\n" \
"v_mfma_f32_16x16x32_bf16 v[4:7], a[196:199], v[128:131], 0\n" \
"v_exp_f32 v150, v150\n" \
"v_mfma_f32_16x16x32_bf16 v[8:11], a[200:203], v[128:131], 0\n" \
"buffer_store_dwordx4 a[72:75], v248, s[32:35], s85 offen offset:1024 sc1\n" \
"v_mfma_f32_16x16x32_bf16 v[12:15], a[204:207], v[128:131], 0\n" \
"s_waitcnt lgkmcnt(3)\n" \
"v_exp_f32 v151, v151\n" \
"v_mfma_f32_16x16x32_bf16 v[16:19], a[208:211], v[128:131], 0\n" \
"v_cvt_pk_bf16_f32 v144, v144, v145\n" \
"v_cvt_pk_bf16_f32 v145, v146, v147\n" \
"v_mfma_f32_16x16x32_bf16 v[20:23], a[212:215], v[128:131], 0\n" \
"v_exp_f32 v152, v152\n" \
"v_mfma_f32_16x16x32_bf16 v[24:27], a[216:219], v[128:131], 0\n" \
"v_cvt_pk_bf16_f32 v146, v148, v149\n" \
"v_cvt_pk_bf16_f32 v147, v150, v151\n" \
"v_mfma_f32_16x16x32_bf16 v[28:31], a[220:223], v[128:131], 0\n" \
"v_exp_f32 v153, v153\n" \
"v_mfma_f32_16x16x32_bf16 v[128:131], a[128:131], v[160:163], 0\n" \
"ds_read_b128 a[236:239], v242 offset:11264\n" \
"v_cvt_pk_bf16_f32 v152, v152, v153\n" \
"v_mfma_f32_16x16x32_bf16 v[128:131], a[132:135], v[164:167], v[128:131]\n" \
"v_exp_f32 v154, v154\n" \
"v_mfma_f32_16x16x32_bf16 v[128:131], a[136:139], v[168:171], v[128:131]\n" \
"ds_read_b128 a[240:243], v242 offset:12288\n" \
"ds_read_b128 a[244:247], v242 offset:13312\n" \
"v_mfma_f32_16x16x32_bf16 v[128:131], a[140:143], v[172:175], v[128:131]\n" \
"v_exp_f32 v155, v155\n" \
"v_mfma_f32_16x16x32_bf16 v[132:135], a[156:159], v[172:175], 0\n" \
"ds_read_b128 a[248:251], v242 offset:14336\n" \
"v_cvt_pk_bf16_f32 v153, v154, v155\n" \
"v_mfma_f32_16x16x32_bf16 v[132:135], a[152:155], v[168:171], v[132:135]\n" \
"v_exp_f32 v156, v156\n" \
"v_mfma_f32_16x16x32_bf16 v[132:135], a[148:151], v[164:167], v[132:135]\n" \
"ds_read_b128 a[252:255], v242 offset:15360\n" \
"v_cvt_pk_bf16_f32 v176, v96, v97\n" \
"v_mfma_f32_16x16x32_bf16 v[132:135], a[144:147], v[160:163], v[132:135]\n" \
"v_exp_f32 v157, v157\n" \
"v_mfma_f32_16x16x32_bf16 v[136:139], a[160:163], v[160:163], 0\n" \
"v_cvt_pk_bf16_f32 v154, v156, v157\n" \
"v_cvt_pk_bf16_f32 v177, v98, v99\n" \
"v_mfma_f32_16x16x32_bf16 v[136:139], a[164:167], v[164:167], v[136:139]\n" \
"v_exp_f32 v158, v158\n" \
"v_mfma_f32_16x16x32_bf16 v[136:139], a[168:171], v[168:171], v[136:139]\n" \
"s_add_u32 s85, s94, 589824\n" \
"v_cvt_pk_bf16_f32 v178, v100, v101\n" \
"v_cvt_pk_bf16_f32 v179, v102, v103\n" \
"v_mfma_f32_16x16x32_bf16 v[136:139], a[172:175], v[172:175], v[136:139]\n" \
"v_exp_f32 v159, v159\n" \
"v_mfma_f32_16x16x32_bf16 v[140:143], a[188:191], v[172:175], 0\n" \
"buffer_store_dwordx4 a[80:83], v248, s[32:35], s85 offen offset:2048 sc1\n" \
"v_mfma_f32_16x16x32_bf16 v[140:143], a[184:187], v[168:171], v[140:143]\n" \
"v_cvt_pk_bf16_f32 v155, v158, v159\n" \
"v_cvt_pk_bf16_f32 v180, v104, v105\n" \
"v_mfma_f32_16x16x32_bf16 v[140:143], a[180:183], v[164:167], v[140:143]\n" \
"v_exp_f32 v128, v128\n" \
"v_mfma_f32_16x16x32_bf16 v[140:143], a[176:179], v[160:163], v[140:143]\n" \
"v_cvt_pk_bf16_f32 v181, v106, v107\n" \
"v_cvt_pk_bf16_f32 v182, v108, v109\n" \
"v_mfma_f32_16x16x32_bf16 a[36:39], a[48:51], v[144:147], 0\n" \
"v_exp_f32 v129, v129\n" \
"v_mfma_f32_16x16x32_bf16 a[36:39], a[48:51], v[152:155], a[36:39]\n" \
"s_waitcnt lgkmcnt(4)\n" \
"ds_write_b128 v249, a[64:67] offset:32768\n" \
"v_cvt_pk_bf16_f32 v128, v128, v129\n" \
"v_mfma_f32_16x16x32_bf16 v[32:35], a[224:227], v[152:155], 0\n" \
"v_exp_f32 v130, v130\n" \
"v_mfma_f32_16x16x32_bf16 v[32:35], a[192:195], v[144:147], v[32:35]\n" \
"v_cvt_pk_bf16_f32 v183, v110, v111\n" \
"v_cvt_pk_bf16_f32 v184, v112, v113\n" \
"v_mfma_f32_16x16x32_bf16 v[36:39], a[196:199], v[144:147], 0\n" \
"v_exp_f32 v131, v131\n" \
"v_mfma_f32_16x16x32_bf16 v[36:39], a[228:231], v[152:155], v[36:39]\n" \
"v_cvt_pk_bf16_f32 v129, v130, v131\n" \
"v_cvt_pk_bf16_f32 v185, v114, v115\n" \
"v_mfma_f32_16x16x32_bf16 v[40:43], a[232:235], v[152:155], 0\n" \
"v_exp_f32 v132, v132\n" \
"v_mfma_f32_16x16x32_bf16 v[40:43], a[200:203], v[144:147], v[40:43]\n" \
"v_cvt_pk_bf16_f32 v186, v116, v117\n" \
"v_cvt_pk_bf16_f32 v187, v118, v119\n" \
"v_mfma_f32_16x16x32_bf16 v[44:47], a[204:207], v[144:147], 0\n" \
"v_exp_f32 v133, v133\n" \
"v_mfma_f32_16x16x32_bf16 v[44:47], a[236:239], v[152:155], v[44:47]\n" \
"s_waitcnt lgkmcnt(1)\n" \
"v_cvt_pk_bf16_f32 v130, v132, v133\n" \
"v_cvt_pk_bf16_f32 v188, v120, v121\n" \
"v_mfma_f32_16x16x32_bf16 v[48:51], a[240:243], v[152:155], 0\n" \
"v_exp_f32 v134, v134\n" \
"v_mfma_f32_16x16x32_bf16 v[48:51], a[208:211], v[144:147], v[48:51]\n" \
"v_cvt_pk_bf16_f32 v189, v122, v123\n" \
"v_cvt_pk_bf16_f32 v190, v124, v125\n" \
"v_mfma_f32_16x16x32_bf16 v[52:55], a[212:215], v[144:147], 0\n" \
"s_add_u32 s85, s94, 589824\n" \
"v_exp_f32 v135, v135\n" \
"v_mfma_f32_16x16x32_bf16 v[52:55], a[244:247], v[152:155], v[52:55]\n" \
"buffer_store_dwordx4 a[88:91], v248, s[32:35], s85 offen offset:3072 sc1\n" \
"v_mfma_f32_16x16x32_bf16 v[56:59], a[248:251], v[152:155], 0\n" \
"ds_write_b128 v249, a[72:75] offset:33792\n" \
"v_cvt_pk_bf16_f32 v131, v134, v135\n" \
"v_mfma_f32_16x16x32_bf16 v[56:59], a[216:219], v[144:147], v[56:59]\n" \
"v_exp_f32 v136, v136\n" \
"v_mfma_f32_16x16x32_bf16 v[60:63], a[220:223], v[144:147], 0\n" \
"v_cvt_pk_bf16_f32 v191, v126, v127\n" \
"v_mfma_f32_16x16x32_bf16 v[60:63], a[252:255], v[152:155], v[60:63]\n" \
"v_exp_f32 v137, v137\n" \
"v_mfma_f32_16x16x32_bf16 v[144:147], a[128:131], v[176:179], 0\n" \
"v_cvt_pk_bf16_f32 v136, v136, v137\n" \
"v_mfma_f32_16x16x32_bf16 v[144:147], a[132:135], v[180:183], v[144:147]\n" \
"v_exp_f32 v138, v138\n" \
"v_mfma_f32_16x16x32_bf16 v[144:147], a[136:139], v[184:187], v[144:147]\n" \
"ds_read_b128 a[128:131], v248 offset:16384\n" \
"ds_read_b128 a[132:135], v248 offset:17408\n" \
"v_mfma_f32_16x16x32_bf16 v[144:147], a[140:143], v[188:191], v[144:147]\n" \
"v_exp_f32 v139, v139\n" \
"v_mfma_f32_16x16x32_bf16 v[148:151], a[156:159], v[188:191], 0\n" \
"ds_read_b128 a[136:139], v248 offset:18432\n" \
"v_cvt_pk_bf16_f32 v137, v138, v139\n" \
"v_mfma_f32_16x16x32_bf16 v[148:151], a[152:155], v[184:187], v[148:151]\n" \
"v_exp_f32 v140, v140\n" \
"v_mfma_f32_16x16x32_bf16 v[148:151], a[148:151], v[180:183], v[148:151]\n" \
"ds_read_b128 a[140:143], v248 offset:19456\n" \
"v_mfma_f32_16x16x32_bf16 v[148:151], a[144:147], v[176:179], v[148:151]\n" \
"v_exp_f32 v141, v141\n" \
"v_mfma_f32_16x16x32_bf16 v[152:155], a[160:163], v[176:179], 0\n" \
"ds_read_b128 a[156:159], v248 offset:23552\n" \
"v_cvt_pk_bf16_f32 v138, v140, v141\n" \
"v_mfma_f32_16x16x32_bf16 v[152:155], a[164:167], v[180:183], v[152:155]\n" \
"v_exp_f32 v142, v142\n" \
"v_mfma_f32_16x16x32_bf16 v[152:155], a[168:171], v[184:187], v[152:155]\n" \
"ds_write_b128 v249, a[80:83] offset:34816\n" \
"v_mfma_f32_16x16x32_bf16 v[152:155], a[172:175], v[188:191], v[152:155]\n" \
"v_exp_f32 v143, v143\n" \
"v_mfma_f32_16x16x32_bf16 v[156:159], a[188:191], v[188:191], 0\n" \
"s_add_u32 s85, s94, 593920\n" \
"ds_read_b128 a[152:155], v248 offset:22528\n" \
"v_cvt_pk_bf16_f32 v139, v142, v143\n" \
"v_mfma_f32_16x16x32_bf16 v[156:159], a[184:187], v[184:187], v[156:159]\n" \
"buffer_store_dwordx4 a[96:99], v248, s[32:35], s85 offen offset:0 sc1\n" \
"v_mfma_f32_16x16x32_bf16 v[156:159], a[180:183], v[180:183], v[156:159]\n" \
"v_exp_f32 v144, v144\n" \
"v_mfma_f32_16x16x32_bf16 v[156:159], a[176:179], v[176:179], v[156:159]\n" \
"ds_read_b128 a[148:151], v248 offset:21504\n" \
"ds_read_b128 a[144:147], v248 offset:20480\n" \
"v_mfma_f32_16x16x32_bf16 a[40:43], a[48:51], v[128:131], 0\n" \
"v_exp_f32 v145, v145\n" \
"v_mfma_f32_16x16x32_bf16 a[40:43], a[48:51], v[136:139], a[40:43]\n" \
"ds_read_b128 a[160:163], v248 offset:24576\n" \
"v_cvt_pk_bf16_f32 v144, v144, v145\n" \
"v_mfma_f32_16x16x32_bf16 v[64:67], a[224:227], v[136:139], 0\n" \
"v_exp_f32 v146, v146\n" \
"v_mfma_f32_16x16x32_bf16 v[64:67], a[192:195], v[128:131], v[64:67]\n" \
"ds_read_b128 a[164:167], v248 offset:25600\n" \
"ds_read_b128 a[168:171], v248 offset:26624\n" \
"v_mfma_f32_16x16x32_bf16 v[68:71], a[196:199], v[128:131], 0\n" \
"v_exp_f32 v147, v147\n" \
"v_mfma_f32_16x16x32_bf16 v[68:71], a[228:231], v[136:139], v[68:71]\n" \
"ds_read_b128 a[172:175], v248 offset:27648\n" \
"v_cvt_pk_bf16_f32 v145, v146, v147\n" \
"v_mfma_f32_16x16x32_bf16 v[72:75], a[232:235], v[136:139], 0\n" \
"v_exp_f32 v148, v148\n" \
"v_mfma_f32_16x16x32_bf16 v[72:75], a[200:203], v[128:131], v[72:75]\n" \
"ds_write_b128 v249, a[88:91] offset:35840\n" \
"v_mfma_f32_16x16x32_bf16 v[76:79], a[204:207], v[128:131], 0\n" \
"v_exp_f32 v149, v149\n" \
"v_mfma_f32_16x16x32_bf16 v[76:79], a[236:239], v[136:139], v[76:79]\n" \
"ds_read_b128 a[188:191], v248 offset:31744\n" \
"v_cvt_pk_bf16_f32 v146, v148, v149\n" \
"v_mfma_f32_16x16x32_bf16 v[80:83], a[240:243], v[136:139], 0\n" \
"v_exp_f32 v150, v150\n" \
"v_mfma_f32_16x16x32_bf16 v[80:83], a[208:211], v[128:131], v[80:83]\n" \
"ds_read_b128 a[184:187], v248 offset:30720\n" \
"ds_read_b128 a[180:183], v248 offset:29696\n" \
"v_mfma_f32_16x16x32_bf16 v[84:87], a[212:215], v[128:131], 0\n" \
"v_exp_f32 v151, v151\n" \
"v_mfma_f32_16x16x32_bf16 v[84:87], a[244:247], v[136:139], v[84:87]\n" \
"ds_read_b128 a[176:179], v248 offset:28672\n" \
"v_cvt_pk_bf16_f32 v147, v150, v151\n" \
"v_mfma_f32_16x16x32_bf16 v[88:91], a[248:251], v[136:139], 0\n" \
"s_add_u32 s85, s94, 593920\n" \
"v_mfma_f32_16x16x32_bf16 v[88:91], a[216:219], v[128:131], v[88:91]\n" \
"v_exp_f32 v152, v152\n" \
"v_mfma_f32_16x16x32_bf16 v[92:95], a[220:223], v[128:131], 0\n" \
"buffer_store_dwordx4 a[104:107], v248, s[32:35], s85 offen offset:1024 sc1\n" \
"v_mfma_f32_16x16x32_bf16 v[92:95], a[252:255], v[136:139], v[92:95]\n" \
"s_waitcnt lgkmcnt(14)\n" \
"v_exp_f32 v153, v153\n" \
"v_mfma_f32_16x16x32_bf16 v[128:131], a[128:131], v[208:211], 0\n" \
"v_mov_b32 v240, v248\n" \
"v_cvt_pk_bf16_f32 v152, v152, v153\n" \
"v_mfma_f32_16x16x32_bf16 v[128:131], a[132:135], v[212:215], v[128:131]\n" \
"v_exp_f32 v154, v154\n" \
"v_mfma_f32_16x16x32_bf16 v[128:131], a[136:139], v[216:219], v[128:131]\n" \
"ds_write_b128 v249, a[96:99] offset:36864\n" \
"v_mfma_f32_16x16x32_bf16 v[128:131], a[140:143], v[220:223], v[128:131]\n" \
"s_waitcnt lgkmcnt(10)\n" \
"v_exp_f32 v155, v155\n" \
"v_mfma_f32_16x16x32_bf16 v[132:135], a[156:159], v[220:223], 0\n" \
"buffer_load_dword v253, v252, s[32:35], s81 offen sc1\n" \
"v_cvt_pk_bf16_f32 v153, v154, v155\n" \
"v_mfma_f32_16x16x32_bf16 v[132:135], a[152:155], v[216:219], v[132:135]\n" \
"v_exp_f32 v156, v156\n" \
"v_mfma_f32_16x16x32_bf16 v[132:135], a[148:151], v[212:215], v[132:135]\n" \
"v_mfma_f32_16x16x32_bf16 v[132:135], a[144:147], v[208:211], v[132:135]\n" \
"s_waitcnt lgkmcnt(6)\n" \
"v_exp_f32 v157, v157\n" \
"v_mfma_f32_16x16x32_bf16 v[136:139], a[160:163], v[208:211], v[232:235]\n" \
"v_cvt_pk_bf16_f32 v154, v156, v157\n" \
"v_mfma_f32_16x16x32_bf16 v[136:139], a[164:167], v[212:215], v[136:139]\n" \
"v_exp_f32 v158, v158\n" \
"v_mfma_f32_16x16x32_bf16 v[136:139], a[168:171], v[216:219], v[136:139]\n" \
"v_mfma_f32_16x16x32_bf16 v[136:139], a[172:175], v[220:223], v[136:139]\n" \
"s_waitcnt lgkmcnt(1)\n" \
"v_exp_f32 v159, v159\n" \
"v_mfma_f32_16x16x32_bf16 v[140:143], a[188:191], v[220:223], v[236:239]\n" \
"v_cvt_pk_bf16_f32 v155, v158, v159\n" \
"v_mfma_f32_16x16x32_bf16 v[140:143], a[184:187], v[216:219], v[140:143]\n" \
"v_mfma_f32_16x16x32_bf16 v[140:143], a[180:183], v[212:215], v[140:143]\n" \
"s_add_u32 s85, s94, 593920\n" \
"v_exp_f32 v128, v128\n" \
"v_mfma_f32_16x16x32_bf16 v[140:143], a[176:179], v[208:211], v[140:143]\n" \
"buffer_store_dwordx4 a[112:115], v248, s[32:35], s85 offen offset:2048 sc1\n" \
"v_mfma_f32_16x16x32_bf16 a[44:47], a[48:51], v[144:147], 0\n" \
"v_exp_f32 v129, v129\n" \
"v_mfma_f32_16x16x32_bf16 a[44:47], a[48:51], v[152:155], a[44:47]\n" \
"ds_write_b128 v249, a[104:107] offset:37888\n" \
"v_cvt_pk_bf16_f32 v128, v128, v129\n" \
"v_mfma_f32_16x16x32_bf16 v[96:99], a[224:227], v[152:155], 0\n" \
"v_exp_f32 v130, v130\n" \
"v_mfma_f32_16x16x32_bf16 v[96:99], a[192:195], v[144:147], v[96:99]\n" \
"ds_read_b128 a[224:227], v242 offset:24576\n" \
"v_mfma_f32_16x16x32_bf16 v[100:103], a[196:199], v[144:147], 0\n" \
"v_exp_f32 v131, v131\n" \
"v_mfma_f32_16x16x32_bf16 v[100:103], a[228:231], v[152:155], v[100:103]\n" \
"ds_read_b128 a[192:195], v242 offset:16384\n" \
"v_cvt_pk_bf16_f32 v129, v130, v131\n" \
"v_mfma_f32_16x16x32_bf16 v[104:107], a[232:235], v[152:155], 0\n" \
"v_exp_f32 v132, v132\n" \
"v_mfma_f32_16x16x32_bf16 v[104:107], a[200:203], v[144:147], v[104:107]\n" \
"ds_read_b128 a[196:199], v242 offset:17408\n" \
"ds_read_b128 a[228:231], v242 offset:25600\n" \
"v_mfma_f32_16x16x32_bf16 v[108:111], a[204:207], v[144:147], 0\n" \
"v_exp_f32 v133, v133\n" \
"v_mfma_f32_16x16x32_bf16 v[108:111], a[236:239], v[152:155], v[108:111]\n" \
"ds_read_b128 a[232:235], v242 offset:26624\n" \
"v_cvt_pk_bf16_f32 v130, v132, v133\n" \
"v_mfma_f32_16x16x32_bf16 v[112:115], a[240:243], v[152:155], 0\n" \
"v_exp_f32 v134, v134\n" \
"v_mfma_f32_16x16x32_bf16 v[112:115], a[208:211], v[144:147], v[112:115]\n" \
"ds_write_b128 v249, a[112:115] offset:38912\n" \
"v_mfma_f32_16x16x32_bf16 v[116:119], a[212:215], v[144:147], 0\n" \
"v_exp_f32 v135, v135\n" \
"v_mfma_f32_16x16x32_bf16 v[116:119], a[244:247], v[152:155], v[116:119]\n" \
"ds_read_b128 a[200:203], v242 offset:18432\n" \
"v_cvt_pk_bf16_f32 v131, v134, v135\n" \
"v_mfma_f32_16x16x32_bf16 v[120:123], a[248:251], v[152:155], 0\n" \
"ds_read_b128 a[204:207], v242 offset:19456\n" \
"ds_read_b128 a[236:239], v242 offset:27648\n" \
"v_mfma_f32_16x16x32_bf16 v[120:123], a[216:219], v[144:147], v[120:123]\n" \
"v_exp_f32 v136, v136\n" \
"v_mfma_f32_16x16x32_bf16 v[124:127], a[220:223], v[144:147], 0\n" \
"s_add_u32 s85, s94, 593920\n" \
"ds_read_b128 a[240:243], v242 offset:28672\n" \
"v_mfma_f32_16x16x32_bf16 v[124:127], a[252:255], v[152:155], v[124:127]\n" \
"v_exp_f32 v137, v137\n" \
"v_mfma_f32_16x16x32_bf16 v[144:147], a[128:131], v[160:163], 0\n" \
"buffer_store_dwordx4 a[120:123], v248, s[32:35], s85 offen offset:3072 sc1\n" \
"v_mfma_f32_16x16x32_bf16 v[144:147], a[132:135], v[164:167], v[144:147]\n" \
"v_exp_f32 v138, v138\n" \
"v_mfma_f32_16x16x32_bf16 v[144:147], a[136:139], v[168:171], v[144:147]\n" \
"ds_read_b128 a[208:211], v242 offset:20480\n" \
"v_cvt_pk_bf16_f32 v136, v136, v137\n" \
"v_mfma_f32_16x16x32_bf16 v[144:147], a[140:143], v[172:175], v[144:147]\n" \
".Lst0_8_%=:\n" \
"s_cbranch_execz .Lst0_12_%=\n" \
"v_exp_f32 v139, v139\n" \
"v_mfma_f32_16x16x32_bf16 v[148:151], a[156:159], v[172:175], 0\n" \
"ds_read_b128 a[212:215], v242 offset:21504\n" \
"v_cvt_pk_bf16_f32 v137, v138, v139\n" \
"v_mfma_f32_16x16x32_bf16 v[148:151], a[152:155], v[168:171], v[148:151]\n" \
"v_exp_f32 v140, v140\n" \
"v_mfma_f32_16x16x32_bf16 v[148:151], a[148:151], v[164:167], v[148:151]\n" \
"ds_write_b128 v249, a[120:123] offset:39936\n" \
"v_mfma_f32_16x16x32_bf16 v[148:151], a[144:147], v[160:163], v[148:151]\n" \
"v_exp_f32 v141, v141\n" \
"v_mfma_f32_16x16x32_bf16 v[152:155], a[160:163], v[160:163], 0\n" \
"ds_read_b128 a[244:247], v242 offset:29696\n" \
"v_cvt_pk_bf16_f32 v138, v140, v141\n" \
"v_mfma_f32_16x16x32_bf16 v[152:155], a[164:167], v[164:167], v[152:155]\n" \
"v_exp_f32 v142, v142\n" \
"v_mfma_f32_16x16x32_bf16 v[152:155], a[168:171], v[168:171], v[152:155]\n" \
"ds_read_b128 a[248:251], v242 offset:30720\n" \
"ds_read_b128 a[216:219], v242 offset:22528\n" \
"v_mfma_f32_16x16x32_bf16 v[152:155], a[172:175], v[172:175], v[152:155]\n" \
"v_exp_f32 v143, v143\n" \
"v_mfma_f32_16x16x32_bf16 v[156:159], a[188:191], v[172:175], 0\n" \
"ds_read_b128 a[220:223], v242 offset:23552\n" \
"v_cvt_pk_bf16_f32 v139, v142, v143\n" \
"v_mfma_f32_16x16x32_bf16 v[156:159], a[184:187], v[168:171], v[156:159]\n" \
"ds_read_b128 a[252:255], v242 offset:31744\n" \
"v_mfma_f32_16x16x32_bf16 v[156:159], a[180:183], v[164:167], v[156:159]\n" \
"v_exp_f32 v144, v144\n" \
"v_mfma_f32_16x16x32_bf16 v[156:159], a[176:179], v[160:163], v[156:159]\n" \
"v_mfma_f32_16x16x32_bf16 a[36:39], a[48:51], v[128:131], a[36:39]\n" \
"v_exp_f32 v145, v145\n" \
"v_mfma_f32_16x16x32_bf16 a[36:39], a[48:51], v[136:139], a[36:39]\n" \
"s_waitcnt lgkmcnt(14)\n" \
"v_cvt_pk_bf16_f32 v144, v144, v145\n" \
"v_mfma_f32_16x16x32_bf16 v[32:35], a[224:227], v[136:139], v[32:35]\n" \
"v_exp_f32 v146, v146\n" \
"v_mfma_f32_16x16x32_bf16 v[32:35], a[192:195], v[128:131], v[32:35]\n" \
"v_mfma_f32_16x16x32_bf16 v[36:39], a[196:199], v[128:131], v[36:39]\n" \
"v_exp_f32 v147, v147\n" \
"v_mfma_f32_16x16x32_bf16 v[36:39], a[228:231], v[136:139], v[36:39]\n" \
"s_waitcnt lgkmcnt(9)\n" \
"v_cvt_pk_bf16_f32 v145, v146, v147\n" \
"v_mfma_f32_16x16x32_bf16 v[40:43], a[232:235], v[136:139], v[40:43]\n" \
"v_exp_f32 v148, v148\n" \
"v_mfma_f32_16x16x32_bf16 v[40:43], a[200:203], v[128:131], v[40:43]\n" \
"v_mfma_f32_16x16x32_bf16 v[44:47], a[204:207], v[128:131], v[44:47]\n" \
"v_exp_f32 v149, v149\n" \
"v_mfma_f32_16x16x32_bf16 v[44:47], a[236:239], v[136:139], v[44:47]\n" \
"s_waitcnt lgkmcnt(4)\n" \
"v_cvt_pk_bf16_f32 v146, v148, v149\n" \
"v_mfma_f32_16x16x32_bf16 v[48:51], a[240:243], v[136:139], v[48:51]\n" \
"s_waitcnt vmcnt(2)\n" \
"v_cmp_eq_u32 vcc, 0, v253\n" \
"v_mfma_f32_16x16x32_bf16 v[48:51], a[208:211], v[128:131], v[48:51]\n" \
"v_exp_f32 v150, v150\n" \
"v_mfma_f32_16x16x32_bf16 v[52:55], a[212:215], v[128:131], v[52:55]\n" \
"v_exp_f32 v151, v151\n" \
"v_mfma_f32_16x16x32_bf16 v[52:55], a[244:247], v[136:139], v[52:55]\n" \
"s_waitcnt lgkmcnt(0)\n" \
"s_cbranch_vccnz .Lspv_%=\n" \
".Lsbv_%=:\n" \
"v_mfma_f32_16x16x32_bf16 v[56:59], a[248:251], v[136:139], v[56:59]\n" \
"s_mov_b32 m0, s19\n" \
"v_cmp_gt_u32 s[98:99], 32, v251\n" \
"v_cvt_pk_bf16_f32 v147, v150, v151\n" \
"v_mfma_f32_16x16x32_bf16 v[56:59], a[216:219], v[128:131], v[56:59]\n" \
"s_barrier\n" \
"v_exp_f32 v152, v152\n" \
"v_mfma_f32_16x16x32_bf16 v[60:63], a[220:223], v[128:131], v[60:63]\n" \
"buffer_load_dwordx4 v248, s[32:35], s88 offen offset:0 sc1 lds\n" \
"v_mfma_f32_16x16x32_bf16 v[60:63], a[252:255], v[136:139], v[60:63]\n" \
"v_exp_f32 v153, v153\n" \
"v_mfma_f32_16x16x32_bf16 v[128:131], a[128:131], v[176:179], 0\n" \
"v_cndmask_b32 v253, v253, 0, s[98:99]\n" \
"v_cvt_pk_bf16_f32 v152, v152, v153\n" \
"v_mfma_f32_16x16x32_bf16 v[128:131], a[132:135], v[180:183], v[128:131]\n" \
"v_exp_f32 v154, v154\n" \
"v_mfma_f32_16x16x32_bf16 v[128:131], a[136:139], v[184:187], v[128:131]\n" \
"buffer_load_dwordx4 v248, s[32:35], s88 offen offset:1024 sc1 lds\n" \
"v_mfma_f32_16x16x32_bf16 v[128:131], a[140:143], v[188:191], v[128:131]\n" \
"v_exp_f32 v155, v155\n" \
"v_mfma_f32_16x16x32_bf16 v[132:135], a[156:159], v[188:191], 0\n" \
"v_max_u32_dpp v253, v253, v253 quad_perm:[1,0,3,2] row_mask:0xf bank_mask:0xf\n" \
"v_cvt_pk_bf16_f32 v153, v154, v155\n" \
"v_mfma_f32_16x16x32_bf16 v[132:135], a[152:155], v[184:187], v[132:135]\n" \
"v_exp_f32 v156, v156\n" \
"v_mfma_f32_16x16x32_bf16 v[132:135], a[148:151], v[180:183], v[132:135]\n" \
"buffer_load_dwordx4 v248, s[32:35], s88 offen offset:2048 sc1 lds\n" \
"v_mfma_f32_16x16x32_bf16 v[132:135], a[144:147], v[176:179], v[132:135]\n" \
"v_exp_f32 v157, v157\n" \
"v_mfma_f32_16x16x32_bf16 v[136:139], a[160:163], v[176:179], 0\n" \
"v_max_u32_dpp v253, v253, v253 quad_perm:[2,3,0,1] row_mask:0xf bank_mask:0xf\n" \
"v_cvt_pk_bf16_f32 v154, v156, v157\n" \
"v_mfma_f32_16x16x32_bf16 v[136:139], a[164:167], v[180:183], v[136:139]\n" \
"v_exp_f32 v158, v158\n" \
"v_mfma_f32_16x16x32_bf16 v[136:139], a[168:171], v[184:187], v[136:139]\n" \
"buffer_load_dwordx4 v248, s[32:35], s88 offen offset:3072 sc1 lds\n" \
"v_mfma_f32_16x16x32_bf16 v[136:139], a[172:175], v[188:191], v[136:139]\n" \
"s_add_u32 s89, s88, 589824\n" \
"v_exp_f32 v159, v159\n" \
"v_mfma_f32_16x16x32_bf16 v[140:143], a[188:191], v[188:191], 0\n" \
"s_add_u32 s88, s88, s27\n" \
"v_max_u32_dpp v253, v253, v253 row_half_mirror row_mask:0xf bank_mask:0xf\n" \
"v_cvt_pk_bf16_f32 v155, v158, v159\n" \
"v_mfma_f32_16x16x32_bf16 v[140:143], a[184:187], v[184:187], v[140:143]\n" \
"s_add_u32 s19, s19, 0x4000\n" \
"ds_read_b128 a[128:131], v248 offset:32768\n" \
"ds_read_b128 a[132:135], v248 offset:33792\n" \
"v_mfma_f32_16x16x32_bf16 v[140:143], a[180:183], v[180:183], v[140:143]\n" \
"s_bitset0_b32 s19, 16\n" \
"v_exp_f32 v128, v128\n" \
"v_mfma_f32_16x16x32_bf16 v[140:143], a[176:179], v[176:179], v[140:143]\n" \
"s_cmp_eq_u32 s88, s16\n" \
"v_max_u32_dpp v253, v253, v253 row_mirror row_mask:0xf bank_mask:0xf\n" \
"v_mov_b32 v254, v253\n" \
"v_mfma_f32_16x16x32_bf16 a[40:43], a[48:51], v[144:147], a[40:43]\n" \
"s_cmov_b32 s88, s21\n" \
"v_exp_f32 v129, v129\n" \
"v_mfma_f32_16x16x32_bf16 a[40:43], a[48:51], v[152:155], a[40:43]\n" \
"s_cmov_b32 s27, s91\n" \
"v_permlane16_swap_b32 v253, v254\n" \
"v_mfma_f32_16x16x32_bf16 v[64:67], a[224:227], v[152:155], v[64:67]\n" \
"s_cmov_b32 s16, s77\n" \
"v_exp_f32 v130, v130\n" \
"v_mfma_f32_16x16x32_bf16 v[64:67], a[192:195], v[144:147], v[64:67]\n" \
"s_cmov_b32 s21, s92\n" \
"v_max_u32 v253, v253, v254\n" \
"v_mov_b32 v254, v253\n" \
"v_mfma_f32_16x16x32_bf16 v[68:71], a[196:199], v[144:147], v[68:71]\n" \
"s_cmovk_i32 s91, 0x4000\n" \
"v_exp_f32 v131, v131\n" \
"v_mfma_f32_16x16x32_bf16 v[68:71], a[228:231], v[152:155], v[68:71]\n" \
"s_cmp_eq_u32 s88, s17\n" \
"v_permlane32_swap_b32 v253, v254\n" \
"v_mfma_f32_16x16x32_bf16 v[72:75], a[232:235], v[152:155], v[72:75]\n" \
"s_cmov_b32 s88, s18\n" \
"v_exp_f32 v132, v132\n" \
"v_mfma_f32_16x16x32_bf16 v[72:75], a[200:203], v[144:147], v[72:75]\n" \
"s_mov_b32 m0, s20\n" \
"v_max_u32 v253, v253, v254\n" \
"v_max_f32 v254, 1.0, v253\n" \
"v_mfma_f32_16x16x32_bf16 v[76:79], a[204:207], v[144:147], v[76:79]\n" \
"v_exp_f32 v133, v133\n" \
"v_mfma_f32_16x16x32_bf16 v[76:79], a[236:239], v[152:155], v[76:79]\n" \
"buffer_load_dwordx4 v248, s[32:35], s89 offen offset:0 sc1 lds\n" \
"v_mfma_f32_16x16x32_bf16 v[80:83], a[240:243], v[152:155], v[80:83]\n" \
"v_exp_f32 v134, v134\n" \
"v_mfma_f32_16x16x32_bf16 v[80:83], a[208:211], v[144:147], v[80:83]\n" \
"buffer_load_dwordx4 v248, s[32:35], s89 offen offset:1024 sc1 lds\n" \
"ds_read_b128 a[136:139], v248 offset:34816\n" \
"ds_read_b128 a[140:143], v248 offset:35840\n" \
"v_mfma_f32_16x16x32_bf16 v[84:87], a[212:215], v[144:147], v[84:87]\n" \
"v_exp_f32 v135, v135\n" \
"v_mfma_f32_16x16x32_bf16 v[84:87], a[244:247], v[152:155], v[84:87]\n" \
"buffer_load_dwordx4 v248, s[32:35], s89 offen offset:2048 sc1 lds\n" \
"v_mfma_f32_16x16x32_bf16 v[88:91], a[248:251], v[152:155], v[88:91]\n" \
"buffer_load_dwordx4 v248, s[32:35], s89 offen offset:3072 sc1 lds\n" \
"v_mfma_f32_16x16x32_bf16 v[88:91], a[216:219], v[144:147], v[88:91]\n" \
"s_add_u32 s20, s20, 0x4000\n" \
"ds_read_b128 a[156:159], v248 offset:39936\n" \
"ds_read_b128 a[152:155], v248 offset:38912\n" \
"ds_read_b128 a[148:151], v248 offset:37888\n" \
"ds_read_b128 a[144:147], v248 offset:36864\n" \
"v_exp_f32 v136, v136\n" \
"v_mfma_f32_16x16x32_bf16 v[92:95], a[220:223], v[144:147], v[92:95]\n" \
"s_and_b32 s20, s20, 0xffff\n" \
"v_min_f32 v253, 1.0, v253\n" \
"v_cvt_pk_bf16_f32 v128, v128, v129\n" \
"v_mfma_f32_16x16x32_bf16 v[92:95], a[252:255], v[152:155], v[92:95]\n" \
"s_waitcnt lgkmcnt(4)\n" \
"v_exp_f32 v137, v137\n" \
"v_mfma_f32_16x16x32_bf16 v[144:147], a[128:131], v[176:179], 0\n" \
"s_bitset1_b32 s20, 16\n" \
"v_rcp_f32 v254, v254\n" \
"v_mfma_f32_16x16x32_bf16 v[144:147], a[132:135], v[180:183], v[144:147]\n" \
"s_mov_b32 m0, s19\n" \
"ds_read_b128 a[160:163], v248 offset:40960\n" \
"ds_read_b128 a[164:167], v248 offset:41984\n" \
"ds_read_b128 a[168:171], v248 offset:43008\n" \
"ds_read_b128 a[172:175], v248 offset:44032\n" \
"v_exp_f32 v138, v138\n" \
"v_mfma_f32_16x16x32_bf16 v[144:147], a[136:139], v[184:187], v[144:147]\n" \
"buffer_load_dwordx4 v248, s[32:35], s88 offen offset:0 sc1 lds\n" \
"v_mfma_f32_16x16x32_bf16 v[144:147], a[140:143], v[188:191], v[144:147]\n" \
"s_waitcnt lgkmcnt(4)\n" \
"v_exp_f32 v139, v139\n" \
"v_mfma_f32_16x16x32_bf16 v[148:151], a[156:159], v[188:191], 0\n" \
"buffer_load_dwordx4 v248, s[32:35], s88 offen offset:1024 sc1 lds\n" \
"v_mfma_f32_16x16x32_bf16 v[148:151], a[152:155], v[184:187], v[148:151]\n" \
"ds_read_b128 a[188:191], v248 offset:48128\n" \
"ds_read_b128 a[184:187], v248 offset:47104\n" \
"ds_read_b128 a[180:183], v248 offset:46080\n" \
"ds_read_b128 a[176:179], v248 offset:45056\n" \
"v_exp_f32 v140, v140\n" \
"v_mfma_f32_16x16x32_bf16 v[148:151], a[148:151], v[180:183], v[148:151]\n" \
"s_lshl_b32 s85, s68, 3\n" \
"buffer_load_dwordx4 v248, s[32:35], s88 offen offset:2048 sc1 lds\n" \
"v_mfma_f32_16x16x32_bf16 v[148:151], a[144:147], v[176:179], v[148:151]\n" \
"s_waitcnt lgkmcnt(4)\n" \
"v_exp_f32 v141, v141\n" \
"v_mfma_f32_16x16x32_bf16 v[152:155], a[160:163], v[176:179], v[232:235]\n" \
"s_add_u32 s85, s85, s67\n" \
"v_rcp_f32 v253, v253\n" \
"v_mfma_f32_16x16x32_bf16 v[152:155], a[164:167], v[180:183], v[152:155]\n" \
"s_lshl2_add_u32 s85, s85, %[wid]\n" \
"v_exp_f32 v142, v142\n" \
"v_mfma_f32_16x16x32_bf16 v[152:155], a[168:171], v[184:187], v[152:155]\n" \
"s_lshl_b32 s85, s85, 2\n" \
"buffer_load_dwordx4 v248, s[32:35], s88 offen offset:3072 sc1 lds\n" \
"v_mfma_f32_16x16x32_bf16 v[152:155], a[172:175], v[188:191], v[152:155]\n" \
"s_waitcnt lgkmcnt(0)\n" \
"v_exp_f32 v143, v143\n" \
"v_mfma_f32_16x16x32_bf16 v[156:159], a[188:191], v[188:191], v[236:239]\n" \
"s_add_u32 s89, s88, 589824\n" \
"v_exp_f32 v144, v144\n" \
"v_mfma_f32_16x16x32_bf16 v[156:159], a[184:187], v[184:187], v[156:159]\n" \
"s_add_u32 s88, s88, s27\n" \
"v_exp_f32 v145, v145\n" \
"v_mfma_f32_16x16x32_bf16 v[156:159], a[180:183], v[180:183], v[156:159]\n" \
"s_add_u32 s19, s19, 0x4000\n" \
"v_exp_f32 v146, v146\n" \
"v_cvt_pk_bf16_f32 v129, v130, v131\n" \
"v_cvt_pk_bf16_f32 v130, v132, v133\n" \
"v_cvt_pk_bf16_f32 v131, v134, v135\n" \
"v_mfma_f32_16x16x32_bf16 v[156:159], a[176:179], v[176:179], v[156:159]\n" \
"s_bitset0_b32 s19, 16\n" \
"v_exp_f32 v147, v147\n" \
"v_cvt_pk_bf16_f32 v136, v136, v137\n" \
"v_cvt_pk_bf16_f32 v137, v138, v139\n" \
"v_cvt_pk_bf16_f32 v138, v140, v141\n" \
"v_cvt_pk_bf16_f32 v139, v142, v143\n" \
"v_mfma_f32_16x16x32_bf16 a[44:47], a[48:51], v[128:131], a[44:47]\n" \
"s_barrier\n" \
"v_exp_f32 v148, v148\n" \
"v_mfma_f32_16x16x32_bf16 a[44:47], a[48:51], v[136:139], a[44:47]\n" \
"s_cmp_eq_u32 s88, s16\n" \
"v_exp_f32 v149, v149\n" \
"v_mfma_f32_16x16x32_bf16 v[96:99], a[224:227], v[136:139], v[96:99]\n" \
"s_cmov_b32 s88, s21\n" \
"v_exp_f32 v150, v150\n" \
"v_mfma_f32_16x16x32_bf16 v[96:99], a[192:195], v[128:131], v[96:99]\n" \
"s_cmov_b32 s27, s91\n" \
"v_mul_f32 v250, 0x7e000000, v254\n" \
"v_mul_f32 v246, 0x0d800000, v253\n" \
"v_mfma_f32_16x16x32_bf16 v[100:103], a[196:199], v[128:131], v[100:103]\n" \
"s_cmov_b32 s16, s77\n" \
"v_exp_f32 v151, v151\n" \
"v_mfma_f32_16x16x32_bf16 v[100:103], a[228:231], v[136:139], v[100:103]\n" \
"s_cmov_b32 s21, s92\n" \
"v_cvt_pk_bf16_f32 v144, v144, v145\n" \
"v_cvt_pk_bf16_f32 v145, v146, v147\n" \
"v_mfma_f32_16x16x32_bf16 v[104:107], a[232:235], v[136:139], v[104:107]\n" \
"s_cmovk_i32 s91, 0x4000\n" \
"v_cvt_pk_bf16_f32 v146, v148, v149\n" \
"v_cvt_pk_bf16_f32 v147, v150, v151\n" \
"v_mfma_f32_16x16x32_bf16 v[104:107], a[200:203], v[128:131], v[104:107]\n" \
"s_cmp_eq_u32 s88, s17\n" \
"v_exp_f32 v152, v152\n" \
"v_mfma_f32_16x16x32_bf16 v[108:111], a[204:207], v[128:131], v[108:111]\n" \
"s_cmov_b32 s88, s18\n" \
"ds_read_b128 a[128:131], v248 offset:49152\n" \
"ds_read_b128 a[132:135], v248 offset:50176\n" \
"v_mfma_f32_16x16x32_bf16 v[108:111], a[236:239], v[136:139], v[108:111]\n" \
"s_mov_b32 m0, s20\n" \
"v_exp_f32 v153, v153\n" \
"v_mfma_f32_16x16x32_bf16 v[112:115], a[240:243], v[136:139], v[112:115]\n" \
"s_add_u32 s85, s85, 655360\n" \
"buffer_load_dwordx4 v248, s[32:35], s89 offen offset:0 sc1 lds\n" \
"v_mfma_f32_16x16x32_bf16 v[112:115], a[208:211], v[128:131], v[112:115]\n" \
"ds_read_b128 a[136:139], v248 offset:51200\n" \
"ds_read_b128 a[140:143], v248 offset:52224\n" \
"v_exp_f32 v154, v154\n" \
"v_mfma_f32_16x16x32_bf16 v[116:119], a[212:215], v[128:131], v[116:119]\n" \
"buffer_load_dwordx4 v248, s[32:35], s89 offen offset:1024 sc1 lds\n" \
"v_mfma_f32_16x16x32_bf16 v[116:119], a[244:247], v[136:139], v[116:119]\n" \
"v_exp_f32 v155, v155\n" \
"v_mfma_f32_16x16x32_bf16 v[120:123], a[248:251], v[136:139], v[120:123]\n" \
"buffer_load_dwordx4 v248, s[32:35], s89 offen offset:2048 sc1 lds\n" \
"v_mfma_f32_16x16x32_bf16 v[120:123], a[216:219], v[128:131], v[120:123]\n" \
"ds_read_b128 a[156:159], v248 offset:56320\n" \
"ds_read_b128 a[152:155], v248 offset:55296\n" \
"ds_read_b128 a[148:151], v248 offset:54272\n" \
"ds_read_b128 a[144:147], v248 offset:53248\n" \
"v_exp_f32 v156, v156\n" \
"v_mfma_f32_16x16x32_bf16 v[124:127], a[220:223], v[128:131], v[124:127]\n" \
"s_waitcnt vmcnt(15)\n" \
"buffer_store_dword a56, off, s[32:35], s85 sc1\n" \
"v_cvt_pk_bf16_f32 v152, v152, v153\n" \
"v_mfma_f32_16x16x32_bf16 v[124:127], a[252:255], v[136:139], v[124:127]\n" \
"s_waitcnt lgkmcnt(4)\n" \
"v_exp_f32 v157, v157\n" \
"v_mfma_f32_16x16x32_bf16 v[128:131], a[128:131], v[160:163], v[224:227]\n" \
"buffer_load_dwordx4 v248, s[32:35], s89 offen offset:3072 sc1 lds\n" \
"v_mfma_f32_16x16x32_bf16 v[128:131], a[132:135], v[164:167], v[128:131]\n" \
"s_add_u32 s20, s20, 0x4000\n" \
"v_exp_f32 v158, v158\n" \
"v_mfma_f32_16x16x32_bf16 v[128:131], a[136:139], v[168:171], v[128:131]\n" \
"s_and_b32 s20, s20, 0xffff\n" \
"v_cvt_pk_bf16_f32 v153, v154, v155\n" \
"v_cvt_pk_bf16_f32 v154, v156, v157\n" \
"v_mfma_f32_16x16x32_bf16 v[128:131], a[140:143], v[172:175], v[128:131]\n" \
"s_waitcnt lgkmcnt(0)\n" \
"ds_read_b128 a[224:227], v242 offset:40960\n" \
"ds_read_b128 a[192:195], v242 offset:32768\n" \
"ds_read_b128 a[196:199], v242 offset:33792\n" \
"ds_read_b128 a[228:231], v242 offset:41984\n" \
"v_exp_f32 v159, v159\n" \
"v_mfma_f32_16x16x32_bf16 v[132:135], a[156:159], v[172:175], v[228:231]\n" \
"s_bitset1_b32 s20, 16\n" \
"ds_read_b128 a[232:235], v242 offset:43008\n" \
"v_cvt_pk_bf16_f32 v155, v158, v159\n" \
"v_mfma_f32_16x16x32_bf16 v[132:135], a[152:155], v[168:171], v[132:135]\n" \
"s_mov_b32 m0, s19\n" \
"ds_read_b128 a[200:203], v242 offset:34816\n" \
"ds_read_b128 a[204:207], v242 offset:35840\n" \
"v_mfma_f32_16x16x32_bf16 v[132:135], a[148:151], v[164:167], v[132:135]\n" \
"v_exp_f32 v128, v128\n" \
"v_mfma_f32_16x16x32_bf16 v[132:135], a[144:147], v[160:163], v[132:135]\n" \
"buffer_load_dwordx4 v248, s[32:35], s88 offen offset:0 sc1 lds\n" \
"ds_read_b128 a[236:239], v242 offset:44032\n" \
"v_mfma_f32_16x16x32_bf16 a[44:47], a[48:51], v[144:147], a[44:47]\n" \
"ds_read_b128 a[160:163], v248 offset:57344\n" \
"ds_read_b128 a[240:243], v242 offset:45056\n" \
"v_mfma_f32_16x16x32_bf16 a[44:47], a[48:51], v[152:155], a[44:47]\n" \
"s_waitcnt lgkmcnt(6)\n" \
"buffer_load_dwordx4 v248, s[32:35], s88 offen offset:1024 sc1 lds\n" \
"v_mfma_f32_16x16x32_bf16 v[96:99], a[224:227], v[152:155], v[96:99]\n" \
"v_exp_f32 v129, v129\n" \
"v_mfma_f32_16x16x32_bf16 v[96:99], a[192:195], v[144:147], v[96:99]\n" \
"ds_read_b128 a[164:167], v248 offset:58368\n" \
"ds_read_b128 a[208:211], v242 offset:36864\n" \
"ds_read_b128 a[168:171], v248 offset:59392\n" \
"ds_read_b128 a[172:175], v248 offset:60416\n" \
"ds_read_b128 a[212:215], v242 offset:37888\n" \
"ds_read_b128 a[244:247], v242 offset:46080\n" \
"v_cvt_pk_bf16_f32 v128, v128, v129\n" \
"v_mfma_f32_16x16x32_bf16 v[100:103], a[196:199], v[144:147], v[100:103]\n" \
"ds_read_b128 a[188:191], v248 offset:64512\n" \
"ds_read_b128 a[248:251], v242 offset:47104\n" \
"v_mfma_f32_16x16x32_bf16 v[100:103], a[228:231], v[152:155], v[100:103]\n" \
"s_waitcnt lgkmcnt(10)\n" \
"ds_read_b128 a[184:187], v248 offset:63488\n" \
"ds_read_b128 a[216:219], v242 offset:38912\n" \
"v_mfma_f32_16x16x32_bf16 v[104:107], a[232:235], v[152:155], v[104:107]\n" \
"buffer_load_dwordx4 v248, s[32:35], s88 offen offset:2048 sc1 lds\n" \
"v_mfma_f32_16x16x32_bf16 v[104:107], a[200:203], v[144:147], v[104:107]\n" \
"ds_read_b128 a[180:183], v248 offset:62464\n" \
"ds_read_b128 a[220:223], v242 offset:39936\n" \
"ds_read_b128 a[176:179], v248 offset:61440\n" \
"ds_read_b128 a[252:255], v242 offset:48128\n" \
"v_exp_f32 v130, v130\n" \
"v_mfma_f32_16x16x32_bf16 v[108:111], a[204:207], v[144:147], v[108:111]\n" \
"ds_read_b128 a[192:195], v242 offset:49152\n" \
"ds_read_b128 a[196:199], v242 offset:50176\n" \
"v_mfma_f32_16x16x32_bf16 v[108:111], a[236:239], v[152:155], v[108:111]\n" \
"s_waitcnt lgkmcnt(10)\n" \
"ds_read_b128 a[200:203], v242 offset:51200\n" \
"ds_read_b128 a[204:207], v242 offset:52224\n" \
"v_mfma_f32_16x16x32_bf16 v[112:115], a[240:243], v[152:155], v[112:115]\n" \
"v_mfma_f32_16x16x32_bf16 v[112:115], a[208:211], v[144:147], v[112:115]\n" \
"v_exp_f32 v131, v131\n" \
"v_mfma_f32_16x16x32_bf16 v[116:119], a[212:215], v[144:147], v[116:119]\n" \
"buffer_load_dwordx4 v248, s[32:35], s88 offen offset:3072 sc1 lds\n" \
"v_mfma_f32_16x16x32_bf16 v[116:119], a[244:247], v[152:155], v[116:119]\n" \
"s_waitcnt lgkmcnt(4)\n" \
"ds_read_b128 a[208:211], v242 offset:53248\n" \
"v_cvt_pk_bf16_f32 v129, v130, v131\n" \
"v_mfma_f32_16x16x32_bf16 v[120:123], a[248:251], v[152:155], v[120:123]\n" \
"s_add_u32 s89, s88, 589824\n" \
"s_add_u32 s88, s88, s27\n" \
"ds_read_b128 a[212:215], v242 offset:54272\n" \
"v_mfma_f32_16x16x32_bf16 v[120:123], a[216:219], v[144:147], v[120:123]\n" \
"s_add_u32 s19, s19, 0x4000\n" \
"s_bitset0_b32 s19, 16\n" \
"s_cmp_eq_u32 s88, s16\n" \
"v_mfma_f32_16x16x32_bf16 v[124:127], a[220:223], v[144:147], v[124:127]\n" \
"s_cmov_b32 s88, s21\n" \
"v_exp_f32 v132, v132\n" \
"v_mfma_f32_16x16x32_bf16 v[124:127], a[252:255], v[152:155], v[124:127]\n" \
"s_cmov_b32 s27, s91\n" \
"ds_read_b128 a[216:219], v242 offset:55296\n" \
"ds_read_b128 a[220:223], v242 offset:56320\n" \
"v_mfma_f32_16x16x32_bf16 v[144:147], a[128:131], v[176:179], 0\n" \
"s_waitcnt vmcnt(13)\n" \
"s_barrier\n" \
"v_and_b32 v230, 7, v251\n" \
"v_mfma_f32_16x16x32_bf16 v[144:147], a[132:135], v[180:183], v[144:147]\n" \
"s_cmov_b32 s16, s77\n" \
"ds_read_b128 a[128:131], v248 offset:0\n" \
"v_lshrrev_b32 v232, 3, v251\n" \
"v_mfma_f32_16x16x32_bf16 v[144:147], a[136:139], v[184:187], v[144:147]\n" \
"s_cmov_b32 s21, s92\n" \
"v_exp_f32 v133, v133\n" \
"v_mfma_f32_16x16x32_bf16 v[144:147], a[140:143], v[188:191], v[144:147]\n" \
"s_cmovk_i32 s91, 0x4000\n" \
"ds_read_b128 a[132:135], v248 offset:1024\n" \
"v_cvt_pk_bf16_f32 v130, v132, v133\n" \
"v_mfma_f32_16x16x32_bf16 v[148:151], a[156:159], v[188:191], 0\n" \
"s_cmp_eq_u32 s88, s17\n" \
"ds_read_b128 a[224:227], v242 offset:57344\n" \
"ds_read_b128 a[136:139], v248 offset:2048\n" \
"v_mfma_f32_16x16x32_bf16 v[148:151], a[152:155], v[184:187], v[148:151]\n" \
"s_cmov_b32 s88, s18\n" \
"ds_read_b128 a[228:231], v242 offset:58368\n" \
"v_lshlrev_b32 v231, 4, v230\n" \
"v_mfma_f32_16x16x32_bf16 v[148:151], a[148:151], v[180:183], v[148:151]\n" \
"s_mov_b32 m0, s20\n" \
"ds_read_b128 a[140:143], v248 offset:3072\n" \
"ds_read_b128 a[232:235], v242 offset:59392\n" \
"v_mfma_f32_16x16x32_bf16 v[148:151], a[144:147], v[176:179], v[148:151]\n" \
"v_exp_f32 v134, v134\n" \
"v_mfma_f32_16x16x32_bf16 v[152:155], a[160:163], v[176:179], 0\n" \
"buffer_load_dwordx4 v248, s[32:35], s89 offen offset:0 sc1 lds\n" \
"v_mfma_f32_16x16x32_bf16 v[152:155], a[164:167], v[180:183], v[152:155]\n" \
"buffer_load_dwordx4 v248, s[32:35], s89 offen offset:1024 sc1 lds\n" \
"v_mfma_f32_16x16x32_bf16 v[152:155], a[168:171], v[184:187], v[152:155]\n" \
"ds_read_b128 a[236:239], v242 offset:60416\n" \
"ds_read_b128 a[240:243], v242 offset:61440\n" \
"v_mfma_f32_16x16x32_bf16 v[152:155], a[172:175], v[188:191], v[152:155]\n" \
"v_exp_f32 v135, v135\n" \
"v_mfma_f32_16x16x32_bf16 v[156:159], a[188:191], v[188:191], 0\n" \
"v_exp_f32 v144, v144\n" \
"v_mfma_f32_16x16x32_bf16 v[156:159], a[184:187], v[184:187], v[156:159]\n" \
"v_exp_f32 v145, v145\n" \
"v_mfma_f32_16x16x32_bf16 v[156:159], a[180:183], v[180:183], v[156:159]\n" \
"v_exp_f32 v146, v146\n" \
"v_cvt_pk_bf16_f32 v131, v134, v135\n" \
"v_mfma_f32_16x16x32_bf16 v[156:159], a[176:179], v[176:179], v[156:159]\n" \
"v_exp_f32 v147, v147\n" \
"v_mfma_f32_16x16x32_bf16 a[40:43], a[48:51], v[128:131], a[40:43]\n" \
"s_waitcnt lgkmcnt(13)\n" \
"v_exp_f32 v148, v148\n" \
"v_mfma_f32_16x16x32_bf16 v[64:67], a[192:195], v[128:131], v[64:67]\n" \
"v_exp_f32 v149, v149\n" \
"v_mfma_f32_16x16x32_bf16 v[68:71], a[196:199], v[128:131], v[68:71]\n" \
"v_exp_f32 v150, v150\n" \
"v_mfma_f32_16x16x32_bf16 v[72:75], a[200:203], v[128:131], v[72:75]\n" \
"buffer_load_dwordx4 v248, s[32:35], s89 offen offset:2048 sc1 lds\n" \
"v_mfma_f32_16x16x32_bf16 v[76:79], a[204:207], v[128:131], v[76:79]\n" \
"s_waitcnt lgkmcnt(9)\n" \
"v_exp_f32 v151, v151\n" \
"v_mfma_f32_16x16x32_bf16 v[80:83], a[208:211], v[128:131], v[80:83]\n" \
"buffer_load_dwordx4 v248, s[32:35], s89 offen offset:3072 sc1 lds\n" \
"v_mfma_f32_16x16x32_bf16 v[84:87], a[212:215], v[128:131], v[84:87]\n" \
"s_add_u32 s20, s20, 0x4000\n" \
"ds_read_b128 a[244:247], v242 offset:62464\n" \
"ds_read_b128 a[156:159], v248 offset:7168\n" \
"ds_read_b128 a[248:251], v242 offset:63488\n" \
"ds_read_b128 a[152:155], v248 offset:6144\n" \
"ds_read_b128 a[252:255], v242 offset:64512\n" \
"ds_read_b128 a[148:151], v248 offset:5120\n" \
"ds_read_b128 a[144:147], v248 offset:4096\n" \
"v_exp_f32 v152, v152\n" \
"v_mfma_f32_16x16x32_bf16 v[88:91], a[216:219], v[128:131], v[88:91]\n" \
"s_and_b32 s20, s20, 0xffff\n" \
"v_exp_f32 v153, v153\n" \
"v_mfma_f32_16x16x32_bf16 v[92:95], a[220:223], v[128:131], v[92:95]\n" \
"s_waitcnt lgkmcnt(10)\n" \
"ds_read_b128 a[160:163], v248 offset:8192\n" \
"ds_read_b128 a[164:167], v248 offset:9216\n" \
"ds_read_b128 a[168:171], v248 offset:10240\n" \
"ds_read_b128 a[172:175], v248 offset:11264\n" \
"v_cvt_pk_bf16_f32 v144, v144, v145\n" \
"v_cvt_pk_bf16_f32 v145, v146, v147\n" \
"v_cvt_pk_bf16_f32 v146, v148, v149\n" \
"v_cvt_pk_bf16_f32 v147, v150, v151\n" \
"s_bitset1_b32 s20, 16\n" \
"v_mul_u32_u24 v230, 7168, v230\n" \
"v_lshl_add_u32 v230, v232, 4, v230\n" \
"v_lshl_or_b32 v232, v232, 14, v231\n" \
"v_and_b32 v231, 3, v251\n" \
"v_lshlrev_b32 v231, 7, v231\n" \
".Ldj_%=:\n" \
"s_mov_b32 s86, 0\n" \
"s_lshl_b32 s95, s67, 1\n" \
"s_cmp_lt_u32 s67, 4\n" \
"s_cbranch_scc1 .Lld_%=\n" \
"s_lshl_b32 s95, s84, 1\n" \
"s_sub_u32 s95, s95, 1\n" \
"s_cbranch_scc1 .Ll0_%=\n" \
".p2align 3\n" \
".Lloop0_%=:\n" \
  \
"v_mfma_f32_16x16x32_bf16 v[128:131], a[128:131], v[192:195], 0\n" \
"s_add_u32 s86, s86, 0x4000\n" \
"ds_read_b128 a[188:191], v240 offset:15360\n" \
"v_cvt_pk_bf16_f32 v152, v152, v153\n" \
"v_mfma_f32_16x16x32_bf16 v[128:131], a[132:135], v[196:199], v[128:131]\n" \
"s_bitset0_b32 s86, 16\n" \
"v_exp_f32 v154, v154\n" \
"v_mfma_f32_16x16x32_bf16 v[128:131], a[136:139], v[200:203], v[128:131]\n" \
"ds_read_b128 a[184:187], v240 offset:14336\n" \
"v_add_u32 v242, 0x10000, v240\n" \
"v_mfma_f32_16x16x32_bf16 v[128:131], a[140:143], v[204:207], v[128:131]\n" \
"s_waitcnt lgkmcnt(6)\n" \
"v_exp_f32 v155, v155\n" \
"v_mfma_f32_16x16x32_bf16 v[132:135], a[156:159], v[204:207], 0\n" \
"s_mov_b32 m0, s19\n" \
"ds_read_b128 a[180:183], v240 offset:13312\n" \
"v_cvt_pk_bf16_f32 v153, v154, v155\n" \
"v_mfma_f32_16x16x32_bf16 v[132:135], a[152:155], v[200:203], v[132:135]\n" \
"ds_read_b128 a[176:179], v240 offset:12288\n" \
"v_exp_f32 v156, v156\n" \
"v_mfma_f32_16x16x32_bf16 v[132:135], a[148:151], v[196:199], v[132:135]\n" \
"buffer_load_dwordx4 v248, s[32:35], s88 offen offset:0 sc1 lds\n" \
"v_mfma_f32_16x16x32_bf16 v[132:135], a[144:147], v[192:195], v[132:135]\n" \
"s_waitcnt lgkmcnt(4)\n" \
"v_exp_f32 v157, v157\n" \
"v_mfma_f32_16x16x32_bf16 v[136:139], a[160:163], v[192:195], 0\n" \
"v_cvt_pk_bf16_f32 v154, v156, v157\n" \
"v_mfma_f32_16x16x32_bf16 v[136:139], a[164:167], v[196:199], v[136:139]\n" \
"v_exp_f32 v158, v158\n" \
"v_mfma_f32_16x16x32_bf16 v[136:139], a[168:171], v[200:203], v[136:139]\n" \
"buffer_load_dwordx4 v248, s[32:35], s88 offen offset:1024 sc1 lds\n" \
"v_mfma_f32_16x16x32_bf16 v[136:139], a[172:175], v[204:207], v[136:139]\n" \
"s_waitcnt lgkmcnt(0)\n" \
"v_exp_f32 v159, v159\n" \
"v_mfma_f32_16x16x32_bf16 v[140:143], a[188:191], v[204:207], 0\n" \
"v_add_u32 v240, s86, v248\n" \
"v_cvt_pk_bf16_f32 v155, v158, v159\n" \
"v_mfma_f32_16x16x32_bf16 v[140:143], a[184:187], v[200:203], v[140:143]\n" \
"buffer_load_dwordx4 v248, s[32:35], s88 offen offset:2048 sc1 lds\n" \
"v_mfma_f32_16x16x32_bf16 v[140:143], a[180:183], v[196:199], v[140:143]\n" \
"v_exp_f32 v128, v128\n" \
"v_mfma_f32_16x16x32_bf16 v[140:143], a[176:179], v[192:195], v[140:143]\n" \
"buffer_load_dwordx4 v248, s[32:35], s88 offen offset:3072 sc1 lds\n" \
"v_mfma_f32_16x16x32_bf16 a[44:47], a[48:51], v[144:147], a[44:47]\n" \
"s_add_u32 s89, s88, 589824\n" \
"v_exp_f32 v129, v129\n" \
"v_mfma_f32_16x16x32_bf16 a[44:47], a[48:51], v[152:155], a[44:47]\n" \
"s_add_u32 s88, s88, s27\n" \
"s_add_u32 s19, s19, 0x4000\n" \
"v_cvt_pk_bf16_f32 v128, v128, v129\n" \
"v_mfma_f32_16x16x32_bf16 v[96:99], a[224:227], v[152:155], v[96:99]\n" \
"s_bitset0_b32 s19, 16\n" \
"v_exp_f32 v130, v130\n" \
"v_mfma_f32_16x16x32_bf16 v[96:99], a[192:195], v[144:147], v[96:99]\n" \
"s_cmp_eq_u32 s88, s16\n" \
"s_cmov_b32 s88, s21\n" \
"ds_read_b128 a[224:227], v242 offset:8192\n" \
"v_mfma_f32_16x16x32_bf16 v[100:103], a[196:199], v[144:147], v[100:103]\n" \
"s_cmov_b32 s27, s91\n" \
"v_exp_f32 v131, v131\n" \
"v_mfma_f32_16x16x32_bf16 v[100:103], a[228:231], v[152:155], v[100:103]\n" \
"s_cmov_b32 s16, s77\n" \
"ds_read_b128 a[192:195], v242 offset:0\n" \
"v_cvt_pk_bf16_f32 v129, v130, v131\n" \
"v_mfma_f32_16x16x32_bf16 v[104:107], a[232:235], v[152:155], v[104:107]\n" \
"s_cmov_b32 s21, s92\n" \
"v_exp_f32 v132, v132\n" \
"v_mfma_f32_16x16x32_bf16 v[104:107], a[200:203], v[144:147], v[104:107]\n" \
"s_cmovk_i32 s91, 0x4000\n" \
"ds_read_b128 a[196:199], v242 offset:1024\n" \
"ds_read_b128 a[228:231], v242 offset:9216\n" \
"v_mfma_f32_16x16x32_bf16 v[108:111], a[204:207], v[144:147], v[108:111]\n" \
"s_cmp_eq_u32 s88, s17\n" \
"v_exp_f32 v133, v133\n" \
"v_mfma_f32_16x16x32_bf16 v[108:111], a[236:239], v[152:155], v[108:111]\n" \
"s_cmov_b32 s88, s18\n" \
"ds_read_b128 a[232:235], v242 offset:10240\n" \
"v_cvt_pk_bf16_f32 v130, v132, v133\n" \
"v_mfma_f32_16x16x32_bf16 v[112:115], a[240:243], v[152:155], v[112:115]\n" \
"v_exp_f32 v134, v134\n" \
"v_mfma_f32_16x16x32_bf16 v[112:115], a[208:211], v[144:147], v[112:115]\n" \
"ds_read_b128 a[200:203], v242 offset:2048\n" \
"v_mfma_f32_16x16x32_bf16 v[116:119], a[212:215], v[144:147], v[116:119]\n" \
"v_exp_f32 v135, v135\n" \
"v_mfma_f32_16x16x32_bf16 v[116:119], a[244:247], v[152:155], v[116:119]\n" \
"ds_read_b128 a[204:207], v242 offset:3072\n" \
"v_cvt_pk_bf16_f32 v131, v134, v135\n" \
"v_mfma_f32_16x16x32_bf16 v[120:123], a[248:251], v[152:155], v[120:123]\n" \
"ds_read_b128 a[236:239], v242 offset:11264\n" \
"v_mfma_f32_16x16x32_bf16 v[120:123], a[216:219], v[144:147], v[120:123]\n" \
"v_exp_f32 v136, v136\n" \
"v_mfma_f32_16x16x32_bf16 v[124:127], a[220:223], v[144:147], v[124:127]\n" \
"ds_read_b128 a[240:243], v242 offset:12288\n" \
"v_mfma_f32_16x16x32_bf16 v[124:127], a[252:255], v[152:155], v[124:127]\n" \
"s_waitcnt vmcnt(12)\n" \
"v_exp_f32 v137, v137\n" \
"v_mfma_f32_16x16x32_bf16 v[144:147], a[128:131], v[208:211], 0\n" \
"s_barrier\n" \
"ds_read_b128 a[208:211], v242 offset:4096\n" \
"v_cvt_pk_bf16_f32 v136, v136, v137\n" \
"v_mfma_f32_16x16x32_bf16 v[144:147], a[132:135], v[212:215], v[144:147]\n" \
"s_mov_b32 m0, s20\n" \
"v_exp_f32 v138, v138\n" \
"v_mfma_f32_16x16x32_bf16 v[144:147], a[136:139], v[216:219], v[144:147]\n" \
"buffer_load_dwordx4 v248, s[32:35], s89 offen offset:0 sc1 lds\n" \
"v_mfma_f32_16x16x32_bf16 v[144:147], a[140:143], v[220:223], v[144:147]\n" \
"v_exp_f32 v139, v139\n" \
"v_mfma_f32_16x16x32_bf16 v[148:151], a[156:159], v[220:223], 0\n" \
"ds_read_b128 a[212:215], v242 offset:5120\n" \
"v_cvt_pk_bf16_f32 v137, v138, v139\n" \
"v_mfma_f32_16x16x32_bf16 v[148:151], a[152:155], v[216:219], v[148:151]\n" \
"v_exp_f32 v140, v140\n" \
"v_mfma_f32_16x16x32_bf16 v[148:151], a[148:151], v[212:215], v[148:151]\n" \
"ds_read_b128 a[244:247], v242 offset:13312\n" \
"ds_read_b128 a[248:251], v242 offset:14336\n" \
"v_mfma_f32_16x16x32_bf16 v[148:151], a[144:147], v[208:211], v[148:151]\n" \
"v_exp_f32 v141, v141\n" \
"v_mfma_f32_16x16x32_bf16 v[152:155], a[160:163], v[208:211], 0\n" \
"buffer_load_dwordx4 v248, s[32:35], s89 offen offset:1024 sc1 lds\n" \
"v_mfma_f32_16x16x32_bf16 v[152:155], a[164:167], v[212:215], v[152:155]\n" \
"v_exp_f32 v142, v142\n" \
"v_mfma_f32_16x16x32_bf16 v[152:155], a[168:171], v[216:219], v[152:155]\n" \
"ds_read_b128 a[216:219], v242 offset:6144\n" \
"v_cvt_pk_bf16_f32 v138, v140, v141\n" \
"v_mfma_f32_16x16x32_bf16 v[152:155], a[172:175], v[220:223], v[152:155]\n" \
"v_exp_f32 v143, v143\n" \
"v_mfma_f32_16x16x32_bf16 v[156:159], a[188:191], v[220:223], 0\n" \
"ds_read_b128 a[128:131], v240 offset:0\n" \
"v_cvt_pk_bf16_f32 v139, v142, v143\n" \
"v_mfma_f32_16x16x32_bf16 v[156:159], a[184:187], v[216:219], v[156:159]\n" \
"ds_read_b128 a[132:135], v240 offset:1024\n" \
"ds_read_b128 a[220:223], v242 offset:7168\n" \
"v_mfma_f32_16x16x32_bf16 v[156:159], a[180:183], v[212:215], v[156:159]\n" \
"v_exp_f32 v144, v144\n" \
"v_mfma_f32_16x16x32_bf16 v[156:159], a[176:179], v[208:211], v[156:159]\n" \
"buffer_load_dwordx4 v248, s[32:35], s89 offen offset:2048 sc1 lds\n" \
"v_mfma_f32_16x16x32_bf16 a[32:35], a[48:51], v[128:131], a[32:35]\n" \
".Lst0_9_%=:\n" \
"s_cbranch_execz .Lst0_13_%=\n" \
"v_exp_f32 v145, v145\n" \
"v_mfma_f32_16x16x32_bf16 a[32:35], a[48:51], v[136:139], a[32:35]\n" \
"s_waitcnt lgkmcnt(13)\n" \
"ds_read_b128 a[136:139], v240 offset:2048\n" \
"v_cvt_pk_bf16_f32 v144, v144, v145\n" \
"v_mfma_f32_16x16x32_bf16 v[0:3], a[224:227], v[136:139], v[0:3]\n" \
"v_exp_f32 v146, v146\n" \
"v_mfma_f32_16x16x32_bf16 v[0:3], a[192:195], v[128:131], v[0:3]\n" \
"ds_read_b128 a[252:255], v242 offset:15360\n" \
"ds_read_b128 a[140:143], v240 offset:3072\n" \
"v_mfma_f32_16x16x32_bf16 v[4:7], a[196:199], v[128:131], v[4:7]\n" \
"v_exp_f32 v147, v147\n" \
"v_mfma_f32_16x16x32_bf16 v[4:7], a[228:231], v[136:139], v[4:7]\n" \
"s_waitcnt lgkmcnt(12)\n" \
"buffer_load_dwordx4 v248, s[32:35], s89 offen offset:3072 sc1 lds\n" \
"v_mfma_f32_16x16x32_bf16 v[8:11], a[232:235], v[136:139], v[8:11]\n" \
"s_add_u32 s20, s20, 0x4000\n" \
"v_exp_f32 v148, v148\n" \
"v_mfma_f32_16x16x32_bf16 v[8:11], a[200:203], v[128:131], v[8:11]\n" \
"s_and_b32 s20, s20, 0xffff\n" \
"ds_read_b128 a[156:159], v240 offset:7168\n" \
"v_cvt_pk_bf16_f32 v145, v146, v147\n" \
"v_mfma_f32_16x16x32_bf16 v[12:15], a[204:207], v[128:131], v[12:15]\n" \
"s_bitset1_b32 s20, 16\n" \
"v_exp_f32 v149, v149\n" \
"v_mfma_f32_16x16x32_bf16 v[12:15], a[236:239], v[136:139], v[12:15]\n" \
"s_waitcnt lgkmcnt(9)\n" \
"ds_read_b128 a[152:155], v240 offset:6144\n" \
"v_cvt_pk_bf16_f32 v146, v148, v149\n" \
"v_mfma_f32_16x16x32_bf16 v[16:19], a[240:243], v[136:139], v[16:19]\n" \
"v_exp_f32 v150, v150\n" \
"v_mfma_f32_16x16x32_bf16 v[16:19], a[208:211], v[128:131], v[16:19]\n" \
"ds_read_b128 a[148:151], v240 offset:5120\n" \
"ds_read_b128 a[144:147], v240 offset:4096\n" \
"v_mfma_f32_16x16x32_bf16 v[20:23], a[212:215], v[128:131], v[20:23]\n" \
"v_exp_f32 v151, v151\n" \
"v_mfma_f32_16x16x32_bf16 v[20:23], a[244:247], v[136:139], v[20:23]\n" \
"s_waitcnt lgkmcnt(5)\n" \
"ds_read_b128 a[160:163], v240 offset:8192\n" \
"v_cvt_pk_bf16_f32 v147, v150, v151\n" \
"v_mfma_f32_16x16x32_bf16 v[24:27], a[248:251], v[136:139], v[24:27]\n" \
"ds_read_b128 a[164:167], v240 offset:9216\n" \
"ds_read_b128 a[168:171], v240 offset:10240\n" \
"v_mfma_f32_16x16x32_bf16 v[24:27], a[216:219], v[128:131], v[24:27]\n" \
"v_exp_f32 v152, v152\n" \
"v_mfma_f32_16x16x32_bf16 v[28:31], a[220:223], v[128:131], v[28:31]\n" \
"ds_read_b128 a[172:175], v240 offset:11264\n" \
"ds_read_b128 a[188:191], v240 offset:15360\n" \
"v_mfma_f32_16x16x32_bf16 v[28:31], a[252:255], v[136:139], v[28:31]\n" \
"s_waitcnt lgkmcnt(9)\n" \
"v_exp_f32 v153, v153\n" \
"v_mfma_f32_16x16x32_bf16 v[128:131], a[128:131], v[192:195], 0\n" \
"s_add_u32 s86, s86, 0x4000\n" \
"ds_read_b128 a[184:187], v240 offset:14336\n" \
"v_cvt_pk_bf16_f32 v152, v152, v153\n" \
"v_mfma_f32_16x16x32_bf16 v[128:131], a[132:135], v[196:199], v[128:131]\n" \
"s_bitset0_b32 s86, 16\n" \
"v_exp_f32 v154, v154\n" \
"v_mfma_f32_16x16x32_bf16 v[128:131], a[136:139], v[200:203], v[128:131]\n" \
"ds_read_b128 a[180:183], v240 offset:13312\n" \
"v_add_u32 v242, 0x10000, v240\n" \
"v_mfma_f32_16x16x32_bf16 v[128:131], a[140:143], v[204:207], v[128:131]\n" \
"s_waitcnt lgkmcnt(7)\n" \
"v_exp_f32 v155, v155\n" \
"v_mfma_f32_16x16x32_bf16 v[132:135], a[156:159], v[204:207], 0\n" \
"s_mov_b32 m0, s19\n" \
"ds_read_b128 a[176:179], v240 offset:12288\n" \
"v_cvt_pk_bf16_f32 v153, v154, v155\n" \
"v_mfma_f32_16x16x32_bf16 v[132:135], a[152:155], v[200:203], v[132:135]\n" \
"v_exp_f32 v156, v156\n" \
"v_mfma_f32_16x16x32_bf16 v[132:135], a[148:151], v[196:199], v[132:135]\n" \
"buffer_load_dwordx4 v248, s[32:35], s88 offen offset:0 sc1 lds\n" \
"v_mfma_f32_16x16x32_bf16 v[132:135], a[144:147], v[192:195], v[132:135]\n" \
"s_waitcnt lgkmcnt(4)\n" \
"v_exp_f32 v157, v157\n" \
"v_mfma_f32_16x16x32_bf16 v[136:139], a[160:163], v[192:195], 0\n" \
"v_cvt_pk_bf16_f32 v154, v156, v157\n" \
"v_mfma_f32_16x16x32_bf16 v[136:139], a[164:167], v[196:199], v[136:139]\n" \
"v_exp_f32 v158, v158\n" \
"v_mfma_f32_16x16x32_bf16 v[136:139], a[168:171], v[200:203], v[136:139]\n" \
"v_add_u32 v240, s86, v248\n" \
"v_mfma_f32_16x16x32_bf16 v[136:139], a[172:175], v[204:207], v[136:139]\n" \
"s_waitcnt lgkmcnt(0)\n" \
"v_exp_f32 v159, v159\n" \
"v_mfma_f32_16x16x32_bf16 v[140:143], a[188:191], v[204:207], 0\n" \
"v_cvt_pk_bf16_f32 v155, v158, v159\n" \
"v_mfma_f32_16x16x32_bf16 v[140:143], a[184:187], v[200:203], v[140:143]\n" \
"buffer_load_dwordx4 v248, s[32:35], s88 offen offset:1024 sc1 lds\n" \
"v_mfma_f32_16x16x32_bf16 v[140:143], a[180:183], v[196:199], v[140:143]\n" \
"v_exp_f32 v128, v128\n" \
"v_mfma_f32_16x16x32_bf16 v[140:143], a[176:179], v[192:195], v[140:143]\n" \
"v_mfma_f32_16x16x32_bf16 a[36:39], a[48:51], v[144:147], a[36:39]\n" \
"v_exp_f32 v129, v129\n" \
"v_mfma_f32_16x16x32_bf16 a[36:39], a[48:51], v[152:155], a[36:39]\n" \
"v_cvt_pk_bf16_f32 v128, v128, v129\n" \
"v_mfma_f32_16x16x32_bf16 v[32:35], a[224:227], v[152:155], v[32:35]\n" \
"v_exp_f32 v130, v130\n" \
"v_mfma_f32_16x16x32_bf16 v[32:35], a[192:195], v[144:147], v[32:35]\n" \
"ds_read_b128 a[224:227], v242 offset:8192\n" \
"v_mfma_f32_16x16x32_bf16 v[36:39], a[196:199], v[144:147], v[36:39]\n" \
"v_exp_f32 v131, v131\n" \
"v_mfma_f32_16x16x32_bf16 v[36:39], a[228:231], v[152:155], v[36:39]\n" \
"ds_read_b128 a[192:195], v242 offset:0\n" \
"v_cvt_pk_bf16_f32 v129, v130, v131\n" \
"v_mfma_f32_16x16x32_bf16 v[40:43], a[232:235], v[152:155], v[40:43]\n" \
"v_exp_f32 v132, v132\n" \
"v_mfma_f32_16x16x32_bf16 v[40:43], a[200:203], v[144:147], v[40:43]\n" \
"ds_read_b128 a[196:199], v242 offset:1024\n" \
"ds_read_b128 a[228:231], v242 offset:9216\n" \
"v_mfma_f32_16x16x32_bf16 v[44:47], a[204:207], v[144:147], v[44:47]\n" \
"v_exp_f32 v133, v133\n" \
"v_mfma_f32_16x16x32_bf16 v[44:47], a[236:239], v[152:155], v[44:47]\n" \
"ds_read_b128 a[232:235], v242 offset:10240\n" \
"v_cvt_pk_bf16_f32 v130, v132, v133\n" \
"v_mfma_f32_16x16x32_bf16 v[48:51], a[240:243], v[152:155], v[48:51]\n" \
"v_exp_f32 v134, v134\n" \
"v_mfma_f32_16x16x32_bf16 v[48:51], a[208:211], v[144:147], v[48:51]\n" \
"ds_read_b128 a[200:203], v242 offset:2048\n" \
"v_mfma_f32_16x16x32_bf16 v[52:55], a[212:215], v[144:147], v[52:55]\n" \
"v_exp_f32 v135, v135\n" \
"v_mfma_f32_16x16x32_bf16 v[52:55], a[244:247], v[152:155], v[52:55]\n" \
"ds_read_b128 a[204:207], v242 offset:3072\n" \
"v_cvt_pk_bf16_f32 v131, v134, v135\n" \
"v_mfma_f32_16x16x32_bf16 v[56:59], a[248:251], v[152:155], v[56:59]\n" \
"ds_read_b128 a[236:239], v242 offset:11264\n" \
"v_mfma_f32_16x16x32_bf16 v[56:59], a[216:219], v[144:147], v[56:59]\n" \
"v_exp_f32 v136, v136\n" \
"v_mfma_f32_16x16x32_bf16 v[60:63], a[220:223], v[144:147], v[60:63]\n" \
"ds_read_b128 a[240:243], v242 offset:12288\n" \
"v_mfma_f32_16x16x32_bf16 v[60:63], a[252:255], v[152:155], v[60:63]\n" \
"v_exp_f32 v137, v137\n" \
"v_mfma_f32_16x16x32_bf16 v[144:147], a[128:131], v[208:211], 0\n" \
"ds_read_b128 a[208:211], v242 offset:4096\n" \
"v_cvt_pk_bf16_f32 v136, v136, v137\n" \
"v_mfma_f32_16x16x32_bf16 v[144:147], a[132:135], v[212:215], v[144:147]\n" \
"v_exp_f32 v138, v138\n" \
"v_mfma_f32_16x16x32_bf16 v[144:147], a[136:139], v[216:219], v[144:147]\n" \
"ds_read_b128 a[212:215], v242 offset:5120\n" \
"ds_read_b128 a[244:247], v242 offset:13312\n" \
"v_mfma_f32_16x16x32_bf16 v[144:147], a[140:143], v[220:223], v[144:147]\n" \
"v_exp_f32 v139, v139\n" \
"v_mfma_f32_16x16x32_bf16 v[148:151], a[156:159], v[220:223], 0\n" \
"ds_read_b128 a[248:251], v242 offset:14336\n" \
"v_cvt_pk_bf16_f32 v137, v138, v139\n" \
"v_mfma_f32_16x16x32_bf16 v[148:151], a[152:155], v[216:219], v[148:151]\n" \
"v_exp_f32 v140, v140\n" \
"v_mfma_f32_16x16x32_bf16 v[148:151], a[148:151], v[212:215], v[148:151]\n" \
"ds_read_b128 a[216:219], v242 offset:6144\n" \
"v_mfma_f32_16x16x32_bf16 v[148:151], a[144:147], v[208:211], v[148:151]\n" \
"v_exp_f32 v141, v141\n" \
"v_mfma_f32_16x16x32_bf16 v[152:155], a[160:163], v[208:211], 0\n" \
"ds_read_b128 a[220:223], v242 offset:7168\n" \
"v_cvt_pk_bf16_f32 v138, v140, v141\n" \
"v_mfma_f32_16x16x32_bf16 v[152:155], a[164:167], v[212:215], v[152:155]\n" \
"v_exp_f32 v142, v142\n" \
"v_mfma_f32_16x16x32_bf16 v[152:155], a[168:171], v[216:219], v[152:155]\n" \
"ds_read_b128 a[252:255], v242 offset:15360\n" \
"v_mfma_f32_16x16x32_bf16 v[152:155], a[172:175], v[220:223], v[152:155]\n" \
"v_exp_f32 v143, v143\n" \
"v_mfma_f32_16x16x32_bf16 v[156:159], a[188:191], v[220:223], 0\n" \
"v_cvt_pk_bf16_f32 v139, v142, v143\n" \
"v_mfma_f32_16x16x32_bf16 v[156:159], a[184:187], v[216:219], v[156:159]\n" \
"buffer_load_dwordx4 v248, s[32:35], s88 offen offset:2048 sc1 lds\n" \
"v_mfma_f32_16x16x32_bf16 v[156:159], a[180:183], v[212:215], v[156:159]\n" \
"v_exp_f32 v144, v144\n" \
"v_mfma_f32_16x16x32_bf16 v[156:159], a[176:179], v[208:211], v[156:159]\n" \
"v_mfma_f32_16x16x32_bf16 a[32:35], a[48:51], v[128:131], a[32:35]\n" \
"v_exp_f32 v145, v145\n" \
"v_mfma_f32_16x16x32_bf16 a[32:35], a[48:51], v[136:139], a[32:35]\n" \
"s_waitcnt lgkmcnt(12)\n" \
"v_cvt_pk_bf16_f32 v144, v144, v145\n" \
"v_mfma_f32_16x16x32_bf16 v[0:3], a[224:227], v[136:139], v[0:3]\n" \
"v_exp_f32 v146, v146\n" \
"v_mfma_f32_16x16x32_bf16 v[0:3], a[192:195], v[128:131], v[0:3]\n" \
"v_mfma_f32_16x16x32_bf16 v[4:7], a[196:199], v[128:131], v[4:7]\n" \
"v_exp_f32 v147, v147\n" \
"v_mfma_f32_16x16x32_bf16 v[4:7], a[228:231], v[136:139], v[4:7]\n" \
"s_waitcnt lgkmcnt(8)\n" \
"v_cvt_pk_bf16_f32 v145, v146, v147\n" \
"v_mfma_f32_16x16x32_bf16 v[8:11], a[232:235], v[136:139], v[8:11]\n" \
"v_exp_f32 v148, v148\n" \
"v_mfma_f32_16x16x32_bf16 v[8:11], a[200:203], v[128:131], v[8:11]\n" \
"v_mfma_f32_16x16x32_bf16 v[12:15], a[204:207], v[128:131], v[12:15]\n" \
"v_exp_f32 v149, v149\n" \
"v_mfma_f32_16x16x32_bf16 v[12:15], a[236:239], v[136:139], v[12:15]\n" \
"s_waitcnt lgkmcnt(4)\n" \
"v_cvt_pk_bf16_f32 v146, v148, v149\n" \
"v_mfma_f32_16x16x32_bf16 v[16:19], a[240:243], v[136:139], v[16:19]\n" \
"v_exp_f32 v150, v150\n" \
"v_mfma_f32_16x16x32_bf16 v[16:19], a[208:211], v[128:131], v[16:19]\n" \
"v_mfma_f32_16x16x32_bf16 v[20:23], a[212:215], v[128:131], v[20:23]\n" \
"v_exp_f32 v151, v151\n" \
"v_mfma_f32_16x16x32_bf16 v[20:23], a[244:247], v[136:139], v[20:23]\n" \
"s_waitcnt lgkmcnt(0)\n" \
"v_cvt_pk_bf16_f32 v147, v150, v151\n" \
"v_mfma_f32_16x16x32_bf16 v[24:27], a[248:251], v[136:139], v[24:27]\n" \
"buffer_load_dwordx4 v248, s[32:35], s88 offen offset:3072 sc1 lds\n" \
"v_mfma_f32_16x16x32_bf16 v[24:27], a[216:219], v[128:131], v[24:27]\n" \
"s_add_u32 s89, s88, 589824\n" \
"v_exp_f32 v152, v152\n" \
"v_mfma_f32_16x16x32_bf16 v[28:31], a[220:223], v[128:131], v[28:31]\n" \
"s_add_u32 s88, s88, s27\n" \
"s_add_u32 s19, s19, 0x4000\n" \
"s_bitset0_b32 s19, 16\n" \
"v_mfma_f32_16x16x32_bf16 v[28:31], a[252:255], v[136:139], v[28:31]\n" \
"s_cmp_eq_u32 s88, s16\n" \
"v_exp_f32 v153, v153\n" \
"v_mfma_f32_16x16x32_bf16 v[128:131], a[128:131], v[160:163], 0\n" \
"s_cmov_b32 s88, s21\n" \
"s_cmov_b32 s27, s91\n" \
"v_cvt_pk_bf16_f32 v152, v152, v153\n" \
"v_mfma_f32_16x16x32_bf16 v[128:131], a[132:135], v[164:167], v[128:131]\n" \
"s_cmov_b32 s16, s77\n" \
"v_exp_f32 v154, v154\n" \
"v_mfma_f32_16x16x32_bf16 v[128:131], a[136:139], v[168:171], v[128:131]\n" \
"s_cmov_b32 s21, s92\n" \
"s_waitcnt vmcnt(12)\n" \
"s_cmovk_i32 s91, 0x4000\n" \
"v_mfma_f32_16x16x32_bf16 v[128:131], a[140:143], v[172:175], v[128:131]\n" \
"s_cmp_eq_u32 s88, s17\n" \
"v_exp_f32 v155, v155\n" \
"v_mfma_f32_16x16x32_bf16 v[132:135], a[156:159], v[172:175], 0\n" \
"s_cmov_b32 s88, s18\n" \
"v_cvt_pk_bf16_f32 v153, v154, v155\n" \
"v_mfma_f32_16x16x32_bf16 v[132:135], a[152:155], v[168:171], v[132:135]\n" \
"v_exp_f32 v156, v156\n" \
"v_mfma_f32_16x16x32_bf16 v[132:135], a[148:151], v[164:167], v[132:135]\n" \
"buffer_store_dwordx4 a[60:63], v232, s[32:35], s39 offen sc1\n" \
"v_mfma_f32_16x16x32_bf16 v[132:135], a[144:147], v[160:163], v[132:135]\n" \
"v_exp_f32 v157, v157\n" \
"v_mfma_f32_16x16x32_bf16 v[136:139], a[160:163], v[160:163], 0\n" \
"v_cvt_pk_bf16_f32 v154, v156, v157\n" \
"v_mfma_f32_16x16x32_bf16 v[136:139], a[164:167], v[164:167], v[136:139]\n" \
"v_exp_f32 v158, v158\n" \
"v_mfma_f32_16x16x32_bf16 v[136:139], a[168:171], v[168:171], v[136:139]\n" \
"v_mfma_f32_16x16x32_bf16 v[136:139], a[172:175], v[172:175], v[136:139]\n" \
"v_exp_f32 v159, v159\n" \
"v_mfma_f32_16x16x32_bf16 v[140:143], a[188:191], v[172:175], 0\n" \
"buffer_load_dwordx4 a[60:63], v230, s[40:43], s38 offen\n" \
"v_cvt_pk_bf16_f32 v155, v158, v159\n" \
"v_mfma_f32_16x16x32_bf16 v[140:143], a[184:187], v[168:171], v[140:143]\n" \
"v_mfma_f32_16x16x32_bf16 v[140:143], a[180:183], v[164:167], v[140:143]\n" \
"v_exp_f32 v128, v128\n" \
"v_mfma_f32_16x16x32_bf16 v[140:143], a[176:179], v[160:163], v[140:143]\n" \
"v_mfma_f32_16x16x32_bf16 a[36:39], a[48:51], v[144:147], a[36:39]\n" \
"s_mov_b32 m0, s87\n" \
"v_exp_f32 v129, v129\n" \
"v_mfma_f32_16x16x32_bf16 a[36:39], a[48:51], v[152:155], a[36:39]\n" \
"v_cvt_pk_bf16_f32 v128, v128, v129\n" \
"v_mfma_f32_16x16x32_bf16 v[32:35], a[224:227], v[152:155], v[32:35]\n" \
"buffer_load_dwordx4 v248, s[32:35], s93 offen sc1 lds\n" \
"v_mfma_f32_16x16x32_bf16 v[32:35], a[192:195], v[144:147], v[32:35]\n" \
"s_add_u32 s93, s93, 1024\n" \
"v_exp_f32 v130, v130\n" \
"v_mfma_f32_16x16x32_bf16 v[36:39], a[196:199], v[144:147], v[36:39]\n" \
"s_add_u32 s87, s87, 1024\n" \
"v_exp_f32 v131, v131\n" \
"v_mfma_f32_16x16x32_bf16 v[36:39], a[228:231], v[152:155], v[36:39]\n" \
"v_cvt_pk_bf16_f32 v129, v130, v131\n" \
"v_mfma_f32_16x16x32_bf16 v[40:43], a[232:235], v[152:155], v[40:43]\n" \
"v_exp_f32 v132, v132\n" \
"v_mfma_f32_16x16x32_bf16 v[40:43], a[200:203], v[144:147], v[40:43]\n" \
"v_mfma_f32_16x16x32_bf16 v[44:47], a[204:207], v[144:147], v[44:47]\n" \
"v_exp_f32 v133, v133\n" \
"v_mfma_f32_16x16x32_bf16 v[44:47], a[236:239], v[152:155], v[44:47]\n" \
"v_cvt_pk_bf16_f32 v130, v132, v133\n" \
"v_mfma_f32_16x16x32_bf16 v[48:51], a[240:243], v[152:155], v[48:51]\n" \
"v_exp_f32 v134, v134\n" \
"v_mfma_f32_16x16x32_bf16 v[48:51], a[208:211], v[144:147], v[48:51]\n" \
"buffer_load_dword v245, v231, s[60:63], s72 offen\n" \
"v_mfma_f32_16x16x32_bf16 v[52:55], a[212:215], v[144:147], v[52:55]\n" \
"s_add_u32 s38, s38, 512\n" \
"v_exp_f32 v135, v135\n" \
"v_mfma_f32_16x16x32_bf16 v[52:55], a[244:247], v[152:155], v[52:55]\n" \
"s_add_u32 s39, s39, 0x80000\n" \
"s_add_u32 s72, s72, 512\n" \
"v_cvt_pk_bf16_f32 v131, v134, v135\n" \
"v_mfma_f32_16x16x32_bf16 v[56:59], a[248:251], v[152:155], v[56:59]\n" \
"v_mfma_f32_16x16x32_bf16 v[56:59], a[216:219], v[144:147], v[56:59]\n" \
"v_exp_f32 v136, v136\n" \
"v_mfma_f32_16x16x32_bf16 v[60:63], a[220:223], v[144:147], v[60:63]\n" \
"v_mfma_f32_16x16x32_bf16 v[60:63], a[252:255], v[152:155], v[60:63]\n" \
"s_waitcnt vmcnt(16)\n" \
"v_exp_f32 v137, v137\n" \
"v_mfma_f32_16x16x32_bf16 v[144:147], a[128:131], v[176:179], 0\n" \
"s_barrier\n" \
"s_mov_b32 m0, s20\n" \
"v_cvt_pk_bf16_f32 v136, v136, v137\n" \
"v_mfma_f32_16x16x32_bf16 v[144:147], a[132:135], v[180:183], v[144:147]\n" \
"v_exp_f32 v138, v138\n" \
"v_mfma_f32_16x16x32_bf16 v[144:147], a[136:139], v[184:187], v[144:147]\n" \
"buffer_load_dwordx4 v248, s[32:35], s89 offen offset:0 sc1 lds\n" \
"v_mfma_f32_16x16x32_bf16 v[144:147], a[140:143], v[188:191], v[144:147]\n" \
"v_exp_f32 v139, v139\n" \
"v_mfma_f32_16x16x32_bf16 v[148:151], a[156:159], v[188:191], 0\n" \
"ds_read_b128 a[128:131], v240 offset:0\n" \
"v_cvt_pk_bf16_f32 v137, v138, v139\n" \
"v_mfma_f32_16x16x32_bf16 v[148:151], a[152:155], v[184:187], v[148:151]\n" \
"v_exp_f32 v140, v140\n" \
"v_mfma_f32_16x16x32_bf16 v[148:151], a[148:151], v[180:183], v[148:151]\n" \
"ds_read_b128 a[132:135], v240 offset:1024\n" \
"ds_read_b128 a[136:139], v240 offset:2048\n" \
"v_mfma_f32_16x16x32_bf16 v[148:151], a[144:147], v[176:179], v[148:151]\n" \
"v_exp_f32 v141, v141\n" \
"v_mfma_f32_16x16x32_bf16 v[152:155], a[160:163], v[176:179], 0\n" \
"buffer_load_dwordx4 v248, s[32:35], s89 offen offset:1024 sc1 lds\n" \
"v_mfma_f32_16x16x32_bf16 v[152:155], a[164:167], v[180:183], v[152:155]\n" \
"v_exp_f32 v142, v142\n" \
"v_mfma_f32_16x16x32_bf16 v[152:155], a[168:171], v[184:187], v[152:155]\n" \
"ds_read_b128 a[140:143], v240 offset:3072\n" \
"v_cvt_pk_bf16_f32 v138, v140, v141\n" \
"v_mfma_f32_16x16x32_bf16 v[152:155], a[172:175], v[188:191], v[152:155]\n" \
"v_exp_f32 v143, v143\n" \
"v_mfma_f32_16x16x32_bf16 v[156:159], a[188:191], v[188:191], 0\n" \
"ds_read_b128 a[156:159], v240 offset:7168\n" \
"v_cvt_pk_bf16_f32 v139, v142, v143\n" \
"v_mfma_f32_16x16x32_bf16 v[156:159], a[184:187], v[184:187], v[156:159]\n" \
"ds_read_b128 a[152:155], v240 offset:6144\n" \
"ds_read_b128 a[148:151], v240 offset:5120\n" \
"v_mfma_f32_16x16x32_bf16 v[156:159], a[180:183], v[180:183], v[156:159]\n" \
"v_exp_f32 v144, v144\n" \
"v_mfma_f32_16x16x32_bf16 v[156:159], a[176:179], v[176:179], v[156:159]\n" \
"buffer_load_dwordx4 v248, s[32:35], s89 offen offset:2048 sc1 lds\n" \
"v_mfma_f32_16x16x32_bf16 a[40:43], a[48:51], v[128:131], a[40:43]\n" \
"v_exp_f32 v145, v145\n" \
"v_mfma_f32_16x16x32_bf16 a[40:43], a[48:51], v[136:139], a[40:43]\n" \
"ds_read_b128 a[144:147], v240 offset:4096\n" \
"v_cvt_pk_bf16_f32 v144, v144, v145\n" \
"v_mfma_f32_16x16x32_bf16 v[64:67], a[224:227], v[136:139], v[64:67]\n" \
"v_exp_f32 v146, v146\n" \
"v_mfma_f32_16x16x32_bf16 v[64:67], a[192:195], v[128:131], v[64:67]\n" \
"ds_read_b128 a[160:163], v240 offset:8192\n" \
"ds_read_b128 a[164:167], v240 offset:9216\n" \
"v_mfma_f32_16x16x32_bf16 v[68:71], a[196:199], v[128:131], v[68:71]\n" \
"v_exp_f32 v147, v147\n" \
"v_mfma_f32_16x16x32_bf16 v[68:71], a[228:231], v[136:139], v[68:71]\n" \
"buffer_load_dwordx4 v248, s[32:35], s89 offen offset:3072 sc1 lds\n" \
"v_mfma_f32_16x16x32_bf16 v[72:75], a[232:235], v[136:139], v[72:75]\n" \
"s_add_u32 s20, s20, 0x4000\n" \
"v_exp_f32 v148, v148\n" \
"v_mfma_f32_16x16x32_bf16 v[72:75], a[200:203], v[128:131], v[72:75]\n" \
"s_and_b32 s20, s20, 0xffff\n" \
"ds_read_b128 a[168:171], v240 offset:10240\n" \
"v_cvt_pk_bf16_f32 v145, v146, v147\n" \
"v_mfma_f32_16x16x32_bf16 v[76:79], a[204:207], v[128:131], v[76:79]\n" \
"s_bitset1_b32 s20, 16\n" \
"v_exp_f32 v149, v149\n" \
"v_mfma_f32_16x16x32_bf16 v[76:79], a[236:239], v[136:139], v[76:79]\n" \
"ds_read_b128 a[172:175], v240 offset:11264\n" \
"v_cvt_pk_bf16_f32 v146, v148, v149\n" \
"v_mfma_f32_16x16x32_bf16 v[80:83], a[240:243], v[136:139], v[80:83]\n" \
"v_exp_f32 v150, v150\n" \
"v_mfma_f32_16x16x32_bf16 v[80:83], a[208:211], v[128:131], v[80:83]\n" \
"v_mfma_f32_16x16x32_bf16 v[84:87], a[212:215], v[128:131], v[84:87]\n" \
"s_sub_u32 s95, s95, 1\n" \
"v_exp_f32 v151, v151\n" \
"v_mfma_f32_16x16x32_bf16 v[84:87], a[244:247], v[136:139], v[84:87]\n" \
"v_cvt_pk_bf16_f32 v147, v150, v151\n" \
"v_mfma_f32_16x16x32_bf16 v[88:91], a[248:251], v[136:139], v[88:91]\n" \
"v_mfma_f32_16x16x32_bf16 v[88:91], a[216:219], v[128:131], v[88:91]\n" \
"v_exp_f32 v152, v152\n" \
"v_mfma_f32_16x16x32_bf16 v[92:95], a[220:223], v[128:131], v[92:95]\n" \
"v_exp_f32 v153, v153\n" \
"v_mfma_f32_16x16x32_bf16 v[92:95], a[252:255], v[136:139], v[92:95]\n" \
"s_waitcnt lgkmcnt(8)\n" \
"s_cbranch_scc0 .Lloop0_%=\n" \
".Ll0_%=:\n" \
".Lst0_10_%=:\n" \
"s_cbranch_execz .Lst0_14_%=\n" \
"s_lshl_b32 s95, s84, 1\n" \
"s_sub_u32 s95, 7, s95\n" \
".p2align 3\n" \
".Lloop_%=:\n" \
  \
"v_mfma_f32_16x16x32_bf16 v[128:131], a[128:131], v[192:195], 0\n" \
"s_add_u32 s86, s86, 0x4000\n" \
"ds_read_b128 a[188:191], v240 offset:15360\n" \
"v_cvt_pk_bf16_f32 v152, v152, v153\n" \
"v_mfma_f32_16x16x32_bf16 v[128:131], a[132:135], v[196:199], v[128:131]\n" \
"s_bitset0_b32 s86, 16\n" \
"v_exp_f32 v154, v154\n" \
"v_mfma_f32_16x16x32_bf16 v[128:131], a[136:139], v[200:203], v[128:131]\n" \
"ds_read_b128 a[184:187], v240 offset:14336\n" \
"v_add_u32 v242, 0x10000, v240\n" \
"v_mfma_f32_16x16x32_bf16 v[128:131], a[140:143], v[204:207], v[128:131]\n" \
"s_waitcnt lgkmcnt(6)\n" \
"v_exp_f32 v155, v155\n" \
"v_mfma_f32_16x16x32_bf16 v[132:135], a[156:159], v[204:207], 0\n" \
"s_mov_b32 m0, s19\n" \
"ds_read_b128 a[180:183], v240 offset:13312\n" \
"v_cvt_pk_bf16_f32 v153, v154, v155\n" \
"v_mfma_f32_16x16x32_bf16 v[132:135], a[152:155], v[200:203], v[132:135]\n" \
"ds_read_b128 a[176:179], v240 offset:12288\n" \
"v_exp_f32 v156, v156\n" \
"v_mfma_f32_16x16x32_bf16 v[132:135], a[148:151], v[196:199], v[132:135]\n" \
"buffer_load_dwordx4 v248, s[32:35], s88 offen offset:0 sc1 lds\n" \
"v_mfma_f32_16x16x32_bf16 v[132:135], a[144:147], v[192:195], v[132:135]\n" \
"s_waitcnt lgkmcnt(4)\n" \
"v_exp_f32 v157, v157\n" \
"v_mfma_f32_16x16x32_bf16 v[136:139], a[160:163], v[192:195], 0\n" \
"v_cvt_pk_bf16_f32 v154, v156, v157\n" \
"v_mfma_f32_16x16x32_bf16 v[136:139], a[164:167], v[196:199], v[136:139]\n" \
"v_exp_f32 v158, v158\n" \
"v_mfma_f32_16x16x32_bf16 v[136:139], a[168:171], v[200:203], v[136:139]\n" \
"v_add_u32 v240, s86, v248\n" \
"v_mfma_f32_16x16x32_bf16 v[136:139], a[172:175], v[204:207], v[136:139]\n" \
"s_waitcnt lgkmcnt(0)\n" \
"v_exp_f32 v159, v159\n" \
"v_mfma_f32_16x16x32_bf16 v[140:143], a[188:191], v[204:207], 0\n" \
"v_cvt_pk_bf16_f32 v155, v158, v159\n" \
"v_mfma_f32_16x16x32_bf16 v[140:143], a[184:187], v[200:203], v[140:143]\n" \
"buffer_load_dwordx4 v248, s[32:35], s88 offen offset:1024 sc1 lds\n" \
"v_mfma_f32_16x16x32_bf16 v[140:143], a[180:183], v[196:199], v[140:143]\n" \
"v_exp_f32 v128, v128\n" \
"v_mfma_f32_16x16x32_bf16 v[140:143], a[176:179], v[192:195], v[140:143]\n" \
"v_mfma_f32_16x16x32_bf16 a[44:47], a[48:51], v[144:147], a[44:47]\n" \
"v_exp_f32 v129, v129\n" \
"v_mfma_f32_16x16x32_bf16 a[44:47], a[48:51], v[152:155], a[44:47]\n" \
"v_cvt_pk_bf16_f32 v128, v128, v129\n" \
"v_mfma_f32_16x16x32_bf16 v[96:99], a[224:227], v[152:155], v[96:99]\n" \
"v_exp_f32 v130, v130\n" \
"v_mfma_f32_16x16x32_bf16 v[96:99], a[192:195], v[144:147], v[96:99]\n" \
"ds_read_b128 a[224:227], v242 offset:8192\n" \
"v_mfma_f32_16x16x32_bf16 v[100:103], a[196:199], v[144:147], v[100:103]\n" \
"v_exp_f32 v131, v131\n" \
"v_mfma_f32_16x16x32_bf16 v[100:103], a[228:231], v[152:155], v[100:103]\n" \
"ds_read_b128 a[192:195], v242 offset:0\n" \
"v_cvt_pk_bf16_f32 v129, v130, v131\n" \
"v_mfma_f32_16x16x32_bf16 v[104:107], a[232:235], v[152:155], v[104:107]\n" \
"v_exp_f32 v132, v132\n" \
"v_mfma_f32_16x16x32_bf16 v[104:107], a[200:203], v[144:147], v[104:107]\n" \
"ds_read_b128 a[196:199], v242 offset:1024\n" \
"ds_read_b128 a[228:231], v242 offset:9216\n" \
"v_mfma_f32_16x16x32_bf16 v[108:111], a[204:207], v[144:147], v[108:111]\n" \
"v_exp_f32 v133, v133\n" \
"v_mfma_f32_16x16x32_bf16 v[108:111], a[236:239], v[152:155], v[108:111]\n" \
"ds_read_b128 a[232:235], v242 offset:10240\n" \
"v_cvt_pk_bf16_f32 v130, v132, v133\n" \
"v_mfma_f32_16x16x32_bf16 v[112:115], a[240:243], v[152:155], v[112:115]\n" \
"v_exp_f32 v134, v134\n" \
"v_mfma_f32_16x16x32_bf16 v[112:115], a[208:211], v[144:147], v[112:115]\n" \
"ds_read_b128 a[200:203], v242 offset:2048\n" \
"v_mfma_f32_16x16x32_bf16 v[116:119], a[212:215], v[144:147], v[116:119]\n" \
"v_exp_f32 v135, v135\n" \
"v_mfma_f32_16x16x32_bf16 v[116:119], a[244:247], v[152:155], v[116:119]\n" \
"ds_read_b128 a[204:207], v242 offset:3072\n" \
"v_cvt_pk_bf16_f32 v131, v134, v135\n" \
"v_mfma_f32_16x16x32_bf16 v[120:123], a[248:251], v[152:155], v[120:123]\n" \
"ds_read_b128 a[236:239], v242 offset:11264\n" \
"v_mfma_f32_16x16x32_bf16 v[120:123], a[216:219], v[144:147], v[120:123]\n" \
"v_exp_f32 v136, v136\n" \
"v_mfma_f32_16x16x32_bf16 v[124:127], a[220:223], v[144:147], v[124:127]\n" \
"ds_read_b128 a[240:243], v242 offset:12288\n" \
"v_mfma_f32_16x16x32_bf16 v[124:127], a[252:255], v[152:155], v[124:127]\n" \
"v_exp_f32 v137, v137\n" \
"v_mfma_f32_16x16x32_bf16 v[144:147], a[128:131], v[208:211], 0\n" \
"ds_read_b128 a[208:211], v242 offset:4096\n" \
"v_cvt_pk_bf16_f32 v136, v136, v137\n" \
"v_mfma_f32_16x16x32_bf16 v[144:147], a[132:135], v[212:215], v[144:147]\n" \
"v_exp_f32 v138, v138\n" \
"v_mfma_f32_16x16x32_bf16 v[144:147], a[136:139], v[216:219], v[144:147]\n" \
"ds_read_b128 a[212:215], v242 offset:5120\n" \
"ds_read_b128 a[244:247], v242 offset:13312\n" \
"v_mfma_f32_16x16x32_bf16 v[144:147], a[140:143], v[220:223], v[144:147]\n" \
"v_exp_f32 v139, v139\n" \
"v_mfma_f32_16x16x32_bf16 v[148:151], a[156:159], v[220:223], 0\n" \
"ds_read_b128 a[248:251], v242 offset:14336\n" \
"v_cvt_pk_bf16_f32 v137, v138, v139\n" \
"v_mfma_f32_16x16x32_bf16 v[148:151], a[152:155], v[216:219], v[148:151]\n" \
"v_exp_f32 v140, v140\n" \
"v_mfma_f32_16x16x32_bf16 v[148:151], a[148:151], v[212:215], v[148:151]\n" \
"ds_read_b128 a[216:219], v242 offset:6144\n" \
"v_mfma_f32_16x16x32_bf16 v[148:151], a[144:147], v[208:211], v[148:151]\n" \
"v_exp_f32 v141, v141\n" \
"v_mfma_f32_16x16x32_bf16 v[152:155], a[160:163], v[208:211], 0\n" \
"ds_read_b128 a[220:223], v242 offset:7168\n" \
"v_cvt_pk_bf16_f32 v138, v140, v141\n" \
"v_mfma_f32_16x16x32_bf16 v[152:155], a[164:167], v[212:215], v[152:155]\n" \
"v_exp_f32 v142, v142\n" \
"v_mfma_f32_16x16x32_bf16 v[152:155], a[168:171], v[216:219], v[152:155]\n" \
"ds_read_b128 a[252:255], v242 offset:15360\n" \
"v_mfma_f32_16x16x32_bf16 v[152:155], a[172:175], v[220:223], v[152:155]\n" \
"v_exp_f32 v143, v143\n" \
"v_mfma_f32_16x16x32_bf16 v[156:159], a[188:191], v[220:223], 0\n" \
"v_cvt_pk_bf16_f32 v139, v142, v143\n" \
"v_mfma_f32_16x16x32_bf16 v[156:159], a[184:187], v[216:219], v[156:159]\n" \
"buffer_load_dwordx4 v248, s[32:35], s88 offen offset:2048 sc1 lds\n" \
"v_mfma_f32_16x16x32_bf16 v[156:159], a[180:183], v[212:215], v[156:159]\n" \
"v_exp_f32 v144, v144\n" \
"v_mfma_f32_16x16x32_bf16 v[156:159], a[176:179], v[208:211], v[156:159]\n" \
"v_mfma_f32_16x16x32_bf16 a[32:35], a[48:51], v[128:131], a[32:35]\n" \
"v_exp_f32 v145, v145\n" \
"v_mfma_f32_16x16x32_bf16 a[32:35], a[48:51], v[136:139], a[32:35]\n" \
"s_waitcnt lgkmcnt(12)\n" \
"v_cvt_pk_bf16_f32 v144, v144, v145\n" \
"v_mfma_f32_16x16x32_bf16 v[0:3], a[224:227], v[136:139], v[0:3]\n" \
"v_exp_f32 v146, v146\n" \
"v_mfma_f32_16x16x32_bf16 v[0:3], a[192:195], v[128:131], v[0:3]\n" \
"v_mfma_f32_16x16x32_bf16 v[4:7], a[196:199], v[128:131], v[4:7]\n" \
"v_exp_f32 v147, v147\n" \
"v_mfma_f32_16x16x32_bf16 v[4:7], a[228:231], v[136:139], v[4:7]\n" \
"s_waitcnt lgkmcnt(8)\n" \
"v_cvt_pk_bf16_f32 v145, v146, v147\n" \
"v_mfma_f32_16x16x32_bf16 v[8:11], a[232:235], v[136:139], v[8:11]\n" \
"v_exp_f32 v148, v148\n" \
"v_mfma_f32_16x16x32_bf16 v[8:11], a[200:203], v[128:131], v[8:11]\n" \
"v_mfma_f32_16x16x32_bf16 v[12:15], a[204:207], v[128:131], v[12:15]\n" \
"v_exp_f32 v149, v149\n" \
"v_mfma_f32_16x16x32_bf16 v[12:15], a[236:239], v[136:139], v[12:15]\n" \
"s_waitcnt lgkmcnt(4)\n" \
"v_cvt_pk_bf16_f32 v146, v148, v149\n" \
"v_mfma_f32_16x16x32_bf16 v[16:19], a[240:243], v[136:139], v[16:19]\n" \
"v_exp_f32 v150, v150\n" \
"v_mfma_f32_16x16x32_bf16 v[16:19], a[208:211], v[128:131], v[16:19]\n" \
"v_mfma_f32_16x16x32_bf16 v[20:23], a[212:215], v[128:131], v[20:23]\n" \
"v_exp_f32 v151, v151\n" \
"v_mfma_f32_16x16x32_bf16 v[20:23], a[244:247], v[136:139], v[20:23]\n" \
"s_waitcnt lgkmcnt(0)\n" \
"v_cvt_pk_bf16_f32 v147, v150, v151\n" \
"v_mfma_f32_16x16x32_bf16 v[24:27], a[248:251], v[136:139], v[24:27]\n" \
"buffer_load_dwordx4 v248, s[32:35], s88 offen offset:3072 sc1 lds\n" \
"v_mfma_f32_16x16x32_bf16 v[24:27], a[216:219], v[128:131], v[24:27]\n" \
"s_add_u32 s89, s88, 589824\n" \
"v_exp_f32 v152, v152\n" \
"v_mfma_f32_16x16x32_bf16 v[28:31], a[220:223], v[128:131], v[28:31]\n" \
"s_add_u32 s88, s88, s27\n" \
"s_add_u32 s19, s19, 0x4000\n" \
"s_bitset0_b32 s19, 16\n" \
"v_mfma_f32_16x16x32_bf16 v[28:31], a[252:255], v[136:139], v[28:31]\n" \
"s_cmp_eq_u32 s88, s16\n" \
"v_exp_f32 v153, v153\n" \
"v_mfma_f32_16x16x32_bf16 v[128:131], a[128:131], v[160:163], 0\n" \
"s_cmov_b32 s88, s21\n" \
"s_cmov_b32 s27, s91\n" \
"v_cvt_pk_bf16_f32 v152, v152, v153\n" \
"v_mfma_f32_16x16x32_bf16 v[128:131], a[132:135], v[164:167], v[128:131]\n" \
"s_cmov_b32 s16, s77\n" \
"v_exp_f32 v154, v154\n" \
"v_mfma_f32_16x16x32_bf16 v[128:131], a[136:139], v[168:171], v[128:131]\n" \
"s_cmov_b32 s21, s92\n" \
"s_waitcnt vmcnt(4)\n" \
"s_cmovk_i32 s91, 0x4000\n" \
"v_mfma_f32_16x16x32_bf16 v[128:131], a[140:143], v[172:175], v[128:131]\n" \
"s_cmp_eq_u32 s88, s17\n" \
"v_exp_f32 v155, v155\n" \
"v_mfma_f32_16x16x32_bf16 v[132:135], a[156:159], v[172:175], 0\n" \
"s_cmov_b32 s88, s18\n" \
"v_cvt_pk_bf16_f32 v153, v154, v155\n" \
"v_mfma_f32_16x16x32_bf16 v[132:135], a[152:155], v[168:171], v[132:135]\n" \
"v_exp_f32 v156, v156\n" \
"v_mfma_f32_16x16x32_bf16 v[132:135], a[148:151], v[164:167], v[132:135]\n" \
"buffer_store_dwordx4 a[60:63], v232, s[32:35], s39 offen sc1\n" \
"v_mfma_f32_16x16x32_bf16 v[132:135], a[144:147], v[160:163], v[132:135]\n" \
"v_exp_f32 v157, v157\n" \
"v_mfma_f32_16x16x32_bf16 v[136:139], a[160:163], v[160:163], 0\n" \
"v_cvt_pk_bf16_f32 v154, v156, v157\n" \
"v_mfma_f32_16x16x32_bf16 v[136:139], a[164:167], v[164:167], v[136:139]\n" \
"v_exp_f32 v158, v158\n" \
"v_mfma_f32_16x16x32_bf16 v[136:139], a[168:171], v[168:171], v[136:139]\n" \
"v_mfma_f32_16x16x32_bf16 v[136:139], a[172:175], v[172:175], v[136:139]\n" \
"v_exp_f32 v159, v159\n" \
"v_mfma_f32_16x16x32_bf16 v[140:143], a[188:191], v[172:175], 0\n" \
"buffer_load_dwordx4 a[60:63], v230, s[40:43], s38 offen\n" \
"v_cvt_pk_bf16_f32 v155, v158, v159\n" \
"v_mfma_f32_16x16x32_bf16 v[140:143], a[184:187], v[168:171], v[140:143]\n" \
"v_mfma_f32_16x16x32_bf16 v[140:143], a[180:183], v[164:167], v[140:143]\n" \
"v_exp_f32 v128, v128\n" \
"v_mfma_f32_16x16x32_bf16 v[140:143], a[176:179], v[160:163], v[140:143]\n" \
"v_mfma_f32_16x16x32_bf16 a[36:39], a[48:51], v[144:147], a[36:39]\n" \
"s_mov_b32 m0, s87\n" \
"v_exp_f32 v129, v129\n" \
"v_mfma_f32_16x16x32_bf16 a[36:39], a[48:51], v[152:155], a[36:39]\n" \
"v_cvt_pk_bf16_f32 v128, v128, v129\n" \
"v_mfma_f32_16x16x32_bf16 v[32:35], a[224:227], v[152:155], v[32:35]\n" \
"buffer_load_dwordx4 v248, s[32:35], s93 offen sc1 lds\n" \
"v_mfma_f32_16x16x32_bf16 v[32:35], a[192:195], v[144:147], v[32:35]\n" \
"s_add_u32 s93, s93, 1024\n" \
"v_exp_f32 v130, v130\n" \
"v_mfma_f32_16x16x32_bf16 v[36:39], a[196:199], v[144:147], v[36:39]\n" \
"s_add_u32 s87, s87, 1024\n" \
"v_exp_f32 v131, v131\n" \
"v_mfma_f32_16x16x32_bf16 v[36:39], a[228:231], v[152:155], v[36:39]\n" \
"v_cvt_pk_bf16_f32 v129, v130, v131\n" \
"v_mfma_f32_16x16x32_bf16 v[40:43], a[232:235], v[152:155], v[40:43]\n" \
"v_exp_f32 v132, v132\n" \
"v_mfma_f32_16x16x32_bf16 v[40:43], a[200:203], v[144:147], v[40:43]\n" \
"v_mfma_f32_16x16x32_bf16 v[44:47], a[204:207], v[144:147], v[44:47]\n" \
"v_exp_f32 v133, v133\n" \
"v_mfma_f32_16x16x32_bf16 v[44:47], a[236:239], v[152:155], v[44:47]\n" \
"v_cvt_pk_bf16_f32 v130, v132, v133\n" \
"v_mfma_f32_16x16x32_bf16 v[48:51], a[240:243], v[152:155], v[48:51]\n" \
"v_exp_f32 v134, v134\n" \
"v_mfma_f32_16x16x32_bf16 v[48:51], a[208:211], v[144:147], v[48:51]\n" \
"buffer_load_dword v245, v231, s[60:63], s72 offen\n" \
"v_mfma_f32_16x16x32_bf16 v[52:55], a[212:215], v[144:147], v[52:55]\n" \
"s_add_u32 s38, s38, 512\n" \
"v_exp_f32 v135, v135\n" \
"v_mfma_f32_16x16x32_bf16 v[52:55], a[244:247], v[152:155], v[52:55]\n" \
"s_add_u32 s39, s39, 0x80000\n" \
"s_add_u32 s72, s72, 512\n" \
"v_cvt_pk_bf16_f32 v131, v134, v135\n" \
"v_mfma_f32_16x16x32_bf16 v[56:59], a[248:251], v[152:155], v[56:59]\n" \
"v_mfma_f32_16x16x32_bf16 v[56:59], a[216:219], v[144:147], v[56:59]\n" \
"v_exp_f32 v136, v136\n" \
"v_mfma_f32_16x16x32_bf16 v[60:63], a[220:223], v[144:147], v[60:63]\n" \
"v_mfma_f32_16x16x32_bf16 v[60:63], a[252:255], v[152:155], v[60:63]\n" \
".Lst0_11_%=:\n" \
"s_cbranch_execz .Lst0_15_%=\n" \
"s_waitcnt vmcnt(16)\n" \
"v_exp_f32 v137, v137\n" \
"v_mfma_f32_16x16x32_bf16 v[144:147], a[128:131], v[176:179], 0\n" \
"s_barrier\n" \
"s_mov_b32 m0, s20\n" \
"v_cvt_pk_bf16_f32 v136, v136, v137\n" \
"v_mfma_f32_16x16x32_bf16 v[144:147], a[132:135], v[180:183], v[144:147]\n" \
"v_exp_f32 v138, v138\n" \
"v_mfma_f32_16x16x32_bf16 v[144:147], a[136:139], v[184:187], v[144:147]\n" \
"buffer_load_dwordx4 v248, s[32:35], s89 offen offset:0 sc1 lds\n" \
"v_mfma_f32_16x16x32_bf16 v[144:147], a[140:143], v[188:191], v[144:147]\n" \
"v_exp_f32 v139, v139\n" \
"v_mfma_f32_16x16x32_bf16 v[148:151], a[156:159], v[188:191], 0\n" \
"ds_read_b128 a[128:131], v240 offset:0\n" \
"v_cvt_pk_bf16_f32 v137, v138, v139\n" \
"v_mfma_f32_16x16x32_bf16 v[148:151], a[152:155], v[184:187], v[148:151]\n" \
"v_exp_f32 v140, v140\n" \
"v_mfma_f32_16x16x32_bf16 v[148:151], a[148:151], v[180:183], v[148:151]\n" \
"ds_read_b128 a[132:135], v240 offset:1024\n" \
"ds_read_b128 a[136:139], v240 offset:2048\n" \
"v_mfma_f32_16x16x32_bf16 v[148:151], a[144:147], v[176:179], v[148:151]\n" \
"v_exp_f32 v141, v141\n" \
"v_mfma_f32_16x16x32_bf16 v[152:155], a[160:163], v[176:179], 0\n" \
"buffer_load_dwordx4 v248, s[32:35], s89 offen offset:1024 sc1 lds\n" \
"v_mfma_f32_16x16x32_bf16 v[152:155], a[164:167], v[180:183], v[152:155]\n" \
"v_exp_f32 v142, v142\n" \
"v_mfma_f32_16x16x32_bf16 v[152:155], a[168:171], v[184:187], v[152:155]\n" \
"ds_read_b128 a[140:143], v240 offset:3072\n" \
"v_cvt_pk_bf16_f32 v138, v140, v141\n" \
"v_mfma_f32_16x16x32_bf16 v[152:155], a[172:175], v[188:191], v[152:155]\n" \
"v_exp_f32 v143, v143\n" \
"v_mfma_f32_16x16x32_bf16 v[156:159], a[188:191], v[188:191], 0\n" \
"ds_read_b128 a[156:159], v240 offset:7168\n" \
"v_cvt_pk_bf16_f32 v139, v142, v143\n" \
"v_mfma_f32_16x16x32_bf16 v[156:159], a[184:187], v[184:187], v[156:159]\n" \
"ds_read_b128 a[152:155], v240 offset:6144\n" \
"ds_read_b128 a[148:151], v240 offset:5120\n" \
"v_mfma_f32_16x16x32_bf16 v[156:159], a[180:183], v[180:183], v[156:159]\n" \
"v_exp_f32 v144, v144\n" \
"v_mfma_f32_16x16x32_bf16 v[156:159], a[176:179], v[176:179], v[156:159]\n" \
"buffer_load_dwordx4 v248, s[32:35], s89 offen offset:2048 sc1 lds\n" \
"v_mfma_f32_16x16x32_bf16 a[40:43], a[48:51], v[128:131], a[40:43]\n" \
"v_exp_f32 v145, v145\n" \
"v_mfma_f32_16x16x32_bf16 a[40:43], a[48:51], v[136:139], a[40:43]\n" \
"ds_read_b128 a[144:147], v240 offset:4096\n" \
"v_cvt_pk_bf16_f32 v144, v144, v145\n" \
"v_mfma_f32_16x16x32_bf16 v[64:67], a[224:227], v[136:139], v[64:67]\n" \
"v_exp_f32 v146, v146\n" \
"v_mfma_f32_16x16x32_bf16 v[64:67], a[192:195], v[128:131], v[64:67]\n" \
"ds_read_b128 a[160:163], v240 offset:8192\n" \
"ds_read_b128 a[164:167], v240 offset:9216\n" \
"v_mfma_f32_16x16x32_bf16 v[68:71], a[196:199], v[128:131], v[68:71]\n" \
"v_exp_f32 v147, v147\n" \
"v_mfma_f32_16x16x32_bf16 v[68:71], a[228:231], v[136:139], v[68:71]\n" \
"buffer_load_dwordx4 v248, s[32:35], s89 offen offset:3072 sc1 lds\n" \
"v_mfma_f32_16x16x32_bf16 v[72:75], a[232:235], v[136:139], v[72:75]\n" \
"s_add_u32 s20, s20, 0x4000\n" \
"v_exp_f32 v148, v148\n" \
"v_mfma_f32_16x16x32_bf16 v[72:75], a[200:203], v[128:131], v[72:75]\n" \
"s_and_b32 s20, s20, 0xffff\n" \
"ds_read_b128 a[168:171], v240 offset:10240\n" \
"v_cvt_pk_bf16_f32 v145, v146, v147\n" \
"v_mfma_f32_16x16x32_bf16 v[76:79], a[204:207], v[128:131], v[76:79]\n" \
"s_bitset1_b32 s20, 16\n" \
"v_exp_f32 v149, v149\n" \
"v_mfma_f32_16x16x32_bf16 v[76:79], a[236:239], v[136:139], v[76:79]\n" \
"ds_read_b128 a[172:175], v240 offset:11264\n" \
"v_cvt_pk_bf16_f32 v146, v148, v149\n" \
"v_mfma_f32_16x16x32_bf16 v[80:83], a[240:243], v[136:139], v[80:83]\n" \
"v_exp_f32 v150, v150\n" \
"v_mfma_f32_16x16x32_bf16 v[80:83], a[208:211], v[128:131], v[80:83]\n" \
"v_mfma_f32_16x16x32_bf16 v[84:87], a[212:215], v[128:131], v[84:87]\n" \
"s_sub_u32 s95, s95, 1\n" \
"v_exp_f32 v151, v151\n" \
"v_mfma_f32_16x16x32_bf16 v[84:87], a[244:247], v[136:139], v[84:87]\n" \
"v_cvt_pk_bf16_f32 v147, v150, v151\n" \
"v_mfma_f32_16x16x32_bf16 v[88:91], a[248:251], v[136:139], v[88:91]\n" \
"v_mfma_f32_16x16x32_bf16 v[88:91], a[216:219], v[128:131], v[88:91]\n" \
"v_exp_f32 v152, v152\n" \
"v_mfma_f32_16x16x32_bf16 v[92:95], a[220:223], v[128:131], v[92:95]\n" \
"v_exp_f32 v153, v153\n" \
"v_mfma_f32_16x16x32_bf16 v[92:95], a[252:255], v[136:139], v[92:95]\n" \
"s_waitcnt lgkmcnt(8)\n" \
"s_cbranch_scc0 .Lloop_%=\n" \
"s_sub_u32 s95, s67, s84\n" \
"s_lshl_b32 s95, s95, 1\n" \
"s_sub_u32 s95, s95, 8\n" \
"s_mov_b32 s76, 1\n" \
".Lld_%=:\n" \
".Lst0_12_%=:\n" \
"s_cbranch_execz .Lstb0_%=\n" \
"s_sub_u32 s95, s95, 1\n" \
"s_cbranch_scc1 .Lhook_%=\n" \
".p2align 3\n" \
".Lloop1_%=:\n" \
  \
"v_mfma_f32_16x16x32_bf16 v[128:131], a[128:131], v[192:195], 0\n" \
"s_add_u32 s86, s86, 0x4000\n" \
"ds_read_b128 a[188:191], v240 offset:15360\n" \
"v_cvt_pk_bf16_f32 v152, v152, v153\n" \
"v_mfma_f32_16x16x32_bf16 v[128:131], a[132:135], v[196:199], v[128:131]\n" \
"s_bitset0_b32 s86, 16\n" \
"v_exp_f32 v154, v154\n" \
"v_mfma_f32_16x16x32_bf16 v[128:131], a[136:139], v[200:203], v[128:131]\n" \
"s_cmp_eq_u32 s88, s24\n" \
"s_cbranch_scc1 .Lchd_%=\n" \
".Lsbhd_%=:\n" \
"v_mfma_f32_16x16x32_bf16 v[128:131], a[140:143], v[204:207], v[128:131]\n" \
"s_waitcnt lgkmcnt(5)\n" \
"v_exp_f32 v155, v155\n" \
"v_mfma_f32_16x16x32_bf16 v[132:135], a[156:159], v[204:207], 0\n" \
"s_mov_b32 m0, s19\n" \
"v_add_u32 v242, 0x10000, v240\n" \
"v_cvt_pk_bf16_f32 v153, v154, v155\n" \
"v_mfma_f32_16x16x32_bf16 v[132:135], a[152:155], v[200:203], v[132:135]\n" \
"ds_read_b128 a[184:187], v240 offset:14336\n" \
"ds_read_b128 a[180:183], v240 offset:13312\n" \
"ds_read_b128 a[176:179], v240 offset:12288\n" \
"v_exp_f32 v156, v156\n" \
"v_mfma_f32_16x16x32_bf16 v[132:135], a[148:151], v[196:199], v[132:135]\n" \
"buffer_load_dwordx4 v248, s[32:35], s88 offen offset:0 sc1 lds\n" \
"v_mfma_f32_16x16x32_bf16 v[132:135], a[144:147], v[192:195], v[132:135]\n" \
"s_waitcnt lgkmcnt(4)\n" \
"v_exp_f32 v157, v157\n" \
"v_mfma_f32_16x16x32_bf16 v[136:139], a[160:163], v[192:195], 0\n" \
"v_cvt_pk_bf16_f32 v154, v156, v157\n" \
"v_mfma_f32_16x16x32_bf16 v[136:139], a[164:167], v[196:199], v[136:139]\n" \
"v_exp_f32 v158, v158\n" \
"v_mfma_f32_16x16x32_bf16 v[136:139], a[168:171], v[200:203], v[136:139]\n" \
"v_add_u32 v240, s86, v248\n" \
"v_mfma_f32_16x16x32_bf16 v[136:139], a[172:175], v[204:207], v[136:139]\n" \
"s_waitcnt lgkmcnt(0)\n" \
"v_exp_f32 v159, v159\n" \
"v_mfma_f32_16x16x32_bf16 v[140:143], a[188:191], v[204:207], 0\n" \
"v_cvt_pk_bf16_f32 v155, v158, v159\n" \
"v_mfma_f32_16x16x32_bf16 v[140:143], a[184:187], v[200:203], v[140:143]\n" \
"buffer_load_dwordx4 v248, s[32:35], s88 offen offset:1024 sc1 lds\n" \
"v_mfma_f32_16x16x32_bf16 v[140:143], a[180:183], v[196:199], v[140:143]\n" \
"v_exp_f32 v128, v128\n" \
"v_mfma_f32_16x16x32_bf16 v[140:143], a[176:179], v[192:195], v[140:143]\n" \
"v_mfma_f32_16x16x32_bf16 a[44:47], a[48:51], v[144:147], a[44:47]\n" \
"v_exp_f32 v129, v129\n" \
"v_mfma_f32_16x16x32_bf16 a[44:47], a[48:51], v[152:155], a[44:47]\n" \
"v_cvt_pk_bf16_f32 v128, v128, v129\n" \
"v_mfma_f32_16x16x32_bf16 v[96:99], a[224:227], v[152:155], v[96:99]\n" \
"v_exp_f32 v130, v130\n" \
"v_mfma_f32_16x16x32_bf16 v[96:99], a[192:195], v[144:147], v[96:99]\n" \
"ds_read_b128 a[224:227], v242 offset:8192\n" \
"v_mfma_f32_16x16x32_bf16 v[100:103], a[196:199], v[144:147], v[100:103]\n" \
"v_exp_f32 v131, v131\n" \
"v_mfma_f32_16x16x32_bf16 v[100:103], a[228:231], v[152:155], v[100:103]\n" \
"ds_read_b128 a[192:195], v242 offset:0\n" \
"v_cvt_pk_bf16_f32 v129, v130, v131\n" \
"v_mfma_f32_16x16x32_bf16 v[104:107], a[232:235], v[152:155], v[104:107]\n" \
"v_exp_f32 v132, v132\n" \
"v_mfma_f32_16x16x32_bf16 v[104:107], a[200:203], v[144:147], v[104:107]\n" \
"ds_read_b128 a[196:199], v242 offset:1024\n" \
"ds_read_b128 a[228:231], v242 offset:9216\n" \
"v_mfma_f32_16x16x32_bf16 v[108:111], a[204:207], v[144:147], v[108:111]\n" \
"v_exp_f32 v133, v133\n" \
"v_mfma_f32_16x16x32_bf16 v[108:111], a[236:239], v[152:155], v[108:111]\n" \
"ds_read_b128 a[232:235], v242 offset:10240\n" \
"v_cvt_pk_bf16_f32 v130, v132, v133\n" \
"v_mfma_f32_16x16x32_bf16 v[112:115], a[240:243], v[152:155], v[112:115]\n" \
"v_exp_f32 v134, v134\n" \
"v_mfma_f32_16x16x32_bf16 v[112:115], a[208:211], v[144:147], v[112:115]\n" \
"ds_read_b128 a[200:203], v242 offset:2048\n" \
"v_mfma_f32_16x16x32_bf16 v[116:119], a[212:215], v[144:147], v[116:119]\n" \
"v_exp_f32 v135, v135\n" \
"v_mfma_f32_16x16x32_bf16 v[116:119], a[244:247], v[152:155], v[116:119]\n" \
"ds_read_b128 a[204:207], v242 offset:3072\n" \
"v_cvt_pk_bf16_f32 v131, v134, v135\n" \
"v_mfma_f32_16x16x32_bf16 v[120:123], a[248:251], v[152:155], v[120:123]\n" \
"ds_read_b128 a[236:239], v242 offset:11264\n" \
"v_mfma_f32_16x16x32_bf16 v[120:123], a[216:219], v[144:147], v[120:123]\n" \
"v_exp_f32 v136, v136\n" \
"v_mfma_f32_16x16x32_bf16 v[124:127], a[220:223], v[144:147], v[124:127]\n" \
"ds_read_b128 a[240:243], v242 offset:12288\n" \
"v_mfma_f32_16x16x32_bf16 v[124:127], a[252:255], v[152:155], v[124:127]\n" \
"v_exp_f32 v137, v137\n" \
"v_mfma_f32_16x16x32_bf16 v[144:147], a[128:131], v[208:211], 0\n" \
"ds_read_b128 a[208:211], v242 offset:4096\n" \
"v_cvt_pk_bf16_f32 v136, v136, v137\n" \
"v_mfma_f32_16x16x32_bf16 v[144:147], a[132:135], v[212:215], v[144:147]\n" \
"v_exp_f32 v138, v138\n" \
"v_mfma_f32_16x16x32_bf16 v[144:147], a[136:139], v[216:219], v[144:147]\n" \
"ds_read_b128 a[212:215], v242 offset:5120\n" \
"ds_read_b128 a[244:247], v242 offset:13312\n" \
"v_mfma_f32_16x16x32_bf16 v[144:147], a[140:143], v[220:223], v[144:147]\n" \
"v_exp_f32 v139, v139\n" \
"v_mfma_f32_16x16x32_bf16 v[148:151], a[156:159], v[220:223], 0\n" \
"ds_read_b128 a[248:251], v242 offset:14336\n" \
"v_cvt_pk_bf16_f32 v137, v138, v139\n" \
"v_mfma_f32_16x16x32_bf16 v[148:151], a[152:155], v[216:219], v[148:151]\n" \
"v_exp_f32 v140, v140\n" \
"v_mfma_f32_16x16x32_bf16 v[148:151], a[148:151], v[212:215], v[148:151]\n" \
"ds_read_b128 a[216:219], v242 offset:6144\n" \
"v_mfma_f32_16x16x32_bf16 v[148:151], a[144:147], v[208:211], v[148:151]\n" \
"v_exp_f32 v141, v141\n" \
"v_mfma_f32_16x16x32_bf16 v[152:155], a[160:163], v[208:211], 0\n" \
"ds_read_b128 a[220:223], v242 offset:7168\n" \
"v_cvt_pk_bf16_f32 v138, v140, v141\n" \
"v_mfma_f32_16x16x32_bf16 v[152:155], a[164:167], v[212:215], v[152:155]\n" \
"v_exp_f32 v142, v142\n" \
"v_mfma_f32_16x16x32_bf16 v[152:155], a[168:171], v[216:219], v[152:155]\n" \
"ds_read_b128 a[252:255], v242 offset:15360\n" \
"v_mfma_f32_16x16x32_bf16 v[152:155], a[172:175], v[220:223], v[152:155]\n" \
"v_exp_f32 v143, v143\n" \
"v_mfma_f32_16x16x32_bf16 v[156:159], a[188:191], v[220:223], 0\n" \
"v_cvt_pk_bf16_f32 v139, v142, v143\n" \
"v_mfma_f32_16x16x32_bf16 v[156:159], a[184:187], v[216:219], v[156:159]\n" \
"buffer_load_dwordx4 v248, s[32:35], s88 offen offset:2048 sc1 lds\n" \
"v_mfma_f32_16x16x32_bf16 v[156:159], a[180:183], v[212:215], v[156:159]\n" \
"v_exp_f32 v144, v144\n" \
"v_mfma_f32_16x16x32_bf16 v[156:159], a[176:179], v[208:211], v[156:159]\n" \
"v_mfma_f32_16x16x32_bf16 a[32:35], a[48:51], v[128:131], a[32:35]\n" \
"v_exp_f32 v145, v145\n" \
"v_mfma_f32_16x16x32_bf16 a[32:35], a[48:51], v[136:139], a[32:35]\n" \
"s_waitcnt lgkmcnt(12)\n" \
"v_cvt_pk_bf16_f32 v144, v144, v145\n" \
"v_mfma_f32_16x16x32_bf16 v[0:3], a[224:227], v[136:139], v[0:3]\n" \
"v_exp_f32 v146, v146\n" \
"v_mfma_f32_16x16x32_bf16 v[0:3], a[192:195], v[128:131], v[0:3]\n" \
"v_mfma_f32_16x16x32_bf16 v[4:7], a[196:199], v[128:131], v[4:7]\n" \
"v_exp_f32 v147, v147\n" \
"v_mfma_f32_16x16x32_bf16 v[4:7], a[228:231], v[136:139], v[4:7]\n" \
"s_waitcnt lgkmcnt(8)\n" \
"v_cvt_pk_bf16_f32 v145, v146, v147\n" \
"v_mfma_f32_16x16x32_bf16 v[8:11], a[232:235], v[136:139], v[8:11]\n" \
"v_exp_f32 v148, v148\n" \
"v_mfma_f32_16x16x32_bf16 v[8:11], a[200:203], v[128:131], v[8:11]\n" \
"v_mfma_f32_16x16x32_bf16 v[12:15], a[204:207], v[128:131], v[12:15]\n" \
"v_exp_f32 v149, v149\n" \
"v_mfma_f32_16x16x32_bf16 v[12:15], a[236:239], v[136:139], v[12:15]\n" \
"s_waitcnt lgkmcnt(4)\n" \
"v_cvt_pk_bf16_f32 v146, v148, v149\n" \
"v_mfma_f32_16x16x32_bf16 v[16:19], a[240:243], v[136:139], v[16:19]\n" \
"v_exp_f32 v150, v150\n" \
"v_mfma_f32_16x16x32_bf16 v[16:19], a[208:211], v[128:131], v[16:19]\n" \
"v_mfma_f32_16x16x32_bf16 v[20:23], a[212:215], v[128:131], v[20:23]\n" \
"v_exp_f32 v151, v151\n" \
"v_mfma_f32_16x16x32_bf16 v[20:23], a[244:247], v[136:139], v[20:23]\n" \
"s_waitcnt lgkmcnt(0)\n" \
"v_cvt_pk_bf16_f32 v147, v150, v151\n" \
"v_mfma_f32_16x16x32_bf16 v[24:27], a[248:251], v[136:139], v[24:27]\n" \
"buffer_load_dwordx4 v248, s[32:35], s88 offen offset:3072 sc1 lds\n" \
"v_mfma_f32_16x16x32_bf16 v[24:27], a[216:219], v[128:131], v[24:27]\n" \
"s_add_u32 s89, s88, 589824\n" \
"v_exp_f32 v152, v152\n" \
"v_mfma_f32_16x16x32_bf16 v[28:31], a[220:223], v[128:131], v[28:31]\n" \
"s_add_u32 s88, s88, s27\n" \
"s_add_u32 s19, s19, 0x4000\n" \
"s_bitset0_b32 s19, 16\n" \
"v_mfma_f32_16x16x32_bf16 v[28:31], a[252:255], v[136:139], v[28:31]\n" \
"s_cmp_eq_u32 s88, s16\n" \
"v_exp_f32 v153, v153\n" \
"v_mfma_f32_16x16x32_bf16 v[128:131], a[128:131], v[160:163], 0\n" \
"s_cmov_b32 s88, s21\n" \
"s_cmov_b32 s27, s91\n" \
"v_cvt_pk_bf16_f32 v152, v152, v153\n" \
"v_mfma_f32_16x16x32_bf16 v[128:131], a[132:135], v[164:167], v[128:131]\n" \
"s_cmov_b32 s16, s77\n" \
"v_exp_f32 v154, v154\n" \
"v_mfma_f32_16x16x32_bf16 v[128:131], a[136:139], v[168:171], v[128:131]\n" \
"s_cmov_b32 s21, s92\n" \
"s_cmovk_i32 s91, 0x4000\n" \
"s_cmp_eq_u32 s88, s17\n" \
"v_mfma_f32_16x16x32_bf16 v[128:131], a[140:143], v[172:175], v[128:131]\n" \
"s_cmov_b32 s88, s18\n" \
"v_exp_f32 v155, v155\n" \
"v_mfma_f32_16x16x32_bf16 v[132:135], a[156:159], v[172:175], 0\n" \
"s_cmp_lg_u32 s76, 0\n" \
"s_cbranch_scc1 .Land_%=\n" \
".Labd_%=:\n" \
"v_mfma_f32_16x16x32_bf16 v[132:135], a[152:155], v[168:171], v[132:135]\n" \
"v_exp_f32 v156, v156\n" \
"v_mfma_f32_16x16x32_bf16 v[132:135], a[148:151], v[164:167], v[132:135]\n" \
"v_cvt_pk_bf16_f32 v153, v154, v155\n" \
"v_mfma_f32_16x16x32_bf16 v[132:135], a[144:147], v[160:163], v[132:135]\n" \
"v_exp_f32 v157, v157\n" \
"v_mfma_f32_16x16x32_bf16 v[136:139], a[160:163], v[160:163], 0\n" \
"v_cvt_pk_bf16_f32 v154, v156, v157\n" \
"v_mfma_f32_16x16x32_bf16 v[136:139], a[164:167], v[164:167], v[136:139]\n" \
"v_exp_f32 v158, v158\n" \
"v_mfma_f32_16x16x32_bf16 v[136:139], a[168:171], v[168:171], v[136:139]\n" \
"v_mfma_f32_16x16x32_bf16 v[136:139], a[172:175], v[172:175], v[136:139]\n" \
"v_exp_f32 v159, v159\n" \
"v_mfma_f32_16x16x32_bf16 v[140:143], a[188:191], v[172:175], 0\n" \
"v_cvt_pk_bf16_f32 v155, v158, v159\n" \
"v_mfma_f32_16x16x32_bf16 v[140:143], a[184:187], v[168:171], v[140:143]\n" \
"v_mfma_f32_16x16x32_bf16 v[140:143], a[180:183], v[164:167], v[140:143]\n" \
"v_exp_f32 v128, v128\n" \
"v_mfma_f32_16x16x32_bf16 v[140:143], a[176:179], v[160:163], v[140:143]\n" \
"v_mfma_f32_16x16x32_bf16 a[36:39], a[48:51], v[144:147], a[36:39]\n" \
"v_exp_f32 v129, v129\n" \
"v_mfma_f32_16x16x32_bf16 a[36:39], a[48:51], v[152:155], a[36:39]\n" \
"v_cvt_pk_bf16_f32 v128, v128, v129\n" \
"v_mfma_f32_16x16x32_bf16 v[32:35], a[224:227], v[152:155], v[32:35]\n" \
"v_exp_f32 v130, v130\n" \
"v_mfma_f32_16x16x32_bf16 v[32:35], a[192:195], v[144:147], v[32:35]\n" \
"v_mfma_f32_16x16x32_bf16 v[36:39], a[196:199], v[144:147], v[36:39]\n" \
"v_exp_f32 v131, v131\n" \
"v_mfma_f32_16x16x32_bf16 v[36:39], a[228:231], v[152:155], v[36:39]\n" \
"v_cvt_pk_bf16_f32 v129, v130, v131\n" \
"v_mfma_f32_16x16x32_bf16 v[40:43], a[232:235], v[152:155], v[40:43]\n" \
"v_exp_f32 v132, v132\n" \
"v_mfma_f32_16x16x32_bf16 v[40:43], a[200:203], v[144:147], v[40:43]\n" \
"v_mfma_f32_16x16x32_bf16 v[44:47], a[204:207], v[144:147], v[44:47]\n" \
"v_exp_f32 v133, v133\n" \
"v_mfma_f32_16x16x32_bf16 v[44:47], a[236:239], v[152:155], v[44:47]\n" \
"v_cvt_pk_bf16_f32 v130, v132, v133\n" \
"v_mfma_f32_16x16x32_bf16 v[48:51], a[240:243], v[152:155], v[48:51]\n" \
"v_exp_f32 v134, v134\n" \
"v_mfma_f32_16x16x32_bf16 v[48:51], a[208:211], v[144:147], v[48:51]\n" \
"v_mfma_f32_16x16x32_bf16 v[52:55], a[212:215], v[144:147], v[52:55]\n" \
"v_exp_f32 v135, v135\n" \
"v_mfma_f32_16x16x32_bf16 v[52:55], a[244:247], v[152:155], v[52:55]\n" \
"v_cvt_pk_bf16_f32 v131, v134, v135\n" \
"v_mfma_f32_16x16x32_bf16 v[56:59], a[248:251], v[152:155], v[56:59]\n" \
"v_mfma_f32_16x16x32_bf16 v[56:59], a[216:219], v[144:147], v[56:59]\n" \
"v_exp_f32 v136, v136\n" \
"v_mfma_f32_16x16x32_bf16 v[60:63], a[220:223], v[144:147], v[60:63]\n" \
"v_mfma_f32_16x16x32_bf16 v[60:63], a[252:255], v[152:155], v[60:63]\n" \
"s_waitcnt vmcnt(12)\n" \
"v_exp_f32 v137, v137\n" \
"v_mfma_f32_16x16x32_bf16 v[144:147], a[128:131], v[176:179], 0\n" \
"s_barrier\n" \
"s_mov_b32 m0, s20\n" \
"v_cvt_pk_bf16_f32 v136, v136, v137\n" \
"v_mfma_f32_16x16x32_bf16 v[144:147], a[132:135], v[180:183], v[144:147]\n" \
"v_exp_f32 v138, v138\n" \
"v_mfma_f32_16x16x32_bf16 v[144:147], a[136:139], v[184:187], v[144:147]\n" \
"buffer_load_dwordx4 v248, s[32:35], s89 offen offset:0 sc1 lds\n" \
"v_mfma_f32_16x16x32_bf16 v[144:147], a[140:143], v[188:191], v[144:147]\n" \
"v_exp_f32 v139, v139\n" \
"v_mfma_f32_16x16x32_bf16 v[148:151], a[156:159], v[188:191], 0\n" \
"ds_read_b128 a[128:131], v240 offset:0\n" \
"v_cvt_pk_bf16_f32 v137, v138, v139\n" \
"v_mfma_f32_16x16x32_bf16 v[148:151], a[152:155], v[184:187], v[148:151]\n" \
"v_exp_f32 v140, v140\n" \
"v_mfma_f32_16x16x32_bf16 v[148:151], a[148:151], v[180:183], v[148:151]\n" \
"ds_read_b128 a[132:135], v240 offset:1024\n" \
"ds_read_b128 a[136:139], v240 offset:2048\n" \
"v_mfma_f32_16x16x32_bf16 v[148:151], a[144:147], v[176:179], v[148:151]\n" \
"v_exp_f32 v141, v141\n" \
"v_mfma_f32_16x16x32_bf16 v[152:155], a[160:163], v[176:179], 0\n" \
"buffer_load_dwordx4 v248, s[32:35], s89 offen offset:1024 sc1 lds\n" \
"v_mfma_f32_16x16x32_bf16 v[152:155], a[164:167], v[180:183], v[152:155]\n" \
"v_exp_f32 v142, v142\n" \
"v_mfma_f32_16x16x32_bf16 v[152:155], a[168:171], v[184:187], v[152:155]\n" \
"ds_read_b128 a[140:143], v240 offset:3072\n" \
"v_cvt_pk_bf16_f32 v138, v140, v141\n" \
"v_mfma_f32_16x16x32_bf16 v[152:155], a[172:175], v[188:191], v[152:155]\n" \
"v_exp_f32 v143, v143\n" \
"v_mfma_f32_16x16x32_bf16 v[156:159], a[188:191], v[188:191], 0\n" \
"ds_read_b128 a[156:159], v240 offset:7168\n" \
"v_cvt_pk_bf16_f32 v139, v142, v143\n" \
"v_mfma_f32_16x16x32_bf16 v[156:159], a[184:187], v[184:187], v[156:159]\n" \
"ds_read_b128 a[152:155], v240 offset:6144\n" \
"ds_read_b128 a[148:151], v240 offset:5120\n" \
"v_mfma_f32_16x16x32_bf16 v[156:159], a[180:183], v[180:183], v[156:159]\n" \
"v_exp_f32 v144, v144\n" \
"v_mfma_f32_16x16x32_bf16 v[156:159], a[176:179], v[176:179], v[156:159]\n" \
"buffer_load_dwordx4 v248, s[32:35], s89 offen offset:2048 sc1 lds\n" \
"v_mfma_f32_16x16x32_bf16 a[40:43], a[48:51], v[128:131], a[40:43]\n" \
"v_exp_f32 v145, v145\n" \
"v_mfma_f32_16x16x32_bf16 a[40:43], a[48:51], v[136:139], a[40:43]\n" \
"ds_read_b128 a[144:147], v240 offset:4096\n" \
"v_cvt_pk_bf16_f32 v144, v144, v145\n" \
"v_mfma_f32_16x16x32_bf16 v[64:67], a[224:227], v[136:139], v[64:67]\n" \
"v_exp_f32 v146, v146\n" \
"v_mfma_f32_16x16x32_bf16 v[64:67], a[192:195], v[128:131], v[64:67]\n" \
"ds_read_b128 a[160:163], v240 offset:8192\n" \
"ds_read_b128 a[164:167], v240 offset:9216\n" \
"v_mfma_f32_16x16x32_bf16 v[68:71], a[196:199], v[128:131], v[68:71]\n" \
"v_exp_f32 v147, v147\n" \
"v_mfma_f32_16x16x32_bf16 v[68:71], a[228:231], v[136:139], v[68:71]\n" \
"buffer_load_dwordx4 v248, s[32:35], s89 offen offset:3072 sc1 lds\n" \
"v_mfma_f32_16x16x32_bf16 v[72:75], a[232:235], v[136:139], v[72:75]\n" \
"s_add_u32 s20, s20, 0x4000\n" \
"v_exp_f32 v148, v148\n" \
"v_mfma_f32_16x16x32_bf16 v[72:75], a[200:203], v[128:131], v[72:75]\n" \
"s_and_b32 s20, s20, 0xffff\n" \
"ds_read_b128 a[168:171], v240 offset:10240\n" \
"v_cvt_pk_bf16_f32 v145, v146, v147\n" \
"v_mfma_f32_16x16x32_bf16 v[76:79], a[204:207], v[128:131], v[76:79]\n" \
"s_bitset1_b32 s20, 16\n" \
"v_exp_f32 v149, v149\n" \
"v_mfma_f32_16x16x32_bf16 v[76:79], a[236:239], v[136:139], v[76:79]\n" \
"ds_read_b128 a[172:175], v240 offset:11264\n" \
"v_cvt_pk_bf16_f32 v146, v148, v149\n" \
"v_mfma_f32_16x16x32_bf16 v[80:83], a[240:243], v[136:139], v[80:83]\n" \
"v_exp_f32 v150, v150\n" \
"v_mfma_f32_16x16x32_bf16 v[80:83], a[208:211], v[128:131], v[80:83]\n" \
"v_mfma_f32_16x16x32_bf16 v[84:87], a[212:215], v[128:131], v[84:87]\n" \
"s_sub_u32 s95, s95, 1\n" \
"v_exp_f32 v151, v151\n" \
"v_mfma_f32_16x16x32_bf16 v[84:87], a[244:247], v[136:139], v[84:87]\n" \
"v_cvt_pk_bf16_f32 v147, v150, v151\n" \
"v_mfma_f32_16x16x32_bf16 v[88:91], a[248:251], v[136:139], v[88:91]\n" \
"v_mfma_f32_16x16x32_bf16 v[88:91], a[216:219], v[128:131], v[88:91]\n" \
"v_exp_f32 v152, v152\n" \
"v_mfma_f32_16x16x32_bf16 v[92:95], a[220:223], v[128:131], v[92:95]\n" \
"v_exp_f32 v153, v153\n" \
"v_mfma_f32_16x16x32_bf16 v[92:95], a[252:255], v[136:139], v[92:95]\n" \
"s_waitcnt lgkmcnt(8)\n" \
"s_cbranch_scc0 .Lloop1_%=\n" \
".Lhook_%=:\n" \
".Lst0_13_%=:\n" \
"s_cbranch_execz .Lstb0_%=\n" \
  \
"s_load_dwordx4 s[48:51], %[ka], 192\n" \
"s_load_dwordx2 s[96:97], %[ka], 208\n" \
"v_accvgpr_read_b32 v252, a32\n" \
"v_accvgpr_read_b32 v254, a36\n" \
"v_min_f32 v253, v252, v254\n" \
"v_max_f32 v255, v252, v254\n" \
"v_cmp_nle_f32 s[98:99], v246, v253\n" \
"v_cmp_nle_f32 vcc, v255, v250\n" \
"v_cmp_u_f32 s[14:15], v252, v254\n" \
"s_nop 1\n" \
"s_or_b64 vcc, vcc, s[98:99]\n" \
"s_or_b64 vcc, vcc, s[14:15]\n" \
"s_cbranch_vccz .Loka_%=\n" \
"s_call_b64 s[98:99], .Lredo0_%=\n" \
"v_accvgpr_read_b32 v252, a32\n" \
"v_accvgpr_read_b32 v254, a36\n" \
".Loka_%=:\n" \
"s_add_u32 s23, s30, 4096\n" \
"v_rcp_f32 v252, v252\n" \
"v_rcp_f32 v254, v254\n" \
"s_nop 0\n" \
"v_mov_b32 v253, v252\n" \
"v_mov_b32 v255, v254\n" \
"v_pk_mul_f32 v[0:1], v[0:1], v[252:253]\n" \
"v_pk_mul_f32 v[2:3], v[2:3], v[252:253]\n" \
"v_pk_mul_f32 v[4:5], v[4:5], v[252:253]\n" \
"v_pk_mul_f32 v[6:7], v[6:7], v[252:253]\n" \
"v_cvt_pk_bf16_f32 v0, v0, v1\n" \
"v_cvt_pk_bf16_f32 v1, v2, v3\n" \
"v_cvt_pk_bf16_f32 v2, v4, v5\n" \
"v_cvt_pk_bf16_f32 v3, v6, v7\n" \
"buffer_store_dwordx4 v[0:3], v248, s[32:35], s30 offen offset:0 sc1\n" \
"v_pk_mul_f32 v[32:33], v[32:33], v[254:255]\n" \
"v_pk_mul_f32 v[34:35], v[34:35], v[254:255]\n" \
"v_pk_mul_f32 v[36:37], v[36:37], v[254:255]\n" \
"v_pk_mul_f32 v[38:39], v[38:39], v[254:255]\n" \
"v_cvt_pk_bf16_f32 v4, v32, v33\n" \
"v_cvt_pk_bf16_f32 v5, v34, v35\n" \
"v_cvt_pk_bf16_f32 v6, v36, v37\n" \
"v_cvt_pk_bf16_f32 v7, v38, v39\n" \
"buffer_store_dwordx4 v[4:7], v248, s[32:35], s30 offen offset:1024 sc1\n" \
"v_pk_mul_f32 v[8:9], v[8:9], v[252:253]\n" \
"v_pk_mul_f32 v[10:11], v[10:11], v[252:253]\n" \
"v_pk_mul_f32 v[12:13], v[12:13], v[252:253]\n" \
"v_pk_mul_f32 v[14:15], v[14:15], v[252:253]\n" \
"v_cvt_pk_bf16_f32 v8, v8, v9\n" \
"v_cvt_pk_bf16_f32 v9, v10, v11\n" \
"v_cvt_pk_bf16_f32 v10, v12, v13\n" \
"v_cvt_pk_bf16_f32 v11, v14, v15\n" \
"buffer_store_dwordx4 v[8:11], v248, s[32:35], s30 offen offset:2048 sc1\n" \
"v_pk_mul_f32 v[40:41], v[40:41], v[254:255]\n" \
"v_pk_mul_f32 v[42:43], v[42:43], v[254:255]\n" \
"v_pk_mul_f32 v[44:45], v[44:45], v[254:255]\n" \
"v_pk_mul_f32 v[46:47], v[46:47], v[254:255]\n" \
"v_cvt_pk_bf16_f32 v12, v40, v41\n" \
"v_cvt_pk_bf16_f32 v13, v42, v43\n" \
"v_cvt_pk_bf16_f32 v14, v44, v45\n" \
"v_cvt_pk_bf16_f32 v15, v46, v47\n" \
"buffer_store_dwordx4 v[12:15], v248, s[32:35], s30 offen offset:3072 sc1\n" \
"v_pk_mul_f32 v[16:17], v[16:17], v[252:253]\n" \
"v_pk_mul_f32 v[18:19], v[18:19], v[252:253]\n" \
"v_pk_mul_f32 v[20:21], v[20:21], v[252:253]\n" \
"v_pk_mul_f32 v[22:23], v[22:23], v[252:253]\n" \
"v_cvt_pk_bf16_f32 v16, v16, v17\n" \
"v_cvt_pk_bf16_f32 v17, v18, v19\n" \
"v_cvt_pk_bf16_f32 v18, v20, v21\n" \
"v_cvt_pk_bf16_f32 v19, v22, v23\n" \
"buffer_store_dwordx4 v[16:19], v248, s[32:35], s23 offen offset:0 sc1\n" \
"v_pk_mul_f32 v[48:49], v[48:49], v[254:255]\n" \
"v_pk_mul_f32 v[50:51], v[50:51], v[254:255]\n" \
"v_pk_mul_f32 v[52:53], v[52:53], v[254:255]\n" \
"v_pk_mul_f32 v[54:55], v[54:55], v[254:255]\n" \
"v_cvt_pk_bf16_f32 v20, v48, v49\n" \
"v_cvt_pk_bf16_f32 v21, v50, v51\n" \
"v_cvt_pk_bf16_f32 v22, v52, v53\n" \
"v_cvt_pk_bf16_f32 v23, v54, v55\n" \
"buffer_store_dwordx4 v[20:23], v248, s[32:35], s23 offen offset:1024 sc1\n" \
"v_pk_mul_f32 v[24:25], v[24:25], v[252:253]\n" \
"v_pk_mul_f32 v[26:27], v[26:27], v[252:253]\n" \
"v_pk_mul_f32 v[28:29], v[28:29], v[252:253]\n" \
"v_pk_mul_f32 v[30:31], v[30:31], v[252:253]\n" \
"v_cvt_pk_bf16_f32 v24, v24, v25\n" \
"v_cvt_pk_bf16_f32 v25, v26, v27\n" \
"v_cvt_pk_bf16_f32 v26, v28, v29\n" \
"v_cvt_pk_bf16_f32 v27, v30, v31\n" \
"buffer_store_dwordx4 v[24:27], v248, s[32:35], s23 offen offset:2048 sc1\n" \
"v_pk_mul_f32 v[56:57], v[56:57], v[254:255]\n" \
"v_pk_mul_f32 v[58:59], v[58:59], v[254:255]\n" \
"v_pk_mul_f32 v[60:61], v[60:61], v[254:255]\n" \
"v_pk_mul_f32 v[62:63], v[62:63], v[254:255]\n" \
"v_cvt_pk_bf16_f32 v28, v56, v57\n" \
"v_cvt_pk_bf16_f32 v29, v58, v59\n" \
"v_cvt_pk_bf16_f32 v30, v60, v61\n" \
"v_cvt_pk_bf16_f32 v31, v62, v63\n" \
"buffer_store_dwordx4 v[28:31], v248, s[32:35], s23 offen offset:3072 sc1\n" \
"s_lshl2_add_u32 s25, s66, s31\n" \
"s_mov_b32 s76, 1\n" \
"s_mov_b64 exec, 0\n" \
"s_cmp_eq_u32 %[wid], 0\n" \
"s_cbranch_scc1 .Lst1_0_%=\n" \
"s_cmp_eq_u32 %[wid], 1\n" \
"s_cbranch_scc1 .Lst1_1_%=\n" \
"s_cmp_eq_u32 %[wid], 2\n" \
"s_cbranch_scc1 .Lst1_2_%=\n" \
"s_branch .Lst1_3_%=\n" \
".Lstb1_%=:\n" \
"s_mov_b64 exec, -1\n" \
"s_lshl_b32 s95, s67, 1\n" \
"s_sub_u32 s95, 11, s95\n" \
"s_max_i32 s95, s95, 0\n" \
"s_waitcnt lgkmcnt(0)\n" \
"s_cmp_lt_u32 s67, 4\n" \
"s_cbranch_scc0 .Lloop3_%=\n" \
"s_mov_b32 s95, 3\n" \
".p2align 3\n" \
".Lloop2_%=:\n" \
  \
"v_mfma_f32_16x16x32_bf16 v[128:131], a[128:131], v[160:163], 0\n" \
"s_add_u32 s86, s86, 0x4000\n" \
"ds_read_b128 a[188:191], v240 offset:15360\n" \
"v_cvt_pk_bf16_f32 v152, v152, v153\n" \
"v_mfma_f32_16x16x32_bf16 v[128:131], a[132:135], v[164:167], v[128:131]\n" \
"s_bitset0_b32 s86, 16\n" \
"v_exp_f32 v154, v154\n" \
"v_mfma_f32_16x16x32_bf16 v[128:131], a[136:139], v[168:171], v[128:131]\n" \
"ds_read_b128 a[184:187], v240 offset:14336\n" \
"v_add_u32 v242, 0x10000, v240\n" \
"v_mfma_f32_16x16x32_bf16 v[128:131], a[140:143], v[172:175], v[128:131]\n" \
"s_waitcnt lgkmcnt(6)\n" \
"v_exp_f32 v155, v155\n" \
"v_mfma_f32_16x16x32_bf16 v[132:135], a[156:159], v[172:175], 0\n" \
"s_mov_b32 m0, s19\n" \
"ds_read_b128 a[180:183], v240 offset:13312\n" \
"v_cvt_pk_bf16_f32 v153, v154, v155\n" \
"v_mfma_f32_16x16x32_bf16 v[132:135], a[152:155], v[168:171], v[132:135]\n" \
"ds_read_b128 a[176:179], v240 offset:12288\n" \
"v_exp_f32 v156, v156\n" \
"v_mfma_f32_16x16x32_bf16 v[132:135], a[148:151], v[164:167], v[132:135]\n" \
"buffer_load_dwordx4 v248, s[32:35], s88 offen offset:0 sc1 lds\n" \
"v_mfma_f32_16x16x32_bf16 v[132:135], a[144:147], v[160:163], v[132:135]\n" \
"s_waitcnt lgkmcnt(4)\n" \
"v_exp_f32 v157, v157\n" \
"v_mfma_f32_16x16x32_bf16 v[136:139], a[160:163], v[160:163], 0\n" \
"v_cvt_pk_bf16_f32 v154, v156, v157\n" \
"v_mfma_f32_16x16x32_bf16 v[136:139], a[164:167], v[164:167], v[136:139]\n" \
"v_exp_f32 v158, v158\n" \
"v_mfma_f32_16x16x32_bf16 v[136:139], a[168:171], v[168:171], v[136:139]\n" \
"buffer_load_dwordx4 v248, s[32:35], s88 offen offset:1024 sc1 lds\n" \
"v_mfma_f32_16x16x32_bf16 v[136:139], a[172:175], v[172:175], v[136:139]\n" \
"s_waitcnt lgkmcnt(0)\n" \
"v_exp_f32 v159, v159\n" \
"v_mfma_f32_16x16x32_bf16 v[140:143], a[188:191], v[172:175], 0\n" \
"v_add_u32 v240, s86, v248\n" \
"v_cvt_pk_bf16_f32 v155, v158, v159\n" \
"v_mfma_f32_16x16x32_bf16 v[140:143], a[184:187], v[168:171], v[140:143]\n" \
"buffer_load_dwordx4 v248, s[32:35], s88 offen offset:2048 sc1 lds\n" \
"v_mfma_f32_16x16x32_bf16 v[140:143], a[180:183], v[164:167], v[140:143]\n" \
"v_exp_f32 v128, v128\n" \
"v_mfma_f32_16x16x32_bf16 v[140:143], a[176:179], v[160:163], v[140:143]\n" \
"buffer_load_dwordx4 v248, s[32:35], s88 offen offset:3072 sc1 lds\n" \
"v_mfma_f32_16x16x32_bf16 a[44:47], a[48:51], v[144:147], a[44:47]\n" \
"s_add_u32 s89, s88, 589824\n" \
"v_exp_f32 v129, v129\n" \
"v_mfma_f32_16x16x32_bf16 a[44:47], a[48:51], v[152:155], a[44:47]\n" \
"s_add_u32 s88, s88, s27\n" \
"s_add_u32 s19, s19, 0x4000\n" \
"v_cvt_pk_bf16_f32 v128, v128, v129\n" \
"v_mfma_f32_16x16x32_bf16 v[96:99], a[224:227], v[152:155], v[96:99]\n" \
"s_bitset0_b32 s19, 16\n" \
"v_exp_f32 v130, v130\n" \
"v_mfma_f32_16x16x32_bf16 v[96:99], a[192:195], v[144:147], v[96:99]\n" \
"s_cmp_eq_u32 s88, s16\n" \
"s_cmov_b32 s88, s21\n" \
"ds_read_b128 a[224:227], v242 offset:8192\n" \
"v_mfma_f32_16x16x32_bf16 v[100:103], a[196:199], v[144:147], v[100:103]\n" \
"s_cmov_b32 s27, s91\n" \
"v_exp_f32 v131, v131\n" \
"v_mfma_f32_16x16x32_bf16 v[100:103], a[228:231], v[152:155], v[100:103]\n" \
"s_cmov_b32 s16, s77\n" \
"ds_read_b128 a[192:195], v242 offset:0\n" \
"v_cvt_pk_bf16_f32 v129, v130, v131\n" \
"v_mfma_f32_16x16x32_bf16 v[104:107], a[232:235], v[152:155], v[104:107]\n" \
"s_cmov_b32 s21, s92\n" \
"v_exp_f32 v132, v132\n" \
"v_mfma_f32_16x16x32_bf16 v[104:107], a[200:203], v[144:147], v[104:107]\n" \
"s_cmovk_i32 s91, 0x4000\n" \
"ds_read_b128 a[196:199], v242 offset:1024\n" \
"ds_read_b128 a[228:231], v242 offset:9216\n" \
"v_mfma_f32_16x16x32_bf16 v[108:111], a[204:207], v[144:147], v[108:111]\n" \
"s_mov_b32 m0, s87\n" \
"v_exp_f32 v133, v133\n" \
"v_mfma_f32_16x16x32_bf16 v[108:111], a[236:239], v[152:155], v[108:111]\n" \
"s_cmp_eq_u32 s88, s17\n" \
"ds_read_b128 a[232:235], v242 offset:10240\n" \
"v_cvt_pk_bf16_f32 v130, v132, v133\n" \
"v_mfma_f32_16x16x32_bf16 v[112:115], a[240:243], v[152:155], v[112:115]\n" \
"s_cmov_b32 s88, s18\n" \
"buffer_load_dwordx4 v248, s[32:35], s93 offen sc1 lds\n" \
"v_mfma_f32_16x16x32_bf16 v[112:115], a[208:211], v[144:147], v[112:115]\n" \
"s_add_u32 s93, s93, 1024\n" \
"v_exp_f32 v134, v134\n" \
"v_mfma_f32_16x16x32_bf16 v[116:119], a[212:215], v[144:147], v[116:119]\n" \
"s_add_u32 s87, s87, 1024\n" \
"v_exp_f32 v135, v135\n" \
"v_mfma_f32_16x16x32_bf16 v[116:119], a[244:247], v[152:155], v[116:119]\n" \
"ds_read_b128 a[200:203], v242 offset:2048\n" \
"v_cvt_pk_bf16_f32 v131, v134, v135\n" \
"v_mfma_f32_16x16x32_bf16 v[120:123], a[248:251], v[152:155], v[120:123]\n" \
"ds_read_b128 a[204:207], v242 offset:3072\n" \
"ds_read_b128 a[236:239], v242 offset:11264\n" \
"v_mfma_f32_16x16x32_bf16 v[120:123], a[216:219], v[144:147], v[120:123]\n" \
".Lst1_0_%=:\n" \
"s_cbranch_execz .Lst1_4_%=\n" \
"v_exp_f32 v136, v136\n" \
"v_mfma_f32_16x16x32_bf16 v[124:127], a[220:223], v[144:147], v[124:127]\n" \
"ds_read_b128 a[240:243], v242 offset:12288\n" \
"v_mfma_f32_16x16x32_bf16 v[124:127], a[252:255], v[152:155], v[124:127]\n" \
"s_waitcnt vmcnt(13)\n" \
"v_exp_f32 v137, v137\n" \
"v_mfma_f32_16x16x32_bf16 v[144:147], a[128:131], v[176:179], 0\n" \
"s_barrier\n" \
"ds_read_b128 a[208:211], v242 offset:4096\n" \
"v_cvt_pk_bf16_f32 v136, v136, v137\n" \
"v_mfma_f32_16x16x32_bf16 v[144:147], a[132:135], v[180:183], v[144:147]\n" \
"s_mov_b32 m0, s20\n" \
"v_exp_f32 v138, v138\n" \
"v_mfma_f32_16x16x32_bf16 v[144:147], a[136:139], v[184:187], v[144:147]\n" \
"buffer_load_dwordx4 v248, s[32:35], s89 offen offset:0 sc1 lds\n" \
"v_mfma_f32_16x16x32_bf16 v[144:147], a[140:143], v[188:191], v[144:147]\n" \
"v_exp_f32 v139, v139\n" \
"v_mfma_f32_16x16x32_bf16 v[148:151], a[156:159], v[188:191], 0\n" \
"ds_read_b128 a[212:215], v242 offset:5120\n" \
"v_cvt_pk_bf16_f32 v137, v138, v139\n" \
"v_mfma_f32_16x16x32_bf16 v[148:151], a[152:155], v[184:187], v[148:151]\n" \
"v_exp_f32 v140, v140\n" \
"v_mfma_f32_16x16x32_bf16 v[148:151], a[148:151], v[180:183], v[148:151]\n" \
"ds_read_b128 a[244:247], v242 offset:13312\n" \
"ds_read_b128 a[248:251], v242 offset:14336\n" \
"v_mfma_f32_16x16x32_bf16 v[148:151], a[144:147], v[176:179], v[148:151]\n" \
"v_exp_f32 v141, v141\n" \
"v_mfma_f32_16x16x32_bf16 v[152:155], a[160:163], v[176:179], 0\n" \
"buffer_load_dwordx4 v248, s[32:35], s89 offen offset:1024 sc1 lds\n" \
"v_mfma_f32_16x16x32_bf16 v[152:155], a[164:167], v[180:183], v[152:155]\n" \
"v_exp_f32 v142, v142\n" \
"v_mfma_f32_16x16x32_bf16 v[152:155], a[168:171], v[184:187], v[152:155]\n" \
"ds_read_b128 a[216:219], v242 offset:6144\n" \
"v_cvt_pk_bf16_f32 v138, v140, v141\n" \
"v_mfma_f32_16x16x32_bf16 v[152:155], a[172:175], v[188:191], v[152:155]\n" \
"v_exp_f32 v143, v143\n" \
"v_mfma_f32_16x16x32_bf16 v[156:159], a[188:191], v[188:191], 0\n" \
"ds_read_b128 a[128:131], v240 offset:0\n" \
"v_cvt_pk_bf16_f32 v139, v142, v143\n" \
"v_mfma_f32_16x16x32_bf16 v[156:159], a[184:187], v[184:187], v[156:159]\n" \
"ds_read_b128 a[132:135], v240 offset:1024\n" \
"ds_read_b128 a[220:223], v242 offset:7168\n" \
"v_mfma_f32_16x16x32_bf16 v[156:159], a[180:183], v[180:183], v[156:159]\n" \
"v_exp_f32 v144, v144\n" \
"v_mfma_f32_16x16x32_bf16 v[156:159], a[176:179], v[176:179], v[156:159]\n" \
"buffer_load_dwordx4 v248, s[32:35], s89 offen offset:2048 sc1 lds\n" \
"v_mfma_f32_16x16x32_bf16 a[40:43], a[48:51], v[128:131], a[40:43]\n" \
"v_exp_f32 v145, v145\n" \
"v_mfma_f32_16x16x32_bf16 a[40:43], a[48:51], v[136:139], a[40:43]\n" \
"s_waitcnt lgkmcnt(13)\n" \
"ds_read_b128 a[136:139], v240 offset:2048\n" \
"v_cvt_pk_bf16_f32 v144, v144, v145\n" \
"v_mfma_f32_16x16x32_bf16 v[64:67], a[224:227], v[136:139], v[64:67]\n" \
"v_exp_f32 v146, v146\n" \
"v_mfma_f32_16x16x32_bf16 v[64:67], a[192:195], v[128:131], v[64:67]\n" \
"ds_read_b128 a[252:255], v242 offset:15360\n" \
"ds_read_b128 a[140:143], v240 offset:3072\n" \
"v_mfma_f32_16x16x32_bf16 v[68:71], a[196:199], v[128:131], v[68:71]\n" \
"v_exp_f32 v147, v147\n" \
"v_mfma_f32_16x16x32_bf16 v[68:71], a[228:231], v[136:139], v[68:71]\n" \
"s_waitcnt lgkmcnt(12)\n" \
"buffer_load_dwordx4 v248, s[32:35], s89 offen offset:3072 sc1 lds\n" \
"v_mfma_f32_16x16x32_bf16 v[72:75], a[232:235], v[136:139], v[72:75]\n" \
"s_add_u32 s20, s20, 0x4000\n" \
"v_exp_f32 v148, v148\n" \
"v_mfma_f32_16x16x32_bf16 v[72:75], a[200:203], v[128:131], v[72:75]\n" \
"s_and_b32 s20, s20, 0xffff\n" \
"ds_read_b128 a[156:159], v240 offset:7168\n" \
"v_cvt_pk_bf16_f32 v145, v146, v147\n" \
"v_mfma_f32_16x16x32_bf16 v[76:79], a[204:207], v[128:131], v[76:79]\n" \
"s_bitset1_b32 s20, 16\n" \
"v_exp_f32 v149, v149\n" \
"v_mfma_f32_16x16x32_bf16 v[76:79], a[236:239], v[136:139], v[76:79]\n" \
"s_waitcnt lgkmcnt(9)\n" \
"ds_read_b128 a[152:155], v240 offset:6144\n" \
"v_cvt_pk_bf16_f32 v146, v148, v149\n" \
"v_mfma_f32_16x16x32_bf16 v[80:83], a[240:243], v[136:139], v[80:83]\n" \
"v_exp_f32 v150, v150\n" \
"v_mfma_f32_16x16x32_bf16 v[80:83], a[208:211], v[128:131], v[80:83]\n" \
"ds_read_b128 a[148:151], v240 offset:5120\n" \
"ds_read_b128 a[144:147], v240 offset:4096\n" \
"v_mfma_f32_16x16x32_bf16 v[84:87], a[212:215], v[128:131], v[84:87]\n" \
"v_exp_f32 v151, v151\n" \
"v_mfma_f32_16x16x32_bf16 v[84:87], a[244:247], v[136:139], v[84:87]\n" \
"s_waitcnt lgkmcnt(5)\n" \
"ds_read_b128 a[160:163], v240 offset:8192\n" \
"v_cvt_pk_bf16_f32 v147, v150, v151\n" \
"v_mfma_f32_16x16x32_bf16 v[88:91], a[248:251], v[136:139], v[88:91]\n" \
"ds_read_b128 a[164:167], v240 offset:9216\n" \
"ds_read_b128 a[168:171], v240 offset:10240\n" \
"v_mfma_f32_16x16x32_bf16 v[88:91], a[216:219], v[128:131], v[88:91]\n" \
"v_exp_f32 v152, v152\n" \
"v_mfma_f32_16x16x32_bf16 v[92:95], a[220:223], v[128:131], v[92:95]\n" \
"ds_read_b128 a[172:175], v240 offset:11264\n" \
"ds_read_b128 a[188:191], v240 offset:15360\n" \
"v_mfma_f32_16x16x32_bf16 v[92:95], a[252:255], v[136:139], v[92:95]\n" \
"s_waitcnt lgkmcnt(9)\n" \
"v_exp_f32 v153, v153\n" \
"v_mfma_f32_16x16x32_bf16 v[128:131], a[128:131], v[160:163], 0\n" \
"s_add_u32 s86, s86, 0x4000\n" \
"ds_read_b128 a[184:187], v240 offset:14336\n" \
"v_cvt_pk_bf16_f32 v152, v152, v153\n" \
"v_mfma_f32_16x16x32_bf16 v[128:131], a[132:135], v[164:167], v[128:131]\n" \
"s_bitset0_b32 s86, 16\n" \
"v_exp_f32 v154, v154\n" \
"v_mfma_f32_16x16x32_bf16 v[128:131], a[136:139], v[168:171], v[128:131]\n" \
"s_cmp_eq_u32 s88, s24\n" \
"s_cbranch_scc1 .Lchtx_%=\n" \
".Lsbhtx_%=:\n" \
"v_mfma_f32_16x16x32_bf16 v[128:131], a[140:143], v[172:175], v[128:131]\n" \
"s_waitcnt lgkmcnt(6)\n" \
"v_exp_f32 v155, v155\n" \
"v_mfma_f32_16x16x32_bf16 v[132:135], a[156:159], v[172:175], 0\n" \
"s_mov_b32 m0, s19\n" \
"v_add_u32 v242, 0x10000, v240\n" \
"v_cvt_pk_bf16_f32 v153, v154, v155\n" \
"v_mfma_f32_16x16x32_bf16 v[132:135], a[152:155], v[168:171], v[132:135]\n" \
"s_cmp_lg_u32 s76, 0\n" \
"ds_read_b128 a[180:183], v240 offset:13312\n" \
"ds_read_b128 a[176:179], v240 offset:12288\n" \
"v_exp_f32 v156, v156\n" \
"v_mfma_f32_16x16x32_bf16 v[132:135], a[148:151], v[164:167], v[132:135]\n" \
"buffer_load_dwordx4 v248, s[32:35], s88 offen offset:0 sc1 lds\n" \
"v_mfma_f32_16x16x32_bf16 v[132:135], a[144:147], v[160:163], v[132:135]\n" \
"s_waitcnt lgkmcnt(4)\n" \
"v_exp_f32 v157, v157\n" \
"v_mfma_f32_16x16x32_bf16 v[136:139], a[160:163], v[160:163], 0\n" \
"s_cbranch_scc1 .Lantx_%=\n" \
".Labtx_%=:\n" \
"v_cvt_pk_bf16_f32 v154, v156, v157\n" \
"v_mfma_f32_16x16x32_bf16 v[136:139], a[164:167], v[164:167], v[136:139]\n" \
"v_exp_f32 v158, v158\n" \
"v_mfma_f32_16x16x32_bf16 v[136:139], a[168:171], v[168:171], v[136:139]\n" \
"buffer_load_dwordx4 v248, s[32:35], s88 offen offset:1024 sc1 lds\n" \
"v_mfma_f32_16x16x32_bf16 v[136:139], a[172:175], v[172:175], v[136:139]\n" \
"s_waitcnt lgkmcnt(0)\n" \
"v_exp_f32 v159, v159\n" \
"v_mfma_f32_16x16x32_bf16 v[140:143], a[188:191], v[172:175], 0\n" \
"v_add_u32 v240, s86, v248\n" \
"v_cvt_pk_bf16_f32 v155, v158, v159\n" \
"v_mfma_f32_16x16x32_bf16 v[140:143], a[184:187], v[168:171], v[140:143]\n" \
"buffer_load_dwordx4 v248, s[32:35], s88 offen offset:2048 sc1 lds\n" \
"v_mfma_f32_16x16x32_bf16 v[140:143], a[180:183], v[164:167], v[140:143]\n" \
"v_exp_f32 v128, v128\n" \
"v_mfma_f32_16x16x32_bf16 v[140:143], a[176:179], v[160:163], v[140:143]\n" \
"buffer_load_dwordx4 v248, s[32:35], s88 offen offset:3072 sc1 lds\n" \
"v_mfma_f32_16x16x32_bf16 a[44:47], a[48:51], v[144:147], a[44:47]\n" \
"s_add_u32 s89, s88, 589824\n" \
"v_exp_f32 v129, v129\n" \
"v_mfma_f32_16x16x32_bf16 a[44:47], a[48:51], v[152:155], a[44:47]\n" \
"s_add_u32 s88, s88, s27\n" \
"s_add_u32 s19, s19, 0x4000\n" \
"v_cvt_pk_bf16_f32 v128, v128, v129\n" \
"v_mfma_f32_16x16x32_bf16 v[96:99], a[224:227], v[152:155], v[96:99]\n" \
"s_bitset0_b32 s19, 16\n" \
"v_exp_f32 v130, v130\n" \
"v_mfma_f32_16x16x32_bf16 v[96:99], a[192:195], v[144:147], v[96:99]\n" \
"s_cmp_eq_u32 s88, s16\n" \
"s_cmov_b32 s88, s21\n" \
"ds_read_b128 a[224:227], v242 offset:8192\n" \
"v_mfma_f32_16x16x32_bf16 v[100:103], a[196:199], v[144:147], v[100:103]\n" \
"s_cmov_b32 s27, s91\n" \
"v_exp_f32 v131, v131\n" \
"v_mfma_f32_16x16x32_bf16 v[100:103], a[228:231], v[152:155], v[100:103]\n" \
"s_cmov_b32 s16, s77\n" \
"ds_read_b128 a[192:195], v242 offset:0\n" \
"v_cvt_pk_bf16_f32 v129, v130, v131\n" \
"v_mfma_f32_16x16x32_bf16 v[104:107], a[232:235], v[152:155], v[104:107]\n" \
"s_cmov_b32 s21, s92\n" \
"v_exp_f32 v132, v132\n" \
"v_mfma_f32_16x16x32_bf16 v[104:107], a[200:203], v[144:147], v[104:107]\n" \
"s_cmovk_i32 s91, 0x4000\n" \
"ds_read_b128 a[196:199], v242 offset:1024\n" \
"ds_read_b128 a[228:231], v242 offset:9216\n" \
"v_mfma_f32_16x16x32_bf16 v[108:111], a[204:207], v[144:147], v[108:111]\n" \
"s_mov_b32 m0, s87\n" \
"v_exp_f32 v133, v133\n" \
"v_mfma_f32_16x16x32_bf16 v[108:111], a[236:239], v[152:155], v[108:111]\n" \
"s_cmp_eq_u32 s88, s17\n" \
"ds_read_b128 a[232:235], v242 offset:10240\n" \
"v_cvt_pk_bf16_f32 v130, v132, v133\n" \
"v_mfma_f32_16x16x32_bf16 v[112:115], a[240:243], v[152:155], v[112:115]\n" \
"s_cmov_b32 s88, s18\n" \
"buffer_load_dwordx4 v248, s[32:35], s93 offen sc1 lds\n" \
"v_mfma_f32_16x16x32_bf16 v[112:115], a[208:211], v[144:147], v[112:115]\n" \
"s_add_u32 s93, s93, 1024\n" \
"v_exp_f32 v134, v134\n" \
"v_mfma_f32_16x16x32_bf16 v[116:119], a[212:215], v[144:147], v[116:119]\n" \
"s_add_u32 s87, s87, 1024\n" \
"v_exp_f32 v135, v135\n" \
"v_mfma_f32_16x16x32_bf16 v[116:119], a[244:247], v[152:155], v[116:119]\n" \
"ds_read_b128 a[200:203], v242 offset:2048\n" \
"v_cvt_pk_bf16_f32 v131, v134, v135\n" \
"v_mfma_f32_16x16x32_bf16 v[120:123], a[248:251], v[152:155], v[120:123]\n" \
"ds_read_b128 a[204:207], v242 offset:3072\n" \
"ds_read_b128 a[236:239], v242 offset:11264\n" \
"v_mfma_f32_16x16x32_bf16 v[120:123], a[216:219], v[144:147], v[120:123]\n" \
"v_exp_f32 v136, v136\n" \
"v_mfma_f32_16x16x32_bf16 v[124:127], a[220:223], v[144:147], v[124:127]\n" \
"ds_read_b128 a[240:243], v242 offset:12288\n" \
"v_mfma_f32_16x16x32_bf16 v[124:127], a[252:255], v[152:155], v[124:127]\n" \
"s_waitcnt vmcnt(14)\n" \
"v_exp_f32 v137, v137\n" \
"v_mfma_f32_16x16x32_bf16 v[144:147], a[128:131], v[176:179], 0\n" \
"s_barrier\n" \
"ds_read_b128 a[208:211], v242 offset:4096\n" \
"v_cvt_pk_bf16_f32 v136, v136, v137\n" \
"v_mfma_f32_16x16x32_bf16 v[144:147], a[132:135], v[180:183], v[144:147]\n" \
"s_mov_b32 m0, s20\n" \
"v_exp_f32 v138, v138\n" \
"v_mfma_f32_16x16x32_bf16 v[144:147], a[136:139], v[184:187], v[144:147]\n" \
"buffer_load_dwordx4 v248, s[32:35], s89 offen offset:0 sc1 lds\n" \
"v_mfma_f32_16x16x32_bf16 v[144:147], a[140:143], v[188:191], v[144:147]\n" \
"v_exp_f32 v139, v139\n" \
"v_mfma_f32_16x16x32_bf16 v[148:151], a[156:159], v[188:191], 0\n" \
"ds_read_b128 a[212:215], v242 offset:5120\n" \
"v_cvt_pk_bf16_f32 v137, v138, v139\n" \
"v_mfma_f32_16x16x32_bf16 v[148:151], a[152:155], v[184:187], v[148:151]\n" \
"v_exp_f32 v140, v140\n" \
"v_mfma_f32_16x16x32_bf16 v[148:151], a[148:151], v[180:183], v[148:151]\n" \
"ds_read_b128 a[244:247], v242 offset:13312\n" \
"ds_read_b128 a[248:251], v242 offset:14336\n" \
"v_mfma_f32_16x16x32_bf16 v[148:151], a[144:147], v[176:179], v[148:151]\n" \
"v_exp_f32 v141, v141\n" \
"v_mfma_f32_16x16x32_bf16 v[152:155], a[160:163], v[176:179], 0\n" \
"buffer_load_dwordx4 v248, s[32:35], s89 offen offset:1024 sc1 lds\n" \
"v_mfma_f32_16x16x32_bf16 v[152:155], a[164:167], v[180:183], v[152:155]\n" \
"v_exp_f32 v142, v142\n" \
"v_mfma_f32_16x16x32_bf16 v[152:155], a[168:171], v[184:187], v[152:155]\n" \
"ds_read_b128 a[216:219], v242 offset:6144\n" \
"v_cvt_pk_bf16_f32 v138, v140, v141\n" \
"v_mfma_f32_16x16x32_bf16 v[152:155], a[172:175], v[188:191], v[152:155]\n" \
"v_exp_f32 v143, v143\n" \
"v_mfma_f32_16x16x32_bf16 v[156:159], a[188:191], v[188:191], 0\n" \
"ds_read_b128 a[128:131], v240 offset:0\n" \
"v_cvt_pk_bf16_f32 v139, v142, v143\n" \
"v_mfma_f32_16x16x32_bf16 v[156:159], a[184:187], v[184:187], v[156:159]\n" \
"ds_read_b128 a[132:135], v240 offset:1024\n" \
"ds_read_b128 a[220:223], v242 offset:7168\n" \
"v_mfma_f32_16x16x32_bf16 v[156:159], a[180:183], v[180:183], v[156:159]\n" \
"v_exp_f32 v144, v144\n" \
"v_mfma_f32_16x16x32_bf16 v[156:159], a[176:179], v[176:179], v[156:159]\n" \
"buffer_load_dwordx4 v248, s[32:35], s89 offen offset:2048 sc1 lds\n" \
"v_mfma_f32_16x16x32_bf16 a[40:43], a[48:51], v[128:131], a[40:43]\n" \
"v_exp_f32 v145, v145\n" \
"v_mfma_f32_16x16x32_bf16 a[40:43], a[48:51], v[136:139], a[40:43]\n" \
"s_waitcnt lgkmcnt(13)\n" \
"ds_read_b128 a[136:139], v240 offset:2048\n" \
"v_cvt_pk_bf16_f32 v144, v144, v145\n" \
"v_mfma_f32_16x16x32_bf16 v[64:67], a[224:227], v[136:139], v[64:67]\n" \
"v_exp_f32 v146, v146\n" \
"v_mfma_f32_16x16x32_bf16 v[64:67], a[192:195], v[128:131], v[64:67]\n" \
"ds_read_b128 a[252:255], v242 offset:15360\n" \
"ds_read_b128 a[140:143], v240 offset:3072\n" \
"v_mfma_f32_16x16x32_bf16 v[68:71], a[196:199], v[128:131], v[68:71]\n" \
"v_exp_f32 v147, v147\n" \
"v_mfma_f32_16x16x32_bf16 v[68:71], a[228:231], v[136:139], v[68:71]\n" \
"s_waitcnt lgkmcnt(12)\n" \
"buffer_load_dwordx4 v248, s[32:35], s89 offen offset:3072 sc1 lds\n" \
"v_mfma_f32_16x16x32_bf16 v[72:75], a[232:235], v[136:139], v[72:75]\n" \
"s_add_u32 s20, s20, 0x4000\n" \
"v_exp_f32 v148, v148\n" \
"v_mfma_f32_16x16x32_bf16 v[72:75], a[200:203], v[128:131], v[72:75]\n" \
"s_and_b32 s20, s20, 0xffff\n" \
"ds_read_b128 a[156:159], v240 offset:7168\n" \
"v_cvt_pk_bf16_f32 v145, v146, v147\n" \
"v_mfma_f32_16x16x32_bf16 v[76:79], a[204:207], v[128:131], v[76:79]\n" \
"s_bitset1_b32 s20, 16\n" \
"v_exp_f32 v149, v149\n" \
"v_mfma_f32_16x16x32_bf16 v[76:79], a[236:239], v[136:139], v[76:79]\n" \
"s_waitcnt lgkmcnt(9)\n" \
"ds_read_b128 a[152:155], v240 offset:6144\n" \
"v_cvt_pk_bf16_f32 v146, v148, v149\n" \
"v_mfma_f32_16x16x32_bf16 v[80:83], a[240:243], v[136:139], v[80:83]\n" \
"v_exp_f32 v150, v150\n" \
"v_mfma_f32_16x16x32_bf16 v[80:83], a[208:211], v[128:131], v[80:83]\n" \
"ds_read_b128 a[148:151], v240 offset:5120\n" \
"ds_read_b128 a[144:147], v240 offset:4096\n" \
"v_mfma_f32_16x16x32_bf16 v[84:87], a[212:215], v[128:131], v[84:87]\n" \
"s_sub_u32 s95, s95, 1\n" \
"v_exp_f32 v151, v151\n" \
"v_mfma_f32_16x16x32_bf16 v[84:87], a[244:247], v[136:139], v[84:87]\n" \
"s_waitcnt lgkmcnt(5)\n" \
"ds_read_b128 a[160:163], v240 offset:8192\n" \
"v_cvt_pk_bf16_f32 v147, v150, v151\n" \
"v_mfma_f32_16x16x32_bf16 v[88:91], a[248:251], v[136:139], v[88:91]\n" \
"ds_read_b128 a[164:167], v240 offset:9216\n" \
"ds_read_b128 a[168:171], v240 offset:10240\n" \
"v_mfma_f32_16x16x32_bf16 v[88:91], a[216:219], v[128:131], v[88:91]\n" \
"v_exp_f32 v152, v152\n" \
"v_mfma_f32_16x16x32_bf16 v[92:95], a[220:223], v[128:131], v[92:95]\n" \
"v_exp_f32 v153, v153\n" \
"v_mfma_f32_16x16x32_bf16 v[92:95], a[252:255], v[136:139], v[92:95]\n" \
"s_waitcnt lgkmcnt(7)\n" \
"ds_read_b128 a[172:175], v240 offset:11264\n" \
"s_cbranch_scc0 .Lloop2_%=\n" \
"s_lshl_b32 s95, s67, 1\n" \
"s_sub_u32 s95, 7, s95\n" \
".p2align 3\n" \
".Lloop3_%=:\n" \
".Lst1_1_%=:\n" \
"s_cbranch_execz .Lst1_5_%=\n" \
  \
"v_mfma_f32_16x16x32_bf16 v[128:131], a[128:131], v[160:163], 0\n" \
"s_add_u32 s86, s86, 0x4000\n" \
"ds_read_b128 a[188:191], v240 offset:15360\n" \
"v_cvt_pk_bf16_f32 v152, v152, v153\n" \
"v_mfma_f32_16x16x32_bf16 v[128:131], a[132:135], v[164:167], v[128:131]\n" \
"s_bitset0_b32 s86, 16\n" \
"v_exp_f32 v154, v154\n" \
"v_mfma_f32_16x16x32_bf16 v[128:131], a[136:139], v[168:171], v[128:131]\n" \
"ds_read_b128 a[184:187], v240 offset:14336\n" \
"v_add_u32 v242, 0x10000, v240\n" \
"v_mfma_f32_16x16x32_bf16 v[128:131], a[140:143], v[172:175], v[128:131]\n" \
"s_waitcnt lgkmcnt(6)\n" \
"v_exp_f32 v155, v155\n" \
"v_mfma_f32_16x16x32_bf16 v[132:135], a[156:159], v[172:175], 0\n" \
"s_mov_b32 m0, s19\n" \
"ds_read_b128 a[180:183], v240 offset:13312\n" \
"v_cvt_pk_bf16_f32 v153, v154, v155\n" \
"v_mfma_f32_16x16x32_bf16 v[132:135], a[152:155], v[168:171], v[132:135]\n" \
"ds_read_b128 a[176:179], v240 offset:12288\n" \
"v_exp_f32 v156, v156\n" \
"v_mfma_f32_16x16x32_bf16 v[132:135], a[148:151], v[164:167], v[132:135]\n" \
"buffer_load_dwordx4 v248, s[32:35], s88 offen offset:0 sc1 lds\n" \
"v_mfma_f32_16x16x32_bf16 v[132:135], a[144:147], v[160:163], v[132:135]\n" \
"s_waitcnt lgkmcnt(4)\n" \
"v_exp_f32 v157, v157\n" \
"v_mfma_f32_16x16x32_bf16 v[136:139], a[160:163], v[160:163], 0\n" \
"v_cvt_pk_bf16_f32 v154, v156, v157\n" \
"v_mfma_f32_16x16x32_bf16 v[136:139], a[164:167], v[164:167], v[136:139]\n" \
"v_exp_f32 v158, v158\n" \
"v_mfma_f32_16x16x32_bf16 v[136:139], a[168:171], v[168:171], v[136:139]\n" \
"buffer_load_dwordx4 v248, s[32:35], s88 offen offset:1024 sc1 lds\n" \
"v_mfma_f32_16x16x32_bf16 v[136:139], a[172:175], v[172:175], v[136:139]\n" \
"s_waitcnt lgkmcnt(0)\n" \
"v_exp_f32 v159, v159\n" \
"v_mfma_f32_16x16x32_bf16 v[140:143], a[188:191], v[172:175], 0\n" \
"v_add_u32 v240, s86, v248\n" \
"v_cvt_pk_bf16_f32 v155, v158, v159\n" \
"v_mfma_f32_16x16x32_bf16 v[140:143], a[184:187], v[168:171], v[140:143]\n" \
"buffer_load_dwordx4 v248, s[32:35], s88 offen offset:2048 sc1 lds\n" \
"v_mfma_f32_16x16x32_bf16 v[140:143], a[180:183], v[164:167], v[140:143]\n" \
"v_exp_f32 v128, v128\n" \
"v_mfma_f32_16x16x32_bf16 v[140:143], a[176:179], v[160:163], v[140:143]\n" \
"buffer_load_dwordx4 v248, s[32:35], s88 offen offset:3072 sc1 lds\n" \
"v_mfma_f32_16x16x32_bf16 a[44:47], a[48:51], v[144:147], a[44:47]\n" \
"s_add_u32 s89, s88, 589824\n" \
"v_exp_f32 v129, v129\n" \
"v_mfma_f32_16x16x32_bf16 a[44:47], a[48:51], v[152:155], a[44:47]\n" \
"s_add_u32 s88, s88, s27\n" \
"s_add_u32 s19, s19, 0x4000\n" \
"v_cvt_pk_bf16_f32 v128, v128, v129\n" \
"v_mfma_f32_16x16x32_bf16 v[96:99], a[224:227], v[152:155], v[96:99]\n" \
"s_bitset0_b32 s19, 16\n" \
"v_exp_f32 v130, v130\n" \
"v_mfma_f32_16x16x32_bf16 v[96:99], a[192:195], v[144:147], v[96:99]\n" \
"s_cmp_eq_u32 s88, s16\n" \
"s_cmov_b32 s88, s21\n" \
"ds_read_b128 a[224:227], v242 offset:8192\n" \
"v_mfma_f32_16x16x32_bf16 v[100:103], a[196:199], v[144:147], v[100:103]\n" \
"s_cmov_b32 s27, s91\n" \
"v_exp_f32 v131, v131\n" \
"v_mfma_f32_16x16x32_bf16 v[100:103], a[228:231], v[152:155], v[100:103]\n" \
"s_cmov_b32 s16, s77\n" \
"ds_read_b128 a[192:195], v242 offset:0\n" \
"v_cvt_pk_bf16_f32 v129, v130, v131\n" \
"v_mfma_f32_16x16x32_bf16 v[104:107], a[232:235], v[152:155], v[104:107]\n" \
"s_cmov_b32 s21, s92\n" \
"v_exp_f32 v132, v132\n" \
"v_mfma_f32_16x16x32_bf16 v[104:107], a[200:203], v[144:147], v[104:107]\n" \
"s_cmovk_i32 s91, 0x4000\n" \
"ds_read_b128 a[196:199], v242 offset:1024\n" \
"ds_read_b128 a[228:231], v242 offset:9216\n" \
"v_mfma_f32_16x16x32_bf16 v[108:111], a[204:207], v[144:147], v[108:111]\n" \
"s_cmp_eq_u32 s88, s17\n" \
"v_exp_f32 v133, v133\n" \
"v_mfma_f32_16x16x32_bf16 v[108:111], a[236:239], v[152:155], v[108:111]\n" \
"s_cmov_b32 s88, s18\n" \
"ds_read_b128 a[232:235], v242 offset:10240\n" \
"v_cvt_pk_bf16_f32 v130, v132, v133\n" \
"v_mfma_f32_16x16x32_bf16 v[112:115], a[240:243], v[152:155], v[112:115]\n" \
"v_exp_f32 v134, v134\n" \
"v_mfma_f32_16x16x32_bf16 v[112:115], a[208:211], v[144:147], v[112:115]\n" \
"ds_read_b128 a[200:203], v242 offset:2048\n" \
"v_mfma_f32_16x16x32_bf16 v[116:119], a[212:215], v[144:147], v[116:119]\n" \
"v_exp_f32 v135, v135\n" \
"v_mfma_f32_16x16x32_bf16 v[116:119], a[244:247], v[152:155], v[116:119]\n" \
"ds_read_b128 a[204:207], v242 offset:3072\n" \
"v_cvt_pk_bf16_f32 v131, v134, v135\n" \
"v_mfma_f32_16x16x32_bf16 v[120:123], a[248:251], v[152:155], v[120:123]\n" \
"ds_read_b128 a[236:239], v242 offset:11264\n" \
"v_mfma_f32_16x16x32_bf16 v[120:123], a[216:219], v[144:147], v[120:123]\n" \
"v_exp_f32 v136, v136\n" \
"v_mfma_f32_16x16x32_bf16 v[124:127], a[220:223], v[144:147], v[124:127]\n" \
"ds_read_b128 a[240:243], v242 offset:12288\n" \
"v_mfma_f32_16x16x32_bf16 v[124:127], a[252:255], v[152:155], v[124:127]\n" \
"s_waitcnt vmcnt(12)\n" \
"v_exp_f32 v137, v137\n" \
"v_mfma_f32_16x16x32_bf16 v[144:147], a[128:131], v[176:179], 0\n" \
"s_barrier\n" \
"ds_read_b128 a[208:211], v242 offset:4096\n" \
"v_cvt_pk_bf16_f32 v136, v136, v137\n" \
"v_mfma_f32_16x16x32_bf16 v[144:147], a[132:135], v[180:183], v[144:147]\n" \
"s_mov_b32 m0, s20\n" \
"v_exp_f32 v138, v138\n" \
"v_mfma_f32_16x16x32_bf16 v[144:147], a[136:139], v[184:187], v[144:147]\n" \
"buffer_load_dwordx4 v248, s[32:35], s89 offen offset:0 sc1 lds\n" \
"v_mfma_f32_16x16x32_bf16 v[144:147], a[140:143], v[188:191], v[144:147]\n" \
"v_exp_f32 v139, v139\n" \
"v_mfma_f32_16x16x32_bf16 v[148:151], a[156:159], v[188:191], 0\n" \
"ds_read_b128 a[212:215], v242 offset:5120\n" \
"v_cvt_pk_bf16_f32 v137, v138, v139\n" \
"v_mfma_f32_16x16x32_bf16 v[148:151], a[152:155], v[184:187], v[148:151]\n" \
"v_exp_f32 v140, v140\n" \
"v_mfma_f32_16x16x32_bf16 v[148:151], a[148:151], v[180:183], v[148:151]\n" \
"ds_read_b128 a[244:247], v242 offset:13312\n" \
"ds_read_b128 a[248:251], v242 offset:14336\n" \
"v_mfma_f32_16x16x32_bf16 v[148:151], a[144:147], v[176:179], v[148:151]\n" \
"v_exp_f32 v141, v141\n" \
"v_mfma_f32_16x16x32_bf16 v[152:155], a[160:163], v[176:179], 0\n" \
"buffer_load_dwordx4 v248, s[32:35], s89 offen offset:1024 sc1 lds\n" \
"v_mfma_f32_16x16x32_bf16 v[152:155], a[164:167], v[180:183], v[152:155]\n" \
"v_exp_f32 v142, v142\n" \
"v_mfma_f32_16x16x32_bf16 v[152:155], a[168:171], v[184:187], v[152:155]\n" \
"ds_read_b128 a[216:219], v242 offset:6144\n" \
"v_cvt_pk_bf16_f32 v138, v140, v141\n" \
"v_mfma_f32_16x16x32_bf16 v[152:155], a[172:175], v[188:191], v[152:155]\n" \
"v_exp_f32 v143, v143\n" \
"v_mfma_f32_16x16x32_bf16 v[156:159], a[188:191], v[188:191], 0\n" \
"ds_read_b128 a[128:131], v240 offset:0\n" \
"v_cvt_pk_bf16_f32 v139, v142, v143\n" \
"v_mfma_f32_16x16x32_bf16 v[156:159], a[184:187], v[184:187], v[156:159]\n" \
"ds_read_b128 a[132:135], v240 offset:1024\n" \
"ds_read_b128 a[220:223], v242 offset:7168\n" \
"v_mfma_f32_16x16x32_bf16 v[156:159], a[180:183], v[180:183], v[156:159]\n" \
"v_exp_f32 v144, v144\n" \
"v_mfma_f32_16x16x32_bf16 v[156:159], a[176:179], v[176:179], v[156:159]\n" \
"buffer_load_dwordx4 v248, s[32:35], s89 offen offset:2048 sc1 lds\n" \
"v_mfma_f32_16x16x32_bf16 a[40:43], a[48:51], v[128:131], a[40:43]\n" \
"v_exp_f32 v145, v145\n" \
"v_mfma_f32_16x16x32_bf16 a[40:43], a[48:51], v[136:139], a[40:43]\n" \
"s_waitcnt lgkmcnt(13)\n" \
"ds_read_b128 a[136:139], v240 offset:2048\n" \
"v_cvt_pk_bf16_f32 v144, v144, v145\n" \
"v_mfma_f32_16x16x32_bf16 v[64:67], a[224:227], v[136:139], v[64:67]\n" \
"v_exp_f32 v146, v146\n" \
"v_mfma_f32_16x16x32_bf16 v[64:67], a[192:195], v[128:131], v[64:67]\n" \
"ds_read_b128 a[252:255], v242 offset:15360\n" \
"ds_read_b128 a[140:143], v240 offset:3072\n" \
"v_mfma_f32_16x16x32_bf16 v[68:71], a[196:199], v[128:131], v[68:71]\n" \
"v_exp_f32 v147, v147\n" \
"v_mfma_f32_16x16x32_bf16 v[68:71], a[228:231], v[136:139], v[68:71]\n" \
"s_waitcnt lgkmcnt(12)\n" \
"buffer_load_dwordx4 v248, s[32:35], s89 offen offset:3072 sc1 lds\n" \
"v_mfma_f32_16x16x32_bf16 v[72:75], a[232:235], v[136:139], v[72:75]\n" \
"s_add_u32 s20, s20, 0x4000\n" \
"v_exp_f32 v148, v148\n" \
"v_mfma_f32_16x16x32_bf16 v[72:75], a[200:203], v[128:131], v[72:75]\n" \
"s_and_b32 s20, s20, 0xffff\n" \
"ds_read_b128 a[156:159], v240 offset:7168\n" \
"v_cvt_pk_bf16_f32 v145, v146, v147\n" \
"v_mfma_f32_16x16x32_bf16 v[76:79], a[204:207], v[128:131], v[76:79]\n" \
"s_bitset1_b32 s20, 16\n" \
"v_exp_f32 v149, v149\n" \
"v_mfma_f32_16x16x32_bf16 v[76:79], a[236:239], v[136:139], v[76:79]\n" \
"s_waitcnt lgkmcnt(9)\n" \
"ds_read_b128 a[152:155], v240 offset:6144\n" \
"v_cvt_pk_bf16_f32 v146, v148, v149\n" \
"v_mfma_f32_16x16x32_bf16 v[80:83], a[240:243], v[136:139], v[80:83]\n" \
"v_exp_f32 v150, v150\n" \
"v_mfma_f32_16x16x32_bf16 v[80:83], a[208:211], v[128:131], v[80:83]\n" \
"ds_read_b128 a[148:151], v240 offset:5120\n" \
"ds_read_b128 a[144:147], v240 offset:4096\n" \
"v_mfma_f32_16x16x32_bf16 v[84:87], a[212:215], v[128:131], v[84:87]\n" \
"v_exp_f32 v151, v151\n" \
"v_mfma_f32_16x16x32_bf16 v[84:87], a[244:247], v[136:139], v[84:87]\n" \
"s_waitcnt lgkmcnt(5)\n" \
"ds_read_b128 a[160:163], v240 offset:8192\n" \
"v_cvt_pk_bf16_f32 v147, v150, v151\n" \
"v_mfma_f32_16x16x32_bf16 v[88:91], a[248:251], v[136:139], v[88:91]\n" \
"ds_read_b128 a[164:167], v240 offset:9216\n" \
"ds_read_b128 a[168:171], v240 offset:10240\n" \
"v_mfma_f32_16x16x32_bf16 v[88:91], a[216:219], v[128:131], v[88:91]\n" \
"v_exp_f32 v152, v152\n" \
"v_mfma_f32_16x16x32_bf16 v[92:95], a[220:223], v[128:131], v[92:95]\n" \
"ds_read_b128 a[172:175], v240 offset:11264\n" \
"ds_read_b128 a[188:191], v240 offset:15360\n" \
"v_mfma_f32_16x16x32_bf16 v[92:95], a[252:255], v[136:139], v[92:95]\n" \
"s_waitcnt lgkmcnt(9)\n" \
"v_exp_f32 v153, v153\n" \
"v_mfma_f32_16x16x32_bf16 v[128:131], a[128:131], v[160:163], 0\n" \
"s_add_u32 s86, s86, 0x4000\n" \
"ds_read_b128 a[184:187], v240 offset:14336\n" \
"v_cvt_pk_bf16_f32 v152, v152, v153\n" \
"v_mfma_f32_16x16x32_bf16 v[128:131], a[132:135], v[164:167], v[128:131]\n" \
"s_bitset0_b32 s86, 16\n" \
"v_exp_f32 v154, v154\n" \
"v_mfma_f32_16x16x32_bf16 v[128:131], a[136:139], v[168:171], v[128:131]\n" \
"s_cmp_eq_u32 s88, s24\n" \
"s_cbranch_scc1 .Lcht_%=\n" \
".Lsbht_%=:\n" \
"v_mfma_f32_16x16x32_bf16 v[128:131], a[140:143], v[172:175], v[128:131]\n" \
"s_waitcnt lgkmcnt(6)\n" \
"v_exp_f32 v155, v155\n" \
"v_mfma_f32_16x16x32_bf16 v[132:135], a[156:159], v[172:175], 0\n" \
"s_mov_b32 m0, s19\n" \
"v_add_u32 v242, 0x10000, v240\n" \
"v_cvt_pk_bf16_f32 v153, v154, v155\n" \
"v_mfma_f32_16x16x32_bf16 v[132:135], a[152:155], v[168:171], v[132:135]\n" \
"s_cmp_lg_u32 s76, 0\n" \
"ds_read_b128 a[180:183], v240 offset:13312\n" \
"ds_read_b128 a[176:179], v240 offset:12288\n" \
"v_exp_f32 v156, v156\n" \
"v_mfma_f32_16x16x32_bf16 v[132:135], a[148:151], v[164:167], v[132:135]\n" \
"buffer_load_dwordx4 v248, s[32:35], s88 offen offset:0 sc1 lds\n" \
"v_mfma_f32_16x16x32_bf16 v[132:135], a[144:147], v[160:163], v[132:135]\n" \
"s_waitcnt lgkmcnt(4)\n" \
"v_exp_f32 v157, v157\n" \
"v_mfma_f32_16x16x32_bf16 v[136:139], a[160:163], v[160:163], 0\n" \
"s_cbranch_scc1 .Lant_%=\n" \
".Labt_%=:\n" \
"v_cvt_pk_bf16_f32 v154, v156, v157\n" \
"v_mfma_f32_16x16x32_bf16 v[136:139], a[164:167], v[164:167], v[136:139]\n" \
"v_exp_f32 v158, v158\n" \
"v_mfma_f32_16x16x32_bf16 v[136:139], a[168:171], v[168:171], v[136:139]\n" \
"buffer_load_dwordx4 v248, s[32:35], s88 offen offset:1024 sc1 lds\n" \
"v_mfma_f32_16x16x32_bf16 v[136:139], a[172:175], v[172:175], v[136:139]\n" \
"s_waitcnt lgkmcnt(0)\n" \
"v_exp_f32 v159, v159\n" \
"v_mfma_f32_16x16x32_bf16 v[140:143], a[188:191], v[172:175], 0\n" \
"v_add_u32 v240, s86, v248\n" \
"v_cvt_pk_bf16_f32 v155, v158, v159\n" \
"v_mfma_f32_16x16x32_bf16 v[140:143], a[184:187], v[168:171], v[140:143]\n" \
"buffer_load_dwordx4 v248, s[32:35], s88 offen offset:2048 sc1 lds\n" \
"v_mfma_f32_16x16x32_bf16 v[140:143], a[180:183], v[164:167], v[140:143]\n" \
"v_exp_f32 v128, v128\n" \
"v_mfma_f32_16x16x32_bf16 v[140:143], a[176:179], v[160:163], v[140:143]\n" \
"buffer_load_dwordx4 v248, s[32:35], s88 offen offset:3072 sc1 lds\n" \
"v_mfma_f32_16x16x32_bf16 a[44:47], a[48:51], v[144:147], a[44:47]\n" \
"s_add_u32 s89, s88, 589824\n" \
"v_exp_f32 v129, v129\n" \
"v_mfma_f32_16x16x32_bf16 a[44:47], a[48:51], v[152:155], a[44:47]\n" \
"s_add_u32 s88, s88, s27\n" \
"s_add_u32 s19, s19, 0x4000\n" \
"v_cvt_pk_bf16_f32 v128, v128, v129\n" \
"v_mfma_f32_16x16x32_bf16 v[96:99], a[224:227], v[152:155], v[96:99]\n" \
"s_bitset0_b32 s19, 16\n" \
"v_exp_f32 v130, v130\n" \
"v_mfma_f32_16x16x32_bf16 v[96:99], a[192:195], v[144:147], v[96:99]\n" \
"s_cmp_eq_u32 s88, s16\n" \
"s_cmov_b32 s88, s21\n" \
"ds_read_b128 a[224:227], v242 offset:8192\n" \
"v_mfma_f32_16x16x32_bf16 v[100:103], a[196:199], v[144:147], v[100:103]\n" \
"s_cmov_b32 s27, s91\n" \
"v_exp_f32 v131, v131\n" \
"v_mfma_f32_16x16x32_bf16 v[100:103], a[228:231], v[152:155], v[100:103]\n" \
"s_cmov_b32 s16, s77\n" \
"ds_read_b128 a[192:195], v242 offset:0\n" \
"v_cvt_pk_bf16_f32 v129, v130, v131\n" \
"v_mfma_f32_16x16x32_bf16 v[104:107], a[232:235], v[152:155], v[104:107]\n" \
"s_cmov_b32 s21, s92\n" \
"v_exp_f32 v132, v132\n" \
"v_mfma_f32_16x16x32_bf16 v[104:107], a[200:203], v[144:147], v[104:107]\n" \
"s_cmovk_i32 s91, 0x4000\n" \
"ds_read_b128 a[196:199], v242 offset:1024\n" \
"ds_read_b128 a[228:231], v242 offset:9216\n" \
"v_mfma_f32_16x16x32_bf16 v[108:111], a[204:207], v[144:147], v[108:111]\n" \
"s_cmp_eq_u32 s88, s17\n" \
"v_exp_f32 v133, v133\n" \
"v_mfma_f32_16x16x32_bf16 v[108:111], a[236:239], v[152:155], v[108:111]\n" \
"s_cmov_b32 s88, s18\n" \
"ds_read_b128 a[232:235], v242 offset:10240\n" \
"v_cvt_pk_bf16_f32 v130, v132, v133\n" \
"v_mfma_f32_16x16x32_bf16 v[112:115], a[240:243], v[152:155], v[112:115]\n" \
"v_exp_f32 v134, v134\n" \
"v_mfma_f32_16x16x32_bf16 v[112:115], a[208:211], v[144:147], v[112:115]\n" \
"ds_read_b128 a[200:203], v242 offset:2048\n" \
"v_mfma_f32_16x16x32_bf16 v[116:119], a[212:215], v[144:147], v[116:119]\n" \
"v_exp_f32 v135, v135\n" \
"v_mfma_f32_16x16x32_bf16 v[116:119], a[244:247], v[152:155], v[116:119]\n" \
"ds_read_b128 a[204:207], v242 offset:3072\n" \
"v_cvt_pk_bf16_f32 v131, v134, v135\n" \
"v_mfma_f32_16x16x32_bf16 v[120:123], a[248:251], v[152:155], v[120:123]\n" \
"ds_read_b128 a[236:239], v242 offset:11264\n" \
"v_mfma_f32_16x16x32_bf16 v[120:123], a[216:219], v[144:147], v[120:123]\n" \
"v_exp_f32 v136, v136\n" \
"v_mfma_f32_16x16x32_bf16 v[124:127], a[220:223], v[144:147], v[124:127]\n" \
"ds_read_b128 a[240:243], v242 offset:12288\n" \
"v_mfma_f32_16x16x32_bf16 v[124:127], a[252:255], v[152:155], v[124:127]\n" \
"s_waitcnt vmcnt(12)\n" \
"v_exp_f32 v137, v137\n" \
"v_mfma_f32_16x16x32_bf16 v[144:147], a[128:131], v[176:179], 0\n" \
"s_barrier\n" \
"ds_read_b128 a[208:211], v242 offset:4096\n" \
"v_cvt_pk_bf16_f32 v136, v136, v137\n" \
"v_mfma_f32_16x16x32_bf16 v[144:147], a[132:135], v[180:183], v[144:147]\n" \
"s_mov_b32 m0, s20\n" \
"v_exp_f32 v138, v138\n" \
"v_mfma_f32_16x16x32_bf16 v[144:147], a[136:139], v[184:187], v[144:147]\n" \
"buffer_load_dwordx4 v248, s[32:35], s89 offen offset:0 sc1 lds\n" \
"v_mfma_f32_16x16x32_bf16 v[144:147], a[140:143], v[188:191], v[144:147]\n" \
"v_exp_f32 v139, v139\n" \
"v_mfma_f32_16x16x32_bf16 v[148:151], a[156:159], v[188:191], 0\n" \
"ds_read_b128 a[212:215], v242 offset:5120\n" \
"v_cvt_pk_bf16_f32 v137, v138, v139\n" \
"v_mfma_f32_16x16x32_bf16 v[148:151], a[152:155], v[184:187], v[148:151]\n" \
"v_exp_f32 v140, v140\n" \
"v_mfma_f32_16x16x32_bf16 v[148:151], a[148:151], v[180:183], v[148:151]\n" \
"ds_read_b128 a[244:247], v242 offset:13312\n" \
"ds_read_b128 a[248:251], v242 offset:14336\n" \
"v_mfma_f32_16x16x32_bf16 v[148:151], a[144:147], v[176:179], v[148:151]\n" \
"v_exp_f32 v141, v141\n" \
"v_mfma_f32_16x16x32_bf16 v[152:155], a[160:163], v[176:179], 0\n" \
".Lst1_2_%=:\n" \
"s_cbranch_execz .Lst1_6_%=\n" \
"buffer_load_dwordx4 v248, s[32:35], s89 offen offset:1024 sc1 lds\n" \
"v_mfma_f32_16x16x32_bf16 v[152:155], a[164:167], v[180:183], v[152:155]\n" \
"v_exp_f32 v142, v142\n" \
"v_mfma_f32_16x16x32_bf16 v[152:155], a[168:171], v[184:187], v[152:155]\n" \
"ds_read_b128 a[216:219], v242 offset:6144\n" \
"v_cvt_pk_bf16_f32 v138, v140, v141\n" \
"v_mfma_f32_16x16x32_bf16 v[152:155], a[172:175], v[188:191], v[152:155]\n" \
"v_exp_f32 v143, v143\n" \
"v_mfma_f32_16x16x32_bf16 v[156:159], a[188:191], v[188:191], 0\n" \
"ds_read_b128 a[128:131], v240 offset:0\n" \
"v_cvt_pk_bf16_f32 v139, v142, v143\n" \
"v_mfma_f32_16x16x32_bf16 v[156:159], a[184:187], v[184:187], v[156:159]\n" \
"ds_read_b128 a[132:135], v240 offset:1024\n" \
"ds_read_b128 a[220:223], v242 offset:7168\n" \
"v_mfma_f32_16x16x32_bf16 v[156:159], a[180:183], v[180:183], v[156:159]\n" \
"v_exp_f32 v144, v144\n" \
"v_mfma_f32_16x16x32_bf16 v[156:159], a[176:179], v[176:179], v[156:159]\n" \
"buffer_load_dwordx4 v248, s[32:35], s89 offen offset:2048 sc1 lds\n" \
"v_mfma_f32_16x16x32_bf16 a[40:43], a[48:51], v[128:131], a[40:43]\n" \
"v_exp_f32 v145, v145\n" \
"v_mfma_f32_16x16x32_bf16 a[40:43], a[48:51], v[136:139], a[40:43]\n" \
"s_waitcnt lgkmcnt(13)\n" \
"ds_read_b128 a[136:139], v240 offset:2048\n" \
"v_cvt_pk_bf16_f32 v144, v144, v145\n" \
"v_mfma_f32_16x16x32_bf16 v[64:67], a[224:227], v[136:139], v[64:67]\n" \
"v_exp_f32 v146, v146\n" \
"v_mfma_f32_16x16x32_bf16 v[64:67], a[192:195], v[128:131], v[64:67]\n" \
"ds_read_b128 a[252:255], v242 offset:15360\n" \
"ds_read_b128 a[140:143], v240 offset:3072\n" \
"v_mfma_f32_16x16x32_bf16 v[68:71], a[196:199], v[128:131], v[68:71]\n" \
"v_exp_f32 v147, v147\n" \
"v_mfma_f32_16x16x32_bf16 v[68:71], a[228:231], v[136:139], v[68:71]\n" \
"s_waitcnt lgkmcnt(12)\n" \
"buffer_load_dwordx4 v248, s[32:35], s89 offen offset:3072 sc1 lds\n" \
"v_mfma_f32_16x16x32_bf16 v[72:75], a[232:235], v[136:139], v[72:75]\n" \
"s_add_u32 s20, s20, 0x4000\n" \
"v_exp_f32 v148, v148\n" \
"v_mfma_f32_16x16x32_bf16 v[72:75], a[200:203], v[128:131], v[72:75]\n" \
"s_and_b32 s20, s20, 0xffff\n" \
"ds_read_b128 a[156:159], v240 offset:7168\n" \
"v_cvt_pk_bf16_f32 v145, v146, v147\n" \
"v_mfma_f32_16x16x32_bf16 v[76:79], a[204:207], v[128:131], v[76:79]\n" \
"s_bitset1_b32 s20, 16\n" \
"v_exp_f32 v149, v149\n" \
"v_mfma_f32_16x16x32_bf16 v[76:79], a[236:239], v[136:139], v[76:79]\n" \
"s_waitcnt lgkmcnt(9)\n" \
"ds_read_b128 a[152:155], v240 offset:6144\n" \
"v_cvt_pk_bf16_f32 v146, v148, v149\n" \
"v_mfma_f32_16x16x32_bf16 v[80:83], a[240:243], v[136:139], v[80:83]\n" \
"v_exp_f32 v150, v150\n" \
"v_mfma_f32_16x16x32_bf16 v[80:83], a[208:211], v[128:131], v[80:83]\n" \
"ds_read_b128 a[148:151], v240 offset:5120\n" \
"ds_read_b128 a[144:147], v240 offset:4096\n" \
"v_mfma_f32_16x16x32_bf16 v[84:87], a[212:215], v[128:131], v[84:87]\n" \
"s_sub_u32 s95, s95, 1\n" \
"v_exp_f32 v151, v151\n" \
"v_mfma_f32_16x16x32_bf16 v[84:87], a[244:247], v[136:139], v[84:87]\n" \
"s_waitcnt lgkmcnt(5)\n" \
"ds_read_b128 a[160:163], v240 offset:8192\n" \
"v_cvt_pk_bf16_f32 v147, v150, v151\n" \
"v_mfma_f32_16x16x32_bf16 v[88:91], a[248:251], v[136:139], v[88:91]\n" \
"ds_read_b128 a[164:167], v240 offset:9216\n" \
"ds_read_b128 a[168:171], v240 offset:10240\n" \
"v_mfma_f32_16x16x32_bf16 v[88:91], a[216:219], v[128:131], v[88:91]\n" \
"v_exp_f32 v152, v152\n" \
"v_mfma_f32_16x16x32_bf16 v[92:95], a[220:223], v[128:131], v[92:95]\n" \
"v_exp_f32 v153, v153\n" \
"v_mfma_f32_16x16x32_bf16 v[92:95], a[252:255], v[136:139], v[92:95]\n" \
"s_waitcnt lgkmcnt(7)\n" \
"ds_read_b128 a[172:175], v240 offset:11264\n" \
"s_cbranch_scc0 .Lloop3_%=\n" \
".Lst1_3_%=:\n" \
"s_cbranch_execz .Lst1_7_%=\n" \
  \
"v_mfma_f32_16x16x32_bf16 v[128:131], a[128:131], v[160:163], 0\n" \
"s_add_u32 s86, s86, 0x4000\n" \
"v_and_b32 v255, 3, v251\n" \
"v_lshlrev_b32 v255, 2, v255\n" \
"v_mfma_f32_16x16x32_bf16 v[128:131], a[132:135], v[164:167], v[128:131]\n" \
"s_bitset0_b32 s86, 16\n" \
"v_exp_f32 v154, v154\n" \
"v_mfma_f32_16x16x32_bf16 v[128:131], a[136:139], v[168:171], v[128:131]\n" \
"buffer_load_dword v247, v255, s[32:35], s31 offen sc1\n" \
"v_add_u32 v242, 0x10000, v240\n" \
"v_mfma_f32_16x16x32_bf16 v[128:131], a[140:143], v[172:175], v[128:131]\n" \
"s_waitcnt lgkmcnt(4)\n" \
"v_exp_f32 v155, v155\n" \
"v_mfma_f32_16x16x32_bf16 v[132:135], a[156:159], v[172:175], 0\n" \
"s_mov_b32 m0, s19\n" \
"v_cvt_pk_bf16_f32 v152, v152, v153\n" \
"v_cvt_pk_bf16_f32 v153, v154, v155\n" \
"v_mfma_f32_16x16x32_bf16 v[132:135], a[152:155], v[168:171], v[132:135]\n" \
"ds_read_b128 a[188:191], v240 offset:15360\n" \
"ds_read_b128 a[184:187], v240 offset:14336\n" \
"ds_read_b128 a[180:183], v240 offset:13312\n" \
"ds_read_b128 a[176:179], v240 offset:12288\n" \
"v_exp_f32 v156, v156\n" \
"v_mfma_f32_16x16x32_bf16 v[132:135], a[148:151], v[164:167], v[132:135]\n" \
"buffer_load_dwordx4 v248, s[32:35], s88 offen offset:0 sc1 lds\n" \
"v_mfma_f32_16x16x32_bf16 v[132:135], a[144:147], v[160:163], v[132:135]\n" \
"s_waitcnt lgkmcnt(4)\n" \
"v_exp_f32 v157, v157\n" \
"v_mfma_f32_16x16x32_bf16 v[136:139], a[160:163], v[160:163], 0\n" \
"v_cvt_pk_bf16_f32 v154, v156, v157\n" \
"v_mfma_f32_16x16x32_bf16 v[136:139], a[164:167], v[164:167], v[136:139]\n" \
"v_exp_f32 v158, v158\n" \
"v_mfma_f32_16x16x32_bf16 v[136:139], a[168:171], v[168:171], v[136:139]\n" \
"buffer_load_dwordx4 v248, s[32:35], s88 offen offset:1024 sc1 lds\n" \
"v_mfma_f32_16x16x32_bf16 v[136:139], a[172:175], v[172:175], v[136:139]\n" \
"s_waitcnt lgkmcnt(0)\n" \
"v_exp_f32 v159, v159\n" \
"v_mfma_f32_16x16x32_bf16 v[140:143], a[188:191], v[172:175], 0\n" \
"v_add_u32 v240, s86, v248\n" \
"v_cvt_pk_bf16_f32 v155, v158, v159\n" \
"v_mfma_f32_16x16x32_bf16 v[140:143], a[184:187], v[168:171], v[140:143]\n" \
"buffer_load_dwordx4 v248, s[32:35], s88 offen offset:2048 sc1 lds\n" \
"v_mfma_f32_16x16x32_bf16 v[140:143], a[180:183], v[164:167], v[140:143]\n" \
"v_exp_f32 v128, v128\n" \
"v_mfma_f32_16x16x32_bf16 v[140:143], a[176:179], v[160:163], v[140:143]\n" \
"buffer_load_dwordx4 v248, s[32:35], s88 offen offset:3072 sc1 lds\n" \
"v_mfma_f32_16x16x32_bf16 a[44:47], a[48:51], v[144:147], a[44:47]\n" \
"s_add_u32 s89, s88, 589824\n" \
"v_exp_f32 v129, v129\n" \
"v_mfma_f32_16x16x32_bf16 a[44:47], a[48:51], v[152:155], a[44:47]\n" \
"s_add_u32 s88, s88, s27\n" \
"s_add_u32 s19, s19, 0x4000\n" \
"v_cvt_pk_bf16_f32 v128, v128, v129\n" \
"v_mfma_f32_16x16x32_bf16 v[96:99], a[224:227], v[152:155], v[96:99]\n" \
"s_bitset0_b32 s19, 16\n" \
"v_exp_f32 v130, v130\n" \
"v_mfma_f32_16x16x32_bf16 v[96:99], a[192:195], v[144:147], v[96:99]\n" \
"s_cmp_eq_u32 s88, s16\n" \
"s_cmov_b32 s88, s21\n" \
"ds_read_b128 a[224:227], v242 offset:8192\n" \
"v_mfma_f32_16x16x32_bf16 v[100:103], a[196:199], v[144:147], v[100:103]\n" \
"s_cmov_b32 s27, s91\n" \
"v_exp_f32 v131, v131\n" \
"v_mfma_f32_16x16x32_bf16 v[100:103], a[228:231], v[152:155], v[100:103]\n" \
"s_cmov_b32 s16, s77\n" \
"ds_read_b128 a[192:195], v242 offset:0\n" \
"v_cvt_pk_bf16_f32 v129, v130, v131\n" \
"v_mfma_f32_16x16x32_bf16 v[104:107], a[232:235], v[152:155], v[104:107]\n" \
"s_cmov_b32 s21, s92\n" \
"v_exp_f32 v132, v132\n" \
"v_mfma_f32_16x16x32_bf16 v[104:107], a[200:203], v[144:147], v[104:107]\n" \
"s_cmovk_i32 s91, 0x4000\n" \
"ds_read_b128 a[196:199], v242 offset:1024\n" \
"ds_read_b128 a[228:231], v242 offset:9216\n" \
"v_mfma_f32_16x16x32_bf16 v[108:111], a[204:207], v[144:147], v[108:111]\n" \
"s_cmp_eq_u32 s88, s17\n" \
"v_exp_f32 v133, v133\n" \
"v_mfma_f32_16x16x32_bf16 v[108:111], a[236:239], v[152:155], v[108:111]\n" \
"s_cmov_b32 s88, s18\n" \
"ds_read_b128 a[232:235], v242 offset:10240\n" \
"v_cvt_pk_bf16_f32 v130, v132, v133\n" \
"v_mfma_f32_16x16x32_bf16 v[112:115], a[240:243], v[152:155], v[112:115]\n" \
"v_exp_f32 v134, v134\n" \
"v_mfma_f32_16x16x32_bf16 v[112:115], a[208:211], v[144:147], v[112:115]\n" \
"ds_read_b128 a[200:203], v242 offset:2048\n" \
"v_mfma_f32_16x16x32_bf16 v[116:119], a[212:215], v[144:147], v[116:119]\n" \
"v_exp_f32 v135, v135\n" \
"v_mfma_f32_16x16x32_bf16 v[116:119], a[244:247], v[152:155], v[116:119]\n" \
"ds_read_b128 a[204:207], v242 offset:3072\n" \
"v_cvt_pk_bf16_f32 v131, v134, v135\n" \
"v_mfma_f32_16x16x32_bf16 v[120:123], a[248:251], v[152:155], v[120:123]\n" \
"s_waitcnt vmcnt(4)\n" \
"ds_read_b128 a[236:239], v242 offset:11264\n" \
"v_cmp_eq_u32 vcc, 0, v247\n" \
"v_mfma_f32_16x16x32_bf16 v[120:123], a[216:219], v[144:147], v[120:123]\n" \
"v_exp_f32 v136, v136\n" \
"v_mfma_f32_16x16x32_bf16 v[124:127], a[220:223], v[144:147], v[124:127]\n" \
"ds_read_b128 a[240:243], v242 offset:12288\n" \
"v_mfma_f32_16x16x32_bf16 v[124:127], a[252:255], v[152:155], v[124:127]\n" \
"s_waitcnt vmcnt(13)\n" \
"v_exp_f32 v137, v137\n" \
"v_mfma_f32_16x16x32_bf16 v[144:147], a[128:131], v[176:179], 0\n" \
"s_barrier\n" \
"s_cbranch_vccnz .Lspl_%=\n" \
".Lsbl_%=:\n" \
"v_mfma_f32_16x16x32_bf16 v[144:147], a[132:135], v[180:183], v[144:147]\n" \
"s_mov_b32 m0, s20\n" \
"v_exp_f32 v138, v138\n" \
"v_mfma_f32_16x16x32_bf16 v[144:147], a[136:139], v[184:187], v[144:147]\n" \
"s_add_u32 s38, s66, 1\n" \
"buffer_load_dwordx4 v248, s[32:35], s89 offen offset:0 sc1 lds\n" \
"v_mfma_f32_16x16x32_bf16 v[144:147], a[140:143], v[188:191], v[144:147]\n" \
"s_and_b32 s38, s38, 3\n" \
"v_exp_f32 v139, v139\n" \
"v_mfma_f32_16x16x32_bf16 v[148:151], a[156:159], v[188:191], 0\n" \
"s_lshl_b32 s38, s38, 13\n" \
"v_cvt_pk_bf16_f32 v136, v136, v137\n" \
"v_cvt_pk_bf16_f32 v137, v138, v139\n" \
"v_mfma_f32_16x16x32_bf16 v[148:151], a[152:155], v[184:187], v[148:151]\n" \
"s_add_u32 s38, s38, s29\n" \
"v_exp_f32 v140, v140\n" \
"v_mfma_f32_16x16x32_bf16 v[148:151], a[148:151], v[180:183], v[148:151]\n" \
"buffer_load_dwordx4 v[32:35], v248, s[32:35], s38 offen offset:0 sc1\n" \
"v_mfma_f32_16x16x32_bf16 v[148:151], a[144:147], v[176:179], v[148:151]\n" \
"v_exp_f32 v141, v141\n" \
"v_mfma_f32_16x16x32_bf16 v[152:155], a[160:163], v[176:179], 0\n" \
"buffer_load_dwordx4 v248, s[32:35], s89 offen offset:1024 sc1 lds\n" \
"v_mfma_f32_16x16x32_bf16 v[152:155], a[164:167], v[180:183], v[152:155]\n" \
"v_exp_f32 v142, v142\n" \
"v_mfma_f32_16x16x32_bf16 v[152:155], a[168:171], v[184:187], v[152:155]\n" \
"ds_read_b128 a[208:211], v242 offset:4096\n" \
"v_cvt_pk_bf16_f32 v138, v140, v141\n" \
"v_mfma_f32_16x16x32_bf16 v[152:155], a[172:175], v[188:191], v[152:155]\n" \
"v_exp_f32 v143, v143\n" \
"v_mfma_f32_16x16x32_bf16 v[156:159], a[188:191], v[188:191], 0\n" \
"buffer_load_dwordx4 v[36:39], v248, s[32:35], s38 offen offset:1024 sc1\n" \
"v_cvt_pk_bf16_f32 v139, v142, v143\n" \
"v_mfma_f32_16x16x32_bf16 v[156:159], a[184:187], v[184:187], v[156:159]\n" \
"ds_read_b128 a[212:215], v242 offset:5120\n" \
"ds_read_b128 a[244:247], v242 offset:13312\n" \
"v_mfma_f32_16x16x32_bf16 v[156:159], a[180:183], v[180:183], v[156:159]\n" \
"v_exp_f32 v144, v144\n" \
"v_mfma_f32_16x16x32_bf16 v[156:159], a[176:179], v[176:179], v[156:159]\n" \
"buffer_load_dwordx4 v248, s[32:35], s89 offen offset:2048 sc1 lds\n" \
"v_mfma_f32_16x16x32_bf16 a[40:43], a[48:51], v[128:131], a[40:43]\n" \
"v_exp_f32 v145, v145\n" \
"v_mfma_f32_16x16x32_bf16 a[40:43], a[48:51], v[136:139], a[40:43]\n" \
"s_waitcnt lgkmcnt(8)\n" \
"ds_read_b128 a[248:251], v242 offset:14336\n" \
"v_cvt_pk_bf16_f32 v144, v144, v145\n" \
"v_mfma_f32_16x16x32_bf16 v[64:67], a[224:227], v[136:139], v[64:67]\n" \
"v_exp_f32 v146, v146\n" \
"v_mfma_f32_16x16x32_bf16 v[64:67], a[192:195], v[128:131], v[64:67]\n" \
"ds_read_b128 a[216:219], v242 offset:6144\n" \
"ds_read_b128 a[128:131], v240 offset:0\n" \
"v_mfma_f32_16x16x32_bf16 v[68:71], a[196:199], v[128:131], v[68:71]\n" \
"v_exp_f32 v147, v147\n" \
"v_mfma_f32_16x16x32_bf16 v[68:71], a[228:231], v[136:139], v[68:71]\n" \
"s_waitcnt lgkmcnt(7)\n" \
"buffer_load_dwordx4 v248, s[32:35], s89 offen offset:3072 sc1 lds\n" \
"v_mfma_f32_16x16x32_bf16 v[72:75], a[232:235], v[136:139], v[72:75]\n" \
"s_add_u32 s20, s20, 0x4000\n" \
"v_exp_f32 v148, v148\n" \
"v_mfma_f32_16x16x32_bf16 v[72:75], a[200:203], v[128:131], v[72:75]\n" \
"s_and_b32 s20, s20, 0xffff\n" \
"buffer_load_dwordx4 v[40:43], v248, s[32:35], s38 offen offset:2048 sc1\n" \
"ds_read_b128 a[132:135], v240 offset:1024\n" \
"ds_read_b128 a[220:223], v242 offset:7168\n" \
"ds_read_b128 a[136:139], v240 offset:2048\n" \
"ds_read_b128 a[252:255], v242 offset:15360\n" \
"v_cvt_pk_bf16_f32 v145, v146, v147\n" \
"v_mfma_f32_16x16x32_bf16 v[76:79], a[204:207], v[128:131], v[76:79]\n" \
"s_bitset1_b32 s20, 16\n" \
"v_exp_f32 v149, v149\n" \
"v_mfma_f32_16x16x32_bf16 v[76:79], a[236:239], v[136:139], v[76:79]\n" \
"s_waitcnt lgkmcnt(7)\n" \
"ds_read_b128 a[140:143], v240 offset:3072\n" \
"v_cvt_pk_bf16_f32 v146, v148, v149\n" \
"v_mfma_f32_16x16x32_bf16 v[80:83], a[240:243], v[136:139], v[80:83]\n" \
"v_exp_f32 v150, v150\n" \
"v_mfma_f32_16x16x32_bf16 v[80:83], a[208:211], v[128:131], v[80:83]\n" \
"ds_read_b128 a[156:159], v240 offset:7168\n" \
"ds_read_b128 a[152:155], v240 offset:6144\n" \
"v_mfma_f32_16x16x32_bf16 v[84:87], a[212:215], v[128:131], v[84:87]\n" \
"v_exp_f32 v151, v151\n" \
"v_mfma_f32_16x16x32_bf16 v[84:87], a[244:247], v[136:139], v[84:87]\n" \
"s_waitcnt lgkmcnt(3)\n" \
"buffer_load_dwordx4 v[44:47], v248, s[32:35], s38 offen offset:3072 sc1\n" \
"v_cvt_pk_bf16_f32 v147, v150, v151\n" \
"v_mfma_f32_16x16x32_bf16 v[88:91], a[248:251], v[136:139], v[88:91]\n" \
"ds_read_b128 a[148:151], v240 offset:5120\n" \
"ds_read_b128 a[144:147], v240 offset:4096\n" \
"v_mfma_f32_16x16x32_bf16 v[88:91], a[216:219], v[128:131], v[88:91]\n" \
"v_exp_f32 v152, v152\n" \
"v_mfma_f32_16x16x32_bf16 v[92:95], a[220:223], v[128:131], v[92:95]\n" \
"ds_read_b128 a[160:163], v240 offset:8192\n" \
"ds_read_b128 a[164:167], v240 offset:9216\n" \
"v_mfma_f32_16x16x32_bf16 v[92:95], a[252:255], v[136:139], v[92:95]\n" \
"s_waitcnt lgkmcnt(6)\n" \
"v_exp_f32 v153, v153\n" \
"v_mfma_f32_16x16x32_bf16 v[128:131], a[128:131], v[160:163], 0\n" \
"s_add_u32 s86, s86, 0x4000\n" \
"ds_read_b128 a[168:171], v240 offset:10240\n" \
"v_cvt_pk_bf16_f32 v152, v152, v153\n" \
"v_mfma_f32_16x16x32_bf16 v[128:131], a[132:135], v[164:167], v[128:131]\n" \
"s_bitset0_b32 s86, 16\n" \
"ds_read_b128 a[172:175], v240 offset:11264\n" \
"v_exp_f32 v154, v154\n" \
"v_mfma_f32_16x16x32_bf16 v[128:131], a[136:139], v[168:171], v[128:131]\n" \
"s_add_u32 s38, s38, 4096\n" \
"buffer_load_dwordx4 v[48:51], v248, s[32:35], s38 offen offset:0 sc1\n" \
"v_add_u32 v242, 0x10000, v240\n" \
"v_mfma_f32_16x16x32_bf16 v[128:131], a[140:143], v[172:175], v[128:131]\n" \
"s_waitcnt lgkmcnt(4)\n" \
"v_exp_f32 v155, v155\n" \
"v_mfma_f32_16x16x32_bf16 v[132:135], a[156:159], v[172:175], 0\n" \
"s_mov_b32 m0, s19\n" \
"ds_read_b128 a[188:191], v240 offset:15360\n" \
"v_cvt_pk_bf16_f32 v153, v154, v155\n" \
"v_mfma_f32_16x16x32_bf16 v[132:135], a[152:155], v[168:171], v[132:135]\n" \
"ds_read_b128 a[184:187], v240 offset:14336\n" \
"ds_read_b128 a[180:183], v240 offset:13312\n" \
"ds_read_b128 a[176:179], v240 offset:12288\n" \
"v_exp_f32 v156, v156\n" \
"v_mfma_f32_16x16x32_bf16 v[132:135], a[148:151], v[164:167], v[132:135]\n" \
"buffer_load_dwordx4 v248, s[32:35], s88 offen offset:0 sc1 lds\n" \
"v_mfma_f32_16x16x32_bf16 v[132:135], a[144:147], v[160:163], v[132:135]\n" \
"s_waitcnt lgkmcnt(4)\n" \
"v_exp_f32 v157, v157\n" \
"v_mfma_f32_16x16x32_bf16 v[136:139], a[160:163], v[160:163], 0\n" \
"v_cvt_pk_bf16_f32 v154, v156, v157\n" \
"v_mfma_f32_16x16x32_bf16 v[136:139], a[164:167], v[164:167], v[136:139]\n" \
"v_exp_f32 v158, v158\n" \
"v_mfma_f32_16x16x32_bf16 v[136:139], a[168:171], v[168:171], v[136:139]\n" \
"buffer_load_dwordx4 v248, s[32:35], s88 offen offset:1024 sc1 lds\n" \
"v_mfma_f32_16x16x32_bf16 v[136:139], a[172:175], v[172:175], v[136:139]\n" \
"s_waitcnt lgkmcnt(0)\n" \
"v_exp_f32 v159, v159\n" \
"v_mfma_f32_16x16x32_bf16 v[140:143], a[188:191], v[172:175], 0\n" \
"buffer_load_dwordx4 v[52:55], v248, s[32:35], s38 offen offset:1024 sc1\n" \
"v_add_u32 v240, s86, v248\n" \
"v_mfma_f32_16x16x32_bf16 v[140:143], a[184:187], v[168:171], v[140:143]\n" \
"buffer_load_dwordx4 v248, s[32:35], s88 offen offset:2048 sc1 lds\n" \
"v_mfma_f32_16x16x32_bf16 v[140:143], a[180:183], v[164:167], v[140:143]\n" \
"v_exp_f32 v128, v128\n" \
"v_mfma_f32_16x16x32_bf16 v[140:143], a[176:179], v[160:163], v[140:143]\n" \
"buffer_load_dwordx4 v248, s[32:35], s88 offen offset:3072 sc1 lds\n" \
"v_cvt_pk_bf16_f32 v155, v158, v159\n" \
"v_mfma_f32_16x16x32_bf16 a[44:47], a[48:51], v[144:147], a[44:47]\n" \
"s_add_u32 s89, s88, 589824\n" \
"v_exp_f32 v129, v129\n" \
"v_mfma_f32_16x16x32_bf16 a[44:47], a[48:51], v[152:155], a[44:47]\n" \
"s_add_u32 s88, s88, s27\n" \
"s_add_u32 s19, s19, 0x4000\n" \
"v_cvt_pk_bf16_f32 v128, v128, v129\n" \
"v_mfma_f32_16x16x32_bf16 v[96:99], a[224:227], v[152:155], v[96:99]\n" \
"s_bitset0_b32 s19, 16\n" \
"v_exp_f32 v130, v130\n" \
"v_mfma_f32_16x16x32_bf16 v[96:99], a[192:195], v[144:147], v[96:99]\n" \
"s_cmp_eq_u32 s88, s16\n" \
"s_cmov_b32 s88, s21\n" \
"buffer_load_dwordx4 v[56:59], v248, s[32:35], s38 offen offset:2048 sc1\n" \
"v_mfma_f32_16x16x32_bf16 v[100:103], a[196:199], v[144:147], v[100:103]\n" \
"s_cmov_b32 s27, s91\n" \
"v_exp_f32 v131, v131\n" \
"v_mfma_f32_16x16x32_bf16 v[100:103], a[228:231], v[152:155], v[100:103]\n" \
"s_cmov_b32 s16, s77\n" \
"ds_read_b128 a[224:227], v242 offset:8192\n" \
"v_cvt_pk_bf16_f32 v129, v130, v131\n" \
"v_mfma_f32_16x16x32_bf16 v[104:107], a[232:235], v[152:155], v[104:107]\n" \
"s_cmov_b32 s21, s92\n" \
"v_exp_f32 v132, v132\n" \
"v_mfma_f32_16x16x32_bf16 v[104:107], a[200:203], v[144:147], v[104:107]\n" \
"s_cmovk_i32 s91, 0x4000\n" \
"ds_read_b128 a[192:195], v242 offset:0\n" \
"ds_read_b128 a[196:199], v242 offset:1024\n" \
"v_mfma_f32_16x16x32_bf16 v[108:111], a[204:207], v[144:147], v[108:111]\n" \
"s_cmp_eq_u32 s88, s17\n" \
"v_exp_f32 v133, v133\n" \
"v_mfma_f32_16x16x32_bf16 v[108:111], a[236:239], v[152:155], v[108:111]\n" \
"s_cmov_b32 s88, s18\n" \
"ds_read_b128 a[228:231], v242 offset:9216\n" \
"v_cvt_pk_bf16_f32 v130, v132, v133\n" \
"v_mfma_f32_16x16x32_bf16 v[112:115], a[240:243], v[152:155], v[112:115]\n" \
"v_exp_f32 v134, v134\n" \
"v_mfma_f32_16x16x32_bf16 v[112:115], a[208:211], v[144:147], v[112:115]\n" \
"buffer_load_dwordx4 v[60:63], v248, s[32:35], s38 offen offset:3072 sc1\n" \
"v_mfma_f32_16x16x32_bf16 v[116:119], a[212:215], v[144:147], v[116:119]\n" \
"v_exp_f32 v135, v135\n" \
"v_mfma_f32_16x16x32_bf16 v[116:119], a[244:247], v[152:155], v[116:119]\n" \
"ds_read_b128 a[232:235], v242 offset:10240\n" \
"v_cvt_pk_bf16_f32 v131, v134, v135\n" \
"v_mfma_f32_16x16x32_bf16 v[120:123], a[248:251], v[152:155], v[120:123]\n" \
"s_add_u32 s38, s66, 2\n" \
"ds_read_b128 a[200:203], v242 offset:2048\n" \
"ds_read_b128 a[204:207], v242 offset:3072\n" \
"v_mfma_f32_16x16x32_bf16 v[120:123], a[216:219], v[144:147], v[120:123]\n" \
"s_and_b32 s38, s38, 3\n" \
"v_exp_f32 v136, v136\n" \
"v_mfma_f32_16x16x32_bf16 v[124:127], a[220:223], v[144:147], v[124:127]\n" \
"s_lshl_b32 s38, s38, 13\n" \
"ds_read_b128 a[236:239], v242 offset:11264\n" \
"ds_read_b128 a[240:243], v242 offset:12288\n" \
"v_mfma_f32_16x16x32_bf16 v[124:127], a[252:255], v[152:155], v[124:127]\n" \
"s_waitcnt vmcnt(21)\n" \
"v_exp_f32 v137, v137\n" \
"v_mfma_f32_16x16x32_bf16 v[144:147], a[128:131], v[176:179], 0\n" \
"s_barrier\n" \
"ds_read_b128 a[208:211], v242 offset:4096\n" \
"v_cvt_pk_bf16_f32 v136, v136, v137\n" \
"v_mfma_f32_16x16x32_bf16 v[144:147], a[132:135], v[180:183], v[144:147]\n" \
"s_mov_b32 m0, s20\n" \
"v_exp_f32 v138, v138\n" \
"v_mfma_f32_16x16x32_bf16 v[144:147], a[136:139], v[184:187], v[144:147]\n" \
"s_add_u32 s38, s38, s29\n" \
"buffer_load_dwordx4 v248, s[32:35], s89 offen offset:0 sc1 lds\n" \
"v_mfma_f32_16x16x32_bf16 v[144:147], a[140:143], v[188:191], v[144:147]\n" \
"buffer_load_dwordx4 a[64:67], v248, s[32:35], s38 offen offset:0 sc1\n" \
"v_exp_f32 v139, v139\n" \
"v_mfma_f32_16x16x32_bf16 v[148:151], a[156:159], v[188:191], 0\n" \
"ds_read_b128 a[212:215], v242 offset:5120\n" \
"v_cvt_pk_bf16_f32 v137, v138, v139\n" \
"v_mfma_f32_16x16x32_bf16 v[148:151], a[152:155], v[184:187], v[148:151]\n" \
"v_exp_f32 v140, v140\n" \
"v_mfma_f32_16x16x32_bf16 v[148:151], a[148:151], v[180:183], v[148:151]\n" \
"ds_read_b128 a[244:247], v242 offset:13312\n" \
"ds_read_b128 a[248:251], v242 offset:14336\n" \
"v_mfma_f32_16x16x32_bf16 v[148:151], a[144:147], v[176:179], v[148:151]\n" \
"v_exp_f32 v141, v141\n" \
"v_mfma_f32_16x16x32_bf16 v[152:155], a[160:163], v[176:179], 0\n" \
"buffer_load_dwordx4 v248, s[32:35], s89 offen offset:1024 sc1 lds\n" \
"v_mfma_f32_16x16x32_bf16 v[152:155], a[164:167], v[180:183], v[152:155]\n" \
"v_exp_f32 v142, v142\n" \
"v_mfma_f32_16x16x32_bf16 v[152:155], a[168:171], v[184:187], v[152:155]\n" \
"buffer_load_dwordx4 a[72:75], v248, s[32:35], s38 offen offset:1024 sc1\n" \
"v_cvt_pk_bf16_f32 v138, v140, v141\n" \
"v_mfma_f32_16x16x32_bf16 v[152:155], a[172:175], v[188:191], v[152:155]\n" \
"v_exp_f32 v143, v143\n" \
"v_mfma_f32_16x16x32_bf16 v[156:159], a[188:191], v[188:191], 0\n" \
"ds_read_b128 a[216:219], v242 offset:6144\n" \
"v_cvt_pk_bf16_f32 v139, v142, v143\n" \
"v_mfma_f32_16x16x32_bf16 v[156:159], a[184:187], v[184:187], v[156:159]\n" \
"ds_read_b128 a[128:131], v240 offset:0\n" \
"ds_read_b128 a[132:135], v240 offset:1024\n" \
"v_mfma_f32_16x16x32_bf16 v[156:159], a[180:183], v[180:183], v[156:159]\n" \
"v_exp_f32 v144, v144\n" \
"v_mfma_f32_16x16x32_bf16 v[156:159], a[176:179], v[176:179], v[156:159]\n" \
"buffer_load_dwordx4 v248, s[32:35], s89 offen offset:2048 sc1 lds\n" \
"v_mfma_f32_16x16x32_bf16 a[40:43], a[48:51], v[128:131], a[40:43]\n" \
"v_exp_f32 v145, v145\n" \
"v_mfma_f32_16x16x32_bf16 a[40:43], a[48:51], v[136:139], a[40:43]\n" \
"s_waitcnt lgkmcnt(12)\n" \
"buffer_load_dwordx4 a[80:83], v248, s[32:35], s38 offen offset:2048 sc1\n" \
"v_cvt_pk_bf16_f32 v144, v144, v145\n" \
"v_mfma_f32_16x16x32_bf16 v[64:67], a[224:227], v[136:139], v[64:67]\n" \
"v_exp_f32 v146, v146\n" \
"v_mfma_f32_16x16x32_bf16 v[64:67], a[192:195], v[128:131], v[64:67]\n" \
"ds_read_b128 a[220:223], v242 offset:7168\n" \
"ds_read_b128 a[136:139], v240 offset:2048\n" \
"v_mfma_f32_16x16x32_bf16 v[68:71], a[196:199], v[128:131], v[68:71]\n" \
"v_exp_f32 v147, v147\n" \
"v_mfma_f32_16x16x32_bf16 v[68:71], a[228:231], v[136:139], v[68:71]\n" \
"s_waitcnt lgkmcnt(10)\n" \
"buffer_load_dwordx4 v248, s[32:35], s89 offen offset:3072 sc1 lds\n" \
"v_mfma_f32_16x16x32_bf16 v[72:75], a[232:235], v[136:139], v[72:75]\n" \
"s_add_u32 s20, s20, 0x4000\n" \
"v_exp_f32 v148, v148\n" \
"v_mfma_f32_16x16x32_bf16 v[72:75], a[200:203], v[128:131], v[72:75]\n" \
"s_and_b32 s20, s20, 0xffff\n" \
"ds_read_b128 a[252:255], v242 offset:15360\n" \
"v_cvt_pk_bf16_f32 v145, v146, v147\n" \
"v_mfma_f32_16x16x32_bf16 v[76:79], a[204:207], v[128:131], v[76:79]\n" \
"s_bitset1_b32 s20, 16\n" \
"v_exp_f32 v149, v149\n" \
"v_mfma_f32_16x16x32_bf16 v[76:79], a[236:239], v[136:139], v[76:79]\n" \
"s_waitcnt lgkmcnt(7)\n" \
"buffer_load_dwordx4 a[88:91], v248, s[32:35], s38 offen offset:3072 sc1\n" \
"v_cvt_pk_bf16_f32 v146, v148, v149\n" \
"v_mfma_f32_16x16x32_bf16 v[80:83], a[240:243], v[136:139], v[80:83]\n" \
"v_exp_f32 v150, v150\n" \
"v_mfma_f32_16x16x32_bf16 v[80:83], a[208:211], v[128:131], v[80:83]\n" \
"ds_read_b128 a[140:143], v240 offset:3072\n" \
"ds_read_b128 a[156:159], v240 offset:7168\n" \
"v_mfma_f32_16x16x32_bf16 v[84:87], a[212:215], v[128:131], v[84:87]\n" \
"v_exp_f32 v151, v151\n" \
"v_mfma_f32_16x16x32_bf16 v[84:87], a[244:247], v[136:139], v[84:87]\n" \
"s_waitcnt lgkmcnt(2)\n" \
"ds_read_b128 a[152:155], v240 offset:6144\n" \
"v_cvt_pk_bf16_f32 v147, v150, v151\n" \
"v_mfma_f32_16x16x32_bf16 v[88:91], a[248:251], v[136:139], v[88:91]\n" \
"ds_read_b128 a[148:151], v240 offset:5120\n" \
"ds_read_b128 a[144:147], v240 offset:4096\n" \
"v_mfma_f32_16x16x32_bf16 v[88:91], a[216:219], v[128:131], v[88:91]\n" \
"v_exp_f32 v152, v152\n" \
"v_mfma_f32_16x16x32_bf16 v[92:95], a[220:223], v[128:131], v[92:95]\n" \
"v_exp_f32 v153, v153\n" \
"v_mfma_f32_16x16x32_bf16 v[92:95], a[252:255], v[136:139], v[92:95]\n" \
"s_waitcnt lgkmcnt(4)\n" \
"ds_read_b128 a[160:163], v240 offset:8192\n" \
"ds_read_b128 a[164:167], v240 offset:9216\n" \
"ds_read_b128 a[168:171], v240 offset:10240\n" \
"ds_read_b128 a[172:175], v240 offset:11264\n" \
".Llast_%=:\n" \
".Lst1_4_%=:\n" \
"s_cbranch_execz .Lst1_8_%=\n" \
  \
"v_mfma_f32_16x16x32_bf16 v[128:131], a[128:131], v[160:163], 0\n" \
"s_add_u32 s86, s86, 0x4000\n" \
"ds_read_b128 a[188:191], v240 offset:15360\n" \
"v_cvt_pk_bf16_f32 v152, v152, v153\n" \
"v_mfma_f32_16x16x32_bf16 v[128:131], a[132:135], v[164:167], v[128:131]\n" \
"s_bitset0_b32 s86, 16\n" \
"v_exp_f32 v154, v154\n" \
"v_mfma_f32_16x16x32_bf16 v[128:131], a[136:139], v[168:171], v[128:131]\n" \
"ds_read_b128 a[184:187], v240 offset:14336\n" \
"v_add_u32 v242, 0x10000, v240\n" \
"v_mfma_f32_16x16x32_bf16 v[128:131], a[140:143], v[172:175], v[128:131]\n" \
"s_waitcnt lgkmcnt(6)\n" \
"v_exp_f32 v155, v155\n" \
"v_mfma_f32_16x16x32_bf16 v[132:135], a[156:159], v[172:175], 0\n" \
"s_mov_b32 m0, s19\n" \
"ds_read_b128 a[180:183], v240 offset:13312\n" \
"v_cvt_pk_bf16_f32 v153, v154, v155\n" \
"v_mfma_f32_16x16x32_bf16 v[132:135], a[152:155], v[168:171], v[132:135]\n" \
"ds_read_b128 a[176:179], v240 offset:12288\n" \
"v_exp_f32 v156, v156\n" \
"v_mfma_f32_16x16x32_bf16 v[132:135], a[148:151], v[164:167], v[132:135]\n" \
"buffer_load_dwordx4 v248, s[32:35], s88 offen offset:0 sc1 lds\n" \
"v_mfma_f32_16x16x32_bf16 v[132:135], a[144:147], v[160:163], v[132:135]\n" \
"s_waitcnt lgkmcnt(4)\n" \
"v_exp_f32 v157, v157\n" \
"v_mfma_f32_16x16x32_bf16 v[136:139], a[160:163], v[160:163], 0\n" \
"v_cvt_pk_bf16_f32 v154, v156, v157\n" \
"v_mfma_f32_16x16x32_bf16 v[136:139], a[164:167], v[164:167], v[136:139]\n" \
"v_exp_f32 v158, v158\n" \
"v_mfma_f32_16x16x32_bf16 v[136:139], a[168:171], v[168:171], v[136:139]\n" \
"buffer_load_dwordx4 v248, s[32:35], s88 offen offset:1024 sc1 lds\n" \
"v_mfma_f32_16x16x32_bf16 v[136:139], a[172:175], v[172:175], v[136:139]\n" \
"s_waitcnt lgkmcnt(0)\n" \
"v_exp_f32 v159, v159\n" \
"v_mfma_f32_16x16x32_bf16 v[140:143], a[188:191], v[172:175], 0\n" \
"v_add_u32 v240, s86, v248\n" \
"v_cvt_pk_bf16_f32 v155, v158, v159\n" \
"v_mfma_f32_16x16x32_bf16 v[140:143], a[184:187], v[168:171], v[140:143]\n" \
"buffer_load_dwordx4 v248, s[32:35], s88 offen offset:2048 sc1 lds\n" \
"v_mfma_f32_16x16x32_bf16 v[140:143], a[180:183], v[164:167], v[140:143]\n" \
"v_exp_f32 v128, v128\n" \
"v_mfma_f32_16x16x32_bf16 v[140:143], a[176:179], v[160:163], v[140:143]\n" \
"buffer_load_dwordx4 v248, s[32:35], s88 offen offset:3072 sc1 lds\n" \
"v_mfma_f32_16x16x32_bf16 a[44:47], a[48:51], v[144:147], a[44:47]\n" \
"s_add_u32 s89, s88, 589824\n" \
"v_exp_f32 v129, v129\n" \
"v_mfma_f32_16x16x32_bf16 a[44:47], a[48:51], v[152:155], a[44:47]\n" \
"s_add_u32 s88, s88, s27\n" \
"s_add_u32 s19, s19, 0x4000\n" \
"v_cvt_pk_bf16_f32 v128, v128, v129\n" \
"v_mfma_f32_16x16x32_bf16 v[96:99], a[224:227], v[152:155], v[96:99]\n" \
"s_bitset0_b32 s19, 16\n" \
"v_exp_f32 v130, v130\n" \
"v_mfma_f32_16x16x32_bf16 v[96:99], a[192:195], v[144:147], v[96:99]\n" \
"s_cmp_eq_u32 s88, s16\n" \
"s_cmov_b32 s88, s21\n" \
"ds_read_b128 a[224:227], v242 offset:8192\n" \
"v_mfma_f32_16x16x32_bf16 v[100:103], a[196:199], v[144:147], v[100:103]\n" \
"s_cmov_b32 s27, s91\n" \
"v_exp_f32 v131, v131\n" \
"v_mfma_f32_16x16x32_bf16 v[100:103], a[228:231], v[152:155], v[100:103]\n" \
"s_cmov_b32 s16, s77\n" \
"ds_read_b128 a[192:195], v242 offset:0\n" \
"v_cvt_pk_bf16_f32 v129, v130, v131\n" \
"v_mfma_f32_16x16x32_bf16 v[104:107], a[232:235], v[152:155], v[104:107]\n" \
"s_cmov_b32 s21, s92\n" \
"v_exp_f32 v132, v132\n" \
"v_mfma_f32_16x16x32_bf16 v[104:107], a[200:203], v[144:147], v[104:107]\n" \
"s_cmovk_i32 s91, 0x4000\n" \
"s_add_u32 s38, s38, 4096\n" \
"ds_read_b128 a[196:199], v242 offset:1024\n" \
"ds_read_b128 a[228:231], v242 offset:9216\n" \
"buffer_load_dwordx4 a[96:99], v248, s[32:35], s38 offen offset:0 sc1\n" \
"v_mfma_f32_16x16x32_bf16 v[108:111], a[204:207], v[144:147], v[108:111]\n" \
"s_cmp_eq_u32 s88, s17\n" \
"v_exp_f32 v133, v133\n" \
"v_mfma_f32_16x16x32_bf16 v[108:111], a[236:239], v[152:155], v[108:111]\n" \
"s_cmov_b32 s88, s18\n" \
"ds_read_b128 a[232:235], v242 offset:10240\n" \
"v_cvt_pk_bf16_f32 v130, v132, v133\n" \
"v_mfma_f32_16x16x32_bf16 v[112:115], a[240:243], v[152:155], v[112:115]\n" \
"v_exp_f32 v134, v134\n" \
"v_mfma_f32_16x16x32_bf16 v[112:115], a[208:211], v[144:147], v[112:115]\n" \
"ds_read_b128 a[200:203], v242 offset:2048\n" \
"v_mfma_f32_16x16x32_bf16 v[116:119], a[212:215], v[144:147], v[116:119]\n" \
"v_exp_f32 v135, v135\n" \
"v_mfma_f32_16x16x32_bf16 v[116:119], a[244:247], v[152:155], v[116:119]\n" \
"ds_read_b128 a[204:207], v242 offset:3072\n" \
"v_cvt_pk_bf16_f32 v131, v134, v135\n" \
"v_mfma_f32_16x16x32_bf16 v[120:123], a[248:251], v[152:155], v[120:123]\n" \
"ds_read_b128 a[236:239], v242 offset:11264\n" \
"v_mfma_f32_16x16x32_bf16 v[120:123], a[216:219], v[144:147], v[120:123]\n" \
"v_exp_f32 v136, v136\n" \
"v_mfma_f32_16x16x32_bf16 v[124:127], a[220:223], v[144:147], v[124:127]\n" \
"ds_read_b128 a[240:243], v242 offset:12288\n" \
"v_mfma_f32_16x16x32_bf16 v[124:127], a[252:255], v[152:155], v[124:127]\n" \
"s_waitcnt vmcnt(13)\n" \
"v_exp_f32 v137, v137\n" \
"v_mfma_f32_16x16x32_bf16 v[144:147], a[128:131], v[176:179], 0\n" \
"s_barrier\n" \
"ds_read_b128 a[208:211], v242 offset:4096\n" \
"v_cvt_pk_bf16_f32 v136, v136, v137\n" \
"v_mfma_f32_16x16x32_bf16 v[144:147], a[132:135], v[180:183], v[144:147]\n" \
"s_mov_b32 m0, s20\n" \
"buffer_load_dwordx4 a[104:107], v248, s[32:35], s38 offen offset:1024 sc1\n" \
"v_exp_f32 v138, v138\n" \
"v_mfma_f32_16x16x32_bf16 v[144:147], a[136:139], v[184:187], v[144:147]\n" \
"buffer_load_dwordx4 v248, s[32:35], s89 offen offset:0 sc1 lds\n" \
"v_mfma_f32_16x16x32_bf16 v[144:147], a[140:143], v[188:191], v[144:147]\n" \
"v_exp_f32 v139, v139\n" \
"v_mfma_f32_16x16x32_bf16 v[148:151], a[156:159], v[188:191], 0\n" \
"ds_read_b128 a[212:215], v242 offset:5120\n" \
"v_cvt_pk_bf16_f32 v137, v138, v139\n" \
"v_mfma_f32_16x16x32_bf16 v[148:151], a[152:155], v[184:187], v[148:151]\n" \
"v_exp_f32 v140, v140\n" \
"v_mfma_f32_16x16x32_bf16 v[148:151], a[148:151], v[180:183], v[148:151]\n" \
"ds_read_b128 a[244:247], v242 offset:13312\n" \
"ds_read_b128 a[248:251], v242 offset:14336\n" \
"v_mfma_f32_16x16x32_bf16 v[148:151], a[144:147], v[176:179], v[148:151]\n" \
"v_exp_f32 v141, v141\n" \
"v_mfma_f32_16x16x32_bf16 v[152:155], a[160:163], v[176:179], 0\n" \
"buffer_load_dwordx4 v248, s[32:35], s89 offen offset:1024 sc1 lds\n" \
"v_mfma_f32_16x16x32_bf16 v[152:155], a[164:167], v[180:183], v[152:155]\n" \
"v_exp_f32 v142, v142\n" \
"v_mfma_f32_16x16x32_bf16 v[152:155], a[168:171], v[184:187], v[152:155]\n" \
"ds_read_b128 a[216:219], v242 offset:6144\n" \
"v_cvt_pk_bf16_f32 v138, v140, v141\n" \
"v_mfma_f32_16x16x32_bf16 v[152:155], a[172:175], v[188:191], v[152:155]\n" \
"v_exp_f32 v143, v143\n" \
"v_mfma_f32_16x16x32_bf16 v[156:159], a[188:191], v[188:191], 0\n" \
"ds_read_b128 a[128:131], v240 offset:0\n" \
"v_cvt_pk_bf16_f32 v139, v142, v143\n" \
"v_mfma_f32_16x16x32_bf16 v[156:159], a[184:187], v[184:187], v[156:159]\n" \
".Lst1_5_%=:\n" \
"s_cbranch_execz .Lst1_9_%=\n" \
"ds_read_b128 a[132:135], v240 offset:1024\n" \
"ds_read_b128 a[220:223], v242 offset:7168\n" \
"v_mfma_f32_16x16x32_bf16 v[156:159], a[180:183], v[180:183], v[156:159]\n" \
"v_exp_f32 v144, v144\n" \
"v_mfma_f32_16x16x32_bf16 v[156:159], a[176:179], v[176:179], v[156:159]\n" \
"buffer_load_dwordx4 v248, s[32:35], s89 offen offset:2048 sc1 lds\n" \
"buffer_load_dwordx4 a[112:115], v248, s[32:35], s38 offen offset:2048 sc1\n" \
"v_mfma_f32_16x16x32_bf16 a[40:43], a[48:51], v[128:131], a[40:43]\n" \
"v_exp_f32 v145, v145\n" \
"v_mfma_f32_16x16x32_bf16 a[40:43], a[48:51], v[136:139], a[40:43]\n" \
"s_waitcnt lgkmcnt(13)\n" \
"ds_read_b128 a[136:139], v240 offset:2048\n" \
"v_cvt_pk_bf16_f32 v144, v144, v145\n" \
"v_mfma_f32_16x16x32_bf16 v[64:67], a[224:227], v[136:139], v[64:67]\n" \
"v_exp_f32 v146, v146\n" \
"v_mfma_f32_16x16x32_bf16 v[64:67], a[192:195], v[128:131], v[64:67]\n" \
"ds_read_b128 a[252:255], v242 offset:15360\n" \
"ds_read_b128 a[140:143], v240 offset:3072\n" \
"v_mfma_f32_16x16x32_bf16 v[68:71], a[196:199], v[128:131], v[68:71]\n" \
"v_exp_f32 v147, v147\n" \
"v_mfma_f32_16x16x32_bf16 v[68:71], a[228:231], v[136:139], v[68:71]\n" \
"s_waitcnt lgkmcnt(12)\n" \
"buffer_load_dwordx4 v248, s[32:35], s89 offen offset:3072 sc1 lds\n" \
"v_mfma_f32_16x16x32_bf16 v[72:75], a[232:235], v[136:139], v[72:75]\n" \
"s_add_u32 s20, s20, 0x4000\n" \
"v_exp_f32 v148, v148\n" \
"v_mfma_f32_16x16x32_bf16 v[72:75], a[200:203], v[128:131], v[72:75]\n" \
"s_and_b32 s20, s20, 0xffff\n" \
"ds_read_b128 a[156:159], v240 offset:7168\n" \
"v_cvt_pk_bf16_f32 v145, v146, v147\n" \
"v_mfma_f32_16x16x32_bf16 v[76:79], a[204:207], v[128:131], v[76:79]\n" \
"s_bitset1_b32 s20, 16\n" \
"v_exp_f32 v149, v149\n" \
"v_mfma_f32_16x16x32_bf16 v[76:79], a[236:239], v[136:139], v[76:79]\n" \
"s_waitcnt lgkmcnt(9)\n" \
"ds_read_b128 a[152:155], v240 offset:6144\n" \
"v_cvt_pk_bf16_f32 v146, v148, v149\n" \
"v_mfma_f32_16x16x32_bf16 v[80:83], a[240:243], v[136:139], v[80:83]\n" \
"v_exp_f32 v150, v150\n" \
"v_mfma_f32_16x16x32_bf16 v[80:83], a[208:211], v[128:131], v[80:83]\n" \
"ds_read_b128 a[148:151], v240 offset:5120\n" \
"ds_read_b128 a[144:147], v240 offset:4096\n" \
"v_mfma_f32_16x16x32_bf16 v[84:87], a[212:215], v[128:131], v[84:87]\n" \
"v_exp_f32 v151, v151\n" \
"v_mfma_f32_16x16x32_bf16 v[84:87], a[244:247], v[136:139], v[84:87]\n" \
"s_waitcnt lgkmcnt(5)\n" \
"ds_read_b128 a[160:163], v240 offset:8192\n" \
"buffer_load_dwordx4 a[116:119], v248, s[32:35], s38 offen offset:3072 sc1\n" \
"v_cvt_pk_bf16_f32 v147, v150, v151\n" \
"v_mfma_f32_16x16x32_bf16 v[88:91], a[248:251], v[136:139], v[88:91]\n" \
"ds_read_b128 a[164:167], v240 offset:9216\n" \
"ds_read_b128 a[168:171], v240 offset:10240\n" \
"v_mfma_f32_16x16x32_bf16 v[88:91], a[216:219], v[128:131], v[88:91]\n" \
"v_exp_f32 v152, v152\n" \
"v_mfma_f32_16x16x32_bf16 v[92:95], a[220:223], v[128:131], v[92:95]\n" \
"ds_read_b128 a[172:175], v240 offset:11264\n" \
"ds_read_b128 a[188:191], v240 offset:15360\n" \
"v_mfma_f32_16x16x32_bf16 v[92:95], a[252:255], v[136:139], v[92:95]\n" \
"s_waitcnt lgkmcnt(9)\n" \
"v_exp_f32 v153, v153\n" \
"v_mfma_f32_16x16x32_bf16 v[128:131], a[128:131], v[160:163], 0\n" \
"ds_read_b128 a[184:187], v240 offset:14336\n" \
"v_cvt_pk_bf16_f32 v152, v152, v153\n" \
"v_mfma_f32_16x16x32_bf16 v[128:131], a[132:135], v[164:167], v[128:131]\n" \
"v_exp_f32 v154, v154\n" \
"v_mfma_f32_16x16x32_bf16 v[128:131], a[136:139], v[168:171], v[128:131]\n" \
"s_add_u32 s38, s66, 3\n" \
"ds_read_b128 a[180:183], v240 offset:13312\n" \
"v_add_u32 v242, 0x10000, v240\n" \
"v_mfma_f32_16x16x32_bf16 v[128:131], a[140:143], v[172:175], v[128:131]\n" \
"s_waitcnt lgkmcnt(7)\n" \
"v_exp_f32 v155, v155\n" \
"v_mfma_f32_16x16x32_bf16 v[132:135], a[156:159], v[172:175], 0\n" \
"s_mov_b32 m0, s19\n" \
"ds_read_b128 a[176:179], v240 offset:12288\n" \
"v_cvt_pk_bf16_f32 v153, v154, v155\n" \
"v_mfma_f32_16x16x32_bf16 v[132:135], a[152:155], v[168:171], v[132:135]\n" \
"s_and_b32 s38, s38, 3\n" \
"v_exp_f32 v156, v156\n" \
"v_mfma_f32_16x16x32_bf16 v[132:135], a[148:151], v[164:167], v[132:135]\n" \
"s_lshl_b32 s38, s38, 13\n" \
"buffer_load_dwordx4 v248, s[32:35], s88 offen offset:0 sc1 lds\n" \
"v_mfma_f32_16x16x32_bf16 v[132:135], a[144:147], v[160:163], v[132:135]\n" \
"s_waitcnt lgkmcnt(4)\n" \
"v_exp_f32 v157, v157\n" \
"v_mfma_f32_16x16x32_bf16 v[136:139], a[160:163], v[160:163], 0\n" \
"s_add_u32 s38, s38, s29\n" \
"buffer_load_dwordx4 a[120:123], v248, s[32:35], s38 offen offset:0 sc1\n" \
"v_cvt_pk_bf16_f32 v154, v156, v157\n" \
"v_mfma_f32_16x16x32_bf16 v[136:139], a[164:167], v[164:167], v[136:139]\n" \
"v_exp_f32 v158, v158\n" \
"v_mfma_f32_16x16x32_bf16 v[136:139], a[168:171], v[168:171], v[136:139]\n" \
"buffer_load_dwordx4 v248, s[32:35], s88 offen offset:1024 sc1 lds\n" \
"v_mfma_f32_16x16x32_bf16 v[136:139], a[172:175], v[172:175], v[136:139]\n" \
"s_waitcnt lgkmcnt(0)\n" \
"v_exp_f32 v159, v159\n" \
"v_mfma_f32_16x16x32_bf16 v[140:143], a[188:191], v[172:175], 0\n" \
"v_cvt_pk_bf16_f32 v155, v158, v159\n" \
"v_mfma_f32_16x16x32_bf16 v[140:143], a[184:187], v[168:171], v[140:143]\n" \
"buffer_load_dwordx4 v248, s[32:35], s88 offen offset:2048 sc1 lds\n" \
"v_mfma_f32_16x16x32_bf16 v[140:143], a[180:183], v[164:167], v[140:143]\n" \
"v_exp_f32 v128, v128\n" \
"v_mfma_f32_16x16x32_bf16 v[140:143], a[176:179], v[160:163], v[140:143]\n" \
"buffer_load_dwordx4 v248, s[32:35], s88 offen offset:3072 sc1 lds\n" \
"v_mfma_f32_16x16x32_bf16 a[44:47], a[48:51], v[144:147], a[44:47]\n" \
"s_add_u32 s89, s88, 589824\n" \
"v_exp_f32 v129, v129\n" \
"v_mfma_f32_16x16x32_bf16 a[44:47], a[48:51], v[152:155], a[44:47]\n" \
"v_cvt_pk_bf16_f32 v128, v128, v129\n" \
"v_lshlrev_b32 v232, 3, v251\n" \
"v_mfma_f32_16x16x32_bf16 v[96:99], a[224:227], v[152:155], v[96:99]\n" \
"s_lshl_b32 s70, %[wid], 9\n" \
"v_exp_f32 v130, v130\n" \
"v_mfma_f32_16x16x32_bf16 v[96:99], a[192:195], v[144:147], v[96:99]\n" \
"s_add_u32 s70, s70, 659456\n" \
"ds_read_b128 a[224:227], v242 offset:8192\n" \
"v_mov_b32 v234, 1\n" \
"v_mfma_f32_16x16x32_bf16 v[100:103], a[196:199], v[144:147], v[100:103]\n" \
"v_exp_f32 v131, v131\n" \
"v_mfma_f32_16x16x32_bf16 v[100:103], a[228:231], v[152:155], v[100:103]\n" \
"ds_read_b128 a[192:195], v242 offset:0\n" \
"v_cvt_pk_bf16_f32 v129, v130, v131\n" \
"v_mfma_f32_16x16x32_bf16 v[104:107], a[232:235], v[152:155], v[104:107]\n" \
"v_exp_f32 v132, v132\n" \
"v_mfma_f32_16x16x32_bf16 v[104:107], a[200:203], v[144:147], v[104:107]\n" \
"ds_read_b128 a[196:199], v242 offset:1024\n" \
"ds_read_b128 a[228:231], v242 offset:9216\n" \
"buffer_load_dwordx4 a[124:127], v248, s[32:35], s38 offen offset:1024 sc1\n" \
"v_mfma_f32_16x16x32_bf16 v[108:111], a[204:207], v[144:147], v[108:111]\n" \
"v_exp_f32 v133, v133\n" \
"v_mfma_f32_16x16x32_bf16 v[108:111], a[236:239], v[152:155], v[108:111]\n" \
"ds_read_b128 a[232:235], v242 offset:10240\n" \
"v_cvt_pk_bf16_f32 v130, v132, v133\n" \
"v_mfma_f32_16x16x32_bf16 v[112:115], a[240:243], v[152:155], v[112:115]\n" \
"v_exp_f32 v134, v134\n" \
"v_mfma_f32_16x16x32_bf16 v[112:115], a[208:211], v[144:147], v[112:115]\n" \
"ds_read_b128 a[200:203], v242 offset:2048\n" \
"v_mov_b32 v235, 1\n" \
"v_mfma_f32_16x16x32_bf16 v[116:119], a[212:215], v[144:147], v[116:119]\n" \
"v_exp_f32 v135, v135\n" \
"v_mfma_f32_16x16x32_bf16 v[116:119], a[244:247], v[152:155], v[116:119]\n" \
"ds_read_b128 a[204:207], v242 offset:3072\n" \
"v_cvt_pk_bf16_f32 v131, v134, v135\n" \
"v_mfma_f32_16x16x32_bf16 v[120:123], a[248:251], v[152:155], v[120:123]\n" \
"ds_read_b128 a[236:239], v242 offset:11264\n" \
"v_mfma_f32_16x16x32_bf16 v[120:123], a[216:219], v[144:147], v[120:123]\n" \
"v_exp_f32 v136, v136\n" \
"v_mfma_f32_16x16x32_bf16 v[124:127], a[220:223], v[144:147], v[124:127]\n" \
"ds_read_b128 a[240:243], v242 offset:12288\n" \
"v_mfma_f32_16x16x32_bf16 v[124:127], a[252:255], v[152:155], v[124:127]\n" \
"s_waitcnt vmcnt(7)\n" \
"v_exp_f32 v137, v137\n" \
"v_mfma_f32_16x16x32_bf16 v[144:147], a[128:131], v[176:179], 0\n" \
"s_barrier\n" \
"ds_read_b128 a[208:211], v242 offset:4096\n" \
"v_cvt_pk_bf16_f32 v136, v136, v137\n" \
"v_mfma_f32_16x16x32_bf16 v[144:147], a[132:135], v[180:183], v[144:147]\n" \
"s_mov_b32 m0, s20\n" \
"v_exp_f32 v138, v138\n" \
"v_mfma_f32_16x16x32_bf16 v[144:147], a[136:139], v[184:187], v[144:147]\n" \
"buffer_load_dwordx4 v248, s[32:35], s89 offen offset:0 sc1 lds\n" \
"v_mfma_f32_16x16x32_bf16 v[144:147], a[140:143], v[188:191], v[144:147]\n" \
"v_exp_f32 v139, v139\n" \
"v_mfma_f32_16x16x32_bf16 v[148:151], a[156:159], v[188:191], 0\n" \
"ds_read_b128 a[212:215], v242 offset:5120\n" \
"v_cvt_pk_bf16_f32 v137, v138, v139\n" \
"v_mfma_f32_16x16x32_bf16 v[148:151], a[152:155], v[184:187], v[148:151]\n" \
"v_exp_f32 v140, v140\n" \
"v_mfma_f32_16x16x32_bf16 v[148:151], a[148:151], v[180:183], v[148:151]\n" \
"ds_read_b128 a[244:247], v242 offset:13312\n" \
"ds_read_b128 a[248:251], v242 offset:14336\n" \
"v_mfma_f32_16x16x32_bf16 v[148:151], a[144:147], v[176:179], v[148:151]\n" \
"v_exp_f32 v141, v141\n" \
"v_mfma_f32_16x16x32_bf16 v[152:155], a[160:163], v[176:179], 0\n" \
"buffer_load_dwordx4 v248, s[32:35], s89 offen offset:1024 sc1 lds\n" \
"v_mfma_f32_16x16x32_bf16 v[152:155], a[164:167], v[180:183], v[152:155]\n" \
"v_exp_f32 v142, v142\n" \
"v_mfma_f32_16x16x32_bf16 v[152:155], a[168:171], v[184:187], v[152:155]\n" \
"ds_read_b128 a[216:219], v242 offset:6144\n" \
"v_cvt_pk_bf16_f32 v138, v140, v141\n" \
"v_mfma_f32_16x16x32_bf16 v[152:155], a[172:175], v[188:191], v[152:155]\n" \
"v_exp_f32 v143, v143\n" \
"v_mfma_f32_16x16x32_bf16 v[156:159], a[188:191], v[188:191], 0\n" \
"v_exp_f32 v144, v144\n" \
"v_mfma_f32_16x16x32_bf16 v[156:159], a[184:187], v[184:187], v[156:159]\n" \
"v_exp_f32 v145, v145\n" \
"v_mfma_f32_16x16x32_bf16 v[156:159], a[180:183], v[180:183], v[156:159]\n" \
"v_exp_f32 v146, v146\n" \
"v_mfma_f32_16x16x32_bf16 v[156:159], a[176:179], v[176:179], v[156:159]\n" \
"v_exp_f32 v147, v147\n" \
"v_cvt_pk_bf16_f32 v139, v142, v143\n" \
"v_mfma_f32_16x16x32_bf16 a[40:43], a[48:51], v[128:131], a[40:43]\n" \
"v_exp_f32 v148, v148\n" \
"v_cvt_pk_bf16_f32 v144, v144, v145\n" \
"v_mfma_f32_16x16x32_bf16 a[40:43], a[48:51], v[136:139], a[40:43]\n" \
"s_waitcnt lgkmcnt(10)\n" \
"v_exp_f32 v149, v149\n" \
"v_cvt_pk_bf16_f32 v145, v146, v147\n" \
"v_mfma_f32_16x16x32_bf16 v[64:67], a[224:227], v[136:139], v[64:67]\n" \
"v_exp_f32 v150, v150\n" \
"v_cvt_pk_bf16_f32 v146, v148, v149\n" \
"v_mfma_f32_16x16x32_bf16 v[64:67], a[192:195], v[128:131], v[64:67]\n" \
"ds_read_b128 a[220:223], v242 offset:7168\n" \
"v_exp_f32 v151, v151\n" \
"v_mfma_f32_16x16x32_bf16 v[68:71], a[196:199], v[128:131], v[68:71]\n" \
"buffer_load_dwordx4 a[128:131], v248, s[32:35], s38 offen offset:2048 sc1\n" \
"v_exp_f32 v152, v152\n" \
"v_cvt_pk_bf16_f32 v147, v150, v151\n" \
"v_mfma_f32_16x16x32_bf16 v[68:71], a[228:231], v[136:139], v[68:71]\n" \
"s_waitcnt lgkmcnt(7)\n" \
"ds_read_b128 a[252:255], v242 offset:15360\n" \
"v_exp_f32 v153, v153\n" \
"v_mfma_f32_16x16x32_bf16 v[72:75], a[232:235], v[136:139], v[72:75]\n" \
"v_exp_f32 v154, v154\n" \
"v_cvt_pk_bf16_f32 v152, v152, v153\n" \
"v_mfma_f32_16x16x32_bf16 v[72:75], a[200:203], v[128:131], v[72:75]\n" \
"buffer_load_dwordx4 a[132:135], v248, s[32:35], s38 offen offset:3072 sc1\n" \
"v_exp_f32 v155, v155\n" \
"v_mfma_f32_16x16x32_bf16 v[76:79], a[204:207], v[128:131], v[76:79]\n" \
"v_exp_f32 v156, v156\n" \
"v_cvt_pk_bf16_f32 v153, v154, v155\n" \
"v_mfma_f32_16x16x32_bf16 v[76:79], a[236:239], v[136:139], v[76:79]\n" \
"s_waitcnt lgkmcnt(4)\n" \
"buffer_load_dwordx4 v248, s[32:35], s89 offen offset:2048 sc1 lds\n" \
"v_accvgpr_read_b32 v224, a40\n" \
"v_mfma_f32_16x16x32_bf16 v[80:83], a[240:243], v[136:139], v[80:83]\n" \
"buffer_load_dwordx2 v[230:231], v232, s[32:35], s70 offen sc1\n" \
"v_exp_f32 v157, v157\n" \
"v_mfma_f32_16x16x32_bf16 v[80:83], a[208:211], v[128:131], v[80:83]\n" \
"v_exp_f32 v158, v158\n" \
"v_cvt_pk_bf16_f32 v154, v156, v157\n" \
"v_mfma_f32_16x16x32_bf16 v[84:87], a[212:215], v[128:131], v[84:87]\n" \
"s_add_u32 s38, s38, 4096\n" \
"buffer_load_dwordx4 v248, s[32:35], s89 offen offset:3072 sc1 lds\n" \
"v_mfma_f32_16x16x32_bf16 v[84:87], a[244:247], v[136:139], v[84:87]\n" \
"s_waitcnt lgkmcnt(0)\n" \
"buffer_load_dwordx4 a[136:139], v248, s[32:35], s38 offen offset:0 sc1\n" \
"v_exp_f32 v159, v159\n" \
"v_mfma_f32_16x16x32_bf16 v[88:91], a[248:251], v[136:139], v[88:91]\n" \
"v_cvt_pk_bf16_f32 v155, v158, v159\n" \
"v_add_u32 v240, s83, v248\n" \
"v_add_u32 v241, 0x10000, v240\n" \
"v_mfma_f32_16x16x32_bf16 v[88:91], a[216:219], v[128:131], v[88:91]\n" \
"v_xor_b32 v244, 0x8000, v240\n" \
"v_xor_b32 v249, 0x8000, v241\n" \
"v_add_u32 v239, 0x20000, v248\n" \
"v_mfma_f32_16x16x32_bf16 v[92:95], a[220:223], v[128:131], v[92:95]\n" \
"s_lshl_b32 s39, s66, 13\n" \
"v_rcp_f32 v224, v224\n" \
"s_nop 0\n" \
"v_mul_f32 v64, v64, v224\n" \
"v_mfma_f32_16x16x32_bf16 v[92:95], a[252:255], v[136:139], v[92:95]\n" \
"s_add_u32 s39, s39, s37\n" \
"v_mul_f32 v65, v65, v224\n" \
"v_mul_f32 v66, v66, v224\n" \
"v_mul_f32 v67, v67, v224\n" \
"v_mfma_f32_16x16x32_bf16 a[44:47], a[48:51], v[144:147], a[44:47]\n" \
"s_add_u32 s19, s39, 4096\n" \
"v_mul_f32 v68, v68, v224\n" \
"v_mul_f32 v69, v69, v224\n" \
"v_mul_f32 v70, v70, v224\n" \
"v_mfma_f32_16x16x32_bf16 a[44:47], a[48:51], v[152:155], a[44:47]\n" \
"buffer_load_dwordx4 a[140:143], v248, s[32:35], s38 offen offset:1024 sc1\n" \
"v_mul_f32 v71, v71, v224\n" \
"v_cvt_pk_bf16_f32 v192, v64, v65\n" \
"v_mfma_f32_16x16x32_bf16 v[96:99], a[224:227], v[152:155], v[96:99]\n" \
"v_cvt_pk_bf16_f32 v193, v66, v67\n" \
"v_cvt_pk_bf16_f32 v194, v68, v69\n" \
"v_cvt_pk_bf16_f32 v195, v70, v71\n" \
"v_mfma_f32_16x16x32_bf16 v[96:99], a[192:195], v[144:147], v[96:99]\n" \
"buffer_store_dwordx4 v[192:195], v248, s[32:35], s39 offen offset:0 sc1\n" \
"v_mul_f32 v72, v72, v224\n" \
"v_mfma_f32_16x16x32_bf16 v[100:103], a[196:199], v[144:147], v[100:103]\n" \
"ds_read_b128 a[0:3], v240 offset:0\n" \
"v_mul_f32 v73, v73, v224\n" \
"v_mul_f32 v74, v74, v224\n" \
"v_mfma_f32_16x16x32_bf16 v[100:103], a[228:231], v[152:155], v[100:103]\n" \
"v_mul_f32 v75, v75, v224\n" \
"v_mul_f32 v76, v76, v224\n" \
"v_mul_f32 v77, v77, v224\n" \
"v_mfma_f32_16x16x32_bf16 v[104:107], a[232:235], v[152:155], v[104:107]\n" \
"v_accvgpr_read_b32 v228, a40\n" \
"v_accvgpr_read_b32 v229, a44\n" \
"v_min_f32 v253, v228, v229\n" \
"v_mfma_f32_16x16x32_bf16 v[104:107], a[200:203], v[144:147], v[104:107]\n" \
"v_max_f32 v252, v228, v229\n" \
"v_cmp_nle_f32 s[98:99], v246, v253\n" \
"v_cmp_nle_f32 vcc, v252, v250\n" \
"v_mfma_f32_16x16x32_bf16 v[108:111], a[204:207], v[144:147], v[108:111]\n" \
"ds_read_b128 a[4:7], v240 offset:8192\n" \
"ds_read_b128 a[8:11], v240 offset:1024\n" \
"v_cmp_u_f32 s[14:15], v228, v229\n" \
"v_mfma_f32_16x16x32_bf16 v[108:111], a[236:239], v[152:155], v[108:111]\n" \
"buffer_load_dwordx4 a[144:147], v248, s[32:35], s38 offen offset:2048 sc1\n" \
"v_mul_f32 v78, v78, v224\n" \
"v_mul_f32 v79, v79, v224\n" \
"v_mfma_f32_16x16x32_bf16 v[112:115], a[240:243], v[152:155], v[112:115]\n" \
"s_or_b64 vcc, vcc, s[98:99]\n" \
"ds_read_b128 a[12:15], v240 offset:9216\n" \
"v_cvt_pk_bf16_f32 v200, v72, v73\n" \
"v_cvt_pk_bf16_f32 v201, v74, v75\n" \
"v_mfma_f32_16x16x32_bf16 v[112:115], a[208:211], v[144:147], v[112:115]\n" \
"s_or_b64 vcc, vcc, s[14:15]\n" \
"v_cvt_pk_bf16_f32 v202, v76, v77\n" \
"v_cvt_pk_bf16_f32 v203, v78, v79\n" \
"v_mul_f32 v80, v80, v224\n" \
"v_mfma_f32_16x16x32_bf16 v[116:119], a[212:215], v[144:147], v[116:119]\n" \
"ds_read_b128 a[16:19], v240 offset:2048\n" \
"v_mul_f32 v81, v81, v224\n" \
"v_mul_f32 v82, v82, v224\n" \
"v_mfma_f32_16x16x32_bf16 v[116:119], a[244:247], v[152:155], v[116:119]\n" \
"buffer_store_dwordx4 v[200:203], v248, s[32:35], s39 offen offset:2048 sc1\n" \
"v_mul_f32 v83, v83, v224\n" \
"v_mfma_f32_16x16x32_bf16 v[120:123], a[248:251], v[152:155], v[120:123]\n" \
"ds_read_b128 a[20:23], v240 offset:10240\n" \
"v_mul_f32 v84, v84, v224\n" \
"v_mul_f32 v85, v85, v224\n" \
"v_mfma_f32_16x16x32_bf16 v[120:123], a[216:219], v[144:147], v[120:123]\n" \
"buffer_load_dwordx4 a[148:151], v248, s[32:35], s38 offen offset:3072 sc1\n" \
"v_mul_f32 v86, v86, v224\n" \
"v_mul_f32 v87, v87, v224\n" \
"v_mfma_f32_16x16x32_bf16 v[124:127], a[220:223], v[144:147], v[124:127]\n" \
"v_cvt_pk_bf16_f32 v208, v80, v81\n" \
"v_cvt_pk_bf16_f32 v209, v82, v83\n" \
"v_cvt_pk_bf16_f32 v210, v84, v85\n" \
"v_mfma_f32_16x16x32_bf16 v[124:127], a[252:255], v[152:155], v[124:127]\n" \
"v_cvt_pk_bf16_f32 v211, v86, v87\n" \
"v_mul_f32 v88, v88, v224\n" \
"v_mul_f32 v89, v89, v224\n" \
"s_cbranch_vccnz .Lrh_%=\n" \
".Lrhb_%=:\n" \
  \
"s_waitcnt lgkmcnt(5)\n" \
"v_mfma_f32_16x16x32_bf16 v[128:131], a[0:3], v[0:3], 0\n" \
"ds_read_b128 a[24:27], v240 offset:3072\n" \
"v_mul_f32 v90, v90, v224\n" \
"v_mul_f32 v91, v91, v224\n" \
"v_mul_f32 v92, v92, v224\n" \
"s_waitcnt lgkmcnt(5)\n" \
"v_mfma_f32_16x16x32_bf16 v[128:131], a[4:7], v[8:11], v[128:131]\n" \
"buffer_store_dwordx4 v[208:211], v248, s[32:35], s19 offen offset:0 sc1\n" \
"v_mul_f32 v93, v93, v224\n" \
"v_mul_f32 v94, v94, v224\n" \
"v_mfma_f32_16x16x32_bf16 v[136:139], a[4:7], v[12:15], 0\n" \
"ds_read_b128 a[28:31], v240 offset:11264\n" \
"v_mul_f32 v95, v95, v224\n" \
"v_cvt_pk_bf16_f32 v216, v88, v89\n" \
"v_cvt_pk_bf16_f32 v217, v90, v91\n" \
"v_mfma_f32_16x16x32_bf16 v[136:139], a[0:3], v[4:7], v[136:139]\n" \
"s_waitcnt lgkmcnt(4)\n" \
"v_cvt_pk_bf16_f32 v218, v92, v93\n" \
"v_cvt_pk_bf16_f32 v219, v94, v95\n" \
"v_accvgpr_read_b32 v225, a44\n" \
"v_mfma_f32_16x16x32_bf16 v[132:135], a[8:11], v[0:3], 0\n" \
"ds_read_b128 a[0:3], v240 offset:4096\n" \
"v_rcp_f32 v225, v225\n" \
"s_nop 0\n" \
"v_mul_f32 v96, v96, v225\n" \
"v_mfma_f32_16x16x32_bf16 v[132:135], a[12:15], v[8:11], v[132:135]\n" \
"buffer_store_dwordx4 v[216:219], v248, s[32:35], s19 offen offset:2048 sc1\n" \
"v_mul_f32 v97, v97, v225\n" \
"v_mul_f32 v98, v98, v225\n" \
"v_mfma_f32_16x16x32_bf16 v[140:143], a[12:15], v[12:15], 0\n" \
"ds_read_b128 a[4:7], v240 offset:12288\n" \
"v_mul_f32 v99, v99, v225\n" \
"v_mul_f32 v100, v100, v225\n" \
"v_mul_f32 v101, v101, v225\n" \
"v_mfma_f32_16x16x32_bf16 v[140:143], a[8:11], v[4:7], v[140:143]\n" \
"s_waitcnt lgkmcnt(4)\n" \
"v_mul_f32 v102, v102, v225\n" \
"v_mul_f32 v103, v103, v225\n" \
"v_cvt_pk_bf16_f32 v196, v96, v97\n" \
"v_cvt_pk_bf16_f32 v197, v98, v99\n" \
"v_mfma_f32_16x16x32_bf16 v[144:147], a[16:19], v[0:3], 0\n" \
"ds_read_b128 a[8:11], v240 offset:5120\n" \
"v_cvt_pk_bf16_f32 v198, v100, v101\n" \
"v_cvt_pk_bf16_f32 v199, v102, v103\n" \
"v_mul_f32 v104, v104, v225\n" \
"v_mfma_f32_16x16x32_bf16 v[144:147], a[20:23], v[8:11], v[144:147]\n" \
"buffer_store_dwordx4 v[196:199], v248, s[32:35], s39 offen offset:1024 sc1\n" \
"v_mul_f32 v105, v105, v225\n" \
"v_mul_f32 v106, v106, v225\n" \
"v_mfma_f32_16x16x32_bf16 v[152:155], a[20:23], v[12:15], 0\n" \
"ds_read_b128 a[12:15], v240 offset:13312\n" \
"v_mul_f32 v107, v107, v225\n" \
"v_mul_f32 v108, v108, v225\n" \
"v_mul_f32 v109, v109, v225\n" \
"v_mfma_f32_16x16x32_bf16 v[152:155], a[16:19], v[4:7], v[152:155]\n" \
"s_waitcnt lgkmcnt(4)\n" \
"v_mul_f32 v110, v110, v225\n" \
"v_mul_f32 v111, v111, v225\n" \
"v_cvt_pk_bf16_f32 v204, v104, v105\n" \
"v_cvt_pk_bf16_f32 v205, v106, v107\n" \
"v_mfma_f32_16x16x32_bf16 v[148:151], a[24:27], v[0:3], 0\n" \
"ds_read_b128 a[16:19], v240 offset:6144\n" \
"v_cvt_pk_bf16_f32 v206, v108, v109\n" \
"v_cvt_pk_bf16_f32 v207, v110, v111\n" \
"v_mul_f32 v112, v112, v225\n" \
"v_mfma_f32_16x16x32_bf16 v[148:151], a[28:31], v[8:11], v[148:151]\n" \
"buffer_store_dwordx4 v[204:207], v248, s[32:35], s39 offen offset:3072 sc1\n" \
"v_mul_f32 v113, v113, v225\n" \
"v_mul_f32 v114, v114, v225\n" \
"v_mfma_f32_16x16x32_bf16 v[156:159], a[28:31], v[12:15], 0\n" \
"ds_read_b128 a[20:23], v240 offset:14336\n" \
"v_mul_f32 v115, v115, v225\n" \
"v_mul_f32 v116, v116, v225\n" \
"v_mul_f32 v117, v117, v225\n" \
"v_mfma_f32_16x16x32_bf16 v[156:159], a[24:27], v[4:7], v[156:159]\n" \
"s_waitcnt lgkmcnt(4)\n" \
"v_mul_f32 v118, v118, v225\n" \
"v_mul_f32 v119, v119, v225\n" \
"v_cvt_pk_bf16_f32 v212, v112, v113\n" \
"v_cvt_pk_bf16_f32 v213, v114, v115\n" \
"v_mfma_f32_16x16x32_bf16 v[160:163], a[0:3], v[0:3], 0\n" \
"ds_read_b128 a[24:27], v240 offset:7168\n" \
"v_cvt_pk_bf16_f32 v214, v116, v117\n" \
"v_cvt_pk_bf16_f32 v215, v118, v119\n" \
"v_mul_f32 v120, v120, v225\n" \
"v_mfma_f32_16x16x32_bf16 v[160:163], a[4:7], v[8:11], v[160:163]\n" \
"buffer_store_dwordx4 v[212:215], v248, s[32:35], s19 offen offset:1024 sc1\n" \
"v_mul_f32 v121, v121, v225\n" \
"v_mul_f32 v122, v122, v225\n" \
"v_mfma_f32_16x16x32_bf16 v[168:171], a[4:7], v[12:15], 0\n" \
"ds_read_b128 a[28:31], v240 offset:15360\n" \
"v_mul_f32 v123, v123, v225\n" \
"v_mul_f32 v124, v124, v225\n" \
"v_mul_f32 v125, v125, v225\n" \
"v_mfma_f32_16x16x32_bf16 v[168:171], a[0:3], v[4:7], v[168:171]\n" \
"s_waitcnt lgkmcnt(4)\n" \
"v_mul_f32 v126, v126, v225\n" \
"v_mul_f32 v127, v127, v225\n" \
"v_cvt_pk_bf16_f32 v220, v120, v121\n" \
"v_cvt_pk_bf16_f32 v221, v122, v123\n" \
"v_mfma_f32_16x16x32_bf16 v[164:167], a[8:11], v[0:3], 0\n" \
"ds_read_b128 a[0:3], v240 offset:16384\n" \
"v_cvt_pk_bf16_f32 v222, v124, v125\n" \
"v_cvt_pk_bf16_f32 v223, v126, v127\n" \
"v_mov_b32 v252, s66\n" \
"v_mfma_f32_16x16x32_bf16 v[164:167], a[12:15], v[8:11], v[164:167]\n" \
"buffer_store_dwordx4 v[220:223], v248, s[32:35], s19 offen offset:3072 sc1\n" \
"v_mfma_f32_16x16x32_bf16 v[172:175], a[12:15], v[12:15], 0\n" \
"s_waitcnt vmcnt(13)\n" \
"ds_read_b128 a[4:7], v240 offset:24576\n" \
"v_cmp_ne_u64 vcc, v[230:231], v[234:235]\n" \
"v_mfma_f32_16x16x32_bf16 v[172:175], a[8:11], v[4:7], v[172:175]\n" \
"s_waitcnt lgkmcnt(4)\n" \
"v_lshlrev_b32 v252, 2, v252\n" \
"v_mov_b32 v253, 1\n" \
"v_mfma_f32_16x16x32_bf16 v[176:179], a[16:19], v[0:3], 0\n" \
"ds_read_b128 a[8:11], v240 offset:17408\n" \
"v_and_b32 v254, 15, v251\n" \
"v_mfma_f32_16x16x32_bf16 v[176:179], a[20:23], v[8:11], v[176:179]\n" \
"s_cbranch_vccnz .Lspg_%=\n" \
".Lsbg_%=:\n" \
"v_lshlrev_b32 v254, 6, v254\n" \
"v_mfma_f32_16x16x32_bf16 v[184:187], a[20:23], v[12:15], 0\n" \
"ds_read_b128 a[12:15], v240 offset:25600\n" \
"v_and_or_b32 v250, v251, 48, v254\n" \
"v_mfma_f32_16x16x32_bf16 v[184:187], a[16:19], v[4:7], v[184:187]\n" \
"s_waitcnt lgkmcnt(4)\n" \
"v_mov_b32 v254, v255\n" \
"v_mfma_f32_16x16x32_bf16 v[180:183], a[24:27], v[0:3], 0\n" \
"ds_read_b128 a[16:19], v240 offset:18432\n" \
"v_mfma_f32_16x16x32_bf16 v[180:183], a[28:31], v[8:11], v[180:183]\n" \
"v_mfma_f32_16x16x32_bf16 v[188:191], a[28:31], v[12:15], 0\n" \
"ds_read_b128 a[20:23], v240 offset:26624\n" \
"v_mfma_f32_16x16x32_bf16 v[188:191], a[24:27], v[4:7], v[188:191]\n" \
"s_waitcnt lgkmcnt(4)\n" \
"v_mfma_f32_16x16x32_bf16 v[128:131], a[0:3], v[16:19], v[128:131]\n" \
"ds_read_b128 a[24:27], v240 offset:19456\n" \
"v_mfma_f32_16x16x32_bf16 v[128:131], a[4:7], v[24:27], v[128:131]\n" \
"v_mfma_f32_16x16x32_bf16 v[136:139], a[4:7], v[28:31], v[136:139]\n" \
"ds_read_b128 a[28:31], v240 offset:27648\n" \
"v_mfma_f32_16x16x32_bf16 v[136:139], a[0:3], v[20:23], v[136:139]\n" \
"s_waitcnt lgkmcnt(4)\n" \
"v_mfma_f32_16x16x32_bf16 v[132:135], a[8:11], v[16:19], v[132:135]\n" \
"ds_read_b128 a[0:3], v240 offset:20480\n" \
"v_mfma_f32_16x16x32_bf16 v[132:135], a[12:15], v[24:27], v[132:135]\n" \
"v_mfma_f32_16x16x32_bf16 v[140:143], a[12:15], v[28:31], v[140:143]\n" \
"ds_read_b128 a[4:7], v240 offset:28672\n" \
"v_mfma_f32_16x16x32_bf16 v[140:143], a[8:11], v[20:23], v[140:143]\n" \
"s_waitcnt lgkmcnt(4)\n" \
"v_mfma_f32_16x16x32_bf16 v[144:147], a[16:19], v[16:19], v[144:147]\n" \
"ds_read_b128 a[8:11], v240 offset:21504\n" \
"v_mfma_f32_16x16x32_bf16 v[144:147], a[20:23], v[24:27], v[144:147]\n" \
"v_mfma_f32_16x16x32_bf16 v[152:155], a[20:23], v[28:31], v[152:155]\n" \
"ds_read_b128 a[12:15], v240 offset:29696\n" \
"v_mfma_f32_16x16x32_bf16 v[152:155], a[16:19], v[20:23], v[152:155]\n" \
"s_waitcnt lgkmcnt(4)\n" \
"v_mfma_f32_16x16x32_bf16 v[148:151], a[24:27], v[16:19], v[148:151]\n" \
"ds_read_b128 a[16:19], v240 offset:22528\n" \
"v_mfma_f32_16x16x32_bf16 v[148:151], a[28:31], v[24:27], v[148:151]\n" \
"v_mfma_f32_16x16x32_bf16 v[156:159], a[28:31], v[28:31], v[156:159]\n" \
"ds_read_b128 a[20:23], v240 offset:30720\n" \
"v_mfma_f32_16x16x32_bf16 v[156:159], a[24:27], v[20:23], v[156:159]\n" \
"s_waitcnt lgkmcnt(4)\n" \
"v_mfma_f32_16x16x32_bf16 v[160:163], a[0:3], v[16:19], v[160:163]\n" \
"s_waitcnt vmcnt(12)\n" \
"s_barrier\n" \
"ds_read_b128 a[24:27], v240 offset:23552\n" \
"v_mfma_f32_16x16x32_bf16 v[160:163], a[4:7], v[24:27], v[160:163]\n" \
"s_lshl2_add_u32 s70, s69, %[wid]\n" \
"s_lshl_b32 s70, s70, 12\n" \
"s_add_u32 s70, s70, 77594624\n" \
"v_mfma_f32_16x16x32_bf16 v[168:171], a[4:7], v[28:31], v[168:171]\n" \
"ds_read_b128 a[28:31], v240 offset:31744\n" \
"v_mfma_f32_16x16x32_bf16 v[168:171], a[0:3], v[20:23], v[168:171]\n" \
"s_waitcnt lgkmcnt(4)\n" \
"buffer_load_dwordx4 a[68:71], v248, s[32:35], s70 offen offset:0 sc1\n" \
"v_mfma_f32_16x16x32_bf16 v[164:167], a[8:11], v[16:19], v[164:167]\n" \
"ds_read_b128 a[0:3], v241 offset:0\n" \
"v_mfma_f32_16x16x32_bf16 v[164:167], a[12:15], v[24:27], v[164:167]\n" \
"v_mfma_f32_16x16x32_bf16 v[172:175], a[12:15], v[28:31], v[172:175]\n" \
"ds_read_b128 a[4:7], v241 offset:8192\n" \
"v_mfma_f32_16x16x32_bf16 v[172:175], a[8:11], v[20:23], v[172:175]\n" \
"s_waitcnt lgkmcnt(4)\n" \
"buffer_load_dwordx4 a[76:79], v248, s[32:35], s70 offen offset:1024 sc1\n" \
"v_mfma_f32_16x16x32_bf16 v[176:179], a[16:19], v[16:19], v[176:179]\n" \
"ds_read_b128 a[8:11], v241 offset:1024\n" \
"v_mfma_f32_16x16x32_bf16 v[176:179], a[20:23], v[24:27], v[176:179]\n" \
"v_mfma_f32_16x16x32_bf16 v[184:187], a[20:23], v[28:31], v[184:187]\n" \
"ds_read_b128 a[12:15], v241 offset:9216\n" \
"v_mfma_f32_16x16x32_bf16 v[184:187], a[16:19], v[20:23], v[184:187]\n" \
"s_waitcnt lgkmcnt(4)\n" \
"buffer_load_dwordx4 a[84:87], v248, s[32:35], s70 offen offset:2048 sc1\n" \
"v_mfma_f32_16x16x32_bf16 v[180:183], a[24:27], v[16:19], v[180:183]\n" \
"ds_read_b128 a[16:19], v241 offset:2048\n" \
"v_mfma_f32_16x16x32_bf16 v[180:183], a[28:31], v[24:27], v[180:183]\n" \
"v_mfma_f32_16x16x32_bf16 v[188:191], a[28:31], v[28:31], v[188:191]\n" \
"ds_read_b128 a[20:23], v241 offset:10240\n" \
"v_mfma_f32_16x16x32_bf16 v[188:191], a[24:27], v[20:23], v[188:191]\n" \
"s_waitcnt lgkmcnt(4)\n" \
"buffer_load_dwordx4 a[92:95], v248, s[32:35], s70 offen offset:3072 sc1\n" \
"v_lshlrev_b32 v231, 2, v251\n" \
"v_mfma_f32_16x16x32_bf16 v[128:131], a[0:3], v[32:35], v[128:131]\n" \
"ds_read_b128 a[24:27], v241 offset:3072\n" \
"v_mfma_f32_16x16x32_bf16 v[128:131], a[4:7], v[40:43], v[128:131]\n" \
"v_mfma_f32_16x16x32_bf16 v[136:139], a[4:7], v[44:47], v[136:139]\n" \
"ds_read_b128 a[28:31], v241 offset:11264\n" \
"v_mfma_f32_16x16x32_bf16 v[136:139], a[0:3], v[36:39], v[136:139]\n" \
"s_waitcnt lgkmcnt(4)\n" \
"buffer_load_dword a100, v231, s[60:63], s79 offen offset:0\n" \
"v_mfma_f32_16x16x32_bf16 v[132:135], a[8:11], v[32:35], v[132:135]\n" \
"ds_read_b128 a[0:3], v241 offset:16384\n" \
"v_mfma_f32_16x16x32_bf16 v[132:135], a[12:15], v[40:43], v[132:135]\n" \
"v_mfma_f32_16x16x32_bf16 v[140:143], a[12:15], v[44:47], v[140:143]\n" \
"ds_read_b128 a[4:7], v241 offset:24576\n" \
"v_mfma_f32_16x16x32_bf16 v[140:143], a[8:11], v[36:39], v[140:143]\n" \
"s_waitcnt lgkmcnt(4)\n" \
"buffer_load_dword a101, v231, s[60:63], s79 offen offset:1024\n" \
"v_mfma_f32_16x16x32_bf16 v[144:147], a[16:19], v[32:35], v[144:147]\n" \
"ds_read_b128 a[8:11], v241 offset:17408\n" \
"v_mfma_f32_16x16x32_bf16 v[144:147], a[20:23], v[40:43], v[144:147]\n" \
"v_mfma_f32_16x16x32_bf16 v[152:155], a[20:23], v[44:47], v[152:155]\n" \
"ds_read_b128 a[12:15], v241 offset:25600\n" \
"v_mfma_f32_16x16x32_bf16 v[152:155], a[16:19], v[36:39], v[152:155]\n" \
"s_waitcnt lgkmcnt(4)\n" \
"buffer_load_dword a102, v231, s[60:63], s79 offen offset:2048\n" \
"v_mfma_f32_16x16x32_bf16 v[148:151], a[24:27], v[32:35], v[148:151]\n" \
"ds_read_b128 a[16:19], v241 offset:18432\n" \
"v_mfma_f32_16x16x32_bf16 v[148:151], a[28:31], v[40:43], v[148:151]\n" \
"v_mfma_f32_16x16x32_bf16 v[156:159], a[28:31], v[44:47], v[156:159]\n" \
"ds_read_b128 a[20:23], v241 offset:26624\n" \
"v_mfma_f32_16x16x32_bf16 v[156:159], a[24:27], v[36:39], v[156:159]\n" \
"s_waitcnt lgkmcnt(4)\n" \
"s_sub_u32 s41, s31, 8192\n" \
"buffer_load_dword a103, v231, s[60:63], s79 offen offset:3072\n" \
"v_mfma_f32_16x16x32_bf16 v[128:131], a[0:3], v[48:51], v[128:131]\n" \
"ds_read_b128 a[24:27], v241 offset:19456\n" \
"v_mfma_f32_16x16x32_bf16 v[128:131], a[4:7], v[56:59], v[128:131]\n" \
"s_waitcnt vmcnt(8)\n" \
"buffer_store_dword v253, v252, s[32:35], s41 offen sc1\n" \
"v_mfma_f32_16x16x32_bf16 v[136:139], a[4:7], v[60:63], v[136:139]\n" \
"ds_read_b128 a[28:31], v241 offset:27648\n" \
"v_mfma_f32_16x16x32_bf16 v[136:139], a[0:3], v[52:55], v[136:139]\n" \
"s_waitcnt lgkmcnt(4)\n" \
"v_mfma_f32_16x16x32_bf16 v[132:135], a[8:11], v[48:51], v[132:135]\n" \
"ds_read_b128 a[0:3], v244 offset:0\n" \
"v_mfma_f32_16x16x32_bf16 v[132:135], a[12:15], v[56:59], v[132:135]\n" \
"buffer_load_dword a108, v231, s[60:63], s80 offen offset:0\n" \
"v_mfma_f32_16x16x32_bf16 v[140:143], a[12:15], v[60:63], v[140:143]\n" \
"ds_read_b128 a[4:7], v244 offset:8192\n" \
"v_mfma_f32_16x16x32_bf16 v[140:143], a[8:11], v[52:55], v[140:143]\n" \
"s_waitcnt lgkmcnt(4)\n" \
"v_mfma_f32_16x16x32_bf16 v[144:147], a[16:19], v[48:51], v[144:147]\n" \
"ds_read_b128 a[8:11], v244 offset:1024\n" \
"v_mfma_f32_16x16x32_bf16 v[144:147], a[20:23], v[56:59], v[144:147]\n" \
"buffer_load_dword a109, v231, s[60:63], s80 offen offset:1024\n" \
"v_mfma_f32_16x16x32_bf16 v[152:155], a[20:23], v[60:63], v[152:155]\n" \
"s_lshl_b32 s17, s67, 18\n" \
"s_sub_u32 s17, 1966080, s17\n" \
"ds_read_b128 a[12:15], v244 offset:9216\n" \
"v_mfma_f32_16x16x32_bf16 v[152:155], a[16:19], v[52:55], v[152:155]\n" \
"s_waitcnt lgkmcnt(4)\n" \
"s_sub_u32 s17, s36, s17\n" \
"s_add_u32 s16, s17, 4096\n" \
"v_mfma_f32_16x16x32_bf16 v[148:151], a[24:27], v[48:51], v[148:151]\n" \
"s_add_u32 s21, s36, 4096\n" \
"ds_read_b128 a[16:19], v244 offset:2048\n" \
"v_and_b32 v246, 7, v251\n" \
"v_mfma_f32_16x16x32_bf16 v[148:151], a[28:31], v[56:59], v[148:151]\n" \
"buffer_load_dword a110, v231, s[60:63], s80 offen offset:2048\n" \
"v_lshlrev_b32 v246, 8, v246\n" \
"v_mfma_f32_16x16x32_bf16 v[156:159], a[28:31], v[60:63], v[156:159]\n" \
"ds_read_b128 a[20:23], v244 offset:10240\n" \
"v_lshlrev_b32 v255, 2, v251\n" \
"v_mfma_f32_16x16x32_bf16 v[156:159], a[24:27], v[52:55], v[156:159]\n" \
"s_waitcnt lgkmcnt(4)\n" \
"v_and_b32 v252, 0x7c, v255\n" \
"v_mfma_f32_16x16x32_bf16 v[128:131], a[0:3], a[64:67], v[128:131]\n" \
"ds_read_b128 a[24:27], v244 offset:3072\n" \
"v_mfma_f32_16x16x32_bf16 v[128:131], a[4:7], a[80:83], v[128:131]\n" \
".Lst1_6_%=:\n" \
"s_cbranch_execz .Lst1_10_%=\n" \
"buffer_load_dword a111, v231, s[60:63], s80 offen offset:3072\n" \
"v_mfma_f32_16x16x32_bf16 v[136:139], a[4:7], a[88:91], v[136:139]\n" \
"ds_read_b128 a[28:31], v244 offset:11264\n" \
"v_mfma_f32_16x16x32_bf16 v[136:139], a[0:3], a[72:75], v[136:139]\n" \
"s_waitcnt lgkmcnt(4)\n" \
"v_mfma_f32_16x16x32_bf16 v[132:135], a[8:11], a[64:67], v[132:135]\n" \
"ds_read_b128 a[0:3], v249 offset:0\n" \
"v_mfma_f32_16x16x32_bf16 v[132:135], a[12:15], a[80:83], v[132:135]\n" \
"v_mfma_f32_16x16x32_bf16 v[140:143], a[12:15], a[88:91], v[140:143]\n" \
"ds_read_b128 a[4:7], v249 offset:8192\n" \
"v_mfma_f32_16x16x32_bf16 v[140:143], a[8:11], a[72:75], v[140:143]\n" \
"s_waitcnt lgkmcnt(4)\n" \
"v_mfma_f32_16x16x32_bf16 v[144:147], a[16:19], a[64:67], v[144:147]\n" \
"ds_read_b128 a[8:11], v249 offset:1024\n" \
"v_mfma_f32_16x16x32_bf16 v[144:147], a[20:23], a[80:83], v[144:147]\n" \
"v_mfma_f32_16x16x32_bf16 v[152:155], a[20:23], a[88:91], v[152:155]\n" \
"ds_read_b128 a[12:15], v249 offset:9216\n" \
"v_mfma_f32_16x16x32_bf16 v[152:155], a[16:19], a[72:75], v[152:155]\n" \
"s_waitcnt lgkmcnt(4)\n" \
"v_mfma_f32_16x16x32_bf16 v[148:151], a[24:27], a[64:67], v[148:151]\n" \
"ds_read_b128 a[16:19], v249 offset:2048\n" \
"v_mfma_f32_16x16x32_bf16 v[148:151], a[28:31], a[80:83], v[148:151]\n" \
"v_mfma_f32_16x16x32_bf16 v[156:159], a[28:31], a[88:91], v[156:159]\n" \
"ds_read_b128 a[20:23], v249 offset:10240\n" \
"v_mfma_f32_16x16x32_bf16 v[156:159], a[24:27], a[72:75], v[156:159]\n" \
"s_waitcnt lgkmcnt(4)\n" \
"v_mfma_f32_16x16x32_bf16 v[128:131], a[0:3], a[96:99], v[128:131]\n" \
"ds_read_b128 a[24:27], v249 offset:3072\n" \
"v_mfma_f32_16x16x32_bf16 v[128:131], a[4:7], a[112:115], v[128:131]\n" \
"v_mfma_f32_16x16x32_bf16 v[136:139], a[4:7], a[116:119], v[136:139]\n" \
"ds_read_b128 a[28:31], v249 offset:11264\n" \
"v_mfma_f32_16x16x32_bf16 v[136:139], a[0:3], a[104:107], v[136:139]\n" \
"s_waitcnt lgkmcnt(4)\n" \
"v_mfma_f32_16x16x32_bf16 v[132:135], a[8:11], a[96:99], v[132:135]\n" \
"ds_read_b128 a[0:3], v239 offset:0\n" \
"v_mfma_f32_16x16x32_bf16 v[132:135], a[12:15], a[112:115], v[132:135]\n" \
"v_mfma_f32_16x16x32_bf16 v[140:143], a[12:15], a[116:119], v[140:143]\n" \
"ds_read_b128 a[4:7], v239 offset:8192\n" \
"v_mfma_f32_16x16x32_bf16 v[140:143], a[8:11], a[104:107], v[140:143]\n" \
"s_waitcnt lgkmcnt(4)\n" \
"v_mfma_f32_16x16x32_bf16 v[144:147], a[16:19], a[96:99], v[144:147]\n" \
"ds_read_b128 a[8:11], v239 offset:1024\n" \
"v_mfma_f32_16x16x32_bf16 v[144:147], a[20:23], a[112:115], v[144:147]\n" \
"v_mfma_f32_16x16x32_bf16 v[152:155], a[20:23], a[116:119], v[152:155]\n" \
"ds_read_b128 a[12:15], v239 offset:9216\n" \
"v_mfma_f32_16x16x32_bf16 v[152:155], a[16:19], a[104:107], v[152:155]\n" \
"s_waitcnt lgkmcnt(4)\n" \
"v_mfma_f32_16x16x32_bf16 v[148:151], a[24:27], a[96:99], v[148:151]\n" \
"ds_read_b128 a[16:19], v239 offset:2048\n" \
"v_mfma_f32_16x16x32_bf16 v[148:151], a[28:31], a[112:115], v[148:151]\n" \
"v_mfma_f32_16x16x32_bf16 v[156:159], a[28:31], a[116:119], v[156:159]\n" \
"ds_read_b128 a[20:23], v239 offset:10240\n" \
"v_mfma_f32_16x16x32_bf16 v[156:159], a[24:27], a[104:107], v[156:159]\n" \
"s_waitcnt lgkmcnt(4)\n" \
"v_mfma_f32_16x16x32_bf16 v[128:131], a[0:3], a[120:123], v[128:131]\n" \
"ds_read_b128 a[24:27], v239 offset:3072\n" \
"v_mfma_f32_16x16x32_bf16 v[128:131], a[4:7], a[128:131], v[128:131]\n" \
"v_mfma_f32_16x16x32_bf16 v[136:139], a[4:7], a[132:135], v[136:139]\n" \
"ds_read_b128 a[28:31], v239 offset:11264\n" \
"v_mfma_f32_16x16x32_bf16 v[136:139], a[0:3], a[124:127], v[136:139]\n" \
"s_waitcnt lgkmcnt(4)\n" \
"v_mfma_f32_16x16x32_bf16 v[132:135], a[8:11], a[120:123], v[132:135]\n" \
"ds_read_b128 a[0:3], v239 offset:16384\n" \
"v_mfma_f32_16x16x32_bf16 v[132:135], a[12:15], a[128:131], v[132:135]\n" \
"v_mfma_f32_16x16x32_bf16 v[140:143], a[12:15], a[132:135], v[140:143]\n" \
"ds_read_b128 a[4:7], v239 offset:24576\n" \
"v_mfma_f32_16x16x32_bf16 v[140:143], a[8:11], a[124:127], v[140:143]\n" \
"s_waitcnt lgkmcnt(4)\n" \
"v_mfma_f32_16x16x32_bf16 v[144:147], a[16:19], a[120:123], v[144:147]\n" \
"ds_read_b128 a[8:11], v239 offset:17408\n" \
"v_mfma_f32_16x16x32_bf16 v[144:147], a[20:23], a[128:131], v[144:147]\n" \
"v_mfma_f32_16x16x32_bf16 v[152:155], a[20:23], a[132:135], v[152:155]\n" \
"ds_read_b128 a[12:15], v239 offset:25600\n" \
"v_mfma_f32_16x16x32_bf16 v[152:155], a[16:19], a[124:127], v[152:155]\n" \
"s_waitcnt lgkmcnt(4)\n" \
"v_mfma_f32_16x16x32_bf16 v[148:151], a[24:27], a[120:123], v[148:151]\n" \
"ds_read_b128 a[16:19], v239 offset:18432\n" \
"v_mfma_f32_16x16x32_bf16 v[148:151], a[28:31], a[128:131], v[148:151]\n" \
"v_mfma_f32_16x16x32_bf16 v[156:159], a[28:31], a[132:135], v[156:159]\n" \
"ds_read_b128 a[20:23], v239 offset:26624\n" \
"v_mfma_f32_16x16x32_bf16 v[156:159], a[24:27], a[124:127], v[156:159]\n" \
"s_waitcnt lgkmcnt(4)\n" \
"v_mfma_f32_16x16x32_bf16 v[128:131], a[0:3], a[136:139], v[128:131]\n" \
"ds_read_b128 a[24:27], v239 offset:19456\n" \
"v_mfma_f32_16x16x32_bf16 v[128:131], a[4:7], a[144:147], v[128:131]\n" \
"v_mfma_f32_16x16x32_bf16 v[136:139], a[4:7], a[148:151], v[136:139]\n" \
"ds_read_b128 a[28:31], v239 offset:27648\n" \
"v_mfma_f32_16x16x32_bf16 v[136:139], a[0:3], a[140:143], v[136:139]\n" \
"s_waitcnt lgkmcnt(4)\n" \
"v_mfma_f32_16x16x32_bf16 v[132:135], a[8:11], a[136:139], v[132:135]\n" \
"s_lshl2_add_u32 s18, s67, %[wid]\n" \
"s_lshl_b32 s18, s18, 11\n" \
"ds_read_b128 a[0:3], v241 offset:4096\n" \
"v_mfma_f32_16x16x32_bf16 v[132:135], a[12:15], a[144:147], v[132:135]\n" \
"s_add_u32 s18, s18, 589824\n" \
"s_lshl2_add_u32 s39, s68, s18\n" \
"s_lshl_b32 s20, s64, 8\n" \
"v_mfma_f32_16x16x32_bf16 v[140:143], a[12:15], a[148:151], v[140:143]\n" \
"s_add_u32 s18, s18, s20\n" \
"ds_read_b128 a[4:7], v241 offset:12288\n" \
"v_mfma_f32_16x16x32_bf16 v[140:143], a[8:11], a[140:143], v[140:143]\n" \
"s_waitcnt lgkmcnt(4)\n" \
"v_mfma_f32_16x16x32_bf16 v[144:147], a[16:19], a[136:139], v[144:147]\n" \
"ds_read_b128 a[8:11], v241 offset:5120\n" \
"v_mfma_f32_16x16x32_bf16 v[144:147], a[20:23], a[144:147], v[144:147]\n" \
"v_mfma_f32_16x16x32_bf16 v[152:155], a[20:23], a[148:151], v[152:155]\n" \
"ds_read_b128 a[12:15], v241 offset:13312\n" \
"v_mfma_f32_16x16x32_bf16 v[152:155], a[16:19], a[140:143], v[152:155]\n" \
"s_waitcnt lgkmcnt(4)\n" \
"v_mfma_f32_16x16x32_bf16 v[148:151], a[24:27], a[136:139], v[148:151]\n" \
"ds_read_b128 a[16:19], v241 offset:20480\n" \
"v_mfma_f32_16x16x32_bf16 v[148:151], a[28:31], a[144:147], v[148:151]\n" \
"v_cvt_pk_bf16_f32 v128, v128, v129\n" \
"v_cvt_pk_bf16_f32 v129, v130, v131\n" \
"v_mfma_f32_16x16x32_bf16 v[156:159], a[28:31], a[148:151], v[156:159]\n" \
"ds_read_b128 a[20:23], v241 offset:28672\n" \
"v_cvt_pk_bf16_f32 v130, v132, v133\n" \
"v_mfma_f32_16x16x32_bf16 v[156:159], a[24:27], a[140:143], v[156:159]\n" \
"s_waitcnt lgkmcnt(4)\n" \
"v_cvt_pk_bf16_f32 v131, v134, v135\n" \
"v_mfma_f32_16x16x32_bf16 v[160:163], a[0:3], v[32:35], v[160:163]\n" \
"ds_read_b128 a[24:27], v241 offset:21504\n" \
"v_mfma_f32_16x16x32_bf16 v[160:163], a[4:7], v[40:43], v[160:163]\n" \
"v_mfma_f32_16x16x32_bf16 v[168:171], a[4:7], v[44:47], v[168:171]\n" \
"ds_read_b128 a[28:31], v241 offset:29696\n" \
"v_mfma_f32_16x16x32_bf16 v[168:171], a[0:3], v[36:39], v[168:171]\n" \
"s_waitcnt lgkmcnt(4)\n" \
"buffer_store_dwordx4 v[128:131], v250, s[32:35], s17 offen offset:0 sc1\n" \
"v_mfma_f32_16x16x32_bf16 v[164:167], a[8:11], v[32:35], v[164:167]\n" \
"ds_read_b128 a[0:3], v244 offset:4096\n" \
"v_mfma_f32_16x16x32_bf16 v[164:167], a[12:15], v[40:43], v[164:167]\n" \
"buffer_load_dword v247, v254, s[32:35], s41 offen sc1\n" \
"v_mfma_f32_16x16x32_bf16 v[172:175], a[12:15], v[44:47], v[172:175]\n" \
"ds_read_b128 a[4:7], v244 offset:12288\n" \
"v_mfma_f32_16x16x32_bf16 v[172:175], a[8:11], v[36:39], v[172:175]\n" \
"s_waitcnt lgkmcnt(4)\n" \
"v_cvt_pk_bf16_f32 v136, v136, v137\n" \
"v_cvt_pk_bf16_f32 v137, v138, v139\n" \
"v_mfma_f32_16x16x32_bf16 v[160:163], a[16:19], v[48:51], v[160:163]\n" \
"ds_read_b128 a[8:11], v244 offset:5120\n" \
"v_cvt_pk_bf16_f32 v138, v140, v141\n" \
"v_mfma_f32_16x16x32_bf16 v[160:163], a[20:23], v[56:59], v[160:163]\n" \
"v_cvt_pk_bf16_f32 v139, v142, v143\n" \
"v_mfma_f32_16x16x32_bf16 v[168:171], a[20:23], v[60:63], v[168:171]\n" \
"ds_read_b128 a[12:15], v244 offset:13312\n" \
"v_mfma_f32_16x16x32_bf16 v[168:171], a[16:19], v[52:55], v[168:171]\n" \
"s_waitcnt lgkmcnt(4)\n" \
"v_mfma_f32_16x16x32_bf16 v[164:167], a[24:27], v[48:51], v[164:167]\n" \
"ds_read_b128 a[16:19], v249 offset:4096\n" \
"v_mfma_f32_16x16x32_bf16 v[164:167], a[28:31], v[56:59], v[164:167]\n" \
"v_mfma_f32_16x16x32_bf16 v[172:175], a[28:31], v[60:63], v[172:175]\n" \
"ds_read_b128 a[20:23], v249 offset:12288\n" \
"v_mfma_f32_16x16x32_bf16 v[172:175], a[24:27], v[52:55], v[172:175]\n" \
"s_waitcnt lgkmcnt(4)\n" \
"buffer_store_dwordx4 v[136:139], v250, s[32:35], s16 offen offset:0 sc1\n" \
"v_mfma_f32_16x16x32_bf16 v[160:163], a[0:3], a[64:67], v[160:163]\n" \
"ds_read_b128 a[24:27], v249 offset:5120\n" \
"v_mfma_f32_16x16x32_bf16 v[160:163], a[4:7], a[80:83], v[160:163]\n" \
"v_mfma_f32_16x16x32_bf16 v[168:171], a[4:7], a[88:91], v[168:171]\n" \
"ds_read_b128 a[28:31], v249 offset:13312\n" \
"v_mfma_f32_16x16x32_bf16 v[168:171], a[0:3], a[72:75], v[168:171]\n" \
"s_waitcnt lgkmcnt(4)\n" \
"v_cvt_pk_bf16_f32 v144, v144, v145\n" \
"v_cvt_pk_bf16_f32 v145, v146, v147\n" \
"v_mfma_f32_16x16x32_bf16 v[164:167], a[8:11], a[64:67], v[164:167]\n" \
"ds_read_b128 a[0:3], v239 offset:4096\n" \
"v_cvt_pk_bf16_f32 v146, v148, v149\n" \
"v_mfma_f32_16x16x32_bf16 v[164:167], a[12:15], a[80:83], v[164:167]\n" \
"v_cvt_pk_bf16_f32 v147, v150, v151\n" \
"v_mfma_f32_16x16x32_bf16 v[172:175], a[12:15], a[88:91], v[172:175]\n" \
"ds_read_b128 a[4:7], v239 offset:12288\n" \
"v_mfma_f32_16x16x32_bf16 v[172:175], a[8:11], a[72:75], v[172:175]\n" \
"s_waitcnt lgkmcnt(4)\n" \
"v_mfma_f32_16x16x32_bf16 v[160:163], a[16:19], a[96:99], v[160:163]\n" \
"ds_read_b128 a[8:11], v239 offset:5120\n" \
"v_mfma_f32_16x16x32_bf16 v[160:163], a[20:23], a[112:115], v[160:163]\n" \
"v_mfma_f32_16x16x32_bf16 v[168:171], a[20:23], a[116:119], v[168:171]\n" \
"ds_read_b128 a[12:15], v239 offset:13312\n" \
"v_mfma_f32_16x16x32_bf16 v[168:171], a[16:19], a[104:107], v[168:171]\n" \
"s_waitcnt lgkmcnt(4)\n" \
"buffer_store_dwordx4 v[144:147], v250, s[32:35], s17 offen offset:1024 sc1\n" \
"v_mfma_f32_16x16x32_bf16 v[164:167], a[24:27], a[96:99], v[164:167]\n" \
"ds_read_b128 a[16:19], v239 offset:20480\n" \
"v_mfma_f32_16x16x32_bf16 v[164:167], a[28:31], a[112:115], v[164:167]\n" \
"v_mfma_f32_16x16x32_bf16 v[172:175], a[28:31], a[116:119], v[172:175]\n" \
"ds_read_b128 a[20:23], v239 offset:28672\n" \
"v_mfma_f32_16x16x32_bf16 v[172:175], a[24:27], a[104:107], v[172:175]\n" \
"s_waitcnt lgkmcnt(4)\n" \
"v_cvt_pk_bf16_f32 v152, v152, v153\n" \
"v_cvt_pk_bf16_f32 v153, v154, v155\n" \
"v_mfma_f32_16x16x32_bf16 v[160:163], a[0:3], a[120:123], v[160:163]\n" \
"ds_read_b128 a[24:27], v239 offset:21504\n" \
"v_cvt_pk_bf16_f32 v154, v156, v157\n" \
"v_mfma_f32_16x16x32_bf16 v[160:163], a[4:7], a[128:131], v[160:163]\n" \
"v_cvt_pk_bf16_f32 v155, v158, v159\n" \
"v_mfma_f32_16x16x32_bf16 v[168:171], a[4:7], a[132:135], v[168:171]\n" \
"ds_read_b128 a[28:31], v239 offset:29696\n" \
"v_mfma_f32_16x16x32_bf16 v[168:171], a[0:3], a[124:127], v[168:171]\n" \
"s_waitcnt lgkmcnt(4)\n" \
"v_mfma_f32_16x16x32_bf16 v[164:167], a[8:11], a[120:123], v[164:167]\n" \
"ds_read_b128 a[0:3], v241 offset:6144\n" \
"v_mfma_f32_16x16x32_bf16 v[164:167], a[12:15], a[128:131], v[164:167]\n" \
"v_mfma_f32_16x16x32_bf16 v[172:175], a[12:15], a[132:135], v[172:175]\n" \
"ds_read_b128 a[4:7], v241 offset:14336\n" \
"v_mfma_f32_16x16x32_bf16 v[172:175], a[8:11], a[124:127], v[172:175]\n" \
"s_waitcnt lgkmcnt(4)\n" \
"buffer_store_dwordx4 v[152:155], v250, s[32:35], s16 offen offset:1024 sc1\n" \
"v_mfma_f32_16x16x32_bf16 v[160:163], a[16:19], a[136:139], v[160:163]\n" \
"ds_read_b128 a[8:11], v241 offset:7168\n" \
"v_mfma_f32_16x16x32_bf16 v[160:163], a[20:23], a[144:147], v[160:163]\n" \
"v_mfma_f32_16x16x32_bf16 v[168:171], a[20:23], a[148:151], v[168:171]\n" \
"ds_read_b128 a[12:15], v241 offset:15360\n" \
"v_mfma_f32_16x16x32_bf16 v[168:171], a[16:19], a[140:143], v[168:171]\n" \
"s_waitcnt lgkmcnt(4)\n" \
"v_mfma_f32_16x16x32_bf16 v[164:167], a[24:27], a[136:139], v[164:167]\n" \
"ds_read_b128 a[16:19], v241 offset:22528\n" \
"v_mfma_f32_16x16x32_bf16 v[164:167], a[28:31], a[144:147], v[164:167]\n" \
"v_mfma_f32_16x16x32_bf16 v[172:175], a[28:31], a[148:151], v[172:175]\n" \
"ds_read_b128 a[20:23], v241 offset:30720\n" \
"v_mfma_f32_16x16x32_bf16 v[172:175], a[24:27], a[140:143], v[172:175]\n" \
"s_waitcnt lgkmcnt(4)\n" \
"v_mfma_f32_16x16x32_bf16 v[176:179], a[0:3], v[32:35], v[176:179]\n" \
"ds_read_b128 a[24:27], v241 offset:23552\n" \
"v_mfma_f32_16x16x32_bf16 v[176:179], a[4:7], v[40:43], v[176:179]\n" \
"v_mfma_f32_16x16x32_bf16 v[184:187], a[4:7], v[44:47], v[184:187]\n" \
"ds_read_b128 a[28:31], v241 offset:31744\n" \
"v_mfma_f32_16x16x32_bf16 v[184:187], a[0:3], v[36:39], v[184:187]\n" \
"s_waitcnt lgkmcnt(4)\n" \
"v_mfma_f32_16x16x32_bf16 v[180:183], a[8:11], v[32:35], v[180:183]\n" \
"ds_read_b128 a[0:3], v244 offset:6144\n" \
"v_mfma_f32_16x16x32_bf16 v[180:183], a[12:15], v[40:43], v[180:183]\n" \
"v_cvt_pk_bf16_f32 v160, v160, v161\n" \
"v_cvt_pk_bf16_f32 v161, v162, v163\n" \
"v_mfma_f32_16x16x32_bf16 v[188:191], a[12:15], v[44:47], v[188:191]\n" \
"ds_read_b128 a[4:7], v244 offset:14336\n" \
"v_cvt_pk_bf16_f32 v162, v164, v165\n" \
"v_mfma_f32_16x16x32_bf16 v[188:191], a[8:11], v[36:39], v[188:191]\n" \
"s_waitcnt lgkmcnt(4)\n" \
"v_cvt_pk_bf16_f32 v163, v166, v167\n" \
"v_mfma_f32_16x16x32_bf16 v[176:179], a[16:19], v[48:51], v[176:179]\n" \
"s_waitcnt vmcnt(3)\n" \
"ds_read_b128 a[8:11], v244 offset:7168\n" \
"v_cmp_eq_u32 vcc, 0, v247\n" \
"v_mfma_f32_16x16x32_bf16 v[176:179], a[20:23], v[56:59], v[176:179]\n" \
"s_add_u32 s38, s66, 1\n" \
"s_and_b32 s38, s38, 3\n" \
"v_mfma_f32_16x16x32_bf16 v[184:187], a[20:23], v[60:63], v[184:187]\n" \
"s_lshl_b32 s38, s38, 13\n" \
"ds_read_b128 a[12:15], v244 offset:15360\n" \
"v_mfma_f32_16x16x32_bf16 v[184:187], a[16:19], v[52:55], v[184:187]\n" \
"s_waitcnt lgkmcnt(4)\n" \
"s_cbranch_vccnz .LspcA_%=\n" \
".LsbcA_%=:\n" \
"v_mfma_f32_16x16x32_bf16 v[180:183], a[24:27], v[48:51], v[180:183]\n" \
"s_add_u32 s38, s38, s37\n" \
"ds_read_b128 a[16:19], v249 offset:6144\n" \
"v_mfma_f32_16x16x32_bf16 v[180:183], a[28:31], v[56:59], v[180:183]\n" \
"buffer_store_dwordx4 v[160:163], v250, s[32:35], s17 offen offset:2048 sc1\n" \
"v_mfma_f32_16x16x32_bf16 v[188:191], a[28:31], v[60:63], v[188:191]\n" \
"ds_read_b128 a[20:23], v249 offset:14336\n" \
"v_mfma_f32_16x16x32_bf16 v[188:191], a[24:27], v[52:55], v[188:191]\n" \
"s_waitcnt lgkmcnt(4)\n" \
"v_cvt_pk_bf16_f32 v168, v168, v169\n" \
"v_cvt_pk_bf16_f32 v169, v170, v171\n" \
"v_mfma_f32_16x16x32_bf16 v[176:179], a[0:3], a[64:67], v[176:179]\n" \
"ds_read_b128 a[24:27], v249 offset:7168\n" \
"v_cvt_pk_bf16_f32 v170, v172, v173\n" \
"v_mfma_f32_16x16x32_bf16 v[176:179], a[4:7], a[80:83], v[176:179]\n" \
"buffer_load_dwordx4 a[192:195], v248, s[32:35], s38 offen offset:0 sc1\n" \
"v_cvt_pk_bf16_f32 v171, v174, v175\n" \
"v_mfma_f32_16x16x32_bf16 v[184:187], a[4:7], a[88:91], v[184:187]\n" \
"ds_read_b128 a[28:31], v249 offset:15360\n" \
"v_mfma_f32_16x16x32_bf16 v[184:187], a[0:3], a[72:75], v[184:187]\n" \
"s_waitcnt lgkmcnt(4)\n" \
"v_mfma_f32_16x16x32_bf16 v[180:183], a[8:11], a[64:67], v[180:183]\n" \
"ds_read_b128 a[0:3], v239 offset:6144\n" \
"v_mfma_f32_16x16x32_bf16 v[180:183], a[12:15], a[80:83], v[180:183]\n" \
"buffer_load_dwordx4 a[196:199], v248, s[32:35], s38 offen offset:1024 sc1\n" \
"v_mfma_f32_16x16x32_bf16 v[188:191], a[12:15], a[88:91], v[188:191]\n" \
"ds_read_b128 a[4:7], v239 offset:14336\n" \
"v_mfma_f32_16x16x32_bf16 v[188:191], a[8:11], a[72:75], v[188:191]\n" \
"s_waitcnt lgkmcnt(4)\n" \
"buffer_store_dwordx4 v[168:171], v250, s[32:35], s16 offen offset:2048 sc1\n" \
"v_mfma_f32_16x16x32_bf16 v[176:179], a[16:19], a[96:99], v[176:179]\n" \
"ds_read_b128 a[8:11], v239 offset:7168\n" \
"v_mfma_f32_16x16x32_bf16 v[176:179], a[20:23], a[112:115], v[176:179]\n" \
"v_mfma_f32_16x16x32_bf16 v[184:187], a[20:23], a[116:119], v[184:187]\n" \
"ds_read_b128 a[12:15], v239 offset:15360\n" \
"v_mfma_f32_16x16x32_bf16 v[184:187], a[16:19], a[104:107], v[184:187]\n" \
"s_waitcnt lgkmcnt(4)\n" \
"buffer_load_dwordx4 a[200:203], v248, s[32:35], s38 offen offset:2048 sc1\n" \
"v_mfma_f32_16x16x32_bf16 v[180:183], a[24:27], a[96:99], v[180:183]\n" \
"ds_read_b128 a[16:19], v239 offset:22528\n" \
"v_mfma_f32_16x16x32_bf16 v[180:183], a[28:31], a[112:115], v[180:183]\n" \
"v_mfma_f32_16x16x32_bf16 v[188:191], a[28:31], a[116:119], v[188:191]\n" \
"ds_read_b128 a[20:23], v239 offset:30720\n" \
"v_mfma_f32_16x16x32_bf16 v[188:191], a[24:27], a[104:107], v[188:191]\n" \
"s_waitcnt lgkmcnt(4)\n" \
"buffer_load_dwordx4 a[204:207], v248, s[32:35], s38 offen offset:3072 sc1\n" \
"v_mfma_f32_16x16x32_bf16 v[176:179], a[0:3], a[120:123], v[176:179]\n" \
"s_add_u32 s38, s38, 4096\n" \
"ds_read_b128 a[24:27], v239 offset:23552\n" \
"v_mfma_f32_16x16x32_bf16 v[176:179], a[4:7], a[128:131], v[176:179]\n" \
"v_mfma_f32_16x16x32_bf16 v[184:187], a[4:7], a[132:135], v[184:187]\n" \
"ds_read_b128 a[28:31], v239 offset:31744\n" \
"v_mfma_f32_16x16x32_bf16 v[184:187], a[0:3], a[124:127], v[184:187]\n" \
"s_waitcnt lgkmcnt(4)\n" \
"buffer_load_dwordx4 a[208:211], v248, s[32:35], s38 offen offset:0 sc1\n" \
"v_mfma_f32_16x16x32_bf16 v[180:183], a[8:11], a[120:123], v[180:183]\n" \
"ds_read_b128 a[0:3], v240 offset:0\n" \
"v_mfma_f32_16x16x32_bf16 v[180:183], a[12:15], a[128:131], v[180:183]\n" \
"v_mfma_f32_16x16x32_bf16 v[188:191], a[12:15], a[132:135], v[188:191]\n" \
"ds_read_b128 a[4:7], v240 offset:8192\n" \
"v_mfma_f32_16x16x32_bf16 v[188:191], a[8:11], a[124:127], v[188:191]\n" \
"s_waitcnt lgkmcnt(4)\n" \
"buffer_load_dwordx4 a[212:215], v248, s[32:35], s38 offen offset:1024 sc1\n" \
"v_mfma_f32_16x16x32_bf16 v[176:179], a[16:19], a[136:139], v[176:179]\n" \
"ds_read_b128 a[8:11], v240 offset:1024\n" \
"v_mfma_f32_16x16x32_bf16 v[176:179], a[20:23], a[144:147], v[176:179]\n" \
"v_mfma_f32_16x16x32_bf16 v[184:187], a[20:23], a[148:151], v[184:187]\n" \
"ds_read_b128 a[12:15], v240 offset:9216\n" \
"v_mfma_f32_16x16x32_bf16 v[184:187], a[16:19], a[140:143], v[184:187]\n" \
"s_waitcnt lgkmcnt(4)\n" \
"buffer_load_dwordx4 a[216:219], v248, s[32:35], s38 offen offset:2048 sc1\n" \
"v_mfma_f32_16x16x32_bf16 v[180:183], a[24:27], a[136:139], v[180:183]\n" \
"ds_read_b128 a[16:19], v240 offset:2048\n" \
"v_mfma_f32_16x16x32_bf16 v[180:183], a[28:31], a[144:147], v[180:183]\n" \
"v_mfma_f32_16x16x32_bf16 v[188:191], a[28:31], a[148:151], v[188:191]\n" \
"ds_read_b128 a[20:23], v240 offset:10240\n" \
"v_mfma_f32_16x16x32_bf16 v[188:191], a[24:27], a[140:143], v[188:191]\n" \
"s_waitcnt lgkmcnt(4)\n" \
"buffer_load_dwordx4 a[220:223], v248, s[32:35], s38 offen offset:3072 sc1\n" \
"v_mfma_f32_16x16x32_bf16 v[64:67], a[0:3], v[192:195], 0\n" \
"s_add_u32 s38, s66, 2\n" \
"s_and_b32 s38, s38, 3\n" \
"ds_read_b128 a[24:27], v240 offset:3072\n" \
"v_mfma_f32_16x16x32_bf16 v[64:67], a[4:7], v[200:203], v[64:67]\n" \
"s_lshl_b32 s38, s38, 13\n" \
"s_add_u32 s38, s38, s37\n" \
"v_mfma_f32_16x16x32_bf16 v[72:75], a[4:7], v[204:207], 0\n" \
"ds_read_b128 a[28:31], v240 offset:11264\n" \
"v_mfma_f32_16x16x32_bf16 v[72:75], a[0:3], v[196:199], v[72:75]\n" \
"s_waitcnt lgkmcnt(4)\n" \
"buffer_load_dwordx4 a[224:227], v248, s[32:35], s38 offen offset:0 sc1\n" \
"v_mfma_f32_16x16x32_bf16 v[68:71], a[8:11], v[192:195], 0\n" \
"ds_read_b128 a[0:3], v240 offset:4096\n" \
"v_mfma_f32_16x16x32_bf16 v[68:71], a[12:15], v[200:203], v[68:71]\n" \
"v_cvt_pk_bf16_f32 v176, v176, v177\n" \
"v_cvt_pk_bf16_f32 v177, v178, v179\n" \
"v_mfma_f32_16x16x32_bf16 v[76:79], a[12:15], v[204:207], 0\n" \
"ds_read_b128 a[4:7], v240 offset:12288\n" \
"v_cvt_pk_bf16_f32 v178, v180, v181\n" \
"v_mfma_f32_16x16x32_bf16 v[76:79], a[8:11], v[196:199], v[76:79]\n" \
"s_waitcnt lgkmcnt(4)\n" \
"buffer_load_dwordx4 a[228:231], v248, s[32:35], s38 offen offset:1024 sc1\n" \
"v_cvt_pk_bf16_f32 v179, v182, v183\n" \
"v_mfma_f32_16x16x32_bf16 v[80:83], a[16:19], v[192:195], 0\n" \
"ds_read_b128 a[8:11], v240 offset:5120\n" \
"v_mfma_f32_16x16x32_bf16 v[80:83], a[20:23], v[200:203], v[80:83]\n" \
"v_mfma_f32_16x16x32_bf16 v[88:91], a[20:23], v[204:207], 0\n" \
"ds_read_b128 a[12:15], v240 offset:13312\n" \
"v_mfma_f32_16x16x32_bf16 v[88:91], a[16:19], v[196:199], v[88:91]\n" \
"s_waitcnt lgkmcnt(4)\n" \
"buffer_store_dwordx4 v[176:179], v250, s[32:35], s17 offen offset:3072 sc1\n" \
"v_mfma_f32_16x16x32_bf16 v[84:87], a[24:27], v[192:195], 0\n" \
"ds_read_b128 a[16:19], v240 offset:6144\n" \
"v_mfma_f32_16x16x32_bf16 v[84:87], a[28:31], v[200:203], v[84:87]\n" \
"v_mfma_f32_16x16x32_bf16 v[92:95], a[28:31], v[204:207], 0\n" \
"ds_read_b128 a[20:23], v240 offset:14336\n" \
"v_mfma_f32_16x16x32_bf16 v[92:95], a[24:27], v[196:199], v[92:95]\n" \
"s_waitcnt lgkmcnt(4)\n" \
"buffer_load_dwordx4 a[232:235], v248, s[32:35], s38 offen offset:2048 sc1\n" \
"v_cvt_pk_bf16_f32 v184, v184, v185\n" \
"v_mfma_f32_16x16x32_bf16 v[96:99], a[0:3], v[192:195], 0\n" \
"ds_read_b128 a[24:27], v240 offset:7168\n" \
"v_cvt_pk_bf16_f32 v185, v186, v187\n" \
"v_mfma_f32_16x16x32_bf16 v[96:99], a[4:7], v[200:203], v[96:99]\n" \
"v_cvt_pk_bf16_f32 v186, v188, v189\n" \
"v_cvt_pk_bf16_f32 v187, v190, v191\n" \
"v_mfma_f32_16x16x32_bf16 v[104:107], a[4:7], v[204:207], 0\n" \
"ds_read_b128 a[28:31], v240 offset:15360\n" \
"v_mfma_f32_16x16x32_bf16 v[104:107], a[0:3], v[196:199], v[104:107]\n" \
"s_waitcnt lgkmcnt(4)\n" \
"buffer_load_dwordx4 a[236:239], v248, s[32:35], s38 offen offset:3072 sc1\n" \
"v_mfma_f32_16x16x32_bf16 v[100:103], a[8:11], v[192:195], 0\n" \
"s_add_u32 s38, s38, 4096\n" \
"ds_read_b128 a[0:3], v240 offset:16384\n" \
"v_mfma_f32_16x16x32_bf16 v[100:103], a[12:15], v[200:203], v[100:103]\n" \
"v_mfma_f32_16x16x32_bf16 v[108:111], a[12:15], v[204:207], 0\n" \
"ds_read_b128 a[4:7], v240 offset:24576\n" \
"v_mfma_f32_16x16x32_bf16 v[108:111], a[8:11], v[196:199], v[108:111]\n" \
"s_waitcnt lgkmcnt(4)\n" \
"buffer_store_dwordx4 v[184:187], v250, s[32:35], s16 offen offset:3072 sc1\n" \
"v_mfma_f32_16x16x32_bf16 v[112:115], a[16:19], v[192:195], 0\n" \
"ds_read_b128 a[8:11], v240 offset:17408\n" \
"v_lshrrev_b32 v188, 2, v251\n" \
"v_mfma_f32_16x16x32_bf16 v[112:115], a[20:23], v[200:203], v[112:115]\n" \
"v_lshlrev_b32 v188, 6, v188\n" \
"v_and_b32 v189, 3, v251\n" \
"v_mfma_f32_16x16x32_bf16 v[120:123], a[20:23], v[204:207], 0\n" \
"ds_read_b128 a[12:15], v240 offset:25600\n" \
"v_lshl_or_b32 v188, v189, 3, v188\n" \
"v_mfma_f32_16x16x32_bf16 v[120:123], a[16:19], v[196:199], v[120:123]\n" \
"s_waitcnt lgkmcnt(4)\n" \
"s_lshl_b32 s19, %[wid], 6\n" \
"buffer_load_dwordx4 a[240:243], v248, s[32:35], s38 offen offset:0 sc1\n" \
"v_mfma_f32_16x16x32_bf16 v[116:119], a[24:27], v[192:195], 0\n" \
"ds_read_b128 a[16:19], v240 offset:18432\n" \
"v_add_u32 v191, s19, v251\n" \
"v_mfma_f32_16x16x32_bf16 v[116:119], a[28:31], v[200:203], v[116:119]\n" \
"v_lshlrev_b32 v190, 3, v191\n" \
"v_mfma_f32_16x16x32_bf16 v[124:127], a[28:31], v[204:207], 0\n" \
"ds_read_b128 a[20:23], v240 offset:26624\n" \
"v_mfma_f32_16x16x32_bf16 v[124:127], a[24:27], v[196:199], v[124:127]\n" \
"s_waitcnt lgkmcnt(4)\n" \
"buffer_load_dwordx4 a[244:247], v248, s[32:35], s38 offen offset:1024 sc1\n" \
"v_mfma_f32_16x16x32_bf16 v[64:67], a[0:3], v[208:211], v[64:67]\n" \
"ds_read_b128 a[24:27], v240 offset:19456\n" \
"v_mfma_f32_16x16x32_bf16 v[64:67], a[4:7], v[216:219], v[64:67]\n" \
"v_mfma_f32_16x16x32_bf16 v[72:75], a[4:7], v[220:223], v[72:75]\n" \
"ds_read_b128 a[28:31], v240 offset:27648\n" \
"v_mfma_f32_16x16x32_bf16 v[72:75], a[0:3], v[212:215], v[72:75]\n" \
"s_waitcnt lgkmcnt(4)\n" \
"buffer_load_dwordx4 a[248:251], v248, s[32:35], s38 offen offset:2048 sc1\n" \
"v_mfma_f32_16x16x32_bf16 v[68:71], a[8:11], v[208:211], v[68:71]\n" \
"ds_read_b128 a[0:3], v240 offset:20480\n" \
"v_mfma_f32_16x16x32_bf16 v[68:71], a[12:15], v[216:219], v[68:71]\n" \
"v_mfma_f32_16x16x32_bf16 v[76:79], a[12:15], v[220:223], v[76:79]\n" \
"ds_read_b128 a[4:7], v240 offset:28672\n" \
"v_mfma_f32_16x16x32_bf16 v[76:79], a[8:11], v[212:215], v[76:79]\n" \
"s_waitcnt lgkmcnt(4)\n" \
"buffer_load_dwordx4 a[252:255], v248, s[32:35], s38 offen offset:3072 sc1\n" \
"v_mfma_f32_16x16x32_bf16 v[80:83], a[16:19], v[208:211], v[80:83]\n" \
"s_add_u32 s38, s66, 3\n" \
"s_and_b32 s38, s38, 3\n" \
"ds_read_b128 a[8:11], v240 offset:21504\n" \
"v_mfma_f32_16x16x32_bf16 v[80:83], a[20:23], v[216:219], v[80:83]\n" \
"s_lshl_b32 s38, s38, 13\n" \
"s_add_u32 s38, s38, s37\n" \
"v_mfma_f32_16x16x32_bf16 v[88:91], a[20:23], v[220:223], v[88:91]\n" \
"ds_read_b128 a[12:15], v240 offset:29696\n" \
"v_mfma_f32_16x16x32_bf16 v[88:91], a[16:19], v[212:215], v[88:91]\n" \
"s_waitcnt lgkmcnt(4)\n" \
"buffer_load_dwordx4 a[64:67], v248, s[32:35], s38 offen offset:0 sc1\n" \
"v_mfma_f32_16x16x32_bf16 v[84:87], a[24:27], v[208:211], v[84:87]\n" \
"ds_read_b128 a[16:19], v240 offset:22528\n" \
"v_mfma_f32_16x16x32_bf16 v[84:87], a[28:31], v[216:219], v[84:87]\n" \
"v_mfma_f32_16x16x32_bf16 v[92:95], a[28:31], v[220:223], v[92:95]\n" \
"ds_read_b128 a[20:23], v240 offset:30720\n" \
"v_mfma_f32_16x16x32_bf16 v[92:95], a[24:27], v[212:215], v[92:95]\n" \
"s_waitcnt lgkmcnt(4)\n" \
"buffer_load_dwordx4 a[72:75], v248, s[32:35], s38 offen offset:1024 sc1\n" \
"v_mfma_f32_16x16x32_bf16 v[96:99], a[0:3], v[208:211], v[96:99]\n" \
"ds_read_b128 a[24:27], v240 offset:23552\n" \
"v_mfma_f32_16x16x32_bf16 v[96:99], a[4:7], v[216:219], v[96:99]\n" \
"v_mfma_f32_16x16x32_bf16 v[104:107], a[4:7], v[220:223], v[104:107]\n" \
"ds_read_b128 a[28:31], v240 offset:31744\n" \
"v_mfma_f32_16x16x32_bf16 v[104:107], a[0:3], v[212:215], v[104:107]\n" \
"s_waitcnt lgkmcnt(4)\n" \
"buffer_load_dwordx4 a[80:83], v248, s[32:35], s38 offen offset:2048 sc1\n" \
"v_mfma_f32_16x16x32_bf16 v[100:103], a[8:11], v[208:211], v[100:103]\n" \
"ds_read_b128 a[0:3], v241 offset:0\n" \
"v_mfma_f32_16x16x32_bf16 v[100:103], a[12:15], v[216:219], v[100:103]\n" \
"v_mfma_f32_16x16x32_bf16 v[108:111], a[12:15], v[220:223], v[108:111]\n" \
"ds_read_b128 a[4:7], v241 offset:8192\n" \
"v_mfma_f32_16x16x32_bf16 v[108:111], a[8:11], v[212:215], v[108:111]\n" \
"s_waitcnt lgkmcnt(4)\n" \
"buffer_load_dwordx4 a[88:91], v248, s[32:35], s38 offen offset:3072 sc1\n" \
"v_mfma_f32_16x16x32_bf16 v[112:115], a[16:19], v[208:211], v[112:115]\n" \
"s_add_u32 s38, s38, 4096\n" \
"ds_read_b128 a[8:11], v241 offset:1024\n" \
"v_mfma_f32_16x16x32_bf16 v[112:115], a[20:23], v[216:219], v[112:115]\n" \
"v_mfma_f32_16x16x32_bf16 v[120:123], a[20:23], v[220:223], v[120:123]\n" \
"ds_read_b128 a[12:15], v241 offset:9216\n" \
"v_mfma_f32_16x16x32_bf16 v[120:123], a[16:19], v[212:215], v[120:123]\n" \
"s_waitcnt lgkmcnt(4)\n" \
"buffer_load_dwordx4 a[96:99], v248, s[32:35], s38 offen offset:0 sc1\n" \
"v_mfma_f32_16x16x32_bf16 v[116:119], a[24:27], v[208:211], v[116:119]\n" \
"ds_read_b128 a[16:19], v241 offset:2048\n" \
"v_mfma_f32_16x16x32_bf16 v[116:119], a[28:31], v[216:219], v[116:119]\n" \
"v_mfma_f32_16x16x32_bf16 v[124:127], a[28:31], v[220:223], v[124:127]\n" \
"s_waitcnt vmcnt(19)\n" \
"ds_read_b128 a[20:23], v241 offset:10240\n" \
"v_mfma_f32_16x16x32_bf16 v[124:127], a[24:27], v[212:215], v[124:127]\n" \
"s_waitcnt lgkmcnt(4)\n" \
"buffer_load_dwordx4 a[104:107], v248, s[32:35], s38 offen offset:1024 sc1\n" \
"v_mfma_f32_16x16x32_bf16 v[64:67], a[0:3], a[192:195], v[64:67]\n" \
"ds_read_b128 a[24:27], v241 offset:3072\n" \
"v_mfma_f32_16x16x32_bf16 v[64:67], a[4:7], a[200:203], v[64:67]\n" \
"v_mfma_f32_16x16x32_bf16 v[72:75], a[4:7], a[204:207], v[72:75]\n" \
"ds_read_b128 a[28:31], v241 offset:11264\n" \
"v_mfma_f32_16x16x32_bf16 v[72:75], a[0:3], a[196:199], v[72:75]\n" \
"s_waitcnt vmcnt(10) lgkmcnt(4)\n" \
"buffer_store_dword v253, v246, s[32:35], s39 offen sc1\n" \
"v_mfma_f32_16x16x32_bf16 v[68:71], a[8:11], a[192:195], v[68:71]\n" \
"ds_read_b128 a[0:3], v241 offset:16384\n" \
"v_mfma_f32_16x16x32_bf16 v[68:71], a[12:15], a[200:203], v[68:71]\n" \
"v_mfma_f32_16x16x32_bf16 v[76:79], a[12:15], a[204:207], v[76:79]\n" \
"ds_read_b128 a[4:7], v241 offset:24576\n" \
"v_lshrrev_b32 v214, 4, v251\n" \
"v_mfma_f32_16x16x32_bf16 v[76:79], a[8:11], a[196:199], v[76:79]\n" \
"s_waitcnt lgkmcnt(4)\n" \
"buffer_load_dwordx4 a[112:115], v248, s[32:35], s38 offen offset:2048 sc1\n" \
"v_lshlrev_b32 v214, 13, v214\n" \
"v_mfma_f32_16x16x32_bf16 v[80:83], a[16:19], a[192:195], v[80:83]\n" \
"ds_read_b128 a[8:11], v241 offset:17408\n" \
"v_and_or_b32 v214, v248, 48, v214\n" \
"v_mfma_f32_16x16x32_bf16 v[80:83], a[20:23], a[200:203], v[80:83]\n" \
"v_bfe_u32 v208, v251, 2, 2\n" \
"v_lshl_or_b32 v214, v208, 10, v214\n" \
"v_mfma_f32_16x16x32_bf16 v[88:91], a[20:23], a[204:207], v[88:91]\n" \
"ds_read_b128 a[12:15], v241 offset:25600\n" \
"v_mov_b32 v208, 0\n" \
"v_mfma_f32_16x16x32_bf16 v[88:91], a[16:19], a[196:199], v[88:91]\n" \
"s_waitcnt lgkmcnt(4)\n" \
"buffer_load_dwordx4 a[116:119], v248, s[32:35], s38 offen offset:3072 sc1\n" \
"v_mov_b32 v209, 0\n" \
"v_mfma_f32_16x16x32_bf16 v[84:87], a[24:27], a[192:195], v[84:87]\n" \
"ds_read_b128 a[16:19], v241 offset:18432\n" \
"v_mov_b32 v212, 0\n" \
"v_mfma_f32_16x16x32_bf16 v[84:87], a[28:31], a[200:203], v[84:87]\n" \
"v_mov_b32 v213, 0\n" \
"v_bfe_u32 v210, v251, 2, 2\n" \
"v_mfma_f32_16x16x32_bf16 v[92:95], a[28:31], a[204:207], v[92:95]\n" \
"ds_read_b128 a[20:23], v241 offset:26624\n" \
"v_lshrrev_b32 v211, 4, v251\n" \
"v_mfma_f32_16x16x32_bf16 v[92:95], a[24:27], a[196:199], v[92:95]\n" \
"s_waitcnt lgkmcnt(4)\n" \
"buffer_load_dwordx4 v[172:175], v248, s[32:35], 0 offen offset:0 sc1\n" \
"v_cmp_eq_u32 s[24:25], v210, v211\n" \
"v_mfma_f32_16x16x32_bf16 v[64:67], a[0:3], a[208:211], v[64:67]\n" \
"ds_read_b128 a[24:27], v241 offset:19456\n" \
"v_and_b32 v210, 1, v251\n" \
"v_mfma_f32_16x16x32_bf16 v[64:67], a[4:7], a[216:219], v[64:67]\n" \
"v_lshlrev_b32 v210, 4, v210\n" \
"v_mov_b32 v211, 0x3f80\n" \
"v_mfma_f32_16x16x32_bf16 v[72:75], a[4:7], a[220:223], v[72:75]\n" \
"ds_read_b128 a[28:31], v241 offset:27648\n" \
"v_lshlrev_b32 v210, v210, v211\n" \
"v_mfma_f32_16x16x32_bf16 v[72:75], a[0:3], a[212:215], v[72:75]\n" \
"s_waitcnt lgkmcnt(4)\n" \
"buffer_load_dwordx4 v[176:179], v248, s[32:35], 0 offen offset:1024 sc1\n" \
"v_cndmask_b32 v210, 0, v210, s[24:25]\n" \
"v_mfma_f32_16x16x32_bf16 v[68:71], a[8:11], a[208:211], v[68:71]\n" \
"ds_read_b128 a[0:3], v244 offset:0\n" \
"v_bfe_i32 v215, v251, 1, 1\n" \
"v_mfma_f32_16x16x32_bf16 v[68:71], a[12:15], a[216:219], v[68:71]\n" \
"v_and_b32 v211, v215, v210\n" \
"v_xor_b32 v210, v210, v211\n" \
"v_mfma_f32_16x16x32_bf16 v[76:79], a[12:15], a[220:223], v[76:79]\n" \
"ds_read_b128 a[4:7], v244 offset:8192\n" \
"v_mfma_f32_16x16x32_bf16 v[76:79], a[8:11], a[212:215], v[76:79]\n" \
"s_waitcnt lgkmcnt(4)\n" \
"buffer_load_dwordx4 v[180:183], v248, s[32:35], 0 offen offset:2048 sc1\n" \
"v_mfma_f32_16x16x32_bf16 v[80:83], a[16:19], a[208:211], v[80:83]\n" \
"ds_read_b128 a[8:11], v244 offset:1024\n" \
"v_mfma_f32_16x16x32_bf16 v[80:83], a[20:23], a[216:219], v[80:83]\n" \
"v_mfma_f32_16x16x32_bf16 v[88:91], a[20:23], a[220:223], v[88:91]\n" \
"ds_read_b128 a[12:15], v244 offset:9216\n" \
"v_mfma_f32_16x16x32_bf16 v[88:91], a[16:19], a[212:215], v[88:91]\n" \
"s_waitcnt lgkmcnt(4)\n" \
"buffer_load_dwordx4 v[184:187], v248, s[32:35], 0 offen offset:3072 sc1\n" \
"v_mfma_f32_16x16x32_bf16 v[84:87], a[24:27], a[208:211], v[84:87]\n" \
".Lst1_7_%=:\n" \
"s_cbranch_execz .Lst1_11_%=\n" \
"ds_read_b128 a[16:19], v244 offset:2048\n" \
"v_mfma_f32_16x16x32_bf16 v[84:87], a[28:31], a[216:219], v[84:87]\n" \
"v_mfma_f32_16x16x32_bf16 v[92:95], a[28:31], a[220:223], v[92:95]\n" \
"ds_read_b128 a[20:23], v244 offset:10240\n" \
"v_mfma_f32_16x16x32_bf16 v[92:95], a[24:27], a[212:215], v[92:95]\n" \
"s_waitcnt lgkmcnt(4)\n" \
"global_load_dwordx2 v[168:169], v188, s[96:97] offset:0\n" \
"v_mfma_f32_16x16x32_bf16 v[64:67], a[0:3], a[224:227], v[64:67]\n" \
"ds_read_b128 a[24:27], v244 offset:3072\n" \
"v_mfma_f32_16x16x32_bf16 v[64:67], a[4:7], a[232:235], v[64:67]\n" \
"v_mfma_f32_16x16x32_bf16 v[72:75], a[4:7], a[236:239], v[72:75]\n" \
"ds_read_b128 a[28:31], v244 offset:11264\n" \
"v_mfma_f32_16x16x32_bf16 v[72:75], a[0:3], a[228:231], v[72:75]\n" \
"s_waitcnt lgkmcnt(4)\n" \
"global_load_dwordx2 v[170:171], v188, s[96:97] offset:32\n" \
"v_mfma_f32_16x16x32_bf16 v[68:71], a[8:11], a[224:227], v[68:71]\n" \
"ds_read_b128 a[0:3], v249 offset:0\n" \
"v_mfma_f32_16x16x32_bf16 v[68:71], a[12:15], a[232:235], v[68:71]\n" \
"v_mfma_f32_16x16x32_bf16 v[76:79], a[12:15], a[236:239], v[76:79]\n" \
"ds_read_b128 a[4:7], v249 offset:8192\n" \
"v_mfma_f32_16x16x32_bf16 v[76:79], a[8:11], a[228:231], v[76:79]\n" \
"s_waitcnt lgkmcnt(4)\n" \
"v_mfma_f32_16x16x32_bf16 v[80:83], a[16:19], a[224:227], v[80:83]\n" \
"ds_read_b128 a[8:11], v249 offset:1024\n" \
"v_mfma_f32_16x16x32_bf16 v[80:83], a[20:23], a[232:235], v[80:83]\n" \
"v_mfma_f32_16x16x32_bf16 v[88:91], a[20:23], a[236:239], v[88:91]\n" \
"ds_read_b128 a[12:15], v249 offset:9216\n" \
"v_mfma_f32_16x16x32_bf16 v[88:91], a[16:19], a[228:231], v[88:91]\n" \
"s_waitcnt lgkmcnt(4)\n" \
"v_mfma_f32_16x16x32_bf16 v[84:87], a[24:27], a[224:227], v[84:87]\n" \
"ds_read_b128 a[16:19], v249 offset:2048\n" \
"v_mfma_f32_16x16x32_bf16 v[84:87], a[28:31], a[232:235], v[84:87]\n" \
"v_mfma_f32_16x16x32_bf16 v[92:95], a[28:31], a[236:239], v[92:95]\n" \
"s_waitcnt vmcnt(15)\n" \
"ds_read_b128 a[20:23], v249 offset:10240\n" \
"v_mfma_f32_16x16x32_bf16 v[92:95], a[24:27], a[228:231], v[92:95]\n" \
"s_waitcnt lgkmcnt(4)\n" \
"v_mfma_f32_16x16x32_bf16 v[64:67], a[0:3], a[240:243], v[64:67]\n" \
"ds_read_b128 a[24:27], v249 offset:3072\n" \
"v_mfma_f32_16x16x32_bf16 v[64:67], a[4:7], a[248:251], v[64:67]\n" \
"v_mfma_f32_16x16x32_bf16 v[72:75], a[4:7], a[252:255], v[72:75]\n" \
"ds_read_b128 a[28:31], v249 offset:11264\n" \
"v_mfma_f32_16x16x32_bf16 v[72:75], a[0:3], a[244:247], v[72:75]\n" \
"s_waitcnt lgkmcnt(4)\n" \
"v_mfma_f32_16x16x32_bf16 v[68:71], a[8:11], a[240:243], v[68:71]\n" \
"ds_read_b128 a[0:3], v239 offset:0\n" \
"v_mfma_f32_16x16x32_bf16 v[68:71], a[12:15], a[248:251], v[68:71]\n" \
"v_mfma_f32_16x16x32_bf16 v[76:79], a[12:15], a[252:255], v[76:79]\n" \
"ds_read_b128 a[4:7], v239 offset:8192\n" \
"v_mfma_f32_16x16x32_bf16 v[76:79], a[8:11], a[244:247], v[76:79]\n" \
"s_waitcnt lgkmcnt(4)\n" \
"v_mfma_f32_16x16x32_bf16 v[80:83], a[16:19], a[240:243], v[80:83]\n" \
"ds_read_b128 a[8:11], v239 offset:1024\n" \
"v_mfma_f32_16x16x32_bf16 v[80:83], a[20:23], a[248:251], v[80:83]\n" \
"v_mfma_f32_16x16x32_bf16 v[88:91], a[20:23], a[252:255], v[88:91]\n" \
"ds_read_b128 a[12:15], v239 offset:9216\n" \
"v_mfma_f32_16x16x32_bf16 v[88:91], a[16:19], a[244:247], v[88:91]\n" \
"s_waitcnt lgkmcnt(4)\n" \
"v_mfma_f32_16x16x32_bf16 v[84:87], a[24:27], a[240:243], v[84:87]\n" \
"ds_read_b128 a[16:19], v239 offset:2048\n" \
"v_mfma_f32_16x16x32_bf16 v[84:87], a[28:31], a[248:251], v[84:87]\n" \
"v_mfma_f32_16x16x32_bf16 v[92:95], a[28:31], a[252:255], v[92:95]\n" \
"s_waitcnt vmcnt(11)\n" \
"ds_read_b128 a[20:23], v239 offset:10240\n" \
"v_mfma_f32_16x16x32_bf16 v[92:95], a[24:27], a[244:247], v[92:95]\n" \
"s_waitcnt lgkmcnt(4)\n" \
"v_mfma_f32_16x16x32_bf16 v[64:67], a[0:3], a[64:67], v[64:67]\n" \
"ds_read_b128 a[24:27], v239 offset:3072\n" \
"v_mfma_f32_16x16x32_bf16 v[64:67], a[4:7], a[80:83], v[64:67]\n" \
"v_mfma_f32_16x16x32_bf16 v[72:75], a[4:7], a[88:91], v[72:75]\n" \
"ds_read_b128 a[28:31], v239 offset:11264\n" \
"v_mfma_f32_16x16x32_bf16 v[72:75], a[0:3], a[72:75], v[72:75]\n" \
"s_waitcnt lgkmcnt(4)\n" \
"v_mfma_f32_16x16x32_bf16 v[68:71], a[8:11], a[64:67], v[68:71]\n" \
"ds_read_b128 a[0:3], v239 offset:16384\n" \
"v_mfma_f32_16x16x32_bf16 v[68:71], a[12:15], a[80:83], v[68:71]\n" \
"v_mfma_f32_16x16x32_bf16 v[76:79], a[12:15], a[88:91], v[76:79]\n" \
"ds_read_b128 a[4:7], v239 offset:24576\n" \
"v_mfma_f32_16x16x32_bf16 v[76:79], a[8:11], a[72:75], v[76:79]\n" \
"s_waitcnt lgkmcnt(4)\n" \
"v_mfma_f32_16x16x32_bf16 v[80:83], a[16:19], a[64:67], v[80:83]\n" \
"ds_read_b128 a[8:11], v239 offset:17408\n" \
"v_mfma_f32_16x16x32_bf16 v[80:83], a[20:23], a[80:83], v[80:83]\n" \
"v_mfma_f32_16x16x32_bf16 v[88:91], a[20:23], a[88:91], v[88:91]\n" \
"ds_read_b128 a[12:15], v239 offset:25600\n" \
"v_mfma_f32_16x16x32_bf16 v[88:91], a[16:19], a[72:75], v[88:91]\n" \
"s_waitcnt lgkmcnt(4)\n" \
"s_lshl2_add_u32 s74, s67, %[wid]\n" \
"s_lshl_b32 s74, s74, 7\n" \
"v_mfma_f32_16x16x32_bf16 v[84:87], a[24:27], a[64:67], v[84:87]\n" \
"s_add_u32 s74, s74, 94208\n" \
"ds_read_b128 a[16:19], v239 offset:18432\n" \
"v_mov_b32 v238, 1\n" \
"v_mfma_f32_16x16x32_bf16 v[84:87], a[28:31], a[80:83], v[84:87]\n" \
"v_mfma_f32_16x16x32_bf16 v[92:95], a[28:31], a[88:91], v[92:95]\n" \
"s_waitcnt vmcnt(6)\n" \
"ds_read_b128 a[20:23], v239 offset:26624\n" \
"v_mfma_f32_16x16x32_bf16 v[92:95], a[24:27], a[72:75], v[92:95]\n" \
"s_waitcnt lgkmcnt(4)\n" \
"v_mfma_f32_16x16x32_bf16 v[64:67], a[0:3], a[96:99], v[64:67]\n" \
"ds_read_b128 a[24:27], v239 offset:19456\n" \
"v_mfma_f32_16x16x32_bf16 v[64:67], a[4:7], a[112:115], v[64:67]\n" \
"v_mfma_f32_16x16x32_bf16 v[72:75], a[4:7], a[116:119], v[72:75]\n" \
"ds_read_b128 a[28:31], v239 offset:27648\n" \
"v_mfma_f32_16x16x32_bf16 v[72:75], a[0:3], a[104:107], v[72:75]\n" \
"s_waitcnt lgkmcnt(4)\n" \
"v_mfma_f32_16x16x32_bf16 v[68:71], a[8:11], a[96:99], v[68:71]\n" \
"ds_read_b128 a[0:3], v241 offset:4096\n" \
"v_mfma_f32_16x16x32_bf16 v[68:71], a[12:15], a[112:115], v[68:71]\n" \
"v_mfma_f32_16x16x32_bf16 v[76:79], a[12:15], a[116:119], v[76:79]\n" \
"ds_read_b128 a[4:7], v241 offset:12288\n" \
"v_mfma_f32_16x16x32_bf16 v[76:79], a[8:11], a[104:107], v[76:79]\n" \
"s_waitcnt lgkmcnt(4)\n" \
"v_mfma_f32_16x16x32_bf16 v[80:83], a[16:19], a[96:99], v[80:83]\n" \
"ds_read_b128 a[8:11], v241 offset:5120\n" \
"v_mfma_f32_16x16x32_bf16 v[80:83], a[20:23], a[112:115], v[80:83]\n" \
"v_mfma_f32_16x16x32_bf16 v[88:91], a[20:23], a[116:119], v[88:91]\n" \
"ds_read_b128 a[12:15], v241 offset:13312\n" \
"v_mfma_f32_16x16x32_bf16 v[88:91], a[16:19], a[104:107], v[88:91]\n" \
"s_waitcnt lgkmcnt(4)\n" \
"v_mfma_f32_16x16x32_bf16 v[84:87], a[24:27], a[96:99], v[84:87]\n" \
"ds_read_b128 a[16:19], v241 offset:20480\n" \
"v_mfma_f32_16x16x32_bf16 v[84:87], a[28:31], a[112:115], v[84:87]\n" \
"v_cvt_pk_bf16_f32 v64, v64, v65\n" \
"v_cvt_pk_bf16_f32 v65, v66, v67\n" \
"v_mfma_f32_16x16x32_bf16 v[92:95], a[28:31], a[116:119], v[92:95]\n" \
"ds_read_b128 a[20:23], v241 offset:28672\n" \
"v_cvt_pk_bf16_f32 v66, v68, v69\n" \
"v_mfma_f32_16x16x32_bf16 v[92:95], a[24:27], a[104:107], v[92:95]\n" \
"s_waitcnt lgkmcnt(4)\n" \
"v_cvt_pk_bf16_f32 v67, v70, v71\n" \
"v_mfma_f32_16x16x32_bf16 v[96:99], a[0:3], a[192:195], v[96:99]\n" \
"ds_read_b128 a[24:27], v241 offset:21504\n" \
"v_mfma_f32_16x16x32_bf16 v[96:99], a[4:7], a[200:203], v[96:99]\n" \
"s_mov_b64 exec, 1\n" \
"buffer_atomic_add v238, off, s[32:35], s74 sc0 sc1\n" \
"s_mov_b64 exec, -1\n" \
"v_mfma_f32_16x16x32_bf16 v[104:107], a[4:7], a[204:207], v[104:107]\n" \
"ds_read_b128 a[28:31], v241 offset:29696\n" \
"v_mfma_f32_16x16x32_bf16 v[104:107], a[0:3], a[196:199], v[104:107]\n" \
"s_waitcnt lgkmcnt(4)\n" \
"buffer_load_dword v245, v255, s[32:35], s18 offen sc1\n" \
"v_mfma_f32_16x16x32_bf16 v[100:103], a[8:11], a[192:195], v[100:103]\n" \
"ds_read_b128 a[0:3], v244 offset:4096\n" \
"v_mfma_f32_16x16x32_bf16 v[100:103], a[12:15], a[200:203], v[100:103]\n" \
"buffer_store_dwordx4 v[64:67], v250, s[32:35], s36 offen offset:0 sc1\n" \
"v_mfma_f32_16x16x32_bf16 v[108:111], a[12:15], a[204:207], v[108:111]\n" \
"ds_read_b128 a[4:7], v244 offset:12288\n" \
"v_cvt_pk_bf16_f32 v72, v72, v73\n" \
"v_mfma_f32_16x16x32_bf16 v[108:111], a[8:11], a[196:199], v[108:111]\n" \
"s_waitcnt lgkmcnt(4)\n" \
"v_cvt_pk_bf16_f32 v73, v74, v75\n" \
"v_cvt_pk_bf16_f32 v74, v76, v77\n" \
"v_mfma_f32_16x16x32_bf16 v[96:99], a[16:19], a[208:211], v[96:99]\n" \
"ds_read_b128 a[8:11], v244 offset:5120\n" \
"v_cvt_pk_bf16_f32 v75, v78, v79\n" \
"v_mfma_f32_16x16x32_bf16 v[96:99], a[20:23], a[216:219], v[96:99]\n" \
"v_mfma_f32_16x16x32_bf16 v[104:107], a[20:23], a[220:223], v[104:107]\n" \
"ds_read_b128 a[12:15], v244 offset:13312\n" \
"v_mfma_f32_16x16x32_bf16 v[104:107], a[16:19], a[212:215], v[104:107]\n" \
"s_waitcnt lgkmcnt(4)\n" \
"v_mfma_f32_16x16x32_bf16 v[100:103], a[24:27], a[208:211], v[100:103]\n" \
"ds_read_b128 a[16:19], v249 offset:4096\n" \
"v_mfma_f32_16x16x32_bf16 v[100:103], a[28:31], a[216:219], v[100:103]\n" \
"buffer_store_dwordx4 v[72:75], v250, s[32:35], s21 offen offset:0 sc1\n" \
"v_mfma_f32_16x16x32_bf16 v[108:111], a[28:31], a[220:223], v[108:111]\n" \
"ds_read_b128 a[20:23], v249 offset:12288\n" \
"v_mfma_f32_16x16x32_bf16 v[108:111], a[24:27], a[212:215], v[108:111]\n" \
"s_waitcnt lgkmcnt(4)\n" \
"v_cvt_pk_bf16_f32 v80, v80, v81\n" \
"v_cvt_pk_bf16_f32 v81, v82, v83\n" \
"v_mfma_f32_16x16x32_bf16 v[96:99], a[0:3], a[224:227], v[96:99]\n" \
"ds_read_b128 a[24:27], v249 offset:5120\n" \
"v_cvt_pk_bf16_f32 v82, v84, v85\n" \
"v_mfma_f32_16x16x32_bf16 v[96:99], a[4:7], a[232:235], v[96:99]\n" \
"v_cvt_pk_bf16_f32 v83, v86, v87\n" \
"v_mfma_f32_16x16x32_bf16 v[104:107], a[4:7], a[236:239], v[104:107]\n" \
"ds_read_b128 a[28:31], v249 offset:13312\n" \
"v_mfma_f32_16x16x32_bf16 v[104:107], a[0:3], a[228:231], v[104:107]\n" \
"s_waitcnt lgkmcnt(4)\n" \
"v_mfma_f32_16x16x32_bf16 v[100:103], a[8:11], a[224:227], v[100:103]\n" \
"ds_read_b128 a[0:3], v239 offset:4096\n" \
"v_mfma_f32_16x16x32_bf16 v[100:103], a[12:15], a[232:235], v[100:103]\n" \
"v_mfma_f32_16x16x32_bf16 v[108:111], a[12:15], a[236:239], v[108:111]\n" \
"ds_read_b128 a[4:7], v239 offset:12288\n" \
"v_mfma_f32_16x16x32_bf16 v[108:111], a[8:11], a[228:231], v[108:111]\n" \
"s_waitcnt lgkmcnt(4)\n" \
"buffer_store_dwordx4 v[80:83], v250, s[32:35], s36 offen offset:1024 sc1\n" \
"v_mfma_f32_16x16x32_bf16 v[96:99], a[16:19], a[240:243], v[96:99]\n" \
"ds_read_b128 a[8:11], v239 offset:5120\n" \
"v_mfma_f32_16x16x32_bf16 v[96:99], a[20:23], a[248:251], v[96:99]\n" \
"v_cvt_pk_bf16_f32 v88, v88, v89\n" \
"v_cvt_pk_bf16_f32 v89, v90, v91\n" \
"v_mfma_f32_16x16x32_bf16 v[104:107], a[20:23], a[252:255], v[104:107]\n" \
"ds_read_b128 a[12:15], v239 offset:13312\n" \
"v_cvt_pk_bf16_f32 v90, v92, v93\n" \
"v_mfma_f32_16x16x32_bf16 v[104:107], a[16:19], a[244:247], v[104:107]\n" \
"s_waitcnt lgkmcnt(4)\n" \
"v_cvt_pk_bf16_f32 v91, v94, v95\n" \
"v_mfma_f32_16x16x32_bf16 v[100:103], a[24:27], a[240:243], v[100:103]\n" \
"ds_read_b128 a[16:19], v239 offset:20480\n" \
"v_mfma_f32_16x16x32_bf16 v[100:103], a[28:31], a[248:251], v[100:103]\n" \
"v_mfma_f32_16x16x32_bf16 v[108:111], a[28:31], a[252:255], v[108:111]\n" \
"ds_read_b128 a[20:23], v239 offset:28672\n" \
"v_mfma_f32_16x16x32_bf16 v[108:111], a[24:27], a[244:247], v[108:111]\n" \
"s_waitcnt lgkmcnt(4)\n" \
"v_mfma_f32_16x16x32_bf16 v[96:99], a[0:3], a[64:67], v[96:99]\n" \
"ds_read_b128 a[24:27], v239 offset:21504\n" \
"v_mfma_f32_16x16x32_bf16 v[96:99], a[4:7], a[80:83], v[96:99]\n" \
"buffer_store_dwordx4 v[88:91], v250, s[32:35], s21 offen offset:1024 sc1\n" \
"v_mfma_f32_16x16x32_bf16 v[104:107], a[4:7], a[88:91], v[104:107]\n" \
"ds_read_b128 a[28:31], v239 offset:29696\n" \
"v_mfma_f32_16x16x32_bf16 v[104:107], a[0:3], a[72:75], v[104:107]\n" \
"s_waitcnt lgkmcnt(4)\n" \
"v_mfma_f32_16x16x32_bf16 v[100:103], a[8:11], a[64:67], v[100:103]\n" \
"ds_read_b128 a[0:3], v241 offset:6144\n" \
"v_mfma_f32_16x16x32_bf16 v[100:103], a[12:15], a[80:83], v[100:103]\n" \
"v_mfma_f32_16x16x32_bf16 v[108:111], a[12:15], a[88:91], v[108:111]\n" \
"ds_read_b128 a[4:7], v241 offset:14336\n" \
"v_mfma_f32_16x16x32_bf16 v[108:111], a[8:11], a[72:75], v[108:111]\n" \
"s_waitcnt lgkmcnt(4)\n" \
"v_mfma_f32_16x16x32_bf16 v[96:99], a[16:19], a[96:99], v[96:99]\n" \
"ds_read_b128 a[8:11], v241 offset:7168\n" \
"v_mfma_f32_16x16x32_bf16 v[96:99], a[20:23], a[112:115], v[96:99]\n" \
"v_mfma_f32_16x16x32_bf16 v[104:107], a[20:23], a[116:119], v[104:107]\n" \
"ds_read_b128 a[12:15], v241 offset:15360\n" \
"v_mfma_f32_16x16x32_bf16 v[104:107], a[16:19], a[104:107], v[104:107]\n" \
"s_waitcnt lgkmcnt(4)\n" \
"v_mfma_f32_16x16x32_bf16 v[100:103], a[24:27], a[96:99], v[100:103]\n" \
"ds_read_b128 a[16:19], v241 offset:22528\n" \
"v_mfma_f32_16x16x32_bf16 v[100:103], a[28:31], a[112:115], v[100:103]\n" \
"v_mfma_f32_16x16x32_bf16 v[108:111], a[28:31], a[116:119], v[108:111]\n" \
"s_waitcnt vmcnt(6)\n" \
"ds_read_b128 a[20:23], v241 offset:30720\n" \
"v_lshlrev_b32 v136, 16, v172\n" \
"v_mfma_f32_16x16x32_bf16 v[108:111], a[24:27], a[104:107], v[108:111]\n" \
"s_waitcnt lgkmcnt(4)\n" \
"v_and_b32 v137, 0xffff0000, v172\n" \
"v_lshlrev_b32 v138, 16, v173\n" \
"v_mfma_f32_16x16x32_bf16 v[112:115], a[0:3], a[192:195], v[112:115]\n" \
"ds_read_b128 a[24:27], v241 offset:23552\n" \
"v_and_b32 v139, 0xffff0000, v173\n" \
"v_mfma_f32_16x16x32_bf16 v[112:115], a[4:7], a[200:203], v[112:115]\n" \
"v_lshlrev_b32 v140, 16, v174\n" \
"v_and_b32 v141, 0xffff0000, v174\n" \
"v_mfma_f32_16x16x32_bf16 v[120:123], a[4:7], a[204:207], v[120:123]\n" \
"ds_read_b128 a[28:31], v241 offset:31744\n" \
"v_lshlrev_b32 v142, 16, v175\n" \
"v_mfma_f32_16x16x32_bf16 v[120:123], a[0:3], a[196:199], v[120:123]\n" \
"s_waitcnt lgkmcnt(4)\n" \
"v_and_b32 v143, 0xffff0000, v175\n" \
"v_lshlrev_b32 v144, 16, v176\n" \
"v_mfma_f32_16x16x32_bf16 v[116:119], a[8:11], a[192:195], v[116:119]\n" \
"ds_read_b128 a[0:3], v244 offset:6144\n" \
"v_and_b32 v145, 0xffff0000, v176\n" \
"v_mfma_f32_16x16x32_bf16 v[116:119], a[12:15], a[200:203], v[116:119]\n" \
"v_cvt_pk_bf16_f32 v96, v96, v97\n" \
"v_cvt_pk_bf16_f32 v97, v98, v99\n" \
"v_mfma_f32_16x16x32_bf16 v[124:127], a[12:15], a[204:207], v[124:127]\n" \
"ds_read_b128 a[4:7], v244 offset:14336\n" \
"v_cvt_pk_bf16_f32 v98, v100, v101\n" \
"v_mfma_f32_16x16x32_bf16 v[124:127], a[8:11], a[196:199], v[124:127]\n" \
"s_waitcnt lgkmcnt(4)\n" \
"v_cvt_pk_bf16_f32 v99, v102, v103\n" \
"v_lshlrev_b32 v146, 16, v177\n" \
"v_mfma_f32_16x16x32_bf16 v[112:115], a[16:19], a[208:211], v[112:115]\n" \
"ds_read_b128 a[8:11], v244 offset:7168\n" \
"v_and_b32 v147, 0xffff0000, v177\n" \
"v_mfma_f32_16x16x32_bf16 v[112:115], a[20:23], a[216:219], v[112:115]\n" \
"v_lshlrev_b32 v148, 16, v178\n" \
"v_and_b32 v149, 0xffff0000, v178\n" \
"v_mfma_f32_16x16x32_bf16 v[120:123], a[20:23], a[220:223], v[120:123]\n" \
"ds_read_b128 a[12:15], v244 offset:15360\n" \
"v_lshlrev_b32 v150, 16, v179\n" \
"v_mfma_f32_16x16x32_bf16 v[120:123], a[16:19], a[212:215], v[120:123]\n" \
"s_waitcnt lgkmcnt(4)\n" \
"buffer_store_dwordx4 v[96:99], v250, s[32:35], s36 offen offset:2048 sc1\n" \
"v_mfma_f32_16x16x32_bf16 v[116:119], a[24:27], a[208:211], v[116:119]\n" \
"ds_read_b128 a[16:19], v249 offset:6144\n" \
"v_and_b32 v151, 0xffff0000, v179\n" \
"v_mfma_f32_16x16x32_bf16 v[116:119], a[28:31], a[216:219], v[116:119]\n" \
"v_cvt_pk_bf16_f32 v104, v104, v105\n" \
"v_cvt_pk_bf16_f32 v105, v106, v107\n" \
"v_mfma_f32_16x16x32_bf16 v[124:127], a[28:31], a[220:223], v[124:127]\n" \
"ds_read_b128 a[20:23], v249 offset:14336\n" \
"v_cvt_pk_bf16_f32 v106, v108, v109\n" \
"v_mfma_f32_16x16x32_bf16 v[124:127], a[24:27], a[212:215], v[124:127]\n" \
"s_waitcnt lgkmcnt(4)\n" \
"v_cvt_pk_bf16_f32 v107, v110, v111\n" \
"v_lshlrev_b32 v152, 16, v180\n" \
"v_mfma_f32_16x16x32_bf16 v[112:115], a[0:3], a[224:227], v[112:115]\n" \
"ds_read_b128 a[24:27], v249 offset:7168\n" \
"v_and_b32 v153, 0xffff0000, v180\n" \
"v_mfma_f32_16x16x32_bf16 v[112:115], a[4:7], a[232:235], v[112:115]\n" \
"v_lshlrev_b32 v154, 16, v181\n" \
"v_and_b32 v155, 0xffff0000, v181\n" \
"v_mfma_f32_16x16x32_bf16 v[120:123], a[4:7], a[236:239], v[120:123]\n" \
"ds_read_b128 a[28:31], v249 offset:15360\n" \
"v_lshlrev_b32 v156, 16, v182\n" \
"v_mfma_f32_16x16x32_bf16 v[120:123], a[0:3], a[228:231], v[120:123]\n" \
"s_waitcnt lgkmcnt(4)\n" \
"v_and_b32 v157, 0xffff0000, v182\n" \
"v_lshlrev_b32 v158, 16, v183\n" \
"v_mfma_f32_16x16x32_bf16 v[116:119], a[8:11], a[224:227], v[116:119]\n" \
"ds_read_b128 a[0:3], v239 offset:6144\n" \
"v_and_b32 v159, 0xffff0000, v183\n" \
"v_mfma_f32_16x16x32_bf16 v[116:119], a[12:15], a[232:235], v[116:119]\n" \
"buffer_store_dwordx4 v[104:107], v250, s[32:35], s21 offen offset:2048 sc1\n" \
"v_mfma_f32_16x16x32_bf16 v[124:127], a[12:15], a[236:239], v[124:127]\n" \
"ds_read_b128 a[4:7], v239 offset:14336\n" \
"v_lshlrev_b32 v160, 16, v184\n" \
"v_mfma_f32_16x16x32_bf16 v[124:127], a[8:11], a[228:231], v[124:127]\n" \
"s_waitcnt lgkmcnt(4)\n" \
"v_and_b32 v161, 0xffff0000, v184\n" \
"v_lshlrev_b32 v162, 16, v185\n" \
"v_mfma_f32_16x16x32_bf16 v[112:115], a[16:19], a[240:243], v[112:115]\n" \
"ds_read_b128 a[8:11], v239 offset:7168\n" \
"v_and_b32 v163, 0xffff0000, v185\n" \
"v_mfma_f32_16x16x32_bf16 v[112:115], a[20:23], a[248:251], v[112:115]\n" \
"v_lshlrev_b32 v164, 16, v186\n" \
"v_and_b32 v165, 0xffff0000, v186\n" \
"v_mfma_f32_16x16x32_bf16 v[120:123], a[20:23], a[252:255], v[120:123]\n" \
"ds_read_b128 a[12:15], v239 offset:15360\n" \
"v_lshlrev_b32 v166, 16, v187\n" \
"v_mfma_f32_16x16x32_bf16 v[120:123], a[16:19], a[244:247], v[120:123]\n" \
"s_waitcnt lgkmcnt(4)\n" \
"v_and_b32 v167, 0xffff0000, v187\n" \
"v_mfma_f32_16x16x32_bf16 v[116:119], a[24:27], a[240:243], v[116:119]\n" \
"s_waitcnt vmcnt(7)\n" \
"ds_read_b128 a[16:19], v239 offset:22528\n" \
"v_readfirstlane_b32 s81, v238\n" \
"v_mfma_f32_16x16x32_bf16 v[116:119], a[28:31], a[248:251], v[116:119]\n" \
"v_mfma_f32_16x16x32_bf16 v[124:127], a[28:31], a[252:255], v[124:127]\n" \
"s_mov_b32 s40, s81\n" \
"s_sub_u32 s86, 15, s67\n" \
"ds_read_b128 a[20:23], v239 offset:30720\n" \
"v_mfma_f32_16x16x32_bf16 v[124:127], a[24:27], a[244:247], v[124:127]\n" \
"s_waitcnt lgkmcnt(4)\n" \
"s_and_b32 s25, s40, 15\n" \
"s_lshl_b32 s25, s25, 1\n" \
"v_mfma_f32_16x16x32_bf16 v[112:115], a[0:3], a[64:67], v[112:115]\n" \
"s_cmp_lt_u32 s40, 16\n" \
"s_cselect_b32 s86, s67, s86\n" \
"ds_read_b128 a[24:27], v239 offset:23552\n" \
"v_mfma_f32_16x16x32_bf16 v[112:115], a[4:7], a[80:83], v[112:115]\n" \
"s_lshl_b32 s95, s86, 2\n" \
"s_add_u32 s95, s95, %[wid]\n" \
"s_lshl_b32 s95, s95, 3\n" \
"v_mfma_f32_16x16x32_bf16 v[120:123], a[4:7], a[88:91], v[120:123]\n" \
"s_lshr_b32 s26, s25, 4\n" \
"s_add_u32 s95, s95, s26\n" \
"ds_read_b128 a[28:31], v239 offset:31744\n" \
"v_mfma_f32_16x16x32_bf16 v[120:123], a[0:3], a[72:75], v[120:123]\n" \
"s_waitcnt lgkmcnt(4)\n" \
"s_lshl_b32 s95, s95, 6\n" \
"s_and_b32 s27, s25, 15\n" \
"v_mfma_f32_16x16x32_bf16 v[116:119], a[8:11], a[64:67], v[116:119]\n" \
"s_add_u32 s95, s95, s27\n" \
"s_lshl_b32 s95, s95, 6\n" \
"s_add_u32 s95, s95, 59768832\n" \
"v_mfma_f32_16x16x32_bf16 v[116:119], a[12:15], a[80:83], v[116:119]\n" \
"s_mul_i32 s26, s26, 7\n" \
"s_xor_b32 s26, s26, %[wid]\n" \
"s_lshl3_add_u32 s85, s86, s26\n" \
"v_mfma_f32_16x16x32_bf16 v[124:127], a[12:15], a[88:91], v[124:127]\n" \
"s_lshl4_add_u32 s85, s85, s27\n" \
"s_and_b32 s84, s40, 16\n" \
"s_lshl_b32 s84, s84, 3\n" \
"v_mfma_f32_16x16x32_bf16 v[124:127], a[8:11], a[72:75], v[124:127]\n" \
"s_waitcnt vmcnt(6) lgkmcnt(2)\n" \
"v_cmp_ne_u32 vcc, 0, v245\n" \
"v_mfma_f32_16x16x32_bf16 v[112:115], a[16:19], a[96:99], v[112:115]\n" \
"s_lshl_b32 s38, s85, 10\n" \
"buffer_load_dwordx2 v[128:129], v188, s[44:47], s38 offen offset:0\n" \
"v_mfma_f32_16x16x32_bf16 v[112:115], a[20:23], a[112:115], v[112:115]\n" \
"v_mfma_f32_16x16x32_bf16 v[120:123], a[20:23], a[116:119], v[120:123]\n" \
"s_cmp_eq_u32 s84, 0\n" \
"s_cselect_b32 s75, vcc_lo, vcc_hi\n" \
"s_lshr_b32 s76, s75, 1\n" \
"v_mfma_f32_16x16x32_bf16 v[120:123], a[16:19], a[104:107], v[120:123]\n" \
"s_waitcnt lgkmcnt(0)\n" \
"s_and_b32 s76, s76, s75\n" \
"s_lshr_b32 s77, s76, 2\n" \
"v_mfma_f32_16x16x32_bf16 v[116:119], a[24:27], a[96:99], v[116:119]\n" \
"s_and_b32 s76, s76, s77\n" \
"s_and_b32 s76, s76, 0x11111111\n" \
"buffer_load_dwordx2 v[130:131], v188, s[44:47], s38 offen offset:32\n" \
"v_mfma_f32_16x16x32_bf16 v[116:119], a[28:31], a[112:115], v[116:119]\n" \
"s_mov_b32 s74, s76\n" \
"s_add_u32 s18, s18, s84\n" \
"v_mfma_f32_16x16x32_bf16 v[124:127], a[28:31], a[116:119], v[124:127]\n" \
"s_add_u32 s22, s95, 0\n" \
"s_bitcmp1_b32 s76, 0\n" \
"s_cselect_b32 s22, s22, 1073741824\n" \
"v_mfma_f32_16x16x32_bf16 v[124:127], a[24:27], a[104:107], v[124:127]\n" \
"buffer_load_dwordx4 a[128:131], v214, s[32:35], s22 offen sc1\n" \
"v_cvt_pk_bf16_f32 v112, v112, v113\n" \
"v_cvt_pk_bf16_f32 v113, v114, v115\n" \
"v_cvt_pk_bf16_f32 v114, v116, v117\n" \
"v_cvt_pk_bf16_f32 v115, v118, v119\n" \
"buffer_store_dwordx4 v[112:115], v250, s[32:35], s36 offen offset:3072 sc1\n" \
"v_cvt_pk_bf16_f32 v120, v120, v121\n" \
"v_cvt_pk_bf16_f32 v121, v122, v123\n" \
"v_cvt_pk_bf16_f32 v122, v124, v125\n" \
"v_cvt_pk_bf16_f32 v123, v126, v127\n" \
"buffer_store_dwordx4 v[120:123], v250, s[32:35], s21 offen offset:3072 sc1\n" \
"s_waitcnt vmcnt(0)\n" \
"buffer_store_dword v253, v246, s[32:35], s39 offen offset:128 sc1\n" \
"buffer_load_dwordx4 a[160:163], v214, s[32:35], s22 offen offset:64 sc1\n" \
"buffer_load_dwordx2 v[132:133], v188, s[44:47], s38 offen offset:1024\n" \
"s_add_u32 s22, s95, 2097152\n" \
"buffer_load_dwordx2 v[134:135], v188, s[44:47], s38 offen offset:1056\n" \
"s_bitcmp1_b32 s76, 4\n" \
"s_cselect_b32 s22, s22, 1073741824\n" \
"buffer_load_dwordx4 a[132:135], v214, s[32:35], s22 offen sc1\n" \
"buffer_load_dwordx4 a[164:167], v214, s[32:35], s22 offen offset:64 sc1\n" \
"s_add_u32 s22, s95, 4194304\n" \
"s_bitcmp1_b32 s76, 8\n" \
"s_cselect_b32 s22, s22, 1073741824\n" \
"buffer_load_dwordx4 a[136:139], v214, s[32:35], s22 offen sc1\n" \
"buffer_load_dwordx4 a[168:171], v214, s[32:35], s22 offen offset:64 sc1\n" \
"s_add_u32 s22, s95, 6291456\n" \
"s_bitcmp1_b32 s76, 12\n" \
"s_cselect_b32 s22, s22, 1073741824\n" \
"buffer_load_dwordx4 a[140:143], v214, s[32:35], s22 offen sc1\n" \
"buffer_load_dwordx4 a[172:175], v214, s[32:35], s22 offen offset:64 sc1\n" \
"s_add_u32 s22, s95, 8388608\n" \
"s_bitcmp1_b32 s76, 16\n" \
"s_cselect_b32 s22, s22, 1073741824\n" \
"buffer_load_dwordx4 a[144:147], v214, s[32:35], s22 offen sc1\n" \
"buffer_load_dwordx4 a[176:179], v214, s[32:35], s22 offen offset:64 sc1\n" \
"s_add_u32 s22, s95, 10485760\n" \
"s_bitcmp1_b32 s76, 20\n" \
"s_cselect_b32 s22, s22, 1073741824\n" \
"buffer_load_dwordx4 a[148:151], v214, s[32:35], s22 offen sc1\n" \
"buffer_load_dwordx4 a[180:183], v214, s[32:35], s22 offen offset:64 sc1\n" \
"s_add_u32 s22, s95, 12582912\n" \
"s_bitcmp1_b32 s76, 24\n" \
"s_cselect_b32 s22, s22, 1073741824\n" \
"buffer_load_dwordx4 a[152:155], v214, s[32:35], s22 offen sc1\n" \
"buffer_load_dwordx4 a[184:187], v214, s[32:35], s22 offen offset:64 sc1\n" \
"s_add_u32 s22, s95, 14680064\n" \
"s_bitcmp1_b32 s76, 28\n" \
"s_cselect_b32 s22, s22, 1073741824\n" \
"buffer_load_dwordx4 a[156:159], v214, s[32:35], s22 offen sc1\n" \
"buffer_load_dwordx4 a[188:191], v214, s[32:35], s22 offen offset:64 sc1\n" \
"v_lshlrev_b32 v236, 2, v191\n" \
"v_lshrrev_b32 v100, 4, v191\n" \
"v_bfe_u32 v101, v191, 1, 2\n" \
"v_lshlrev_b32 v101, 4, v101\n" \
"v_and_or_b32 v101, v191, 8, v101\n" \
"v_and_or_b32 v101, v236, 4, v101\n" \
"v_and_b32 v102, 3, v100\n" \
"v_lshl_or_b32 v239, v102, 10, v101\n" \
"v_lshrrev_b32 v102, 2, v100\n" \
"v_lshl_or_b32 v239, v102, 13, v239\n" \
"v_lshl_or_b32 v101, v100, 6, v101\n" \
"buffer_load_dword v172, v101, s[32:35], 0 offen offset:0 sc1\n" \
"buffer_load_dword v174, v101, s[32:35], 0 offen offset:1024 sc1\n" \
"buffer_load_dword v176, v101, s[32:35], 0 offen offset:2048 sc1\n" \
"buffer_load_dword v178, v101, s[32:35], 0 offen offset:3072 sc1\n" \
  \
".Ljoin_%=:\n" \
"s_nop 1\n" \
"buffer_load_dword v245, v252, s[32:35], s18 offen sc1\n" \
"s_xor_b32 s94, s83, 0xc000\n" \
"s_lshl3_add_u32 s24, %[wid], s94\n" \
"v_mov_b32 v232, s24\n" \
"v_mov_b32 v233, s94\n" \
"v_lshlrev_b32 v234, 11, v251\n" \
"v_cmp_gt_u32 vcc, 8, v251\n" \
"v_mov_b32 v235, 1073741824\n" \
"s_lshl2_add_u32 s38, s64, %[wid]\n" \
"v_cndmask_b32 v234, v235, v234, vcc\n" \
"v_lshlrev_b32 v235, 3, v251\n" \
"s_lshl_b32 s38, s38, 9\n" \
"s_add_u32 s38, s38, 557056\n" \
"s_lshl_b64 s[42:43], 1, %[bid]\n" \
"s_lshr_b32 s24, %[bid], 6\n" \
"s_cmp_eq_u32 s24, %[wid]\n" \
"s_cselect_b64 s[42:43], s[42:43], 0\n" \
"s_call_b64 s[98:99], .Lch3_%=\n" \
"s_mov_b32 s82, 0x100000\n" \
".Lg0_%=:\n" \
"s_waitcnt vmcnt(0)\n" \
"s_cmp_eq_u32 s74, 0x11111111\n" \
"s_cbranch_scc0 .Lga_%=\n" \
"v_mfma_f32_16x16x32_bf16 v[92:95], v[210:213], a[128:131], 0\n" \
"v_lshlrev_b32 v48, 16, v128\n" \
"v_mfma_f32_16x16x32_bf16 v[96:99], v[208:211], a[128:131], 0\n" \
"v_and_b32 v49, 0xffff0000, v128\n" \
"v_mfma_f32_16x16x32_bf16 v[92:95], v[210:213], a[132:135], v[92:95]\n" \
"v_lshlrev_b32 v50, 16, v129\n" \
"v_mfma_f32_16x16x32_bf16 v[96:99], v[208:211], a[132:135], v[96:99]\n" \
"v_and_b32 v51, 0xffff0000, v129\n" \
"v_mfma_f32_16x16x32_bf16 v[92:95], v[210:213], a[136:139], v[92:95]\n" \
"v_lshlrev_b32 v52, 16, v130\n" \
"v_mfma_f32_16x16x32_bf16 v[96:99], v[208:211], a[136:139], v[96:99]\n" \
"v_and_b32 v53, 0xffff0000, v130\n" \
"v_mfma_f32_16x16x32_bf16 v[92:95], v[210:213], a[140:143], v[92:95]\n" \
"v_lshlrev_b32 v54, 16, v131\n" \
"v_mfma_f32_16x16x32_bf16 v[96:99], v[208:211], a[140:143], v[96:99]\n" \
"v_and_b32 v55, 0xffff0000, v131\n" \
"v_mfma_f32_16x16x32_bf16 v[92:95], v[210:213], a[144:147], v[92:95]\n" \
"v_lshlrev_b32 v56, 16, v132\n" \
"v_mfma_f32_16x16x32_bf16 v[96:99], v[208:211], a[144:147], v[96:99]\n" \
"v_and_b32 v57, 0xffff0000, v132\n" \
"v_mfma_f32_16x16x32_bf16 v[92:95], v[210:213], a[148:151], v[92:95]\n" \
"v_lshlrev_b32 v58, 16, v133\n" \
"v_mfma_f32_16x16x32_bf16 v[96:99], v[208:211], a[148:151], v[96:99]\n" \
"v_and_b32 v59, 0xffff0000, v133\n" \
"v_mfma_f32_16x16x32_bf16 v[92:95], v[210:213], a[152:155], v[92:95]\n" \
"v_lshlrev_b32 v60, 16, v134\n" \
"v_mfma_f32_16x16x32_bf16 v[96:99], v[208:211], a[152:155], v[96:99]\n" \
"v_and_b32 v61, 0xffff0000, v134\n" \
"v_mfma_f32_16x16x32_bf16 v[92:95], v[210:213], a[156:159], v[92:95]\n" \
"v_lshlrev_b32 v62, 16, v135\n" \
"v_mfma_f32_16x16x32_bf16 v[96:99], v[208:211], a[156:159], v[96:99]\n" \
"v_and_b32 v63, 0xffff0000, v135\n" \
"s_nop 4\n" \
"v_pk_fma_f32 v[92:93], v[92:93], v[136:137], v[48:49]\n" \
"v_mfma_f32_16x16x32_bf16 v[64:67], v[210:213], a[160:163], 0\n" \
"v_pk_fma_f32 v[94:95], v[94:95], v[138:139], v[50:51]\n" \
"v_mfma_f32_16x16x32_bf16 v[68:71], v[208:211], a[160:163], 0\n" \
"v_pk_fma_f32 v[96:97], v[96:97], v[140:141], v[52:53]\n" \
"v_mfma_f32_16x16x32_bf16 v[64:67], v[210:213], a[164:167], v[64:67]\n" \
"v_pk_fma_f32 v[98:99], v[98:99], v[142:143], v[54:55]\n" \
"v_mfma_f32_16x16x32_bf16 v[68:71], v[208:211], a[164:167], v[68:71]\n" \
"v_cvt_pk_bf16_f32 v100, v92, v93\n" \
"v_mfma_f32_16x16x32_bf16 v[64:67], v[210:213], a[168:171], v[64:67]\n" \
"v_cvt_pk_bf16_f32 v101, v94, v95\n" \
"v_mfma_f32_16x16x32_bf16 v[68:71], v[208:211], a[168:171], v[68:71]\n" \
"v_cvt_pk_bf16_f32 v102, v96, v97\n" \
"v_mfma_f32_16x16x32_bf16 v[64:67], v[210:213], a[172:175], v[64:67]\n" \
"v_cvt_pk_bf16_f32 v103, v98, v99\n" \
"v_mfma_f32_16x16x32_bf16 v[68:71], v[208:211], a[172:175], v[68:71]\n" \
"v_lshlrev_b32 v92, 16, v100\n" \
"v_mfma_f32_16x16x32_bf16 v[64:67], v[210:213], a[176:179], v[64:67]\n" \
"v_and_b32 v93, 0xffff0000, v100\n" \
"v_mfma_f32_16x16x32_bf16 v[68:71], v[208:211], a[176:179], v[68:71]\n" \
"v_lshlrev_b32 v94, 16, v101\n" \
"v_mfma_f32_16x16x32_bf16 v[64:67], v[210:213], a[180:183], v[64:67]\n" \
"v_readfirstlane_b32 s78, v92\n" \
"v_mfma_f32_16x16x32_bf16 v[68:71], v[208:211], a[180:183], v[68:71]\n" \
"v_and_b32 v95, 0xffff0000, v101\n" \
"v_mfma_f32_16x16x32_bf16 v[64:67], v[210:213], a[184:187], v[64:67]\n" \
"v_lshlrev_b32 v96, 16, v102\n" \
"v_mfma_f32_16x16x32_bf16 v[68:71], v[208:211], a[184:187], v[68:71]\n" \
"s_mov_b32 s79, s78\n" \
"v_mfma_f32_16x16x32_bf16 v[64:67], v[210:213], a[188:191], v[64:67]\n" \
"v_and_b32 v97, 0xffff0000, v102\n" \
"v_mfma_f32_16x16x32_bf16 v[68:71], v[208:211], a[188:191], v[68:71]\n" \
"v_lshlrev_b32 v98, 16, v103\n" \
"s_nop 2\n" \
"v_and_b32 v99, 0xffff0000, v103\n" \
"s_nop 2\n" \
"v_pk_add_f32 v[104:105], v[92:93], s[78:79] neg_lo:[0,1] neg_hi:[0,1]\n" \
"v_pk_fma_f32 v[64:65], v[64:65], v[136:137], v[56:57]\n" \
"v_pk_add_f32 v[106:107], v[94:95], s[78:79] neg_lo:[0,1] neg_hi:[0,1]\n" \
"v_pk_fma_f32 v[66:67], v[66:67], v[138:139], v[58:59]\n" \
"v_pk_add_f32 v[108:109], v[96:97], s[78:79] neg_lo:[0,1] neg_hi:[0,1]\n" \
"v_pk_fma_f32 v[68:69], v[68:69], v[140:141], v[60:61]\n" \
"v_pk_add_f32 v[110:111], v[98:99], s[78:79] neg_lo:[0,1] neg_hi:[0,1]\n" \
"v_pk_fma_f32 v[70:71], v[70:71], v[142:143], v[62:63]\n" \
"v_pk_add_f32 v[112:113], v[104:105], v[106:107]\n" \
"v_cvt_pk_bf16_f32 v72, v64, v65\n" \
"v_pk_mul_f32 v[114:115], v[104:105], v[104:105]\n" \
"v_cvt_pk_bf16_f32 v73, v66, v67\n" \
"v_pk_add_f32 v[116:117], v[108:109], v[110:111]\n" \
"v_cvt_pk_bf16_f32 v74, v68, v69\n" \
"v_pk_mul_f32 v[118:119], v[108:109], v[108:109]\n" \
"v_cvt_pk_bf16_f32 v75, v70, v71\n" \
"v_pk_fma_f32 v[114:115], v[106:107], v[106:107], v[114:115]\n" \
"v_lshlrev_b32 v64, 16, v72\n" \
"v_pk_fma_f32 v[118:119], v[110:111], v[110:111], v[118:119]\n" \
"v_and_b32 v65, 0xffff0000, v72\n" \
"v_pk_add_f32 v[112:113], v[112:113], v[116:117]\n" \
"v_lshlrev_b32 v66, 16, v73\n" \
"v_pk_add_f32 v[114:115], v[114:115], v[118:119]\n" \
"v_readfirstlane_b32 s24, v64\n" \
"v_add_f32 v112, v112, v113\n" \
"v_and_b32 v67, 0xffff0000, v73\n" \
"v_add_f32 v114, v114, v115\n" \
"v_lshlrev_b32 v68, 16, v74\n" \
"s_nop 0\n" \
"v_permlane32_swap_b32 v112, v114\n" \
"s_mov_b32 s25, s24\n" \
"v_add_f32 v112, v112, v114\n" \
"v_and_b32 v69, 0xffff0000, v74\n" \
"s_nop 0\n" \
"v_add_f32_dpp v112, v112, v112 quad_perm:[1,0,3,2] row_mask:0xf bank_mask:0xf\n" \
"v_lshlrev_b32 v70, 16, v75\n" \
"s_nop 0\n" \
"v_add_f32_dpp v112, v112, v112 quad_perm:[2,3,0,1] row_mask:0xf bank_mask:0xf\n" \
"v_and_b32 v71, 0xffff0000, v75\n" \
"s_nop 0\n" \
"v_add_f32_dpp v112, v112, v112 row_half_mirror row_mask:0xf bank_mask:0xf\n" \
"v_pk_add_f32 v[76:77], v[64:65], s[24:25] neg_lo:[0,1] neg_hi:[0,1]\n" \
"s_nop 0\n" \
"v_add_f32_dpp v112, v112, v112 row_mirror row_mask:0xf bank_mask:0xf\n" \
"v_pk_add_f32 v[78:79], v[66:67], s[24:25] neg_lo:[0,1] neg_hi:[0,1]\n" \
"s_nop 0\n" \
"v_add_f32_dpp v112, v112, v112 row_bcast:15 row_mask:0xa bank_mask:0xf\n" \
"v_pk_add_f32 v[80:81], v[68:69], s[24:25] neg_lo:[0,1] neg_hi:[0,1]\n" \
"v_mul_f32 v112, 0x3b000000, v112\n" \
"v_pk_add_f32 v[82:83], v[70:71], s[24:25] neg_lo:[0,1] neg_hi:[0,1]\n" \
".Lst3_0_%=:\n" \
"s_cbranch_execz .Lst3_4_%=\n" \
"v_readlane_b32 s80, v112, 31\n" \
"v_pk_add_f32 v[84:85], v[76:77], v[78:79]\n" \
"v_readlane_b32 s81, v112, 63\n" \
"v_pk_mul_f32 v[86:87], v[76:77], v[76:77]\n" \
"v_mov_b32 v113, s78\n" \
"v_pk_add_f32 v[88:89], v[80:81], v[82:83]\n" \
"v_mov_b32 v114, s81\n" \
"v_pk_mul_f32 v[90:91], v[80:81], v[80:81]\n" \
"v_add_f32 v113, s80, v113\n" \
"v_pk_fma_f32 v[86:87], v[78:79], v[78:79], v[86:87]\n" \
"v_fma_f32 v114, -s80, s80, v114\n" \
"v_pk_fma_f32 v[90:91], v[82:83], v[82:83], v[90:91]\n" \
"v_max_f32 v115, 0, v114\n" \
"v_pk_add_f32 v[84:85], v[84:85], v[88:89]\n" \
"v_cmp_u_f32 s[22:23], v114, v114\n" \
"v_pk_add_f32 v[86:87], v[86:87], v[90:91]\n" \
"s_nop 0\n" \
"v_cndmask_b32 v114, v115, v114, s[22:23]\n" \
"v_add_f32 v84, v84, v85\n" \
"v_cvt_pk_bf16_f32 v116, v114, v113\n" \
"v_add_f32 v86, v86, v87\n" \
"v_lshlrev_b32 v114, 16, v116\n" \
"s_nop 0\n" \
"v_permlane32_swap_b32 v84, v86\n" \
"v_and_b32 v112, 0xffff0000, v116\n" \
"v_add_f32 v84, v84, v86\n" \
"v_add_f32 v114, 0x3727c5ac, v114\n" \
"s_nop 0\n" \
"v_add_f32_dpp v84, v84, v84 quad_perm:[1,0,3,2] row_mask:0xf bank_mask:0xf\n" \
"v_cvt_pk_bf16_f32 v114, v114, v114\n" \
"s_nop 0\n" \
"v_add_f32_dpp v84, v84, v84 quad_perm:[2,3,0,1] row_mask:0xf bank_mask:0xf\n" \
"v_pk_add_f32 v[104:105], v[92:93], v[112:113] op_sel_hi:[1,0] neg_lo:[0,1] neg_hi:[0,1]\n" \
"s_nop 0\n" \
"v_add_f32_dpp v84, v84, v84 row_half_mirror row_mask:0xf bank_mask:0xf\n" \
"v_lshlrev_b32 v114, 16, v114\n" \
"s_nop 0\n" \
"v_add_f32_dpp v84, v84, v84 row_mirror row_mask:0xf bank_mask:0xf\n" \
"v_pk_add_f32 v[106:107], v[94:95], v[112:113] op_sel_hi:[1,0] neg_lo:[0,1] neg_hi:[0,1]\n" \
"s_nop 0\n" \
"v_add_f32_dpp v84, v84, v84 row_bcast:15 row_mask:0xa bank_mask:0xf\n" \
"v_sqrt_f32 v114, v114\n" \
"v_mul_f32 v84, 0x3b000000, v84\n" \
"v_pk_add_f32 v[108:109], v[96:97], v[112:113] op_sel_hi:[1,0] neg_lo:[0,1] neg_hi:[0,1]\n" \
"v_readlane_b32 s26, v84, 31\n" \
"v_pk_add_f32 v[110:111], v[98:99], v[112:113] op_sel_hi:[1,0] neg_lo:[0,1] neg_hi:[0,1]\n" \
"v_readlane_b32 s27, v84, 63\n" \
"v_cvt_pk_bf16_f32 v116, v114, v112\n" \
"v_mov_b32 v85, s24\n" \
"v_pk_mul_f32 v[104:105], v[104:105], v[152:153]\n" \
"v_mov_b32 v86, s27\n" \
"v_lshlrev_b32 v114, 16, v116\n" \
"v_add_f32 v85, s26, v85\n" \
"v_pk_mul_f32 v[106:107], v[106:107], v[154:155]\n" \
"v_fma_f32 v86, -s26, s26, v86\n" \
"v_rcp_f32 v114, v114\n" \
"v_max_f32 v87, 0, v86\n" \
"v_pk_mul_f32 v[108:109], v[108:109], v[156:157]\n" \
"v_cmp_u_f32 s[20:21], v86, v86\n" \
"v_pk_mul_f32 v[110:111], v[110:111], v[158:159]\n" \
"s_nop 0\n" \
"v_cndmask_b32 v86, v87, v86, s[20:21]\n" \
"v_readfirstlane_b32 s72, v116\n" \
"v_cvt_pk_bf16_f32 v88, v86, v85\n" \
"v_pk_fma_f32 v[104:105], v[104:105], v[114:115], v[144:145] op_sel_hi:[1,0,1]\n" \
"v_lshlrev_b32 v86, 16, v88\n" \
"v_pk_fma_f32 v[106:107], v[106:107], v[114:115], v[146:147] op_sel_hi:[1,0,1]\n" \
"v_and_b32 v84, 0xffff0000, v88\n" \
"v_pk_fma_f32 v[108:109], v[108:109], v[114:115], v[148:149] op_sel_hi:[1,0,1]\n" \
"v_add_f32 v86, 0x3727c5ac, v86\n" \
"v_pk_fma_f32 v[110:111], v[110:111], v[114:115], v[150:151] op_sel_hi:[1,0,1]\n" \
"v_cvt_pk_bf16_f32 v86, v86, v86\n" \
"v_cvt_pk_bf16_f32 v220, v104, v105\n" \
"v_pk_add_f32 v[76:77], v[64:65], v[84:85] op_sel_hi:[1,0] neg_lo:[0,1] neg_hi:[0,1]\n" \
"v_cvt_pk_bf16_f32 v221, v106, v107\n" \
"v_lshlrev_b32 v86, 16, v86\n" \
"v_cvt_pk_bf16_f32 v222, v108, v109\n" \
"v_pk_add_f32 v[78:79], v[66:67], v[84:85] op_sel_hi:[1,0] neg_lo:[0,1] neg_hi:[0,1]\n" \
"v_cvt_pk_bf16_f32 v223, v110, v111\n" \
"v_sqrt_f32 v86, v86\n" \
"v_dot2_f32_bf16 v112, v220, v168, 0\n" \
"v_pk_add_f32 v[80:81], v[68:69], v[84:85] op_sel_hi:[1,0] neg_lo:[0,1] neg_hi:[0,1]\n" \
"v_dot2_f32_bf16 v113, v221, v169, 0\n" \
"v_pk_add_f32 v[82:83], v[70:71], v[84:85] op_sel_hi:[1,0] neg_lo:[0,1] neg_hi:[0,1]\n" \
"v_dot2_f32_bf16 v112, v222, v170, v112\n" \
"v_cvt_pk_bf16_f32 v88, v86, v84\n" \
"v_dot2_f32_bf16 v113, v223, v171, v113\n" \
"v_pk_mul_f32 v[76:77], v[76:77], v[152:153]\n" \
"s_nop 2\n" \
"v_lshlrev_b32 v86, 16, v88\n" \
"v_add_f32 v112, v112, v113\n" \
"v_pk_mul_f32 v[78:79], v[78:79], v[154:155]\n" \
"s_nop 0\n" \
"v_add_f32_dpp v112, v112, v112 quad_perm:[1,0,3,2] row_mask:0xf bank_mask:0xf\n" \
"v_rcp_f32 v86, v86\n" \
"s_nop 0\n" \
"v_add_f32_dpp v112, v112, v112 quad_perm:[2,3,0,1] row_mask:0xf bank_mask:0xf\n" \
"v_pk_mul_f32 v[80:81], v[80:81], v[156:157]\n" \
"s_nop 0\n" \
"v_add_f32_dpp v112, v112, v112 row_half_mirror row_mask:0xf bank_mask:0xf\n" \
"v_pk_mul_f32 v[82:83], v[82:83], v[158:159]\n" \
"s_nop 0\n" \
"v_add_f32_dpp v112, v112, v112 row_mirror row_mask:0xf bank_mask:0xf\n" \
"v_readfirstlane_b32 s73, v88\n" \
"s_nop 0\n" \
"v_add_f32_dpp v112, v112, v112 row_bcast:15 row_mask:0xa bank_mask:0xf\n" \
"v_pk_fma_f32 v[76:77], v[76:77], v[86:87], v[144:145] op_sel_hi:[1,0,1]\n" \
"s_nop 0\n" \
"v_add_f32_dpp v112, v112, v112 row_bcast:31 row_mask:0xc bank_mask:0xf\n" \
"v_pk_fma_f32 v[78:79], v[78:79], v[86:87], v[146:147] op_sel_hi:[1,0,1]\n" \
"v_cvt_pk_bf16_f32 v112, v112, v112\n" \
"v_pk_fma_f32 v[80:81], v[80:81], v[86:87], v[148:149] op_sel_hi:[1,0,1]\n" \
"s_sub_u32 s79, 2047, s85\n" \
"v_pk_fma_f32 v[82:83], v[82:83], v[86:87], v[150:151] op_sel_hi:[1,0,1]\n" \
"s_lshl_b32 s79, s79, 5\n" \
"v_cvt_pk_bf16_f32 v216, v76, v77\n" \
"s_or_b32 s79, s79, 3\n" \
"v_cvt_pk_bf16_f32 v217, v78, v79\n" \
"v_readlane_b32 s78, v112, 63\n" \
"v_cvt_pk_bf16_f32 v218, v80, v81\n" \
"s_and_b32 s78, s78, 0xffff\n" \
"v_cvt_pk_bf16_f32 v219, v82, v83\n" \
"s_or_b32 s80, s78, 0x8000\n" \
"v_dot2_f32_bf16 v84, v216, v168, 0\n" \
"s_andn2_b32 s81, 0xffff, s78\n" \
"v_dot2_f32_bf16 v85, v217, v169, 0\n" \
"s_bitcmp1_b32 s78, 15\n" \
"s_cselect_b32 s80, s81, s80\n" \
"v_dot2_f32_bf16 v84, v218, v170, v84\n" \
"s_and_b32 s78, s78, 0x7fff\n" \
"v_dot2_f32_bf16 v85, v219, v171, v85\n" \
"s_cmp_gt_u32 s78, 0x7f80\n" \
"s_cselect_b32 s80, 0, s80\n" \
"s_nop 2\n" \
"s_pack_ll_b32_b16 s87, s79, s80\n" \
"v_add_f32 v84, v84, v85\n" \
"s_nop 1\n" \
"v_add_f32_dpp v84, v84, v84 quad_perm:[1,0,3,2] row_mask:0xf bank_mask:0xf\n" \
"s_nop 1\n" \
"v_add_f32_dpp v84, v84, v84 quad_perm:[2,3,0,1] row_mask:0xf bank_mask:0xf\n" \
"s_nop 1\n" \
"v_add_f32_dpp v84, v84, v84 row_half_mirror row_mask:0xf bank_mask:0xf\n" \
"s_nop 1\n" \
"v_add_f32_dpp v84, v84, v84 row_mirror row_mask:0xf bank_mask:0xf\n" \
"s_nop 1\n" \
"v_add_f32_dpp v84, v84, v84 row_bcast:15 row_mask:0xa bank_mask:0xf\n" \
"s_nop 1\n" \
"v_add_f32_dpp v84, v84, v84 row_bcast:31 row_mask:0xc bank_mask:0xf\n" \
"v_cvt_pk_bf16_f32 v84, v84, v84\n" \
"s_sub_u32 s25, 2046, s85\n" \
"s_lshl_b32 s25, s25, 5\n" \
"s_or_b32 s25, s25, 3\n" \
"v_readlane_b32 s24, v84, 63\n" \
"s_and_b32 s24, s24, 0xffff\n" \
"s_or_b32 s26, s24, 0x8000\n" \
"s_andn2_b32 s27, 0xffff, s24\n" \
"s_bitcmp1_b32 s24, 15\n" \
"s_cselect_b32 s26, s27, s26\n" \
"s_and_b32 s24, s24, 0x7fff\n" \
"s_cmp_gt_u32 s24, 0x7f80\n" \
"s_cselect_b32 s26, 0, s26\n" \
"s_pack_ll_b32_b16 s88, s25, s26\n" \
"s_cmp_gt_u32 s87, s88\n" \
"s_cselect_b32 s25, s87, s88\n" \
"s_cselect_b32 s24, s72, s73\n" \
  \
"v_mov_b32 v85, s25\n" \
"v_mov_b32 v84, s24\n" \
"ds_write_b64 v232, v[84:85]\n" \
"s_waitcnt lgkmcnt(0)\n" \
"s_barrier\n" \
"ds_read_b128 v[84:87], v233\n" \
"ds_read_b128 v[88:91], v233 offset:16\n" \
"s_lshl_b32 s26, %[bid], 3\n" \
"s_add_u32 s26, s26, 557056\n" \
"s_cmp_eq_u32 %[wid], 0\n" \
"s_cselect_b32 s26, s26, 1073741824\n" \
"s_waitcnt lgkmcnt(0)\n" \
"v_cmp_gt_u32 vcc, v85, v87\n" \
"v_max_u32 v85, v85, v87\n" \
"s_nop 0\n" \
"v_cndmask_b32 v84, v86, v84, vcc\n" \
"v_cmp_gt_u32 vcc, v89, v91\n" \
"v_max_u32 v89, v89, v91\n" \
"s_nop 0\n" \
"v_cndmask_b32 v88, v90, v88, vcc\n" \
"v_cmp_gt_u32 vcc, v85, v89\n" \
"v_max_u32 v85, v85, v89\n" \
"s_nop 0\n" \
"v_cndmask_b32 v84, v88, v84, vcc\n" \
"buffer_store_dwordx2 v[84:85], v234, s[32:35], s26 offen sc1\n" \
"buffer_load_dwordx2 v[86:87], v235, s[32:35], s38 offen sc1\n" \
"v_readfirstlane_b32 s92, v85\n" \
"v_lshlrev_b32 v104, 16, v220\n" \
"v_lshlrev_b32 v76, 16, v216\n" \
"v_and_b32 v105, 0xffff0000, v220\n" \
"v_and_b32 v77, 0xffff0000, v216\n" \
"v_lshlrev_b32 v106, 16, v221\n" \
"v_lshlrev_b32 v78, 16, v217\n" \
"v_and_b32 v107, 0xffff0000, v221\n" \
"v_and_b32 v79, 0xffff0000, v217\n" \
"v_lshlrev_b32 v108, 16, v222\n" \
"v_lshlrev_b32 v80, 16, v218\n" \
"v_and_b32 v109, 0xffff0000, v222\n" \
"v_and_b32 v81, 0xffff0000, v218\n" \
"v_lshlrev_b32 v110, 16, v223\n" \
"v_lshlrev_b32 v82, 16, v219\n" \
"v_and_b32 v111, 0xffff0000, v223\n" \
"v_and_b32 v83, 0xffff0000, v219\n" \
"v_pk_fma_f32 v[104:105], v[104:105], v[160:161], v[92:93]\n" \
"v_pk_fma_f32 v[76:77], v[76:77], v[160:161], v[64:65]\n" \
"v_pk_fma_f32 v[106:107], v[106:107], v[162:163], v[94:95]\n" \
"v_pk_fma_f32 v[78:79], v[78:79], v[162:163], v[66:67]\n" \
"v_pk_fma_f32 v[108:109], v[108:109], v[164:165], v[96:97]\n" \
"v_pk_fma_f32 v[80:81], v[80:81], v[164:165], v[68:69]\n" \
"v_pk_fma_f32 v[110:111], v[110:111], v[166:167], v[98:99]\n" \
"v_pk_fma_f32 v[82:83], v[82:83], v[166:167], v[70:71]\n" \
"v_cvt_pk_bf16_f32 v100, v104, v105\n" \
"v_cvt_pk_bf16_f32 v72, v76, v77\n" \
"v_cvt_pk_bf16_f32 v101, v106, v107\n" \
"v_cvt_pk_bf16_f32 v73, v78, v79\n" \
"v_cvt_pk_bf16_f32 v102, v108, v109\n" \
"v_cvt_pk_bf16_f32 v74, v80, v81\n" \
"v_cvt_pk_bf16_f32 v103, v110, v111\n" \
"v_cvt_pk_bf16_f32 v75, v82, v83\n" \
"s_mov_b32 vcc_lo, 0xcccccccc\n" \
"s_mov_b32 vcc_lo, 0xcccccccc\n" \
"s_mov_b32 vcc_hi, 0xcccccccc\n" \
"s_mov_b32 vcc_hi, 0xcccccccc\n" \
"v_mov_b32_dpp v104, v102 quad_perm:[0,2,0,2] row_mask:0xf bank_mask:0xf\n" \
"v_mov_b32_dpp v76, v74 quad_perm:[0,2,0,2] row_mask:0xf bank_mask:0xf\n" \
"v_mov_b32_dpp v105, v103 quad_perm:[0,2,0,2] row_mask:0xf bank_mask:0xf\n" \
"v_mov_b32_dpp v77, v75 quad_perm:[0,2,0,2] row_mask:0xf bank_mask:0xf\n" \
"v_mov_b32_dpp v106, v102 quad_perm:[1,3,1,3] row_mask:0xf bank_mask:0xf\n" \
"v_mov_b32_dpp v78, v74 quad_perm:[1,3,1,3] row_mask:0xf bank_mask:0xf\n" \
"v_mov_b32_dpp v107, v103 quad_perm:[1,3,1,3] row_mask:0xf bank_mask:0xf\n" \
"v_mov_b32_dpp v79, v75 quad_perm:[1,3,1,3] row_mask:0xf bank_mask:0xf\n" \
"v_cndmask_b32_dpp v224, v100, v104, vcc quad_perm:[0,2,0,2] row_mask:0xf bank_mask:0xf\n" \
"v_cndmask_b32_dpp v228, v72, v76, vcc quad_perm:[0,2,0,2] row_mask:0xf bank_mask:0xf\n" \
"v_cndmask_b32_dpp v225, v101, v105, vcc quad_perm:[0,2,0,2] row_mask:0xf bank_mask:0xf\n" \
"v_cndmask_b32_dpp v229, v73, v77, vcc quad_perm:[0,2,0,2] row_mask:0xf bank_mask:0xf\n" \
"v_cndmask_b32_dpp v226, v100, v106, vcc quad_perm:[1,3,1,3] row_mask:0xf bank_mask:0xf\n" \
"v_cndmask_b32_dpp v230, v72, v78, vcc quad_perm:[1,3,1,3] row_mask:0xf bank_mask:0xf\n" \
"v_cndmask_b32_dpp v227, v101, v107, vcc quad_perm:[1,3,1,3] row_mask:0xf bank_mask:0xf\n" \
"v_cndmask_b32_dpp v231, v73, v79, vcc quad_perm:[1,3,1,3] row_mask:0xf bank_mask:0xf\n" \
"v_accvgpr_read_b32 v180, a100\n" \
"v_accvgpr_read_b32 v181, a101\n" \
"v_accvgpr_read_b32 v182, a102\n" \
"v_accvgpr_read_b32 v183, a103\n" \
"v_accvgpr_read_b32 v184, a108\n" \
"v_accvgpr_read_b32 v185, a109\n" \
"v_accvgpr_read_b32 v186, a110\n" \
"v_accvgpr_read_b32 v187, a111\n" \
"s_mov_b32 s24, 0x05040100\n" \
"s_mov_b32 s25, 0x07060302\n" \
"v_accvgpr_read_b32 v32, a68\n" \
"v_accvgpr_read_b32 v33, a69\n" \
"v_accvgpr_read_b32 v34, a70\n" \
"v_accvgpr_read_b32 v35, a71\n" \
"v_accvgpr_read_b32 v36, a76\n" \
"v_accvgpr_read_b32 v37, a77\n" \
"v_accvgpr_read_b32 v38, a78\n" \
"v_accvgpr_read_b32 v39, a79\n" \
"v_accvgpr_read_b32 v40, a84\n" \
"v_accvgpr_read_b32 v41, a85\n" \
"v_accvgpr_read_b32 v42, a86\n" \
"v_accvgpr_read_b32 v43, a87\n" \
"v_accvgpr_read_b32 v44, a92\n" \
"v_accvgpr_read_b32 v45, a93\n" \
"v_accvgpr_read_b32 v46, a94\n" \
"v_accvgpr_read_b32 v47, a95\n" \
"v_perm_b32 v0, v36, v32, s24\n" \
"v_perm_b32 v1, v36, v32, s25\n" \
"v_perm_b32 v2, v37, v33, s24\n" \
"v_perm_b32 v3, v37, v33, s25\n" \
"v_perm_b32 v4, v38, v34, s24\n" \
"v_perm_b32 v5, v38, v34, s25\n" \
"v_perm_b32 v6, v39, v35, s24\n" \
"v_perm_b32 v7, v39, v35, s25\n" \
"v_perm_b32 v8, v44, v40, s24\n" \
"v_perm_b32 v9, v44, v40, s25\n" \
"v_perm_b32 v10, v45, v41, s24\n" \
"v_perm_b32 v11, v45, v41, s25\n" \
"v_perm_b32 v12, v46, v42, s24\n" \
"v_perm_b32 v13, v46, v42, s25\n" \
"v_perm_b32 v14, v47, v43, s24\n" \
"v_perm_b32 v15, v47, v43, s25\n" \
"v_lshlrev_b32 v16, 16, v180\n" \
"v_and_b32 v17, 0xffff0000, v180\n" \
"v_lshlrev_b32 v18, 16, v181\n" \
"v_and_b32 v19, 0xffff0000, v181\n" \
"v_lshlrev_b32 v20, 16, v182\n" \
"v_and_b32 v21, 0xffff0000, v182\n" \
"v_lshlrev_b32 v22, 16, v183\n" \
"v_and_b32 v23, 0xffff0000, v183\n" \
"v_lshlrev_b32 v24, 16, v184\n" \
"v_and_b32 v25, 0xffff0000, v184\n" \
"v_lshlrev_b32 v26, 16, v185\n" \
"v_and_b32 v27, 0xffff0000, v185\n" \
"v_lshlrev_b32 v28, 16, v186\n" \
"v_and_b32 v29, 0xffff0000, v186\n" \
"v_lshlrev_b32 v30, 16, v187\n" \
"v_and_b32 v31, 0xffff0000, v187\n" \
"v_and_b32 v173, 0xffff0000, v172\n" \
"v_lshlrev_b32 v172, 16, v172\n" \
"v_and_b32 v175, 0xffff0000, v174\n" \
"v_lshlrev_b32 v174, 16, v174\n" \
"v_and_b32 v177, 0xffff0000, v176\n" \
"v_lshlrev_b32 v176, 16, v176\n" \
"v_and_b32 v179, 0xffff0000, v178\n" \
"v_lshlrev_b32 v178, 16, v178\n" \
"v_lshrrev_b32 v100, 4, v191\n" \
"v_lshl_add_u32 v180, v100, 4, s94\n" \
"v_add_u32 v180, 256, v180\n" \
"v_and_b32 v101, 28, v236\n" \
"v_add_u32 v181, s94, v101\n" \
"v_add_u32 v181, 256, v181\n" \
"v_lshrrev_b32 v101, 1, v191\n" \
"v_and_b32 v102, 1, v191\n" \
"v_lshlrev_b32 v102, 4, v102\n" \
"v_lshl_or_b32 v182, v101, 11, v102\n" \
"s_lshl_b32 s24, %[wid], 5\n" \
"s_add_u32 s24, s24, s94\n" \
"s_add_u32 s24, s24, 512\n" \
"v_bfe_u32 v101, v251, 4, 2\n" \
"v_lshl_add_u32 v183, v101, 2, v102\n" \
"v_add_u32 v183, s24, v183\n" \
"v_and_b32 v101, 3, v191\n" \
"s_add_u32 s24, s94, 512\n" \
"v_lshl_add_u32 v238, v101, 3, s24\n" \
"s_lshl3_add_u32 s24, s64, s65\n" \
"v_lshrrev_b32 v101, 2, v191\n" \
"v_cmp_eq_u32 vcc, s24, v101\n" \
"v_mov_b32 v237, 1073741824\n" \
"s_lshl_b32 s24, s24, 5\n" \
"s_add_u32 s37, s24, 98304\n" \
"v_cndmask_b32 v237, v237, v236, vcc\n" \
"s_add_u32 s29, s37, 262144\n" \
"s_cmp_lt_u32 %[wid], 3\n" \
"s_cselect_b32 s29, s29, 1073741824\n" \
"s_lshl_b32 s24, s69, 11\n" \
"s_add_u32 s24, s24, 98304\n" \
"s_cmp_lt_u32 s65, 28\n" \
"s_cselect_b32 s41, s24, 1073741824\n" \
"s_lshl_b32 s85, s85, 10\n" \
"s_cmp_eq_u32 s87, s92\n" \
"s_cselect_b32 s24, 1073741824, s85\n" \
"buffer_store_dwordx4 v[224:227], v248, s[48:51], s24 offen offset:0 sc1\n" \
"s_cmp_eq_u32 s88, s92\n" \
"s_cselect_b32 s24, 1073741824, s85\n" \
"buffer_store_dwordx4 v[228:231], v248, s[48:51], s24 offen offset:1024 sc1\n" \
"s_mov_b32 s82, 0x100000\n" \
"s_mov_b32 s93, 0\n" \
"s_waitcnt vmcnt(2)\n" \
".Lkp_%=:\n" \
"v_cndmask_b32 v87, v87, v85, s[42:43]\n" \
"v_cndmask_b32 v86, v86, v84, s[42:43]\n" \
"v_cmp_eq_u32 vcc, 0, v87\n" \
"v_mov_b32 v88, v87\n" \
"s_nop 1\n" \
"s_cbranch_vccnz .Lkr_%=\n" \
".Lkb_%=:\n" \
"v_max_u32_dpp v88, v88, v88 quad_perm:[1,0,3,2] row_mask:0xf bank_mask:0xf\n" \
"s_nop 1\n" \
"v_max_u32_dpp v88, v88, v88 quad_perm:[2,3,0,1] row_mask:0xf bank_mask:0xf\n" \
"s_nop 1\n" \
"v_max_u32_dpp v88, v88, v88 row_half_mirror row_mask:0xf bank_mask:0xf\n" \
"s_nop 1\n" \
"v_max_u32_dpp v88, v88, v88 row_mirror row_mask:0xf bank_mask:0xf\n" \
"s_nop 1\n" \
"v_max_u32_dpp v88, v88, v88 row_bcast:15 row_mask:0xa bank_mask:0xf\n" \
"s_nop 1\n" \
"v_max_u32_dpp v88, v88, v88 row_bcast:31 row_mask:0xc bank_mask:0xf\n" \
"s_nop 0\n" \
"v_readlane_b32 s25, v88, 63\n" \
"s_nop 1\n" \
"v_cmp_eq_u32 vcc, s25, v87\n" \
"v_mov_b32 v89, s25\n" \
"s_nop 2\n" \
"s_ff1_i32_b64 s26, vcc\n" \
"s_and_b32 s26, s26, 63\n" \
"v_readlane_b32 s24, v86, s26\n" \
"s_nop 1\n" \
"v_mov_b32 v88, s24\n" \
"ds_write_b64 v232, v[88:89] offset:64\n" \
"s_waitcnt lgkmcnt(0)\n" \
"s_barrier\n" \
"ds_read_b128 v[84:87], v233 offset:64\n" \
"ds_read_b128 v[88:91], v233 offset:80\n" \
"s_waitcnt lgkmcnt(0)\n" \
"v_cmp_gt_u32 vcc, v85, v87\n" \
"v_max_u32 v85, v85, v87\n" \
"s_nop 0\n" \
"v_cndmask_b32 v84, v86, v84, vcc\n" \
"v_cmp_gt_u32 vcc, v89, v91\n" \
"v_max_u32 v89, v89, v91\n" \
"s_nop 0\n" \
"v_cndmask_b32 v88, v90, v88, vcc\n" \
"v_cmp_gt_u32 vcc, v85, v89\n" \
"v_max_u32 v85, v85, v89\n" \
"s_nop 0\n" \
"v_cndmask_b32 v84, v88, v84, vcc\n" \
"v_readfirstlane_b32 s93, v85\n" \
"s_bfe_u32 s30, s93, 0xb0005\n" \
"s_sub_u32 s30, 2047, s30\n" \
"s_mov_b32 s22, 0x18100800\n" \
"s_mov_b32 s23, 0x01091119\n" \
"s_bfe_u32 s24, s30, 0x30004\n" \
"s_lshl_b32 s24, s24, 3\n" \
"s_lshr_b64 s[22:23], s[22:23], s24\n" \
"s_and_b32 s22, s22, 0xff\n" \
"s_lshr_b32 s25, s30, 7\n" \
"s_lshl_b32 s25, s25, 5\n" \
"s_add_u32 s25, s25, s22\n" \
"s_lshl_b32 s25, s25, 6\n" \
"s_and_b32 s24, s30, 15\n" \
"s_add_u32 s25, s25, s24\n" \
"s_lshl_b32 s25, s25, 6\n" \
"s_add_u32 s25, s25, 59768832\n" \
"s_lshl_b32 s30, s30, 10\n" \
  \
"s_add_u32 s24, s25, 0\n" \
"buffer_load_dword v32, v239, s[32:35], s24 offen sc1\n" \
"s_add_u32 s24, s25, 2097152\n" \
"buffer_load_dword v33, v239, s[32:35], s24 offen sc1\n" \
"s_add_u32 s24, s25, 4194304\n" \
"buffer_load_dword v34, v239, s[32:35], s24 offen sc1\n" \
"s_add_u32 s24, s25, 6291456\n" \
"buffer_load_dword v35, v239, s[32:35], s24 offen sc1\n" \
"s_add_u32 s24, s25, 8388608\n" \
"buffer_load_dword v36, v239, s[32:35], s24 offen sc1\n" \
"s_add_u32 s24, s25, 10485760\n" \
"buffer_load_dword v37, v239, s[32:35], s24 offen sc1\n" \
"s_add_u32 s24, s25, 12582912\n" \
"buffer_load_dword v38, v239, s[32:35], s24 offen sc1\n" \
"s_add_u32 s24, s25, 14680064\n" \
"buffer_load_dword v39, v239, s[32:35], s24 offen sc1\n" \
"buffer_load_dword v40, v236, s[44:47], s30 offen\n" \
"s_cmp_eq_u32 s93, s92\n" \
"s_cselect_b32 s92, 0, s92\n" \
"s_cmp_eq_u32 s87, s92\n" \
"s_cselect_b32 s24, s85, 1073741824\n" \
"buffer_store_dwordx4 v[224:227], v248, s[48:51], s24 offen offset:0 sc1\n" \
"s_cmp_eq_u32 s88, s92\n" \
"s_cselect_b32 s24, s85, 1073741824\n" \
"buffer_store_dwordx4 v[228:231], v248, s[48:51], s24 offen offset:1024 sc1\n" \
"v_lshlrev_b32 v86, 16, v84\n" \
"v_and_b32 v88, 0xffff0000, v84\n" \
"v_rcp_f32 v86, v86\n" \
"s_lshr_b32 s24, s93, 16\n" \
"s_and_b32 s25, s24, 0x7fff\n" \
"s_andn2_b32 s26, 0xffff, s24\n" \
"s_bitcmp1_b32 s24, 15\n" \
"s_cselect_b32 s24, s25, s26\n" \
"s_lshl_b32 s24, s24, 16\n" \
"v_mov_b32 v90, 0xbfb8aa3b\n" \
"v_mul_f32 v90, s24, v90\n" \
"v_exp_f32 v90, v90\n" \
"s_nop 0\n" \
"v_add_f32 v90, 1.0, v90\n" \
"v_rcp_f32 v90, v90\n" \
"s_nop 0\n" \
"v_cvt_pk_bf16_f32 v90, v90, v90\n" \
"v_lshlrev_b32 v90, 16, v90\n" \
"s_waitcnt vmcnt(2)\n" \
"v_lshlrev_b32 v44, 16, v32\n" \
"v_and_b32 v45, 0xffff0000, v32\n" \
"v_lshlrev_b32 v46, 16, v33\n" \
"v_and_b32 v47, 0xffff0000, v33\n" \
"v_lshlrev_b32 v48, 16, v34\n" \
"v_and_b32 v49, 0xffff0000, v34\n" \
"v_lshlrev_b32 v50, 16, v35\n" \
"v_and_b32 v51, 0xffff0000, v35\n" \
"v_lshlrev_b32 v52, 16, v36\n" \
"v_and_b32 v53, 0xffff0000, v36\n" \
"v_lshlrev_b32 v54, 16, v37\n" \
"v_and_b32 v55, 0xffff0000, v37\n" \
"v_lshlrev_b32 v56, 16, v38\n" \
"v_and_b32 v57, 0xffff0000, v38\n" \
"v_lshlrev_b32 v58, 16, v39\n" \
"v_and_b32 v59, 0xffff0000, v39\n" \
"v_pk_add_f32 v[44:45], v[44:45], v[46:47]\n" \
"v_pk_add_f32 v[48:49], v[48:49], v[50:51]\n" \
"v_pk_add_f32 v[52:53], v[52:53], v[54:55]\n" \
"v_pk_add_f32 v[56:57], v[56:57], v[58:59]\n" \
"v_pk_add_f32 v[44:45], v[44:45], v[48:49]\n" \
"v_pk_add_f32 v[52:53], v[52:53], v[56:57]\n" \
"v_lshlrev_b32 v60, 16, v40\n" \
"v_pk_add_f32 v[44:45], v[44:45], v[52:53]\n" \
"v_and_b32 v61, 0xffff0000, v40\n" \
"v_pk_fma_f32 v[60:61], v[44:45], v[172:173], v[60:61]\n" \
"v_cvt_pk_bf16_f32 v46, v60, v61\n" \
"v_lshlrev_b32 v60, 16, v46\n" \
"v_and_b32 v61, 0xffff0000, v46\n" \
"v_pk_add_f32 v[44:45], v[60:61], v[88:89] op_sel_hi:[1,0] neg_lo:[0,1] neg_hi:[0,1]\n" \
"v_pk_mul_f32 v[44:45], v[44:45], v[176:177]\n" \
"v_pk_fma_f32 v[62:63], v[44:45], v[86:87], v[174:175] op_sel_hi:[1,0,1]\n" \
"v_cvt_pk_bf16_f32 v46, v62, v63\n" \
"v_lshlrev_b32 v62, 16, v46\n" \
"v_and_b32 v63, 0xffff0000, v46\n" \
"v_dot2_f32_bf16 v64, v0, v46, 0\n" \
"v_dot2_f32_bf16 v65, v1, v46, 0\n" \
"v_dot2_f32_bf16 v66, v2, v46, 0\n" \
"v_dot2_f32_bf16 v67, v3, v46, 0\n" \
"v_dot2_f32_bf16 v68, v4, v46, 0\n" \
"v_dot2_f32_bf16 v69, v5, v46, 0\n" \
"v_dot2_f32_bf16 v70, v6, v46, 0\n" \
"v_dot2_f32_bf16 v71, v7, v46, 0\n" \
"v_dot2_f32_bf16 v72, v8, v46, 0\n" \
"v_dot2_f32_bf16 v73, v9, v46, 0\n" \
"v_dot2_f32_bf16 v74, v10, v46, 0\n" \
"v_dot2_f32_bf16 v75, v11, v46, 0\n" \
"v_dot2_f32_bf16 v76, v12, v46, 0\n" \
"v_dot2_f32_bf16 v77, v13, v46, 0\n" \
"v_dot2_f32_bf16 v78, v14, v46, 0\n" \
"v_dot2_f32_bf16 v79, v15, v46, 0\n" \
"s_nop 1\n" \
"v_permlane32_swap_b32 v64, v72\n" \
"v_permlane32_swap_b32 v65, v73\n" \
"v_permlane32_swap_b32 v66, v74\n" \
"v_permlane32_swap_b32 v67, v75\n" \
"v_permlane32_swap_b32 v68, v76\n" \
"v_permlane32_swap_b32 v69, v77\n" \
"v_permlane32_swap_b32 v70, v78\n" \
"v_permlane32_swap_b32 v71, v79\n" \
"v_pk_add_f32 v[64:65], v[64:65], v[72:73]\n" \
"v_pk_add_f32 v[66:67], v[66:67], v[74:75]\n" \
"v_pk_add_f32 v[68:69], v[68:69], v[76:77]\n" \
"v_pk_add_f32 v[70:71], v[70:71], v[78:79]\n" \
"s_nop 0\n" \
"v_permlane16_swap_b32 v64, v68\n" \
"v_permlane16_swap_b32 v65, v69\n" \
"v_permlane16_swap_b32 v66, v70\n" \
"v_permlane16_swap_b32 v67, v71\n" \
"v_pk_add_f32 v[64:65], v[64:65], v[68:69]\n" \
"v_pk_add_f32 v[66:67], v[66:67], v[70:71]\n" \
"s_nop 0\n" \
"v_add_f32_dpp v64, v64, v64 quad_perm:[1,0,3,2] row_mask:0xf bank_mask:0xf\n" \
"v_add_f32_dpp v65, v65, v65 quad_perm:[1,0,3,2] row_mask:0xf bank_mask:0xf\n" \
"v_add_f32_dpp v66, v66, v66 quad_perm:[1,0,3,2] row_mask:0xf bank_mask:0xf\n" \
"v_add_f32_dpp v67, v67, v67 quad_perm:[1,0,3,2] row_mask:0xf bank_mask:0xf\n" \
"v_add_f32_dpp v64, v64, v64 quad_perm:[2,3,0,1] row_mask:0xf bank_mask:0xf\n" \
"v_add_f32_dpp v65, v65, v65 quad_perm:[2,3,0,1] row_mask:0xf bank_mask:0xf\n" \
"v_add_f32_dpp v66, v66, v66 quad_perm:[2,3,0,1] row_mask:0xf bank_mask:0xf\n" \
"v_add_f32_dpp v67, v67, v67 quad_perm:[2,3,0,1] row_mask:0xf bank_mask:0xf\n" \
"v_add_f32_dpp v64, v64, v64 row_half_mirror row_mask:0xf bank_mask:0xf\n" \
"v_add_f32_dpp v65, v65, v65 row_half_mirror row_mask:0xf bank_mask:0xf\n" \
"v_add_f32_dpp v66, v66, v66 row_half_mirror row_mask:0xf bank_mask:0xf\n" \
"v_add_f32_dpp v67, v67, v67 row_half_mirror row_mask:0xf bank_mask:0xf\n" \
"v_add_f32_dpp v64, v64, v64 row_mirror row_mask:0xf bank_mask:0xf\n" \
"v_add_f32_dpp v65, v65, v65 row_mirror row_mask:0xf bank_mask:0xf\n" \
"v_add_f32_dpp v66, v66, v66 row_mirror row_mask:0xf bank_mask:0xf\n" \
"v_add_f32_dpp v67, v67, v67 row_mirror row_mask:0xf bank_mask:0xf\n" \
"ds_write_b128 v180, v[64:67]\n" \
"s_waitcnt lgkmcnt(0)\n" \
"s_barrier\n" \
"ds_read2_b32 v[64:65], v181 offset0:0 offset1:8\n" \
"ds_read2_b32 v[66:67], v181 offset0:16 offset1:24\n" \
"ds_read2_b32 v[68:69], v181 offset0:32 offset1:40\n" \
"ds_read2_b32 v[70:71], v181 offset0:48 offset1:56\n" \
"s_waitcnt lgkmcnt(0)\n" \
"v_pk_add_f32 v[64:65], v[64:65], v[66:67]\n" \
"v_pk_add_f32 v[68:69], v[68:69], v[70:71]\n" \
"v_pk_add_f32 v[64:65], v[64:65], v[68:69]\n" \
"v_cvt_pk_bf16_f32 v66, v64, v65\n" \
"v_lshlrev_b32 v64, 16, v66\n" \
"v_and_b32 v65, 0xffff0000, v66\n" \
"v_mul_f32 v66, 0xbfb8aa3b, v64\n" \
"v_exp_f32 v66, v66\n" \
"s_nop 0\n" \
"v_add_f32 v66, 1.0, v66\n" \
"v_rcp_f32 v66, v66\n" \
"s_nop 0\n" \
"v_mul_f32 v64, v64, v66\n" \
"v_cvt_pk_bf16_f32 v64, v64, v64\n" \
"v_lshlrev_b32 v64, 16, v64\n" \
"v_mul_f32 v64, v64, v65\n" \
"v_cvt_pk_bf16_f32 v64, v64, v64\n" \
"v_lshlrev_b32 v64, 16, v64\n" \
"s_nop 0\n" \
"v_readlane_b32 s16, v64, 0\n" \
"v_readlane_b32 s17, v64, 1\n" \
"v_readlane_b32 s18, v64, 2\n" \
"v_readlane_b32 s19, v64, 3\n" \
"v_readlane_b32 s20, v64, 4\n" \
"v_readlane_b32 s21, v64, 5\n" \
"v_readlane_b32 s22, v64, 6\n" \
"v_readlane_b32 s23, v64, 7\n" \
"v_mul_f32 v66, s16, v16\n" \
"v_mul_f32 v67, s16, v17\n" \
"v_mul_f32 v68, s17, v18\n" \
"v_mul_f32 v69, s17, v19\n" \
"v_fmac_f32 v66, s18, v20\n" \
"v_fmac_f32 v67, s18, v21\n" \
"v_fmac_f32 v68, s19, v22\n" \
"v_fmac_f32 v69, s19, v23\n" \
"v_fmac_f32 v66, s20, v24\n" \
"v_fmac_f32 v67, s20, v25\n" \
"v_fmac_f32 v68, s21, v26\n" \
"v_fmac_f32 v69, s21, v27\n" \
"v_fmac_f32 v66, s22, v28\n" \
"v_fmac_f32 v67, s22, v29\n" \
"v_fmac_f32 v68, s23, v30\n" \
"v_fmac_f32 v69, s23, v31\n" \
"v_pk_add_f32 v[66:67], v[66:67], v[68:69]\n" \
"v_min_u32 v66, -2, v66\n" \
"v_min_u32 v67, -2, v67\n" \
"buffer_store_dwordx2 v[66:67], v190, s[32:35], s41 offen sc1\n" \
  \
"s_cmp_ge_u32 %[bid], 64\n" \
"s_cbranch_scc1 .Lend_%=\n" \
"s_mov_b32 s82, 0x100000\n" \
"s_sleep 14\n" \
".Lfp_%=:\n" \
"buffer_load_dwordx4 v[64:67], v182, s[32:35], s37 offen sc1\n" \
"buffer_load_dwordx4 v[68:71], v182, s[32:35], s29 offen sc1\n" \
"s_waitcnt vmcnt(0)\n" \
"v_max3_u32 v72, v64, v65, v66\n" \
"v_max3_u32 v73, v67, v68, v69\n" \
"v_max3_u32 v72, v72, v70, v71\n" \
"v_max_u32 v72, v72, v73\n" \
"v_cmp_eq_u32 vcc, -1, v72\n" \
"v_pk_add_f32 v[64:65], v[64:65], v[68:69]\n" \
"v_pk_add_f32 v[66:67], v[66:67], v[70:71]\n" \
"s_nop 0\n" \
"s_cbranch_vccnz .Lfr_%=\n" \
".Lfb_%=:\n" \
"v_permlane32_swap_b32 v64, v66\n" \
"v_permlane32_swap_b32 v65, v67\n" \
"v_pk_add_f32 v[64:65], v[64:65], v[66:67]\n" \
"s_nop 1\n" \
"v_permlane16_swap_b32 v64, v65\n" \
"v_add_f32 v64, v64, v65\n" \
"s_nop 1\n" \
"v_add_f32_dpp v64, v64, v64 quad_perm:[2,3,0,1] row_mask:0xf bank_mask:0xf\n" \
"s_nop 1\n" \
"v_add_f32_dpp v64, v64, v64 row_ror:4 row_mask:0xf bank_mask:0xf\n" \
"s_nop 1\n" \
"v_add_f32_dpp v64, v64, v64 row_ror:8 row_mask:0xf bank_mask:0xf\n" \
"ds_write_b32 v183, v64\n" \
"s_waitcnt lgkmcnt(0)\n" \
"s_barrier\n" \
"ds_read2_b64 v[64:67], v238 offset0:0 offset1:4\n" \
"ds_read2_b64 v[68:71], v238 offset0:8 offset1:12\n" \
"s_waitcnt lgkmcnt(0)\n" \
"v_pk_add_f32 v[64:65], v[64:65], v[66:67]\n" \
"v_pk_add_f32 v[68:69], v[68:69], v[70:71]\n" \
"v_pk_add_f32 v[64:65], v[64:65], v[68:69]\n" \
"v_cvt_pk_bf16_f32 v46, v64, v65\n" \
"v_lshlrev_b32 v64, 16, v46\n" \
"v_and_b32 v65, 0xffff0000, v46\n" \
"v_pk_mul_f32 v[64:65], v[64:65], v[90:91] op_sel_hi:[1,0]\n" \
"v_cvt_pk_bf16_f32 v46, v64, v65\n" \
"v_lshlrev_b32 v64, 16, v46\n" \
"v_and_b32 v65, 0xffff0000, v46\n" \
"v_pk_add_f32 v[64:65], v[64:65], v[62:63]\n" \
".Lst3_1_%=:\n" \
"s_cbranch_execz .Lst3_5_%=\n" \
"v_cvt_pk_bf16_f32 v46, v64, v65\n" \
"v_lshlrev_b32 v64, 16, v46\n" \
"v_and_b32 v65, 0xffff0000, v46\n" \
"v_pk_mul_f32 v[64:65], v[64:65], v[178:179]\n" \
"v_cvt_pk_bf16_f32 v46, v64, v65\n" \
"v_lshlrev_b32 v64, 16, v46\n" \
"v_and_b32 v65, 0xffff0000, v46\n" \
"v_pk_add_f32 v[64:65], v[64:65], v[60:61]\n" \
"v_cvt_pk_bf16_f32 v64, v64, v65\n" \
"buffer_store_dword v64, v237, s[48:51], s30 offen sc1\n" \
"s_branch .Lend_%=\n" \
".Lrare_%=:\n" \
  \
".Ldhi_%=:\n" \
".Lst0_14_%=:\n" \
"s_cbranch_execz .Lstb0_%=\n" \
"v_mfma_f32_16x16x32_bf16 v[128:131], a[128:131], v[192:195], 0\n" \
"v_mfma_f32_16x16x32_bf16 v[128:131], a[132:135], v[196:199], v[128:131]\n" \
"s_barrier\n" \
"v_mfma_f32_16x16x32_bf16 v[128:131], a[136:139], v[200:203], v[128:131]\n" \
"ds_read_b128 a[224:227], v242 offset:8192\n" \
"v_cvt_pk_bf16_f32 v172, v88, v89\n" \
"v_mfma_f32_16x16x32_bf16 v[128:131], a[140:143], v[204:207], v[128:131]\n" \
"ds_read_b128 a[192:195], v242 offset:0\n" \
"v_cvt_pk_bf16_f32 v173, v90, v91\n" \
"v_mfma_f32_16x16x32_bf16 v[132:135], a[156:159], v[204:207], 0\n" \
"ds_read_b128 a[196:199], v242 offset:1024\n" \
"v_cvt_pk_bf16_f32 v174, v92, v93\n" \
"v_mfma_f32_16x16x32_bf16 v[132:135], a[152:155], v[200:203], v[132:135]\n" \
"ds_read_b128 a[228:231], v242 offset:9216\n" \
"v_cvt_pk_bf16_f32 v175, v94, v95\n" \
"v_mfma_f32_16x16x32_bf16 v[132:135], a[148:151], v[196:199], v[132:135]\n" \
"v_exp_f32 v128, v128\n" \
"v_mfma_f32_16x16x32_bf16 v[132:135], a[144:147], v[192:195], v[132:135]\n" \
"s_add_u32 s85, s94, 589824\n" \
"v_exp_f32 v129, v129\n" \
"v_mfma_f32_16x16x32_bf16 v[136:139], a[160:163], v[192:195], v[224:227]\n" \
"buffer_store_dwordx4 a[64:67], v248, s[32:35], s85 offen offset:0 sc1\n" \
"v_mfma_f32_16x16x32_bf16 v[136:139], a[164:167], v[196:199], v[136:139]\n" \
"v_exp_f32 v130, v130\n" \
"v_mfma_f32_16x16x32_bf16 v[136:139], a[168:171], v[200:203], v[136:139]\n" \
"v_exp_f32 v131, v131\n" \
"v_mfma_f32_16x16x32_bf16 v[136:139], a[172:175], v[204:207], v[136:139]\n" \
"v_cvt_pk_bf16_f32 v128, v128, v129\n" \
"v_cvt_pk_bf16_f32 v129, v130, v131\n" \
"v_mfma_f32_16x16x32_bf16 v[140:143], a[188:191], v[204:207], v[228:231]\n" \
"v_exp_f32 v132, v132\n" \
"v_mfma_f32_16x16x32_bf16 v[140:143], a[184:187], v[200:203], v[140:143]\n" \
"v_exp_f32 v133, v133\n" \
"v_mfma_f32_16x16x32_bf16 v[140:143], a[180:183], v[196:199], v[140:143]\n" \
"v_exp_f32 v134, v134\n" \
"v_mfma_f32_16x16x32_bf16 v[140:143], a[176:179], v[192:195], v[140:143]\n" \
"ds_read_b128 a[232:235], v242 offset:10240\n" \
"v_cvt_pk_bf16_f32 v130, v132, v133\n" \
"v_mfma_f32_16x16x32_bf16 v[144:147], a[128:131], v[208:211], 0\n" \
"v_exp_f32 v135, v135\n" \
"v_mfma_f32_16x16x32_bf16 v[144:147], a[132:135], v[212:215], v[144:147]\n" \
"v_exp_f32 v136, v136\n" \
"v_mfma_f32_16x16x32_bf16 v[144:147], a[136:139], v[216:219], v[144:147]\n" \
"ds_read_b128 a[200:203], v242 offset:2048\n" \
"v_cvt_pk_bf16_f32 v131, v134, v135\n" \
"v_mfma_f32_16x16x32_bf16 v[144:147], a[140:143], v[220:223], v[144:147]\n" \
"v_exp_f32 v137, v137\n" \
"v_mfma_f32_16x16x32_bf16 v[148:151], a[156:159], v[220:223], 0\n" \
"v_exp_f32 v138, v138\n" \
"v_mfma_f32_16x16x32_bf16 v[148:151], a[152:155], v[216:219], v[148:151]\n" \
"v_exp_f32 v139, v139\n" \
"v_mfma_f32_16x16x32_bf16 v[148:151], a[148:151], v[212:215], v[148:151]\n" \
"v_cvt_pk_bf16_f32 v136, v136, v137\n" \
"v_cvt_pk_bf16_f32 v137, v138, v139\n" \
"v_mfma_f32_16x16x32_bf16 v[148:151], a[144:147], v[208:211], v[148:151]\n" \
"v_exp_f32 v140, v140\n" \
"v_mfma_f32_16x16x32_bf16 v[152:155], a[160:163], v[208:211], 0\n" \
"v_exp_f32 v141, v141\n" \
"v_mfma_f32_16x16x32_bf16 v[152:155], a[164:167], v[212:215], v[152:155]\n" \
"s_add_u32 s85, s94, 589824\n" \
"ds_read_b128 a[204:207], v242 offset:3072\n" \
"v_cvt_pk_bf16_f32 v138, v140, v141\n" \
"v_mfma_f32_16x16x32_bf16 v[152:155], a[168:171], v[216:219], v[152:155]\n" \
"v_exp_f32 v142, v142\n" \
"v_mfma_f32_16x16x32_bf16 v[152:155], a[172:175], v[220:223], v[152:155]\n" \
"v_exp_f32 v143, v143\n" \
"v_mfma_f32_16x16x32_bf16 v[156:159], a[188:191], v[220:223], 0\n" \
"buffer_store_dwordx4 a[72:75], v248, s[32:35], s85 offen offset:1024 sc1\n" \
"v_mfma_f32_16x16x32_bf16 v[156:159], a[184:187], v[216:219], v[156:159]\n" \
"ds_read_b128 a[236:239], v242 offset:11264\n" \
"v_cvt_pk_bf16_f32 v139, v142, v143\n" \
"v_mfma_f32_16x16x32_bf16 v[156:159], a[180:183], v[212:215], v[156:159]\n" \
"v_exp_f32 v144, v144\n" \
"v_mfma_f32_16x16x32_bf16 v[156:159], a[176:179], v[208:211], v[156:159]\n" \
"ds_read_b128 a[240:243], v242 offset:12288\n" \
"ds_read_b128 a[208:211], v242 offset:4096\n" \
"v_mfma_f32_16x16x32_bf16 a[32:35], a[48:51], v[128:131], 0\n" \
"v_exp_f32 v145, v145\n" \
"v_mfma_f32_16x16x32_bf16 a[32:35], a[48:51], v[136:139], a[32:35]\n" \
"s_waitcnt lgkmcnt(6)\n" \
"ds_read_b128 a[212:215], v242 offset:5120\n" \
"v_cvt_pk_bf16_f32 v144, v144, v145\n" \
"v_mfma_f32_16x16x32_bf16 v[0:3], a[224:227], v[136:139], 0\n" \
"v_exp_f32 v146, v146\n" \
"v_mfma_f32_16x16x32_bf16 v[0:3], a[192:195], v[128:131], v[0:3]\n" \
"ds_read_b128 a[244:247], v242 offset:13312\n" \
"ds_read_b128 a[248:251], v242 offset:14336\n" \
"v_mfma_f32_16x16x32_bf16 v[4:7], a[196:199], v[128:131], 0\n" \
"v_exp_f32 v147, v147\n" \
"v_mfma_f32_16x16x32_bf16 v[4:7], a[228:231], v[136:139], v[4:7]\n" \
"s_waitcnt lgkmcnt(5)\n" \
"ds_read_b128 a[216:219], v242 offset:6144\n" \
"v_cvt_pk_bf16_f32 v145, v146, v147\n" \
"v_mfma_f32_16x16x32_bf16 v[8:11], a[232:235], v[136:139], 0\n" \
"v_exp_f32 v148, v148\n" \
"v_mfma_f32_16x16x32_bf16 v[8:11], a[200:203], v[128:131], v[8:11]\n" \
"ds_read_b128 a[220:223], v242 offset:7168\n" \
"ds_read_b128 a[252:255], v242 offset:15360\n" \
"v_mfma_f32_16x16x32_bf16 v[12:15], a[204:207], v[128:131], 0\n" \
"v_exp_f32 v149, v149\n" \
"v_mfma_f32_16x16x32_bf16 v[12:15], a[236:239], v[136:139], v[12:15]\n" \
"s_waitcnt lgkmcnt(4)\n" \
"v_cvt_pk_bf16_f32 v146, v148, v149\n" \
"v_cvt_pk_bf16_f32 v176, v96, v97\n" \
"v_mfma_f32_16x16x32_bf16 v[16:19], a[240:243], v[136:139], 0\n" \
"v_exp_f32 v150, v150\n" \
"v_mfma_f32_16x16x32_bf16 v[16:19], a[208:211], v[128:131], v[16:19]\n" \
"s_add_u32 s85, s94, 589824\n" \
"v_cvt_pk_bf16_f32 v177, v98, v99\n" \
"v_cvt_pk_bf16_f32 v178, v100, v101\n" \
"v_mfma_f32_16x16x32_bf16 v[20:23], a[212:215], v[128:131], 0\n" \
"v_exp_f32 v151, v151\n" \
"v_mfma_f32_16x16x32_bf16 v[20:23], a[244:247], v[136:139], v[20:23]\n" \
"s_waitcnt lgkmcnt(0)\n" \
"buffer_store_dwordx4 a[80:83], v248, s[32:35], s85 offen offset:2048 sc1\n" \
"v_mfma_f32_16x16x32_bf16 v[24:27], a[248:251], v[136:139], 0\n" \
"v_cvt_pk_bf16_f32 v147, v150, v151\n" \
"v_cvt_pk_bf16_f32 v179, v102, v103\n" \
"v_mfma_f32_16x16x32_bf16 v[24:27], a[216:219], v[128:131], v[24:27]\n" \
"v_exp_f32 v152, v152\n" \
"v_mfma_f32_16x16x32_bf16 v[28:31], a[220:223], v[128:131], 0\n" \
"v_cvt_pk_bf16_f32 v180, v104, v105\n" \
"v_cvt_pk_bf16_f32 v181, v106, v107\n" \
"v_mfma_f32_16x16x32_bf16 v[28:31], a[252:255], v[136:139], v[28:31]\n" \
"v_exp_f32 v153, v153\n" \
"v_mfma_f32_16x16x32_bf16 v[128:131], a[128:131], v[160:163], 0\n" \
"ds_write_b128 v249, a[64:67] offset:32768\n" \
"v_cvt_pk_bf16_f32 v152, v152, v153\n" \
"v_mfma_f32_16x16x32_bf16 v[128:131], a[132:135], v[164:167], v[128:131]\n" \
"v_exp_f32 v154, v154\n" \
"v_mfma_f32_16x16x32_bf16 v[128:131], a[136:139], v[168:171], v[128:131]\n" \
"v_cvt_pk_bf16_f32 v182, v108, v109\n" \
"v_cvt_pk_bf16_f32 v183, v110, v111\n" \
"v_mfma_f32_16x16x32_bf16 v[128:131], a[140:143], v[172:175], v[128:131]\n" \
"v_exp_f32 v155, v155\n" \
"v_mfma_f32_16x16x32_bf16 v[132:135], a[156:159], v[172:175], 0\n" \
"v_cvt_pk_bf16_f32 v153, v154, v155\n" \
"v_cvt_pk_bf16_f32 v184, v112, v113\n" \
"v_mfma_f32_16x16x32_bf16 v[132:135], a[152:155], v[168:171], v[132:135]\n" \
"v_exp_f32 v156, v156\n" \
"v_mfma_f32_16x16x32_bf16 v[132:135], a[148:151], v[164:167], v[132:135]\n" \
"v_cvt_pk_bf16_f32 v185, v114, v115\n" \
"v_cvt_pk_bf16_f32 v186, v116, v117\n" \
"v_mfma_f32_16x16x32_bf16 v[132:135], a[144:147], v[160:163], v[132:135]\n" \
"v_exp_f32 v157, v157\n" \
"v_mfma_f32_16x16x32_bf16 v[136:139], a[160:163], v[160:163], 0\n" \
"v_cvt_pk_bf16_f32 v154, v156, v157\n" \
"v_cvt_pk_bf16_f32 v187, v118, v119\n" \
"v_mfma_f32_16x16x32_bf16 v[136:139], a[164:167], v[164:167], v[136:139]\n" \
"v_exp_f32 v158, v158\n" \
"v_mfma_f32_16x16x32_bf16 v[136:139], a[168:171], v[168:171], v[136:139]\n" \
"v_cvt_pk_bf16_f32 v188, v120, v121\n" \
"v_cvt_pk_bf16_f32 v189, v122, v123\n" \
"v_mfma_f32_16x16x32_bf16 v[136:139], a[172:175], v[172:175], v[136:139]\n" \
"s_add_u32 s85, s94, 589824\n" \
"v_exp_f32 v159, v159\n" \
"v_mfma_f32_16x16x32_bf16 v[140:143], a[188:191], v[172:175], 0\n" \
"buffer_store_dwordx4 a[88:91], v248, s[32:35], s85 offen offset:3072 sc1\n" \
"v_mfma_f32_16x16x32_bf16 v[140:143], a[184:187], v[168:171], v[140:143]\n" \
"ds_write_b128 v249, a[72:75] offset:33792\n" \
"v_cvt_pk_bf16_f32 v155, v158, v159\n" \
"v_mfma_f32_16x16x32_bf16 v[140:143], a[180:183], v[164:167], v[140:143]\n" \
"v_exp_f32 v128, v128\n" \
"v_mfma_f32_16x16x32_bf16 v[140:143], a[176:179], v[160:163], v[140:143]\n" \
"v_cvt_pk_bf16_f32 v190, v124, v125\n" \
"v_cvt_pk_bf16_f32 v191, v126, v127\n" \
"v_mfma_f32_16x16x32_bf16 a[36:39], a[48:51], v[144:147], 0\n" \
"v_exp_f32 v129, v129\n" \
"v_mfma_f32_16x16x32_bf16 a[36:39], a[48:51], v[152:155], a[36:39]\n" \
"v_cvt_pk_bf16_f32 v128, v128, v129\n" \
"v_mfma_f32_16x16x32_bf16 v[32:35], a[224:227], v[152:155], 0\n" \
"v_exp_f32 v130, v130\n" \
"v_mfma_f32_16x16x32_bf16 v[32:35], a[192:195], v[144:147], v[32:35]\n" \
"v_mfma_f32_16x16x32_bf16 v[36:39], a[196:199], v[144:147], 0\n" \
"v_exp_f32 v131, v131\n" \
"v_mfma_f32_16x16x32_bf16 v[36:39], a[228:231], v[152:155], v[36:39]\n" \
"v_cvt_pk_bf16_f32 v129, v130, v131\n" \
"v_mfma_f32_16x16x32_bf16 v[40:43], a[232:235], v[152:155], 0\n" \
"v_exp_f32 v132, v132\n" \
"v_mfma_f32_16x16x32_bf16 v[40:43], a[200:203], v[144:147], v[40:43]\n" \
"v_mfma_f32_16x16x32_bf16 v[44:47], a[204:207], v[144:147], 0\n" \
"v_exp_f32 v133, v133\n" \
"v_mfma_f32_16x16x32_bf16 v[44:47], a[236:239], v[152:155], v[44:47]\n" \
"v_cvt_pk_bf16_f32 v130, v132, v133\n" \
"v_mfma_f32_16x16x32_bf16 v[48:51], a[240:243], v[152:155], 0\n" \
"v_exp_f32 v134, v134\n" \
"v_mfma_f32_16x16x32_bf16 v[48:51], a[208:211], v[144:147], v[48:51]\n" \
"ds_write_b128 v249, a[80:83] offset:34816\n" \
"v_mfma_f32_16x16x32_bf16 v[52:55], a[212:215], v[144:147], 0\n" \
"v_exp_f32 v135, v135\n" \
"v_mfma_f32_16x16x32_bf16 v[52:55], a[244:247], v[152:155], v[52:55]\n" \
"s_add_u32 s85, s94, 593920\n" \
"v_cvt_pk_bf16_f32 v131, v134, v135\n" \
"v_mfma_f32_16x16x32_bf16 v[56:59], a[248:251], v[152:155], 0\n" \
"buffer_store_dwordx4 a[96:99], v248, s[32:35], s85 offen offset:0 sc1\n" \
"v_mfma_f32_16x16x32_bf16 v[56:59], a[216:219], v[144:147], v[56:59]\n" \
"v_exp_f32 v136, v136\n" \
"v_mfma_f32_16x16x32_bf16 v[60:63], a[220:223], v[144:147], 0\n" \
"v_mfma_f32_16x16x32_bf16 v[60:63], a[252:255], v[152:155], v[60:63]\n" \
"v_exp_f32 v137, v137\n" \
"v_mfma_f32_16x16x32_bf16 v[144:147], a[128:131], v[176:179], 0\n" \
"v_cvt_pk_bf16_f32 v136, v136, v137\n" \
"v_mfma_f32_16x16x32_bf16 v[144:147], a[132:135], v[180:183], v[144:147]\n" \
"v_exp_f32 v138, v138\n" \
"v_mfma_f32_16x16x32_bf16 v[144:147], a[136:139], v[184:187], v[144:147]\n" \
"ds_read_b128 a[128:131], v248 offset:16384\n" \
"ds_read_b128 a[132:135], v248 offset:17408\n" \
"v_mfma_f32_16x16x32_bf16 v[144:147], a[140:143], v[188:191], v[144:147]\n" \
"v_exp_f32 v139, v139\n" \
"v_mfma_f32_16x16x32_bf16 v[148:151], a[156:159], v[188:191], 0\n" \
"ds_read_b128 a[136:139], v248 offset:18432\n" \
"v_cvt_pk_bf16_f32 v137, v138, v139\n" \
"v_mfma_f32_16x16x32_bf16 v[148:151], a[152:155], v[184:187], v[148:151]\n" \
"v_exp_f32 v140, v140\n" \
"v_mfma_f32_16x16x32_bf16 v[148:151], a[148:151], v[180:183], v[148:151]\n" \
"ds_write_b128 v249, a[88:91] offset:35840\n" \
"v_mfma_f32_16x16x32_bf16 v[148:151], a[144:147], v[176:179], v[148:151]\n" \
"v_exp_f32 v141, v141\n" \
"v_mfma_f32_16x16x32_bf16 v[152:155], a[160:163], v[176:179], 0\n" \
"ds_read_b128 a[140:143], v248 offset:19456\n" \
"v_cvt_pk_bf16_f32 v138, v140, v141\n" \
"v_mfma_f32_16x16x32_bf16 v[152:155], a[164:167], v[180:183], v[152:155]\n" \
"v_exp_f32 v142, v142\n" \
"v_mfma_f32_16x16x32_bf16 v[152:155], a[168:171], v[184:187], v[152:155]\n" \
"ds_read_b128 a[156:159], v248 offset:23552\n" \
"ds_read_b128 a[152:155], v248 offset:22528\n" \
"v_mfma_f32_16x16x32_bf16 v[152:155], a[172:175], v[188:191], v[152:155]\n" \
"v_exp_f32 v143, v143\n" \
"v_mfma_f32_16x16x32_bf16 v[156:159], a[188:191], v[188:191], 0\n" \
"v_exp_f32 v144, v144\n" \
"v_mfma_f32_16x16x32_bf16 v[156:159], a[184:187], v[184:187], v[156:159]\n" \
"s_add_u32 s85, s94, 593920\n" \
"v_exp_f32 v145, v145\n" \
"v_mfma_f32_16x16x32_bf16 v[156:159], a[180:183], v[180:183], v[156:159]\n" \
"v_exp_f32 v146, v146\n" \
"v_mfma_f32_16x16x32_bf16 v[156:159], a[176:179], v[176:179], v[156:159]\n" \
"v_exp_f32 v147, v147\n" \
"v_cvt_pk_bf16_f32 v139, v142, v143\n" \
"v_mfma_f32_16x16x32_bf16 a[40:43], a[48:51], v[128:131], 0\n" \
"v_exp_f32 v148, v148\n" \
"v_mfma_f32_16x16x32_bf16 a[40:43], a[48:51], v[136:139], a[40:43]\n" \
"v_exp_f32 v149, v149\n" \
"v_mfma_f32_16x16x32_bf16 v[64:67], a[224:227], v[136:139], 0\n" \
"v_exp_f32 v150, v150\n" \
"v_mfma_f32_16x16x32_bf16 v[64:67], a[192:195], v[128:131], v[64:67]\n" \
"buffer_store_dwordx4 a[104:107], v248, s[32:35], s85 offen offset:1024 sc1\n" \
"v_mfma_f32_16x16x32_bf16 v[68:71], a[196:199], v[128:131], 0\n" \
"v_exp_f32 v151, v151\n" \
"v_mfma_f32_16x16x32_bf16 v[68:71], a[228:231], v[136:139], v[68:71]\n" \
"buffer_load_dword v253, v252, s[32:35], s81 offen sc1\n" \
"v_mov_b32 v240, v248\n" \
"v_mfma_f32_16x16x32_bf16 v[72:75], a[232:235], v[136:139], 0\n" \
"ds_write_b128 v249, a[96:99] offset:36864\n" \
"v_cvt_pk_bf16_f32 v144, v144, v145\n" \
"v_mfma_f32_16x16x32_bf16 v[72:75], a[200:203], v[128:131], v[72:75]\n" \
"v_exp_f32 v152, v152\n" \
"v_mfma_f32_16x16x32_bf16 v[76:79], a[204:207], v[128:131], 0\n" \
"v_cvt_pk_bf16_f32 v145, v146, v147\n" \
"v_cvt_pk_bf16_f32 v146, v148, v149\n" \
"v_mfma_f32_16x16x32_bf16 v[76:79], a[236:239], v[136:139], v[76:79]\n" \
"v_exp_f32 v153, v153\n" \
"v_mfma_f32_16x16x32_bf16 v[80:83], a[240:243], v[136:139], 0\n" \
"v_cvt_pk_bf16_f32 v147, v150, v151\n" \
"v_cvt_pk_bf16_f32 v152, v152, v153\n" \
"v_mfma_f32_16x16x32_bf16 v[80:83], a[208:211], v[128:131], v[80:83]\n" \
"v_exp_f32 v154, v154\n" \
"v_mfma_f32_16x16x32_bf16 v[84:87], a[212:215], v[128:131], 0\n" \
"ds_read_b128 a[148:151], v248 offset:21504\n" \
"ds_read_b128 a[144:147], v248 offset:20480\n" \
"v_mfma_f32_16x16x32_bf16 v[84:87], a[244:247], v[136:139], v[84:87]\n" \
"v_exp_f32 v155, v155\n" \
"v_mfma_f32_16x16x32_bf16 v[88:91], a[248:251], v[136:139], 0\n" \
"ds_read_b128 a[160:163], v248 offset:24576\n" \
"v_cvt_pk_bf16_f32 v153, v154, v155\n" \
"v_mfma_f32_16x16x32_bf16 v[88:91], a[216:219], v[128:131], v[88:91]\n" \
"s_add_u32 s85, s94, 593920\n" \
"v_exp_f32 v156, v156\n" \
"v_mfma_f32_16x16x32_bf16 v[92:95], a[220:223], v[128:131], 0\n" \
"buffer_store_dwordx4 a[112:115], v248, s[32:35], s85 offen offset:2048 sc1\n" \
"v_mfma_f32_16x16x32_bf16 v[92:95], a[252:255], v[136:139], v[92:95]\n" \
"s_waitcnt lgkmcnt(6)\n" \
"v_exp_f32 v157, v157\n" \
"v_mfma_f32_16x16x32_bf16 v[128:131], a[128:131], v[208:211], v[232:235]\n" \
"ds_write_b128 v249, a[104:107] offset:37888\n" \
"v_cvt_pk_bf16_f32 v154, v156, v157\n" \
"v_mfma_f32_16x16x32_bf16 v[128:131], a[132:135], v[212:215], v[128:131]\n" \
"v_exp_f32 v158, v158\n" \
"v_mfma_f32_16x16x32_bf16 v[128:131], a[136:139], v[216:219], v[128:131]\n" \
"ds_read_b128 a[164:167], v248 offset:25600\n" \
"ds_read_b128 a[168:171], v248 offset:26624\n" \
"v_mfma_f32_16x16x32_bf16 v[128:131], a[140:143], v[220:223], v[128:131]\n" \
"s_waitcnt lgkmcnt(4)\n" \
"v_exp_f32 v159, v159\n" \
"v_mfma_f32_16x16x32_bf16 v[132:135], a[156:159], v[220:223], v[236:239]\n" \
"ds_read_b128 a[172:175], v248 offset:27648\n" \
"v_cvt_pk_bf16_f32 v155, v158, v159\n" \
"v_mfma_f32_16x16x32_bf16 v[132:135], a[152:155], v[216:219], v[132:135]\n" \
"ds_read_b128 a[188:191], v248 offset:31744\n" \
"ds_read_b128 a[184:187], v248 offset:30720\n" \
"v_mfma_f32_16x16x32_bf16 v[132:135], a[148:151], v[212:215], v[132:135]\n" \
"v_exp_f32 v128, v128\n" \
"v_mfma_f32_16x16x32_bf16 v[132:135], a[144:147], v[208:211], v[132:135]\n" \
"ds_read_b128 a[180:183], v248 offset:29696\n" \
"ds_read_b128 a[176:179], v248 offset:28672\n" \
"v_mfma_f32_16x16x32_bf16 a[44:47], a[48:51], v[144:147], 0\n" \
"v_mfma_f32_16x16x32_bf16 a[44:47], a[48:51], v[152:155], a[44:47]\n" \
"v_mfma_f32_16x16x32_bf16 v[96:99], a[224:227], v[152:155], 0\n" \
"v_exp_f32 v129, v129\n" \
"v_mfma_f32_16x16x32_bf16 v[96:99], a[192:195], v[144:147], v[96:99]\n" \
"ds_write_b128 v249, a[112:115] offset:38912\n" \
"v_cvt_pk_bf16_f32 v128, v128, v129\n" \
"v_mfma_f32_16x16x32_bf16 v[100:103], a[196:199], v[144:147], 0\n" \
"ds_read_b128 a[192:195], v242 offset:16384\n" \
"v_mfma_f32_16x16x32_bf16 v[100:103], a[228:231], v[152:155], v[100:103]\n" \
"ds_read_b128 a[196:199], v242 offset:17408\n" \
"v_mfma_f32_16x16x32_bf16 v[104:107], a[232:235], v[152:155], 0\n" \
"v_mfma_f32_16x16x32_bf16 v[104:107], a[200:203], v[144:147], v[104:107]\n" \
"s_add_u32 s85, s94, 593920\n" \
"v_exp_f32 v130, v130\n" \
"v_mfma_f32_16x16x32_bf16 v[108:111], a[204:207], v[144:147], 0\n" \
"buffer_store_dwordx4 a[120:123], v248, s[32:35], s85 offen offset:3072 sc1\n" \
"v_mfma_f32_16x16x32_bf16 v[108:111], a[236:239], v[152:155], v[108:111]\n" \
"ds_read_b128 a[200:203], v242 offset:18432\n" \
"ds_read_b128 a[204:207], v242 offset:19456\n" \
"v_mfma_f32_16x16x32_bf16 v[112:115], a[240:243], v[152:155], 0\n" \
"v_mfma_f32_16x16x32_bf16 v[112:115], a[208:211], v[144:147], v[112:115]\n" \
"v_exp_f32 v131, v131\n" \
"v_mfma_f32_16x16x32_bf16 v[116:119], a[212:215], v[144:147], 0\n" \
"ds_read_b128 a[208:211], v242 offset:20480\n" \
"v_cvt_pk_bf16_f32 v129, v130, v131\n" \
"v_mfma_f32_16x16x32_bf16 v[116:119], a[244:247], v[152:155], v[116:119]\n" \
"ds_read_b128 a[212:215], v242 offset:21504\n" \
"v_mfma_f32_16x16x32_bf16 v[120:123], a[248:251], v[152:155], 0\n" \
"v_mfma_f32_16x16x32_bf16 v[120:123], a[216:219], v[144:147], v[120:123]\n" \
"ds_write_b128 v249, a[120:123] offset:39936\n" \
"v_mfma_f32_16x16x32_bf16 v[124:127], a[220:223], v[144:147], 0\n" \
"v_exp_f32 v132, v132\n" \
"v_mfma_f32_16x16x32_bf16 v[124:127], a[252:255], v[152:155], v[124:127]\n" \
"ds_read_b128 a[216:219], v242 offset:22528\n" \
"ds_read_b128 a[220:223], v242 offset:23552\n" \
"v_mfma_f32_16x16x32_bf16 v[144:147], a[128:131], v[160:163], 0\n" \
"ds_read_b128 a[224:227], v242 offset:24576\n" \
"ds_read_b128 a[228:231], v242 offset:25600\n" \
"v_mfma_f32_16x16x32_bf16 v[144:147], a[132:135], v[164:167], v[144:147]\n" \
"ds_read_b128 a[232:235], v242 offset:26624\n" \
"v_mfma_f32_16x16x32_bf16 v[144:147], a[136:139], v[168:171], v[144:147]\n" \
"v_exp_f32 v133, v133\n" \
"v_mfma_f32_16x16x32_bf16 v[144:147], a[140:143], v[172:175], v[144:147]\n" \
"ds_read_b128 a[236:239], v242 offset:27648\n" \
"v_cvt_pk_bf16_f32 v130, v132, v133\n" \
"v_mfma_f32_16x16x32_bf16 v[148:151], a[156:159], v[172:175], 0\n" \
"ds_read_b128 a[240:243], v242 offset:28672\n" \
"v_mfma_f32_16x16x32_bf16 v[148:151], a[152:155], v[168:171], v[148:151]\n" \
"ds_read_b128 a[244:247], v242 offset:29696\n" \
"v_mfma_f32_16x16x32_bf16 v[148:151], a[148:151], v[164:167], v[148:151]\n" \
"v_mfma_f32_16x16x32_bf16 v[148:151], a[144:147], v[160:163], v[148:151]\n" \
"s_waitcnt lgkmcnt(15)\n" \
"v_exp_f32 v134, v134\n" \
"v_mfma_f32_16x16x32_bf16 v[152:155], a[160:163], v[160:163], 0\n" \
"ds_read_b128 a[248:251], v242 offset:30720\n" \
"v_mfma_f32_16x16x32_bf16 v[152:155], a[164:167], v[164:167], v[152:155]\n" \
"ds_read_b128 a[252:255], v242 offset:31744\n" \
"v_mfma_f32_16x16x32_bf16 v[152:155], a[168:171], v[168:171], v[152:155]\n" \
"v_mfma_f32_16x16x32_bf16 v[152:155], a[172:175], v[172:175], v[152:155]\n" \
"v_exp_f32 v135, v135\n" \
"v_mfma_f32_16x16x32_bf16 v[156:159], a[188:191], v[172:175], 0\n" \
"v_exp_f32 v144, v144\n" \
"v_mfma_f32_16x16x32_bf16 v[156:159], a[184:187], v[168:171], v[156:159]\n" \
"v_exp_f32 v145, v145\n" \
"v_mfma_f32_16x16x32_bf16 v[156:159], a[180:183], v[164:167], v[156:159]\n" \
"v_exp_f32 v146, v146\n" \
"v_cvt_pk_bf16_f32 v131, v134, v135\n" \
"v_mfma_f32_16x16x32_bf16 v[156:159], a[176:179], v[160:163], v[156:159]\n" \
"v_exp_f32 v147, v147\n" \
"v_mfma_f32_16x16x32_bf16 a[36:39], a[48:51], v[128:131], a[36:39]\n" \
"s_waitcnt lgkmcnt(13)\n" \
"v_exp_f32 v148, v148\n" \
"v_mfma_f32_16x16x32_bf16 v[32:35], a[192:195], v[128:131], v[32:35]\n" \
"s_waitcnt vmcnt(2)\n" \
"v_cmp_eq_u32 vcc, 0, v253\n" \
"v_cvt_pk_bf16_f32 v144, v144, v145\n" \
"v_mfma_f32_16x16x32_bf16 v[36:39], a[196:199], v[128:131], v[36:39]\n" \
"v_exp_f32 v149, v149\n" \
"v_mfma_f32_16x16x32_bf16 v[40:43], a[200:203], v[128:131], v[40:43]\n" \
"v_exp_f32 v150, v150\n" \
"v_mfma_f32_16x16x32_bf16 v[44:47], a[204:207], v[128:131], v[44:47]\n" \
"s_waitcnt lgkmcnt(8)\n" \
"s_cbranch_vccnz .Lspw_%=\n" \
".Lsbw_%=:\n" \
"v_mfma_f32_16x16x32_bf16 v[48:51], a[208:211], v[128:131], v[48:51]\n" \
"s_mov_b32 m0, s19\n" \
"v_exp_f32 v151, v151\n" \
"v_mfma_f32_16x16x32_bf16 v[52:55], a[212:215], v[128:131], v[52:55]\n" \
"s_barrier\n" \
"v_exp_f32 v152, v152\n" \
"v_mfma_f32_16x16x32_bf16 v[56:59], a[216:219], v[128:131], v[56:59]\n" \
"buffer_load_dwordx4 v248, s[32:35], s88 offen offset:0 sc1 lds\n" \
"v_mfma_f32_16x16x32_bf16 v[60:63], a[220:223], v[128:131], v[60:63]\n" \
"v_exp_f32 v153, v153\n" \
"v_mfma_f32_16x16x32_bf16 v[128:131], a[128:131], v[176:179], 0\n" \
"v_cmp_gt_u32 s[98:99], 32, v251\n" \
"v_cvt_pk_bf16_f32 v145, v146, v147\n" \
"v_mfma_f32_16x16x32_bf16 v[128:131], a[132:135], v[180:183], v[128:131]\n" \
"v_exp_f32 v154, v154\n" \
"v_mfma_f32_16x16x32_bf16 v[128:131], a[136:139], v[184:187], v[128:131]\n" \
"buffer_load_dwordx4 v248, s[32:35], s88 offen offset:1024 sc1 lds\n" \
"v_mfma_f32_16x16x32_bf16 v[128:131], a[140:143], v[188:191], v[128:131]\n" \
"v_exp_f32 v155, v155\n" \
"v_mfma_f32_16x16x32_bf16 v[132:135], a[156:159], v[188:191], 0\n" \
"v_cndmask_b32 v253, v253, 0, s[98:99]\n" \
"v_cvt_pk_bf16_f32 v146, v148, v149\n" \
"v_mfma_f32_16x16x32_bf16 v[132:135], a[152:155], v[184:187], v[132:135]\n" \
"v_exp_f32 v156, v156\n" \
"v_mfma_f32_16x16x32_bf16 v[132:135], a[148:151], v[180:183], v[132:135]\n" \
"buffer_load_dwordx4 v248, s[32:35], s88 offen offset:2048 sc1 lds\n" \
"v_mfma_f32_16x16x32_bf16 v[132:135], a[144:147], v[176:179], v[132:135]\n" \
"v_exp_f32 v157, v157\n" \
"v_mfma_f32_16x16x32_bf16 v[136:139], a[160:163], v[176:179], 0\n" \
"v_max_u32_dpp v253, v253, v253 quad_perm:[1,0,3,2] row_mask:0xf bank_mask:0xf\n" \
"v_cvt_pk_bf16_f32 v147, v150, v151\n" \
"v_mfma_f32_16x16x32_bf16 v[136:139], a[164:167], v[180:183], v[136:139]\n" \
"v_exp_f32 v158, v158\n" \
"v_mfma_f32_16x16x32_bf16 v[136:139], a[168:171], v[184:187], v[136:139]\n" \
"buffer_load_dwordx4 v248, s[32:35], s88 offen offset:3072 sc1 lds\n" \
"v_mfma_f32_16x16x32_bf16 v[136:139], a[172:175], v[188:191], v[136:139]\n" \
"s_add_u32 s89, s88, 589824\n" \
"v_exp_f32 v159, v159\n" \
"v_mfma_f32_16x16x32_bf16 v[140:143], a[188:191], v[188:191], 0\n" \
"s_add_u32 s88, s88, s27\n" \
"v_exp_f32 v128, v128\n" \
"v_mfma_f32_16x16x32_bf16 v[140:143], a[184:187], v[184:187], v[140:143]\n" \
"s_add_u32 s19, s19, 0x4000\n" \
"v_exp_f32 v129, v129\n" \
"v_mfma_f32_16x16x32_bf16 v[140:143], a[180:183], v[180:183], v[140:143]\n" \
"s_bitset0_b32 s19, 16\n" \
"v_exp_f32 v130, v130\n" \
"v_mfma_f32_16x16x32_bf16 v[140:143], a[176:179], v[176:179], v[140:143]\n" \
"s_cmp_eq_u32 s88, s16\n" \
"v_exp_f32 v131, v131\n" \
"v_cvt_pk_bf16_f32 v152, v152, v153\n" \
"v_cvt_pk_bf16_f32 v153, v154, v155\n" \
"v_cvt_pk_bf16_f32 v154, v156, v157\n" \
"v_cvt_pk_bf16_f32 v155, v158, v159\n" \
"v_mfma_f32_16x16x32_bf16 a[40:43], a[48:51], v[144:147], a[40:43]\n" \
"s_cmov_b32 s88, s21\n" \
"v_exp_f32 v132, v132\n" \
"v_mfma_f32_16x16x32_bf16 a[40:43], a[48:51], v[152:155], a[40:43]\n" \
"s_waitcnt lgkmcnt(4)\n" \
"v_exp_f32 v133, v133\n" \
"v_mfma_f32_16x16x32_bf16 v[64:67], a[224:227], v[152:155], v[64:67]\n" \
"s_cmov_b32 s27, s91\n" \
"v_exp_f32 v134, v134\n" \
"v_mfma_f32_16x16x32_bf16 v[64:67], a[192:195], v[144:147], v[64:67]\n" \
"s_cmov_b32 s16, s77\n" \
"v_max_u32_dpp v253, v253, v253 quad_perm:[2,3,0,1] row_mask:0xf bank_mask:0xf\n" \
"v_cvt_pk_bf16_f32 v128, v128, v129\n" \
"v_mfma_f32_16x16x32_bf16 v[68:71], a[196:199], v[144:147], v[68:71]\n" \
"s_cmov_b32 s21, s92\n" \
"v_exp_f32 v135, v135\n" \
"v_mfma_f32_16x16x32_bf16 v[68:71], a[228:231], v[152:155], v[68:71]\n" \
"s_cmovk_i32 s91, 0x4000\n" \
"v_max_u32_dpp v253, v253, v253 row_half_mirror row_mask:0xf bank_mask:0xf\n" \
"v_cvt_pk_bf16_f32 v129, v130, v131\n" \
"v_mfma_f32_16x16x32_bf16 v[72:75], a[232:235], v[152:155], v[72:75]\n" \
"s_cmp_eq_u32 s88, s17\n" \
"v_cvt_pk_bf16_f32 v130, v132, v133\n" \
"v_cvt_pk_bf16_f32 v131, v134, v135\n" \
"v_mfma_f32_16x16x32_bf16 v[72:75], a[200:203], v[144:147], v[72:75]\n" \
"s_cmov_b32 s88, s18\n" \
"v_exp_f32 v136, v136\n" \
"v_mfma_f32_16x16x32_bf16 v[76:79], a[204:207], v[144:147], v[76:79]\n" \
"s_mov_b32 m0, s20\n" \
"v_max_u32_dpp v253, v253, v253 row_mirror row_mask:0xf bank_mask:0xf\n" \
"v_mov_b32 v254, v253\n" \
"v_mfma_f32_16x16x32_bf16 v[76:79], a[236:239], v[152:155], v[76:79]\n" \
"s_waitcnt lgkmcnt(0)\n" \
"v_exp_f32 v137, v137\n" \
"v_mfma_f32_16x16x32_bf16 v[80:83], a[240:243], v[152:155], v[80:83]\n" \
"buffer_load_dwordx4 v248, s[32:35], s89 offen offset:0 sc1 lds\n" \
"v_mfma_f32_16x16x32_bf16 v[80:83], a[208:211], v[144:147], v[80:83]\n" \
"ds_read_b128 a[128:131], v248 offset:32768\n" \
"ds_read_b128 a[132:135], v248 offset:33792\n" \
"ds_read_b128 a[136:139], v248 offset:34816\n" \
"ds_read_b128 a[140:143], v248 offset:35840\n" \
"v_exp_f32 v138, v138\n" \
"v_mfma_f32_16x16x32_bf16 v[84:87], a[212:215], v[144:147], v[84:87]\n" \
"buffer_load_dwordx4 v248, s[32:35], s89 offen offset:1024 sc1 lds\n" \
"v_mfma_f32_16x16x32_bf16 v[84:87], a[244:247], v[152:155], v[84:87]\n" \
"v_exp_f32 v139, v139\n" \
"v_mfma_f32_16x16x32_bf16 v[88:91], a[248:251], v[152:155], v[88:91]\n" \
"buffer_load_dwordx4 v248, s[32:35], s89 offen offset:2048 sc1 lds\n" \
"v_mfma_f32_16x16x32_bf16 v[88:91], a[216:219], v[144:147], v[88:91]\n" \
"ds_read_b128 a[156:159], v248 offset:39936\n" \
"ds_read_b128 a[152:155], v248 offset:38912\n" \
"ds_read_b128 a[148:151], v248 offset:37888\n" \
"ds_read_b128 a[144:147], v248 offset:36864\n" \
"v_exp_f32 v140, v140\n" \
"v_mfma_f32_16x16x32_bf16 v[92:95], a[220:223], v[144:147], v[92:95]\n" \
"buffer_load_dwordx4 v248, s[32:35], s89 offen offset:3072 sc1 lds\n" \
"v_mfma_f32_16x16x32_bf16 v[92:95], a[252:255], v[152:155], v[92:95]\n" \
"s_waitcnt lgkmcnt(4)\n" \
"v_exp_f32 v141, v141\n" \
"v_mfma_f32_16x16x32_bf16 v[144:147], a[128:131], v[176:179], v[232:235]\n" \
"s_add_u32 s20, s20, 0x4000\n" \
"v_permlane16_swap_b32 v253, v254\n" \
"v_mfma_f32_16x16x32_bf16 v[144:147], a[132:135], v[180:183], v[144:147]\n" \
"s_and_b32 s20, s20, 0xffff\n" \
"v_exp_f32 v142, v142\n" \
"v_mfma_f32_16x16x32_bf16 v[144:147], a[136:139], v[184:187], v[144:147]\n" \
"s_bitset1_b32 s20, 16\n" \
"v_max_u32 v253, v253, v254\n" \
"v_mov_b32 v254, v253\n" \
"v_mfma_f32_16x16x32_bf16 v[144:147], a[140:143], v[188:191], v[144:147]\n" \
"s_waitcnt lgkmcnt(0)\n" \
"v_exp_f32 v143, v143\n" \
"v_mfma_f32_16x16x32_bf16 v[148:151], a[156:159], v[188:191], v[236:239]\n" \
"s_mov_b32 m0, s19\n" \
"v_permlane32_swap_b32 v253, v254\n" \
"v_mfma_f32_16x16x32_bf16 v[148:151], a[152:155], v[184:187], v[148:151]\n" \
"buffer_load_dwordx4 v248, s[32:35], s88 offen offset:0 sc1 lds\n" \
"v_mfma_f32_16x16x32_bf16 v[148:151], a[148:151], v[180:183], v[148:151]\n" \
"s_lshl_b32 s85, s68, 3\n" \
"v_exp_f32 v144, v144\n" \
"v_mfma_f32_16x16x32_bf16 v[148:151], a[144:147], v[176:179], v[148:151]\n" \
"s_add_u32 s85, s85, s67\n" \
"buffer_load_dwordx4 v248, s[32:35], s88 offen offset:1024 sc1 lds\n" \
"v_cvt_pk_bf16_f32 v136, v136, v137\n" \
"v_cvt_pk_bf16_f32 v137, v138, v139\n" \
"v_cvt_pk_bf16_f32 v138, v140, v141\n" \
"v_cvt_pk_bf16_f32 v139, v142, v143\n" \
"v_mfma_f32_16x16x32_bf16 a[44:47], a[48:51], v[128:131], a[44:47]\n" \
"s_barrier\n" \
"buffer_load_dwordx4 v248, s[32:35], s88 offen offset:2048 sc1 lds\n" \
"v_mfma_f32_16x16x32_bf16 a[44:47], a[48:51], v[136:139], a[44:47]\n" \
"s_lshl2_add_u32 s85, s85, %[wid]\n" \
"buffer_load_dwordx4 v248, s[32:35], s88 offen offset:3072 sc1 lds\n" \
"v_mfma_f32_16x16x32_bf16 v[96:99], a[224:227], v[136:139], v[96:99]\n" \
"s_add_u32 s89, s88, 589824\n" \
"v_exp_f32 v145, v145\n" \
"v_mfma_f32_16x16x32_bf16 v[96:99], a[192:195], v[128:131], v[96:99]\n" \
"s_add_u32 s88, s88, s27\n" \
"v_max_u32 v253, v253, v254\n" \
"v_max_f32 v254, 1.0, v253\n" \
"v_mfma_f32_16x16x32_bf16 v[100:103], a[196:199], v[128:131], v[100:103]\n" \
"s_add_u32 s19, s19, 0x4000\n" \
"v_min_f32 v253, 1.0, v253\n" \
"v_cvt_pk_bf16_f32 v144, v144, v145\n" \
"v_mfma_f32_16x16x32_bf16 v[100:103], a[228:231], v[136:139], v[100:103]\n" \
"s_bitset0_b32 s19, 16\n" \
"v_rcp_f32 v254, v254\n" \
"v_mfma_f32_16x16x32_bf16 v[104:107], a[232:235], v[136:139], v[104:107]\n" \
"s_cmp_eq_u32 s88, s16\n" \
"v_rcp_f32 v253, v253\n" \
"v_mfma_f32_16x16x32_bf16 v[104:107], a[200:203], v[128:131], v[104:107]\n" \
"s_cmov_b32 s88, s21\n" \
"v_exp_f32 v146, v146\n" \
"v_mfma_f32_16x16x32_bf16 v[108:111], a[204:207], v[128:131], v[108:111]\n" \
"s_cmov_b32 s27, s91\n" \
"v_mul_f32 v250, 0x7e000000, v254\n" \
"v_mul_f32 v246, 0x0d800000, v253\n" \
"v_mfma_f32_16x16x32_bf16 v[108:111], a[236:239], v[136:139], v[108:111]\n" \
"s_cmov_b32 s16, s77\n" \
"ds_read_b128 a[128:131], v248 offset:49152\n" \
"ds_read_b128 a[132:135], v248 offset:50176\n" \
"v_mfma_f32_16x16x32_bf16 v[112:115], a[240:243], v[136:139], v[112:115]\n" \
"s_cmov_b32 s21, s92\n" \
"ds_read_b128 a[136:139], v248 offset:51200\n" \
"ds_read_b128 a[140:143], v248 offset:52224\n" \
"v_mfma_f32_16x16x32_bf16 v[112:115], a[208:211], v[128:131], v[112:115]\n" \
"s_cmovk_i32 s91, 0x4000\n" \
"v_exp_f32 v147, v147\n" \
"v_mfma_f32_16x16x32_bf16 v[116:119], a[212:215], v[128:131], v[116:119]\n" \
"s_cmp_eq_u32 s88, s17\n" \
"ds_read_b128 a[156:159], v248 offset:56320\n" \
"v_cvt_pk_bf16_f32 v145, v146, v147\n" \
"v_mfma_f32_16x16x32_bf16 v[116:119], a[244:247], v[136:139], v[116:119]\n" \
"s_cmov_b32 s88, s18\n" \
"ds_read_b128 a[152:155], v248 offset:55296\n" \
"ds_read_b128 a[148:151], v248 offset:54272\n" \
"v_mfma_f32_16x16x32_bf16 v[120:123], a[248:251], v[136:139], v[120:123]\n" \
".Lst0_15_%=:\n" \
"s_cbranch_execz .Lstb0_%=\n" \
"s_mov_b32 m0, s20\n" \
"ds_read_b128 a[144:147], v248 offset:53248\n" \
"ds_read_b128 a[160:163], v248 offset:57344\n" \
"v_mfma_f32_16x16x32_bf16 v[120:123], a[216:219], v[128:131], v[120:123]\n" \
"s_lshl_b32 s85, s85, 2\n" \
"buffer_load_dwordx4 v248, s[32:35], s89 offen offset:0 sc1 lds\n" \
"v_mfma_f32_16x16x32_bf16 v[124:127], a[220:223], v[128:131], v[124:127]\n" \
"s_add_u32 s85, s85, 655360\n" \
"v_exp_f32 v148, v148\n" \
"v_mfma_f32_16x16x32_bf16 v[124:127], a[252:255], v[136:139], v[124:127]\n" \
"s_waitcnt lgkmcnt(5)\n" \
"buffer_load_dwordx4 v248, s[32:35], s89 offen offset:1024 sc1 lds\n" \
"v_mfma_f32_16x16x32_bf16 v[128:131], a[128:131], v[160:163], 0\n" \
"buffer_load_dwordx4 v248, s[32:35], s89 offen offset:2048 sc1 lds\n" \
"v_mfma_f32_16x16x32_bf16 v[128:131], a[132:135], v[164:167], v[128:131]\n" \
"buffer_load_dwordx4 v248, s[32:35], s89 offen offset:3072 sc1 lds\n" \
"ds_read_b128 a[164:167], v248 offset:58368\n" \
"ds_read_b128 a[168:171], v248 offset:59392\n" \
"ds_read_b128 a[172:175], v248 offset:60416\n" \
"v_mfma_f32_16x16x32_bf16 v[128:131], a[136:139], v[168:171], v[128:131]\n" \
"s_add_u32 s20, s20, 0x4000\n" \
"v_exp_f32 v149, v149\n" \
"v_mfma_f32_16x16x32_bf16 v[128:131], a[140:143], v[172:175], v[128:131]\n" \
"s_waitcnt vmcnt(16) lgkmcnt(4)\n" \
"buffer_store_dword a56, off, s[32:35], s85 sc1\n" \
"v_mfma_f32_16x16x32_bf16 v[132:135], a[156:159], v[172:175], 0\n" \
"s_and_b32 s20, s20, 0xffff\n" \
"ds_read_b128 a[192:195], v242 offset:32768\n" \
"v_cvt_pk_bf16_f32 v146, v148, v149\n" \
"v_mfma_f32_16x16x32_bf16 v[132:135], a[152:155], v[168:171], v[132:135]\n" \
"s_bitset1_b32 s20, 16\n" \
"ds_read_b128 a[196:199], v242 offset:33792\n" \
"ds_read_b128 a[188:191], v248 offset:64512\n" \
"ds_read_b128 a[200:203], v242 offset:34816\n" \
"ds_read_b128 a[204:207], v242 offset:35840\n" \
"ds_read_b128 a[184:187], v248 offset:63488\n" \
"ds_read_b128 a[180:183], v248 offset:62464\n" \
"ds_read_b128 a[176:179], v248 offset:61440\n" \
"v_mfma_f32_16x16x32_bf16 v[132:135], a[148:151], v[164:167], v[132:135]\n" \
"s_mov_b32 m0, s19\n" \
"ds_read_b128 a[208:211], v242 offset:36864\n" \
"ds_read_b128 a[212:215], v242 offset:37888\n" \
"v_mfma_f32_16x16x32_bf16 v[132:135], a[144:147], v[160:163], v[132:135]\n" \
"s_waitcnt lgkmcnt(10)\n" \
"v_exp_f32 v150, v150\n" \
"v_mfma_f32_16x16x32_bf16 v[136:139], a[160:163], v[160:163], v[224:227]\n" \
"buffer_load_dwordx4 v248, s[32:35], s88 offen offset:0 sc1 lds\n" \
"v_mfma_f32_16x16x32_bf16 v[136:139], a[164:167], v[164:167], v[136:139]\n" \
"buffer_load_dwordx4 v248, s[32:35], s88 offen offset:1024 sc1 lds\n" \
"v_mfma_f32_16x16x32_bf16 v[136:139], a[168:171], v[168:171], v[136:139]\n" \
"ds_read_b128 a[216:219], v242 offset:38912\n" \
"ds_read_b128 a[220:223], v242 offset:39936\n" \
"v_mfma_f32_16x16x32_bf16 v[136:139], a[172:175], v[172:175], v[136:139]\n" \
"s_waitcnt lgkmcnt(4)\n" \
"v_exp_f32 v151, v151\n" \
"v_mfma_f32_16x16x32_bf16 v[140:143], a[188:191], v[172:175], v[228:231]\n" \
"v_exp_f32 v128, v128\n" \
"v_mfma_f32_16x16x32_bf16 v[140:143], a[184:187], v[168:171], v[140:143]\n" \
"v_exp_f32 v129, v129\n" \
"v_mfma_f32_16x16x32_bf16 v[140:143], a[180:183], v[164:167], v[140:143]\n" \
"v_exp_f32 v130, v130\n" \
"v_cvt_pk_bf16_f32 v147, v150, v151\n" \
"v_mfma_f32_16x16x32_bf16 v[140:143], a[176:179], v[160:163], v[140:143]\n" \
"v_exp_f32 v131, v131\n" \
"v_mfma_f32_16x16x32_bf16 a[44:47], a[48:51], v[144:147], a[44:47]\n" \
"v_exp_f32 v132, v132\n" \
"v_mfma_f32_16x16x32_bf16 v[96:99], a[192:195], v[144:147], v[96:99]\n" \
"v_exp_f32 v133, v133\n" \
"v_mfma_f32_16x16x32_bf16 v[100:103], a[196:199], v[144:147], v[100:103]\n" \
"v_exp_f32 v134, v134\n" \
"v_mfma_f32_16x16x32_bf16 v[104:107], a[200:203], v[144:147], v[104:107]\n" \
"buffer_load_dwordx4 v248, s[32:35], s88 offen offset:2048 sc1 lds\n" \
"v_mfma_f32_16x16x32_bf16 v[108:111], a[204:207], v[144:147], v[108:111]\n" \
"s_waitcnt lgkmcnt(0)\n" \
"v_exp_f32 v135, v135\n" \
"v_mfma_f32_16x16x32_bf16 v[112:115], a[208:211], v[144:147], v[112:115]\n" \
"buffer_load_dwordx4 v248, s[32:35], s88 offen offset:3072 sc1 lds\n" \
"v_mfma_f32_16x16x32_bf16 v[116:119], a[212:215], v[144:147], v[116:119]\n" \
"s_add_u32 s89, s88, 589824\n" \
"v_exp_f32 v136, v136\n" \
"v_mfma_f32_16x16x32_bf16 v[120:123], a[216:219], v[144:147], v[120:123]\n" \
"s_add_u32 s88, s88, s27\n" \
"v_cvt_pk_bf16_f32 v128, v128, v129\n" \
"v_cvt_pk_bf16_f32 v129, v130, v131\n" \
"v_mfma_f32_16x16x32_bf16 v[124:127], a[220:223], v[144:147], v[124:127]\n" \
"s_add_u32 s19, s19, 0x4000\n" \
"v_exp_f32 v137, v137\n" \
"v_mfma_f32_16x16x32_bf16 v[144:147], a[128:131], v[176:179], 0\n" \
"s_waitcnt vmcnt(13)\n" \
"s_barrier\n" \
"v_cvt_pk_bf16_f32 v130, v132, v133\n" \
"v_mfma_f32_16x16x32_bf16 v[144:147], a[132:135], v[180:183], v[144:147]\n" \
"s_bitset0_b32 s19, 16\n" \
"v_exp_f32 v138, v138\n" \
"v_mfma_f32_16x16x32_bf16 v[144:147], a[136:139], v[184:187], v[144:147]\n" \
"s_cmp_eq_u32 s88, s16\n" \
"v_cvt_pk_bf16_f32 v131, v134, v135\n" \
"v_cvt_pk_bf16_f32 v136, v136, v137\n" \
"v_mfma_f32_16x16x32_bf16 v[144:147], a[140:143], v[188:191], v[144:147]\n" \
"s_cmov_b32 s88, s21\n" \
"v_exp_f32 v139, v139\n" \
"v_mfma_f32_16x16x32_bf16 v[148:151], a[156:159], v[188:191], 0\n" \
"s_cmov_b32 s27, s91\n" \
"ds_read_b128 a[224:227], v242 offset:57344\n" \
"v_cvt_pk_bf16_f32 v137, v138, v139\n" \
"v_mfma_f32_16x16x32_bf16 v[148:151], a[152:155], v[184:187], v[148:151]\n" \
"s_cmov_b32 s16, s77\n" \
"v_exp_f32 v140, v140\n" \
"v_mfma_f32_16x16x32_bf16 v[148:151], a[148:151], v[180:183], v[148:151]\n" \
"s_cmov_b32 s21, s92\n" \
"ds_read_b128 a[192:195], v242 offset:49152\n" \
"ds_read_b128 a[196:199], v242 offset:50176\n" \
"v_mfma_f32_16x16x32_bf16 v[148:151], a[144:147], v[176:179], v[148:151]\n" \
"s_cmovk_i32 s91, 0x4000\n" \
"v_exp_f32 v141, v141\n" \
"v_mfma_f32_16x16x32_bf16 v[152:155], a[160:163], v[176:179], 0\n" \
"s_cmp_eq_u32 s88, s17\n" \
"ds_read_b128 a[228:231], v242 offset:58368\n" \
"v_cvt_pk_bf16_f32 v138, v140, v141\n" \
"v_mfma_f32_16x16x32_bf16 v[152:155], a[164:167], v[180:183], v[152:155]\n" \
"s_cmov_b32 s88, s18\n" \
"v_exp_f32 v142, v142\n" \
"v_mfma_f32_16x16x32_bf16 v[152:155], a[168:171], v[184:187], v[152:155]\n" \
"s_mov_b32 m0, s20\n" \
"ds_read_b128 a[232:235], v242 offset:59392\n" \
"ds_read_b128 a[200:203], v242 offset:51200\n" \
"v_mfma_f32_16x16x32_bf16 v[152:155], a[172:175], v[188:191], v[152:155]\n" \
"v_exp_f32 v143, v143\n" \
"v_mfma_f32_16x16x32_bf16 v[156:159], a[188:191], v[188:191], 0\n" \
"buffer_load_dwordx4 v248, s[32:35], s89 offen offset:0 sc1 lds\n" \
"v_mfma_f32_16x16x32_bf16 v[156:159], a[184:187], v[184:187], v[156:159]\n" \
"buffer_load_dwordx4 v248, s[32:35], s89 offen offset:1024 sc1 lds\n" \
"v_mfma_f32_16x16x32_bf16 v[156:159], a[180:183], v[180:183], v[156:159]\n" \
"v_exp_f32 v144, v144\n" \
"v_mfma_f32_16x16x32_bf16 v[156:159], a[176:179], v[176:179], v[156:159]\n" \
"buffer_load_dwordx4 v248, s[32:35], s89 offen offset:2048 sc1 lds\n" \
"ds_read_b128 a[204:207], v242 offset:52224\n" \
"ds_read_b128 a[236:239], v242 offset:60416\n" \
"v_cvt_pk_bf16_f32 v139, v142, v143\n" \
"v_mfma_f32_16x16x32_bf16 a[40:43], a[48:51], v[128:131], a[40:43]\n" \
"v_exp_f32 v145, v145\n" \
"v_mfma_f32_16x16x32_bf16 a[40:43], a[48:51], v[136:139], a[40:43]\n" \
"s_waitcnt lgkmcnt(4)\n" \
"ds_read_b128 a[240:243], v242 offset:61440\n" \
"v_cvt_pk_bf16_f32 v144, v144, v145\n" \
"v_mfma_f32_16x16x32_bf16 v[64:67], a[224:227], v[136:139], v[64:67]\n" \
"v_exp_f32 v146, v146\n" \
"v_mfma_f32_16x16x32_bf16 v[64:67], a[192:195], v[128:131], v[64:67]\n" \
"ds_read_b128 a[208:211], v242 offset:53248\n" \
"ds_read_b128 a[128:131], v248 offset:0\n" \
"ds_read_b128 a[212:215], v242 offset:54272\n" \
"ds_read_b128 a[132:135], v248 offset:1024\n" \
"ds_read_b128 a[244:247], v242 offset:62464\n" \
"v_mfma_f32_16x16x32_bf16 v[68:71], a[196:199], v[128:131], v[68:71]\n" \
"v_exp_f32 v147, v147\n" \
"v_mfma_f32_16x16x32_bf16 v[68:71], a[228:231], v[136:139], v[68:71]\n" \
"s_waitcnt lgkmcnt(6)\n" \
"buffer_load_dwordx4 v248, s[32:35], s89 offen offset:3072 sc1 lds\n" \
"v_mfma_f32_16x16x32_bf16 v[72:75], a[232:235], v[136:139], v[72:75]\n" \
"s_add_u32 s20, s20, 0x4000\n" \
"v_exp_f32 v148, v148\n" \
"v_mfma_f32_16x16x32_bf16 v[72:75], a[200:203], v[128:131], v[72:75]\n" \
"s_and_b32 s20, s20, 0xffff\n" \
"ds_read_b128 a[136:139], v248 offset:2048\n" \
"ds_read_b128 a[140:143], v248 offset:3072\n" \
"ds_read_b128 a[248:251], v242 offset:63488\n" \
"ds_read_b128 a[216:219], v242 offset:55296\n" \
"ds_read_b128 a[156:159], v248 offset:7168\n" \
"ds_read_b128 a[220:223], v242 offset:56320\n" \
"ds_read_b128 a[152:155], v248 offset:6144\n" \
"ds_read_b128 a[252:255], v242 offset:64512\n" \
"v_cvt_pk_bf16_f32 v145, v146, v147\n" \
"v_mfma_f32_16x16x32_bf16 v[76:79], a[204:207], v[128:131], v[76:79]\n" \
"s_bitset1_b32 s20, 16\n" \
"v_exp_f32 v149, v149\n" \
"v_mfma_f32_16x16x32_bf16 v[76:79], a[236:239], v[136:139], v[76:79]\n" \
"s_waitcnt lgkmcnt(8)\n" \
"ds_read_b128 a[148:151], v248 offset:5120\n" \
"v_cvt_pk_bf16_f32 v146, v148, v149\n" \
"v_mfma_f32_16x16x32_bf16 v[80:83], a[240:243], v[136:139], v[80:83]\n" \
"v_exp_f32 v150, v150\n" \
"v_mfma_f32_16x16x32_bf16 v[80:83], a[208:211], v[128:131], v[80:83]\n" \
"ds_read_b128 a[144:147], v248 offset:4096\n" \
"ds_read_b128 a[160:163], v248 offset:8192\n" \
"v_mfma_f32_16x16x32_bf16 v[84:87], a[212:215], v[128:131], v[84:87]\n" \
"v_exp_f32 v151, v151\n" \
"v_mfma_f32_16x16x32_bf16 v[84:87], a[244:247], v[136:139], v[84:87]\n" \
"s_waitcnt lgkmcnt(3)\n" \
"ds_read_b128 a[164:167], v248 offset:9216\n" \
"v_cvt_pk_bf16_f32 v147, v150, v151\n" \
"v_mfma_f32_16x16x32_bf16 v[88:91], a[248:251], v[136:139], v[88:91]\n" \
"ds_read_b128 a[168:171], v248 offset:10240\n" \
"ds_read_b128 a[172:175], v248 offset:11264\n" \
"v_mfma_f32_16x16x32_bf16 v[88:91], a[216:219], v[128:131], v[88:91]\n" \
"v_exp_f32 v152, v152\n" \
"v_mfma_f32_16x16x32_bf16 v[92:95], a[220:223], v[128:131], v[92:95]\n" \
"v_exp_f32 v153, v153\n" \
"v_mfma_f32_16x16x32_bf16 v[92:95], a[252:255], v[136:139], v[92:95]\n" \
"v_and_b32 v230, 7, v251\n" \
"v_lshrrev_b32 v232, 3, v251\n" \
"v_lshlrev_b32 v231, 4, v230\n" \
"v_mul_u32_u24 v230, 7168, v230\n" \
"v_lshl_add_u32 v230, v232, 4, v230\n" \
"v_lshl_or_b32 v232, v232, 14, v231\n" \
"v_and_b32 v231, 3, v251\n" \
"v_lshlrev_b32 v231, 7, v231\n" \
"s_branch .Ldj_%=\n" \
".Lspq_%=:\n" \
"s_mov_b32 s82, 0x100000\n" \
".Lsqq_%=:\n" \
"s_sleep 6\n" \
"buffer_load_dwordx2 v[204:205], v253, s[32:35], s81 offen sc1\n" \
"s_waitcnt vmcnt(0)\n" \
"v_cmp_ne_u64 vcc, v[204:205], v[202:203]\n" \
"s_cbranch_vccz .Lsbq_%=\n" \
"s_sub_u32 s82, s82, 1\n" \
"s_cbranch_scc0 .Lsqq_%=\n" \
"s_branch .Lsbq_%=\n" \
".Lsph_%=:\n" \
"s_mov_b32 s82, 0x100000\n" \
".Lsqh_%=:\n" \
"s_sleep 6\n" \
"buffer_load_dword v247, v252, s[32:35], s81 offen sc1\n" \
"s_waitcnt vmcnt(0)\n" \
"v_cmp_eq_u32 vcc, 0, v247\n" \
"s_cbranch_vccz .Lsbh_%=\n" \
"s_sub_u32 s82, s82, 1\n" \
"s_cbranch_scc0 .Lsqh_%=\n" \
"s_branch .Lsbh_%=\n" \
".Lspv_%=:\n" \
"s_mov_b32 s82, 0x100000\n" \
".Lsqv_%=:\n" \
"s_sleep 6\n" \
"buffer_load_dword v253, v252, s[32:35], s81 offen sc1\n" \
"s_waitcnt vmcnt(0)\n" \
"v_cmp_eq_u32 vcc, 0, v253\n" \
"s_cbranch_vccz .Lsbv_%=\n" \
"s_sub_u32 s82, s82, 1\n" \
"s_cbranch_scc0 .Lsqv_%=\n" \
"s_branch .Lsbv_%=\n" \
".Lspw_%=:\n" \
"s_mov_b32 s82, 0x100000\n" \
".Lsqw_%=:\n" \
"s_sleep 6\n" \
"buffer_load_dword v253, v252, s[32:35], s81 offen sc1\n" \
"s_waitcnt vmcnt(0)\n" \
"v_cmp_eq_u32 vcc, 0, v253\n" \
"s_cbranch_vccz .Lsbw_%=\n" \
"s_sub_u32 s82, s82, 1\n" \
"s_cbranch_scc0 .Lsqw_%=\n" \
"s_branch .Lsbw_%=\n" \
".Lchd_%=:\n" \
".Lst1_8_%=:\n" \
"s_cbranch_execz .Lst1_12_%=\n" \
"v_cmp_eq_u32 vcc, 0, v247\n" \
"s_sub_u32 s23, s18, 262144\n" \
"s_cmp_eq_u32 s24, s23\n" \
"s_cbranch_scc1 .Ldhd_%=\n" \
"s_mov_b32 s24, s23\n" \
"buffer_load_dword v247, v244, s[32:35], s26 offen sc1\n" \
"s_branch .Lsbhd_%=\n" \
".Ldhd_%=:\n" \
"s_cbranch_vccz .Lsbhd_%=\n" \
".Lsphd_%=:\n" \
"s_mov_b32 s82, 0x100000\n" \
".Lsqhd_%=:\n" \
"s_sleep 6\n" \
"buffer_load_dword v247, v244, s[32:35], s26 offen sc1\n" \
"s_waitcnt vmcnt(0)\n" \
"v_cmp_eq_u32 vcc, 0, v247\n" \
"s_cbranch_vccz .Lsbhd_%=\n" \
"s_sub_u32 s82, s82, 1\n" \
"s_cbranch_scc0 .Lsqhd_%=\n" \
"s_branch .Lsbhd_%=\n" \
".Land_%=:\n" \
".Lst1_9_%=:\n" \
"s_cbranch_execz .Lst1_13_%=\n" \
"s_waitcnt vmcnt(4)\n" \
"buffer_store_dword a56, off, s[32:35], s25 sc1\n" \
"buffer_store_dword a56, off, s[32:35], s78 sc1\n" \
"s_mov_b32 s78, 1073741824\n" \
"s_mov_b32 s76, 0\n" \
"s_branch .Labd_%=\n" \
".Lchtx_%=:\n" \
".Lst1_10_%=:\n" \
"s_cbranch_execz .Lst1_14_%=\n" \
"v_cmp_eq_u32 vcc, 0, v247\n" \
"s_sub_u32 s23, s18, 262144\n" \
"s_cmp_eq_u32 s24, s23\n" \
"s_cbranch_scc1 .Ldhtx_%=\n" \
"s_mov_b32 s24, s23\n" \
"buffer_load_dword v247, v244, s[32:35], s26 offen sc1\n" \
"s_branch .Lsbhtx_%=\n" \
".Ldhtx_%=:\n" \
"s_cbranch_vccz .Lsbhtx_%=\n" \
".Lsphtx_%=:\n" \
"s_mov_b32 s82, 0x100000\n" \
".Lsqhtx_%=:\n" \
"s_sleep 6\n" \
"buffer_load_dword v247, v244, s[32:35], s26 offen sc1\n" \
"s_waitcnt vmcnt(0)\n" \
"v_cmp_eq_u32 vcc, 0, v247\n" \
"s_cbranch_vccz .Lsbhtx_%=\n" \
"s_sub_u32 s82, s82, 1\n" \
"s_cbranch_scc0 .Lsqhtx_%=\n" \
"s_branch .Lsbhtx_%=\n" \
".Lantx_%=:\n" \
".Lst1_11_%=:\n" \
"s_cbranch_execz .Lstb1_%=\n" \
"s_waitcnt vmcnt(10)\n" \
"buffer_store_dword a56, off, s[32:35], s25 sc1\n" \
"buffer_store_dword a56, off, s[32:35], s78 sc1\n" \
"s_mov_b32 s78, 1073741824\n" \
"s_mov_b32 s76, 0\n" \
"s_branch .Labtx_%=\n" \
".Lcht_%=:\n" \
".Lst1_12_%=:\n" \
"s_cbranch_execz .Lstb1_%=\n" \
"v_cmp_eq_u32 vcc, 0, v247\n" \
"s_sub_u32 s23, s18, 262144\n" \
"s_cmp_eq_u32 s24, s23\n" \
"s_cbranch_scc1 .Ldht_%=\n" \
"s_mov_b32 s24, s23\n" \
"buffer_load_dword v247, v244, s[32:35], s26 offen sc1\n" \
"s_branch .Lsbht_%=\n" \
".Ldht_%=:\n" \
"s_cbranch_vccz .Lsbht_%=\n" \
".Lspht_%=:\n" \
"s_mov_b32 s82, 0x100000\n" \
".Lsqht_%=:\n" \
"s_sleep 6\n" \
"buffer_load_dword v247, v244, s[32:35], s26 offen sc1\n" \
"s_waitcnt vmcnt(0)\n" \
"v_cmp_eq_u32 vcc, 0, v247\n" \
"s_cbranch_vccz .Lsbht_%=\n" \
"s_sub_u32 s82, s82, 1\n" \
"s_cbranch_scc0 .Lsqht_%=\n" \
"s_branch .Lsbht_%=\n" \
".Lant_%=:\n" \
".Lst1_13_%=:\n" \
"s_cbranch_execz .Lstb1_%=\n" \
"s_waitcnt vmcnt(9)\n" \
"buffer_store_dword a56, off, s[32:35], s25 sc1\n" \
"buffer_store_dword a56, off, s[32:35], s78 sc1\n" \
"s_mov_b32 s78, 1073741824\n" \
"s_mov_b32 s76, 0\n" \
"s_branch .Labt_%=\n" \
".Lspl_%=:\n" \
"s_mov_b32 s82, 0x100000\n" \
".Lsql_%=:\n" \
"s_sleep 6\n" \
"buffer_load_dword v247, v255, s[32:35], s31 offen sc1\n" \
"s_waitcnt vmcnt(0)\n" \
"v_cmp_eq_u32 vcc, 0, v247\n" \
"s_cbranch_vccz .Lsbl_%=\n" \
"s_sub_u32 s82, s82, 1\n" \
"s_cbranch_scc0 .Lsql_%=\n" \
"s_branch .Lsbl_%=\n" \
".Lrh_%=:\n" \
"s_call_b64 s[98:99], .Lredo2_%=\n" \
"v_accvgpr_read_b32 v224, a40\n" \
"v_rcp_f32 v224, v224\n" \
"s_nop 0\n" \
"v_mul_f32 v64, v64, v224\n" \
"v_mul_f32 v65, v65, v224\n" \
"v_mul_f32 v66, v66, v224\n" \
"v_mul_f32 v67, v67, v224\n" \
"v_mul_f32 v68, v68, v224\n" \
"v_mul_f32 v69, v69, v224\n" \
"v_mul_f32 v70, v70, v224\n" \
"v_mul_f32 v71, v71, v224\n" \
"v_cvt_pk_bf16_f32 v192, v64, v65\n" \
"v_cvt_pk_bf16_f32 v193, v66, v67\n" \
"v_cvt_pk_bf16_f32 v194, v68, v69\n" \
"v_cvt_pk_bf16_f32 v195, v70, v71\n" \
"v_mul_f32 v72, v72, v224\n" \
"v_mul_f32 v73, v73, v224\n" \
"v_mul_f32 v74, v74, v224\n" \
"v_mul_f32 v75, v75, v224\n" \
"v_mul_f32 v76, v76, v224\n" \
"v_mul_f32 v77, v77, v224\n" \
"v_mul_f32 v78, v78, v224\n" \
"v_mul_f32 v79, v79, v224\n" \
"v_cvt_pk_bf16_f32 v200, v72, v73\n" \
"v_cvt_pk_bf16_f32 v201, v74, v75\n" \
"v_cvt_pk_bf16_f32 v202, v76, v77\n" \
"v_cvt_pk_bf16_f32 v203, v78, v79\n" \
"v_mul_f32 v80, v80, v224\n" \
"v_mul_f32 v81, v81, v224\n" \
"v_mul_f32 v82, v82, v224\n" \
"v_mul_f32 v83, v83, v224\n" \
"v_mul_f32 v84, v84, v224\n" \
"v_mul_f32 v85, v85, v224\n" \
"v_mul_f32 v86, v86, v224\n" \
"v_mul_f32 v87, v87, v224\n" \
"v_cvt_pk_bf16_f32 v208, v80, v81\n" \
"v_cvt_pk_bf16_f32 v209, v82, v83\n" \
"v_cvt_pk_bf16_f32 v210, v84, v85\n" \
"v_cvt_pk_bf16_f32 v211, v86, v87\n" \
"v_mul_f32 v88, v88, v224\n" \
"v_mul_f32 v89, v89, v224\n" \
"buffer_store_dwordx4 v[192:195], v248, s[32:35], s39 offen offset:0 sc1\n" \
"buffer_store_dwordx4 v[200:203], v248, s[32:35], s39 offen offset:2048 sc1\n" \
"s_add_u32 s38, s66, 3\n" \
"s_and_b32 s38, s38, 3\n" \
"s_lshl_b32 s38, s38, 13\n" \
"s_add_u32 s38, s38, s29\n" \
"buffer_load_dwordx4 a[120:123], v248, s[32:35], s38 offen offset:0 sc1\n" \
"buffer_load_dwordx4 a[124:127], v248, s[32:35], s38 offen offset:1024 sc1\n" \
"buffer_load_dwordx4 a[128:131], v248, s[32:35], s38 offen offset:2048 sc1\n" \
"buffer_load_dwordx4 a[132:135], v248, s[32:35], s38 offen offset:3072 sc1\n" \
"s_add_u32 s38, s38, 4096\n" \
"buffer_load_dwordx4 a[136:139], v248, s[32:35], s38 offen offset:0 sc1\n" \
"buffer_load_dwordx4 a[140:143], v248, s[32:35], s38 offen offset:1024 sc1\n" \
"buffer_load_dwordx4 a[144:147], v248, s[32:35], s38 offen offset:2048 sc1\n" \
"buffer_load_dwordx4 a[148:151], v248, s[32:35], s38 offen offset:3072 sc1\n" \
"s_waitcnt vmcnt(0)\n" \
"s_branch .Lrhb_%=\n" \
".LspcA_%=:\n" \
"s_mov_b32 s82, 0x100000\n" \
".LsqcA_%=:\n" \
"s_sleep 6\n" \
"buffer_load_dword v247, v254, s[32:35], s41 offen sc1\n" \
"s_waitcnt vmcnt(0)\n" \
"v_cmp_eq_u32 vcc, 0, v247\n" \
"s_cbranch_vccz .LsbcA_%=\n" \
"s_sub_u32 s82, s82, 1\n" \
"s_cbranch_scc0 .LsqcA_%=\n" \
"s_branch .LsbcA_%=\n" \
".Lspg_%=:\n" \
"s_mov_b32 s82, 0x100000\n" \
".Lsqg_%=:\n" \
"s_sleep 6\n" \
"buffer_load_dwordx2 v[230:231], v232, s[32:35], s70 offen sc1\n" \
"s_waitcnt vmcnt(0)\n" \
"v_cmp_ne_u64 vcc, v[230:231], v[234:235]\n" \
"s_cbranch_vccz .Lsbg_%=\n" \
"s_sub_u32 s82, s82, 1\n" \
"s_cbranch_scc0 .Lsqg_%=\n" \
"s_branch .Lsbg_%=\n" \
".Lch3_%=:\n" \
".Lst1_14_%=:\n" \
"s_cbranch_execz .Lstb1_%=\n" \
"s_mov_b64 exec, 0\n" \
"s_cmp_eq_u32 %[wid], 0\n" \
"s_cbranch_scc1 .Lst3_0_%=\n" \
"s_cmp_eq_u32 %[wid], 1\n" \
"s_cbranch_scc1 .Lst3_1_%=\n" \
"s_cmp_eq_u32 %[wid], 2\n" \
"s_cbranch_scc1 .Lst3_2_%=\n" \
"s_branch .Lst3_3_%=\n" \
".Lstb3_%=:\n" \
"s_mov_b64 exec, -1\n" \
"s_setpc_b64 s[98:99]\n" \
".Lga_%=:\n" \
".Lst3_2_%=:\n" \
"s_cbranch_execz .Lstb3_%=\n" \
"v_cmp_ne_u32 vcc, 0, v245\n" \
"s_nop 3\n" \
"s_lshr_b32 s76, vcc_lo, 1\n" \
"s_and_b32 s76, s76, vcc_lo\n" \
"s_lshr_b32 s77, s76, 2\n" \
"s_and_b32 s76, s76, s77\n" \
"s_sub_u32 s82, s82, 1\n" \
"s_cselect_b32 s76, -1, s76\n" \
"s_and_b32 s76, s76, 0x11111111\n" \
"s_andn2_b32 s76, s76, s74\n" \
"s_cbranch_scc0 .Lgx_%=\n" \
"s_or_b32 s74, s74, s76\n" \
"s_bitcmp1_b32 s76, 0\n" \
"s_cbranch_scc0 .Lgs0_%=\n" \
"s_add_u32 s24, s95, 0\n" \
"buffer_load_dwordx4 a[128:131], v214, s[32:35], s24 offen sc1\n" \
"buffer_load_dwordx4 a[160:163], v214, s[32:35], s24 offen offset:64 sc1\n" \
".Lgs0_%=:\n" \
"s_bitcmp1_b32 s76, 4\n" \
"s_cbranch_scc0 .Lgs1_%=\n" \
"s_add_u32 s24, s95, 2097152\n" \
"buffer_load_dwordx4 a[132:135], v214, s[32:35], s24 offen sc1\n" \
"buffer_load_dwordx4 a[164:167], v214, s[32:35], s24 offen offset:64 sc1\n" \
".Lgs1_%=:\n" \
"s_bitcmp1_b32 s76, 8\n" \
"s_cbranch_scc0 .Lgs2_%=\n" \
"s_add_u32 s24, s95, 4194304\n" \
"buffer_load_dwordx4 a[136:139], v214, s[32:35], s24 offen sc1\n" \
"buffer_load_dwordx4 a[168:171], v214, s[32:35], s24 offen offset:64 sc1\n" \
".Lgs2_%=:\n" \
"s_bitcmp1_b32 s76, 12\n" \
"s_cbranch_scc0 .Lgs3_%=\n" \
"s_add_u32 s24, s95, 6291456\n" \
"buffer_load_dwordx4 a[140:143], v214, s[32:35], s24 offen sc1\n" \
"buffer_load_dwordx4 a[172:175], v214, s[32:35], s24 offen offset:64 sc1\n" \
".Lgs3_%=:\n" \
"s_bitcmp1_b32 s76, 16\n" \
"s_cbranch_scc0 .Lgs4_%=\n" \
"s_add_u32 s24, s95, 8388608\n" \
"buffer_load_dwordx4 a[144:147], v214, s[32:35], s24 offen sc1\n" \
"buffer_load_dwordx4 a[176:179], v214, s[32:35], s24 offen offset:64 sc1\n" \
".Lgs4_%=:\n" \
"s_bitcmp1_b32 s76, 20\n" \
"s_cbranch_scc0 .Lgs5_%=\n" \
"s_add_u32 s24, s95, 10485760\n" \
"buffer_load_dwordx4 a[148:151], v214, s[32:35], s24 offen sc1\n" \
"buffer_load_dwordx4 a[180:183], v214, s[32:35], s24 offen offset:64 sc1\n" \
".Lgs5_%=:\n" \
"s_bitcmp1_b32 s76, 24\n" \
"s_cbranch_scc0 .Lgs6_%=\n" \
"s_add_u32 s24, s95, 12582912\n" \
"buffer_load_dwordx4 a[152:155], v214, s[32:35], s24 offen sc1\n" \
"buffer_load_dwordx4 a[184:187], v214, s[32:35], s24 offen offset:64 sc1\n" \
".Lgs6_%=:\n" \
"s_bitcmp1_b32 s76, 28\n" \
"s_cbranch_scc0 .Lgs7_%=\n" \
"s_add_u32 s24, s95, 14680064\n" \
"buffer_load_dwordx4 a[156:159], v214, s[32:35], s24 offen sc1\n" \
"buffer_load_dwordx4 a[188:191], v214, s[32:35], s24 offen offset:64 sc1\n" \
".Lgs7_%=:\n" \
"s_cmp_eq_u32 s74, 0x11111111\n" \
"s_cbranch_scc1 .Lg0_%=\n" \
".Lgx_%=:\n" \
"s_not_b32 vcc_lo, vcc_lo\n" \
"s_mov_b32 vcc_hi, 0\n" \
"s_bcnt1_i32_b64 s27, vcc\n" \
"s_cmp_lt_u32 s27, 8\n" \
"s_cbranch_scc1 .Lnap1_%=\n" \
"s_sleep 10\n" \
".Lnap1_%=:\n" \
"s_sleep 2\n" \
"buffer_load_dword v245, v252, s[32:35], s18 offen sc1\n" \
"s_branch .Lg0_%=\n" \
".Lkr_%=:\n" \
".Lst3_3_%=:\n" \
"s_cbranch_execz .Lstb3_%=\n" \
"s_sub_u32 s82, s82, 1\n" \
"s_cbranch_scc1 .Lkb_%=\n" \
"s_cmp_lt_u32 s65, 28\n" \
"s_cbranch_scc1 .Lkn_%=\n" \
"v_max_u32_dpp v88, v88, v88 quad_perm:[1,0,3,2] row_mask:0xf bank_mask:0xf\n" \
"s_nop 1\n" \
"v_max_u32_dpp v88, v88, v88 quad_perm:[2,3,0,1] row_mask:0xf bank_mask:0xf\n" \
"s_nop 1\n" \
"v_max_u32_dpp v88, v88, v88 row_half_mirror row_mask:0xf bank_mask:0xf\n" \
"s_nop 1\n" \
"v_max_u32_dpp v88, v88, v88 row_mirror row_mask:0xf bank_mask:0xf\n" \
"s_nop 1\n" \
"v_max_u32_dpp v88, v88, v88 row_bcast:15 row_mask:0xa bank_mask:0xf\n" \
"s_nop 1\n" \
"v_max_u32_dpp v88, v88, v88 row_bcast:31 row_mask:0xc bank_mask:0xf\n" \
"s_nop 0\n" \
"v_readlane_b32 s25, v88, 63\n" \
"v_lshrrev_b32 v89, 4, v251\n" \
"v_and_b32 v90, 15, v251\n" \
"s_cmp_eq_u32 s25, s93\n" \
"s_cbranch_scc1 .Lkn_%=\n" \
"s_mov_b32 s93, s25\n" \
"s_bfe_u32 s25, s25, 0xb0005\n" \
"s_sub_u32 s25, 2047, s25\n" \
"s_mov_b32 s22, 0x18100800\n" \
"s_mov_b32 s23, 0x01091119\n" \
"s_bfe_u32 s24, s25, 0x30004\n" \
"s_lshl_b32 s24, s24, 3\n" \
"s_lshr_b64 s[22:23], s[22:23], s24\n" \
"s_and_b32 s22, s22, 0xff\n" \
"s_lshr_b32 s26, s25, 7\n" \
"s_lshl_b32 s26, s26, 5\n" \
"s_add_u32 s26, s26, s22\n" \
"s_lshl_b32 s26, s26, 6\n" \
"s_and_b32 s24, s25, 15\n" \
"s_add_u32 s26, s26, s24\n" \
"s_lshl_b32 s26, s26, 6\n" \
"s_add_u32 s26, s26, 59768832\n" \
"v_lshlrev_b32 v89, 21, v89\n" \
"v_and_b32 v91, 3, v251\n" \
"v_lshl_or_b32 v89, v91, 10, v89\n" \
"v_lshrrev_b32 v91, 2, v90\n" \
"v_lshl_or_b32 v89, v91, 13, v89\n" \
"v_lshlrev_b32 v90, 6, v90\n" \
"s_lshl_b32 s25, s25, 10\n" \
"s_add_u32 s24, s26, 8388608\n" \
"buffer_load_dword v91, v89, s[32:35], s26 offen sc1\n" \
"buffer_load_dword v91, v89, s[32:35], s24 offen sc1\n" \
"buffer_load_dword v91, v90, s[44:47], s25 offen\n" \
".Lkn_%=:\n" \
"s_bcnt1_i32_b64 s27, vcc\n" \
"s_cmp_lt_u32 s27, 6\n" \
"s_cbranch_scc1 .Lnap2_%=\n" \
"s_sleep 20\n" \
".Lnap2_%=:\n" \
"s_sleep 2\n" \
"buffer_load_dwordx2 v[86:87], v235, s[32:35], s38 offen sc1\n" \
"s_waitcnt vmcnt(0)\n" \
"s_branch .Lkp_%=\n" \
".Lfr_%=:\n" \
".Lst3_4_%=:\n" \
"s_cbranch_execz .Lstb3_%=\n" \
"s_sub_u32 s82, s82, 1\n" \
"s_cbranch_scc1 .Lfb_%=\n" \
"s_sleep 2\n" \
"s_branch .Lfp_%=\n" \
  \
".Lredo0_%=:\n" \
"s_nop 7\n" \
"v_and_b32 v228, 15, v251\n" \
"v_lshrrev_b32 v229, 4, v251\n" \
"v_lshlrev_b32 v229, 2, v229\n" \
"v_sub_u32 v228, v228, v229\n" \
"v_mov_b32 v231, 0xff800000\n" \
"s_mov_b32 s14, s67\n" \
"s_xor_b32 s15, %[wid], 0\n" \
"s_lshl3_add_u32 s15, s14, s15\n" \
"s_lshl_b32 s15, s15, 4\n" \
"v_add_u32 v226, s15, v228\n" \
"v_mov_b32 v224, 0xff7fffff\n" \
"s_xor_b32 s15, %[wid], 7\n" \
"s_lshl3_add_u32 s15, s14, s15\n" \
"s_lshl_b32 s15, s15, 4\n" \
"v_add_u32 v227, s15, v228\n" \
"v_mov_b32 v225, 0xff7fffff\n" \
"v_mov_b32 v0, 0\n" \
"v_mov_b32 v1, 0\n" \
"v_mov_b32 v2, 0\n" \
"v_mov_b32 v3, 0\n" \
"v_mov_b32 v4, 0\n" \
"v_mov_b32 v5, 0\n" \
"v_mov_b32 v6, 0\n" \
"v_mov_b32 v7, 0\n" \
"v_mov_b32 v8, 0\n" \
"v_mov_b32 v9, 0\n" \
"v_mov_b32 v10, 0\n" \
"v_mov_b32 v11, 0\n" \
"v_mov_b32 v12, 0\n" \
"v_mov_b32 v13, 0\n" \
"v_mov_b32 v14, 0\n" \
"v_mov_b32 v15, 0\n" \
"v_mov_b32 v16, 0\n" \
"v_mov_b32 v17, 0\n" \
"v_mov_b32 v18, 0\n" \
"v_mov_b32 v19, 0\n" \
"v_mov_b32 v20, 0\n" \
"v_mov_b32 v21, 0\n" \
"v_mov_b32 v22, 0\n" \
"v_mov_b32 v23, 0\n" \
"v_mov_b32 v24, 0\n" \
"v_mov_b32 v25, 0\n" \
"v_mov_b32 v26, 0\n" \
"v_mov_b32 v27, 0\n" \
"v_mov_b32 v28, 0\n" \
"v_mov_b32 v29, 0\n" \
"v_mov_b32 v30, 0\n" \
"v_mov_b32 v31, 0\n" \
"v_mov_b32 v32, 0\n" \
"v_mov_b32 v33, 0\n" \
"v_mov_b32 v34, 0\n" \
"v_mov_b32 v35, 0\n" \
"v_mov_b32 v36, 0\n" \
"v_mov_b32 v37, 0\n" \
"v_mov_b32 v38, 0\n" \
"v_mov_b32 v39, 0\n" \
"v_mov_b32 v40, 0\n" \
"v_mov_b32 v41, 0\n" \
"v_mov_b32 v42, 0\n" \
"v_mov_b32 v43, 0\n" \
"v_mov_b32 v44, 0\n" \
"v_mov_b32 v45, 0\n" \
"v_mov_b32 v46, 0\n" \
"v_mov_b32 v47, 0\n" \
"v_mov_b32 v48, 0\n" \
"v_mov_b32 v49, 0\n" \
"v_mov_b32 v50, 0\n" \
"v_mov_b32 v51, 0\n" \
"v_mov_b32 v52, 0\n" \
"v_mov_b32 v53, 0\n" \
"v_mov_b32 v54, 0\n" \
"v_mov_b32 v55, 0\n" \
"v_mov_b32 v56, 0\n" \
"v_mov_b32 v57, 0\n" \
"v_mov_b32 v58, 0\n" \
"v_mov_b32 v59, 0\n" \
"v_mov_b32 v60, 0\n" \
"v_mov_b32 v61, 0\n" \
"v_mov_b32 v62, 0\n" \
"v_mov_b32 v63, 0\n" \
"v_accvgpr_write_b32 a32, 0\n" \
"v_accvgpr_write_b32 a33, 0\n" \
"v_accvgpr_write_b32 a34, 0\n" \
"v_accvgpr_write_b32 a35, 0\n" \
"v_accvgpr_write_b32 a36, 0\n" \
"v_accvgpr_write_b32 a37, 0\n" \
"v_accvgpr_write_b32 a38, 0\n" \
"v_accvgpr_write_b32 a39, 0\n" \
"s_lshl1_add_u32 s14, s14, 1\n" \
"s_mov_b32 s15, s73\n" \
".Lslr0_%=:\n" \
"s_add_u32 s82, s15, 0\n" \
"buffer_load_dwordx4 a[64:67], v248, s[32:35], s82 offen offset:0 sc1\n" \
"buffer_load_dwordx4 a[68:71], v248, s[32:35], s82 offen offset:1024 sc1\n" \
"buffer_load_dwordx4 a[72:75], v248, s[32:35], s82 offen offset:2048 sc1\n" \
"buffer_load_dwordx4 a[76:79], v248, s[32:35], s82 offen offset:3072 sc1\n" \
"s_add_u32 s82, s15, 4096\n" \
"buffer_load_dwordx4 a[80:83], v248, s[32:35], s82 offen offset:0 sc1\n" \
"buffer_load_dwordx4 a[84:87], v248, s[32:35], s82 offen offset:1024 sc1\n" \
"buffer_load_dwordx4 a[88:91], v248, s[32:35], s82 offen offset:2048 sc1\n" \
"buffer_load_dwordx4 a[92:95], v248, s[32:35], s82 offen offset:3072 sc1\n" \
"s_add_u32 s82, s15, 8192\n" \
"buffer_load_dwordx4 a[96:99], v248, s[32:35], s82 offen offset:0 sc1\n" \
"buffer_load_dwordx4 a[100:103], v248, s[32:35], s82 offen offset:1024 sc1\n" \
"buffer_load_dwordx4 a[104:107], v248, s[32:35], s82 offen offset:2048 sc1\n" \
"buffer_load_dwordx4 a[108:111], v248, s[32:35], s82 offen offset:3072 sc1\n" \
"s_add_u32 s82, s15, 12288\n" \
"buffer_load_dwordx4 a[112:115], v248, s[32:35], s82 offen offset:0 sc1\n" \
"buffer_load_dwordx4 a[116:119], v248, s[32:35], s82 offen offset:1024 sc1\n" \
"buffer_load_dwordx4 a[120:123], v248, s[32:35], s82 offen offset:2048 sc1\n" \
"buffer_load_dwordx4 a[124:127], v248, s[32:35], s82 offen offset:3072 sc1\n" \
"s_waitcnt vmcnt(0)\n" \
"v_mfma_f32_16x16x32_bf16 v[128:131], a[64:67], v[192:195], 0\n" \
"v_mfma_f32_16x16x32_bf16 v[128:131], a[68:71], v[196:199], v[128:131]\n" \
"v_mfma_f32_16x16x32_bf16 v[128:131], a[72:75], v[200:203], v[128:131]\n" \
"v_mfma_f32_16x16x32_bf16 v[128:131], a[76:79], v[204:207], v[128:131]\n" \
"v_mfma_f32_16x16x32_bf16 v[132:135], a[92:95], v[204:207], 0\n" \
"v_mfma_f32_16x16x32_bf16 v[132:135], a[88:91], v[200:203], v[132:135]\n" \
"v_mfma_f32_16x16x32_bf16 v[132:135], a[84:87], v[196:199], v[132:135]\n" \
"v_mfma_f32_16x16x32_bf16 v[132:135], a[80:83], v[192:195], v[132:135]\n" \
"v_mfma_f32_16x16x32_bf16 v[136:139], a[96:99], v[192:195], 0\n" \
"v_mfma_f32_16x16x32_bf16 v[136:139], a[100:103], v[196:199], v[136:139]\n" \
"v_mfma_f32_16x16x32_bf16 v[136:139], a[104:107], v[200:203], v[136:139]\n" \
"v_mfma_f32_16x16x32_bf16 v[136:139], a[108:111], v[204:207], v[136:139]\n" \
"v_mfma_f32_16x16x32_bf16 v[140:143], a[124:127], v[204:207], 0\n" \
"v_mfma_f32_16x16x32_bf16 v[140:143], a[120:123], v[200:203], v[140:143]\n" \
"v_mfma_f32_16x16x32_bf16 v[140:143], a[116:119], v[196:199], v[140:143]\n" \
"v_mfma_f32_16x16x32_bf16 v[140:143], a[112:115], v[192:195], v[140:143]\n" \
"v_cmp_gt_i32 vcc, 0, v226\n" \
"s_nop 1\n" \
"v_cndmask_b32 v128, v128, v231, vcc\n" \
"v_cmp_gt_i32 vcc, 1, v226\n" \
"s_nop 1\n" \
"v_cndmask_b32 v129, v129, v231, vcc\n" \
"v_cmp_gt_i32 vcc, 2, v226\n" \
"s_nop 1\n" \
"v_cndmask_b32 v130, v130, v231, vcc\n" \
"v_cmp_gt_i32 vcc, 3, v226\n" \
"s_nop 1\n" \
"v_cndmask_b32 v131, v131, v231, vcc\n" \
"v_cmp_gt_i32 vcc, 16, v226\n" \
"s_nop 1\n" \
"v_cndmask_b32 v132, v132, v231, vcc\n" \
"v_cmp_gt_i32 vcc, 17, v226\n" \
"s_nop 1\n" \
"v_cndmask_b32 v133, v133, v231, vcc\n" \
"v_cmp_gt_i32 vcc, 18, v226\n" \
"s_nop 1\n" \
"v_cndmask_b32 v134, v134, v231, vcc\n" \
"v_cmp_gt_i32 vcc, 19, v226\n" \
"s_nop 1\n" \
"v_cndmask_b32 v135, v135, v231, vcc\n" \
"v_cmp_gt_i32 vcc, 32, v226\n" \
"s_nop 1\n" \
"v_cndmask_b32 v136, v136, v231, vcc\n" \
"v_cmp_gt_i32 vcc, 33, v226\n" \
"s_nop 1\n" \
"v_cndmask_b32 v137, v137, v231, vcc\n" \
"v_cmp_gt_i32 vcc, 34, v226\n" \
"s_nop 1\n" \
"v_cndmask_b32 v138, v138, v231, vcc\n" \
"v_cmp_gt_i32 vcc, 35, v226\n" \
"s_nop 1\n" \
"v_cndmask_b32 v139, v139, v231, vcc\n" \
"v_cmp_gt_i32 vcc, 48, v226\n" \
"s_nop 1\n" \
"v_cndmask_b32 v140, v140, v231, vcc\n" \
"v_cmp_gt_i32 vcc, 49, v226\n" \
"s_nop 1\n" \
"v_cndmask_b32 v141, v141, v231, vcc\n" \
"v_cmp_gt_i32 vcc, 50, v226\n" \
"s_nop 1\n" \
"v_cndmask_b32 v142, v142, v231, vcc\n" \
"v_cmp_gt_i32 vcc, 51, v226\n" \
"s_nop 1\n" \
"v_cndmask_b32 v143, v143, v231, vcc\n" \
"v_max3_f32 v228, v224, v128, v129\n" \
"v_max3_f32 v228, v228, v130, v131\n" \
"v_max3_f32 v228, v228, v132, v133\n" \
"v_max3_f32 v228, v228, v134, v135\n" \
"v_max3_f32 v228, v228, v136, v137\n" \
"v_max3_f32 v228, v228, v138, v139\n" \
"v_max3_f32 v228, v228, v140, v141\n" \
"v_max3_f32 v228, v228, v142, v143\n" \
"v_mov_b32 v229, v228\n" \
"s_nop 1\n" \
"v_permlane32_swap_b32 v228, v229\n" \
"v_max_f32 v228, v228, v229\n" \
"v_mov_b32 v229, v228\n" \
"s_nop 1\n" \
"v_permlane16_swap_b32 v228, v229\n" \
"v_max_f32 v228, v228, v229\n" \
"v_sub_f32 v230, v224, v228\n" \
"v_mov_b32 v224, v228\n" \
"v_exp_f32 v230, v230\n" \
"v_subrev_u32 v226, 64, v226\n" \
"s_add_u32 s82, s15, 589824\n" \
"buffer_load_dwordx4 a[64:67], v248, s[32:35], s82 offen offset:0 sc1\n" \
"buffer_load_dwordx4 a[68:71], v248, s[32:35], s82 offen offset:1024 sc1\n" \
"buffer_load_dwordx4 a[72:75], v248, s[32:35], s82 offen offset:2048 sc1\n" \
"buffer_load_dwordx4 a[76:79], v248, s[32:35], s82 offen offset:3072 sc1\n" \
"s_add_u32 s82, s15, 593920\n" \
"buffer_load_dwordx4 a[80:83], v248, s[32:35], s82 offen offset:0 sc1\n" \
"buffer_load_dwordx4 a[84:87], v248, s[32:35], s82 offen offset:1024 sc1\n" \
"buffer_load_dwordx4 a[88:91], v248, s[32:35], s82 offen offset:2048 sc1\n" \
"buffer_load_dwordx4 a[92:95], v248, s[32:35], s82 offen offset:3072 sc1\n" \
"s_add_u32 s82, s15, 598016\n" \
"buffer_load_dwordx4 a[96:99], v248, s[32:35], s82 offen offset:0 sc1\n" \
"buffer_load_dwordx4 a[100:103], v248, s[32:35], s82 offen offset:1024 sc1\n" \
"buffer_load_dwordx4 a[104:107], v248, s[32:35], s82 offen offset:2048 sc1\n" \
"buffer_load_dwordx4 a[108:111], v248, s[32:35], s82 offen offset:3072 sc1\n" \
"s_add_u32 s82, s15, 602112\n" \
"buffer_load_dwordx4 a[112:115], v248, s[32:35], s82 offen offset:0 sc1\n" \
"buffer_load_dwordx4 a[116:119], v248, s[32:35], s82 offen offset:1024 sc1\n" \
"buffer_load_dwordx4 a[120:123], v248, s[32:35], s82 offen offset:2048 sc1\n" \
"buffer_load_dwordx4 a[124:127], v248, s[32:35], s82 offen offset:3072 sc1\n" \
"s_waitcnt vmcnt(0)\n" \
"v_sub_f32 v128, v128, v228\n" \
"v_sub_f32 v129, v129, v228\n" \
"v_sub_f32 v130, v130, v228\n" \
"v_sub_f32 v131, v131, v228\n" \
"v_sub_f32 v132, v132, v228\n" \
"v_sub_f32 v133, v133, v228\n" \
"v_sub_f32 v134, v134, v228\n" \
"v_sub_f32 v135, v135, v228\n" \
"v_sub_f32 v136, v136, v228\n" \
"v_sub_f32 v137, v137, v228\n" \
"v_sub_f32 v138, v138, v228\n" \
"v_sub_f32 v139, v139, v228\n" \
"v_sub_f32 v140, v140, v228\n" \
"v_sub_f32 v141, v141, v228\n" \
"v_sub_f32 v142, v142, v228\n" \
"v_sub_f32 v143, v143, v228\n" \
"v_exp_f32 v128, v128\n" \
"v_exp_f32 v129, v129\n" \
"v_exp_f32 v130, v130\n" \
"v_exp_f32 v131, v131\n" \
"v_exp_f32 v132, v132\n" \
"v_exp_f32 v133, v133\n" \
"v_exp_f32 v134, v134\n" \
"v_exp_f32 v135, v135\n" \
"v_exp_f32 v136, v136\n" \
"v_exp_f32 v137, v137\n" \
"v_exp_f32 v138, v138\n" \
"v_exp_f32 v139, v139\n" \
"v_exp_f32 v140, v140\n" \
"v_exp_f32 v141, v141\n" \
"v_exp_f32 v142, v142\n" \
"v_exp_f32 v143, v143\n" \
"v_cvt_pk_bf16_f32 v128, v128, v129\n" \
"v_cvt_pk_bf16_f32 v129, v130, v131\n" \
"v_cvt_pk_bf16_f32 v130, v132, v133\n" \
"v_cvt_pk_bf16_f32 v131, v134, v135\n" \
"v_cvt_pk_bf16_f32 v136, v136, v137\n" \
"v_cvt_pk_bf16_f32 v137, v138, v139\n" \
"v_cvt_pk_bf16_f32 v138, v140, v141\n" \
"v_cvt_pk_bf16_f32 v139, v142, v143\n" \
"v_mul_f32 v0, v230, v0\n" \
"v_mul_f32 v1, v230, v1\n" \
"v_mul_f32 v2, v230, v2\n" \
"v_mul_f32 v3, v230, v3\n" \
"v_mul_f32 v4, v230, v4\n" \
"v_mul_f32 v5, v230, v5\n" \
"v_mul_f32 v6, v230, v6\n" \
"v_mul_f32 v7, v230, v7\n" \
"v_mul_f32 v8, v230, v8\n" \
"v_mul_f32 v9, v230, v9\n" \
"v_mul_f32 v10, v230, v10\n" \
"v_mul_f32 v11, v230, v11\n" \
"v_mul_f32 v12, v230, v12\n" \
"v_mul_f32 v13, v230, v13\n" \
"v_mul_f32 v14, v230, v14\n" \
"v_mul_f32 v15, v230, v15\n" \
"v_mul_f32 v16, v230, v16\n" \
"v_mul_f32 v17, v230, v17\n" \
"v_mul_f32 v18, v230, v18\n" \
"v_mul_f32 v19, v230, v19\n" \
"v_mul_f32 v20, v230, v20\n" \
"v_mul_f32 v21, v230, v21\n" \
"v_mul_f32 v22, v230, v22\n" \
"v_mul_f32 v23, v230, v23\n" \
"v_mul_f32 v24, v230, v24\n" \
"v_mul_f32 v25, v230, v25\n" \
"v_mul_f32 v26, v230, v26\n" \
"v_mul_f32 v27, v230, v27\n" \
"v_mul_f32 v28, v230, v28\n" \
"v_mul_f32 v29, v230, v29\n" \
"v_mul_f32 v30, v230, v30\n" \
"v_mul_f32 v31, v230, v31\n" \
"v_accvgpr_read_b32 v229, a32\n" \
"v_mul_f32 v229, v230, v229\n" \
"v_accvgpr_write_b32 a32, v229\n" \
"v_accvgpr_read_b32 v229, a33\n" \
"v_mul_f32 v229, v230, v229\n" \
"v_accvgpr_write_b32 a33, v229\n" \
"v_accvgpr_read_b32 v229, a34\n" \
"v_mul_f32 v229, v230, v229\n" \
"v_accvgpr_write_b32 a34, v229\n" \
"v_accvgpr_read_b32 v229, a35\n" \
"v_mul_f32 v229, v230, v229\n" \
"v_accvgpr_write_b32 a35, v229\n" \
"s_nop 1\n" \
"v_mfma_f32_16x16x32_bf16 a[32:35], a[48:51], v[128:131], a[32:35]\n" \
"v_mfma_f32_16x16x32_bf16 a[32:35], a[48:51], v[136:139], a[32:35]\n" \
"v_mfma_f32_16x16x32_bf16 v[0:3], a[96:99], v[136:139], v[0:3]\n" \
"v_mfma_f32_16x16x32_bf16 v[0:3], a[64:67], v[128:131], v[0:3]\n" \
"v_mfma_f32_16x16x32_bf16 v[4:7], a[68:71], v[128:131], v[4:7]\n" \
"v_mfma_f32_16x16x32_bf16 v[4:7], a[100:103], v[136:139], v[4:7]\n" \
"v_mfma_f32_16x16x32_bf16 v[8:11], a[104:107], v[136:139], v[8:11]\n" \
"v_mfma_f32_16x16x32_bf16 v[8:11], a[72:75], v[128:131], v[8:11]\n" \
"v_mfma_f32_16x16x32_bf16 v[12:15], a[76:79], v[128:131], v[12:15]\n" \
"v_mfma_f32_16x16x32_bf16 v[12:15], a[108:111], v[136:139], v[12:15]\n" \
"v_mfma_f32_16x16x32_bf16 v[16:19], a[112:115], v[136:139], v[16:19]\n" \
"v_mfma_f32_16x16x32_bf16 v[16:19], a[80:83], v[128:131], v[16:19]\n" \
"v_mfma_f32_16x16x32_bf16 v[20:23], a[84:87], v[128:131], v[20:23]\n" \
"v_mfma_f32_16x16x32_bf16 v[20:23], a[116:119], v[136:139], v[20:23]\n" \
"v_mfma_f32_16x16x32_bf16 v[24:27], a[120:123], v[136:139], v[24:27]\n" \
"v_mfma_f32_16x16x32_bf16 v[24:27], a[88:91], v[128:131], v[24:27]\n" \
"v_mfma_f32_16x16x32_bf16 v[28:31], a[92:95], v[128:131], v[28:31]\n" \
"v_mfma_f32_16x16x32_bf16 v[28:31], a[124:127], v[136:139], v[28:31]\n" \
"s_nop 7\n" \
"s_add_u32 s82, s15, 0\n" \
"buffer_load_dwordx4 a[64:67], v248, s[32:35], s82 offen offset:0 sc1\n" \
"buffer_load_dwordx4 a[68:71], v248, s[32:35], s82 offen offset:1024 sc1\n" \
"buffer_load_dwordx4 a[72:75], v248, s[32:35], s82 offen offset:2048 sc1\n" \
"buffer_load_dwordx4 a[76:79], v248, s[32:35], s82 offen offset:3072 sc1\n" \
"s_add_u32 s82, s15, 4096\n" \
"buffer_load_dwordx4 a[80:83], v248, s[32:35], s82 offen offset:0 sc1\n" \
"buffer_load_dwordx4 a[84:87], v248, s[32:35], s82 offen offset:1024 sc1\n" \
"buffer_load_dwordx4 a[88:91], v248, s[32:35], s82 offen offset:2048 sc1\n" \
"buffer_load_dwordx4 a[92:95], v248, s[32:35], s82 offen offset:3072 sc1\n" \
"s_add_u32 s82, s15, 8192\n" \
"buffer_load_dwordx4 a[96:99], v248, s[32:35], s82 offen offset:0 sc1\n" \
"buffer_load_dwordx4 a[100:103], v248, s[32:35], s82 offen offset:1024 sc1\n" \
"buffer_load_dwordx4 a[104:107], v248, s[32:35], s82 offen offset:2048 sc1\n" \
"buffer_load_dwordx4 a[108:111], v248, s[32:35], s82 offen offset:3072 sc1\n" \
"s_add_u32 s82, s15, 12288\n" \
"buffer_load_dwordx4 a[112:115], v248, s[32:35], s82 offen offset:0 sc1\n" \
"buffer_load_dwordx4 a[116:119], v248, s[32:35], s82 offen offset:1024 sc1\n" \
"buffer_load_dwordx4 a[120:123], v248, s[32:35], s82 offen offset:2048 sc1\n" \
"buffer_load_dwordx4 a[124:127], v248, s[32:35], s82 offen offset:3072 sc1\n" \
"s_waitcnt vmcnt(0)\n" \
"v_mfma_f32_16x16x32_bf16 v[128:131], a[64:67], v[208:211], 0\n" \
"v_mfma_f32_16x16x32_bf16 v[128:131], a[68:71], v[212:215], v[128:131]\n" \
"v_mfma_f32_16x16x32_bf16 v[128:131], a[72:75], v[216:219], v[128:131]\n" \
"v_mfma_f32_16x16x32_bf16 v[128:131], a[76:79], v[220:223], v[128:131]\n" \
"v_mfma_f32_16x16x32_bf16 v[132:135], a[92:95], v[220:223], 0\n" \
"v_mfma_f32_16x16x32_bf16 v[132:135], a[88:91], v[216:219], v[132:135]\n" \
"v_mfma_f32_16x16x32_bf16 v[132:135], a[84:87], v[212:215], v[132:135]\n" \
"v_mfma_f32_16x16x32_bf16 v[132:135], a[80:83], v[208:211], v[132:135]\n" \
"v_mfma_f32_16x16x32_bf16 v[136:139], a[96:99], v[208:211], 0\n" \
"v_mfma_f32_16x16x32_bf16 v[136:139], a[100:103], v[212:215], v[136:139]\n" \
"v_mfma_f32_16x16x32_bf16 v[136:139], a[104:107], v[216:219], v[136:139]\n" \
"v_mfma_f32_16x16x32_bf16 v[136:139], a[108:111], v[220:223], v[136:139]\n" \
"v_mfma_f32_16x16x32_bf16 v[140:143], a[124:127], v[220:223], 0\n" \
"v_mfma_f32_16x16x32_bf16 v[140:143], a[120:123], v[216:219], v[140:143]\n" \
"v_mfma_f32_16x16x32_bf16 v[140:143], a[116:119], v[212:215], v[140:143]\n" \
"v_mfma_f32_16x16x32_bf16 v[140:143], a[112:115], v[208:211], v[140:143]\n" \
"v_cmp_gt_i32 vcc, 0, v227\n" \
"s_nop 1\n" \
"v_cndmask_b32 v128, v128, v231, vcc\n" \
"v_cmp_gt_i32 vcc, 1, v227\n" \
"s_nop 1\n" \
"v_cndmask_b32 v129, v129, v231, vcc\n" \
"v_cmp_gt_i32 vcc, 2, v227\n" \
"s_nop 1\n" \
"v_cndmask_b32 v130, v130, v231, vcc\n" \
"v_cmp_gt_i32 vcc, 3, v227\n" \
"s_nop 1\n" \
"v_cndmask_b32 v131, v131, v231, vcc\n" \
"v_cmp_gt_i32 vcc, 16, v227\n" \
"s_nop 1\n" \
"v_cndmask_b32 v132, v132, v231, vcc\n" \
"v_cmp_gt_i32 vcc, 17, v227\n" \
"s_nop 1\n" \
"v_cndmask_b32 v133, v133, v231, vcc\n" \
"v_cmp_gt_i32 vcc, 18, v227\n" \
"s_nop 1\n" \
"v_cndmask_b32 v134, v134, v231, vcc\n" \
"v_cmp_gt_i32 vcc, 19, v227\n" \
"s_nop 1\n" \
"v_cndmask_b32 v135, v135, v231, vcc\n" \
"v_cmp_gt_i32 vcc, 32, v227\n" \
"s_nop 1\n" \
"v_cndmask_b32 v136, v136, v231, vcc\n" \
"v_cmp_gt_i32 vcc, 33, v227\n" \
"s_nop 1\n" \
"v_cndmask_b32 v137, v137, v231, vcc\n" \
"v_cmp_gt_i32 vcc, 34, v227\n" \
"s_nop 1\n" \
"v_cndmask_b32 v138, v138, v231, vcc\n" \
"v_cmp_gt_i32 vcc, 35, v227\n" \
"s_nop 1\n" \
"v_cndmask_b32 v139, v139, v231, vcc\n" \
"v_cmp_gt_i32 vcc, 48, v227\n" \
"s_nop 1\n" \
"v_cndmask_b32 v140, v140, v231, vcc\n" \
"v_cmp_gt_i32 vcc, 49, v227\n" \
"s_nop 1\n" \
"v_cndmask_b32 v141, v141, v231, vcc\n" \
"v_cmp_gt_i32 vcc, 50, v227\n" \
"s_nop 1\n" \
"v_cndmask_b32 v142, v142, v231, vcc\n" \
"v_cmp_gt_i32 vcc, 51, v227\n" \
"s_nop 1\n" \
"v_cndmask_b32 v143, v143, v231, vcc\n" \
"v_max3_f32 v228, v225, v128, v129\n" \
"v_max3_f32 v228, v228, v130, v131\n" \
"v_max3_f32 v228, v228, v132, v133\n" \
"v_max3_f32 v228, v228, v134, v135\n" \
"v_max3_f32 v228, v228, v136, v137\n" \
"v_max3_f32 v228, v228, v138, v139\n" \
"v_max3_f32 v228, v228, v140, v141\n" \
"v_max3_f32 v228, v228, v142, v143\n" \
"v_mov_b32 v229, v228\n" \
"s_nop 1\n" \
"v_permlane32_swap_b32 v228, v229\n" \
"v_max_f32 v228, v228, v229\n" \
"v_mov_b32 v229, v228\n" \
"s_nop 1\n" \
"v_permlane16_swap_b32 v228, v229\n" \
"v_max_f32 v228, v228, v229\n" \
"v_sub_f32 v230, v225, v228\n" \
"v_mov_b32 v225, v228\n" \
"v_exp_f32 v230, v230\n" \
"v_subrev_u32 v227, 64, v227\n" \
"s_add_u32 s82, s15, 589824\n" \
"buffer_load_dwordx4 a[64:67], v248, s[32:35], s82 offen offset:0 sc1\n" \
"buffer_load_dwordx4 a[68:71], v248, s[32:35], s82 offen offset:1024 sc1\n" \
"buffer_load_dwordx4 a[72:75], v248, s[32:35], s82 offen offset:2048 sc1\n" \
"buffer_load_dwordx4 a[76:79], v248, s[32:35], s82 offen offset:3072 sc1\n" \
"s_add_u32 s82, s15, 593920\n" \
"buffer_load_dwordx4 a[80:83], v248, s[32:35], s82 offen offset:0 sc1\n" \
"buffer_load_dwordx4 a[84:87], v248, s[32:35], s82 offen offset:1024 sc1\n" \
"buffer_load_dwordx4 a[88:91], v248, s[32:35], s82 offen offset:2048 sc1\n" \
"buffer_load_dwordx4 a[92:95], v248, s[32:35], s82 offen offset:3072 sc1\n" \
"s_add_u32 s82, s15, 598016\n" \
"buffer_load_dwordx4 a[96:99], v248, s[32:35], s82 offen offset:0 sc1\n" \
"buffer_load_dwordx4 a[100:103], v248, s[32:35], s82 offen offset:1024 sc1\n" \
"buffer_load_dwordx4 a[104:107], v248, s[32:35], s82 offen offset:2048 sc1\n" \
"buffer_load_dwordx4 a[108:111], v248, s[32:35], s82 offen offset:3072 sc1\n" \
"s_add_u32 s82, s15, 602112\n" \
"buffer_load_dwordx4 a[112:115], v248, s[32:35], s82 offen offset:0 sc1\n" \
"buffer_load_dwordx4 a[116:119], v248, s[32:35], s82 offen offset:1024 sc1\n" \
"buffer_load_dwordx4 a[120:123], v248, s[32:35], s82 offen offset:2048 sc1\n" \
"buffer_load_dwordx4 a[124:127], v248, s[32:35], s82 offen offset:3072 sc1\n" \
"s_waitcnt vmcnt(0)\n" \
"v_sub_f32 v128, v128, v228\n" \
"v_sub_f32 v129, v129, v228\n" \
"v_sub_f32 v130, v130, v228\n" \
"v_sub_f32 v131, v131, v228\n" \
"v_sub_f32 v132, v132, v228\n" \
"v_sub_f32 v133, v133, v228\n" \
"v_sub_f32 v134, v134, v228\n" \
"v_sub_f32 v135, v135, v228\n" \
"v_sub_f32 v136, v136, v228\n" \
"v_sub_f32 v137, v137, v228\n" \
"v_sub_f32 v138, v138, v228\n" \
"v_sub_f32 v139, v139, v228\n" \
"v_sub_f32 v140, v140, v228\n" \
"v_sub_f32 v141, v141, v228\n" \
"v_sub_f32 v142, v142, v228\n" \
"v_sub_f32 v143, v143, v228\n" \
"v_exp_f32 v128, v128\n" \
"v_exp_f32 v129, v129\n" \
"v_exp_f32 v130, v130\n" \
"v_exp_f32 v131, v131\n" \
"v_exp_f32 v132, v132\n" \
"v_exp_f32 v133, v133\n" \
"v_exp_f32 v134, v134\n" \
"v_exp_f32 v135, v135\n" \
"v_exp_f32 v136, v136\n" \
"v_exp_f32 v137, v137\n" \
"v_exp_f32 v138, v138\n" \
"v_exp_f32 v139, v139\n" \
"v_exp_f32 v140, v140\n" \
"v_exp_f32 v141, v141\n" \
"v_exp_f32 v142, v142\n" \
"v_exp_f32 v143, v143\n" \
"v_cvt_pk_bf16_f32 v128, v128, v129\n" \
"v_cvt_pk_bf16_f32 v129, v130, v131\n" \
"v_cvt_pk_bf16_f32 v130, v132, v133\n" \
"v_cvt_pk_bf16_f32 v131, v134, v135\n" \
"v_cvt_pk_bf16_f32 v136, v136, v137\n" \
"v_cvt_pk_bf16_f32 v137, v138, v139\n" \
"v_cvt_pk_bf16_f32 v138, v140, v141\n" \
"v_cvt_pk_bf16_f32 v139, v142, v143\n" \
"v_mul_f32 v32, v230, v32\n" \
"v_mul_f32 v33, v230, v33\n" \
"v_mul_f32 v34, v230, v34\n" \
"v_mul_f32 v35, v230, v35\n" \
"v_mul_f32 v36, v230, v36\n" \
"v_mul_f32 v37, v230, v37\n" \
"v_mul_f32 v38, v230, v38\n" \
"v_mul_f32 v39, v230, v39\n" \
"v_mul_f32 v40, v230, v40\n" \
"v_mul_f32 v41, v230, v41\n" \
"v_mul_f32 v42, v230, v42\n" \
"v_mul_f32 v43, v230, v43\n" \
"v_mul_f32 v44, v230, v44\n" \
"v_mul_f32 v45, v230, v45\n" \
"v_mul_f32 v46, v230, v46\n" \
"v_mul_f32 v47, v230, v47\n" \
"v_mul_f32 v48, v230, v48\n" \
"v_mul_f32 v49, v230, v49\n" \
"v_mul_f32 v50, v230, v50\n" \
"v_mul_f32 v51, v230, v51\n" \
"v_mul_f32 v52, v230, v52\n" \
"v_mul_f32 v53, v230, v53\n" \
"v_mul_f32 v54, v230, v54\n" \
"v_mul_f32 v55, v230, v55\n" \
"v_mul_f32 v56, v230, v56\n" \
"v_mul_f32 v57, v230, v57\n" \
"v_mul_f32 v58, v230, v58\n" \
"v_mul_f32 v59, v230, v59\n" \
"v_mul_f32 v60, v230, v60\n" \
"v_mul_f32 v61, v230, v61\n" \
"v_mul_f32 v62, v230, v62\n" \
"v_mul_f32 v63, v230, v63\n" \
"v_accvgpr_read_b32 v229, a36\n" \
"v_mul_f32 v229, v230, v229\n" \
"v_accvgpr_write_b32 a36, v229\n" \
"v_accvgpr_read_b32 v229, a37\n" \
"v_mul_f32 v229, v230, v229\n" \
"v_accvgpr_write_b32 a37, v229\n" \
"v_accvgpr_read_b32 v229, a38\n" \
"v_mul_f32 v229, v230, v229\n" \
"v_accvgpr_write_b32 a38, v229\n" \
"v_accvgpr_read_b32 v229, a39\n" \
"v_mul_f32 v229, v230, v229\n" \
"v_accvgpr_write_b32 a39, v229\n" \
"s_nop 1\n" \
"v_mfma_f32_16x16x32_bf16 a[36:39], a[48:51], v[128:131], a[36:39]\n" \
"v_mfma_f32_16x16x32_bf16 a[36:39], a[48:51], v[136:139], a[36:39]\n" \
"v_mfma_f32_16x16x32_bf16 v[32:35], a[96:99], v[136:139], v[32:35]\n" \
"v_mfma_f32_16x16x32_bf16 v[32:35], a[64:67], v[128:131], v[32:35]\n" \
"v_mfma_f32_16x16x32_bf16 v[36:39], a[68:71], v[128:131], v[36:39]\n" \
"v_mfma_f32_16x16x32_bf16 v[36:39], a[100:103], v[136:139], v[36:39]\n" \
"v_mfma_f32_16x16x32_bf16 v[40:43], a[104:107], v[136:139], v[40:43]\n" \
"v_mfma_f32_16x16x32_bf16 v[40:43], a[72:75], v[128:131], v[40:43]\n" \
"v_mfma_f32_16x16x32_bf16 v[44:47], a[76:79], v[128:131], v[44:47]\n" \
"v_mfma_f32_16x16x32_bf16 v[44:47], a[108:111], v[136:139], v[44:47]\n" \
"v_mfma_f32_16x16x32_bf16 v[48:51], a[112:115], v[136:139], v[48:51]\n" \
"v_mfma_f32_16x16x32_bf16 v[48:51], a[80:83], v[128:131], v[48:51]\n" \
"v_mfma_f32_16x16x32_bf16 v[52:55], a[84:87], v[128:131], v[52:55]\n" \
"v_mfma_f32_16x16x32_bf16 v[52:55], a[116:119], v[136:139], v[52:55]\n" \
"v_mfma_f32_16x16x32_bf16 v[56:59], a[120:123], v[136:139], v[56:59]\n" \
"v_mfma_f32_16x16x32_bf16 v[56:59], a[88:91], v[128:131], v[56:59]\n" \
"v_mfma_f32_16x16x32_bf16 v[60:63], a[92:95], v[128:131], v[60:63]\n" \
"v_mfma_f32_16x16x32_bf16 v[60:63], a[124:127], v[136:139], v[60:63]\n" \
"s_nop 7\n" \
"s_add_u32 s15, s15, 0x4000\n" \
"s_sub_u32 s14, s14, 1\n" \
"s_cbranch_scc0 .Lslr0_%=\n" \
"s_nop 7\n" \
"s_setpc_b64 s[98:99]\n" \
  \
".Lredo2_%=:\n" \
"s_nop 7\n" \
"v_and_b32 v196, 31, v251\n" \
"v_lshlrev_b32 v196, 2, v196\n" \
"s_branch .Lspr2_%=\n" \
".Lsbr2_%=:\n" \
"v_and_b32 v196, 15, v251\n" \
"v_lshrrev_b32 v197, 4, v251\n" \
"v_lshlrev_b32 v197, 2, v197\n" \
"v_sub_u32 v196, v196, v197\n" \
"v_mov_b32 v199, 0xff800000\n" \
"s_sub_u32 s14, 15, s67\n" \
"s_xor_b32 s15, %[wid], 0\n" \
"s_lshl3_add_u32 s15, s14, s15\n" \
"s_lshl_b32 s15, s15, 4\n" \
"v_add_u32 v194, s15, v196\n" \
"v_mov_b32 v192, 0xff7fffff\n" \
"s_xor_b32 s15, %[wid], 7\n" \
"s_lshl3_add_u32 s15, s14, s15\n" \
"s_lshl_b32 s15, s15, 4\n" \
"v_add_u32 v195, s15, v196\n" \
"v_mov_b32 v193, 0xff7fffff\n" \
"v_mov_b32 v64, 0\n" \
"v_mov_b32 v65, 0\n" \
"v_mov_b32 v66, 0\n" \
"v_mov_b32 v67, 0\n" \
"v_mov_b32 v68, 0\n" \
"v_mov_b32 v69, 0\n" \
"v_mov_b32 v70, 0\n" \
"v_mov_b32 v71, 0\n" \
"v_mov_b32 v72, 0\n" \
"v_mov_b32 v73, 0\n" \
"v_mov_b32 v74, 0\n" \
"v_mov_b32 v75, 0\n" \
"v_mov_b32 v76, 0\n" \
"v_mov_b32 v77, 0\n" \
"v_mov_b32 v78, 0\n" \
"v_mov_b32 v79, 0\n" \
"v_mov_b32 v80, 0\n" \
"v_mov_b32 v81, 0\n" \
"v_mov_b32 v82, 0\n" \
"v_mov_b32 v83, 0\n" \
"v_mov_b32 v84, 0\n" \
"v_mov_b32 v85, 0\n" \
"v_mov_b32 v86, 0\n" \
"v_mov_b32 v87, 0\n" \
"v_mov_b32 v88, 0\n" \
"v_mov_b32 v89, 0\n" \
"v_mov_b32 v90, 0\n" \
"v_mov_b32 v91, 0\n" \
"v_mov_b32 v92, 0\n" \
"v_mov_b32 v93, 0\n" \
"v_mov_b32 v94, 0\n" \
"v_mov_b32 v95, 0\n" \
"v_mov_b32 v96, 0\n" \
"v_mov_b32 v97, 0\n" \
"v_mov_b32 v98, 0\n" \
"v_mov_b32 v99, 0\n" \
"v_mov_b32 v100, 0\n" \
"v_mov_b32 v101, 0\n" \
"v_mov_b32 v102, 0\n" \
"v_mov_b32 v103, 0\n" \
"v_mov_b32 v104, 0\n" \
"v_mov_b32 v105, 0\n" \
"v_mov_b32 v106, 0\n" \
"v_mov_b32 v107, 0\n" \
"v_mov_b32 v108, 0\n" \
"v_mov_b32 v109, 0\n" \
"v_mov_b32 v110, 0\n" \
"v_mov_b32 v111, 0\n" \
"v_mov_b32 v112, 0\n" \
"v_mov_b32 v113, 0\n" \
"v_mov_b32 v114, 0\n" \
"v_mov_b32 v115, 0\n" \
"v_mov_b32 v116, 0\n" \
"v_mov_b32 v117, 0\n" \
"v_mov_b32 v118, 0\n" \
"v_mov_b32 v119, 0\n" \
"v_mov_b32 v120, 0\n" \
"v_mov_b32 v121, 0\n" \
"v_mov_b32 v122, 0\n" \
"v_mov_b32 v123, 0\n" \
"v_mov_b32 v124, 0\n" \
"v_mov_b32 v125, 0\n" \
"v_mov_b32 v126, 0\n" \
"v_mov_b32 v127, 0\n" \
"v_accvgpr_write_b32 a40, 0\n" \
"v_accvgpr_write_b32 a41, 0\n" \
"v_accvgpr_write_b32 a42, 0\n" \
"v_accvgpr_write_b32 a43, 0\n" \
"v_accvgpr_write_b32 a44, 0\n" \
"v_accvgpr_write_b32 a45, 0\n" \
"v_accvgpr_write_b32 a46, 0\n" \
"v_accvgpr_write_b32 a47, 0\n" \
"s_lshl1_add_u32 s14, s14, 1\n" \
"s_mov_b32 s15, s73\n" \
".Lslr2_%=:\n" \
"s_add_u32 s82, s15, 0\n" \
"buffer_load_dwordx4 a[128:131], v248, s[32:35], s82 offen offset:0 sc1\n" \
"buffer_load_dwordx4 a[132:135], v248, s[32:35], s82 offen offset:1024 sc1\n" \
"buffer_load_dwordx4 a[136:139], v248, s[32:35], s82 offen offset:2048 sc1\n" \
"buffer_load_dwordx4 a[140:143], v248, s[32:35], s82 offen offset:3072 sc1\n" \
"s_add_u32 s82, s15, 4096\n" \
"buffer_load_dwordx4 a[144:147], v248, s[32:35], s82 offen offset:0 sc1\n" \
"buffer_load_dwordx4 a[148:151], v248, s[32:35], s82 offen offset:1024 sc1\n" \
"buffer_load_dwordx4 a[152:155], v248, s[32:35], s82 offen offset:2048 sc1\n" \
"buffer_load_dwordx4 a[156:159], v248, s[32:35], s82 offen offset:3072 sc1\n" \
"s_add_u32 s82, s15, 8192\n" \
"buffer_load_dwordx4 a[160:163], v248, s[32:35], s82 offen offset:0 sc1\n" \
"buffer_load_dwordx4 a[164:167], v248, s[32:35], s82 offen offset:1024 sc1\n" \
"buffer_load_dwordx4 a[168:171], v248, s[32:35], s82 offen offset:2048 sc1\n" \
"buffer_load_dwordx4 a[172:175], v248, s[32:35], s82 offen offset:3072 sc1\n" \
"s_add_u32 s82, s15, 12288\n" \
"buffer_load_dwordx4 a[176:179], v248, s[32:35], s82 offen offset:0 sc1\n" \
"buffer_load_dwordx4 a[180:183], v248, s[32:35], s82 offen offset:1024 sc1\n" \
"buffer_load_dwordx4 a[184:187], v248, s[32:35], s82 offen offset:2048 sc1\n" \
"buffer_load_dwordx4 a[188:191], v248, s[32:35], s82 offen offset:3072 sc1\n" \
"s_waitcnt vmcnt(0)\n" \
"v_mfma_f32_16x16x32_bf16 v[128:131], a[128:131], v[160:163], 0\n" \
"v_mfma_f32_16x16x32_bf16 v[128:131], a[132:135], v[164:167], v[128:131]\n" \
"v_mfma_f32_16x16x32_bf16 v[128:131], a[136:139], v[168:171], v[128:131]\n" \
"v_mfma_f32_16x16x32_bf16 v[128:131], a[140:143], v[172:175], v[128:131]\n" \
"v_mfma_f32_16x16x32_bf16 v[132:135], a[156:159], v[172:175], 0\n" \
"v_mfma_f32_16x16x32_bf16 v[132:135], a[152:155], v[168:171], v[132:135]\n" \
"v_mfma_f32_16x16x32_bf16 v[132:135], a[148:151], v[164:167], v[132:135]\n" \
"v_mfma_f32_16x16x32_bf16 v[132:135], a[144:147], v[160:163], v[132:135]\n" \
"v_mfma_f32_16x16x32_bf16 v[136:139], a[160:163], v[160:163], 0\n" \
"v_mfma_f32_16x16x32_bf16 v[136:139], a[164:167], v[164:167], v[136:139]\n" \
"v_mfma_f32_16x16x32_bf16 v[136:139], a[168:171], v[168:171], v[136:139]\n" \
"v_mfma_f32_16x16x32_bf16 v[136:139], a[172:175], v[172:175], v[136:139]\n" \
"v_mfma_f32_16x16x32_bf16 v[140:143], a[188:191], v[172:175], 0\n" \
"v_mfma_f32_16x16x32_bf16 v[140:143], a[184:187], v[168:171], v[140:143]\n" \
"v_mfma_f32_16x16x32_bf16 v[140:143], a[180:183], v[164:167], v[140:143]\n" \
"v_mfma_f32_16x16x32_bf16 v[140:143], a[176:179], v[160:163], v[140:143]\n" \
"v_cmp_gt_i32 vcc, 0, v194\n" \
"s_nop 1\n" \
"v_cndmask_b32 v128, v128, v199, vcc\n" \
"v_cmp_gt_i32 vcc, 1, v194\n" \
"s_nop 1\n" \
"v_cndmask_b32 v129, v129, v199, vcc\n" \
"v_cmp_gt_i32 vcc, 2, v194\n" \
"s_nop 1\n" \
"v_cndmask_b32 v130, v130, v199, vcc\n" \
"v_cmp_gt_i32 vcc, 3, v194\n" \
"s_nop 1\n" \
"v_cndmask_b32 v131, v131, v199, vcc\n" \
"v_cmp_gt_i32 vcc, 16, v194\n" \
"s_nop 1\n" \
"v_cndmask_b32 v132, v132, v199, vcc\n" \
"v_cmp_gt_i32 vcc, 17, v194\n" \
"s_nop 1\n" \
"v_cndmask_b32 v133, v133, v199, vcc\n" \
"v_cmp_gt_i32 vcc, 18, v194\n" \
"s_nop 1\n" \
"v_cndmask_b32 v134, v134, v199, vcc\n" \
"v_cmp_gt_i32 vcc, 19, v194\n" \
"s_nop 1\n" \
"v_cndmask_b32 v135, v135, v199, vcc\n" \
"v_cmp_gt_i32 vcc, 32, v194\n" \
"s_nop 1\n" \
"v_cndmask_b32 v136, v136, v199, vcc\n" \
"v_cmp_gt_i32 vcc, 33, v194\n" \
"s_nop 1\n" \
"v_cndmask_b32 v137, v137, v199, vcc\n" \
"v_cmp_gt_i32 vcc, 34, v194\n" \
"s_nop 1\n" \
"v_cndmask_b32 v138, v138, v199, vcc\n" \
"v_cmp_gt_i32 vcc, 35, v194\n" \
"s_nop 1\n" \
"v_cndmask_b32 v139, v139, v199, vcc\n" \
"v_cmp_gt_i32 vcc, 48, v194\n" \
"s_nop 1\n" \
"v_cndmask_b32 v140, v140, v199, vcc\n" \
"v_cmp_gt_i32 vcc, 49, v194\n" \
"s_nop 1\n" \
"v_cndmask_b32 v141, v141, v199, vcc\n" \
"v_cmp_gt_i32 vcc, 50, v194\n" \
"s_nop 1\n" \
"v_cndmask_b32 v142, v142, v199, vcc\n" \
"v_cmp_gt_i32 vcc, 51, v194\n" \
"s_nop 1\n" \
"v_cndmask_b32 v143, v143, v199, vcc\n" \
"v_max3_f32 v196, v192, v128, v129\n" \
"v_max3_f32 v196, v196, v130, v131\n" \
"v_max3_f32 v196, v196, v132, v133\n" \
"v_max3_f32 v196, v196, v134, v135\n" \
"v_max3_f32 v196, v196, v136, v137\n" \
"v_max3_f32 v196, v196, v138, v139\n" \
"v_max3_f32 v196, v196, v140, v141\n" \
"v_max3_f32 v196, v196, v142, v143\n" \
"v_mov_b32 v197, v196\n" \
"s_nop 1\n" \
"v_permlane32_swap_b32 v196, v197\n" \
"v_max_f32 v196, v196, v197\n" \
"v_mov_b32 v197, v196\n" \
"s_nop 1\n" \
"v_permlane16_swap_b32 v196, v197\n" \
"v_max_f32 v196, v196, v197\n" \
"v_sub_f32 v198, v192, v196\n" \
"v_mov_b32 v192, v196\n" \
"v_exp_f32 v198, v198\n" \
"v_subrev_u32 v194, 64, v194\n" \
"s_add_u32 s82, s15, 589824\n" \
"buffer_load_dwordx4 a[192:195], v248, s[32:35], s82 offen offset:0 sc1\n" \
"buffer_load_dwordx4 a[196:199], v248, s[32:35], s82 offen offset:1024 sc1\n" \
"buffer_load_dwordx4 a[200:203], v248, s[32:35], s82 offen offset:2048 sc1\n" \
"buffer_load_dwordx4 a[204:207], v248, s[32:35], s82 offen offset:3072 sc1\n" \
"s_add_u32 s82, s15, 593920\n" \
"buffer_load_dwordx4 a[208:211], v248, s[32:35], s82 offen offset:0 sc1\n" \
"buffer_load_dwordx4 a[212:215], v248, s[32:35], s82 offen offset:1024 sc1\n" \
"buffer_load_dwordx4 a[216:219], v248, s[32:35], s82 offen offset:2048 sc1\n" \
"buffer_load_dwordx4 a[220:223], v248, s[32:35], s82 offen offset:3072 sc1\n" \
"s_add_u32 s82, s15, 598016\n" \
"buffer_load_dwordx4 a[224:227], v248, s[32:35], s82 offen offset:0 sc1\n" \
"buffer_load_dwordx4 a[228:231], v248, s[32:35], s82 offen offset:1024 sc1\n" \
"buffer_load_dwordx4 a[232:235], v248, s[32:35], s82 offen offset:2048 sc1\n" \
"buffer_load_dwordx4 a[236:239], v248, s[32:35], s82 offen offset:3072 sc1\n" \
"s_add_u32 s82, s15, 602112\n" \
"buffer_load_dwordx4 a[240:243], v248, s[32:35], s82 offen offset:0 sc1\n" \
"buffer_load_dwordx4 a[244:247], v248, s[32:35], s82 offen offset:1024 sc1\n" \
"buffer_load_dwordx4 a[248:251], v248, s[32:35], s82 offen offset:2048 sc1\n" \
"buffer_load_dwordx4 a[252:255], v248, s[32:35], s82 offen offset:3072 sc1\n" \
"s_waitcnt vmcnt(0)\n" \
"v_sub_f32 v128, v128, v196\n" \
"v_sub_f32 v129, v129, v196\n" \
"v_sub_f32 v130, v130, v196\n" \
"v_sub_f32 v131, v131, v196\n" \
"v_sub_f32 v132, v132, v196\n" \
"v_sub_f32 v133, v133, v196\n" \
"v_sub_f32 v134, v134, v196\n" \
"v_sub_f32 v135, v135, v196\n" \
"v_sub_f32 v136, v136, v196\n" \
"v_sub_f32 v137, v137, v196\n" \
"v_sub_f32 v138, v138, v196\n" \
"v_sub_f32 v139, v139, v196\n" \
"v_sub_f32 v140, v140, v196\n" \
"v_sub_f32 v141, v141, v196\n" \
"v_sub_f32 v142, v142, v196\n" \
"v_sub_f32 v143, v143, v196\n" \
"v_exp_f32 v128, v128\n" \
"v_exp_f32 v129, v129\n" \
"v_exp_f32 v130, v130\n" \
"v_exp_f32 v131, v131\n" \
"v_exp_f32 v132, v132\n" \
"v_exp_f32 v133, v133\n" \
"v_exp_f32 v134, v134\n" \
"v_exp_f32 v135, v135\n" \
"v_exp_f32 v136, v136\n" \
"v_exp_f32 v137, v137\n" \
"v_exp_f32 v138, v138\n" \
"v_exp_f32 v139, v139\n" \
"v_exp_f32 v140, v140\n" \
"v_exp_f32 v141, v141\n" \
"v_exp_f32 v142, v142\n" \
"v_exp_f32 v143, v143\n" \
"v_cvt_pk_bf16_f32 v128, v128, v129\n" \
"v_cvt_pk_bf16_f32 v129, v130, v131\n" \
"v_cvt_pk_bf16_f32 v130, v132, v133\n" \
"v_cvt_pk_bf16_f32 v131, v134, v135\n" \
"v_cvt_pk_bf16_f32 v136, v136, v137\n" \
"v_cvt_pk_bf16_f32 v137, v138, v139\n" \
"v_cvt_pk_bf16_f32 v138, v140, v141\n" \
"v_cvt_pk_bf16_f32 v139, v142, v143\n" \
"v_mul_f32 v64, v198, v64\n" \
"v_mul_f32 v65, v198, v65\n" \
"v_mul_f32 v66, v198, v66\n" \
"v_mul_f32 v67, v198, v67\n" \
"v_mul_f32 v68, v198, v68\n" \
"v_mul_f32 v69, v198, v69\n" \
"v_mul_f32 v70, v198, v70\n" \
"v_mul_f32 v71, v198, v71\n" \
"v_mul_f32 v72, v198, v72\n" \
"v_mul_f32 v73, v198, v73\n" \
"v_mul_f32 v74, v198, v74\n" \
"v_mul_f32 v75, v198, v75\n" \
"v_mul_f32 v76, v198, v76\n" \
"v_mul_f32 v77, v198, v77\n" \
"v_mul_f32 v78, v198, v78\n" \
"v_mul_f32 v79, v198, v79\n" \
"v_mul_f32 v80, v198, v80\n" \
"v_mul_f32 v81, v198, v81\n" \
"v_mul_f32 v82, v198, v82\n" \
"v_mul_f32 v83, v198, v83\n" \
"v_mul_f32 v84, v198, v84\n" \
"v_mul_f32 v85, v198, v85\n" \
"v_mul_f32 v86, v198, v86\n" \
"v_mul_f32 v87, v198, v87\n" \
"v_mul_f32 v88, v198, v88\n" \
"v_mul_f32 v89, v198, v89\n" \
"v_mul_f32 v90, v198, v90\n" \
"v_mul_f32 v91, v198, v91\n" \
"v_mul_f32 v92, v198, v92\n" \
"v_mul_f32 v93, v198, v93\n" \
"v_mul_f32 v94, v198, v94\n" \
"v_mul_f32 v95, v198, v95\n" \
"v_accvgpr_read_b32 v197, a40\n" \
"v_mul_f32 v197, v198, v197\n" \
"v_accvgpr_write_b32 a40, v197\n" \
"v_accvgpr_read_b32 v197, a41\n" \
"v_mul_f32 v197, v198, v197\n" \
"v_accvgpr_write_b32 a41, v197\n" \
"v_accvgpr_read_b32 v197, a42\n" \
"v_mul_f32 v197, v198, v197\n" \
"v_accvgpr_write_b32 a42, v197\n" \
"v_accvgpr_read_b32 v197, a43\n" \
"v_mul_f32 v197, v198, v197\n" \
"v_accvgpr_write_b32 a43, v197\n" \
"s_nop 1\n" \
"v_mfma_f32_16x16x32_bf16 a[40:43], a[48:51], v[128:131], a[40:43]\n" \
"v_mfma_f32_16x16x32_bf16 a[40:43], a[48:51], v[136:139], a[40:43]\n" \
"v_mfma_f32_16x16x32_bf16 v[64:67], a[224:227], v[136:139], v[64:67]\n" \
"v_mfma_f32_16x16x32_bf16 v[64:67], a[192:195], v[128:131], v[64:67]\n" \
"v_mfma_f32_16x16x32_bf16 v[68:71], a[196:199], v[128:131], v[68:71]\n" \
"v_mfma_f32_16x16x32_bf16 v[68:71], a[228:231], v[136:139], v[68:71]\n" \
"v_mfma_f32_16x16x32_bf16 v[72:75], a[232:235], v[136:139], v[72:75]\n" \
"v_mfma_f32_16x16x32_bf16 v[72:75], a[200:203], v[128:131], v[72:75]\n" \
"v_mfma_f32_16x16x32_bf16 v[76:79], a[204:207], v[128:131], v[76:79]\n" \
"v_mfma_f32_16x16x32_bf16 v[76:79], a[236:239], v[136:139], v[76:79]\n" \
"v_mfma_f32_16x16x32_bf16 v[80:83], a[240:243], v[136:139], v[80:83]\n" \
"v_mfma_f32_16x16x32_bf16 v[80:83], a[208:211], v[128:131], v[80:83]\n" \
"v_mfma_f32_16x16x32_bf16 v[84:87], a[212:215], v[128:131], v[84:87]\n" \
"v_mfma_f32_16x16x32_bf16 v[84:87], a[244:247], v[136:139], v[84:87]\n" \
"v_mfma_f32_16x16x32_bf16 v[88:91], a[248:251], v[136:139], v[88:91]\n" \
"v_mfma_f32_16x16x32_bf16 v[88:91], a[216:219], v[128:131], v[88:91]\n" \
"v_mfma_f32_16x16x32_bf16 v[92:95], a[220:223], v[128:131], v[92:95]\n" \
"v_mfma_f32_16x16x32_bf16 v[92:95], a[252:255], v[136:139], v[92:95]\n" \
"s_nop 7\n" \
"v_mfma_f32_16x16x32_bf16 v[128:131], a[128:131], v[176:179], 0\n" \
"v_mfma_f32_16x16x32_bf16 v[128:131], a[132:135], v[180:183], v[128:131]\n" \
"v_mfma_f32_16x16x32_bf16 v[128:131], a[136:139], v[184:187], v[128:131]\n" \
"v_mfma_f32_16x16x32_bf16 v[128:131], a[140:143], v[188:191], v[128:131]\n" \
"v_mfma_f32_16x16x32_bf16 v[132:135], a[156:159], v[188:191], 0\n" \
"v_mfma_f32_16x16x32_bf16 v[132:135], a[152:155], v[184:187], v[132:135]\n" \
"v_mfma_f32_16x16x32_bf16 v[132:135], a[148:151], v[180:183], v[132:135]\n" \
"v_mfma_f32_16x16x32_bf16 v[132:135], a[144:147], v[176:179], v[132:135]\n" \
"v_mfma_f32_16x16x32_bf16 v[136:139], a[160:163], v[176:179], 0\n" \
"v_mfma_f32_16x16x32_bf16 v[136:139], a[164:167], v[180:183], v[136:139]\n" \
"v_mfma_f32_16x16x32_bf16 v[136:139], a[168:171], v[184:187], v[136:139]\n" \
"v_mfma_f32_16x16x32_bf16 v[136:139], a[172:175], v[188:191], v[136:139]\n" \
"v_mfma_f32_16x16x32_bf16 v[140:143], a[188:191], v[188:191], 0\n" \
"v_mfma_f32_16x16x32_bf16 v[140:143], a[184:187], v[184:187], v[140:143]\n" \
"v_mfma_f32_16x16x32_bf16 v[140:143], a[180:183], v[180:183], v[140:143]\n" \
"v_mfma_f32_16x16x32_bf16 v[140:143], a[176:179], v[176:179], v[140:143]\n" \
"v_cmp_gt_i32 vcc, 0, v195\n" \
"s_nop 1\n" \
"v_cndmask_b32 v128, v128, v199, vcc\n" \
"v_cmp_gt_i32 vcc, 1, v195\n" \
"s_nop 1\n" \
"v_cndmask_b32 v129, v129, v199, vcc\n" \
"v_cmp_gt_i32 vcc, 2, v195\n" \
"s_nop 1\n" \
"v_cndmask_b32 v130, v130, v199, vcc\n" \
"v_cmp_gt_i32 vcc, 3, v195\n" \
"s_nop 1\n" \
"v_cndmask_b32 v131, v131, v199, vcc\n" \
"v_cmp_gt_i32 vcc, 16, v195\n" \
"s_nop 1\n" \
"v_cndmask_b32 v132, v132, v199, vcc\n" \
"v_cmp_gt_i32 vcc, 17, v195\n" \
"s_nop 1\n" \
"v_cndmask_b32 v133, v133, v199, vcc\n" \
"v_cmp_gt_i32 vcc, 18, v195\n" \
"s_nop 1\n" \
"v_cndmask_b32 v134, v134, v199, vcc\n" \
"v_cmp_gt_i32 vcc, 19, v195\n" \
"s_nop 1\n" \
"v_cndmask_b32 v135, v135, v199, vcc\n" \
"v_cmp_gt_i32 vcc, 32, v195\n" \
"s_nop 1\n" \
"v_cndmask_b32 v136, v136, v199, vcc\n" \
"v_cmp_gt_i32 vcc, 33, v195\n" \
"s_nop 1\n" \
"v_cndmask_b32 v137, v137, v199, vcc\n" \
"v_cmp_gt_i32 vcc, 34, v195\n" \
"s_nop 1\n" \
"v_cndmask_b32 v138, v138, v199, vcc\n" \
"v_cmp_gt_i32 vcc, 35, v195\n" \
"s_nop 1\n" \
"v_cndmask_b32 v139, v139, v199, vcc\n" \
"v_cmp_gt_i32 vcc, 48, v195\n" \
"s_nop 1\n" \
"v_cndmask_b32 v140, v140, v199, vcc\n" \
"v_cmp_gt_i32 vcc, 49, v195\n" \
"s_nop 1\n" \
"v_cndmask_b32 v141, v141, v199, vcc\n" \
"v_cmp_gt_i32 vcc, 50, v195\n" \
"s_nop 1\n" \
"v_cndmask_b32 v142, v142, v199, vcc\n" \
"v_cmp_gt_i32 vcc, 51, v195\n" \
"s_nop 1\n" \
"v_cndmask_b32 v143, v143, v199, vcc\n" \
"v_max3_f32 v196, v193, v128, v129\n" \
"v_max3_f32 v196, v196, v130, v131\n" \
"v_max3_f32 v196, v196, v132, v133\n" \
"v_max3_f32 v196, v196, v134, v135\n" \
"v_max3_f32 v196, v196, v136, v137\n" \
"v_max3_f32 v196, v196, v138, v139\n" \
"v_max3_f32 v196, v196, v140, v141\n" \
"v_max3_f32 v196, v196, v142, v143\n" \
"v_mov_b32 v197, v196\n" \
"s_nop 1\n" \
"v_permlane32_swap_b32 v196, v197\n" \
"v_max_f32 v196, v196, v197\n" \
"v_mov_b32 v197, v196\n" \
"s_nop 1\n" \
"v_permlane16_swap_b32 v196, v197\n" \
"v_max_f32 v196, v196, v197\n" \
"v_sub_f32 v198, v193, v196\n" \
"v_mov_b32 v193, v196\n" \
"v_exp_f32 v198, v198\n" \
"v_subrev_u32 v195, 64, v195\n" \
"v_sub_f32 v128, v128, v196\n" \
"v_sub_f32 v129, v129, v196\n" \
"v_sub_f32 v130, v130, v196\n" \
"v_sub_f32 v131, v131, v196\n" \
"v_sub_f32 v132, v132, v196\n" \
"v_sub_f32 v133, v133, v196\n" \
"v_sub_f32 v134, v134, v196\n" \
"v_sub_f32 v135, v135, v196\n" \
"v_sub_f32 v136, v136, v196\n" \
"v_sub_f32 v137, v137, v196\n" \
"v_sub_f32 v138, v138, v196\n" \
"v_sub_f32 v139, v139, v196\n" \
"v_sub_f32 v140, v140, v196\n" \
"v_sub_f32 v141, v141, v196\n" \
"v_sub_f32 v142, v142, v196\n" \
"v_sub_f32 v143, v143, v196\n" \
"v_exp_f32 v128, v128\n" \
"v_exp_f32 v129, v129\n" \
"v_exp_f32 v130, v130\n" \
"v_exp_f32 v131, v131\n" \
"v_exp_f32 v132, v132\n" \
"v_exp_f32 v133, v133\n" \
"v_exp_f32 v134, v134\n" \
"v_exp_f32 v135, v135\n" \
"v_exp_f32 v136, v136\n" \
"v_exp_f32 v137, v137\n" \
"v_exp_f32 v138, v138\n" \
"v_exp_f32 v139, v139\n" \
"v_exp_f32 v140, v140\n" \
"v_exp_f32 v141, v141\n" \
"v_exp_f32 v142, v142\n" \
"v_exp_f32 v143, v143\n" \
"v_cvt_pk_bf16_f32 v128, v128, v129\n" \
"v_cvt_pk_bf16_f32 v129, v130, v131\n" \
"v_cvt_pk_bf16_f32 v130, v132, v133\n" \
"v_cvt_pk_bf16_f32 v131, v134, v135\n" \
"v_cvt_pk_bf16_f32 v136, v136, v137\n" \
"v_cvt_pk_bf16_f32 v137, v138, v139\n" \
"v_cvt_pk_bf16_f32 v138, v140, v141\n" \
"v_cvt_pk_bf16_f32 v139, v142, v143\n" \
"v_mul_f32 v96, v198, v96\n" \
"v_mul_f32 v97, v198, v97\n" \
"v_mul_f32 v98, v198, v98\n" \
"v_mul_f32 v99, v198, v99\n" \
"v_mul_f32 v100, v198, v100\n" \
"v_mul_f32 v101, v198, v101\n" \
"v_mul_f32 v102, v198, v102\n" \
"v_mul_f32 v103, v198, v103\n" \
"v_mul_f32 v104, v198, v104\n" \
"v_mul_f32 v105, v198, v105\n" \
"v_mul_f32 v106, v198, v106\n" \
"v_mul_f32 v107, v198, v107\n" \
"v_mul_f32 v108, v198, v108\n" \
"v_mul_f32 v109, v198, v109\n" \
"v_mul_f32 v110, v198, v110\n" \
"v_mul_f32 v111, v198, v111\n" \
"v_mul_f32 v112, v198, v112\n" \
"v_mul_f32 v113, v198, v113\n" \
"v_mul_f32 v114, v198, v114\n" \
"v_mul_f32 v115, v198, v115\n" \
"v_mul_f32 v116, v198, v116\n" \
"v_mul_f32 v117, v198, v117\n" \
"v_mul_f32 v118, v198, v118\n" \
"v_mul_f32 v119, v198, v119\n" \
"v_mul_f32 v120, v198, v120\n" \
"v_mul_f32 v121, v198, v121\n" \
"v_mul_f32 v122, v198, v122\n" \
"v_mul_f32 v123, v198, v123\n" \
"v_mul_f32 v124, v198, v124\n" \
"v_mul_f32 v125, v198, v125\n" \
"v_mul_f32 v126, v198, v126\n" \
"v_mul_f32 v127, v198, v127\n" \
"v_accvgpr_read_b32 v197, a44\n" \
"v_mul_f32 v197, v198, v197\n" \
"v_accvgpr_write_b32 a44, v197\n" \
"v_accvgpr_read_b32 v197, a45\n" \
"v_mul_f32 v197, v198, v197\n" \
"v_accvgpr_write_b32 a45, v197\n" \
"v_accvgpr_read_b32 v197, a46\n" \
"v_mul_f32 v197, v198, v197\n" \
"v_accvgpr_write_b32 a46, v197\n" \
"v_accvgpr_read_b32 v197, a47\n" \
"v_mul_f32 v197, v198, v197\n" \
"v_accvgpr_write_b32 a47, v197\n" \
"s_nop 1\n" \
"v_mfma_f32_16x16x32_bf16 a[44:47], a[48:51], v[128:131], a[44:47]\n" \
"v_mfma_f32_16x16x32_bf16 a[44:47], a[48:51], v[136:139], a[44:47]\n" \
"v_mfma_f32_16x16x32_bf16 v[96:99], a[224:227], v[136:139], v[96:99]\n" \
"v_mfma_f32_16x16x32_bf16 v[96:99], a[192:195], v[128:131], v[96:99]\n" \
"v_mfma_f32_16x16x32_bf16 v[100:103], a[196:199], v[128:131], v[100:103]\n" \
"v_mfma_f32_16x16x32_bf16 v[100:103], a[228:231], v[136:139], v[100:103]\n" \
"v_mfma_f32_16x16x32_bf16 v[104:107], a[232:235], v[136:139], v[104:107]\n" \
"v_mfma_f32_16x16x32_bf16 v[104:107], a[200:203], v[128:131], v[104:107]\n" \
"v_mfma_f32_16x16x32_bf16 v[108:111], a[204:207], v[128:131], v[108:111]\n" \
"v_mfma_f32_16x16x32_bf16 v[108:111], a[236:239], v[136:139], v[108:111]\n" \
"v_mfma_f32_16x16x32_bf16 v[112:115], a[240:243], v[136:139], v[112:115]\n" \
"v_mfma_f32_16x16x32_bf16 v[112:115], a[208:211], v[128:131], v[112:115]\n" \
"v_mfma_f32_16x16x32_bf16 v[116:119], a[212:215], v[128:131], v[116:119]\n" \
"v_mfma_f32_16x16x32_bf16 v[116:119], a[244:247], v[136:139], v[116:119]\n" \
"v_mfma_f32_16x16x32_bf16 v[120:123], a[248:251], v[136:139], v[120:123]\n" \
"v_mfma_f32_16x16x32_bf16 v[120:123], a[216:219], v[128:131], v[120:123]\n" \
"v_mfma_f32_16x16x32_bf16 v[124:127], a[220:223], v[128:131], v[124:127]\n" \
"v_mfma_f32_16x16x32_bf16 v[124:127], a[252:255], v[136:139], v[124:127]\n" \
"s_nop 7\n" \
"s_add_u32 s15, s15, 0x4000\n" \
"s_sub_u32 s14, s14, 1\n" \
"s_cbranch_scc0 .Lslr2_%=\n" \
"s_nop 7\n" \
"s_setpc_b64 s[98:99]\n" \
".Lspr2_%=:\n" \
"s_mov_b32 s82, 0x100000\n" \
".Lsqr2_%=:\n" \
"s_sleep 6\n" \
"buffer_load_dword v197, v196, s[32:35], s26 offen sc1\n" \
"s_waitcnt vmcnt(0)\n" \
"v_cmp_eq_u32 vcc, 0, v197\n" \
"s_cbranch_vccz .Lsbr2_%=\n" \
"s_sub_u32 s82, s82, 1\n" \
"s_cbranch_scc0 .Lsqr2_%=\n" \
"s_branch .Lsbr2_%=\n" \
".Lend_%=:\n" \
".Lst3_5_%=:\n" \
"s_cbranch_execz .Lstb3_%=\n"
struct Desc { const void* p; unsigned bytes, flags; };
struct Args { Desc ckv, w_uk, w_uv, w_q, ws, w_mod, cvec, x, w_o, wg, wu, wd, out; const void* w_r; };
constexpr size_t WS_DBG = (size_t)56 << 20; constexpr unsigned WS_BYTES = 80u << 20, LDS_BYTES = 163840;
typedef const __attribute__((address_space(4))) Args* kargs_t;
typedef unsigned u32x16 __attribute__((ext_vector_type(16)));
#define VCLOB_LOW "v0","v1","v2","v3","v4","v5","v6","v7","v8","v9","v10","v11","v12","v13","v14","v15","v16","v17","v18","v19","v20","v21","v22","v23","v24","v25","v26","v27","v28","v29","v30","v31","v32","v33","v34","v35","v36","v37","v38","v39","v40","v41","v42","v43","v44","v45","v46","v47","v48","v49","v50","v51","v52","v53","v54","v55","v56","v57","v58","v59","v60","v61","v62","v63","v64","v65","v66","v67","v68","v69","v70","v71","v72","v73","v74","v75","v76","v77","v78","v79","v80","v81","v82","v83","v84","v85","v86","v87","v88","v89","v90","v91","v92","v93","v94","v95","v96","v97","v98","v99","v100","v101","v102","v103","v104","v105","v106","v107","v108","v109","v110","v111","v112","v113","v114","v115","v116","v117","v118","v119","v120","v121","v122","v123","v124","v125","v126","v127","v128","v129","v130","v131","v132","v133","v134","v135","v136","v137","v138","v139","v140","v141","v142","v143","v144","v145","v146","v147","v148","v149","v150","v151","v152","v153","v154","v155","v156","v157","v158","v159","v160","v161","v162","v163","v164","v165","v166","v167","v168","v169","v170","v171","v172","v173","v174","v175","v176","v177","v178","v179","v180","v181","v182","v183","v184","v185","v186","v187","v188","v189","v190","v191","v192","v193","v194","v195","v196","v197","v198","v199","v200","v201","v202","v203","v204","v205","v206","v207","v208","v209","v210","v211","v212","v213","v214","v215","v216","v217","v218","v219","v220","v221","v222","v223","v224","v225","v226","v227","v228","v229","v230","v231","v232","v233","v234","v235","v236","v237","v238","v239"
#define SCLOB "s14","s15","s16","s17","s18","s19","s20","s21","s22","s23","s24","s25","s26","s27","s28","s29","s30","s31","s32","s33","s34","s35","s36","s37","s38","s39","s40","s41","s42","s43","s44","s45","s46","s47","s48","s49","s50","s51","s52","s53","s54","s55","s56","s57","s58","s59","s60","s61","s62","s63","s64","s65","s66","s67","s68","s69","s70","s71","s72","s73","s74","s75","s76","s77","s78","s79","s80","s81","s82","s83","s84","s85","s86","s87","s88","s89","s90","s91","s92","s93","s94","s95","s96","s97","s98","s99","s100","s101"
__global__ __launch_bounds__(256, 1) __attribute__((aligned(4096))) void k_all(const Args a) {
    unsigned wid = __builtin_amdgcn_readfirstlane(threadIdx.x >> 6);
    u32x16 RB;
    asm volatile(MAIN_ASM : "={v[240:255]}"(RB)
                 : [ka] "s"(__builtin_amdgcn_kernarg_segment_ptr()), [bid] "s"(blockIdx.x), [wid] "s"(wid) : "memory", "vcc", "scc", VCLOB_LOW, SCLOB, ACLOB_ALL);
    asm volatile(".Lat_code_e:");
}
extern "C" void kernel_launch(void* const* d_in, const int* in_sizes, int n_in, void* d_out, int out_size, void* d_ws, size_t ws_size, hipStream_t stream) {
    Args a;
    auto D = [](const void* p, unsigned bytes) { return Desc{p, bytes, 0x00020000u}; };
    a.ckv = D(d_in[1], SEQ * DM * 2); a.w_uk = D(d_in[3], DM * HD * 2); a.w_uv = D(d_in[4], DM * HD * 2); a.w_q = D(d_in[2], DM * HD * 2);
    a.ws = D(d_ws, WS_BYTES); a.w_mod = D(d_in[12], DM * 6 * DM * 2); a.cvec = D(d_in[11], DM * 2); a.x = D(d_in[0], SEQ * DM * 2);
    a.w_o = D(d_in[5], HD * DM * 2); a.wg = D(d_in[8], DM * FF * 2); a.wu = D(d_in[9], DM * FF * 2); a.wd = D(d_in[10], FF * DM * 2);
    a.out = D(d_out, SEQ * DM * 2); a.w_r = d_in[7];
    (void)hipFuncSetAttribute((const void*)k_all, hipFuncAttributeMaxDynamicSharedMemorySize, LDS_BYTES);
    k_all<<<256, 256, LDS_BYTES, stream>>>(a);
}
```

```cpp
#include <hip/hip_runtime.h>
#include <stdint.h>
constexpr int SEQ = 2048, DM = 512, HD = 4096, FF = 1792;
#define ACLOB_ALL "a0","a1","a2","a3","a4","a5","a6","a7","a8","a9","a10","a11","a12","a13","a14","a15","a16","a17","a18","a19","a20","a21","a22","a23","a24","a25","a26","a27","a28","a29","a30","a31","a32","a33","a34","a35","a36","a37","a38","a39","a40","a41","a42","a43","a44","a45","a46","a47","a48","a49","a50","a51","a52","a53","a54","a55","a56","a57","a58","a59","a60","a61","a62","a63","a64","a65","a66","a67","a68","a69","a70","a71","a72","a73","a74","a75","a76","a77","a78","a79","a80","a81","a82","a83","a84","a85","a86","a87","a88","a89","a90","a91","a92","a93","a94","a95","a96","a97","a98","a99","a100","a101","a102","a103","a104","a105","a106","a107","a108","a109","a110","a111","a112","a113","a114","a115","a116","a117","a118","a119","a120","a121","a122","a123","a124","a125","a126","a127","a128","a129","a130","a131","a132","a133","a134","a135","a136","a137","a138","a139","a140","a141","a142","a143","a144","a145","a146","a147","a148","a149","a150","a151","a152","a153","a154","a155","a156","a157","a158","a159","a160","a161","a162","a163","a164","a165","a166","a167","a168","a169","a170","a171","a172","a173","a174","a175","a176","a177","a178","a179","a180","a181","a182","a183","a184","a185","a186","a187","a188","a189","a190","a191","a192","a193","a194","a195","a196","a197","a198","a199","a200","a201","a202","a203","a204","a205","a206","a207","a208","a209","a210","a211","a212","a213","a214","a215","a216","a217","a218","a219","a220","a221","a222","a223","a224","a225","a226","a227","a228","a229","a230","a231","a232","a233","a234","a235","a236","a237","a238","a239","a240","a241","a242","a243","a244","a245","a246","a247","a248","a249","a250","a251","a252","a253","a254","a255"
#define MAIN_ASM \
  \
".p2align 4\n" \
"s_load_dwordx16 s[16:31], %[ka], 0\n" \
"s_load_dwordx16 s[32:47], %[ka], 64\n" \
"s_load_dwordx16 s[48:63], %[ka], 128\n" \
"v_mbcnt_lo_u32_b32 v251, -1, 0\n" \
"v_mbcnt_hi_u32_b32 v251, -1, v251\n" \
"s_and_b32 s64, %[bid], 7\n" \
"s_lshr_b32 s65, %[bid], 3\n" \
"s_and_b32 s66, s65, 3\n" \
"s_lshr_b32 s67, %[bid], 5\n" \
"s_lshl2_add_u32 s68, s64, s66\n" \
"v_lshlrev_b32 v248, 4, v251\n" \
"v_lshlrev_b32 v0, 7, v251\n" \
"s_getpc_b64 s[96:97]\n" \
".Lpf0_%=:\n" \
"s_and_b32 s97, s97, 0xffff\n" \
"s_mov_b32 s98, .Lat_code_e-.Lpf0_%=\n" \
"s_mov_b32 s99, 0x20000\n" \
"s_lshl2_add_u32 s74, s66, %[wid]\n" \
"s_lshl_b32 s74, s74, 13\n" \
"buffer_load_dword v245, v0, s[96:99], s74 offen\n" \
"v_bfe_u32 v1, v251, 2, 2\n" \
"v_bfe_u32 v2, v251, 4, 1\n" \
"v_lshl_add_u32 v1, v2, 3, v1\n" \
"v_lshlrev_b32 v1, 13, v1\n" \
"v_lshrrev_b32 v3, 5, v251\n" \
"v_lshl_or_b32 v1, v3, 6, v1\n" \
"v_and_b32 v4, 3, v251\n" \
"v_lshlrev_b32 v5, 1, v2\n" \
"v_xor_b32 v4, v4, v5\n" \
"v_lshl_or_b32 v246, v4, 4, v1\n" \
"v_lshrrev_b32 v6, 3, v251\n" \
"v_and_b32 v7, 7, v251\n" \
"v_lshrrev_b32 v8, 4, v251\n" \
"v_xor_b32 v9, v7, v8\n" \
"v_lshlrev_b32 v9, 4, v9\n" \
"v_lshl_or_b32 v249, v6, 10, v9\n" \
"v_xor_b32 v250, 64, v249\n" \
"s_sub_u32 s76, %[bid], 192\n" \
"s_min_u32 s76, s76, %[bid]\n" \
"s_mul_i32 s77, s76, 1366\n" \
"s_lshr_b32 s77, s77, 16\n" \
"s_mul_i32 s78, s77, 48\n" \
"s_sub_u32 s76, s76, s78\n" \
"s_lshl2_add_u32 s78, s77, %[wid]\n" \
"s_lshl_b32 s78, s78, 5\n" \
"v_lshl_add_u32 v10, v6, 2, s78\n" \
"v_lshlrev_b32 v11, 1, v10\n" \
"v_mul_u32_u24 v12, 0x1800, v10\n" \
"v_lshl_add_u32 v12, v7, 4, v12\n" \
"s_lshl_b32 s74, s76, 7\n" \
"s_add_u32 s74, s74, 3072\n" \
"v_add_u32 v12, s74, v12\n" \
"v_add_u32 v13, 0x3000, v12\n" \
"v_mov_b32 v14, 720896\n" \
"s_mul_i32 s81, s77, 12288\n" \
"s_lshl_b32 s74, s76, 8\n" \
"s_add_u32 s81, s81, s74\n" \
"s_add_u32 s81, s81, 917504\n" \
"s_lshl_b32 s74, %[wid], 8\n" \
"s_or_b32 s74, s74, %[bid]\n" \
"s_cmp_lt_u32 s74, 192\n" \
"s_cselect_b32 s81, s81, 1073741824\n" \
"s_lshr_b32 s74, %[wid], 1\n" \
"s_lshl_b32 s69, s74, 17\n" \
"s_and_b32 s74, %[wid], 1\n" \
"s_lshl_b32 s74, s74, 15\n" \
"s_add_u32 s69, s69, s74\n" \
"s_lshl_b32 s74, s68, 8\n" \
"s_add_u32 s69, s69, s74\n" \
"s_lshl_b32 s70, %[wid], 11\n" \
"s_add_u32 s70, s70, 65536\n" \
"s_lshl2_add_u32 s71, s67, %[wid]\n" \
"s_lshl_b32 s71, s71, 15\n" \
"s_lshl_b32 s72, %[wid], 14\n" \
"s_lshl_b32 s74, s67, 18\n" \
"s_sub_u32 s94, s71, s74\n" \
"s_add_u32 s94, s94, 1933312\n" \
"s_sub_u32 s74, 15, s67\n" \
"s_cmp_lt_u32 s68, 16\n" \
"s_cselect_b32 s74, s67, s74\n" \
"s_lshl_b32 s74, s74, 4\n" \
"s_bfe_u32 s76, s68, 0x20002\n" \
"s_sub_u32 s78, 7, s76\n" \
"s_bitcmp1_b32 s68, 1\n" \
"s_cselect_b32 s76, s78, s76\n" \
"s_lshl1_add_u32 s74, s76, s74\n" \
"s_and_b32 s76, s68, 1\n" \
"s_add_u32 s74, s74, s76\n" \
"s_lshl_b32 s78, s74, 3\n" \
"s_lshl1_add_u32 s74, %[wid], s78\n" \
"s_lshl_b32 s75, s74, 10\n" \
"s_waitcnt lgkmcnt(0)\n" \
"global_load_dwordx2 v[16:17], v11, s[40:41]\n" \
"global_load_dwordx4 v[20:23], v12, s[36:37] offset:-3072 nt\n" \
"global_load_dwordx4 v[24:27], v12, s[36:37] offset:3072 nt\n" \
"global_load_dwordx4 v[28:31], v13, s[36:37] offset:-3072 nt\n" \
"global_load_dwordx4 v[32:35], v13, s[36:37] offset:3072 nt\n" \
"s_add_u32 m0, s70, 0\n" \
"s_add_u32 s74, s69, 0\n" \
"buffer_load_dwordx4 v246, s[20:23], s74 offen lds\n" \
"s_add_u32 m0, s70, 1024\n" \
"s_add_u32 s74, s69, 128\n" \
"buffer_load_dwordx4 v246, s[20:23], s74 offen lds\n" \
"s_add_u32 m0, s72, 0\n" \
"s_add_u32 s74, s71, 0\n" \
"buffer_load_dwordx4 v249, s[16:19], s74 offen lds\n" \
"s_add_u32 m0, s72, 1024\n" \
"s_add_u32 s74, s71, 8192\n" \
"buffer_load_dwordx4 v250, s[16:19], s74 offen lds\n" \
"s_add_u32 m0, s72, 2048\n" \
"s_add_u32 s74, s71, 16384\n" \
"buffer_load_dwordx4 v249, s[16:19], s74 offen lds\n" \
"s_add_u32 m0, s72, 3072\n" \
"s_add_u32 s74, s71, 24576\n" \
"buffer_load_dwordx4 v250, s[16:19], s74 offen lds\n" \
"s_add_u32 m0, s72, 4096\n" \
"s_add_u32 s74, s94, 32768\n" \
"buffer_load_dwordx4 v249, s[16:19], s74 offen lds\n" \
"s_add_u32 m0, s72, 5120\n" \
"s_add_u32 s74, s94, 40960\n" \
"buffer_load_dwordx4 v250, s[16:19], s74 offen lds\n" \
"s_add_u32 m0, s72, 6144\n" \
"s_add_u32 s74, s94, 49152\n" \
"buffer_load_dwordx4 v249, s[16:19], s74 offen lds\n" \
"s_add_u32 m0, s72, 7168\n" \
"s_add_u32 s74, s94, 57344\n" \
"buffer_load_dwordx4 v250, s[16:19], s74 offen lds\n" \
"s_add_u32 m0, s70, 8192\n" \
"s_add_u32 s74, s69, 262144\n" \
"buffer_load_dwordx4 v246, s[20:23], s74 offen lds\n" \
"s_add_u32 m0, s70, 9216\n" \
"s_add_u32 s74, s69, 262272\n" \
"buffer_load_dwordx4 v246, s[20:23], s74 offen lds\n" \
"s_mov_b64 exec, 0\n" \
"s_cmp_eq_u32 %[wid], 0\n" \
"s_cbranch_scc1 .Lst0_0_%=\n" \
"s_cmp_eq_u32 %[wid], 1\n" \
"s_cbranch_scc1 .Lst0_1_%=\n" \
"s_cmp_eq_u32 %[wid], 2\n" \
"s_cbranch_scc1 .Lst0_2_%=\n" \
"s_branch .Lst0_3_%=\n" \
".Lstb0_%=:\n" \
"s_mov_b64 exec, -1\n" \
"v_mov_b32 v224, -1\n" \
"v_mov_b32 v225, -1\n" \
"s_min_u32 s74, %[bid], 223\n" \
"s_lshl_b32 s74, s74, 11\n" \
"s_lshl_b32 s76, %[wid], 9\n" \
"s_add_u32 s74, s74, s76\n" \
"s_add_u32 s74, s74, 98304\n" \
"v_lshl_add_u32 v228, v251, 3, s74\n" \
"s_lshl_b32 s74, %[wid], 8\n" \
"s_or_b32 s74, s74, %[bid]\n" \
"s_lshl_b32 s76, %[bid], 7\n" \
"s_add_u32 s76, s76, 65536\n" \
"s_cmp_lt_u32 s74, 256\n" \
"s_cselect_b32 s77, s76, 1073741824\n" \
"s_add_u32 s76, s76, 491392\n" \
"s_cmp_lt_u32 s74, 128\n" \
"s_cselect_b32 s76, s76, 1073741824\n" \
"v_mov_b32 v37, s77\n" \
"v_mov_b32 v38, s76\n" \
"v_cmp_gt_u32 vcc, 32, v251\n" \
"v_mov_b32 v230, 0\n" \
"s_lshl_b32 s77, %[wid], 16\n" \
"v_cndmask_b32 v37, v38, v37, vcc\n" \
"s_lshl_b32 s76, %[bid], 8\n" \
"s_add_u32 s76, s76, s77\n" \
"s_add_u32 s76, s76, 524288\n" \
"v_mov_b32 v38, s76\n" \
"s_sub_u32 s77, %[wid], 1\n" \
"s_cmp_lt_u32 s77, 2\n" \
"s_cselect_b64 vcc, -1, 0\n" \
"v_cndmask_b32 v37, v37, v38, vcc\n" \
"v_lshl_add_u32 v229, v251, 2, v37\n" \
"v_lshlrev_b32 v40, 12, v3\n" \
"v_lshl_or_b32 v40, v2, 8, v40\n" \
"v_bfe_u32 v41, v251, 2, 2\n" \
"v_lshl_or_b32 v40, v41, 6, v40\n" \
"v_lshl_or_b32 v40, v2, 5, v40\n" \
"v_and_b32 v41, 3, v251\n" \
"v_lshl_or_b32 v40, v41, 3, v40\n" \
"v_add_u32 v240, 65536, v40\n" \
"v_xor_b32 v241, 32, v240\n" \
"v_and_b32 v42, 15, v251\n" \
"v_bfe_u32 v43, v251, 1, 3\n" \
"v_xor_b32 v43, v43, v8\n" \
"v_lshlrev_b32 v43, 4, v43\n" \
"v_lshl_or_b32 v43, v42, 7, v43\n" \
"v_add_u32 v242, s72, v43\n" \
"v_xor_b32 v244, 64, v242\n" \
"s_mov_b32 s76, 0x05040100\n" \
"s_mov_b32 s77, 0x07060302\n" \
"s_add_u32 m0, s70, 16384\n" \
"s_add_u32 s74, s69, 0\n" \
"buffer_load_dwordx4 v246, s[24:27], s74 offen lds\n" \
"s_add_u32 m0, s70, 17408\n" \
"s_add_u32 s74, s69, 128\n" \
"buffer_load_dwordx4 v246, s[24:27], s74 offen lds\n" \
"s_waitcnt vmcnt(14)\n" \
"v_lshlrev_b32 v44, 16, v16\n" \
"v_and_b32 v45, 0xffff0000, v16\n" \
"v_lshlrev_b32 v46, 16, v17\n" \
"v_and_b32 v47, 0xffff0000, v17\n" \
"v_mul_f32 v48, 0xbfb8aa3b, v44\n" \
"v_mul_f32 v49, 0xbfb8aa3b, v45\n" \
"v_mul_f32 v50, 0xbfb8aa3b, v46\n" \
"v_mul_f32 v51, 0xbfb8aa3b, v47\n" \
"v_exp_f32 v48, v48\n" \
"v_exp_f32 v49, v49\n" \
"v_exp_f32 v50, v50\n" \
"v_exp_f32 v51, v51\n" \
"v_add_f32 v48, 1.0, v48\n" \
"v_add_f32 v49, 1.0, v49\n" \
"v_add_f32 v50, 1.0, v50\n" \
"v_add_f32 v51, 1.0, v51\n" \
"v_rcp_f32 v48, v48\n" \
"v_rcp_f32 v49, v49\n" \
"v_rcp_f32 v50, v50\n" \
"v_rcp_f32 v51, v51\n" \
"v_mul_f32 v44, v44, v48\n" \
"v_mul_f32 v45, v45, v49\n" \
"v_mul_f32 v46, v46, v50\n" \
"v_mul_f32 v47, v47, v51\n" \
"v_cvt_pk_bf16_f32 v44, v44, v45\n" \
"v_cvt_pk_bf16_f32 v45, v46, v47\n" \
"s_add_u32 m0, s70, 24576\n" \
"s_add_u32 s74, s69, 262144\n" \
"buffer_load_dwordx4 v246, s[24:27], s74 offen lds\n" \
"v_perm_b32 v64, v24, v20, s76\n" \
"v_perm_b32 v65, v24, v20, s77\n" \
"v_perm_b32 v66, v25, v21, s76\n" \
"v_perm_b32 v67, v25, v21, s77\n" \
"v_perm_b32 v68, v26, v22, s76\n" \
"v_perm_b32 v69, v26, v22, s77\n" \
"v_perm_b32 v70, v27, v23, s76\n" \
"v_perm_b32 v71, v27, v23, s77\n" \
"v_dot2_f32_bf16 v52, v64, v44, 0\n" \
"v_dot2_f32_bf16 v53, v65, v44, 0\n" \
"v_dot2_f32_bf16 v54, v66, v44, 0\n" \
"v_dot2_f32_bf16 v55, v67, v44, 0\n" \
"v_dot2_f32_bf16 v56, v68, v44, 0\n" \
"v_dot2_f32_bf16 v57, v69, v44, 0\n" \
"v_dot2_f32_bf16 v58, v70, v44, 0\n" \
"v_dot2_f32_bf16 v59, v71, v44, 0\n" \
"s_add_u32 m0, s70, 25600\n" \
"s_add_u32 s74, s69, 262272\n" \
"buffer_load_dwordx4 v246, s[24:27], s74 offen lds\n" \
"v_perm_b32 v64, v32, v28, s76\n" \
"v_perm_b32 v65, v32, v28, s77\n" \
"v_perm_b32 v66, v33, v29, s76\n" \
"v_perm_b32 v67, v33, v29, s77\n" \
"v_perm_b32 v68, v34, v30, s76\n" \
"v_perm_b32 v69, v34, v30, s77\n" \
"v_perm_b32 v70, v35, v31, s76\n" \
"v_perm_b32 v71, v35, v31, s77\n" \
"v_dot2_f32_bf16 v52, v64, v45, v52\n" \
"v_dot2_f32_bf16 v53, v65, v45, v53\n" \
"v_dot2_f32_bf16 v54, v66, v45, v54\n" \
"v_dot2_f32_bf16 v55, v67, v45, v55\n" \
"v_dot2_f32_bf16 v56, v68, v45, v56\n" \
"v_dot2_f32_bf16 v57, v69, v45, v57\n" \
"v_dot2_f32_bf16 v58, v70, v45, v58\n" \
"v_dot2_f32_bf16 v59, v71, v45, v59\n" \
"buffer_load_dwordx2 v[202:203], v14, s[32:35], 0 offen sc1\n" \
"s_nop 2\n" \
"v_permlane32_swap_b32 v52, v56\n" \
"v_permlane32_swap_b32 v53, v57\n" \
"v_permlane32_swap_b32 v54, v58\n" \
"v_permlane32_swap_b32 v55, v59\n" \
"v_add_f32 v52, v52, v56\n" \
"v_add_f32 v53, v53, v57\n" \
"v_add_f32 v54, v54, v58\n" \
"v_add_f32 v55, v55, v59\n" \
"s_nop 0\n" \
"v_permlane16_swap_b32 v52, v54\n" \
"v_permlane16_swap_b32 v53, v55\n" \
"v_add_f32 v52, v52, v54\n" \
"v_add_f32 v53, v53, v55\n" \
"s_lshl_b32 s74, %[wid], 8\n" \
"s_add_u32 s74, s74, 147456\n" \
"v_lshl_add_u32 v60, v7, 5, s74\n" \
"v_lshl_add_u32 v60, v8, 3, v60\n" \
"v_lshlrev_b32 v231, 2, v251\n" \
"v_add_u32 v62, 147456, v231\n" \
"v_add_f32_dpp v52, v52, v52 row_ror:8 row_mask:0xf bank_mask:0xf\n" \
"v_add_f32_dpp v53, v53, v53 row_ror:8 row_mask:0xf bank_mask:0xf\n" \
"ds_write_b64 v60, v[52:53]\n" \
"buffer_load_dwordx4 v[128:131], v248, s[44:47], s75 offen\n" \
"buffer_load_dwordx4 v[132:135], v248, s[44:47], s75 offen offset:1024\n" \
"s_waitcnt vmcnt(7) lgkmcnt(0)\n" \
"s_barrier\n" \
"ds_read2st64_b32 v[232:233], v62 offset1:1\n" \
"ds_read2st64_b32 v[234:235], v62 offset0:2 offset1:3\n" \
"ds_read_b64_tr_b16 a[192:193], v240 offset:0\n" \
"ds_read_b64_tr_b16 a[194:195], v240 offset:2048\n" \
"ds_read_b64_tr_b16 a[196:197], v240 offset:8192\n" \
"ds_read_b64_tr_b16 a[198:199], v240 offset:10240\n" \
"ds_read_b64_tr_b16 a[200:201], v241 offset:0\n" \
"ds_read_b64_tr_b16 a[202:203], v241 offset:2048\n" \
"ds_read_b64_tr_b16 a[204:205], v241 offset:8192\n" \
"ds_read_b64_tr_b16 a[206:207], v241 offset:10240\n" \
"ds_read_b64_tr_b16 a[208:209], v240 offset:512\n" \
"ds_read_b64_tr_b16 a[210:211], v240 offset:2560\n" \
"ds_read_b64_tr_b16 a[212:213], v240 offset:8704\n" \
"ds_read_b64_tr_b16 a[214:215], v240 offset:10752\n" \
"ds_read_b128 a[128:131], v242 offset:0\n" \
"ds_read_b128 a[132:135], v242 offset:2048\n" \
"ds_read_b128 a[136:139], v242 offset:4096\n" \
"ds_read_b128 a[140:143], v242 offset:6144\n" \
"ds_read_b128 a[144:147], v244 offset:0\n" \
"ds_read_b128 a[148:151], v244 offset:2048\n" \
"ds_read_b128 a[152:155], v244 offset:4096\n" \
"ds_read_b128 a[156:159], v244 offset:6144\n" \
"s_waitcnt lgkmcnt(0)\n" \
  \
"v_mfma_f32_16x16x32_bf16 v[0:3], a[192:195], a[128:131], 0\n" \
"s_add_u32 m0, s72, 8192\n" \
"ds_read_b64_tr_b16 a[216:217], v241 offset:512\n" \
"v_add_f32 v232, v232, v233\n" \
"v_mfma_f32_16x16x32_bf16 v[0:3], a[196:199], a[144:147], v[0:3]\n" \
"s_add_u32 s74, s71, 128\n" \
"buffer_load_dwordx4 v249, s[16:19], s74 offen lds\n" \
"v_mfma_f32_16x16x32_bf16 v[32:35], a[196:199], a[148:151], 0\n" \
"s_add_u32 m0, s72, 9216\n" \
"ds_read_b64_tr_b16 a[218:219], v241 offset:2560\n" \
"v_add_f32 v234, v234, v235\n" \
"v_mfma_f32_16x16x32_bf16 v[32:35], a[192:195], a[132:135], v[32:35]\n" \
"s_add_u32 s74, s71, 8320\n" \
"v_add_f32 v232, v232, v234\n" \
"v_mfma_f32_16x16x32_bf16 v[64:67], a[192:195], a[136:139], 0\n" \
"buffer_load_dwordx4 v250, s[16:19], s74 offen lds\n" \
"v_mfma_f32_16x16x32_bf16 v[64:67], a[196:199], a[152:155], v[64:67]\n" \
"s_add_u32 m0, s72, 10240\n" \
"buffer_store_dword v232, v231, s[32:35], s81 offen sc1\n" \
"v_mov_b32 v239, 1\n" \
"v_mfma_f32_16x16x32_bf16 v[96:99], a[196:199], a[156:159], 0\n" \
"s_add_u32 s74, s71, 16512\n" \
"buffer_store_dwordx2 v[224:225], v228, s[32:35], 0 offen sc1\n" \
"v_mfma_f32_16x16x32_bf16 v[96:99], a[192:195], a[140:143], v[96:99]\n" \
"ds_read_b64_tr_b16 a[220:221], v241 offset:8704\n" \
"ds_read_b64_tr_b16 a[222:223], v241 offset:10752\n" \
"v_mfma_f32_16x16x32_bf16 v[4:7], a[200:203], a[128:131], 0\n" \
"buffer_store_dword v230, v229, s[32:35], 0 offen sc1\n" \
"v_bfe_u32 v210, v251, 1, 3\n" \
"v_mfma_f32_16x16x32_bf16 v[4:7], a[204:207], a[144:147], v[4:7]\n" \
"ds_read_b64_tr_b16 a[192:193], v240 offset:1024\n" \
"v_lshlrev_b32 v210, 13, v210\n" \
"v_mfma_f32_16x16x32_bf16 v[36:39], a[204:207], a[148:151], 0\n" \
"ds_read_b64_tr_b16 a[194:195], v240 offset:3072\n" \
"v_and_b32 v208, 1, v251\n" \
"v_mfma_f32_16x16x32_bf16 v[36:39], a[200:203], a[132:135], v[36:39]\n" \
"buffer_load_dwordx4 v249, s[16:19], s74 offen lds\n" \
"v_mfma_f32_16x16x32_bf16 v[68:71], a[200:203], a[136:139], 0\n" \
"s_add_u32 m0, s72, 11264\n" \
"ds_read_b64_tr_b16 a[196:197], v240 offset:9216\n" \
"v_lshl_or_b32 v208, v208, 7, v210\n" \
"v_mfma_f32_16x16x32_bf16 v[68:71], a[204:207], a[152:155], v[68:71]\n" \
"s_add_u32 s74, s71, 24704\n" \
"v_and_b32 v209, 15, v251\n" \
"v_lshlrev_b32 v209, 7, v209\n" \
"v_mfma_f32_16x16x32_bf16 v[100:103], a[204:207], a[156:159], 0\n" \
"buffer_load_dwordx4 v250, s[16:19], s74 offen lds\n" \
"v_mfma_f32_16x16x32_bf16 v[100:103], a[200:203], a[140:143], v[100:103]\n" \
"s_add_u32 m0, s72, 12288\n" \
"ds_read_b64_tr_b16 a[198:199], v240 offset:11264\n" \
"v_and_b32 v211, 0x7fff, v208\n" \
"v_mfma_f32_16x16x32_bf16 v[8:11], a[208:211], a[128:131], 0\n" \
"s_add_u32 s74, s94, 32896\n" \
"ds_read_b64_tr_b16 a[200:201], v241 offset:1024\n" \
"v_mfma_f32_16x16x32_bf16 v[8:11], a[212:215], a[144:147], v[8:11]\n" \
"buffer_load_dwordx4 v249, s[16:19], s74 offen lds\n" \
"v_mfma_f32_16x16x32_bf16 v[40:43], a[212:215], a[148:151], 0\n" \
"s_add_u32 m0, s72, 13312\n" \
"s_add_u32 s74, s94, 41088\n" \
"ds_read_b64_tr_b16 a[202:203], v241 offset:3072\n" \
"v_mfma_f32_16x16x32_bf16 v[40:43], a[208:211], a[132:135], v[40:43]\n" \
"v_mfma_f32_16x16x32_bf16 v[72:75], a[208:211], a[136:139], 0\n" \
"buffer_load_dwordx4 v250, s[16:19], s74 offen lds\n" \
"v_mfma_f32_16x16x32_bf16 v[72:75], a[212:215], a[152:155], v[72:75]\n" \
"s_add_u32 m0, s72, 14336\n" \
"s_add_u32 s74, s94, 49280\n" \
"ds_read_b64_tr_b16 a[204:205], v241 offset:9216\n" \
"v_mfma_f32_16x16x32_bf16 v[104:107], a[212:215], a[156:159], 0\n" \
"ds_read_b64_tr_b16 a[206:207], v241 offset:11264\n" \
"v_mfma_f32_16x16x32_bf16 v[104:107], a[208:211], a[140:143], v[104:107]\n" \
"s_waitcnt lgkmcnt(8)\n" \
"buffer_load_dwordx4 v249, s[16:19], s74 offen lds\n" \
"v_mfma_f32_16x16x32_bf16 v[12:15], a[216:219], a[128:131], 0\n" \
"s_add_u32 m0, s72, 15360\n" \
"s_add_u32 s74, s94, 57472\n" \
"ds_read_b64_tr_b16 a[208:209], v240 offset:1536\n" \
"v_mfma_f32_16x16x32_bf16 v[12:15], a[220:223], a[144:147], v[12:15]\n" \
"v_mfma_f32_16x16x32_bf16 v[44:47], a[220:223], a[148:151], 0\n" \
"buffer_load_dwordx4 v250, s[16:19], s74 offen lds\n" \
"v_mfma_f32_16x16x32_bf16 v[44:47], a[216:219], a[132:135], v[44:47]\n" \
"s_add_u32 m0, s70, 32768\n" \
"s_add_u32 s74, s69, 524288\n" \
"ds_read_b64_tr_b16 a[210:211], v240 offset:3584\n" \
"v_mfma_f32_16x16x32_bf16 v[76:79], a[216:219], a[136:139], 0\n" \
"ds_read_b64_tr_b16 a[212:213], v240 offset:9728\n" \
"v_mfma_f32_16x16x32_bf16 v[76:79], a[220:223], a[152:155], v[76:79]\n" \
"buffer_load_dwordx4 v246, s[20:23], s74 offen lds\n" \
"v_mfma_f32_16x16x32_bf16 v[108:111], a[220:223], a[156:159], 0\n" \
"s_add_u32 m0, s70, 33792\n" \
"s_add_u32 s74, s69, 524416\n" \
"ds_read_b64_tr_b16 a[214:215], v240 offset:11776\n" \
"v_mfma_f32_16x16x32_bf16 v[108:111], a[216:219], a[140:143], v[108:111]\n" \
"s_waitcnt lgkmcnt(8)\n" \
"v_mfma_f32_16x16x32_bf16 v[16:19], a[192:195], a[128:131], 0\n" \
"buffer_load_dwordx4 v246, s[20:23], s74 offen lds\n" \
"v_mfma_f32_16x16x32_bf16 v[16:19], a[196:199], a[144:147], v[16:19]\n" \
"s_add_u32 m0, s70, 40960\n" \
"s_add_u32 s74, s69, 786432\n" \
"ds_read_b64_tr_b16 a[216:217], v241 offset:1536\n" \
"v_mfma_f32_16x16x32_bf16 v[48:51], a[196:199], a[148:151], 0\n" \
"ds_read_b64_tr_b16 a[218:219], v241 offset:3584\n" \
"v_mfma_f32_16x16x32_bf16 v[48:51], a[192:195], a[132:135], v[48:51]\n" \
"buffer_load_dwordx4 v246, s[20:23], s74 offen lds\n" \
"v_mfma_f32_16x16x32_bf16 v[80:83], a[192:195], a[136:139], 0\n" \
"s_add_u32 m0, s70, 41984\n" \
"ds_read_b64_tr_b16 a[220:221], v241 offset:9728\n" \
"ds_read_b64_tr_b16 a[222:223], v241 offset:11776\n" \
"v_mfma_f32_16x16x32_bf16 v[80:83], a[196:199], a[152:155], v[80:83]\n" \
"s_add_u32 s74, s69, 786560\n" \
"v_mfma_f32_16x16x32_bf16 v[112:115], a[196:199], a[156:159], 0\n" \
"buffer_load_dwordx4 v246, s[20:23], s74 offen lds\n" \
"v_mfma_f32_16x16x32_bf16 v[112:115], a[192:195], a[140:143], v[112:115]\n" \
"s_waitcnt lgkmcnt(8)\n" \
"s_add_u32 m0, s70, 49152\n" \
"s_add_u32 s74, s69, 524288\n" \
"v_mfma_f32_16x16x32_bf16 v[20:23], a[200:203], a[128:131], 0\n" \
"s_mov_b32 s77, 0x7f4a7c15\n" \
"v_mov_b32 v253, 0x9e3779b9\n" \
"v_mfma_f32_16x16x32_bf16 v[20:23], a[204:207], a[144:147], v[20:23]\n" \
"buffer_load_dwordx4 v246, s[24:27], s74 offen lds\n" \
"v_mfma_f32_16x16x32_bf16 v[52:55], a[204:207], a[148:151], 0\n" \
"s_waitcnt vmcnt(18)\n" \
"s_add_u32 m0, s70, 50176\n" \
"v_add_co_u32 v202, vcc, s77, v202\n" \
"v_mfma_f32_16x16x32_bf16 v[52:55], a[200:203], a[132:135], v[52:55]\n" \
"s_waitcnt vmcnt(16)\n" \
"s_add_u32 s74, s69, 524416\n" \
"v_lshlrev_b32 v136, 16, v128\n" \
"v_mfma_f32_16x16x32_bf16 v[84:87], a[200:203], a[136:139], 0\n" \
"s_waitcnt vmcnt(11) lgkmcnt(0)\n" \
"s_barrier\n" \
"v_addc_co_u32 v203, vcc, v253, v203, vcc\n" \
"v_mfma_f32_16x16x32_bf16 v[84:87], a[204:207], a[152:155], v[84:87]\n" \
"s_lshl_b32 s85, %[bid], 3\n" \
"buffer_load_dwordx4 v246, s[24:27], s74 offen lds\n" \
"v_mfma_f32_16x16x32_bf16 v[116:119], a[204:207], a[156:159], 0\n" \
"s_add_u32 m0, s70, 57344\n" \
"ds_read_b64_tr_b16 a[192:193], v240 offset:16384\n" \
"ds_read_b64_tr_b16 a[194:195], v240 offset:18432\n" \
"v_mfma_f32_16x16x32_bf16 v[116:119], a[200:203], a[140:143], v[116:119]\n" \
"s_add_u32 s74, s69, 786432\n" \
"ds_read_b64_tr_b16 a[196:197], v240 offset:24576\n" \
"ds_read_b64_tr_b16 a[198:199], v240 offset:26624\n" \
"v_mfma_f32_16x16x32_bf16 v[24:27], a[208:211], a[128:131], 0\n" \
"s_add_u32 s85, s85, 573440\n" \
"buffer_load_dwordx4 v246, s[24:27], s74 offen lds\n" \
"v_mfma_f32_16x16x32_bf16 v[24:27], a[212:215], a[144:147], v[24:27]\n" \
"s_add_u32 m0, s70, 58368\n" \
"s_cmp_eq_u32 %[wid], 0\n" \
"s_cselect_b32 s85, s85, 1073741824\n" \
"v_mfma_f32_16x16x32_bf16 v[56:59], a[212:215], a[148:151], 0\n" \
"s_add_u32 s74, s69, 786560\n" \
"ds_read_b64_tr_b16 a[200:201], v241 offset:16384\n" \
"ds_read_b64_tr_b16 a[202:203], v241 offset:18432\n" \
"v_mfma_f32_16x16x32_bf16 v[56:59], a[208:211], a[132:135], v[56:59]\n" \
"buffer_load_dwordx4 v246, s[24:27], s74 offen lds\n" \
"v_mfma_f32_16x16x32_bf16 v[88:91], a[208:211], a[136:139], 0\n" \
"s_add_u32 m0, s72, 0\n" \
"ds_read_b64_tr_b16 a[204:205], v241 offset:24576\n" \
"v_and_b32 v252, 7, v251\n" \
"v_mfma_f32_16x16x32_bf16 v[88:91], a[212:215], a[152:155], v[88:91]\n" \
"s_add_u32 s74, s71, 256\n" \
"v_lshlrev_b32 v252, 11, v252\n" \
"v_and_b32 v137, 0xffff0000, v128\n" \
"v_mfma_f32_16x16x32_bf16 v[120:123], a[212:215], a[156:159], 0\n" \
"buffer_load_dwordx4 v249, s[16:19], s74 offen lds\n" \
"v_mfma_f32_16x16x32_bf16 v[120:123], a[208:211], a[140:143], v[120:123]\n" \
"s_add_u32 m0, s72, 1024\n" \
"buffer_store_dwordx2 v[202:203], v252, s[32:35], s85 offen sc1\n" \
"v_lshrrev_b32 v253, 1, v248\n" \
"v_mfma_f32_16x16x32_bf16 v[28:31], a[216:219], a[128:131], 0\n" \
"s_add_u32 s74, s71, 8448\n" \
"ds_read_b64_tr_b16 a[206:207], v241 offset:26624\n" \
"ds_read_b64_tr_b16 a[208:209], v240 offset:16896\n" \
"v_mfma_f32_16x16x32_bf16 v[28:31], a[220:223], a[144:147], v[28:31]\n" \
"v_lshlrev_b32 v138, 16, v129\n" \
"v_and_b32 v139, 0xffff0000, v129\n" \
"v_mfma_f32_16x16x32_bf16 v[60:63], a[220:223], a[148:151], 0\n" \
"buffer_load_dwordx4 v250, s[16:19], s74 offen lds\n" \
"v_mfma_f32_16x16x32_bf16 v[60:63], a[216:219], a[132:135], v[60:63]\n" \
"s_add_u32 m0, s72, 2048\n" \
"ds_read_b64_tr_b16 a[210:211], v240 offset:18944\n" \
"v_lshlrev_b32 v140, 16, v130\n" \
"v_mfma_f32_16x16x32_bf16 v[92:95], a[216:219], a[136:139], 0\n" \
"s_add_u32 s74, s71, 16640\n" \
"ds_read_b64_tr_b16 a[212:213], v240 offset:25088\n" \
"v_and_b32 v141, 0xffff0000, v130\n" \
"v_mfma_f32_16x16x32_bf16 v[92:95], a[220:223], a[152:155], v[92:95]\n" \
"buffer_load_dwordx4 v249, s[16:19], s74 offen lds\n" \
"v_mfma_f32_16x16x32_bf16 v[124:127], a[220:223], a[156:159], 0\n" \
"s_add_u32 m0, s72, 3072\n" \
"ds_read_b64_tr_b16 a[214:215], v240 offset:27136\n" \
"v_lshlrev_b32 v142, 16, v131\n" \
"v_mfma_f32_16x16x32_bf16 v[124:127], a[216:219], a[140:143], v[124:127]\n" \
"s_waitcnt lgkmcnt(8)\n" \
"v_and_b32 v143, 0xffff0000, v131\n" \
"v_lshlrev_b32 v144, 16, v132\n" \
"v_mfma_f32_16x16x32_bf16 a[0:3], a[128:131], a[192:195], 0\n" \
"s_add_u32 s74, s71, 24832\n" \
"buffer_load_dwordx4 v250, s[16:19], s74 offen lds\n" \
"v_mfma_f32_16x16x32_bf16 a[0:3], a[144:147], a[196:199], a[0:3]\n" \
"s_add_u32 m0, s72, 4096\n" \
"ds_read_b64_tr_b16 a[216:217], v241 offset:16896\n" \
"v_and_b32 v145, 0xffff0000, v132\n" \
"v_mfma_f32_16x16x32_bf16 a[4:7], a[148:151], a[196:199], 0\n" \
"s_add_u32 s74, s94, 33024\n" \
"ds_read_b64_tr_b16 a[218:219], v241 offset:18944\n" \
"v_lshlrev_b32 v146, 16, v133\n" \
"v_mfma_f32_16x16x32_bf16 a[4:7], a[132:135], a[192:195], a[4:7]\n" \
"buffer_load_dwordx4 v249, s[16:19], s74 offen lds\n" \
"v_mfma_f32_16x16x32_bf16 a[64:67], a[136:139], a[192:195], 0\n" \
"s_add_u32 m0, s70, 0\n" \
"ds_read_b64_tr_b16 a[220:221], v241 offset:25088\n" \
"v_and_b32 v147, 0xffff0000, v133\n" \
"v_mfma_f32_16x16x32_bf16 a[64:67], a[152:155], a[196:199], a[64:67]\n" \
"s_add_u32 s74, s69, 1048576\n" \
"v_lshlrev_b32 v148, 16, v134\n" \
"v_and_b32 v149, 0xffff0000, v134\n" \
"v_mfma_f32_16x16x32_bf16 a[68:71], a[156:159], a[196:199], 0\n" \
"buffer_load_dwordx4 v246, s[20:23], s74 offen lds\n" \
"v_mfma_f32_16x16x32_bf16 a[68:71], a[140:143], a[192:195], a[68:71]\n" \
"s_waitcnt lgkmcnt(7)\n" \
"ds_read_b64_tr_b16 a[222:223], v241 offset:27136\n" \
"v_lshlrev_b32 v150, 16, v135\n" \
"v_mfma_f32_16x16x32_bf16 a[8:11], a[128:131], a[200:203], 0\n" \
"s_add_u32 m0, s72, 5120\n" \
"ds_read_b64_tr_b16 a[192:193], v240 offset:17408\n" \
"v_and_b32 v151, 0xffff0000, v135\n" \
"v_mfma_f32_16x16x32_bf16 a[8:11], a[144:147], a[204:207], a[8:11]\n" \
"s_add_u32 s74, s94, 41216\n" \
"buffer_load_dwordx4 v250, s[16:19], s74 offen lds\n" \
"v_mfma_f32_16x16x32_bf16 a[12:15], a[148:151], a[204:207], 0\n" \
"s_add_u32 m0, s72, 6144\n" \
"ds_read_b64_tr_b16 a[194:195], v240 offset:19456\n" \
"v_add_f32 v152, v136, v137\n" \
"v_mfma_f32_16x16x32_bf16 a[12:15], a[132:135], a[200:203], a[12:15]\n" \
"s_add_u32 s74, s94, 49408\n" \
"v_add_f32 v154, v138, v139\n" \
"v_add_f32 v153, v144, v145\n" \
"v_mfma_f32_16x16x32_bf16 a[72:75], a[136:139], a[200:203], 0\n" \
"buffer_load_dwordx4 v249, s[16:19], s74 offen lds\n" \
"v_mfma_f32_16x16x32_bf16 a[72:75], a[152:155], a[204:207], a[72:75]\n" \
"s_add_u32 m0, s70, 1024\n" \
"ds_read_b64_tr_b16 a[196:197], v240 offset:25600\n" \
"v_add_f32 v155, v146, v147\n" \
"v_mfma_f32_16x16x32_bf16 a[76:79], a[156:159], a[204:207], 0\n" \
"s_add_u32 s74, s69, 1048704\n" \
"ds_read_b64_tr_b16 a[198:199], v240 offset:27648\n" \
"v_add_f32 v152, v152, v140\n" \
"v_mfma_f32_16x16x32_bf16 a[76:79], a[140:143], a[200:203], a[76:79]\n" \
"s_waitcnt lgkmcnt(8)\n" \
"buffer_load_dwordx4 v246, s[20:23], s74 offen lds\n" \
"v_mfma_f32_16x16x32_bf16 a[16:19], a[128:131], a[208:211], 0\n" \
"s_add_u32 m0, s72, 7168\n" \
"ds_read_b64_tr_b16 a[200:201], v241 offset:17408\n" \
"v_add_f32 v154, v154, v141\n" \
"v_mfma_f32_16x16x32_bf16 a[16:19], a[144:147], a[212:215], a[16:19]\n" \
"s_add_u32 s74, s94, 57600\n" \
"v_add_f32 v153, v153, v148\n" \
"v_add_f32 v155, v155, v149\n" \
"v_mfma_f32_16x16x32_bf16 a[20:23], a[148:151], a[212:215], 0\n" \
"buffer_load_dwordx4 v250, s[16:19], s74 offen lds\n" \
"v_mfma_f32_16x16x32_bf16 a[20:23], a[132:135], a[208:211], a[20:23]\n" \
"s_add_u32 m0, s70, 8192\n" \
"ds_read_b64_tr_b16 a[202:203], v241 offset:19456\n" \
"v_add_f32 v152, v152, v142\n" \
"v_mfma_f32_16x16x32_bf16 a[80:83], a[136:139], a[208:211], 0\n" \
"s_add_u32 s74, s69, 1310720\n" \
"ds_read_b64_tr_b16 a[204:205], v241 offset:25600\n" \
"v_add_f32 v154, v154, v143\n" \
"v_mfma_f32_16x16x32_bf16 a[80:83], a[152:155], a[212:215], a[80:83]\n" \
"buffer_load_dwordx4 v246, s[20:23], s74 offen lds\n" \
"v_mfma_f32_16x16x32_bf16 a[84:87], a[156:159], a[212:215], 0\n" \
"s_add_u32 m0, s70, 9216\n" \
"ds_read_b64_tr_b16 a[206:207], v241 offset:27648\n" \
"v_add_f32 v153, v153, v150\n" \
"v_mfma_f32_16x16x32_bf16 a[84:87], a[140:143], a[208:211], a[84:87]\n" \
"s_waitcnt lgkmcnt(8)\n" \
"v_add_f32 v155, v155, v151\n" \
"v_add_f32 v152, v152, v154\n" \
"v_mfma_f32_16x16x32_bf16 a[24:27], a[128:131], a[216:219], 0\n" \
"s_add_u32 s74, s69, 1310848\n" \
"buffer_load_dwordx4 v246, s[20:23], s74 offen lds\n" \
"v_mfma_f32_16x16x32_bf16 a[24:27], a[144:147], a[220:223], a[24:27]\n" \
"ds_read_b64_tr_b16 a[208:209], v240 offset:17920\n" \
"v_add_f32 v153, v153, v155\n" \
"v_mfma_f32_16x16x32_bf16 a[28:31], a[148:151], a[220:223], 0\n" \
"ds_read_b64_tr_b16 a[210:211], v240 offset:19968\n" \
"v_add_f32_dpp v152, v152, v152 quad_perm:[1,0,3,2] row_mask:0xf bank_mask:0xf\n" \
"v_mfma_f32_16x16x32_bf16 a[28:31], a[132:135], a[216:219], a[28:31]\n" \
"v_add_f32_dpp v153, v153, v153 quad_perm:[1,0,3,2] row_mask:0xf bank_mask:0xf\n" \
"v_mfma_f32_16x16x32_bf16 a[88:91], a[136:139], a[216:219], 0\n" \
"ds_read_b64_tr_b16 a[212:213], v240 offset:26112\n" \
"v_add_f32_dpp v152, v152, v152 quad_perm:[2,3,0,1] row_mask:0xf bank_mask:0xf\n" \
"v_mfma_f32_16x16x32_bf16 a[88:91], a[152:155], a[220:223], a[88:91]\n" \
".Lst0_0_%=:\n" \
"s_cbranch_execz .Lst0_4_%=\n" \
"v_add_f32_dpp v153, v153, v153 quad_perm:[2,3,0,1] row_mask:0xf bank_mask:0xf\n" \
"v_mfma_f32_16x16x32_bf16 a[92:95], a[156:159], a[220:223], 0\n" \
"ds_read_b64_tr_b16 a[214:215], v240 offset:28160\n" \
"v_add_f32_dpp v152, v152, v152 row_half_mirror row_mask:0xf bank_mask:0xf\n" \
"v_mfma_f32_16x16x32_bf16 a[92:95], a[140:143], a[216:219], a[92:95]\n" \
"s_waitcnt lgkmcnt(8)\n" \
"v_add_f32_dpp v153, v153, v153 row_half_mirror row_mask:0xf bank_mask:0xf\n" \
"v_mfma_f32_16x16x32_bf16 a[32:35], a[128:131], a[192:195], 0\n" \
"s_waitcnt vmcnt(21)\n" \
"ds_read_b64_tr_b16 a[216:217], v241 offset:17920\n" \
"ds_read_b128 a[160:163], v242 offset:8192\n" \
"v_mfma_f32_16x16x32_bf16 a[32:35], a[144:147], a[196:199], a[32:35]\n" \
"v_add_f32_dpp v152, v152, v152 row_mirror row_mask:0xf bank_mask:0xf\n" \
"v_add_f32_dpp v153, v153, v153 row_mirror row_mask:0xf bank_mask:0xf\n" \
"v_mfma_f32_16x16x32_bf16 a[36:39], a[148:151], a[196:199], 0\n" \
"ds_read_b64_tr_b16 a[218:219], v241 offset:19968\n" \
"ds_read_b128 a[164:167], v242 offset:10240\n" \
"v_mfma_f32_16x16x32_bf16 a[36:39], a[132:135], a[192:195], a[36:39]\n" \
"ds_read_b64_tr_b16 a[220:221], v241 offset:26112\n" \
"v_mov_b32 v154, v152\n" \
"v_mfma_f32_16x16x32_bf16 a[96:99], a[136:139], a[192:195], 0\n" \
"ds_read_b64_tr_b16 a[222:223], v241 offset:28160\n" \
"ds_read_b128 a[168:171], v242 offset:12288\n" \
"v_mfma_f32_16x16x32_bf16 a[96:99], a[152:155], a[196:199], a[96:99]\n" \
"v_mov_b32 v155, v153\n" \
"v_mfma_f32_16x16x32_bf16 a[100:103], a[156:159], a[196:199], 0\n" \
"ds_read_b128 a[172:175], v242 offset:14336\n" \
"v_mfma_f32_16x16x32_bf16 a[100:103], a[140:143], a[192:195], a[100:103]\n" \
"s_waitcnt lgkmcnt(12)\n" \
"v_permlane16_swap_b32 v152, v154\n" \
"v_mfma_f32_16x16x32_bf16 a[40:43], a[128:131], a[200:203], 0\n" \
"v_permlane16_swap_b32 v153, v155\n" \
"v_mfma_f32_16x16x32_bf16 a[40:43], a[144:147], a[204:207], a[40:43]\n" \
"v_add_f32 v152, v152, v154\n" \
"v_add_f32 v153, v153, v155\n" \
"v_mfma_f32_16x16x32_bf16 a[44:47], a[148:151], a[204:207], 0\n" \
"v_mov_b32 v154, v152\n" \
"v_mov_b32 v155, v153\n" \
"v_mfma_f32_16x16x32_bf16 a[44:47], a[132:135], a[200:203], a[44:47]\n" \
"v_mfma_f32_16x16x32_bf16 a[104:107], a[136:139], a[200:203], 0\n" \
"s_waitcnt vmcnt(17) lgkmcnt(2)\n" \
"s_barrier\n" \
"ds_read_b128 a[176:179], v244 offset:8192\n" \
"v_mfma_f32_16x16x32_bf16 a[104:107], a[152:155], a[204:207], a[104:107]\n" \
"s_add_u32 m0, s70, 16384\n" \
"ds_read_b64_tr_b16 a[192:193], v240 offset:32768\n" \
"ds_read_b64_tr_b16 a[194:195], v240 offset:34816\n" \
"v_mfma_f32_16x16x32_bf16 a[108:111], a[156:159], a[204:207], 0\n" \
"s_add_u32 s74, s69, 1048576\n" \
"buffer_load_dwordx4 v246, s[24:27], s74 offen lds\n" \
"v_mfma_f32_16x16x32_bf16 a[108:111], a[140:143], a[200:203], a[108:111]\n" \
"ds_read_b64_tr_b16 a[196:197], v240 offset:40960\n" \
"ds_read_b64_tr_b16 a[198:199], v240 offset:43008\n" \
"v_mfma_f32_16x16x32_bf16 a[48:51], a[128:131], a[208:211], 0\n" \
"ds_read_b64_tr_b16 a[200:201], v241 offset:32768\n" \
"ds_read_b128 a[180:183], v244 offset:10240\n" \
"v_mfma_f32_16x16x32_bf16 a[48:51], a[144:147], a[212:215], a[48:51]\n" \
"ds_read_b128 a[184:187], v244 offset:12288\n" \
"v_mfma_f32_16x16x32_bf16 a[52:55], a[148:151], a[212:215], 0\n" \
"ds_read_b64_tr_b16 a[202:203], v241 offset:34816\n" \
"ds_read_b128 a[188:191], v244 offset:14336\n" \
"v_mfma_f32_16x16x32_bf16 a[52:55], a[132:135], a[208:211], a[52:55]\n" \
"v_permlane32_swap_b32 v152, v154\n" \
"v_mfma_f32_16x16x32_bf16 a[112:115], a[136:139], a[208:211], 0\n" \
"ds_read_b64_tr_b16 a[204:205], v241 offset:40960\n" \
"v_mfma_f32_16x16x32_bf16 a[112:115], a[152:155], a[212:215], a[112:115]\n" \
"v_permlane32_swap_b32 v153, v155\n" \
"v_mfma_f32_16x16x32_bf16 a[116:119], a[156:159], a[212:215], 0\n" \
"ds_read_b64_tr_b16 a[206:207], v241 offset:43008\n" \
"v_add_f32 v152, v152, v154\n" \
"v_mfma_f32_16x16x32_bf16 a[116:119], a[140:143], a[208:211], a[116:119]\n" \
"v_add_f32 v153, v153, v155\n" \
"v_mul_f32 v152, 0x3b000000, v152\n" \
"v_mfma_f32_16x16x32_bf16 a[56:59], a[128:131], a[216:219], 0\n" \
"ds_read_b64_tr_b16 a[208:209], v240 offset:33280\n" \
"v_mul_f32 v153, 0x3b000000, v153\n" \
"v_mfma_f32_16x16x32_bf16 a[56:59], a[144:147], a[220:223], a[56:59]\n" \
"v_sub_f32 v254, v136, v152\n" \
"v_sub_f32 v255, v144, v153\n" \
"v_mfma_f32_16x16x32_bf16 a[60:63], a[148:151], a[220:223], 0\n" \
"s_add_u32 m0, s70, 17408\n" \
"ds_read_b64_tr_b16 a[210:211], v240 offset:35328\n" \
"v_mul_f32 v156, v254, v254\n" \
"v_mfma_f32_16x16x32_bf16 a[60:63], a[132:135], a[216:219], a[60:63]\n" \
"s_add_u32 s74, s69, 1048704\n" \
"v_mul_f32 v157, v255, v255\n" \
"v_sub_f32 v254, v137, v152\n" \
"v_mfma_f32_16x16x32_bf16 a[120:123], a[136:139], a[216:219], 0\n" \
"buffer_load_dwordx4 v246, s[24:27], s74 offen lds\n" \
"v_mfma_f32_16x16x32_bf16 a[120:123], a[152:155], a[220:223], a[120:123]\n" \
"ds_read_b64_tr_b16 a[212:213], v240 offset:41472\n" \
"v_sub_f32 v255, v145, v153\n" \
"v_mfma_f32_16x16x32_bf16 a[124:127], a[156:159], a[220:223], 0\n" \
"s_waitcnt lgkmcnt(5)\n" \
"ds_read_b64_tr_b16 a[214:215], v240 offset:43520\n" \
"v_mfma_f32_16x16x32_bf16 a[124:127], a[140:143], a[216:219], a[124:127]\n" \
"s_add_u32 m0, s72, 8192\n" \
"v_mul_f32 v158, v254, v254\n" \
"v_mul_f32 v159, v255, v255\n" \
"v_mfma_f32_16x16x32_bf16 v[0:3], a[192:195], a[160:163], v[0:3]\n" \
"s_add_u32 s74, s71, 384\n" \
"buffer_load_dwordx4 v249, s[16:19], s74 offen lds\n" \
"v_mfma_f32_16x16x32_bf16 v[0:3], a[196:199], a[176:179], v[0:3]\n" \
"ds_read_b64_tr_b16 a[216:217], v241 offset:33280\n" \
"v_sub_f32 v254, v138, v152\n" \
"v_mfma_f32_16x16x32_bf16 v[32:35], a[196:199], a[180:183], v[32:35]\n" \
"ds_read_b64_tr_b16 a[218:219], v241 offset:35328\n" \
"v_sub_f32 v255, v146, v153\n" \
"v_mfma_f32_16x16x32_bf16 v[32:35], a[192:195], a[164:167], v[32:35]\n" \
"v_fmac_f32 v156, v254, v254\n" \
"v_fmac_f32 v157, v255, v255\n" \
"v_mfma_f32_16x16x32_bf16 v[64:67], a[192:195], a[168:171], v[64:67]\n" \
"s_add_u32 m0, s72, 9216\n" \
"ds_read_b64_tr_b16 a[220:221], v241 offset:41472\n" \
"v_sub_f32 v254, v139, v152\n" \
"v_mfma_f32_16x16x32_bf16 v[64:67], a[196:199], a[184:187], v[64:67]\n" \
"s_add_u32 s74, s71, 8576\n" \
"v_sub_f32 v255, v147, v153\n" \
"v_fmac_f32 v158, v254, v254\n" \
"v_mfma_f32_16x16x32_bf16 v[96:99], a[196:199], a[188:191], v[96:99]\n" \
"buffer_load_dwordx4 v250, s[16:19], s74 offen lds\n" \
"v_mfma_f32_16x16x32_bf16 v[96:99], a[192:195], a[172:175], v[96:99]\n" \
"s_waitcnt lgkmcnt(7)\n" \
"ds_read_b64_tr_b16 a[222:223], v241 offset:43520\n" \
"v_fmac_f32 v159, v255, v255\n" \
"v_mfma_f32_16x16x32_bf16 v[4:7], a[200:203], a[160:163], v[4:7]\n" \
"s_add_u32 m0, s70, 24576\n" \
"ds_read_b64_tr_b16 a[192:193], v240 offset:33792\n" \
"v_sub_f32 v254, v140, v152\n" \
"v_mfma_f32_16x16x32_bf16 v[4:7], a[204:207], a[176:179], v[4:7]\n" \
"s_add_u32 s74, s69, 1310720\n" \
"v_sub_f32 v255, v148, v153\n" \
"v_fmac_f32 v156, v254, v254\n" \
"v_mfma_f32_16x16x32_bf16 v[36:39], a[204:207], a[180:183], v[36:39]\n" \
"buffer_load_dwordx4 v246, s[24:27], s74 offen lds\n" \
"v_mfma_f32_16x16x32_bf16 v[36:39], a[200:203], a[164:167], v[36:39]\n" \
"s_add_u32 m0, s72, 10240\n" \
"ds_read_b64_tr_b16 a[194:195], v240 offset:35840\n" \
"v_fmac_f32 v157, v255, v255\n" \
"v_mfma_f32_16x16x32_bf16 v[68:71], a[200:203], a[168:171], v[68:71]\n" \
"s_add_u32 s74, s71, 16768\n" \
"ds_read_b64_tr_b16 a[196:197], v240 offset:41984\n" \
"v_sub_f32 v254, v141, v152\n" \
"v_mfma_f32_16x16x32_bf16 v[68:71], a[204:207], a[184:187], v[68:71]\n" \
"buffer_load_dwordx4 v249, s[16:19], s74 offen lds\n" \
"v_mfma_f32_16x16x32_bf16 v[100:103], a[204:207], a[188:191], v[100:103]\n" \
"ds_read_b64_tr_b16 a[198:199], v240 offset:44032\n" \
"v_sub_f32 v255, v149, v153\n" \
"v_mfma_f32_16x16x32_bf16 v[100:103], a[200:203], a[172:175], v[100:103]\n" \
"s_waitcnt lgkmcnt(8)\n" \
"v_fmac_f32 v158, v254, v254\n" \
"v_fmac_f32 v159, v255, v255\n" \
"v_mfma_f32_16x16x32_bf16 v[8:11], a[208:211], a[160:163], v[8:11]\n" \
"s_add_u32 m0, s72, 11264\n" \
"ds_read_b64_tr_b16 a[200:201], v241 offset:33792\n" \
"v_sub_f32 v254, v142, v152\n" \
"v_mfma_f32_16x16x32_bf16 v[8:11], a[212:215], a[176:179], v[8:11]\n" \
"s_add_u32 s74, s71, 24960\n" \
"v_sub_f32 v255, v150, v153\n" \
"v_fmac_f32 v156, v254, v254\n" \
"v_mfma_f32_16x16x32_bf16 v[40:43], a[212:215], a[180:183], v[40:43]\n" \
"s_lshl2_add_u32 s85, s67, %[wid]\n" \
"buffer_load_dwordx4 v250, s[16:19], s74 offen lds\n" \
"v_mfma_f32_16x16x32_bf16 v[40:43], a[208:211], a[164:167], v[40:43]\n" \
"s_lshl_b32 s85, s85, 16\n" \
"ds_read_b64_tr_b16 a[202:203], v241 offset:35840\n" \
"v_fmac_f32 v157, v255, v255\n" \
"v_mfma_f32_16x16x32_bf16 v[72:75], a[208:211], a[168:171], v[72:75]\n" \
"s_lshl_b32 s86, s68, 8\n" \
"ds_read_b64_tr_b16 a[204:205], v241 offset:41984\n" \
"v_sub_f32 v254, v143, v152\n" \
"v_mfma_f32_16x16x32_bf16 v[72:75], a[212:215], a[184:187], v[72:75]\n" \
"s_add_u32 s85, s85, s86\n" \
"v_sub_f32 v255, v151, v153\n" \
"v_fmac_f32 v158, v254, v254\n" \
"v_mfma_f32_16x16x32_bf16 v[104:107], a[212:215], a[188:191], v[104:107]\n" \
"s_add_u32 m0, s70, 25600\n" \
"ds_read_b64_tr_b16 a[206:207], v241 offset:44032\n" \
"v_fmac_f32 v159, v255, v255\n" \
"v_mfma_f32_16x16x32_bf16 v[104:107], a[208:211], a[172:175], v[104:107]\n" \
"s_waitcnt lgkmcnt(8)\n" \
"v_add_f32 v156, v156, v158\n" \
"v_add_f32 v157, v157, v159\n" \
"v_mfma_f32_16x16x32_bf16 v[12:15], a[216:219], a[160:163], v[12:15]\n" \
"s_add_u32 s74, s69, 1310848\n" \
"buffer_load_dwordx4 v246, s[24:27], s74 offen lds\n" \
"v_mfma_f32_16x16x32_bf16 v[12:15], a[220:223], a[176:179], v[12:15]\n" \
"s_add_u32 m0, s72, 12288\n" \
"ds_read_b64_tr_b16 a[208:209], v240 offset:34304\n" \
"v_add_f32_dpp v156, v156, v156 quad_perm:[1,0,3,2] row_mask:0xf bank_mask:0xf\n" \
"v_mfma_f32_16x16x32_bf16 v[44:47], a[220:223], a[180:183], v[44:47]\n" \
"s_add_u32 s74, s94, 33152\n" \
"ds_read_b64_tr_b16 a[210:211], v240 offset:36352\n" \
"v_add_f32_dpp v157, v157, v157 quad_perm:[1,0,3,2] row_mask:0xf bank_mask:0xf\n" \
"v_mfma_f32_16x16x32_bf16 v[44:47], a[216:219], a[164:167], v[44:47]\n" \
"s_add_u32 s85, s85, 2097152\n" \
"buffer_load_dwordx4 v249, s[16:19], s74 offen lds\n" \
"v_mfma_f32_16x16x32_bf16 v[76:79], a[216:219], a[168:171], v[76:79]\n" \
"s_add_u32 m0, s72, 13312\n" \
"buffer_load_dword v245, v208, s[20:23], s85 offen\n" \
"v_add_f32_dpp v156, v156, v156 quad_perm:[2,3,0,1] row_mask:0xf bank_mask:0xf\n" \
"v_mfma_f32_16x16x32_bf16 v[76:79], a[220:223], a[184:187], v[76:79]\n" \
"s_add_u32 s74, s94, 41344\n" \
"ds_read_b64_tr_b16 a[212:213], v240 offset:42496\n" \
"v_add_f32_dpp v157, v157, v157 quad_perm:[2,3,0,1] row_mask:0xf bank_mask:0xf\n" \
"v_mfma_f32_16x16x32_bf16 v[108:111], a[220:223], a[188:191], v[108:111]\n" \
"ds_read_b64_tr_b16 a[214:215], v240 offset:44544\n" \
"v_add_f32_dpp v156, v156, v156 row_half_mirror row_mask:0xf bank_mask:0xf\n" \
"v_mfma_f32_16x16x32_bf16 v[108:111], a[216:219], a[172:175], v[108:111]\n" \
"s_waitcnt lgkmcnt(8)\n" \
"buffer_load_dwordx4 v250, s[16:19], s74 offen lds\n" \
"v_mfma_f32_16x16x32_bf16 v[16:19], a[192:195], a[160:163], v[16:19]\n" \
"ds_read_b64_tr_b16 a[216:217], v241 offset:34304\n" \
"v_add_f32_dpp v157, v157, v157 row_half_mirror row_mask:0xf bank_mask:0xf\n" \
"v_mfma_f32_16x16x32_bf16 v[16:19], a[196:199], a[176:179], v[16:19]\n" \
"v_add_f32_dpp v156, v156, v156 row_mirror row_mask:0xf bank_mask:0xf\n" \
"v_mfma_f32_16x16x32_bf16 v[48:51], a[196:199], a[180:183], v[48:51]\n" \
"s_add_u32 m0, s72, 14336\n" \
"ds_read_b64_tr_b16 a[218:219], v241 offset:36352\n" \
"v_add_f32_dpp v157, v157, v157 row_mirror row_mask:0xf bank_mask:0xf\n" \
"v_mfma_f32_16x16x32_bf16 v[48:51], a[192:195], a[164:167], v[48:51]\n" \
"s_add_u32 s74, s94, 49536\n" \
"ds_read_b64_tr_b16 a[220:221], v241 offset:42496\n" \
"v_mov_b32 v158, v156\n" \
"v_mfma_f32_16x16x32_bf16 v[80:83], a[192:195], a[168:171], v[80:83]\n" \
"buffer_load_dwordx4 v249, s[16:19], s74 offen lds\n" \
"v_mfma_f32_16x16x32_bf16 v[80:83], a[196:199], a[184:187], v[80:83]\n" \
"ds_read_b64_tr_b16 a[222:223], v241 offset:44544\n" \
"v_mov_b32 v159, v157\n" \
"v_mfma_f32_16x16x32_bf16 v[112:115], a[196:199], a[188:191], v[112:115]\n" \
"v_permlane16_swap_b32 v156, v158\n" \
"v_mfma_f32_16x16x32_bf16 v[112:115], a[192:195], a[172:175], v[112:115]\n" \
"s_waitcnt lgkmcnt(8)\n" \
"v_permlane16_swap_b32 v157, v159\n" \
"v_mfma_f32_16x16x32_bf16 v[20:23], a[200:203], a[160:163], v[20:23]\n" \
"s_add_u32 m0, s72, 15360\n" \
"v_add_f32 v156, v156, v158\n" \
"v_add_f32 v157, v157, v159\n" \
"v_mfma_f32_16x16x32_bf16 v[20:23], a[204:207], a[176:179], v[20:23]\n" \
"s_add_u32 s74, s94, 57728\n" \
"v_mov_b32 v158, v156\n" \
"v_mov_b32 v159, v157\n" \
"v_mfma_f32_16x16x32_bf16 v[52:55], a[204:207], a[180:183], v[52:55]\n" \
"buffer_load_dwordx4 v250, s[16:19], s74 offen lds\n" \
"v_mfma_f32_16x16x32_bf16 v[52:55], a[200:203], a[164:167], v[52:55]\n" \
"v_permlane32_swap_b32 v156, v158\n" \
"v_mfma_f32_16x16x32_bf16 v[84:87], a[200:203], a[168:171], v[84:87]\n" \
"s_waitcnt vmcnt(26) lgkmcnt(0)\n" \
"s_barrier\n" \
"s_add_u32 m0, s70, 32768\n" \
"v_mfma_f32_16x16x32_bf16 v[84:87], a[204:207], a[184:187], v[84:87]\n" \
"s_add_u32 s74, s69, 1572864\n" \
"ds_read_b64_tr_b16 a[192:193], v240 offset:49152\n" \
"ds_read_b64_tr_b16 a[194:195], v240 offset:51200\n" \
"v_mfma_f32_16x16x32_bf16 v[116:119], a[204:207], a[188:191], v[116:119]\n" \
"buffer_load_dwordx4 v246, s[20:23], s74 offen lds\n" \
"v_mfma_f32_16x16x32_bf16 v[116:119], a[200:203], a[172:175], v[116:119]\n" \
"ds_read_b64_tr_b16 a[196:197], v240 offset:57344\n" \
"ds_read_b64_tr_b16 a[198:199], v240 offset:59392\n" \
"v_mfma_f32_16x16x32_bf16 v[24:27], a[208:211], a[160:163], v[24:27]\n" \
"ds_read_b64_tr_b16 a[200:201], v241 offset:49152\n" \
"v_mfma_f32_16x16x32_bf16 v[24:27], a[212:215], a[176:179], v[24:27]\n" \
"v_permlane32_swap_b32 v157, v159\n" \
"v_mfma_f32_16x16x32_bf16 v[56:59], a[212:215], a[180:183], v[56:59]\n" \
"ds_read_b64_tr_b16 a[202:203], v241 offset:51200\n" \
"v_add_f32 v156, v156, v158\n" \
"v_mfma_f32_16x16x32_bf16 v[56:59], a[208:211], a[164:167], v[56:59]\n" \
"v_add_f32 v157, v157, v159\n" \
"v_mul_f32 v156, 0x3b000000, v156\n" \
"v_mfma_f32_16x16x32_bf16 v[88:91], a[208:211], a[168:171], v[88:91]\n" \
"ds_read_b64_tr_b16 a[204:205], v241 offset:57344\n" \
"v_mul_f32 v157, 0x3b000000, v157\n" \
"v_mfma_f32_16x16x32_bf16 v[88:91], a[212:215], a[184:187], v[88:91]\n" \
"v_cvt_pk_bf16_f32 v254, v156, v157\n" \
"v_cvt_pk_bf16_f32 v255, v152, v153\n" \
"v_mfma_f32_16x16x32_bf16 v[120:123], a[212:215], a[188:191], v[120:123]\n" \
"ds_read_b64_tr_b16 a[206:207], v241 offset:59392\n" \
"v_lshlrev_b32 v156, 16, v254\n" \
"v_mfma_f32_16x16x32_bf16 v[120:123], a[208:211], a[172:175], v[120:123]\n" \
"v_and_b32 v157, 0xffff0000, v254\n" \
"v_lshlrev_b32 v152, 16, v255\n" \
"v_mfma_f32_16x16x32_bf16 v[28:31], a[216:219], a[160:163], v[28:31]\n" \
"ds_read_b64_tr_b16 a[208:209], v240 offset:49664\n" \
"v_and_b32 v153, 0xffff0000, v255\n" \
"v_mfma_f32_16x16x32_bf16 v[28:31], a[220:223], a[176:179], v[28:31]\n" \
"v_add_f32 v156, 0x3727c5ac, v156\n" \
"v_add_f32 v157, 0x3727c5ac, v157\n" \
"v_mfma_f32_16x16x32_bf16 v[60:63], a[220:223], a[180:183], v[60:63]\n" \
"s_add_u32 m0, s70, 33792\n" \
"ds_read_b64_tr_b16 a[210:211], v240 offset:51712\n" \
"v_cvt_pk_bf16_f32 v254, v156, v157\n" \
"v_mfma_f32_16x16x32_bf16 v[60:63], a[216:219], a[164:167], v[60:63]\n" \
"s_add_u32 s74, s69, 1572992\n" \
"v_lshlrev_b32 v156, 16, v254\n" \
"v_and_b32 v157, 0xffff0000, v254\n" \
"v_mfma_f32_16x16x32_bf16 v[92:95], a[216:219], a[168:171], v[92:95]\n" \
"buffer_load_dwordx4 v246, s[20:23], s74 offen lds\n" \
"v_mfma_f32_16x16x32_bf16 v[92:95], a[220:223], a[184:187], v[92:95]\n" \
"ds_read_b64_tr_b16 a[212:213], v240 offset:57856\n" \
"v_mfma_f32_16x16x32_bf16 v[124:127], a[220:223], a[188:191], v[124:127]\n" \
"s_waitcnt vmcnt(17)\n" \
"ds_read_b64_tr_b16 a[214:215], v240 offset:59904\n" \
"ds_read_b128 a[224:227], v242 offset:0\n" \
"v_mfma_f32_16x16x32_bf16 v[124:127], a[216:219], a[172:175], v[124:127]\n" \
"s_waitcnt lgkmcnt(9)\n" \
"v_sqrt_f32 v156, v156\n" \
"v_mfma_f32_16x16x32_bf16 a[0:3], a[160:163], a[192:195], a[0:3]\n" \
"ds_read_b64_tr_b16 a[216:217], v241 offset:49664\n" \
"ds_read_b128 a[228:231], v242 offset:2048\n" \
"v_mfma_f32_16x16x32_bf16 a[0:3], a[176:179], a[196:199], a[0:3]\n" \
"v_sqrt_f32 v157, v157\n" \
"v_mfma_f32_16x16x32_bf16 a[4:7], a[180:183], a[196:199], a[4:7]\n" \
"ds_read_b64_tr_b16 a[218:219], v241 offset:51712\n" \
"ds_read_b128 a[232:235], v242 offset:4096\n" \
"v_mfma_f32_16x16x32_bf16 a[4:7], a[164:167], a[192:195], a[4:7]\n" \
"v_cvt_pk_bf16_f32 v254, v156, v157\n" \
"v_lshlrev_b32 v156, 16, v254\n" \
"v_mfma_f32_16x16x32_bf16 a[64:67], a[168:171], a[192:195], a[64:67]\n" \
"ds_read_b64_tr_b16 a[220:221], v241 offset:57856\n" \
"ds_read_b128 a[236:239], v242 offset:6144\n" \
"v_mfma_f32_16x16x32_bf16 a[64:67], a[184:187], a[196:199], a[64:67]\n" \
"v_and_b32 v157, 0xffff0000, v254\n" \
"v_mfma_f32_16x16x32_bf16 a[68:71], a[188:191], a[196:199], a[68:71]\n" \
"ds_read_b64_tr_b16 a[222:223], v241 offset:59904\n" \
"v_mfma_f32_16x16x32_bf16 a[68:71], a[172:175], a[192:195], a[68:71]\n" \
"s_waitcnt lgkmcnt(12)\n" \
"v_rcp_f32 v156, v156\n" \
"v_mfma_f32_16x16x32_bf16 a[8:11], a[160:163], a[200:203], a[8:11]\n" \
"s_add_u32 m0, s70, 40960\n" \
"ds_read_b64_tr_b16 a[192:193], v240 offset:50176\n" \
"v_mfma_f32_16x16x32_bf16 a[8:11], a[176:179], a[204:207], a[8:11]\n" \
"s_add_u32 s74, s69, 1835008\n" \
"v_rcp_f32 v157, v157\n" \
"v_mfma_f32_16x16x32_bf16 a[12:15], a[180:183], a[204:207], a[12:15]\n" \
"buffer_load_dwordx4 v246, s[20:23], s74 offen lds\n" \
"v_mfma_f32_16x16x32_bf16 a[12:15], a[164:167], a[200:203], a[12:15]\n" \
"ds_read_b64_tr_b16 a[194:195], v240 offset:52224\n" \
"ds_read_b128 a[240:243], v244 offset:0\n" \
"v_mfma_f32_16x16x32_bf16 a[72:75], a[168:171], a[200:203], a[72:75]\n" \
"ds_read_b64_tr_b16 a[196:197], v240 offset:58368\n" \
"ds_read_b128 a[244:247], v244 offset:2048\n" \
"v_mfma_f32_16x16x32_bf16 a[72:75], a[184:187], a[204:207], a[72:75]\n" \
"v_sub_f32 v136, v136, v152\n" \
"v_sub_f32 v137, v137, v152\n" \
"v_mfma_f32_16x16x32_bf16 a[76:79], a[188:191], a[204:207], a[76:79]\n" \
"ds_read_b64_tr_b16 a[198:199], v240 offset:60416\n" \
"ds_read_b128 a[248:251], v244 offset:4096\n" \
"v_mfma_f32_16x16x32_bf16 a[76:79], a[172:175], a[200:203], a[76:79]\n" \
"s_waitcnt lgkmcnt(15)\n" \
"v_cvt_pk_bf16_f32 v128, v136, v137\n" \
"v_lshlrev_b32 v136, 16, v128\n" \
"v_mfma_f32_16x16x32_bf16 a[16:19], a[160:163], a[208:211], a[16:19]\n" \
"s_lshl2_add_u32 s81, s64, %[wid]\n" \
"ds_read_b64_tr_b16 a[200:201], v241 offset:50176\n" \
"ds_read_b128 a[252:255], v244 offset:6144\n" \
"v_mfma_f32_16x16x32_bf16 a[16:19], a[176:179], a[212:215], a[16:19]\n" \
"s_lshl_b32 s81, s81, 9\n" \
"v_and_b32 v137, 0xffff0000, v128\n" \
"v_mul_f32 v136, v136, v156\n" \
"v_mfma_f32_16x16x32_bf16 a[20:23], a[180:183], a[212:215], a[20:23]\n" \
"s_add_u32 s81, s81, 573440\n" \
"buffer_load_dwordx2 v[204:205], v253, s[32:35], s81 offen sc1\n" \
"v_mul_f32 v137, v137, v156\n" \
"v_mfma_f32_16x16x32_bf16 a[20:23], a[164:167], a[208:211], a[20:23]\n" \
"ds_read_b64_tr_b16 a[202:203], v241 offset:52224\n" \
"v_cvt_pk_bf16_f32 v128, v136, v137\n" \
"v_mfma_f32_16x16x32_bf16 a[80:83], a[168:171], a[208:211], a[80:83]\n" \
"ds_read_b64_tr_b16 a[204:205], v241 offset:58368\n" \
"v_sub_f32 v138, v138, v152\n" \
"v_mfma_f32_16x16x32_bf16 a[80:83], a[184:187], a[212:215], a[80:83]\n" \
"v_sub_f32 v139, v139, v152\n" \
"v_cvt_pk_bf16_f32 v129, v138, v139\n" \
"v_mfma_f32_16x16x32_bf16 a[84:87], a[188:191], a[212:215], a[84:87]\n" \
"s_add_u32 m0, s70, 41984\n" \
"ds_read_b64_tr_b16 a[206:207], v241 offset:60416\n" \
"v_lshlrev_b32 v138, 16, v129\n" \
"v_mfma_f32_16x16x32_bf16 a[84:87], a[172:175], a[208:211], a[84:87]\n" \
"s_waitcnt lgkmcnt(12)\n" \
"v_and_b32 v139, 0xffff0000, v129\n" \
"v_mul_f32 v138, v138, v156\n" \
"v_mfma_f32_16x16x32_bf16 a[24:27], a[160:163], a[216:219], a[24:27]\n" \
"s_add_u32 s74, s69, 1835136\n" \
"buffer_load_dwordx4 v246, s[20:23], s74 offen lds\n" \
"v_mfma_f32_16x16x32_bf16 a[24:27], a[176:179], a[220:223], a[24:27]\n" \
"ds_read_b64_tr_b16 a[208:209], v240 offset:50688\n" \
"v_mul_f32 v139, v139, v156\n" \
"v_mfma_f32_16x16x32_bf16 a[28:31], a[180:183], a[220:223], a[28:31]\n" \
"ds_read_b64_tr_b16 a[210:211], v240 offset:52736\n" \
"v_cvt_pk_bf16_f32 v129, v138, v139\n" \
"v_mfma_f32_16x16x32_bf16 a[28:31], a[164:167], a[216:219], a[28:31]\n" \
"v_sub_f32 v140, v140, v152\n" \
"v_sub_f32 v141, v141, v152\n" \
"v_mfma_f32_16x16x32_bf16 a[88:91], a[168:171], a[216:219], a[88:91]\n" \
"s_add_u32 m0, s72, 0\n" \
"ds_read_b64_tr_b16 a[212:213], v240 offset:58880\n" \
"v_cvt_pk_bf16_f32 v130, v140, v141\n" \
"v_mfma_f32_16x16x32_bf16 a[88:91], a[184:187], a[220:223], a[88:91]\n" \
"s_add_u32 s74, s71, 512\n" \
"v_lshlrev_b32 v140, 16, v130\n" \
"v_and_b32 v141, 0xffff0000, v130\n" \
"v_mfma_f32_16x16x32_bf16 a[92:95], a[188:191], a[220:223], a[92:95]\n" \
"s_waitcnt lgkmcnt(6)\n" \
"buffer_load_dwordx4 v249, s[16:19], s74 offen lds\n" \
"v_mfma_f32_16x16x32_bf16 a[92:95], a[172:175], a[216:219], a[92:95]\n" \
"ds_read_b64_tr_b16 a[214:215], v240 offset:60928\n" \
"v_mul_f32 v140, v140, v156\n" \
"v_mfma_f32_16x16x32_bf16 a[32:35], a[160:163], a[192:195], a[32:35]\n" \
"ds_read_b64_tr_b16 a[216:217], v241 offset:50688\n" \
"v_mul_f32 v141, v141, v156\n" \
"v_mfma_f32_16x16x32_bf16 a[32:35], a[176:179], a[196:199], a[32:35]\n" \
"s_lshl2_add_u32 s85, s67, %[wid]\n" \
"v_cvt_pk_bf16_f32 v130, v140, v141\n" \
"v_sub_f32 v142, v142, v152\n" \
"v_mfma_f32_16x16x32_bf16 a[36:39], a[180:183], a[196:199], a[36:39]\n" \
"s_add_u32 m0, s72, 1024\n" \
"ds_read_b64_tr_b16 a[218:219], v241 offset:52736\n" \
"v_sub_f32 v143, v143, v152\n" \
"v_mfma_f32_16x16x32_bf16 a[36:39], a[164:167], a[192:195], a[36:39]\n" \
"s_add_u32 s74, s71, 8704\n" \
"ds_read_b64_tr_b16 a[220:221], v241 offset:58880\n" \
"v_cvt_pk_bf16_f32 v131, v142, v143\n" \
"v_mfma_f32_16x16x32_bf16 a[96:99], a[168:171], a[192:195], a[96:99]\n" \
"s_lshl_b32 s85, s85, 16\n" \
"buffer_load_dwordx4 v250, s[16:19], s74 offen lds\n" \
"v_mfma_f32_16x16x32_bf16 a[96:99], a[184:187], a[196:199], a[96:99]\n" \
"s_lshl_b32 s86, s68, 8\n" \
"ds_read_b64_tr_b16 a[222:223], v241 offset:60928\n" \
"v_lshlrev_b32 v142, 16, v131\n" \
"v_mfma_f32_16x16x32_bf16 a[100:103], a[188:191], a[196:199], a[100:103]\n" \
"s_add_u32 s85, s85, s86\n" \
"v_and_b32 v143, 0xffff0000, v131\n" \
"v_mul_f32 v142, v142, v156\n" \
"v_mfma_f32_16x16x32_bf16 a[100:103], a[172:175], a[192:195], a[100:103]\n" \
"s_waitcnt lgkmcnt(8)\n" \
"v_mul_f32 v143, v143, v156\n" \
"v_cvt_pk_bf16_f32 v131, v142, v143\n" \
"v_mfma_f32_16x16x32_bf16 a[40:43], a[160:163], a[200:203], a[40:43]\n" \
"s_add_u32 m0, s72, 2048\n" \
"v_sub_f32 v144, v144, v153\n" \
"v_sub_f32 v145, v145, v153\n" \
"v_mfma_f32_16x16x32_bf16 a[40:43], a[176:179], a[204:207], a[40:43]\n" \
"s_add_u32 s74, s71, 16896\n" \
"v_cvt_pk_bf16_f32 v132, v144, v145\n" \
"v_lshlrev_b32 v144, 16, v132\n" \
"v_mfma_f32_16x16x32_bf16 a[44:47], a[180:183], a[204:207], a[44:47]\n" \
"s_add_u32 s85, s85, 2097152\n" \
"buffer_load_dwordx4 v249, s[16:19], s74 offen lds\n" \
"v_mfma_f32_16x16x32_bf16 a[44:47], a[164:167], a[200:203], a[44:47]\n" \
"buffer_load_dword v245, v208, s[24:27], s85 offen\n" \
"v_and_b32 v145, 0xffff0000, v132\n" \
"v_mfma_f32_16x16x32_bf16 a[104:107], a[168:171], a[200:203], a[104:107]\n" \
"s_waitcnt vmcnt(22) lgkmcnt(0)\n" \
"s_barrier\n" \
"v_mul_f32 v144, v144, v157\n" \
"v_mfma_f32_16x16x32_bf16 a[104:107], a[184:187], a[204:207], a[104:107]\n" \
"s_add_u32 m0, s70, 49152\n" \
"ds_read_b64_tr_b16 a[192:193], v240 offset:0\n" \
"ds_read_b64_tr_b16 a[194:195], v240 offset:2048\n" \
"v_mfma_f32_16x16x32_bf16 a[108:111], a[188:191], a[204:207], a[108:111]\n" \
"s_add_u32 s74, s69, 1572864\n" \
"buffer_load_dwordx4 v246, s[24:27], s74 offen lds\n" \
"v_mfma_f32_16x16x32_bf16 a[108:111], a[172:175], a[200:203], a[108:111]\n" \
"s_add_u32 m0, s72, 3072\n" \
"ds_read_b64_tr_b16 a[196:197], v240 offset:8192\n" \
"ds_read_b64_tr_b16 a[198:199], v240 offset:10240\n" \
"v_mfma_f32_16x16x32_bf16 a[48:51], a[160:163], a[208:211], a[48:51]\n" \
"s_add_u32 s74, s71, 25088\n" \
"ds_read_b64_tr_b16 a[200:201], v241 offset:0\n" \
"v_mul_f32 v145, v145, v157\n" \
"v_mfma_f32_16x16x32_bf16 a[48:51], a[176:179], a[212:215], a[48:51]\n" \
"buffer_load_dwordx4 v250, s[16:19], s74 offen lds\n" \
"v_mfma_f32_16x16x32_bf16 a[52:55], a[180:183], a[212:215], a[52:55]\n" \
"ds_read_b64_tr_b16 a[202:203], v241 offset:2048\n" \
"v_cvt_pk_bf16_f32 v132, v144, v145\n" \
"v_mfma_f32_16x16x32_bf16 a[52:55], a[164:167], a[208:211], a[52:55]\n" \
"v_sub_f32 v146, v146, v153\n" \
"v_sub_f32 v147, v147, v153\n" \
"v_mfma_f32_16x16x32_bf16 a[112:115], a[168:171], a[208:211], a[112:115]\n" \
"s_add_u32 m0, s72, 4096\n" \
"ds_read_b64_tr_b16 a[204:205], v241 offset:8192\n" \
"v_cvt_pk_bf16_f32 v133, v146, v147\n" \
"v_mfma_f32_16x16x32_bf16 a[112:115], a[184:187], a[212:215], a[112:115]\n" \
"s_add_u32 s74, s94, 33280\n" \
"v_lshlrev_b32 v146, 16, v133\n" \
"v_and_b32 v147, 0xffff0000, v133\n" \
"v_mfma_f32_16x16x32_bf16 a[116:119], a[188:191], a[212:215], a[116:119]\n" \
"buffer_load_dwordx4 v249, s[16:19], s74 offen lds\n" \
"v_mfma_f32_16x16x32_bf16 a[116:119], a[172:175], a[208:211], a[116:119]\n" \
"ds_read_b64_tr_b16 a[206:207], v241 offset:10240\n" \
"v_mul_f32 v146, v146, v157\n" \
"v_mfma_f32_16x16x32_bf16 a[56:59], a[160:163], a[216:219], a[56:59]\n" \
"ds_read_b64_tr_b16 a[208:209], v240 offset:512\n" \
"v_mul_f32 v147, v147, v157\n" \
"v_mfma_f32_16x16x32_bf16 a[56:59], a[176:179], a[220:223], a[56:59]\n" \
"v_cvt_pk_bf16_f32 v133, v146, v147\n" \
"v_sub_f32 v148, v148, v153\n" \
"v_mfma_f32_16x16x32_bf16 a[60:63], a[180:183], a[220:223], a[60:63]\n" \
"s_add_u32 m0, s70, 50176\n" \
"ds_read_b64_tr_b16 a[210:211], v240 offset:2560\n" \
"v_sub_f32 v149, v149, v153\n" \
"v_mfma_f32_16x16x32_bf16 a[60:63], a[164:167], a[216:219], a[60:63]\n" \
"s_add_u32 s74, s69, 1572992\n" \
"v_cvt_pk_bf16_f32 v134, v148, v149\n" \
"v_lshlrev_b32 v148, 16, v134\n" \
"v_mfma_f32_16x16x32_bf16 a[120:123], a[168:171], a[216:219], a[120:123]\n" \
"buffer_load_dwordx4 v246, s[24:27], s74 offen lds\n" \
"v_mfma_f32_16x16x32_bf16 a[120:123], a[184:187], a[220:223], a[120:123]\n" \
"s_add_u32 m0, s72, 5120\n" \
"ds_read_b64_tr_b16 a[212:213], v240 offset:8704\n" \
"v_and_b32 v149, 0xffff0000, v134\n" \
"v_mfma_f32_16x16x32_bf16 a[124:127], a[188:191], a[220:223], a[124:127]\n" \
"s_add_u32 s74, s94, 41472\n" \
"ds_read_b64_tr_b16 a[214:215], v240 offset:10752\n" \
"v_mul_f32 v148, v148, v157\n" \
"v_mfma_f32_16x16x32_bf16 a[124:127], a[172:175], a[216:219], a[124:127]\n" \
"s_waitcnt lgkmcnt(8)\n" \
"buffer_load_dwordx4 v250, s[16:19], s74 offen lds\n" \
"v_mfma_f32_16x16x32_bf16 v[0:3], a[192:195], a[224:227], v[0:3]\n" \
"s_add_u32 m0, s72, 6144\n" \
"ds_read_b64_tr_b16 a[216:217], v241 offset:512\n" \
"v_mul_f32 v149, v149, v157\n" \
"v_mfma_f32_16x16x32_bf16 v[0:3], a[196:199], a[240:243], v[0:3]\n" \
"s_add_u32 s74, s94, 49664\n" \
"v_cvt_pk_bf16_f32 v134, v148, v149\n" \
"v_sub_f32 v150, v150, v153\n" \
"v_mfma_f32_16x16x32_bf16 v[32:35], a[196:199], a[244:247], v[32:35]\n" \
"buffer_load_dwordx4 v249, s[16:19], s74 offen lds\n" \
"v_mfma_f32_16x16x32_bf16 v[32:35], a[192:195], a[228:231], v[32:35]\n" \
"ds_read_b64_tr_b16 a[218:219], v241 offset:2560\n" \
"v_sub_f32 v151, v151, v153\n" \
"v_mfma_f32_16x16x32_bf16 v[64:67], a[192:195], a[232:235], v[64:67]\n" \
"ds_read_b64_tr_b16 a[220:221], v241 offset:8704\n" \
"v_cvt_pk_bf16_f32 v135, v150, v151\n" \
"v_mfma_f32_16x16x32_bf16 v[64:67], a[196:199], a[248:251], v[64:67]\n" \
"v_lshlrev_b32 v150, 16, v135\n" \
"v_and_b32 v151, 0xffff0000, v135\n" \
"v_mfma_f32_16x16x32_bf16 v[96:99], a[196:199], a[252:255], v[96:99]\n" \
"s_add_u32 m0, s72, 7168\n" \
"ds_read_b64_tr_b16 a[222:223], v241 offset:10752\n" \
"v_mul_f32 v150, v150, v157\n" \
"v_mfma_f32_16x16x32_bf16 v[96:99], a[192:195], a[236:239], v[96:99]\n" \
"s_waitcnt lgkmcnt(8)\n" \
"v_mul_f32 v151, v151, v157\n" \
"v_cvt_pk_bf16_f32 v135, v150, v151\n" \
"v_mfma_f32_16x16x32_bf16 v[4:7], a[200:203], a[224:227], v[4:7]\n" \
"s_add_u32 s74, s94, 57856\n" \
"buffer_load_dwordx4 v250, s[16:19], s74 offen lds\n" \
"v_mfma_f32_16x16x32_bf16 v[4:7], a[204:207], a[240:243], v[4:7]\n" \
"s_add_u32 m0, s70, 57344\n" \
"s_add_u32 s74, s69, 1835008\n" \
"ds_read_b64_tr_b16 a[192:193], v240 offset:1024\n" \
"v_mfma_f32_16x16x32_bf16 v[36:39], a[204:207], a[244:247], v[36:39]\n" \
"s_mul_i32 s76, %[wid], 2080\n" \
"s_add_u32 s76, s76, 149504\n" \
"ds_read_b64_tr_b16 a[194:195], v240 offset:3072\n" \
"v_mfma_f32_16x16x32_bf16 v[36:39], a[200:203], a[228:231], v[36:39]\n" \
"buffer_load_dwordx4 v246, s[24:27], s74 offen lds\n" \
"v_mfma_f32_16x16x32_bf16 v[68:71], a[200:203], a[232:235], v[68:71]\n" \
"ds_read_b64_tr_b16 a[196:197], v240 offset:9216\n" \
"v_add_u32 v254, s76, v248\n" \
"v_mfma_f32_16x16x32_bf16 v[68:71], a[204:207], a[248:251], v[68:71]\n" \
"ds_write_b128 v254, v[128:131]\n" \
"v_mfma_f32_16x16x32_bf16 v[100:103], a[204:207], a[252:255], v[100:103]\n" \
"ds_read_b64_tr_b16 a[198:199], v240 offset:11264\n" \
"v_mfma_f32_16x16x32_bf16 v[100:103], a[200:203], a[236:239], v[100:103]\n" \
"s_waitcnt lgkmcnt(9)\n" \
"ds_write_b128 v254, v[132:135] offset:1040\n" \
"v_and_b32 v254, 7, v251\n" \
"v_mfma_f32_16x16x32_bf16 v[8:11], a[208:211], a[224:227], v[8:11]\n" \
"ds_read_b64_tr_b16 a[200:201], v241 offset:1024\n" \
"v_mul_u32_u24 v254, 1040, v254\n" \
"v_mfma_f32_16x16x32_bf16 v[8:11], a[212:215], a[240:243], v[8:11]\n" \
"v_lshrrev_b32 v255, 3, v251\n" \
"v_lshl_add_u32 v254, v255, 4, v254\n" \
"v_mfma_f32_16x16x32_bf16 v[40:43], a[212:215], a[244:247], v[40:43]\n" \
"s_lshl_b32 s76, %[wid], 7\n" \
"s_add_u32 s76, s76, 149504\n" \
"ds_read_b64_tr_b16 a[202:203], v241 offset:3072\n" \
"v_mfma_f32_16x16x32_bf16 v[40:43], a[208:211], a[228:231], v[40:43]\n" \
"v_add_u32 v254, s76, v254\n" \
"v_mfma_f32_16x16x32_bf16 v[72:75], a[208:211], a[232:235], v[72:75]\n" \
"ds_read_b64_tr_b16 a[204:205], v241 offset:9216\n" \
"v_mfma_f32_16x16x32_bf16 v[72:75], a[212:215], a[248:251], v[72:75]\n" \
"v_mfma_f32_16x16x32_bf16 v[104:107], a[212:215], a[252:255], v[104:107]\n" \
"s_add_u32 m0, s70, 58368\n" \
"ds_read_b64_tr_b16 a[206:207], v241 offset:11264\n" \
"v_mfma_f32_16x16x32_bf16 v[104:107], a[208:211], a[236:239], v[104:107]\n" \
".Lst0_1_%=:\n" \
"s_cbranch_execz .Lst0_5_%=\n" \
"s_waitcnt lgkmcnt(10)\n" \
"s_add_u32 s74, s69, 1835136\n" \
"v_mfma_f32_16x16x32_bf16 v[12:15], a[216:219], a[224:227], v[12:15]\n" \
"buffer_load_dwordx4 v246, s[24:27], s74 offen lds\n" \
"v_mfma_f32_16x16x32_bf16 v[12:15], a[220:223], a[240:243], v[12:15]\n" \
"ds_read_b64_tr_b16 a[208:209], v240 offset:1536\n" \
"v_mfma_f32_16x16x32_bf16 v[44:47], a[220:223], a[244:247], v[44:47]\n" \
"ds_read_b64_tr_b16 a[210:211], v240 offset:3584\n" \
"v_mfma_f32_16x16x32_bf16 v[44:47], a[216:219], a[228:231], v[44:47]\n" \
"v_mfma_f32_16x16x32_bf16 v[76:79], a[216:219], a[232:235], v[76:79]\n" \
"ds_read_b64_tr_b16 a[212:213], v240 offset:9728\n" \
"v_mfma_f32_16x16x32_bf16 v[76:79], a[220:223], a[248:251], v[76:79]\n" \
"v_mfma_f32_16x16x32_bf16 v[108:111], a[220:223], a[252:255], v[108:111]\n" \
"ds_read_b64_tr_b16 a[214:215], v240 offset:11776\n" \
"v_mfma_f32_16x16x32_bf16 v[108:111], a[216:219], a[236:239], v[108:111]\n" \
"s_waitcnt lgkmcnt(9)\n" \
"v_mfma_f32_16x16x32_bf16 v[16:19], a[192:195], a[224:227], v[16:19]\n" \
"s_waitcnt vmcnt(14)\n" \
"ds_read_b64_tr_b16 a[216:217], v241 offset:1536\n" \
"v_cmp_ne_u64 vcc, v[204:205], v[202:203]\n" \
"v_mfma_f32_16x16x32_bf16 v[16:19], a[196:199], a[240:243], v[16:19]\n" \
"v_mfma_f32_16x16x32_bf16 v[48:51], a[196:199], a[244:247], v[48:51]\n" \
"ds_read_b64_tr_b16 a[218:219], v241 offset:3584\n" \
"v_mfma_f32_16x16x32_bf16 v[48:51], a[192:195], a[228:231], v[48:51]\n" \
"s_cbranch_vccnz .Lspq_%=\n" \
".Lsbq_%=:\n" \
"ds_read_b64_tr_b16 a[220:221], v241 offset:9728\n" \
"v_mfma_f32_16x16x32_bf16 v[80:83], a[192:195], a[232:235], v[80:83]\n" \
"ds_read_b64_tr_b16 a[222:223], v241 offset:11776\n" \
"v_mfma_f32_16x16x32_bf16 v[80:83], a[196:199], a[248:251], v[80:83]\n" \
"v_mfma_f32_16x16x32_bf16 v[112:115], a[196:199], a[252:255], v[112:115]\n" \
"v_mfma_f32_16x16x32_bf16 v[112:115], a[192:195], a[236:239], v[112:115]\n" \
"s_waitcnt lgkmcnt(8)\n" \
"v_mfma_f32_16x16x32_bf16 v[20:23], a[200:203], a[224:227], v[20:23]\n" \
"v_mfma_f32_16x16x32_bf16 v[20:23], a[204:207], a[240:243], v[20:23]\n" \
"v_mfma_f32_16x16x32_bf16 v[52:55], a[204:207], a[244:247], v[52:55]\n" \
"v_mfma_f32_16x16x32_bf16 v[52:55], a[200:203], a[228:231], v[52:55]\n" \
"v_mfma_f32_16x16x32_bf16 v[84:87], a[200:203], a[232:235], v[84:87]\n" \
"s_waitcnt lgkmcnt(0)\n" \
"s_barrier\n" \
"ds_read_b128 a[128:131], v242 offset:8192\n" \
"v_mfma_f32_16x16x32_bf16 v[84:87], a[204:207], a[248:251], v[84:87]\n" \
"s_add_u32 m0, s70, 0\n" \
"ds_read_b64_tr_b16 a[192:193], v240 offset:16384\n" \
"ds_read_b64_tr_b16 a[194:195], v240 offset:18432\n" \
"v_mfma_f32_16x16x32_bf16 v[116:119], a[204:207], a[252:255], v[116:119]\n" \
"s_add_u32 s74, s69, 2097152\n" \
"buffer_load_dwordx4 v246, s[20:23], s74 offen lds\n" \
"v_mfma_f32_16x16x32_bf16 v[116:119], a[200:203], a[236:239], v[116:119]\n" \
"s_lshl_b32 s85, %[wid], 10\n" \
"ds_read_b64_tr_b16 a[196:197], v240 offset:24576\n" \
"ds_read_b64_tr_b16 a[198:199], v240 offset:26624\n" \
"v_mfma_f32_16x16x32_bf16 v[24:27], a[208:211], a[224:227], v[24:27]\n" \
"s_add_u32 s85, s85, 917504\n" \
"buffer_load_dwordx4 v[168:171], v248, s[32:35], s85 offen sc1\n" \
"v_and_b32 v252, 3, v251\n" \
"v_mfma_f32_16x16x32_bf16 v[24:27], a[212:215], a[240:243], v[24:27]\n" \
"s_lshl_b32 s85, %[wid], 10\n" \
"ds_read_b64_tr_b16 a[200:201], v241 offset:16384\n" \
"ds_read_b128 a[132:135], v242 offset:10240\n" \
"v_mfma_f32_16x16x32_bf16 v[56:59], a[212:215], a[244:247], v[56:59]\n" \
"s_add_u32 s85, s85, 929792\n" \
"buffer_load_dwordx4 v[172:175], v248, s[32:35], s85 offen sc1\n" \
"v_mul_u32_u24 v252, 0x3000, v252\n" \
"v_mfma_f32_16x16x32_bf16 v[56:59], a[208:211], a[228:231], v[56:59]\n" \
"s_lshl_b32 s85, %[wid], 10\n" \
"ds_read_b64_tr_b16 a[202:203], v241 offset:18432\n" \
"ds_read_b128 a[136:139], v242 offset:12288\n" \
"v_mfma_f32_16x16x32_bf16 v[88:91], a[208:211], a[232:235], v[88:91]\n" \
"s_add_u32 s85, s85, 942080\n" \
"buffer_load_dwordx4 v[176:179], v248, s[32:35], s85 offen sc1\n" \
"v_and_b32 v253, 28, v251\n" \
"v_mfma_f32_16x16x32_bf16 v[88:91], a[212:215], a[248:251], v[88:91]\n" \
"s_lshl_b32 s85, %[wid], 10\n" \
"ds_read_b64_tr_b16 a[204:205], v241 offset:24576\n" \
"ds_read_b128 a[140:143], v242 offset:14336\n" \
"v_mfma_f32_16x16x32_bf16 v[120:123], a[212:215], a[252:255], v[120:123]\n" \
"s_add_u32 s85, s85, 954368\n" \
"buffer_load_dwordx4 v[180:183], v248, s[32:35], s85 offen sc1\n" \
"v_add_u32 v252, v252, v253\n" \
"v_mfma_f32_16x16x32_bf16 v[120:123], a[208:211], a[236:239], v[120:123]\n" \
"s_lshl_b32 s85, %[bid], 5\n" \
"s_add_u32 s85, s85, 921600\n" \
"ds_read_b64_tr_b16 a[206:207], v241 offset:26624\n" \
"v_mfma_f32_16x16x32_bf16 v[28:31], a[216:219], a[224:227], v[28:31]\n" \
"s_cmp_lt_u32 %[wid], 2\n" \
"s_cselect_b32 s83, 0, 1.0\n" \
"buffer_load_dword v200, v252, s[32:35], s85 offen sc1\n" \
"v_mfma_f32_16x16x32_bf16 v[28:31], a[220:223], a[240:243], v[28:31]\n" \
"ds_read_b64_tr_b16 a[208:209], v240 offset:16896\n" \
"ds_read_b128 a[144:147], v244 offset:8192\n" \
"v_mfma_f32_16x16x32_bf16 v[60:63], a[220:223], a[244:247], v[60:63]\n" \
"s_add_u32 m0, s70, 1024\n" \
"ds_read_b64_tr_b16 a[210:211], v240 offset:18944\n" \
"ds_read_b128 a[148:151], v244 offset:10240\n" \
"v_mfma_f32_16x16x32_bf16 v[60:63], a[216:219], a[228:231], v[60:63]\n" \
"s_add_u32 s74, s69, 2097280\n" \
"v_mfma_f32_16x16x32_bf16 v[92:95], a[216:219], a[232:235], v[92:95]\n" \
"buffer_load_dwordx4 v246, s[20:23], s74 offen lds\n" \
"v_mfma_f32_16x16x32_bf16 v[92:95], a[220:223], a[248:251], v[92:95]\n" \
"ds_read_b64_tr_b16 a[212:213], v240 offset:25088\n" \
"ds_read_b128 a[152:155], v244 offset:12288\n" \
"v_mfma_f32_16x16x32_bf16 v[124:127], a[220:223], a[252:255], v[124:127]\n" \
"ds_read_b64_tr_b16 a[214:215], v240 offset:27136\n" \
"ds_read_b128 a[156:159], v244 offset:14336\n" \
"v_mfma_f32_16x16x32_bf16 v[124:127], a[216:219], a[236:239], v[124:127]\n" \
"s_waitcnt lgkmcnt(15)\n" \
"v_mfma_f32_16x16x32_bf16 a[0:3], a[224:227], a[192:195], a[0:3]\n" \
"ds_read_b64_tr_b16 a[216:217], v241 offset:16896\n" \
"v_mfma_f32_16x16x32_bf16 a[0:3], a[240:243], a[196:199], a[0:3]\n" \
"v_mfma_f32_16x16x32_bf16 a[4:7], a[244:247], a[196:199], a[4:7]\n" \
"ds_read_b64_tr_b16 a[218:219], v241 offset:18944\n" \
"v_mfma_f32_16x16x32_bf16 a[4:7], a[228:231], a[192:195], a[4:7]\n" \
"v_mfma_f32_16x16x32_bf16 a[64:67], a[232:235], a[192:195], a[64:67]\n" \
"ds_read_b64_tr_b16 a[220:221], v241 offset:25088\n" \
"v_mfma_f32_16x16x32_bf16 a[64:67], a[248:251], a[196:199], a[64:67]\n" \
"v_mfma_f32_16x16x32_bf16 a[68:71], a[252:255], a[196:199], a[68:71]\n" \
"ds_read_b64_tr_b16 a[222:223], v241 offset:27136\n" \
"v_mfma_f32_16x16x32_bf16 a[68:71], a[236:239], a[192:195], a[68:71]\n" \
"s_waitcnt lgkmcnt(12)\n" \
"v_mfma_f32_16x16x32_bf16 a[8:11], a[224:227], a[200:203], a[8:11]\n" \
"s_add_u32 m0, s70, 8192\n" \
"ds_read_b64_tr_b16 a[192:193], v240 offset:17408\n" \
"v_mfma_f32_16x16x32_bf16 a[8:11], a[240:243], a[204:207], a[8:11]\n" \
"s_add_u32 s74, s69, 2359296\n" \
"v_mfma_f32_16x16x32_bf16 a[12:15], a[244:247], a[204:207], a[12:15]\n" \
"s_lshl_b32 s89, %[wid], 10\n" \
"buffer_load_dwordx4 v246, s[20:23], s74 offen lds\n" \
"v_mfma_f32_16x16x32_bf16 a[12:15], a[228:231], a[200:203], a[12:15]\n" \
"s_add_u32 m0, s72, 8192\n" \
"s_add_u32 s74, s71, 640\n" \
"ds_read_b64_tr_b16 a[194:195], v240 offset:19456\n" \
"v_mfma_f32_16x16x32_bf16 a[72:75], a[232:235], a[200:203], a[72:75]\n" \
"s_add_u32 s89, s89, 159744\n" \
"ds_read_b64_tr_b16 a[196:197], v240 offset:25600\n" \
"v_add_u32 v236, s89, v248\n" \
"v_mfma_f32_16x16x32_bf16 a[72:75], a[248:251], a[204:207], a[72:75]\n" \
"s_waitcnt lgkmcnt(7)\n" \
"buffer_load_dwordx4 v249, s[16:19], s74 offen lds\n" \
"v_mfma_f32_16x16x32_bf16 a[76:79], a[252:255], a[204:207], a[76:79]\n" \
"s_mov_b32 s77, 0x80b583ec\n" \
"ds_read_b64_tr_b16 a[198:199], v240 offset:27648\n" \
"v_lshrrev_b32 v237, 3, v251\n" \
"v_mfma_f32_16x16x32_bf16 a[76:79], a[236:239], a[200:203], a[76:79]\n" \
"v_lshlrev_b32 v237, 5, v237\n" \
"v_mov_b32 v253, 0x61c88646\n" \
"v_mfma_f32_16x16x32_bf16 a[16:19], a[224:227], a[208:211], a[16:19]\n" \
"s_add_u32 m0, s72, 9216\n" \
"ds_read_b64_tr_b16 a[200:201], v241 offset:17408\n" \
"v_add_co_u32 v202, vcc, s77, v202\n" \
"v_mfma_f32_16x16x32_bf16 a[16:19], a[240:243], a[212:215], a[16:19]\n" \
"s_add_u32 s74, s71, 8832\n" \
"s_lshl_b32 s89, %[wid], 8\n" \
"s_add_u32 s89, s89, 159744\n" \
"v_mfma_f32_16x16x32_bf16 a[20:23], a[244:247], a[212:215], a[20:23]\n" \
"buffer_load_dwordx4 v250, s[16:19], s74 offen lds\n" \
"v_mfma_f32_16x16x32_bf16 a[20:23], a[228:231], a[208:211], a[20:23]\n" \
"s_or_b32 s85, %[bid], %[wid]\n" \
"ds_read_b64_tr_b16 a[202:203], v241 offset:19456\n" \
"v_add_u32 v237, s89, v237\n" \
"v_mfma_f32_16x16x32_bf16 a[80:83], a[232:235], a[208:211], a[80:83]\n" \
"s_cmp_eq_u32 s85, 0\n" \
"s_cselect_b32 s85, 720896, -1\n" \
"ds_read_b64_tr_b16 a[204:205], v241 offset:25600\n" \
"v_mfma_f32_16x16x32_bf16 a[80:83], a[248:251], a[212:215], a[80:83]\n" \
"s_lshr_b32 s93, %[bid], 6\n" \
"v_addc_co_u32 v203, vcc, v253, v203, vcc\n" \
"v_mov_b32 v253, 0\n" \
"v_mfma_f32_16x16x32_bf16 a[84:87], a[252:255], a[212:215], a[84:87]\n" \
"s_add_u32 m0, s70, 9216\n" \
"buffer_store_dwordx2 v[202:203], v253, s[32:35], s85 offen sc1\n" \
"v_mfma_f32_16x16x32_bf16 a[84:87], a[236:239], a[208:211], a[84:87]\n" \
"s_waitcnt lgkmcnt(7)\n" \
"s_add_u32 s74, s69, 2359424\n" \
"ds_read_b64_tr_b16 a[206:207], v241 offset:27648\n" \
"v_mfma_f32_16x16x32_bf16 a[24:27], a[224:227], a[216:219], a[24:27]\n" \
"s_cmp_eq_u32 s93, 2\n" \
"s_cselect_b32 s93, 1.0, 0\n" \
"ds_read_b64_tr_b16 a[208:209], v240 offset:17920\n" \
"v_mfma_f32_16x16x32_bf16 a[24:27], a[240:243], a[220:223], a[24:27]\n" \
"buffer_load_dwordx4 v246, s[20:23], s74 offen lds\n" \
"v_mfma_f32_16x16x32_bf16 a[28:31], a[244:247], a[220:223], a[28:31]\n" \
"s_add_u32 m0, s72, 10240\n" \
"s_add_u32 s74, s71, 17024\n" \
"ds_read_b64_tr_b16 a[210:211], v240 offset:19968\n" \
"v_mfma_f32_16x16x32_bf16 a[28:31], a[228:231], a[216:219], a[28:31]\n" \
"v_mfma_f32_16x16x32_bf16 a[88:91], a[232:235], a[216:219], a[88:91]\n" \
"buffer_load_dwordx4 v249, s[16:19], s74 offen lds\n" \
"v_mfma_f32_16x16x32_bf16 a[88:91], a[248:251], a[220:223], a[88:91]\n" \
"s_add_u32 m0, s72, 11264\n" \
"s_add_u32 s74, s71, 25216\n" \
"ds_read_b64_tr_b16 a[212:213], v240 offset:26112\n" \
"v_mfma_f32_16x16x32_bf16 a[92:95], a[252:255], a[220:223], a[92:95]\n" \
"ds_read_b64_tr_b16 a[214:215], v240 offset:28160\n" \
"v_mfma_f32_16x16x32_bf16 a[92:95], a[236:239], a[216:219], a[92:95]\n" \
"s_waitcnt lgkmcnt(8)\n" \
"buffer_load_dwordx4 v250, s[16:19], s74 offen lds\n" \
"v_mfma_f32_16x16x32_bf16 a[32:35], a[224:227], a[192:195], a[32:35]\n" \
"ds_read_b64_tr_b16 a[216:217], v241 offset:17920\n" \
"v_mfma_f32_16x16x32_bf16 a[32:35], a[240:243], a[196:199], a[32:35]\n" \
"v_mfma_f32_16x16x32_bf16 a[36:39], a[244:247], a[196:199], a[36:39]\n" \
"s_add_u32 m0, s72, 12288\n" \
"ds_read_b64_tr_b16 a[218:219], v241 offset:19968\n" \
"v_mfma_f32_16x16x32_bf16 a[36:39], a[228:231], a[192:195], a[36:39]\n" \
"s_add_u32 s74, s94, 33408\n" \
"ds_read_b64_tr_b16 a[220:221], v241 offset:26112\n" \
"v_mfma_f32_16x16x32_bf16 a[96:99], a[232:235], a[192:195], a[96:99]\n" \
"buffer_load_dwordx4 v249, s[16:19], s74 offen lds\n" \
"v_mfma_f32_16x16x32_bf16 a[96:99], a[248:251], a[196:199], a[96:99]\n" \
"ds_read_b64_tr_b16 a[222:223], v241 offset:28160\n" \
"v_mfma_f32_16x16x32_bf16 a[100:103], a[252:255], a[196:199], a[100:103]\n" \
"v_mfma_f32_16x16x32_bf16 a[100:103], a[236:239], a[192:195], a[100:103]\n" \
"s_waitcnt lgkmcnt(8)\n" \
"v_mfma_f32_16x16x32_bf16 a[40:43], a[224:227], a[200:203], a[40:43]\n" \
"s_add_u32 m0, s72, 13312\n" \
"v_mfma_f32_16x16x32_bf16 a[40:43], a[240:243], a[204:207], a[40:43]\n" \
"s_add_u32 s74, s94, 41600\n" \
"v_mfma_f32_16x16x32_bf16 a[44:47], a[244:247], a[204:207], a[44:47]\n" \
"buffer_load_dwordx4 v250, s[16:19], s74 offen lds\n" \
"v_mfma_f32_16x16x32_bf16 a[44:47], a[228:231], a[200:203], a[44:47]\n" \
"v_mfma_f32_16x16x32_bf16 a[104:107], a[232:235], a[200:203], a[104:107]\n" \
"s_waitcnt vmcnt(29) lgkmcnt(0)\n" \
"s_barrier\n" \
"s_add_u32 m0, s70, 16384\n" \
"v_mfma_f32_16x16x32_bf16 a[104:107], a[248:251], a[204:207], a[104:107]\n" \
"s_add_u32 s74, s69, 2621440\n" \
"ds_read_b64_tr_b16 a[192:193], v240 offset:32768\n" \
"ds_read_b64_tr_b16 a[194:195], v240 offset:34816\n" \
"v_mfma_f32_16x16x32_bf16 a[108:111], a[252:255], a[204:207], a[108:111]\n" \
"buffer_load_dwordx4 v246, s[20:23], s74 offen lds\n" \
"v_mfma_f32_16x16x32_bf16 a[108:111], a[236:239], a[200:203], a[108:111]\n" \
"s_add_u32 m0, s72, 14336\n" \
"ds_read_b64_tr_b16 a[196:197], v240 offset:40960\n" \
"ds_read_b64_tr_b16 a[198:199], v240 offset:43008\n" \
"v_mfma_f32_16x16x32_bf16 a[48:51], a[224:227], a[208:211], a[48:51]\n" \
"s_add_u32 s74, s94, 49792\n" \
"ds_read_b64_tr_b16 a[200:201], v241 offset:32768\n" \
"ds_read_b128 v[160:163], v254\n" \
"v_mfma_f32_16x16x32_bf16 a[48:51], a[240:243], a[212:215], a[48:51]\n" \
"s_lshl2_add_u32 s85, s65, %[wid]\n" \
"buffer_load_dwordx4 v249, s[16:19], s74 offen lds\n" \
"v_mfma_f32_16x16x32_bf16 a[52:55], a[244:247], a[212:215], a[52:55]\n" \
"s_lshl_b32 s85, s85, 15\n" \
"ds_read_b64_tr_b16 a[202:203], v241 offset:34816\n" \
"ds_read_b128 v[164:167], v254 offset:512\n" \
"v_mfma_f32_16x16x32_bf16 a[52:55], a[228:231], a[208:211], a[52:55]\n" \
"s_waitcnt vmcnt(13)\n" \
"s_lshl_b32 s86, s64, 10\n" \
"v_add_f32 v168, v168, v172\n" \
"v_mfma_f32_16x16x32_bf16 a[112:115], a[232:235], a[208:211], a[112:115]\n" \
"s_add_u32 m0, s72, 15360\n" \
"ds_read_b64_tr_b16 a[204:205], v241 offset:40960\n" \
"v_add_f32 v176, v176, v180\n" \
"v_mfma_f32_16x16x32_bf16 a[112:115], a[248:251], a[212:215], a[112:115]\n" \
"s_add_u32 s74, s94, 57984\n" \
"v_add_f32 v169, v169, v173\n" \
"v_add_f32 v177, v177, v181\n" \
"v_mfma_f32_16x16x32_bf16 a[116:119], a[252:255], a[212:215], a[116:119]\n" \
"s_add_u32 s85, s85, s86\n" \
"buffer_load_dwordx4 v250, s[16:19], s74 offen lds\n" \
"v_mfma_f32_16x16x32_bf16 a[116:119], a[236:239], a[208:211], a[116:119]\n" \
"buffer_load_dword v245, v211, s[28:31], s85 offen offset:256\n" \
"v_add_f32 v170, v170, v174\n" \
"v_mfma_f32_16x16x32_bf16 a[56:59], a[224:227], a[216:219], a[56:59]\n" \
"s_getpc_b64 s[96:97]\n" \
".Lpf1_%=:\n" \
"s_add_u32 s96, s96, .Lat_b_%=-.Lpf1_%=\n" \
"s_addc_u32 s97, s97, 0\n" \
"v_mfma_f32_16x16x32_bf16 a[56:59], a[240:243], a[220:223], a[56:59]\n" \
"s_and_b32 s97, s97, 0xffff\n" \
"ds_read_b64_tr_b16 a[206:207], v241 offset:43008\n" \
"ds_read_b64_tr_b16 a[208:209], v240 offset:33280\n" \
"v_mfma_f32_16x16x32_bf16 a[60:63], a[244:247], a[220:223], a[60:63]\n" \
"s_add_u32 m0, s70, 17408\n" \
"ds_read_b64_tr_b16 a[210:211], v240 offset:35328\n" \
"v_add_f32 v178, v178, v182\n" \
"v_mfma_f32_16x16x32_bf16 a[60:63], a[228:231], a[216:219], a[60:63]\n" \
"s_add_u32 s74, s69, 2621568\n" \
"v_add_f32 v171, v171, v175\n" \
"v_add_f32 v179, v179, v183\n" \
"v_mfma_f32_16x16x32_bf16 a[120:123], a[232:235], a[216:219], a[120:123]\n" \
"s_mov_b32 s98, .Lat_code_e-.Lat_b_%=\n" \
"buffer_load_dwordx4 v246, s[20:23], s74 offen lds\n" \
"v_mfma_f32_16x16x32_bf16 a[120:123], a[248:251], a[220:223], a[120:123]\n" \
"s_lshl2_add_u32 s85, s65, %[wid]\n" \
"ds_read_b64_tr_b16 a[212:213], v240 offset:41472\n" \
"v_add_f32 v168, v168, v176\n" \
"v_mfma_f32_16x16x32_bf16 a[124:127], a[252:255], a[220:223], a[124:127]\n" \
"s_and_b32 s85, s85, 31\n" \
"ds_read_b64_tr_b16 a[214:215], v240 offset:43520\n" \
"v_add_f32 v169, v169, v177\n" \
"v_mfma_f32_16x16x32_bf16 a[124:127], a[236:239], a[216:219], a[124:127]\n" \
"s_waitcnt lgkmcnt(10)\n" \
"v_add_f32 v170, v170, v178\n" \
"v_add_f32 v171, v171, v179\n" \
"v_mfma_f32_16x16x32_bf16 v[0:3], a[192:195], a[128:131], v[0:3]\n" \
"s_lshl_b32 s85, s85, 11\n" \
"buffer_load_dword v245, v209, s[96:99], s85 offen\n" \
"v_cvt_pk_bf16_f32 v172, v168, v169\n" \
"v_mfma_f32_16x16x32_bf16 v[0:3], a[196:199], a[144:147], v[0:3]\n" \
"ds_read_b64_tr_b16 a[216:217], v241 offset:33280\n" \
"v_cvt_pk_bf16_f32 v173, v170, v171\n" \
"v_mfma_f32_16x16x32_bf16 v[32:35], a[196:199], a[148:151], v[32:35]\n" \
"ds_read_b64_tr_b16 a[218:219], v241 offset:35328\n" \
"v_lshlrev_b32 v168, 16, v172\n" \
"v_mfma_f32_16x16x32_bf16 v[32:35], a[192:195], a[132:135], v[32:35]\n" \
"v_and_b32 v169, 0xffff0000, v172\n" \
"v_lshlrev_b32 v170, 16, v173\n" \
"v_mfma_f32_16x16x32_bf16 v[64:67], a[192:195], a[136:139], v[64:67]\n" \
"ds_read_b64_tr_b16 a[220:221], v241 offset:41472\n" \
"v_and_b32 v171, 0xffff0000, v173\n" \
"v_mfma_f32_16x16x32_bf16 v[64:67], a[196:199], a[152:155], v[64:67]\n" \
"v_add_f32 v168, s83, v168\n" \
"v_add_f32 v169, s83, v169\n" \
"v_mfma_f32_16x16x32_bf16 v[96:99], a[196:199], a[156:159], v[96:99]\n" \
"ds_read_b64_tr_b16 a[222:223], v241 offset:43520\n" \
"v_add_f32 v170, s83, v170\n" \
"v_mfma_f32_16x16x32_bf16 v[96:99], a[192:195], a[140:143], v[96:99]\n" \
"s_waitcnt lgkmcnt(8)\n" \
"v_add_f32 v171, s83, v171\n" \
"v_cvt_pk_bf16_f32 v172, v168, v169\n" \
"v_mfma_f32_16x16x32_bf16 v[4:7], a[200:203], a[128:131], v[4:7]\n" \
"s_add_u32 m0, s70, 24576\n" \
"ds_read_b64_tr_b16 a[192:193], v240 offset:33792\n" \
"v_cvt_pk_bf16_f32 v173, v170, v171\n" \
"v_mfma_f32_16x16x32_bf16 v[4:7], a[204:207], a[144:147], v[4:7]\n" \
"s_add_u32 s74, s69, 2883584\n" \
"v_lshlrev_b32 v168, 16, v172\n" \
"v_and_b32 v169, 0xffff0000, v172\n" \
"v_mfma_f32_16x16x32_bf16 v[36:39], a[204:207], a[148:151], v[36:39]\n" \
"buffer_load_dwordx4 v246, s[20:23], s74 offen lds\n" \
"v_mfma_f32_16x16x32_bf16 v[36:39], a[200:203], a[132:135], v[36:39]\n" \
"ds_read_b64_tr_b16 a[194:195], v240 offset:35840\n" \
"v_lshlrev_b32 v170, 16, v173\n" \
"v_mfma_f32_16x16x32_bf16 v[68:71], a[200:203], a[136:139], v[68:71]\n" \
"ds_read_b64_tr_b16 a[196:197], v240 offset:41984\n" \
"v_and_b32 v171, 0xffff0000, v173\n" \
"v_mfma_f32_16x16x32_bf16 v[68:71], a[204:207], a[152:155], v[68:71]\n" \
"s_waitcnt vmcnt(17)\n" \
"ds_write_b128 v236, v[168:171]\n" \
"v_add_f32_dpp v200, v200, v200 quad_perm:[1,0,3,2] row_mask:0xf bank_mask:0xf\n" \
"v_mfma_f32_16x16x32_bf16 v[100:103], a[204:207], a[156:159], v[100:103]\n" \
"ds_read_b64_tr_b16 a[198:199], v240 offset:44032\n" \
"v_lshrrev_b32 v152, 5, v251\n" \
"v_mfma_f32_16x16x32_bf16 v[100:103], a[200:203], a[140:143], v[100:103]\n" \
"s_waitcnt lgkmcnt(9)\n" \
"v_add_f32_dpp v200, v200, v200 quad_perm:[2,3,0,1] row_mask:0xf bank_mask:0xf\n" \
"v_cvt_pk_bf16_f32 v200, v200, v200\n" \
"v_mfma_f32_16x16x32_bf16 v[8:11], a[208:211], a[128:131], v[8:11]\n" \
"s_lshl3_add_u32 s79, s67, %[wid]\n" \
"ds_read_b64_tr_b16 a[200:201], v241 offset:33792\n" \
"ds_read_b128 a[160:163], v242 offset:0\n" \
"v_mfma_f32_16x16x32_bf16 v[8:11], a[212:215], a[144:147], v[8:11]\n" \
"s_lshl_b32 s79, s79, 14\n" \
"v_lshlrev_b32 v200, 16, v200\n" \
"v_and_b32 v252, 0x23, v251\n" \
"v_mfma_f32_16x16x32_bf16 v[40:43], a[212:215], a[148:151], v[40:43]\n" \
"s_add_u32 s79, s79, 1048576\n" \
"ds_read_b64_tr_b16 a[202:203], v241 offset:35840\n" \
"ds_read_b128 a[164:167], v242 offset:2048\n" \
"v_mfma_f32_16x16x32_bf16 v[40:43], a[208:211], a[132:135], v[40:43]\n" \
"s_lshl_b32 s90, %[wid], 15\n" \
"v_cmp_eq_u32 vcc, 0, v252\n" \
"v_add_f32 v200, s93, v200\n" \
"v_mfma_f32_16x16x32_bf16 v[72:75], a[208:211], a[136:139], v[72:75]\n" \
"s_sub_u32 s80, s79, s90\n" \
"ds_read_b64_tr_b16 a[204:205], v241 offset:41984\n" \
"ds_read_b128 a[168:171], v242 offset:4096\n" \
"v_mfma_f32_16x16x32_bf16 v[72:75], a[212:215], a[152:155], v[72:75]\n" \
"s_add_u32 s80, s80, 114688\n" \
"v_cvt_pk_bf16_f32 v200, v200, v200\n" \
"v_bfe_u32 v201, v251, 1, 3\n" \
"v_mfma_f32_16x16x32_bf16 v[104:107], a[212:215], a[156:159], v[104:107]\n" \
"s_add_u32 m0, s70, 25600\n" \
"ds_read_b64_tr_b16 a[206:207], v241 offset:44032\n" \
"ds_read_b128 a[172:175], v242 offset:6144\n" \
"v_mfma_f32_16x16x32_bf16 v[104:107], a[208:211], a[140:143], v[104:107]\n" \
"s_waitcnt lgkmcnt(13)\n" \
"v_and_or_b32 v201, v251, 16, v201\n" \
"v_mov_b32 v252, 1073741824\n" \
"v_mfma_f32_16x16x32_bf16 v[12:15], a[216:219], a[128:131], v[12:15]\n" \
"s_add_u32 s74, s69, 2883712\n" \
"buffer_load_dwordx4 v246, s[20:23], s74 offen lds\n" \
"v_mfma_f32_16x16x32_bf16 v[12:15], a[220:223], a[144:147], v[12:15]\n" \
"s_lshl_b32 s90, s67, 18\n" \
"ds_read_b64_tr_b16 a[208:209], v240 offset:34304\n" \
"v_cndmask_b32 v252, v252, v201, vcc\n" \
"v_mfma_f32_16x16x32_bf16 v[44:47], a[220:223], a[148:151], v[44:47]\n" \
"s_sub_u32 s90, 1966080, s90\n" \
"ds_read_b64_tr_b16 a[210:211], v240 offset:36352\n" \
"v_lshlrev_b32 v152, 10, v152\n" \
"v_mfma_f32_16x16x32_bf16 v[44:47], a[216:219], a[132:135], v[44:47]\n" \
"s_add_u32 s36, s79, s90\n" \
"v_bfe_u32 v255, v251, 3, 2\n" \
"v_lshl_or_b32 v152, v255, 8, v152\n" \
"v_mfma_f32_16x16x32_bf16 v[76:79], a[216:219], a[136:139], v[76:79]\n" \
"s_add_u32 s37, s80, s90\n" \
"ds_read_b64_tr_b16 a[212:213], v240 offset:42496\n" \
"ds_read_b128 a[176:179], v244 offset:0\n" \
"v_mfma_f32_16x16x32_bf16 v[76:79], a[220:223], a[152:155], v[76:79]\n" \
"s_mul_i32 s73, s68, 1179648\n" \
"v_and_b32 v255, 7, v251\n" \
"v_lshl_or_b32 v152, v255, 4, v152\n" \
"v_mfma_f32_16x16x32_bf16 v[108:111], a[220:223], a[156:159], v[108:111]\n" \
"s_add_u32 s73, s73, 3145728\n" \
"ds_read_b64_tr_b16 a[214:215], v240 offset:44544\n" \
"ds_read_b128 a[180:183], v244 offset:2048\n" \
"v_mfma_f32_16x16x32_bf16 v[108:111], a[216:219], a[140:143], v[108:111]\n" \
"s_waitcnt lgkmcnt(14)\n" \
"v_lshlrev_b32 v136, 16, v160\n" \
"v_and_b32 v137, 0xffff0000, v160\n" \
"v_mfma_f32_16x16x32_bf16 v[16:19], a[192:195], a[128:131], v[16:19]\n" \
"ds_read_b64_tr_b16 a[216:217], v241 offset:34304\n" \
"ds_read_b128 a[184:187], v244 offset:4096\n" \
"v_mfma_f32_16x16x32_bf16 v[16:19], a[196:199], a[144:147], v[16:19]\n" \
"v_lshlrev_b32 v138, 16, v161\n" \
"v_and_b32 v139, 0xffff0000, v161\n" \
"v_mfma_f32_16x16x32_bf16 v[48:51], a[196:199], a[148:151], v[48:51]\n" \
"ds_read_b64_tr_b16 a[218:219], v241 offset:36352\n" \
"ds_read_b128 a[188:191], v244 offset:6144\n" \
"v_mfma_f32_16x16x32_bf16 v[48:51], a[192:195], a[132:135], v[48:51]\n" \
"ds_read_b64_tr_b16 a[220:221], v241 offset:42496\n" \
"v_lshlrev_b32 v140, 16, v162\n" \
"v_mfma_f32_16x16x32_bf16 v[80:83], a[192:195], a[136:139], v[80:83]\n" \
"ds_read_b64_tr_b16 a[222:223], v241 offset:44544\n" \
"v_and_b32 v141, 0xffff0000, v162\n" \
"v_mfma_f32_16x16x32_bf16 v[80:83], a[196:199], a[152:155], v[80:83]\n" \
"v_lshlrev_b32 v142, 16, v163\n" \
"v_and_b32 v143, 0xffff0000, v163\n" \
"v_mfma_f32_16x16x32_bf16 v[112:115], a[196:199], a[156:159], v[112:115]\n" \
"v_lshlrev_b32 v144, 16, v164\n" \
"v_and_b32 v145, 0xffff0000, v164\n" \
"v_mfma_f32_16x16x32_bf16 v[112:115], a[192:195], a[140:143], v[112:115]\n" \
"s_waitcnt lgkmcnt(13)\n" \
"v_lshlrev_b32 v146, 16, v165\n" \
"v_and_b32 v147, 0xffff0000, v165\n" \
"v_mfma_f32_16x16x32_bf16 v[20:23], a[200:203], a[128:131], v[20:23]\n" \
"v_lshlrev_b32 v148, 16, v166\n" \
"v_and_b32 v149, 0xffff0000, v166\n" \
"v_mfma_f32_16x16x32_bf16 v[20:23], a[204:207], a[144:147], v[20:23]\n" \
"v_lshlrev_b32 v150, 16, v167\n" \
"v_and_b32 v151, 0xffff0000, v167\n" \
"v_mfma_f32_16x16x32_bf16 v[52:55], a[204:207], a[148:151], v[52:55]\n" \
"v_mfma_f32_16x16x32_bf16 v[52:55], a[200:203], a[132:135], v[52:55]\n" \
"v_mfma_f32_16x16x32_bf16 v[84:87], a[200:203], a[136:139], v[84:87]\n" \
"s_waitcnt lgkmcnt(0)\n" \
"s_barrier\n" \
"s_add_u32 m0, s70, 32768\n" \
"v_mfma_f32_16x16x32_bf16 v[84:87], a[204:207], a[152:155], v[84:87]\n" \
"s_add_u32 s74, s69, 3145728\n" \
"ds_read_b128 v[184:187], v237 offset:2048\n" \
"ds_read_b128 v[188:191], v237 offset:2064\n" \
"v_mfma_f32_16x16x32_bf16 v[116:119], a[204:207], a[156:159], v[116:119]\n" \
"s_lshr_b32 s85, %[bid], 2\n" \
"buffer_load_dwordx4 v246, s[20:23], s74 offen lds\n" \
"v_mfma_f32_16x16x32_bf16 v[116:119], a[200:203], a[140:143], v[116:119]\n" \
"s_add_u32 m0, s72, 0\n" \
"ds_read_b128 v[168:171], v237 offset:0\n" \
"ds_read_b128 v[172:175], v237 offset:16\n" \
"v_mfma_f32_16x16x32_bf16 v[24:27], a[208:211], a[128:131], v[24:27]\n" \
"s_add_u32 s74, s71, 768\n" \
"ds_read_b128 v[192:195], v237 offset:3072\n" \
"ds_read_b128 v[196:199], v237 offset:3088\n" \
"v_mfma_f32_16x16x32_bf16 v[24:27], a[212:215], a[144:147], v[24:27]\n" \
"s_lshl_b32 s85, s85, 6\n" \
"buffer_load_dwordx4 v249, s[16:19], s74 offen lds\n" \
"v_mfma_f32_16x16x32_bf16 v[56:59], a[212:215], a[148:151], v[56:59]\n" \
"s_and_b32 s86, %[bid], 2\n" \
"ds_read_b128 v[176:179], v237 offset:1024\n" \
"ds_read_b128 v[180:183], v237 offset:1040\n" \
"v_mfma_f32_16x16x32_bf16 v[56:59], a[208:211], a[132:135], v[56:59]\n" \
"s_lshl2_add_u32 s85, s86, s85\n" \
"ds_read_b64_tr_b16 a[192:193], v240 offset:49152\n" \
"ds_read_b64_tr_b16 a[194:195], v240 offset:51200\n" \
"v_mfma_f32_16x16x32_bf16 v[88:91], a[208:211], a[136:139], v[88:91]\n" \
"s_add_u32 m0, s72, 1024\n" \
"ds_read_b64_tr_b16 a[196:197], v240 offset:57344\n" \
"ds_read_b64_tr_b16 a[198:199], v240 offset:59392\n" \
"v_mfma_f32_16x16x32_bf16 v[88:91], a[212:215], a[152:155], v[88:91]\n" \
"s_add_u32 s74, s71, 8960\n" \
"ds_read_b64_tr_b16 a[200:201], v241 offset:49152\n" \
"ds_read_b64_tr_b16 a[202:203], v241 offset:51200\n" \
"v_mfma_f32_16x16x32_bf16 v[120:123], a[212:215], a[156:159], v[120:123]\n" \
"s_and_b32 s86, %[bid], 1\n" \
"buffer_load_dwordx4 v250, s[16:19], s74 offen lds\n" \
"v_mfma_f32_16x16x32_bf16 v[120:123], a[208:211], a[140:143], v[120:123]\n" \
"s_lshl_b32 s86, s86, 5\n" \
"ds_read_b64_tr_b16 a[204:205], v241 offset:57344\n" \
"ds_read_b64_tr_b16 a[206:207], v241 offset:59392\n" \
"v_mfma_f32_16x16x32_bf16 v[28:31], a[216:219], a[128:131], v[28:31]\n" \
"s_add_u32 s85, s85, s86\n" \
"s_cmp_eq_u32 %[wid], 0\n" \
"s_cselect_b32 s85, s85, 1073741824\n" \
"v_mfma_f32_16x16x32_bf16 v[28:31], a[220:223], a[144:147], v[28:31]\n" \
"s_mov_b32 s84, 1040356334\n" \
"buffer_store_short v200, v252, s[32:35], s85 offen sc1\n" \
"v_mfma_f32_16x16x32_bf16 v[60:63], a[220:223], a[148:151], v[60:63]\n" \
"s_add_u32 m0, s70, 33792\n" \
"ds_read_b64_tr_b16 a[208:209], v240 offset:49664\n" \
"ds_read_b64_tr_b16 a[210:211], v240 offset:51712\n" \
"v_mfma_f32_16x16x32_bf16 v[60:63], a[216:219], a[132:135], v[60:63]\n" \
"s_waitcnt lgkmcnt(14)\n" \
"s_add_u32 s74, s69, 3145856\n" \
"v_mul_f32 v136, v136, v184\n" \
"v_mfma_f32_16x16x32_bf16 v[92:95], a[216:219], a[136:139], v[92:95]\n" \
"buffer_load_dwordx4 v246, s[20:23], s74 offen lds\n" \
"v_mfma_f32_16x16x32_bf16 v[92:95], a[220:223], a[152:155], v[92:95]\n" \
"s_add_u32 m0, s72, 2048\n" \
"ds_read_b64_tr_b16 a[212:213], v240 offset:57856\n" \
"v_mul_f32 v138, v138, v186\n" \
"v_mfma_f32_16x16x32_bf16 v[124:127], a[220:223], a[156:159], v[124:127]\n" \
"s_add_u32 s74, s71, 17152\n" \
"ds_read_b64_tr_b16 a[214:215], v240 offset:59904\n" \
"v_mul_f32 v137, v137, v185\n" \
"v_mfma_f32_16x16x32_bf16 v[124:127], a[216:219], a[140:143], v[124:127]\n" \
"s_waitcnt lgkmcnt(8)\n" \
"buffer_load_dwordx4 v249, s[16:19], s74 offen lds\n" \
"v_mfma_f32_16x16x32_bf16 a[0:3], a[128:131], a[192:195], a[0:3]\n" \
"s_add_u32 m0, s72, 3072\n" \
"ds_read_b64_tr_b16 a[216:217], v241 offset:49664\n" \
"v_mul_f32 v139, v139, v187\n" \
"v_mfma_f32_16x16x32_bf16 a[0:3], a[144:147], a[196:199], a[0:3]\n" \
"s_add_u32 s74, s71, 25344\n" \
"v_cvt_pk_bf16_f32 v160, v136, v137\n" \
"v_cvt_pk_bf16_f32 v161, v138, v139\n" \
"v_mfma_f32_16x16x32_bf16 a[4:7], a[148:151], a[196:199], a[4:7]\n" \
"buffer_load_dwordx4 v250, s[16:19], s74 offen lds\n" \
"v_mfma_f32_16x16x32_bf16 a[4:7], a[132:135], a[192:195], a[4:7]\n" \
"ds_read_b64_tr_b16 a[218:219], v241 offset:51712\n" \
"v_lshlrev_b32 v136, 16, v160\n" \
"v_mfma_f32_16x16x32_bf16 a[64:67], a[136:139], a[192:195], a[64:67]\n" \
"ds_read_b64_tr_b16 a[220:221], v241 offset:57856\n" \
"v_lshlrev_b32 v138, 16, v161\n" \
"v_mfma_f32_16x16x32_bf16 a[64:67], a[152:155], a[196:199], a[64:67]\n" \
"v_and_b32 v137, 0xffff0000, v160\n" \
"v_and_b32 v139, 0xffff0000, v161\n" \
"v_mfma_f32_16x16x32_bf16 a[68:71], a[156:159], a[196:199], a[68:71]\n" \
"s_add_u32 m0, s72, 4096\n" \
"ds_read_b64_tr_b16 a[222:223], v241 offset:59904\n" \
"v_add_f32 v136, v136, v168\n" \
"v_mfma_f32_16x16x32_bf16 a[68:71], a[140:143], a[192:195], a[68:71]\n" \
"s_waitcnt lgkmcnt(8)\n" \
"v_add_f32 v138, v138, v170\n" \
"v_add_f32 v137, v137, v169\n" \
"v_mfma_f32_16x16x32_bf16 a[8:11], a[128:131], a[200:203], a[8:11]\n" \
"s_add_u32 s74, s94, 33536\n" \
"buffer_load_dwordx4 v249, s[16:19], s74 offen lds\n" \
"v_mfma_f32_16x16x32_bf16 a[8:11], a[144:147], a[204:207], a[8:11]\n" \
"s_add_u32 m0, s70, 40960\n" \
"ds_read_b64_tr_b16 a[192:193], v240 offset:50176\n" \
"v_add_f32 v139, v139, v171\n" \
"v_mfma_f32_16x16x32_bf16 a[12:15], a[148:151], a[204:207], a[12:15]\n" \
"s_add_u32 s74, s69, 3407872\n" \
"ds_read_b64_tr_b16 a[194:195], v240 offset:52224\n" \
"v_cvt_pk_bf16_f32 v160, v136, v137\n" \
"v_mfma_f32_16x16x32_bf16 a[12:15], a[132:135], a[200:203], a[12:15]\n" \
"buffer_load_dwordx4 v246, s[20:23], s74 offen lds\n" \
"v_mfma_f32_16x16x32_bf16 a[72:75], a[136:139], a[200:203], a[72:75]\n" \
"s_add_u32 m0, s72, 5120\n" \
"ds_read_b64_tr_b16 a[196:197], v240 offset:58368\n" \
"v_cvt_pk_bf16_f32 v161, v138, v139\n" \
"v_mfma_f32_16x16x32_bf16 a[72:75], a[152:155], a[204:207], a[72:75]\n" \
"s_add_u32 s74, s94, 41728\n" \
"v_mul_f32 v140, v140, v188\n" \
"v_mul_f32 v142, v142, v190\n" \
"v_mfma_f32_16x16x32_bf16 a[76:79], a[156:159], a[204:207], a[76:79]\n" \
"s_lshr_b32 s85, s78, 4\n" \
"buffer_load_dwordx4 v250, s[16:19], s74 offen lds\n" \
"v_mfma_f32_16x16x32_bf16 a[76:79], a[140:143], a[200:203], a[76:79]\n" \
"s_waitcnt lgkmcnt(7)\n" \
"ds_read_b64_tr_b16 a[198:199], v240 offset:60416\n" \
"v_mul_f32 v141, v141, v189\n" \
"v_mfma_f32_16x16x32_bf16 a[16:19], a[128:131], a[208:211], a[16:19]\n" \
"s_lshl_b32 s85, s85, 14\n" \
"ds_read_b64_tr_b16 a[200:201], v241 offset:50176\n" \
"v_mul_f32 v143, v143, v191\n" \
"v_mfma_f32_16x16x32_bf16 a[16:19], a[144:147], a[212:215], a[16:19]\n" \
"s_and_b32 s86, s78, 8\n" \
"v_cvt_pk_bf16_f32 v162, v140, v141\n" \
"v_cvt_pk_bf16_f32 v163, v142, v143\n" \
"v_mfma_f32_16x16x32_bf16 a[20:23], a[148:151], a[212:215], a[20:23]\n" \
".Lst0_2_%=:\n" \
"s_cbranch_execz .Lst0_6_%=\n" \
"s_add_u32 m0, s72, 6144\n" \
"ds_read_b64_tr_b16 a[202:203], v241 offset:52224\n" \
"v_lshlrev_b32 v140, 16, v162\n" \
"v_mfma_f32_16x16x32_bf16 a[20:23], a[132:135], a[208:211], a[20:23]\n" \
"s_add_u32 s74, s94, 49920\n" \
"v_lshlrev_b32 v142, 16, v163\n" \
"v_and_b32 v141, 0xffff0000, v162\n" \
"v_mfma_f32_16x16x32_bf16 a[80:83], a[136:139], a[208:211], a[80:83]\n" \
"s_lshl4_add_u32 s85, s86, s85\n" \
"buffer_load_dwordx4 v249, s[16:19], s74 offen lds\n" \
"v_mfma_f32_16x16x32_bf16 a[80:83], a[152:155], a[212:215], a[80:83]\n" \
"s_lshl_b32 s86, %[wid], 11\n" \
"ds_read_b64_tr_b16 a[204:205], v241 offset:58368\n" \
"v_and_b32 v143, 0xffff0000, v163\n" \
"v_mfma_f32_16x16x32_bf16 a[84:87], a[156:159], a[212:215], a[84:87]\n" \
"s_add_u32 m0, s70, 41984\n" \
"ds_read_b64_tr_b16 a[206:207], v241 offset:60416\n" \
"v_add_f32 v140, v140, v172\n" \
"v_mfma_f32_16x16x32_bf16 a[84:87], a[140:143], a[208:211], a[84:87]\n" \
"s_waitcnt lgkmcnt(8)\n" \
"v_add_f32 v142, v142, v174\n" \
"v_add_f32 v141, v141, v173\n" \
"v_mfma_f32_16x16x32_bf16 a[24:27], a[128:131], a[216:219], a[24:27]\n" \
"s_add_u32 s74, s69, 3408000\n" \
"buffer_load_dwordx4 v246, s[20:23], s74 offen lds\n" \
"v_mfma_f32_16x16x32_bf16 a[24:27], a[144:147], a[220:223], a[24:27]\n" \
"s_add_u32 m0, s72, 7168\n" \
"ds_read_b64_tr_b16 a[208:209], v240 offset:50688\n" \
"v_add_f32 v143, v143, v175\n" \
"v_mfma_f32_16x16x32_bf16 a[28:31], a[148:151], a[220:223], a[28:31]\n" \
"s_add_u32 s74, s94, 58112\n" \
"ds_read_b64_tr_b16 a[210:211], v240 offset:52736\n" \
"v_cvt_pk_bf16_f32 v162, v140, v141\n" \
"v_mfma_f32_16x16x32_bf16 a[28:31], a[132:135], a[216:219], a[28:31]\n" \
"s_add_u32 s85, s85, s86\n" \
"buffer_load_dwordx4 v250, s[16:19], s74 offen lds\n" \
"v_mfma_f32_16x16x32_bf16 a[88:91], a[136:139], a[216:219], a[88:91]\n" \
"s_add_u32 s85, s85, 1048576\n" \
"ds_read_b64_tr_b16 a[212:213], v240 offset:58880\n" \
"v_cvt_pk_bf16_f32 v163, v142, v143\n" \
"v_mfma_f32_16x16x32_bf16 a[88:91], a[152:155], a[220:223], a[88:91]\n" \
"v_mul_f32 v144, v144, v192\n" \
"v_mul_f32 v146, v146, v194\n" \
"v_mfma_f32_16x16x32_bf16 a[92:95], a[156:159], a[220:223], a[92:95]\n" \
"buffer_store_dwordx4 v[160:163], v152, s[32:35], s85 offen sc1\n" \
"v_mfma_f32_16x16x32_bf16 a[92:95], a[140:143], a[216:219], a[92:95]\n" \
"s_waitcnt lgkmcnt(7)\n" \
"ds_read_b64_tr_b16 a[214:215], v240 offset:60928\n" \
"v_mul_f32 v145, v145, v193\n" \
"v_mfma_f32_16x16x32_bf16 a[32:35], a[128:131], a[192:195], a[32:35]\n" \
"s_add_u32 s85, s85, 8192\n" \
"ds_read_b64_tr_b16 a[216:217], v241 offset:50688\n" \
"v_mul_f32 v147, v147, v195\n" \
"v_mfma_f32_16x16x32_bf16 a[32:35], a[144:147], a[196:199], a[32:35]\n" \
"s_waitcnt vmcnt(19)\n" \
"ds_read_b128 a[224:227], v242 offset:8192\n" \
"v_cvt_pk_bf16_f32 v164, v144, v145\n" \
"v_mfma_f32_16x16x32_bf16 a[36:39], a[148:151], a[196:199], a[36:39]\n" \
"ds_read_b64_tr_b16 a[218:219], v241 offset:52736\n" \
"ds_read_b128 a[228:231], v242 offset:10240\n" \
"v_mfma_f32_16x16x32_bf16 a[36:39], a[132:135], a[192:195], a[36:39]\n" \
"ds_read_b64_tr_b16 a[220:221], v241 offset:58880\n" \
"v_cvt_pk_bf16_f32 v165, v146, v147\n" \
"v_mfma_f32_16x16x32_bf16 a[96:99], a[136:139], a[192:195], a[96:99]\n" \
"ds_read_b64_tr_b16 a[222:223], v241 offset:60928\n" \
"ds_read_b128 a[232:235], v242 offset:12288\n" \
"v_mfma_f32_16x16x32_bf16 a[96:99], a[152:155], a[196:199], a[96:99]\n" \
"v_lshlrev_b32 v144, 16, v164\n" \
"v_lshlrev_b32 v146, 16, v165\n" \
"v_mfma_f32_16x16x32_bf16 a[100:103], a[156:159], a[196:199], a[100:103]\n" \
"ds_read_b128 a[236:239], v242 offset:14336\n" \
"v_and_b32 v145, 0xffff0000, v164\n" \
"v_mfma_f32_16x16x32_bf16 a[100:103], a[140:143], a[192:195], a[100:103]\n" \
"s_waitcnt lgkmcnt(12)\n" \
"v_and_b32 v147, 0xffff0000, v165\n" \
"v_add_f32 v144, v144, v176\n" \
"v_mfma_f32_16x16x32_bf16 a[40:43], a[128:131], a[200:203], a[40:43]\n" \
"v_add_f32 v146, v146, v178\n" \
"v_add_f32 v145, v145, v177\n" \
"v_mfma_f32_16x16x32_bf16 a[40:43], a[144:147], a[204:207], a[40:43]\n" \
"v_add_f32 v147, v147, v179\n" \
"v_cvt_pk_bf16_f32 v164, v144, v145\n" \
"v_mfma_f32_16x16x32_bf16 a[44:47], a[148:151], a[204:207], a[44:47]\n" \
"v_cvt_pk_bf16_f32 v165, v146, v147\n" \
"v_mul_f32 v148, v148, v196\n" \
"v_mfma_f32_16x16x32_bf16 a[44:47], a[132:135], a[200:203], a[44:47]\n" \
"v_mul_f32 v150, v150, v198\n" \
"v_mul_f32 v149, v149, v197\n" \
"v_mfma_f32_16x16x32_bf16 a[104:107], a[136:139], a[200:203], a[104:107]\n" \
"s_waitcnt lgkmcnt(2)\n" \
"s_barrier\n" \
"ds_read_b128 a[240:243], v244 offset:8192\n" \
"v_mfma_f32_16x16x32_bf16 a[104:107], a[152:155], a[204:207], a[104:107]\n" \
"s_add_u32 m0, s70, 49152\n" \
"ds_read_b64_tr_b16 a[192:193], v240 offset:0\n" \
"ds_read_b64_tr_b16 a[194:195], v240 offset:2048\n" \
"v_mfma_f32_16x16x32_bf16 a[108:111], a[156:159], a[204:207], a[108:111]\n" \
"s_add_u32 s74, s69, 3670016\n" \
"buffer_load_dwordx4 v246, s[20:23], s74 offen lds\n" \
"v_mfma_f32_16x16x32_bf16 a[108:111], a[140:143], a[200:203], a[108:111]\n" \
"ds_read_b64_tr_b16 a[196:197], v240 offset:8192\n" \
"ds_read_b64_tr_b16 a[198:199], v240 offset:10240\n" \
"v_mfma_f32_16x16x32_bf16 a[48:51], a[128:131], a[208:211], a[48:51]\n" \
"ds_read_b64_tr_b16 a[200:201], v241 offset:0\n" \
"ds_read_b128 a[244:247], v244 offset:10240\n" \
"v_mfma_f32_16x16x32_bf16 a[48:51], a[144:147], a[212:215], a[48:51]\n" \
"ds_read_b128 a[248:251], v244 offset:12288\n" \
"v_mul_f32 v151, v151, v199\n" \
"v_mfma_f32_16x16x32_bf16 a[52:55], a[148:151], a[212:215], a[52:55]\n" \
"ds_read_b64_tr_b16 a[202:203], v241 offset:2048\n" \
"ds_read_b128 a[252:255], v244 offset:14336\n" \
"v_mfma_f32_16x16x32_bf16 a[52:55], a[132:135], a[208:211], a[52:55]\n" \
"v_cvt_pk_bf16_f32 v166, v148, v149\n" \
"v_cvt_pk_bf16_f32 v167, v150, v151\n" \
"v_mfma_f32_16x16x32_bf16 a[112:115], a[136:139], a[208:211], a[112:115]\n" \
"ds_read_b64_tr_b16 a[204:205], v241 offset:8192\n" \
"v_lshlrev_b32 v148, 16, v166\n" \
"v_mfma_f32_16x16x32_bf16 a[112:115], a[152:155], a[212:215], a[112:115]\n" \
"v_lshlrev_b32 v150, 16, v167\n" \
"v_and_b32 v149, 0xffff0000, v166\n" \
"v_mfma_f32_16x16x32_bf16 a[116:119], a[156:159], a[212:215], a[116:119]\n" \
"ds_read_b64_tr_b16 a[206:207], v241 offset:10240\n" \
"v_and_b32 v151, 0xffff0000, v167\n" \
"v_mfma_f32_16x16x32_bf16 a[116:119], a[140:143], a[208:211], a[116:119]\n" \
"v_add_f32 v148, v148, v180\n" \
"v_add_f32 v150, v150, v182\n" \
"v_mfma_f32_16x16x32_bf16 a[56:59], a[128:131], a[216:219], a[56:59]\n" \
"ds_read_b64_tr_b16 a[208:209], v240 offset:512\n" \
"v_add_f32 v149, v149, v181\n" \
"v_mfma_f32_16x16x32_bf16 a[56:59], a[144:147], a[220:223], a[56:59]\n" \
"v_add_f32 v151, v151, v183\n" \
"v_cvt_pk_bf16_f32 v166, v148, v149\n" \
"v_mfma_f32_16x16x32_bf16 a[60:63], a[148:151], a[220:223], a[60:63]\n" \
"s_add_u32 m0, s70, 50176\n" \
"ds_read_b64_tr_b16 a[210:211], v240 offset:2560\n" \
"v_cvt_pk_bf16_f32 v167, v150, v151\n" \
"v_mfma_f32_16x16x32_bf16 a[60:63], a[132:135], a[216:219], a[60:63]\n" \
"s_add_u32 s74, s69, 3670144\n" \
"buffer_store_dwordx4 v[164:167], v152, s[32:35], s85 offen sc1\n" \
"v_mfma_f32_16x16x32_bf16 a[120:123], a[136:139], a[216:219], a[120:123]\n" \
"ds_read_b64_tr_b16 a[212:213], v240 offset:8704\n" \
"v_mfma_f32_16x16x32_bf16 a[120:123], a[152:155], a[220:223], a[120:123]\n" \
"v_mfma_f32_16x16x32_bf16 a[124:127], a[156:159], a[220:223], a[124:127]\n" \
"buffer_load_dwordx4 v246, s[20:23], s74 offen lds\n" \
"v_mfma_f32_16x16x32_bf16 a[124:127], a[140:143], a[216:219], a[124:127]\n" \
"s_waitcnt lgkmcnt(10)\n" \
"s_add_u32 m0, s72, 8192\n" \
"ds_read_b64_tr_b16 a[214:215], v240 offset:10752\n" \
  \
"v_mfma_f32_16x16x32_bf16 v[0:3], a[192:195], a[160:163], v[0:3]\n" \
"s_add_u32 s74, s71, 896\n" \
"ds_read_b64_tr_b16 a[216:217], v241 offset:512\n" \
"v_mfma_f32_16x16x32_bf16 v[0:3], a[196:199], a[176:179], v[0:3]\n" \
"s_waitcnt lgkmcnt(7)\n" \
"buffer_load_dwordx4 v249, s[16:19], s74 offen lds\n" \
"v_mfma_f32_16x16x32_bf16 v[32:35], a[196:199], a[180:183], v[32:35]\n" \
"ds_read_b64_tr_b16 a[218:219], v241 offset:2560\n" \
"v_mfma_f32_16x16x32_bf16 v[32:35], a[192:195], a[164:167], v[32:35]\n" \
"v_mfma_f32_16x16x32_bf16 v[64:67], a[192:195], a[168:171], v[64:67]\n" \
"s_add_u32 m0, s72, 9216\n" \
"ds_read_b64_tr_b16 a[220:221], v241 offset:8704\n" \
"v_mfma_f32_16x16x32_bf16 v[64:67], a[196:199], a[184:187], v[64:67]\n" \
"s_add_u32 s74, s71, 9088\n" \
"v_mfma_f32_16x16x32_bf16 v[96:99], a[196:199], a[188:191], v[96:99]\n" \
"buffer_load_dwordx4 v250, s[16:19], s74 offen lds\n" \
"v_mfma_f32_16x16x32_bf16 v[96:99], a[192:195], a[172:175], v[96:99]\n" \
"s_waitcnt lgkmcnt(7)\n" \
"ds_read_b64_tr_b16 a[222:223], v241 offset:10752\n" \
"v_mfma_f32_16x16x32_bf16 v[4:7], a[200:203], a[160:163], v[4:7]\n" \
"s_add_u32 m0, s70, 57344\n" \
"ds_read_b64_tr_b16 a[192:193], v240 offset:1024\n" \
"v_mfma_f32_16x16x32_bf16 v[4:7], a[204:207], a[176:179], v[4:7]\n" \
"s_add_u32 s74, s69, 3932160\n" \
"v_mfma_f32_16x16x32_bf16 v[36:39], a[204:207], a[180:183], v[36:39]\n" \
"buffer_load_dwordx4 v246, s[20:23], s74 offen lds\n" \
"v_mfma_f32_16x16x32_bf16 v[36:39], a[200:203], a[164:167], v[36:39]\n" \
"s_add_u32 m0, s72, 10240\n" \
"s_add_u32 s74, s71, 17280\n" \
"ds_read_b64_tr_b16 a[194:195], v240 offset:3072\n" \
"v_mfma_f32_16x16x32_bf16 v[68:71], a[200:203], a[168:171], v[68:71]\n" \
"ds_read_b64_tr_b16 a[196:197], v240 offset:9216\n" \
"v_mfma_f32_16x16x32_bf16 v[68:71], a[204:207], a[184:187], v[68:71]\n" \
"buffer_load_dwordx4 v249, s[16:19], s74 offen lds\n" \
"v_mfma_f32_16x16x32_bf16 v[100:103], a[204:207], a[188:191], v[100:103]\n" \
"ds_read_b64_tr_b16 a[198:199], v240 offset:11264\n" \
"v_mfma_f32_16x16x32_bf16 v[100:103], a[200:203], a[172:175], v[100:103]\n" \
"s_waitcnt lgkmcnt(8)\n" \
"v_mfma_f32_16x16x32_bf16 v[8:11], a[208:211], a[160:163], v[8:11]\n" \
"s_add_u32 m0, s72, 11264\n" \
"ds_read_b64_tr_b16 a[200:201], v241 offset:1024\n" \
"v_mfma_f32_16x16x32_bf16 v[8:11], a[212:215], a[176:179], v[8:11]\n" \
"s_add_u32 s74, s71, 25472\n" \
"v_mfma_f32_16x16x32_bf16 v[40:43], a[212:215], a[180:183], v[40:43]\n" \
"buffer_load_dwordx4 v250, s[16:19], s74 offen lds\n" \
"v_mfma_f32_16x16x32_bf16 v[40:43], a[208:211], a[164:167], v[40:43]\n" \
"ds_read_b64_tr_b16 a[202:203], v241 offset:3072\n" \
"v_mfma_f32_16x16x32_bf16 v[72:75], a[208:211], a[168:171], v[72:75]\n" \
"ds_read_b64_tr_b16 a[204:205], v241 offset:9216\n" \
"v_mfma_f32_16x16x32_bf16 v[72:75], a[212:215], a[184:187], v[72:75]\n" \
"v_mfma_f32_16x16x32_bf16 v[104:107], a[212:215], a[188:191], v[104:107]\n" \
"s_add_u32 m0, s70, 58368\n" \
"ds_read_b64_tr_b16 a[206:207], v241 offset:11264\n" \
"v_mfma_f32_16x16x32_bf16 v[104:107], a[208:211], a[172:175], v[104:107]\n" \
"s_waitcnt lgkmcnt(8)\n" \
"s_add_u32 s74, s69, 3932288\n" \
"v_mfma_f32_16x16x32_bf16 v[12:15], a[216:219], a[160:163], v[12:15]\n" \
"buffer_load_dwordx4 v246, s[20:23], s74 offen lds\n" \
"v_mfma_f32_16x16x32_bf16 v[12:15], a[220:223], a[176:179], v[12:15]\n" \
"s_add_u32 m0, s72, 12288\n" \
"s_add_u32 s74, s94, 33664\n" \
"ds_read_b64_tr_b16 a[208:209], v240 offset:1536\n" \
"v_mfma_f32_16x16x32_bf16 v[44:47], a[220:223], a[180:183], v[44:47]\n" \
"ds_read_b64_tr_b16 a[210:211], v240 offset:3584\n" \
"v_mfma_f32_16x16x32_bf16 v[44:47], a[216:219], a[164:167], v[44:47]\n" \
"buffer_load_dwordx4 v249, s[16:19], s74 offen lds\n" \
"v_mfma_f32_16x16x32_bf16 v[76:79], a[216:219], a[168:171], v[76:79]\n" \
"s_add_u32 m0, s72, 13312\n" \
"ds_read_b64_tr_b16 a[212:213], v240 offset:9728\n" \
"v_mfma_f32_16x16x32_bf16 v[76:79], a[220:223], a[184:187], v[76:79]\n" \
"s_add_u32 s74, s94, 41856\n" \
"v_mfma_f32_16x16x32_bf16 v[108:111], a[220:223], a[188:191], v[108:111]\n" \
"buffer_load_dwordx4 v250, s[16:19], s74 offen lds\n" \
"v_mfma_f32_16x16x32_bf16 v[108:111], a[216:219], a[172:175], v[108:111]\n" \
"s_waitcnt lgkmcnt(7)\n" \
"ds_read_b64_tr_b16 a[214:215], v240 offset:11776\n" \
"v_mfma_f32_16x16x32_bf16 v[16:19], a[192:195], a[160:163], v[16:19]\n" \
"ds_read_b64_tr_b16 a[216:217], v241 offset:1536\n" \
"v_mfma_f32_16x16x32_bf16 v[16:19], a[196:199], a[176:179], v[16:19]\n" \
"v_mfma_f32_16x16x32_bf16 v[48:51], a[196:199], a[180:183], v[48:51]\n" \
"s_add_u32 m0, s72, 14336\n" \
"ds_read_b64_tr_b16 a[218:219], v241 offset:3584\n" \
"v_mfma_f32_16x16x32_bf16 v[48:51], a[192:195], a[164:167], v[48:51]\n" \
"s_add_u32 s74, s94, 50048\n" \
"ds_read_b64_tr_b16 a[220:221], v241 offset:9728\n" \
"v_mfma_f32_16x16x32_bf16 v[80:83], a[192:195], a[168:171], v[80:83]\n" \
"buffer_load_dwordx4 v249, s[16:19], s74 offen lds\n" \
"v_mfma_f32_16x16x32_bf16 v[80:83], a[196:199], a[184:187], v[80:83]\n" \
"ds_read_b64_tr_b16 a[222:223], v241 offset:11776\n" \
"v_mfma_f32_16x16x32_bf16 v[112:115], a[196:199], a[188:191], v[112:115]\n" \
"v_mfma_f32_16x16x32_bf16 v[112:115], a[192:195], a[172:175], v[112:115]\n" \
"s_waitcnt lgkmcnt(8)\n" \
"v_mfma_f32_16x16x32_bf16 v[20:23], a[200:203], a[160:163], v[20:23]\n" \
"s_add_u32 m0, s72, 15360\n" \
"v_mfma_f32_16x16x32_bf16 v[20:23], a[204:207], a[176:179], v[20:23]\n" \
"s_add_u32 s74, s94, 58240\n" \
"v_mfma_f32_16x16x32_bf16 v[52:55], a[204:207], a[180:183], v[52:55]\n" \
"buffer_load_dwordx4 v250, s[16:19], s74 offen lds\n" \
"v_mfma_f32_16x16x32_bf16 v[52:55], a[200:203], a[164:167], v[52:55]\n" \
"v_mfma_f32_16x16x32_bf16 v[84:87], a[200:203], a[168:171], v[84:87]\n" \
"s_waitcnt vmcnt(27) lgkmcnt(0)\n" \
"s_barrier\n" \
"s_add_u32 m0, s70, 0\n" \
"v_mfma_f32_16x16x32_bf16 v[84:87], a[204:207], a[184:187], v[84:87]\n" \
"s_add_u32 s74, s69, 3670016\n" \
"ds_read_b64_tr_b16 a[192:193], v240 offset:16384\n" \
"ds_read_b64_tr_b16 a[194:195], v240 offset:18432\n" \
"v_mfma_f32_16x16x32_bf16 v[116:119], a[204:207], a[188:191], v[116:119]\n" \
"buffer_load_dwordx4 v246, s[24:27], s74 offen lds\n" \
"v_mfma_f32_16x16x32_bf16 v[116:119], a[200:203], a[172:175], v[116:119]\n" \
"ds_read_b64_tr_b16 a[196:197], v240 offset:24576\n" \
"ds_read_b64_tr_b16 a[198:199], v240 offset:26624\n" \
"v_mfma_f32_16x16x32_bf16 v[24:27], a[208:211], a[160:163], v[24:27]\n" \
"s_waitcnt vmcnt(15)\n" \
"ds_read_b64_tr_b16 a[200:201], v241 offset:16384\n" \
"ds_read_b128 a[128:131], v242 offset:0\n" \
"v_mfma_f32_16x16x32_bf16 v[24:27], a[212:215], a[176:179], v[24:27]\n" \
"v_mfma_f32_16x16x32_bf16 v[56:59], a[212:215], a[180:183], v[56:59]\n" \
"ds_read_b64_tr_b16 a[202:203], v241 offset:18432\n" \
"ds_read_b128 a[132:135], v242 offset:2048\n" \
"v_mfma_f32_16x16x32_bf16 v[56:59], a[208:211], a[164:167], v[56:59]\n" \
"v_mfma_f32_16x16x32_bf16 v[88:91], a[208:211], a[168:171], v[88:91]\n" \
"ds_read_b64_tr_b16 a[204:205], v241 offset:24576\n" \
"ds_read_b128 a[136:139], v242 offset:4096\n" \
"v_mfma_f32_16x16x32_bf16 v[88:91], a[212:215], a[184:187], v[88:91]\n" \
"v_mfma_f32_16x16x32_bf16 v[120:123], a[212:215], a[188:191], v[120:123]\n" \
"ds_read_b64_tr_b16 a[206:207], v241 offset:26624\n" \
"ds_read_b128 a[140:143], v242 offset:6144\n" \
"v_mfma_f32_16x16x32_bf16 v[120:123], a[208:211], a[172:175], v[120:123]\n" \
"v_mfma_f32_16x16x32_bf16 v[28:31], a[216:219], a[160:163], v[28:31]\n" \
"ds_read_b64_tr_b16 a[208:209], v240 offset:16896\n" \
"v_mfma_f32_16x16x32_bf16 v[28:31], a[220:223], a[176:179], v[28:31]\n" \
"v_mfma_f32_16x16x32_bf16 v[60:63], a[220:223], a[180:183], v[60:63]\n" \
"s_add_u32 m0, s70, 1024\n" \
"ds_read_b64_tr_b16 a[210:211], v240 offset:18944\n" \
"v_mfma_f32_16x16x32_bf16 v[60:63], a[216:219], a[164:167], v[60:63]\n" \
"s_add_u32 s74, s69, 3670144\n" \
"v_mfma_f32_16x16x32_bf16 v[92:95], a[216:219], a[168:171], v[92:95]\n" \
"buffer_load_dwordx4 v246, s[24:27], s74 offen lds\n" \
"v_mfma_f32_16x16x32_bf16 v[92:95], a[220:223], a[184:187], v[92:95]\n" \
"ds_read_b64_tr_b16 a[212:213], v240 offset:25088\n" \
"ds_read_b128 a[144:147], v244 offset:0\n" \
"v_mfma_f32_16x16x32_bf16 v[124:127], a[220:223], a[188:191], v[124:127]\n" \
"ds_read_b64_tr_b16 a[214:215], v240 offset:27136\n" \
"ds_read_b128 a[148:151], v244 offset:2048\n" \
"v_mfma_f32_16x16x32_bf16 v[124:127], a[216:219], a[172:175], v[124:127]\n" \
"s_waitcnt lgkmcnt(14)\n" \
"v_mfma_f32_16x16x32_bf16 v[0:3], a[192:195], a[224:227], v[0:3]\n" \
"ds_read_b64_tr_b16 a[216:217], v241 offset:16896\n" \
"ds_read_b128 a[152:155], v244 offset:4096\n" \
"v_mfma_f32_16x16x32_bf16 v[0:3], a[196:199], a[240:243], v[0:3]\n" \
"v_mfma_f32_16x16x32_bf16 v[32:35], a[196:199], a[244:247], v[32:35]\n" \
"ds_read_b64_tr_b16 a[218:219], v241 offset:18944\n" \
"ds_read_b128 a[156:159], v244 offset:6144\n" \
"v_mfma_f32_16x16x32_bf16 v[32:35], a[192:195], a[228:231], v[32:35]\n" \
"v_mfma_f32_16x16x32_bf16 v[64:67], a[192:195], a[232:235], v[64:67]\n" \
"ds_read_b64_tr_b16 a[220:221], v241 offset:25088\n" \
"v_mfma_f32_16x16x32_bf16 v[64:67], a[196:199], a[248:251], v[64:67]\n" \
"v_mfma_f32_16x16x32_bf16 v[96:99], a[196:199], a[252:255], v[96:99]\n" \
"ds_read_b64_tr_b16 a[222:223], v241 offset:27136\n" \
"v_mfma_f32_16x16x32_bf16 v[96:99], a[192:195], a[236:239], v[96:99]\n" \
"s_waitcnt lgkmcnt(13)\n" \
"s_lshl_b32 s85, s67, 5\n" \
"v_mfma_f32_16x16x32_bf16 v[4:7], a[200:203], a[224:227], v[4:7]\n" \
"s_add_u32 m0, s70, 8192\n" \
"s_add_u32 s85, s85, s68\n" \
"ds_read_b64_tr_b16 a[192:193], v240 offset:17408\n" \
"v_mfma_f32_16x16x32_bf16 v[4:7], a[204:207], a[240:243], v[4:7]\n" \
"s_add_u32 s74, s69, 3932160\n" \
"s_lshl2_add_u32 s85, s85, %[wid]\n" \
"v_mfma_f32_16x16x32_bf16 v[36:39], a[204:207], a[244:247], v[36:39]\n" \
"s_lshl_b32 s85, s85, 2\n" \
"buffer_load_dwordx4 v246, s[24:27], s74 offen lds\n" \
"v_mfma_f32_16x16x32_bf16 v[36:39], a[200:203], a[228:231], v[36:39]\n" \
"s_add_u32 s85, s85, 90112\n" \
"ds_read_b64_tr_b16 a[194:195], v240 offset:19456\n" \
"v_mfma_f32_16x16x32_bf16 v[68:71], a[200:203], a[232:235], v[68:71]\n" \
"s_waitcnt vmcnt(14)\n" \
"buffer_store_dword v239, off, s[32:35], s85 sc1\n" \
"v_mfma_f32_16x16x32_bf16 v[68:71], a[204:207], a[248:251], v[68:71]\n" \
"ds_read_b64_tr_b16 a[196:197], v240 offset:25600\n" \
"v_mfma_f32_16x16x32_bf16 v[100:103], a[204:207], a[252:255], v[100:103]\n" \
"ds_read_b64_tr_b16 a[198:199], v240 offset:27648\n" \
"v_mfma_f32_16x16x32_bf16 v[100:103], a[200:203], a[236:239], v[100:103]\n" \
"s_waitcnt lgkmcnt(11)\n" \
"v_mfma_f32_16x16x32_bf16 v[8:11], a[208:211], a[224:227], v[8:11]\n" \
"ds_read_b64_tr_b16 a[200:201], v241 offset:17408\n" \
"v_mfma_f32_16x16x32_bf16 v[8:11], a[212:215], a[240:243], v[8:11]\n" \
"v_mfma_f32_16x16x32_bf16 v[40:43], a[212:215], a[244:247], v[40:43]\n" \
"ds_read_b64_tr_b16 a[202:203], v241 offset:19456\n" \
"v_mfma_f32_16x16x32_bf16 v[40:43], a[208:211], a[228:231], v[40:43]\n" \
"v_mfma_f32_16x16x32_bf16 v[72:75], a[208:211], a[232:235], v[72:75]\n" \
"ds_read_b64_tr_b16 a[204:205], v241 offset:25600\n" \
"v_mfma_f32_16x16x32_bf16 v[72:75], a[212:215], a[248:251], v[72:75]\n" \
"v_mfma_f32_16x16x32_bf16 v[104:107], a[212:215], a[252:255], v[104:107]\n" \
"s_add_u32 m0, s70, 9216\n" \
"ds_read_b64_tr_b16 a[206:207], v241 offset:27648\n" \
"v_mfma_f32_16x16x32_bf16 v[104:107], a[208:211], a[236:239], v[104:107]\n" \
"s_waitcnt lgkmcnt(8)\n" \
"s_add_u32 s74, s69, 3932288\n" \
"v_mfma_f32_16x16x32_bf16 v[12:15], a[216:219], a[224:227], v[12:15]\n" \
"buffer_load_dwordx4 v246, s[24:27], s74 offen lds\n" \
"v_mfma_f32_16x16x32_bf16 v[12:15], a[220:223], a[240:243], v[12:15]\n" \
"ds_read_b64_tr_b16 a[208:209], v240 offset:17920\n" \
"v_mfma_f32_16x16x32_bf16 v[44:47], a[220:223], a[244:247], v[44:47]\n" \
"ds_read_b64_tr_b16 a[210:211], v240 offset:19968\n" \
"v_mfma_f32_16x16x32_bf16 v[44:47], a[216:219], a[228:231], v[44:47]\n" \
"v_mfma_f32_16x16x32_bf16 v[76:79], a[216:219], a[232:235], v[76:79]\n" \
"ds_read_b64_tr_b16 a[212:213], v240 offset:26112\n" \
"v_mfma_f32_16x16x32_bf16 v[76:79], a[220:223], a[248:251], v[76:79]\n" \
"v_mfma_f32_16x16x32_bf16 v[108:111], a[220:223], a[252:255], v[108:111]\n" \
"ds_read_b64_tr_b16 a[214:215], v240 offset:28160\n" \
"v_mfma_f32_16x16x32_bf16 v[108:111], a[216:219], a[236:239], v[108:111]\n" \
"s_waitcnt lgkmcnt(8)\n" \
"v_mfma_f32_16x16x32_bf16 v[16:19], a[192:195], a[224:227], v[16:19]\n" \
"ds_read_b64_tr_b16 a[216:217], v241 offset:17920\n" \
"v_mfma_f32_16x16x32_bf16 v[16:19], a[196:199], a[240:243], v[16:19]\n" \
"v_mfma_f32_16x16x32_bf16 v[48:51], a[196:199], a[244:247], v[48:51]\n" \
"ds_read_b64_tr_b16 a[218:219], v241 offset:19968\n" \
"v_mfma_f32_16x16x32_bf16 v[48:51], a[192:195], a[228:231], v[48:51]\n" \
"ds_read_b64_tr_b16 a[220:221], v241 offset:26112\n" \
"v_mfma_f32_16x16x32_bf16 v[80:83], a[192:195], a[232:235], v[80:83]\n" \
"ds_read_b64_tr_b16 a[222:223], v241 offset:28160\n" \
"v_mfma_f32_16x16x32_bf16 v[80:83], a[196:199], a[248:251], v[80:83]\n" \
"v_mfma_f32_16x16x32_bf16 v[112:115], a[196:199], a[252:255], v[112:115]\n" \
"v_mfma_f32_16x16x32_bf16 v[112:115], a[192:195], a[236:239], v[112:115]\n" \
"s_waitcnt lgkmcnt(8)\n" \
"v_mfma_f32_16x16x32_bf16 v[20:23], a[200:203], a[224:227], v[20:23]\n" \
"v_mfma_f32_16x16x32_bf16 v[20:23], a[204:207], a[240:243], v[20:23]\n" \
"v_mfma_f32_16x16x32_bf16 v[52:55], a[204:207], a[244:247], v[52:55]\n" \
"v_mfma_f32_16x16x32_bf16 v[52:55], a[200:203], a[228:231], v[52:55]\n" \
"v_mfma_f32_16x16x32_bf16 v[84:87], a[200:203], a[232:235], v[84:87]\n" \
"s_waitcnt lgkmcnt(0)\n" \
"s_barrier\n" \
"s_add_u32 m0, s70, 16384\n" \
"v_mfma_f32_16x16x32_bf16 v[84:87], a[204:207], a[248:251], v[84:87]\n" \
"s_add_u32 s74, s69, 2097152\n" \
"ds_read_b64_tr_b16 a[192:193], v240 offset:32768\n" \
"ds_read_b64_tr_b16 a[194:195], v240 offset:34816\n" \
"v_mfma_f32_16x16x32_bf16 v[116:119], a[204:207], a[252:255], v[116:119]\n" \
"buffer_load_dwordx4 v246, s[24:27], s74 offen lds\n" \
"v_mfma_f32_16x16x32_bf16 v[116:119], a[200:203], a[236:239], v[116:119]\n" \
"ds_read_b64_tr_b16 a[196:197], v240 offset:40960\n" \
"ds_read_b64_tr_b16 a[198:199], v240 offset:43008\n" \
"v_mfma_f32_16x16x32_bf16 v[24:27], a[208:211], a[224:227], v[24:27]\n" \
"ds_read_b64_tr_b16 a[200:201], v241 offset:32768\n" \
"v_mfma_f32_16x16x32_bf16 v[24:27], a[212:215], a[240:243], v[24:27]\n" \
"v_mfma_f32_16x16x32_bf16 v[56:59], a[212:215], a[244:247], v[56:59]\n" \
"ds_read_b64_tr_b16 a[202:203], v241 offset:34816\n" \
"v_mfma_f32_16x16x32_bf16 v[56:59], a[208:211], a[228:231], v[56:59]\n" \
"v_mfma_f32_16x16x32_bf16 v[88:91], a[208:211], a[232:235], v[88:91]\n" \
"ds_read_b64_tr_b16 a[204:205], v241 offset:40960\n" \
"v_mfma_f32_16x16x32_bf16 v[88:91], a[212:215], a[248:251], v[88:91]\n" \
"v_mfma_f32_16x16x32_bf16 v[120:123], a[212:215], a[252:255], v[120:123]\n" \
"ds_read_b64_tr_b16 a[206:207], v241 offset:43008\n" \
"v_mfma_f32_16x16x32_bf16 v[120:123], a[208:211], a[236:239], v[120:123]\n" \
"v_mfma_f32_16x16x32_bf16 v[28:31], a[216:219], a[224:227], v[28:31]\n" \
"ds_read_b64_tr_b16 a[208:209], v240 offset:33280\n" \
"v_mfma_f32_16x16x32_bf16 v[28:31], a[220:223], a[240:243], v[28:31]\n" \
"v_mfma_f32_16x16x32_bf16 v[60:63], a[220:223], a[244:247], v[60:63]\n" \
"s_add_u32 m0, s70, 17408\n" \
"ds_read_b64_tr_b16 a[210:211], v240 offset:35328\n" \
"v_mfma_f32_16x16x32_bf16 v[60:63], a[216:219], a[228:231], v[60:63]\n" \
"s_add_u32 s74, s69, 2097280\n" \
"s_lshl2_add_u32 s85, s67, %[wid]\n" \
"v_mfma_f32_16x16x32_bf16 v[92:95], a[216:219], a[232:235], v[92:95]\n" \
"s_lshl_b32 s85, s85, 16\n" \
"buffer_load_dwordx4 v246, s[24:27], s74 offen lds\n" \
"v_mfma_f32_16x16x32_bf16 v[92:95], a[220:223], a[248:251], v[92:95]\n" \
"s_lshl_b32 s86, s68, 8\n" \
"ds_read_b64_tr_b16 a[212:213], v240 offset:41472\n" \
"v_mfma_f32_16x16x32_bf16 v[124:127], a[220:223], a[252:255], v[124:127]\n" \
"s_add_u32 s85, s85, s86\n" \
"ds_read_b64_tr_b16 a[214:215], v240 offset:43520\n" \
"v_mfma_f32_16x16x32_bf16 v[124:127], a[216:219], a[236:239], v[124:127]\n" \
"s_waitcnt lgkmcnt(8)\n" \
"s_add_u32 s85, s85, 0\n" \
"v_mfma_f32_16x16x32_bf16 v[0:3], a[192:195], a[128:131], v[0:3]\n" \
"buffer_load_dword v245, v208, s[28:31], s85 offen\n" \
"v_mfma_f32_16x16x32_bf16 v[0:3], a[196:199], a[144:147], v[0:3]\n" \
"s_lshl2_add_u32 s85, s67, %[wid]\n" \
"s_lshl_b32 s85, s85, 16\n" \
"ds_read_b64_tr_b16 a[216:217], v241 offset:33280\n" \
"v_mfma_f32_16x16x32_bf16 v[32:35], a[196:199], a[148:151], v[32:35]\n" \
"s_lshl_b32 s86, s68, 8\n" \
"s_add_u32 s85, s85, s86\n" \
"ds_read_b64_tr_b16 a[218:219], v241 offset:35328\n" \
"v_mfma_f32_16x16x32_bf16 v[32:35], a[192:195], a[132:135], v[32:35]\n" \
"s_add_u32 s85, s85, 2097152\n" \
"v_mfma_f32_16x16x32_bf16 v[64:67], a[192:195], a[136:139], v[64:67]\n" \
"buffer_load_dword v245, v208, s[28:31], s85 offen\n" \
"v_mfma_f32_16x16x32_bf16 v[64:67], a[196:199], a[152:155], v[64:67]\n" \
"ds_read_b64_tr_b16 a[220:221], v241 offset:41472\n" \
"v_mfma_f32_16x16x32_bf16 v[96:99], a[196:199], a[156:159], v[96:99]\n" \
"ds_read_b64_tr_b16 a[222:223], v241 offset:43520\n" \
"v_mfma_f32_16x16x32_bf16 v[96:99], a[192:195], a[140:143], v[96:99]\n" \
"s_waitcnt lgkmcnt(8)\n" \
"v_mfma_f32_16x16x32_bf16 v[4:7], a[200:203], a[128:131], v[4:7]\n" \
"s_add_u32 m0, s70, 24576\n" \
"ds_read_b64_tr_b16 a[192:193], v240 offset:33792\n" \
"v_mfma_f32_16x16x32_bf16 v[4:7], a[204:207], a[144:147], v[4:7]\n" \
"s_add_u32 s74, s69, 2359296\n" \
"v_mfma_f32_16x16x32_bf16 v[36:39], a[204:207], a[148:151], v[36:39]\n" \
"buffer_load_dwordx4 v246, s[24:27], s74 offen lds\n" \
"v_mfma_f32_16x16x32_bf16 v[36:39], a[200:203], a[132:135], v[36:39]\n" \
"s_waitcnt vmcnt(10)\n" \
"ds_read_b64_tr_b16 a[194:195], v240 offset:35840\n" \
"ds_read_b128 v[192:195], v242 offset:8192\n" \
"v_mfma_f32_16x16x32_bf16 v[68:71], a[200:203], a[136:139], v[68:71]\n" \
"ds_read_b64_tr_b16 a[196:197], v240 offset:41984\n" \
"ds_read_b128 v[196:199], v242 offset:10240\n" \
"v_mfma_f32_16x16x32_bf16 v[68:71], a[204:207], a[152:155], v[68:71]\n" \
"v_mfma_f32_16x16x32_bf16 v[100:103], a[204:207], a[156:159], v[100:103]\n" \
"ds_read_b64_tr_b16 a[198:199], v240 offset:44032\n" \
"ds_read_b128 v[200:203], v242 offset:12288\n" \
"v_mfma_f32_16x16x32_bf16 v[100:103], a[200:203], a[140:143], v[100:103]\n" \
"s_waitcnt lgkmcnt(11)\n" \
"v_mfma_f32_16x16x32_bf16 v[8:11], a[208:211], a[128:131], v[8:11]\n" \
"ds_read_b64_tr_b16 a[200:201], v241 offset:33792\n" \
"ds_read_b128 v[204:207], v242 offset:14336\n" \
"v_mfma_f32_16x16x32_bf16 v[8:11], a[212:215], a[144:147], v[8:11]\n" \
"v_mfma_f32_16x16x32_bf16 v[40:43], a[212:215], a[148:151], v[40:43]\n" \
"ds_read_b64_tr_b16 a[202:203], v241 offset:35840\n" \
"v_mfma_f32_16x16x32_bf16 v[40:43], a[208:211], a[132:135], v[40:43]\n" \
"v_mfma_f32_16x16x32_bf16 v[72:75], a[208:211], a[136:139], v[72:75]\n" \
"ds_read_b64_tr_b16 a[204:205], v241 offset:41984\n" \
"ds_read_b128 v[208:211], v244 offset:8192\n" \
"v_mfma_f32_16x16x32_bf16 v[72:75], a[212:215], a[152:155], v[72:75]\n" \
"v_mfma_f32_16x16x32_bf16 v[104:107], a[212:215], a[156:159], v[104:107]\n" \
"s_add_u32 m0, s70, 25600\n" \
"ds_read_b64_tr_b16 a[206:207], v241 offset:44032\n" \
"ds_read_b128 v[212:215], v244 offset:10240\n" \
"v_mfma_f32_16x16x32_bf16 v[104:107], a[208:211], a[140:143], v[104:107]\n" \
"s_waitcnt lgkmcnt(14)\n" \
"s_add_u32 s74, s69, 2359424\n" \
"v_mfma_f32_16x16x32_bf16 v[12:15], a[216:219], a[128:131], v[12:15]\n" \
"buffer_load_dwordx4 v246, s[24:27], s74 offen lds\n" \
"v_mfma_f32_16x16x32_bf16 v[12:15], a[220:223], a[144:147], v[12:15]\n" \
"ds_read_b64_tr_b16 a[208:209], v240 offset:34304\n" \
"ds_read_b128 v[216:219], v244 offset:12288\n" \
"v_mfma_f32_16x16x32_bf16 v[44:47], a[220:223], a[148:151], v[44:47]\n" \
"ds_read_b64_tr_b16 a[210:211], v240 offset:36352\n" \
"ds_read_b128 v[220:223], v244 offset:14336\n" \
"v_mfma_f32_16x16x32_bf16 v[44:47], a[216:219], a[132:135], v[44:47]\n" \
"v_mfma_f32_16x16x32_bf16 v[76:79], a[216:219], a[136:139], v[76:79]\n" \
"ds_read_b64_tr_b16 a[212:213], v240 offset:42496\n" \
"v_mfma_f32_16x16x32_bf16 v[76:79], a[220:223], a[152:155], v[76:79]\n" \
"v_mfma_f32_16x16x32_bf16 v[108:111], a[220:223], a[156:159], v[108:111]\n" \
"ds_read_b64_tr_b16 a[214:215], v240 offset:44544\n" \
"v_mfma_f32_16x16x32_bf16 v[108:111], a[216:219], a[140:143], v[108:111]\n" \
"s_waitcnt lgkmcnt(14)\n" \
"v_mfma_f32_16x16x32_bf16 v[16:19], a[192:195], a[128:131], v[16:19]\n" \
"ds_read_b64_tr_b16 a[216:217], v241 offset:34304\n" \
"v_mfma_f32_16x16x32_bf16 v[16:19], a[196:199], a[144:147], v[16:19]\n" \
"v_mfma_f32_16x16x32_bf16 v[48:51], a[196:199], a[148:151], v[48:51]\n" \
"ds_read_b64_tr_b16 a[218:219], v241 offset:36352\n" \
"v_mfma_f32_16x16x32_bf16 v[48:51], a[192:195], a[132:135], v[48:51]\n" \
"ds_read_b64_tr_b16 a[220:221], v241 offset:42496\n" \
"v_mfma_f32_16x16x32_bf16 v[80:83], a[192:195], a[136:139], v[80:83]\n" \
"ds_read_b64_tr_b16 a[222:223], v241 offset:44544\n" \
"v_mfma_f32_16x16x32_bf16 v[80:83], a[196:199], a[152:155], v[80:83]\n" \
"v_mfma_f32_16x16x32_bf16 v[112:115], a[196:199], a[156:159], v[112:115]\n" \
"v_mfma_f32_16x16x32_bf16 v[112:115], a[192:195], a[140:143], v[112:115]\n" \
"s_waitcnt lgkmcnt(11)\n" \
"v_mfma_f32_16x16x32_bf16 v[20:23], a[200:203], a[128:131], v[20:23]\n" \
"v_mfma_f32_16x16x32_bf16 v[20:23], a[204:207], a[144:147], v[20:23]\n" \
"v_mfma_f32_16x16x32_bf16 v[52:55], a[204:207], a[148:151], v[52:55]\n" \
"v_mfma_f32_16x16x32_bf16 v[52:55], a[200:203], a[132:135], v[52:55]\n" \
"v_mfma_f32_16x16x32_bf16 v[84:87], a[200:203], a[136:139], v[84:87]\n" \
"s_waitcnt lgkmcnt(0)\n" \
"s_barrier\n" \
"s_add_u32 m0, s70, 32768\n" \
"v_mfma_f32_16x16x32_bf16 v[84:87], a[204:207], a[152:155], v[84:87]\n" \
"s_add_u32 s74, s69, 2621440\n" \
"ds_read_b64_tr_b16 a[192:193], v240 offset:49152\n" \
"ds_read_b64_tr_b16 a[194:195], v240 offset:51200\n" \
"v_mfma_f32_16x16x32_bf16 v[116:119], a[204:207], a[156:159], v[116:119]\n" \
"buffer_load_dwordx4 v246, s[24:27], s74 offen lds\n" \
"v_mfma_f32_16x16x32_bf16 v[116:119], a[200:203], a[140:143], v[116:119]\n" \
"ds_read_b64_tr_b16 a[196:197], v240 offset:57344\n" \
"ds_read_b64_tr_b16 a[198:199], v240 offset:59392\n" \
"v_mfma_f32_16x16x32_bf16 v[24:27], a[208:211], a[128:131], v[24:27]\n" \
"ds_read_b64_tr_b16 a[200:201], v241 offset:49152\n" \
"v_mfma_f32_16x16x32_bf16 v[24:27], a[212:215], a[144:147], v[24:27]\n" \
"v_mfma_f32_16x16x32_bf16 v[56:59], a[212:215], a[148:151], v[56:59]\n" \
"ds_read_b64_tr_b16 a[202:203], v241 offset:51200\n" \
"v_mfma_f32_16x16x32_bf16 v[56:59], a[208:211], a[132:135], v[56:59]\n" \
"v_mfma_f32_16x16x32_bf16 v[88:91], a[208:211], a[136:139], v[88:91]\n" \
"ds_read_b64_tr_b16 a[204:205], v241 offset:57344\n" \
"v_mfma_f32_16x16x32_bf16 v[88:91], a[212:215], a[152:155], v[88:91]\n" \
"v_mfma_f32_16x16x32_bf16 v[120:123], a[212:215], a[156:159], v[120:123]\n" \
"ds_read_b64_tr_b16 a[206:207], v241 offset:59392\n" \
"v_mfma_f32_16x16x32_bf16 v[120:123], a[208:211], a[140:143], v[120:123]\n" \
"v_mfma_f32_16x16x32_bf16 v[28:31], a[216:219], a[128:131], v[28:31]\n" \
"ds_read_b64_tr_b16 a[208:209], v240 offset:49664\n" \
"v_mfma_f32_16x16x32_bf16 v[28:31], a[220:223], a[144:147], v[28:31]\n" \
"v_mfma_f32_16x16x32_bf16 v[60:63], a[220:223], a[148:151], v[60:63]\n" \
"s_add_u32 m0, s70, 33792\n" \
"ds_read_b64_tr_b16 a[210:211], v240 offset:51712\n" \
"v_mfma_f32_16x16x32_bf16 v[60:63], a[216:219], a[132:135], v[60:63]\n" \
".Lst0_3_%=:\n" \
"s_cbranch_execz .Lst0_7_%=\n" \
"s_add_u32 s74, s69, 2621568\n" \
"v_mfma_f32_16x16x32_bf16 v[92:95], a[216:219], a[136:139], v[92:95]\n" \
"s_lshr_b32 s71, s71, 2\n" \
"buffer_load_dwordx4 v246, s[24:27], s74 offen lds\n" \
"v_mfma_f32_16x16x32_bf16 v[92:95], a[220:223], a[152:155], v[92:95]\n" \
"s_add_u32 s71, s71, s73\n" \
"s_add_u32 s94, s94, 32768\n" \
"ds_read_b64_tr_b16 a[212:213], v240 offset:57856\n" \
"v_mfma_f32_16x16x32_bf16 v[124:127], a[220:223], a[156:159], v[124:127]\n" \
"s_lshr_b32 s94, s94, 2\n" \
"s_add_u32 s94, s94, s73\n" \
"ds_read_b64_tr_b16 a[214:215], v240 offset:59904\n" \
"v_mfma_f32_16x16x32_bf16 v[124:127], a[216:219], a[140:143], v[124:127]\n" \
"s_waitcnt lgkmcnt(8)\n" \
"s_lshl_b32 s72, %[wid], 13\n" \
"v_mfma_f32_16x16x32_bf16 v[0:3], a[192:195], v[192:195], v[0:3]\n" \
"ds_read_b64_tr_b16 a[216:217], v241 offset:49664\n" \
"v_mfma_f32_16x16x32_bf16 v[0:3], a[196:199], v[208:211], v[0:3]\n" \
"v_mfma_f32_16x16x32_bf16 v[32:35], a[196:199], v[212:215], v[32:35]\n" \
"ds_read_b64_tr_b16 a[218:219], v241 offset:51712\n" \
"v_add_u32 v244, s72, v248\n" \
"v_mfma_f32_16x16x32_bf16 v[32:35], a[192:195], v[196:199], v[32:35]\n" \
"v_add_u32 v249, 0x10000, v244\n" \
"v_mov_b32 v250, 0\n" \
"v_mfma_f32_16x16x32_bf16 v[64:67], a[192:195], v[200:203], v[64:67]\n" \
"ds_read_b64_tr_b16 a[220:221], v241 offset:57856\n" \
"v_mfma_f32_16x16x32_bf16 v[64:67], a[196:199], v[216:219], v[64:67]\n" \
"v_mfma_f32_16x16x32_bf16 v[96:99], a[196:199], v[220:223], v[96:99]\n" \
"ds_read_b64_tr_b16 a[222:223], v241 offset:59904\n" \
"v_mfma_f32_16x16x32_bf16 v[96:99], a[192:195], v[204:207], v[96:99]\n" \
"s_waitcnt lgkmcnt(8)\n" \
"v_mfma_f32_16x16x32_bf16 v[4:7], a[200:203], v[192:195], v[4:7]\n" \
"s_add_u32 m0, s70, 40960\n" \
"ds_read_b64_tr_b16 a[192:193], v240 offset:50176\n" \
"v_mfma_f32_16x16x32_bf16 v[4:7], a[204:207], v[208:211], v[4:7]\n" \
"s_add_u32 s74, s69, 2883584\n" \
"v_mfma_f32_16x16x32_bf16 v[36:39], a[204:207], v[212:215], v[36:39]\n" \
"buffer_load_dwordx4 v246, s[24:27], s74 offen lds\n" \
"v_mfma_f32_16x16x32_bf16 v[36:39], a[200:203], v[196:199], v[36:39]\n" \
"ds_read_b64_tr_b16 a[194:195], v240 offset:52224\n" \
"v_mfma_f32_16x16x32_bf16 v[68:71], a[200:203], v[200:203], v[68:71]\n" \
"ds_read_b64_tr_b16 a[196:197], v240 offset:58368\n" \
"v_mfma_f32_16x16x32_bf16 v[68:71], a[204:207], v[216:219], v[68:71]\n" \
"v_mfma_f32_16x16x32_bf16 v[100:103], a[204:207], v[220:223], v[100:103]\n" \
"ds_read_b64_tr_b16 a[198:199], v240 offset:60416\n" \
"v_mfma_f32_16x16x32_bf16 v[100:103], a[200:203], v[204:207], v[100:103]\n" \
"s_waitcnt lgkmcnt(8)\n" \
"v_mfma_f32_16x16x32_bf16 v[8:11], a[208:211], v[192:195], v[8:11]\n" \
"ds_read_b64_tr_b16 a[200:201], v241 offset:50176\n" \
"v_mfma_f32_16x16x32_bf16 v[8:11], a[212:215], v[208:211], v[8:11]\n" \
"v_mfma_f32_16x16x32_bf16 v[40:43], a[212:215], v[212:215], v[40:43]\n" \
"ds_read_b64_tr_b16 a[202:203], v241 offset:52224\n" \
"v_mfma_f32_16x16x32_bf16 v[40:43], a[208:211], v[196:199], v[40:43]\n" \
"v_mfma_f32_16x16x32_bf16 v[72:75], a[208:211], v[200:203], v[72:75]\n" \
"ds_read_b64_tr_b16 a[204:205], v241 offset:58368\n" \
"v_mfma_f32_16x16x32_bf16 v[72:75], a[212:215], v[216:219], v[72:75]\n" \
"v_mfma_f32_16x16x32_bf16 v[104:107], a[212:215], v[220:223], v[104:107]\n" \
"s_add_u32 m0, s70, 41984\n" \
"ds_read_b64_tr_b16 a[206:207], v241 offset:60416\n" \
"v_mfma_f32_16x16x32_bf16 v[104:107], a[208:211], v[204:207], v[104:107]\n" \
"s_waitcnt lgkmcnt(8)\n" \
"s_add_u32 s74, s69, 2883712\n" \
"v_mfma_f32_16x16x32_bf16 v[12:15], a[216:219], v[192:195], v[12:15]\n" \
"buffer_load_dwordx4 v246, s[24:27], s74 offen lds\n" \
"v_mfma_f32_16x16x32_bf16 v[12:15], a[220:223], v[208:211], v[12:15]\n" \
"ds_read_b64_tr_b16 a[208:209], v240 offset:50688\n" \
"v_mfma_f32_16x16x32_bf16 v[44:47], a[220:223], v[212:215], v[44:47]\n" \
"ds_read_b64_tr_b16 a[210:211], v240 offset:52736\n" \
"v_mfma_f32_16x16x32_bf16 v[44:47], a[216:219], v[196:199], v[44:47]\n" \
"v_mfma_f32_16x16x32_bf16 v[76:79], a[216:219], v[200:203], v[76:79]\n" \
"ds_read_b64_tr_b16 a[212:213], v240 offset:58880\n" \
"v_mfma_f32_16x16x32_bf16 v[76:79], a[220:223], v[216:219], v[76:79]\n" \
"v_mfma_f32_16x16x32_bf16 v[108:111], a[220:223], v[220:223], v[108:111]\n" \
"ds_read_b64_tr_b16 a[214:215], v240 offset:60928\n" \
"v_mfma_f32_16x16x32_bf16 v[108:111], a[216:219], v[204:207], v[108:111]\n" \
"s_waitcnt lgkmcnt(8)\n" \
"v_mfma_f32_16x16x32_bf16 v[16:19], a[192:195], v[192:195], v[16:19]\n" \
"ds_read_b64_tr_b16 a[216:217], v241 offset:50688\n" \
"v_mfma_f32_16x16x32_bf16 v[16:19], a[196:199], v[208:211], v[16:19]\n" \
"v_mfma_f32_16x16x32_bf16 v[48:51], a[196:199], v[212:215], v[48:51]\n" \
"ds_read_b64_tr_b16 a[218:219], v241 offset:52736\n" \
"v_mfma_f32_16x16x32_bf16 v[48:51], a[192:195], v[196:199], v[48:51]\n" \
"ds_read_b64_tr_b16 a[220:221], v241 offset:58880\n" \
"v_mfma_f32_16x16x32_bf16 v[80:83], a[192:195], v[200:203], v[80:83]\n" \
"ds_read_b64_tr_b16 a[222:223], v241 offset:60928\n" \
"v_mul_f32 v0, s84, v0\n" \
"v_mfma_f32_16x16x32_bf16 v[80:83], a[196:199], v[216:219], v[80:83]\n" \
"v_mul_f32 v1, s84, v1\n" \
"v_mul_f32 v2, s84, v2\n" \
"v_mfma_f32_16x16x32_bf16 v[112:115], a[196:199], v[220:223], v[112:115]\n" \
"v_mul_f32 v3, s84, v3\n" \
"v_mul_f32 v4, s84, v4\n" \
"v_mfma_f32_16x16x32_bf16 v[112:115], a[192:195], v[204:207], v[112:115]\n" \
"s_waitcnt lgkmcnt(8)\n" \
"v_mul_f32 v5, s84, v5\n" \
"v_mul_f32 v6, s84, v6\n" \
"v_mfma_f32_16x16x32_bf16 v[20:23], a[200:203], v[192:195], v[20:23]\n" \
"v_mul_f32 v7, s84, v7\n" \
"v_cvt_pk_bf16_f32 v128, v0, v1\n" \
"v_mfma_f32_16x16x32_bf16 v[20:23], a[204:207], v[208:211], v[20:23]\n" \
"v_cvt_pk_bf16_f32 v129, v2, v3\n" \
"v_cvt_pk_bf16_f32 v130, v4, v5\n" \
"v_mfma_f32_16x16x32_bf16 v[52:55], a[204:207], v[212:215], v[52:55]\n" \
"v_cvt_pk_bf16_f32 v131, v6, v7\n" \
"v_mfma_f32_16x16x32_bf16 v[52:55], a[200:203], v[196:199], v[52:55]\n" \
"ds_write_b128 v244, v[128:131] offset:0\n" \
"v_mfma_f32_16x16x32_bf16 v[84:87], a[200:203], v[200:203], v[84:87]\n" \
"s_waitcnt vmcnt(10) lgkmcnt(1)\n" \
"s_barrier\n" \
"s_add_u32 m0, s70, 49152\n" \
"v_mfma_f32_16x16x32_bf16 v[84:87], a[204:207], v[216:219], v[84:87]\n" \
"s_add_u32 s74, s69, 3145728\n" \
"ds_read_b64_tr_b16 a[192:193], v240 offset:0\n" \
"ds_read_b64_tr_b16 a[194:195], v240 offset:2048\n" \
"v_mfma_f32_16x16x32_bf16 v[116:119], a[204:207], v[220:223], v[116:119]\n" \
"buffer_load_dwordx4 v246, s[24:27], s74 offen lds\n" \
"v_mfma_f32_16x16x32_bf16 v[116:119], a[200:203], v[204:207], v[116:119]\n" \
"ds_read_b64_tr_b16 a[196:197], v240 offset:8192\n" \
"ds_read_b64_tr_b16 a[198:199], v240 offset:10240\n" \
"v_mfma_f32_16x16x32_bf16 v[24:27], a[208:211], v[192:195], v[24:27]\n" \
"ds_read_b64_tr_b16 a[200:201], v241 offset:0\n" \
"v_mfma_f32_16x16x32_bf16 v[24:27], a[212:215], v[208:211], v[24:27]\n" \
"v_mfma_f32_16x16x32_bf16 v[56:59], a[212:215], v[212:215], v[56:59]\n" \
"ds_read_b64_tr_b16 a[202:203], v241 offset:2048\n" \
"v_mul_f32 v8, s84, v8\n" \
"v_mfma_f32_16x16x32_bf16 v[56:59], a[208:211], v[196:199], v[56:59]\n" \
"v_mul_f32 v9, s84, v9\n" \
"v_mul_f32 v10, s84, v10\n" \
"v_mfma_f32_16x16x32_bf16 v[88:91], a[208:211], v[200:203], v[88:91]\n" \
"ds_read_b64_tr_b16 a[204:205], v241 offset:8192\n" \
"v_and_b32 v252, 15, v251\n" \
"v_mfma_f32_16x16x32_bf16 v[88:91], a[212:215], v[216:219], v[88:91]\n" \
"v_lshlrev_b32 v252, 2, v252\n" \
"v_and_b32 v253, 16, v251\n" \
"v_mfma_f32_16x16x32_bf16 v[120:123], a[212:215], v[220:223], v[120:123]\n" \
"ds_read_b64_tr_b16 a[206:207], v241 offset:10240\n" \
"v_lshl_or_b32 v252, v253, 4, v252\n" \
"v_mfma_f32_16x16x32_bf16 v[120:123], a[208:211], v[204:207], v[120:123]\n" \
"v_mul_f32 v11, s84, v11\n" \
"v_mul_f32 v12, s84, v12\n" \
"v_mfma_f32_16x16x32_bf16 v[28:31], a[216:219], v[192:195], v[28:31]\n" \
"ds_read_b64_tr_b16 a[208:209], v240 offset:512\n" \
"v_mul_f32 v13, s84, v13\n" \
"v_mfma_f32_16x16x32_bf16 v[28:31], a[220:223], v[208:211], v[28:31]\n" \
"v_mul_f32 v14, s84, v14\n" \
"v_mul_f32 v15, s84, v15\n" \
"v_mfma_f32_16x16x32_bf16 v[60:63], a[220:223], v[212:215], v[60:63]\n" \
"s_add_u32 m0, s70, 50176\n" \
"ds_read_b64_tr_b16 a[210:211], v240 offset:2560\n" \
"v_cvt_pk_bf16_f32 v132, v8, v9\n" \
"v_mfma_f32_16x16x32_bf16 v[60:63], a[216:219], v[196:199], v[60:63]\n" \
"s_add_u32 s74, s69, 3145856\n" \
"v_cvt_pk_bf16_f32 v133, v10, v11\n" \
"v_cvt_pk_bf16_f32 v134, v12, v13\n" \
"v_mfma_f32_16x16x32_bf16 v[92:95], a[216:219], v[200:203], v[92:95]\n" \
"buffer_load_dwordx4 v246, s[24:27], s74 offen lds\n" \
"v_mfma_f32_16x16x32_bf16 v[92:95], a[220:223], v[216:219], v[92:95]\n" \
"ds_read_b64_tr_b16 a[212:213], v240 offset:8704\n" \
"v_cvt_pk_bf16_f32 v135, v14, v15\n" \
"v_mfma_f32_16x16x32_bf16 v[124:127], a[220:223], v[220:223], v[124:127]\n" \
"ds_read_b64_tr_b16 a[214:215], v240 offset:10752\n" \
"v_mfma_f32_16x16x32_bf16 v[124:127], a[216:219], v[204:207], v[124:127]\n" \
"s_waitcnt lgkmcnt(8)\n" \
"ds_write_b128 v244, v[132:135] offset:1024\n" \
  \
"v_mfma_f32_16x16x32_bf16 a[0:3], v[192:195], a[192:195], a[0:3]\n" \
"ds_read_b64_tr_b16 a[216:217], v241 offset:512\n" \
"v_mul_f32 v16, s84, v16\n" \
"v_mfma_f32_16x16x32_bf16 a[0:3], v[208:211], a[196:199], a[0:3]\n" \
"v_mul_f32 v17, s84, v17\n" \
"v_mul_f32 v18, s84, v18\n" \
"v_mfma_f32_16x16x32_bf16 a[4:7], v[212:215], a[196:199], a[4:7]\n" \
"ds_read_b64_tr_b16 a[218:219], v241 offset:2560\n" \
"v_mul_f32 v19, s84, v19\n" \
"v_mfma_f32_16x16x32_bf16 a[4:7], v[196:199], a[192:195], a[4:7]\n" \
"v_mul_f32 v20, s84, v20\n" \
"v_mul_f32 v21, s84, v21\n" \
"v_mfma_f32_16x16x32_bf16 a[64:67], v[200:203], a[192:195], a[64:67]\n" \
"ds_read_b64_tr_b16 a[220:221], v241 offset:8704\n" \
"v_mul_f32 v22, s84, v22\n" \
"v_mfma_f32_16x16x32_bf16 a[64:67], v[216:219], a[196:199], a[64:67]\n" \
"v_mul_f32 v23, s84, v23\n" \
"v_cvt_pk_bf16_f32 v136, v16, v17\n" \
"v_mfma_f32_16x16x32_bf16 a[68:71], v[220:223], a[196:199], a[68:71]\n" \
"ds_read_b64_tr_b16 a[222:223], v241 offset:10752\n" \
"v_cvt_pk_bf16_f32 v137, v18, v19\n" \
"v_mfma_f32_16x16x32_bf16 a[68:71], v[204:207], a[192:195], a[68:71]\n" \
"s_waitcnt lgkmcnt(9)\n" \
"v_cvt_pk_bf16_f32 v138, v20, v21\n" \
"v_cvt_pk_bf16_f32 v139, v22, v23\n" \
"v_mfma_f32_16x16x32_bf16 a[8:11], v[192:195], a[200:203], a[8:11]\n" \
"s_add_u32 m0, s70, 57344\n" \
"ds_read_b64_tr_b16 a[192:193], v240 offset:1024\n" \
"v_mfma_f32_16x16x32_bf16 a[8:11], v[208:211], a[204:207], a[8:11]\n" \
"s_add_u32 s74, s69, 3407872\n" \
"ds_write_b128 v244, v[136:139] offset:2048\n" \
"v_mfma_f32_16x16x32_bf16 a[12:15], v[212:215], a[204:207], a[12:15]\n" \
"buffer_load_dwordx4 v246, s[24:27], s74 offen lds\n" \
"v_mfma_f32_16x16x32_bf16 a[12:15], v[196:199], a[200:203], a[12:15]\n" \
"ds_read_b64_tr_b16 a[194:195], v240 offset:3072\n" \
"v_mfma_f32_16x16x32_bf16 a[72:75], v[200:203], a[200:203], a[72:75]\n" \
"ds_read_b64_tr_b16 a[196:197], v240 offset:9216\n" \
"v_mfma_f32_16x16x32_bf16 a[72:75], v[216:219], a[204:207], a[72:75]\n" \
"v_mfma_f32_16x16x32_bf16 a[76:79], v[220:223], a[204:207], a[76:79]\n" \
"ds_read_b64_tr_b16 a[198:199], v240 offset:11264\n" \
"v_mul_f32 v24, s84, v24\n" \
"v_mfma_f32_16x16x32_bf16 a[76:79], v[204:207], a[200:203], a[76:79]\n" \
"s_waitcnt lgkmcnt(10)\n" \
"v_mul_f32 v25, s84, v25\n" \
"v_mul_f32 v26, s84, v26\n" \
"v_mfma_f32_16x16x32_bf16 a[16:19], v[192:195], a[208:211], a[16:19]\n" \
"ds_read_b64_tr_b16 a[200:201], v241 offset:1024\n" \
"v_mul_f32 v27, s84, v27\n" \
"v_mfma_f32_16x16x32_bf16 a[16:19], v[208:211], a[212:215], a[16:19]\n" \
"v_mul_f32 v28, s84, v28\n" \
"v_mul_f32 v29, s84, v29\n" \
"v_mfma_f32_16x16x32_bf16 a[20:23], v[212:215], a[212:215], a[20:23]\n" \
"ds_read_b64_tr_b16 a[202:203], v241 offset:3072\n" \
"v_mul_f32 v30, s84, v30\n" \
"v_mfma_f32_16x16x32_bf16 a[20:23], v[196:199], a[208:211], a[20:23]\n" \
"v_mul_f32 v31, s84, v31\n" \
"v_cvt_pk_bf16_f32 v140, v24, v25\n" \
"v_mfma_f32_16x16x32_bf16 a[80:83], v[200:203], a[208:211], a[80:83]\n" \
"ds_read_b64_tr_b16 a[204:205], v241 offset:9216\n" \
"v_cvt_pk_bf16_f32 v141, v26, v27\n" \
"v_mfma_f32_16x16x32_bf16 a[80:83], v[216:219], a[212:215], a[80:83]\n" \
"s_add_u32 s85, s71, 0\n" \
"v_cvt_pk_bf16_f32 v142, v28, v29\n" \
"v_cvt_pk_bf16_f32 v143, v30, v31\n" \
"v_mfma_f32_16x16x32_bf16 a[84:87], v[220:223], a[212:215], a[84:87]\n" \
"s_add_u32 m0, s70, 58368\n" \
"buffer_store_dwordx4 v[128:131], v248, s[32:35], s85 offen offset:0 sc1\n" \
"v_mfma_f32_16x16x32_bf16 a[84:87], v[204:207], a[208:211], a[84:87]\n" \
"s_waitcnt lgkmcnt(8)\n" \
"s_add_u32 s74, s69, 3408000\n" \
"ds_read_b64_tr_b16 a[206:207], v241 offset:11264\n" \
"v_mfma_f32_16x16x32_bf16 a[24:27], v[192:195], a[216:219], a[24:27]\n" \
"s_lshl_b32 s81, s67, 3\n" \
"ds_read_b64_tr_b16 a[208:209], v240 offset:1536\n" \
"v_mfma_f32_16x16x32_bf16 a[24:27], v[208:211], a[220:223], a[24:27]\n" \
"s_add_u32 s81, s81, %[wid]\n" \
"buffer_load_dwordx4 v246, s[24:27], s74 offen lds\n" \
"v_mfma_f32_16x16x32_bf16 a[28:31], v[212:215], a[220:223], a[28:31]\n" \
"s_lshl_b32 s81, s81, 6\n" \
"ds_read_b64_tr_b16 a[210:211], v240 offset:3584\n" \
"v_mfma_f32_16x16x32_bf16 a[28:31], v[196:199], a[216:219], a[28:31]\n" \
"s_add_u32 s81, s81, 90112\n" \
"ds_write_b128 v244, v[140:143] offset:3072\n" \
"v_mfma_f32_16x16x32_bf16 a[88:91], v[200:203], a[216:219], a[88:91]\n" \
"buffer_load_dword v247, v252, s[32:35], s81 offen sc1\n" \
"v_mul_f32 v32, s84, v32\n" \
"v_mfma_f32_16x16x32_bf16 a[88:91], v[216:219], a[220:223], a[88:91]\n" \
"ds_read_b64_tr_b16 a[212:213], v240 offset:9728\n" \
"v_mul_f32 v33, s84, v33\n" \
"v_mfma_f32_16x16x32_bf16 a[92:95], v[220:223], a[220:223], a[92:95]\n" \
"ds_read_b64_tr_b16 a[214:215], v240 offset:11776\n" \
"v_mul_f32 v34, s84, v34\n" \
"v_mfma_f32_16x16x32_bf16 a[92:95], v[204:207], a[216:219], a[92:95]\n" \
"s_waitcnt lgkmcnt(9)\n" \
"v_mul_f32 v35, s84, v35\n" \
"v_mul_f32 v36, s84, v36\n" \
"v_mfma_f32_16x16x32_bf16 a[32:35], v[192:195], a[192:195], a[32:35]\n" \
"ds_read_b64_tr_b16 a[216:217], v241 offset:1536\n" \
"v_mul_f32 v37, s84, v37\n" \
"v_mfma_f32_16x16x32_bf16 a[32:35], v[208:211], a[196:199], a[32:35]\n" \
"v_mul_f32 v38, s84, v38\n" \
"v_mul_f32 v39, s84, v39\n" \
"v_mfma_f32_16x16x32_bf16 a[36:39], v[212:215], a[196:199], a[36:39]\n" \
"ds_read_b64_tr_b16 a[218:219], v241 offset:3584\n" \
"v_cvt_pk_bf16_f32 v144, v32, v33\n" \
"v_mfma_f32_16x16x32_bf16 a[36:39], v[196:199], a[192:195], a[36:39]\n" \
"ds_read_b64_tr_b16 a[220:221], v241 offset:9728\n" \
"v_cvt_pk_bf16_f32 v145, v34, v35\n" \
"v_mfma_f32_16x16x32_bf16 a[96:99], v[200:203], a[192:195], a[96:99]\n" \
"ds_read_b64_tr_b16 a[222:223], v241 offset:11776\n" \
"v_cvt_pk_bf16_f32 v146, v36, v37\n" \
"v_mfma_f32_16x16x32_bf16 a[96:99], v[216:219], a[196:199], a[96:99]\n" \
"v_cvt_pk_bf16_f32 v147, v38, v39\n" \
"v_mfma_f32_16x16x32_bf16 a[100:103], v[220:223], a[196:199], a[100:103]\n" \
"ds_write_b128 v244, v[144:147] offset:4096\n" \
"v_mfma_f32_16x16x32_bf16 a[100:103], v[204:207], a[192:195], a[100:103]\n" \
"s_waitcnt lgkmcnt(10)\n" \
"v_mfma_f32_16x16x32_bf16 a[40:43], v[192:195], a[200:203], a[40:43]\n" \
"v_mfma_f32_16x16x32_bf16 a[40:43], v[208:211], a[204:207], a[40:43]\n" \
"v_mfma_f32_16x16x32_bf16 a[44:47], v[212:215], a[204:207], a[44:47]\n" \
"v_mul_f32 v40, s84, v40\n" \
"v_mul_f32 v41, s84, v41\n" \
"v_mfma_f32_16x16x32_bf16 a[44:47], v[196:199], a[200:203], a[44:47]\n" \
"v_mul_f32 v42, s84, v42\n" \
"v_mul_f32 v43, s84, v43\n" \
"v_mfma_f32_16x16x32_bf16 a[104:107], v[200:203], a[200:203], a[104:107]\n" \
"s_waitcnt vmcnt(10) lgkmcnt(1)\n" \
"s_barrier\n" \
"v_mul_f32 v44, s84, v44\n" \
"v_mfma_f32_16x16x32_bf16 a[104:107], v[216:219], a[204:207], a[104:107]\n" \
"s_add_u32 m0, s70, 65536\n" \
"ds_read_b64_tr_b16 a[192:193], v240 offset:16384\n" \
"ds_read_b64_tr_b16 a[194:195], v240 offset:18432\n" \
"v_mfma_f32_16x16x32_bf16 a[108:111], v[220:223], a[204:207], a[108:111]\n" \
"s_add_u32 s74, s69, 0\n" \
"buffer_load_dwordx4 v246, s[28:31], s74 offen lds\n" \
"v_mfma_f32_16x16x32_bf16 a[108:111], v[204:207], a[200:203], a[108:111]\n" \
"ds_read_b64_tr_b16 a[196:197], v240 offset:24576\n" \
"ds_read_b64_tr_b16 a[198:199], v240 offset:26624\n" \
"v_mfma_f32_16x16x32_bf16 a[48:51], v[192:195], a[208:211], a[48:51]\n" \
"ds_read_b64_tr_b16 a[200:201], v241 offset:16384\n" \
"v_mul_f32 v45, s84, v45\n" \
"v_mfma_f32_16x16x32_bf16 a[48:51], v[208:211], a[212:215], a[48:51]\n" \
"v_mul_f32 v46, s84, v46\n" \
"v_mul_f32 v47, s84, v47\n" \
"v_mfma_f32_16x16x32_bf16 a[52:55], v[212:215], a[212:215], a[52:55]\n" \
"ds_read_b64_tr_b16 a[202:203], v241 offset:18432\n" \
"v_cvt_pk_bf16_f32 v148, v40, v41\n" \
"v_mfma_f32_16x16x32_bf16 a[52:55], v[196:199], a[208:211], a[52:55]\n" \
"s_add_u32 s85, s71, 0\n" \
"v_cvt_pk_bf16_f32 v149, v42, v43\n" \
"v_cvt_pk_bf16_f32 v150, v44, v45\n" \
"v_mfma_f32_16x16x32_bf16 a[112:115], v[200:203], a[208:211], a[112:115]\n" \
"buffer_store_dwordx4 v[132:135], v248, s[32:35], s85 offen offset:1024 sc1\n" \
"v_mfma_f32_16x16x32_bf16 a[112:115], v[216:219], a[212:215], a[112:115]\n" \
"ds_read_b64_tr_b16 a[204:205], v241 offset:24576\n" \
"v_cvt_pk_bf16_f32 v151, v46, v47\n" \
"v_mfma_f32_16x16x32_bf16 a[116:119], v[220:223], a[212:215], a[116:119]\n" \
"ds_read_b64_tr_b16 a[206:207], v241 offset:26624\n" \
"v_mfma_f32_16x16x32_bf16 a[116:119], v[204:207], a[208:211], a[116:119]\n" \
"ds_write_b128 v244, v[148:151] offset:5120\n" \
"v_mfma_f32_16x16x32_bf16 a[56:59], v[192:195], a[216:219], a[56:59]\n" \
"ds_read_b64_tr_b16 a[208:209], v240 offset:16896\n" \
"v_mul_f32 v48, s84, v48\n" \
"v_mfma_f32_16x16x32_bf16 a[56:59], v[208:211], a[220:223], a[56:59]\n" \
"v_mul_f32 v49, s84, v49\n" \
"v_mul_f32 v50, s84, v50\n" \
"v_mfma_f32_16x16x32_bf16 a[60:63], v[212:215], a[220:223], a[60:63]\n" \
"s_add_u32 m0, s70, 66560\n" \
"ds_read_b64_tr_b16 a[210:211], v240 offset:18944\n" \
"v_mul_f32 v51, s84, v51\n" \
"v_mfma_f32_16x16x32_bf16 a[60:63], v[196:199], a[216:219], a[60:63]\n" \
"s_add_u32 s74, s69, 128\n" \
"v_mul_f32 v52, s84, v52\n" \
"v_mul_f32 v53, s84, v53\n" \
"v_mfma_f32_16x16x32_bf16 a[120:123], v[200:203], a[216:219], a[120:123]\n" \
"buffer_load_dwordx4 v246, s[28:31], s74 offen lds\n" \
"v_mfma_f32_16x16x32_bf16 a[120:123], v[216:219], a[220:223], a[120:123]\n" \
"ds_read_b64_tr_b16 a[212:213], v240 offset:25088\n" \
"v_mul_f32 v54, s84, v54\n" \
"v_mfma_f32_16x16x32_bf16 a[124:127], v[220:223], a[220:223], a[124:127]\n" \
"ds_read_b64_tr_b16 a[214:215], v240 offset:27136\n" \
"v_mul_f32 v55, s84, v55\n" \
"v_mfma_f32_16x16x32_bf16 a[124:127], v[204:207], a[216:219], a[124:127]\n" \
"s_waitcnt lgkmcnt(9)\n" \
"v_cvt_pk_bf16_f32 v152, v48, v49\n" \
"v_cvt_pk_bf16_f32 v153, v50, v51\n" \
"v_mfma_f32_16x16x32_bf16 a[0:3], a[160:163], a[192:195], a[0:3]\n" \
"ds_read_b64_tr_b16 a[216:217], v241 offset:16896\n" \
"v_cvt_pk_bf16_f32 v154, v52, v53\n" \
"v_mfma_f32_16x16x32_bf16 a[0:3], a[176:179], a[196:199], a[0:3]\n" \
"v_cvt_pk_bf16_f32 v155, v54, v55\n" \
"v_mfma_f32_16x16x32_bf16 a[4:7], a[180:183], a[196:199], a[4:7]\n" \
"ds_read_b64_tr_b16 a[218:219], v241 offset:18944\n" \
"v_mfma_f32_16x16x32_bf16 a[4:7], a[164:167], a[192:195], a[4:7]\n" \
"ds_write_b128 v244, v[152:155] offset:6144\n" \
"v_mfma_f32_16x16x32_bf16 a[64:67], a[168:171], a[192:195], a[64:67]\n" \
"ds_read_b64_tr_b16 a[220:221], v241 offset:25088\n" \
"v_mfma_f32_16x16x32_bf16 a[64:67], a[184:187], a[196:199], a[64:67]\n" \
"v_mfma_f32_16x16x32_bf16 a[68:71], a[188:191], a[196:199], a[68:71]\n" \
"ds_read_b64_tr_b16 a[222:223], v241 offset:27136\n" \
"v_mul_f32 v56, s84, v56\n" \
"v_mfma_f32_16x16x32_bf16 a[68:71], a[172:175], a[192:195], a[68:71]\n" \
"s_waitcnt lgkmcnt(10)\n" \
"v_mul_f32 v57, s84, v57\n" \
"v_mul_f32 v58, s84, v58\n" \
"v_mfma_f32_16x16x32_bf16 a[8:11], a[160:163], a[200:203], a[8:11]\n" \
"s_add_u32 m0, s70, 73728\n" \
"ds_read_b64_tr_b16 a[192:193], v240 offset:17408\n" \
"v_mul_f32 v59, s84, v59\n" \
"v_mfma_f32_16x16x32_bf16 a[8:11], a[176:179], a[204:207], a[8:11]\n" \
"s_add_u32 s74, s69, 262144\n" \
"v_mul_f32 v60, s84, v60\n" \
"v_mul_f32 v61, s84, v61\n" \
"v_mfma_f32_16x16x32_bf16 a[12:15], a[180:183], a[204:207], a[12:15]\n" \
"buffer_load_dwordx4 v246, s[28:31], s74 offen lds\n" \
"v_mfma_f32_16x16x32_bf16 a[12:15], a[164:167], a[200:203], a[12:15]\n" \
"ds_read_b64_tr_b16 a[194:195], v240 offset:19456\n" \
"v_mul_f32 v62, s84, v62\n" \
"v_mfma_f32_16x16x32_bf16 a[72:75], a[168:171], a[200:203], a[72:75]\n" \
"ds_read_b64_tr_b16 a[196:197], v240 offset:25600\n" \
"v_mul_f32 v63, s84, v63\n" \
"v_mfma_f32_16x16x32_bf16 a[72:75], a[184:187], a[204:207], a[72:75]\n" \
"v_cvt_pk_bf16_f32 v156, v56, v57\n" \
"v_cvt_pk_bf16_f32 v157, v58, v59\n" \
"v_mfma_f32_16x16x32_bf16 a[76:79], a[188:191], a[204:207], a[76:79]\n" \
"ds_read_b64_tr_b16 a[198:199], v240 offset:27648\n" \
"v_cvt_pk_bf16_f32 v158, v60, v61\n" \
"v_mfma_f32_16x16x32_bf16 a[76:79], a[172:175], a[200:203], a[76:79]\n" \
"s_waitcnt lgkmcnt(9)\n" \
"v_cvt_pk_bf16_f32 v159, v62, v63\n" \
"v_mfma_f32_16x16x32_bf16 a[16:19], a[160:163], a[208:211], a[16:19]\n" \
"ds_read_b64_tr_b16 a[200:201], v241 offset:17408\n" \
"v_mfma_f32_16x16x32_bf16 a[16:19], a[176:179], a[212:215], a[16:19]\n" \
"s_add_u32 s85, s71, 0\n" \
"ds_write_b128 v244, v[156:159] offset:7168\n" \
"v_mfma_f32_16x16x32_bf16 a[20:23], a[180:183], a[212:215], a[20:23]\n" \
"buffer_store_dwordx4 v[136:139], v248, s[32:35], s85 offen offset:2048 sc1\n" \
"v_mfma_f32_16x16x32_bf16 a[20:23], a[164:167], a[208:211], a[20:23]\n" \
"ds_read_b64_tr_b16 a[202:203], v241 offset:19456\n" \
"v_mfma_f32_16x16x32_bf16 a[80:83], a[168:171], a[208:211], a[80:83]\n" \
"s_waitcnt vmcnt(5)\n" \
"ds_read_b64_tr_b16 a[204:205], v241 offset:25600\n" \
"v_cmp_eq_u32 vcc, 0, v247\n" \
"v_mfma_f32_16x16x32_bf16 a[80:83], a[184:187], a[212:215], a[80:83]\n" \
"v_mul_f32 v64, s84, v64\n" \
"v_mul_f32 v65, s84, v65\n" \
"v_mfma_f32_16x16x32_bf16 a[84:87], a[188:191], a[212:215], a[84:87]\n" \
"s_add_u32 m0, s70, 74752\n" \
"ds_read_b64_tr_b16 a[206:207], v241 offset:27648\n" \
"v_mul_f32 v66, s84, v66\n" \
"v_mfma_f32_16x16x32_bf16 a[84:87], a[172:175], a[208:211], a[84:87]\n" \
"s_waitcnt lgkmcnt(9)\n" \
"s_cbranch_vccnz .Lsph_%=\n" \
".Lsbh_%=:\n" \
"v_mfma_f32_16x16x32_bf16 a[24:27], a[160:163], a[216:219], a[24:27]\n" \
"s_add_u32 s74, s69, 262272\n" \
"buffer_load_dwordx4 v246, s[28:31], s74 offen lds\n" \
"v_mfma_f32_16x16x32_bf16 a[24:27], a[176:179], a[220:223], a[24:27]\n" \
"ds_read_b64_tr_b16 a[208:209], v240 offset:17920\n" \
"v_mul_f32 v67, s84, v67\n" \
"v_mfma_f32_16x16x32_bf16 a[28:31], a[180:183], a[220:223], a[28:31]\n" \
"buffer_load_dwordx4 v[192:195], v248, s[32:35], s79 offen offset:0 sc1\n" \
"v_mul_f32 v68, s84, v68\n" \
"v_mfma_f32_16x16x32_bf16 a[28:31], a[164:167], a[216:219], a[28:31]\n" \
"ds_read_b64_tr_b16 a[210:211], v240 offset:19968\n" \
"v_mul_f32 v69, s84, v69\n" \
"v_mfma_f32_16x16x32_bf16 a[88:91], a[168:171], a[216:219], a[88:91]\n" \
"ds_read_b64_tr_b16 a[212:213], v240 offset:26112\n" \
"v_mul_f32 v70, s84, v70\n" \
"v_mfma_f32_16x16x32_bf16 a[88:91], a[184:187], a[220:223], a[88:91]\n" \
"v_mul_f32 v71, s84, v71\n" \
"v_cvt_pk_bf16_f32 v160, v64, v65\n" \
"v_mfma_f32_16x16x32_bf16 a[92:95], a[188:191], a[220:223], a[92:95]\n" \
"ds_read_b64_tr_b16 a[214:215], v240 offset:28160\n" \
"v_cvt_pk_bf16_f32 v161, v66, v67\n" \
"v_mfma_f32_16x16x32_bf16 a[92:95], a[172:175], a[216:219], a[92:95]\n" \
"s_waitcnt lgkmcnt(9)\n" \
"v_cvt_pk_bf16_f32 v162, v68, v69\n" \
"v_cvt_pk_bf16_f32 v163, v70, v71\n" \
"v_mfma_f32_16x16x32_bf16 a[32:35], a[160:163], a[192:195], a[32:35]\n" \
"ds_read_b64_tr_b16 a[216:217], v241 offset:17920\n" \
"v_xor_b32 v244, 0x4000, v244\n" \
"v_mfma_f32_16x16x32_bf16 a[32:35], a[176:179], a[196:199], a[32:35]\n" \
"ds_write_b128 v244, v[160:163] offset:32768\n" \
"v_mfma_f32_16x16x32_bf16 a[36:39], a[180:183], a[196:199], a[36:39]\n" \
"buffer_load_dwordx4 v[196:199], v248, s[32:35], s80 offen offset:0 sc1\n" \
"v_mul_f32 v72, s84, v72\n" \
"v_mfma_f32_16x16x32_bf16 a[36:39], a[164:167], a[192:195], a[36:39]\n" \
"ds_read_b64_tr_b16 a[218:219], v241 offset:19968\n" \
"ds_read_b64_tr_b16 a[220:221], v241 offset:26112\n" \
"v_mfma_f32_16x16x32_bf16 a[96:99], a[168:171], a[192:195], a[96:99]\n" \
"ds_read_b64_tr_b16 a[222:223], v241 offset:28160\n" \
"v_mul_f32 v73, s84, v73\n" \
"v_mfma_f32_16x16x32_bf16 a[96:99], a[184:187], a[196:199], a[96:99]\n" \
"v_mul_f32 v74, s84, v74\n" \
"v_mul_f32 v75, s84, v75\n" \
"v_mfma_f32_16x16x32_bf16 a[100:103], a[188:191], a[196:199], a[100:103]\n" \
"v_mul_f32 v76, s84, v76\n" \
"v_mul_f32 v77, s84, v77\n" \
"v_mfma_f32_16x16x32_bf16 a[100:103], a[172:175], a[192:195], a[100:103]\n" \
"s_waitcnt lgkmcnt(9)\n" \
"v_mul_f32 v78, s84, v78\n" \
"v_mul_f32 v79, s84, v79\n" \
"v_mfma_f32_16x16x32_bf16 a[40:43], a[160:163], a[200:203], a[40:43]\n" \
"v_cvt_pk_bf16_f32 v164, v72, v73\n" \
"v_cvt_pk_bf16_f32 v165, v74, v75\n" \
"v_mfma_f32_16x16x32_bf16 a[40:43], a[176:179], a[204:207], a[40:43]\n" \
"v_cvt_pk_bf16_f32 v166, v76, v77\n" \
"v_cvt_pk_bf16_f32 v167, v78, v79\n" \
"v_mfma_f32_16x16x32_bf16 a[44:47], a[180:183], a[204:207], a[44:47]\n" \
"buffer_load_dwordx4 v[200:203], v248, s[32:35], s36 offen offset:0 sc1\n" \
"v_mfma_f32_16x16x32_bf16 a[44:47], a[164:167], a[200:203], a[44:47]\n" \
"ds_write_b128 v244, v[164:167] offset:33792\n" \
"v_mfma_f32_16x16x32_bf16 a[104:107], a[168:171], a[200:203], a[104:107]\n" \
"s_waitcnt lgkmcnt(1)\n" \
"s_barrier\n" \
"s_add_u32 m0, s70, 81920\n" \
"v_mfma_f32_16x16x32_bf16 a[104:107], a[184:187], a[204:207], a[104:107]\n" \
"s_add_u32 s74, s69, 524288\n" \
"ds_read_b64_tr_b16 a[192:193], v240 offset:32768\n" \
"ds_read_b64_tr_b16 a[194:195], v240 offset:34816\n" \
"v_mfma_f32_16x16x32_bf16 a[108:111], a[188:191], a[204:207], a[108:111]\n" \
"buffer_load_dwordx4 v246, s[28:31], s74 offen lds\n" \
"v_mfma_f32_16x16x32_bf16 a[108:111], a[172:175], a[200:203], a[108:111]\n" \
"s_add_u32 s85, s71, 0\n" \
"ds_read_b64_tr_b16 a[196:197], v240 offset:40960\n" \
"ds_read_b64_tr_b16 a[198:199], v240 offset:43008\n" \
"v_mfma_f32_16x16x32_bf16 a[48:51], a[160:163], a[208:211], a[48:51]\n" \
"ds_read_b64_tr_b16 a[200:201], v241 offset:32768\n" \
"v_mul_f32 v80, s84, v80\n" \
"v_mfma_f32_16x16x32_bf16 a[48:51], a[176:179], a[212:215], a[48:51]\n" \
"buffer_store_dwordx4 v[140:143], v248, s[32:35], s85 offen offset:3072 sc1\n" \
"v_mfma_f32_16x16x32_bf16 a[52:55], a[180:183], a[212:215], a[52:55]\n" \
"buffer_load_dwordx4 v[204:207], v248, s[32:35], s37 offen offset:0 sc1\n" \
"v_mul_f32 v81, s84, v81\n" \
"v_mfma_f32_16x16x32_bf16 a[52:55], a[164:167], a[208:211], a[52:55]\n" \
"ds_read_b64_tr_b16 a[202:203], v241 offset:34816\n" \
"v_mul_f32 v82, s84, v82\n" \
"v_mfma_f32_16x16x32_bf16 a[112:115], a[168:171], a[208:211], a[112:115]\n" \
"ds_read_b64_tr_b16 a[204:205], v241 offset:40960\n" \
"v_mul_f32 v83, s84, v83\n" \
"v_mfma_f32_16x16x32_bf16 a[112:115], a[184:187], a[212:215], a[112:115]\n" \
"v_mul_f32 v84, s84, v84\n" \
"v_mul_f32 v85, s84, v85\n" \
"v_mfma_f32_16x16x32_bf16 a[116:119], a[188:191], a[212:215], a[116:119]\n" \
"ds_read_b64_tr_b16 a[206:207], v241 offset:43008\n" \
"v_mul_f32 v86, s84, v86\n" \
"v_mfma_f32_16x16x32_bf16 a[116:119], a[172:175], a[208:211], a[116:119]\n" \
"v_mul_f32 v87, s84, v87\n" \
"v_cvt_pk_bf16_f32 v168, v80, v81\n" \
"v_mfma_f32_16x16x32_bf16 a[56:59], a[160:163], a[216:219], a[56:59]\n" \
"ds_read_b64_tr_b16 a[208:209], v240 offset:33280\n" \
"v_cvt_pk_bf16_f32 v169, v82, v83\n" \
"v_mfma_f32_16x16x32_bf16 a[56:59], a[176:179], a[220:223], a[56:59]\n" \
"v_cvt_pk_bf16_f32 v170, v84, v85\n" \
"v_cvt_pk_bf16_f32 v171, v86, v87\n" \
"v_mfma_f32_16x16x32_bf16 a[60:63], a[180:183], a[220:223], a[60:63]\n" \
"s_add_u32 m0, s70, 82944\n" \
"ds_read_b64_tr_b16 a[210:211], v240 offset:35328\n" \
"v_mfma_f32_16x16x32_bf16 a[60:63], a[164:167], a[216:219], a[60:63]\n" \
"s_add_u32 s74, s69, 524416\n" \
"ds_write_b128 v244, v[168:171] offset:34816\n" \
"v_mfma_f32_16x16x32_bf16 a[120:123], a[168:171], a[216:219], a[120:123]\n" \
"buffer_load_dwordx4 v246, s[28:31], s74 offen lds\n" \
"v_mfma_f32_16x16x32_bf16 a[120:123], a[184:187], a[220:223], a[120:123]\n" \
"ds_read_b64_tr_b16 a[212:213], v240 offset:41472\n" \
"v_mfma_f32_16x16x32_bf16 a[124:127], a[188:191], a[220:223], a[124:127]\n" \
"ds_read_b64_tr_b16 a[214:215], v240 offset:43520\n" \
"v_mul_f32 v88, s84, v88\n" \
"v_mfma_f32_16x16x32_bf16 a[124:127], a[172:175], a[216:219], a[124:127]\n" \
"s_waitcnt lgkmcnt(9)\n" \
"v_mul_f32 v89, s84, v89\n" \
"v_mul_f32 v90, s84, v90\n" \
"v_mfma_f32_16x16x32_bf16 a[0:3], a[224:227], a[192:195], a[0:3]\n" \
"ds_read_b64_tr_b16 a[216:217], v241 offset:33280\n" \
"v_mul_f32 v91, s84, v91\n" \
"v_mfma_f32_16x16x32_bf16 a[0:3], a[240:243], a[196:199], a[0:3]\n" \
"buffer_load_dwordx4 a[160:163], v248, s[32:35], s79 offen offset:1024 sc1\n" \
"v_mul_f32 v92, s84, v92\n" \
"v_mfma_f32_16x16x32_bf16 a[4:7], a[244:247], a[196:199], a[4:7]\n" \
"ds_read_b64_tr_b16 a[218:219], v241 offset:35328\n" \
"v_mul_f32 v93, s84, v93\n" \
"v_mfma_f32_16x16x32_bf16 a[4:7], a[228:231], a[192:195], a[4:7]\n" \
"v_mul_f32 v94, s84, v94\n" \
"v_mul_f32 v95, s84, v95\n" \
"v_mfma_f32_16x16x32_bf16 a[64:67], a[232:235], a[192:195], a[64:67]\n" \
"ds_read_b64_tr_b16 a[220:221], v241 offset:41472\n" \
"v_cvt_pk_bf16_f32 v172, v88, v89\n" \
"v_mfma_f32_16x16x32_bf16 a[64:67], a[248:251], a[196:199], a[64:67]\n" \
"v_cvt_pk_bf16_f32 v173, v90, v91\n" \
"v_cvt_pk_bf16_f32 v174, v92, v93\n" \
"v_mfma_f32_16x16x32_bf16 a[68:71], a[252:255], a[196:199], a[68:71]\n" \
"ds_read_b64_tr_b16 a[222:223], v241 offset:43520\n" \
"v_cvt_pk_bf16_f32 v175, v94, v95\n" \
"v_mfma_f32_16x16x32_bf16 a[68:71], a[236:239], a[192:195], a[68:71]\n" \
"s_waitcnt lgkmcnt(9)\n" \
"ds_write_b128 v244, v[172:175] offset:35840\n" \
"v_mfma_f32_16x16x32_bf16 a[8:11], a[224:227], a[200:203], a[8:11]\n" \
"ds_read_b64_tr_b16 a[192:193], v240 offset:33792\n" \
"v_mfma_f32_16x16x32_bf16 a[8:11], a[240:243], a[204:207], a[8:11]\n" \
"buffer_load_dwordx4 a[164:167], v248, s[32:35], s80 offen offset:1024 sc1\n" \
"v_mfma_f32_16x16x32_bf16 a[12:15], a[244:247], a[204:207], a[12:15]\n" \
"s_add_u32 m0, s70, 90112\n" \
"s_add_u32 s74, s69, 786432\n" \
"ds_read_b64_tr_b16 a[194:195], v240 offset:35840\n" \
"v_mfma_f32_16x16x32_bf16 a[12:15], a[228:231], a[200:203], a[12:15]\n" \
"v_mfma_f32_16x16x32_bf16 a[72:75], a[232:235], a[200:203], a[72:75]\n" \
"buffer_load_dwordx4 v246, s[28:31], s74 offen lds\n" \
"v_mfma_f32_16x16x32_bf16 a[72:75], a[248:251], a[204:207], a[72:75]\n" \
"s_add_u32 s85, s71, 4096\n" \
"ds_read_b64_tr_b16 a[196:197], v240 offset:41984\n" \
"v_mul_f32 v96, s84, v96\n" \
"v_mfma_f32_16x16x32_bf16 a[76:79], a[252:255], a[204:207], a[76:79]\n" \
"ds_read_b64_tr_b16 a[198:199], v240 offset:44032\n" \
"v_mul_f32 v97, s84, v97\n" \
"v_mfma_f32_16x16x32_bf16 a[76:79], a[236:239], a[200:203], a[76:79]\n" \
"s_waitcnt lgkmcnt(9)\n" \
"buffer_store_dwordx4 v[144:147], v248, s[32:35], s85 offen offset:0 sc1\n" \
"v_mfma_f32_16x16x32_bf16 a[16:19], a[224:227], a[208:211], a[16:19]\n" \
"ds_read_b64_tr_b16 a[200:201], v241 offset:33792\n" \
"v_mul_f32 v98, s84, v98\n" \
"v_mfma_f32_16x16x32_bf16 a[16:19], a[240:243], a[212:215], a[16:19]\n" \
".Lst0_4_%=:\n" \
"s_cbranch_execz .Lst0_8_%=\n" \
"buffer_load_dwordx4 a[168:171], v248, s[32:35], s36 offen offset:1024 sc1\n" \
"v_mul_f32 v99, s84, v99\n" \
"v_mfma_f32_16x16x32_bf16 a[20:23], a[244:247], a[212:215], a[20:23]\n" \
"ds_read_b64_tr_b16 a[202:203], v241 offset:35840\n" \
"v_mul_f32 v100, s84, v100\n" \
"v_mfma_f32_16x16x32_bf16 a[20:23], a[228:231], a[208:211], a[20:23]\n" \
"v_mul_f32 v101, s84, v101\n" \
"v_mul_f32 v102, s84, v102\n" \
"v_mfma_f32_16x16x32_bf16 a[80:83], a[232:235], a[208:211], a[80:83]\n" \
"ds_read_b64_tr_b16 a[204:205], v241 offset:41984\n" \
"v_mul_f32 v103, s84, v103\n" \
"v_mfma_f32_16x16x32_bf16 a[80:83], a[248:251], a[212:215], a[80:83]\n" \
"v_cvt_pk_bf16_f32 v176, v96, v97\n" \
"v_cvt_pk_bf16_f32 v177, v98, v99\n" \
"v_mfma_f32_16x16x32_bf16 a[84:87], a[252:255], a[212:215], a[84:87]\n" \
"s_add_u32 m0, s70, 91136\n" \
"ds_read_b64_tr_b16 a[206:207], v241 offset:44032\n" \
"v_cvt_pk_bf16_f32 v178, v100, v101\n" \
"v_mfma_f32_16x16x32_bf16 a[84:87], a[236:239], a[208:211], a[84:87]\n" \
"s_waitcnt lgkmcnt(9)\n" \
"s_add_u32 s74, s69, 786560\n" \
"v_cvt_pk_bf16_f32 v179, v102, v103\n" \
"v_mfma_f32_16x16x32_bf16 a[24:27], a[224:227], a[216:219], a[24:27]\n" \
"buffer_load_dwordx4 v246, s[28:31], s74 offen lds\n" \
"v_mfma_f32_16x16x32_bf16 a[24:27], a[240:243], a[220:223], a[24:27]\n" \
"buffer_load_dwordx4 a[172:175], v248, s[32:35], s37 offen offset:1024 sc1\n" \
"v_mfma_f32_16x16x32_bf16 a[28:31], a[244:247], a[220:223], a[28:31]\n" \
"buffer_load_dwordx4 a[176:179], v248, s[32:35], s79 offen offset:2048 sc1\n" \
"v_mfma_f32_16x16x32_bf16 a[28:31], a[228:231], a[216:219], a[28:31]\n" \
"ds_read_b64_tr_b16 a[208:209], v240 offset:34304\n" \
"ds_read_b64_tr_b16 a[210:211], v240 offset:36352\n" \
"v_mfma_f32_16x16x32_bf16 a[88:91], a[232:235], a[216:219], a[88:91]\n" \
"ds_read_b64_tr_b16 a[212:213], v240 offset:42496\n" \
"v_mfma_f32_16x16x32_bf16 a[88:91], a[248:251], a[220:223], a[88:91]\n" \
"ds_write_b128 v244, v[176:179] offset:36864\n" \
"v_mul_f32 v104, s84, v104\n" \
"v_mfma_f32_16x16x32_bf16 a[92:95], a[252:255], a[220:223], a[92:95]\n" \
"ds_read_b64_tr_b16 a[214:215], v240 offset:44544\n" \
"v_mul_f32 v105, s84, v105\n" \
"v_mfma_f32_16x16x32_bf16 a[92:95], a[236:239], a[216:219], a[92:95]\n" \
"s_waitcnt lgkmcnt(9)\n" \
"v_mul_f32 v106, s84, v106\n" \
"v_mul_f32 v107, s84, v107\n" \
"v_mfma_f32_16x16x32_bf16 a[32:35], a[224:227], a[192:195], a[32:35]\n" \
"ds_read_b64_tr_b16 a[216:217], v241 offset:34304\n" \
"v_mul_f32 v108, s84, v108\n" \
"v_mfma_f32_16x16x32_bf16 a[32:35], a[240:243], a[196:199], a[32:35]\n" \
"v_mul_f32 v109, s84, v109\n" \
"v_mul_f32 v110, s84, v110\n" \
"v_mfma_f32_16x16x32_bf16 a[36:39], a[244:247], a[196:199], a[36:39]\n" \
"buffer_load_dwordx4 a[180:183], v248, s[32:35], s80 offen offset:2048 sc1\n" \
"v_mul_f32 v111, s84, v111\n" \
"v_mfma_f32_16x16x32_bf16 a[36:39], a[228:231], a[192:195], a[36:39]\n" \
"ds_read_b64_tr_b16 a[218:219], v241 offset:36352\n" \
"ds_read_b64_tr_b16 a[220:221], v241 offset:42496\n" \
"v_mfma_f32_16x16x32_bf16 a[96:99], a[232:235], a[192:195], a[96:99]\n" \
"ds_read_b64_tr_b16 a[222:223], v241 offset:44544\n" \
"v_cvt_pk_bf16_f32 v180, v104, v105\n" \
"v_mfma_f32_16x16x32_bf16 a[96:99], a[248:251], a[196:199], a[96:99]\n" \
"v_cvt_pk_bf16_f32 v181, v106, v107\n" \
"v_cvt_pk_bf16_f32 v182, v108, v109\n" \
"v_mfma_f32_16x16x32_bf16 a[100:103], a[252:255], a[196:199], a[100:103]\n" \
"v_cvt_pk_bf16_f32 v183, v110, v111\n" \
"v_mfma_f32_16x16x32_bf16 a[100:103], a[236:239], a[192:195], a[100:103]\n" \
"s_waitcnt lgkmcnt(9)\n" \
"ds_write_b128 v244, v[180:183] offset:37888\n" \
"v_mfma_f32_16x16x32_bf16 a[40:43], a[224:227], a[200:203], a[40:43]\n" \
"v_mul_f32 v112, s84, v112\n" \
"v_mul_f32 v113, s84, v113\n" \
"v_mfma_f32_16x16x32_bf16 a[40:43], a[240:243], a[204:207], a[40:43]\n" \
"v_mul_f32 v114, s84, v114\n" \
"v_mul_f32 v115, s84, v115\n" \
"v_mfma_f32_16x16x32_bf16 a[44:47], a[244:247], a[204:207], a[44:47]\n" \
"buffer_load_dwordx4 a[184:187], v248, s[32:35], s36 offen offset:2048 sc1\n" \
"v_mul_f32 v116, s84, v116\n" \
"v_mfma_f32_16x16x32_bf16 a[44:47], a[228:231], a[200:203], a[44:47]\n" \
"s_add_u32 s85, s71, 4096\n" \
"v_mul_f32 v117, s84, v117\n" \
"v_mul_f32 v118, s84, v118\n" \
"v_mfma_f32_16x16x32_bf16 a[104:107], a[232:235], a[200:203], a[104:107]\n" \
"s_waitcnt lgkmcnt(1)\n" \
"s_barrier\n" \
"v_mul_f32 v119, s84, v119\n" \
"v_mfma_f32_16x16x32_bf16 a[104:107], a[248:251], a[204:207], a[104:107]\n" \
"s_add_u32 m0, s70, 32768\n" \
"buffer_store_dwordx4 v[148:151], v248, s[32:35], s85 offen offset:1024 sc1\n" \
"v_mfma_f32_16x16x32_bf16 a[108:111], a[252:255], a[204:207], a[108:111]\n" \
"s_add_u32 s74, s69, 1048576\n" \
"ds_read_b64_tr_b16 a[192:193], v240 offset:49152\n" \
"ds_read_b64_tr_b16 a[194:195], v240 offset:51200\n" \
"v_mfma_f32_16x16x32_bf16 a[108:111], a[236:239], a[200:203], a[108:111]\n" \
"ds_read_b64_tr_b16 a[196:197], v240 offset:57344\n" \
"ds_read_b64_tr_b16 a[198:199], v240 offset:59392\n" \
"v_mfma_f32_16x16x32_bf16 a[48:51], a[224:227], a[208:211], a[48:51]\n" \
"buffer_load_dwordx4 v246, s[28:31], s74 offen lds\n" \
"v_mfma_f32_16x16x32_bf16 a[48:51], a[240:243], a[212:215], a[48:51]\n" \
"ds_read_b64_tr_b16 a[200:201], v241 offset:49152\n" \
"v_cvt_pk_bf16_f32 v184, v112, v113\n" \
"v_mfma_f32_16x16x32_bf16 a[52:55], a[244:247], a[212:215], a[52:55]\n" \
"buffer_load_dwordx4 a[188:191], v248, s[32:35], s37 offen offset:2048 sc1\n" \
"v_cvt_pk_bf16_f32 v185, v114, v115\n" \
"v_mfma_f32_16x16x32_bf16 a[52:55], a[228:231], a[208:211], a[52:55]\n" \
"ds_read_b64_tr_b16 a[202:203], v241 offset:51200\n" \
"v_cvt_pk_bf16_f32 v186, v116, v117\n" \
"v_mfma_f32_16x16x32_bf16 a[112:115], a[232:235], a[208:211], a[112:115]\n" \
"ds_read_b64_tr_b16 a[204:205], v241 offset:57344\n" \
"v_cvt_pk_bf16_f32 v187, v118, v119\n" \
"v_mfma_f32_16x16x32_bf16 a[112:115], a[248:251], a[212:215], a[112:115]\n" \
"ds_write_b128 v244, v[184:187] offset:38912\n" \
"v_mfma_f32_16x16x32_bf16 a[116:119], a[252:255], a[212:215], a[116:119]\n" \
"ds_read_b64_tr_b16 a[206:207], v241 offset:59392\n" \
"v_mul_f32 v120, s84, v120\n" \
"v_mfma_f32_16x16x32_bf16 a[116:119], a[236:239], a[208:211], a[116:119]\n" \
"v_mul_f32 v121, s84, v121\n" \
"v_mul_f32 v122, s84, v122\n" \
"v_mfma_f32_16x16x32_bf16 a[56:59], a[224:227], a[216:219], a[56:59]\n" \
"ds_read_b64_tr_b16 a[208:209], v240 offset:49664\n" \
"v_mul_f32 v123, s84, v123\n" \
"v_mfma_f32_16x16x32_bf16 a[56:59], a[240:243], a[220:223], a[56:59]\n" \
"v_mul_f32 v124, s84, v124\n" \
"v_mul_f32 v125, s84, v125\n" \
"v_mfma_f32_16x16x32_bf16 a[60:63], a[244:247], a[220:223], a[60:63]\n" \
"s_add_u32 m0, s70, 33792\n" \
"ds_read_b64_tr_b16 a[210:211], v240 offset:51712\n" \
"v_mul_f32 v126, s84, v126\n" \
"v_mfma_f32_16x16x32_bf16 a[60:63], a[228:231], a[216:219], a[60:63]\n" \
"s_add_u32 s74, s69, 1048704\n" \
"v_mul_f32 v127, s84, v127\n" \
"v_cvt_pk_bf16_f32 v188, v120, v121\n" \
"v_mfma_f32_16x16x32_bf16 a[120:123], a[232:235], a[216:219], a[120:123]\n" \
"buffer_load_dwordx4 v246, s[28:31], s74 offen lds\n" \
"v_mfma_f32_16x16x32_bf16 a[120:123], a[248:251], a[220:223], a[120:123]\n" \
"ds_read_b64_tr_b16 a[212:213], v240 offset:57856\n" \
"v_cvt_pk_bf16_f32 v189, v122, v123\n" \
"v_mfma_f32_16x16x32_bf16 a[124:127], a[252:255], a[220:223], a[124:127]\n" \
"ds_read_b64_tr_b16 a[214:215], v240 offset:59904\n" \
"v_cvt_pk_bf16_f32 v190, v124, v125\n" \
"v_mfma_f32_16x16x32_bf16 a[124:127], a[236:239], a[216:219], a[124:127]\n" \
"s_waitcnt lgkmcnt(9)\n" \
"v_cvt_pk_bf16_f32 v191, v126, v127\n" \
"v_mfma_f32_16x16x32_bf16 a[0:3], a[128:131], a[192:195], a[0:3]\n" \
"ds_read_b64_tr_b16 a[216:217], v241 offset:49664\n" \
"v_mfma_f32_16x16x32_bf16 a[0:3], a[144:147], a[196:199], a[0:3]\n" \
"buffer_load_dwordx4 a[224:227], v248, s[32:35], s79 offen offset:3072 sc1\n" \
"v_mfma_f32_16x16x32_bf16 a[4:7], a[148:151], a[196:199], a[4:7]\n" \
"ds_read_b64_tr_b16 a[218:219], v241 offset:51712\n" \
"v_mfma_f32_16x16x32_bf16 a[4:7], a[132:135], a[192:195], a[4:7]\n" \
"ds_write_b128 v244, v[188:191] offset:39936\n" \
"v_mfma_f32_16x16x32_bf16 a[64:67], a[136:139], a[192:195], a[64:67]\n" \
"ds_read_b64_tr_b16 a[220:221], v241 offset:57856\n" \
"v_mfma_f32_16x16x32_bf16 a[64:67], a[152:155], a[196:199], a[64:67]\n" \
"v_mfma_f32_16x16x32_bf16 a[68:71], a[156:159], a[196:199], a[68:71]\n" \
"ds_read_b64_tr_b16 a[222:223], v241 offset:59904\n" \
"v_mfma_f32_16x16x32_bf16 a[68:71], a[140:143], a[192:195], a[68:71]\n" \
"s_waitcnt lgkmcnt(9)\n" \
"v_mfma_f32_16x16x32_bf16 a[8:11], a[128:131], a[200:203], a[8:11]\n" \
"ds_read_b64_tr_b16 a[192:193], v240 offset:50176\n" \
"v_mfma_f32_16x16x32_bf16 a[8:11], a[144:147], a[204:207], a[8:11]\n" \
"s_add_u32 s85, s71, 4096\n" \
"buffer_load_dwordx4 a[228:231], v248, s[32:35], s80 offen offset:3072 sc1\n" \
"v_mfma_f32_16x16x32_bf16 a[12:15], a[148:151], a[204:207], a[12:15]\n" \
"s_add_u32 m0, s70, 40960\n" \
"s_add_u32 s74, s69, 1310720\n" \
"ds_read_b64_tr_b16 a[194:195], v240 offset:52224\n" \
"v_mfma_f32_16x16x32_bf16 a[12:15], a[132:135], a[200:203], a[12:15]\n" \
"v_mfma_f32_16x16x32_bf16 a[72:75], a[136:139], a[200:203], a[72:75]\n" \
"buffer_load_dwordx4 v246, s[28:31], s74 offen lds\n" \
"v_mfma_f32_16x16x32_bf16 a[72:75], a[152:155], a[204:207], a[72:75]\n" \
"ds_read_b64_tr_b16 a[196:197], v240 offset:58368\n" \
"v_mfma_f32_16x16x32_bf16 a[76:79], a[156:159], a[204:207], a[76:79]\n" \
"ds_read_b64_tr_b16 a[198:199], v240 offset:60416\n" \
"v_mfma_f32_16x16x32_bf16 a[76:79], a[140:143], a[200:203], a[76:79]\n" \
"s_waitcnt lgkmcnt(9)\n" \
"buffer_store_dwordx4 v[152:155], v248, s[32:35], s85 offen offset:2048 sc1\n" \
"v_mfma_f32_16x16x32_bf16 a[16:19], a[128:131], a[208:211], a[16:19]\n" \
"ds_read_b64_tr_b16 a[200:201], v241 offset:50176\n" \
"v_mfma_f32_16x16x32_bf16 a[16:19], a[144:147], a[212:215], a[16:19]\n" \
"buffer_load_dwordx4 a[232:235], v248, s[32:35], s36 offen offset:3072 sc1\n" \
"v_mfma_f32_16x16x32_bf16 a[20:23], a[148:151], a[212:215], a[20:23]\n" \
"ds_read_b64_tr_b16 a[202:203], v241 offset:52224\n" \
"v_mfma_f32_16x16x32_bf16 a[20:23], a[132:135], a[208:211], a[20:23]\n" \
"v_mfma_f32_16x16x32_bf16 a[80:83], a[136:139], a[208:211], a[80:83]\n" \
"ds_read_b64_tr_b16 a[204:205], v241 offset:58368\n" \
"v_mfma_f32_16x16x32_bf16 a[80:83], a[152:155], a[212:215], a[80:83]\n" \
"v_mfma_f32_16x16x32_bf16 a[84:87], a[156:159], a[212:215], a[84:87]\n" \
"s_add_u32 m0, s70, 41984\n" \
"ds_read_b64_tr_b16 a[206:207], v241 offset:60416\n" \
"v_mfma_f32_16x16x32_bf16 a[84:87], a[140:143], a[208:211], a[84:87]\n" \
"s_waitcnt lgkmcnt(8)\n" \
"s_add_u32 s74, s69, 1310848\n" \
"v_mfma_f32_16x16x32_bf16 a[24:27], a[128:131], a[216:219], a[24:27]\n" \
"buffer_load_dwordx4 v246, s[28:31], s74 offen lds\n" \
"v_mfma_f32_16x16x32_bf16 a[24:27], a[144:147], a[220:223], a[24:27]\n" \
"buffer_load_dwordx4 a[236:239], v248, s[32:35], s37 offen offset:3072 sc1\n" \
"v_mfma_f32_16x16x32_bf16 a[28:31], a[148:151], a[220:223], a[28:31]\n" \
"s_add_u32 s79, s79, 4096\n" \
"buffer_load_dwordx4 a[240:243], v248, s[32:35], s79 offen offset:0 sc1\n" \
"v_mfma_f32_16x16x32_bf16 a[28:31], a[132:135], a[216:219], a[28:31]\n" \
"ds_read_b64_tr_b16 a[208:209], v240 offset:50688\n" \
"ds_read_b64_tr_b16 a[210:211], v240 offset:52736\n" \
"v_mfma_f32_16x16x32_bf16 a[88:91], a[136:139], a[216:219], a[88:91]\n" \
"ds_read_b64_tr_b16 a[212:213], v240 offset:58880\n" \
"v_mfma_f32_16x16x32_bf16 a[88:91], a[152:155], a[220:223], a[88:91]\n" \
"v_mfma_f32_16x16x32_bf16 a[92:95], a[156:159], a[220:223], a[92:95]\n" \
"ds_read_b64_tr_b16 a[214:215], v240 offset:60928\n" \
"v_mfma_f32_16x16x32_bf16 a[92:95], a[140:143], a[216:219], a[92:95]\n" \
"s_waitcnt lgkmcnt(8)\n" \
"v_mfma_f32_16x16x32_bf16 a[32:35], a[128:131], a[192:195], a[32:35]\n" \
"ds_read_b64_tr_b16 a[216:217], v241 offset:50688\n" \
"v_mfma_f32_16x16x32_bf16 a[32:35], a[144:147], a[196:199], a[32:35]\n" \
"s_add_u32 s80, s80, 4096\n" \
"v_mfma_f32_16x16x32_bf16 a[36:39], a[148:151], a[196:199], a[36:39]\n" \
"buffer_load_dwordx4 a[244:247], v248, s[32:35], s80 offen offset:0 sc1\n" \
"v_mfma_f32_16x16x32_bf16 a[36:39], a[132:135], a[192:195], a[36:39]\n" \
"ds_read_b64_tr_b16 a[218:219], v241 offset:52736\n" \
"ds_read_b64_tr_b16 a[220:221], v241 offset:58880\n" \
"v_mfma_f32_16x16x32_bf16 a[96:99], a[136:139], a[192:195], a[96:99]\n" \
"ds_read_b64_tr_b16 a[222:223], v241 offset:60928\n" \
"v_xor_b32 v240, 0x30000, v240\n" \
"v_mfma_f32_16x16x32_bf16 a[96:99], a[152:155], a[196:199], a[96:99]\n" \
"v_xor_b32 v241, 0x30000, v241\n" \
"v_mfma_f32_16x16x32_bf16 a[100:103], a[156:159], a[196:199], a[100:103]\n" \
"v_mfma_f32_16x16x32_bf16 a[100:103], a[140:143], a[192:195], a[100:103]\n" \
"s_waitcnt lgkmcnt(8)\n" \
"s_add_u32 s85, s71, 4096\n" \
"v_mfma_f32_16x16x32_bf16 a[40:43], a[128:131], a[200:203], a[40:43]\n" \
"buffer_store_dwordx4 v[156:159], v248, s[32:35], s85 offen offset:3072 sc1\n" \
"v_mfma_f32_16x16x32_bf16 a[40:43], a[144:147], a[204:207], a[40:43]\n" \
"s_add_u32 s36, s36, 4096\n" \
"v_mfma_f32_16x16x32_bf16 a[44:47], a[148:151], a[204:207], a[44:47]\n" \
"buffer_load_dwordx4 a[248:251], v248, s[32:35], s36 offen offset:0 sc1\n" \
"v_mfma_f32_16x16x32_bf16 a[44:47], a[132:135], a[200:203], a[44:47]\n" \
"v_mfma_f32_16x16x32_bf16 a[104:107], a[136:139], a[200:203], a[104:107]\n" \
"s_waitcnt vmcnt(32) lgkmcnt(0)\n" \
"s_barrier\n" \
"s_add_u32 m0, s70, 49152\n" \
"v_mfma_f32_16x16x32_bf16 a[104:107], a[152:155], a[204:207], a[104:107]\n" \
"s_add_u32 s74, s69, 1572864\n" \
"ds_read_b64_tr_b16 a[192:193], v240 offset:0\n" \
"ds_read_b64_tr_b16 a[194:195], v240 offset:2048\n" \
"v_mfma_f32_16x16x32_bf16 a[108:111], a[156:159], a[204:207], a[108:111]\n" \
"buffer_load_dwordx4 v246, s[28:31], s74 offen lds\n" \
"v_mfma_f32_16x16x32_bf16 a[108:111], a[140:143], a[200:203], a[108:111]\n" \
"ds_read_b64_tr_b16 a[196:197], v240 offset:8192\n" \
"ds_read_b64_tr_b16 a[198:199], v240 offset:10240\n" \
"v_mfma_f32_16x16x32_bf16 a[48:51], a[128:131], a[208:211], a[48:51]\n" \
"ds_read_b64_tr_b16 a[200:201], v241 offset:0\n" \
"v_mfma_f32_16x16x32_bf16 a[48:51], a[144:147], a[212:215], a[48:51]\n" \
"s_add_u32 s37, s37, 4096\n" \
"v_mfma_f32_16x16x32_bf16 a[52:55], a[148:151], a[212:215], a[52:55]\n" \
"buffer_load_dwordx4 a[252:255], v248, s[32:35], s37 offen offset:0 sc1\n" \
"v_mfma_f32_16x16x32_bf16 a[52:55], a[132:135], a[208:211], a[52:55]\n" \
"ds_read_b64_tr_b16 a[202:203], v241 offset:2048\n" \
"v_mfma_f32_16x16x32_bf16 a[112:115], a[136:139], a[208:211], a[112:115]\n" \
"ds_read_b64_tr_b16 a[204:205], v241 offset:8192\n" \
"v_mfma_f32_16x16x32_bf16 a[112:115], a[152:155], a[212:215], a[112:115]\n" \
"v_mfma_f32_16x16x32_bf16 a[116:119], a[156:159], a[212:215], a[116:119]\n" \
"ds_read_b64_tr_b16 a[206:207], v241 offset:10240\n" \
"v_mfma_f32_16x16x32_bf16 a[116:119], a[140:143], a[208:211], a[116:119]\n" \
"v_mfma_f32_16x16x32_bf16 a[56:59], a[128:131], a[216:219], a[56:59]\n" \
"ds_read_b64_tr_b16 a[208:209], v240 offset:512\n" \
"v_mfma_f32_16x16x32_bf16 a[56:59], a[144:147], a[220:223], a[56:59]\n" \
"v_mfma_f32_16x16x32_bf16 a[60:63], a[148:151], a[220:223], a[60:63]\n" \
"s_add_u32 m0, s70, 50176\n" \
"ds_read_b64_tr_b16 a[210:211], v240 offset:2560\n" \
"v_mfma_f32_16x16x32_bf16 a[60:63], a[132:135], a[216:219], a[60:63]\n" \
"s_add_u32 s74, s69, 1572992\n" \
"v_mfma_f32_16x16x32_bf16 a[120:123], a[136:139], a[216:219], a[120:123]\n" \
"buffer_load_dwordx4 v246, s[28:31], s74 offen lds\n" \
"v_mfma_f32_16x16x32_bf16 a[120:123], a[152:155], a[220:223], a[120:123]\n" \
"ds_read_b64_tr_b16 a[212:213], v240 offset:8704\n" \
"v_mfma_f32_16x16x32_bf16 a[124:127], a[156:159], a[220:223], a[124:127]\n" \
"s_waitcnt vmcnt(21)\n" \
"ds_read_b64_tr_b16 a[214:215], v240 offset:10752\n" \
"v_mfma_f32_16x16x32_bf16 a[124:127], a[140:143], a[216:219], a[124:127]\n" \
"s_waitcnt lgkmcnt(8)\n" \
  \
"v_mfma_f32_16x16x32_bf16 v[0:3], a[192:195], v[192:195], 0\n" \
"ds_read_b64_tr_b16 a[216:217], v241 offset:512\n" \
"v_mfma_f32_16x16x32_bf16 v[0:3], a[196:199], a[160:163], v[0:3]\n" \
"buffer_load_dwordx4 a[128:131], v248, s[32:35], s79 offen offset:1024 sc1\n" \
"v_accvgpr_read_b32 v144, a0\n" \
"v_mfma_f32_16x16x32_bf16 v[32:35], a[196:199], a[164:167], 0\n" \
"ds_read_b64_tr_b16 a[218:219], v241 offset:2560\n" \
"v_accvgpr_read_b32 v145, a1\n" \
"v_mfma_f32_16x16x32_bf16 v[32:35], a[192:195], v[196:199], v[32:35]\n" \
"v_accvgpr_read_b32 v146, a2\n" \
"v_accvgpr_read_b32 v147, a3\n" \
"v_mfma_f32_16x16x32_bf16 v[64:67], a[192:195], v[200:203], 0\n" \
"ds_read_b64_tr_b16 a[220:221], v241 offset:8704\n" \
"v_accvgpr_read_b32 v148, a4\n" \
"v_mfma_f32_16x16x32_bf16 v[64:67], a[196:199], a[168:171], v[64:67]\n" \
"v_accvgpr_read_b32 v149, a5\n" \
"v_accvgpr_read_b32 v150, a6\n" \
"v_mfma_f32_16x16x32_bf16 v[96:99], a[196:199], a[172:175], 0\n" \
"ds_read_b64_tr_b16 a[222:223], v241 offset:10752\n" \
"v_accvgpr_read_b32 v151, a7\n" \
"v_mfma_f32_16x16x32_bf16 v[96:99], a[192:195], v[204:207], v[96:99]\n" \
"s_waitcnt lgkmcnt(8)\n" \
"v_max3_f32 v250, |v144|, |v145|, v250\n" \
"v_max3_f32 v250, |v146|, |v147|, v250\n" \
"v_mfma_f32_16x16x32_bf16 v[4:7], a[200:203], v[192:195], 0\n" \
"ds_read_b64_tr_b16 a[192:193], v240 offset:1024\n" \
"v_max3_f32 v250, |v148|, |v149|, v250\n" \
"v_mfma_f32_16x16x32_bf16 v[4:7], a[204:207], a[160:163], v[4:7]\n" \
"buffer_load_dwordx4 a[132:135], v248, s[32:35], s80 offen offset:1024 sc1\n" \
"v_max3_f32 v250, |v150|, |v151|, v250\n" \
"v_mfma_f32_16x16x32_bf16 v[36:39], a[204:207], a[164:167], 0\n" \
"s_add_u32 m0, s70, 57344\n" \
"ds_read_b64_tr_b16 a[194:195], v240 offset:3072\n" \
"v_cvt_pk_bf16_f32 v144, v144, v145\n" \
"v_mfma_f32_16x16x32_bf16 v[36:39], a[200:203], v[196:199], v[36:39]\n" \
"s_add_u32 s74, s69, 1835008\n" \
"v_cvt_pk_bf16_f32 v145, v146, v147\n" \
"v_cvt_pk_bf16_f32 v146, v148, v149\n" \
"v_mfma_f32_16x16x32_bf16 v[68:71], a[200:203], v[200:203], 0\n" \
"buffer_load_dwordx4 v246, s[28:31], s74 offen lds\n" \
"v_mfma_f32_16x16x32_bf16 v[68:71], a[204:207], a[168:171], v[68:71]\n" \
"ds_read_b64_tr_b16 a[196:197], v240 offset:9216\n" \
"v_cvt_pk_bf16_f32 v147, v150, v151\n" \
"v_mfma_f32_16x16x32_bf16 v[100:103], a[204:207], a[172:175], 0\n" \
"ds_read_b64_tr_b16 a[198:199], v240 offset:11264\n" \
"v_mfma_f32_16x16x32_bf16 v[100:103], a[200:203], v[204:207], v[100:103]\n" \
"s_waitcnt lgkmcnt(8)\n" \
"ds_write_b128 v249, v[144:147] offset:0\n" \
"v_accvgpr_read_b32 v152, a64\n" \
"v_mfma_f32_16x16x32_bf16 v[8:11], a[208:211], v[192:195], 0\n" \
"ds_read_b64_tr_b16 a[200:201], v241 offset:1024\n" \
"v_accvgpr_read_b32 v153, a65\n" \
"v_mfma_f32_16x16x32_bf16 v[8:11], a[212:215], a[160:163], v[8:11]\n" \
"buffer_load_dwordx4 a[136:139], v248, s[32:35], s36 offen offset:1024 sc1\n" \
"v_accvgpr_read_b32 v154, a66\n" \
"v_mfma_f32_16x16x32_bf16 v[40:43], a[212:215], a[164:167], 0\n" \
"ds_read_b64_tr_b16 a[202:203], v241 offset:3072\n" \
"v_accvgpr_read_b32 v155, a67\n" \
"v_mfma_f32_16x16x32_bf16 v[40:43], a[208:211], v[196:199], v[40:43]\n" \
"v_accvgpr_read_b32 v156, a68\n" \
"v_accvgpr_read_b32 v157, a69\n" \
"v_mfma_f32_16x16x32_bf16 v[72:75], a[208:211], v[200:203], 0\n" \
"s_add_u32 s85, s71, 589824\n" \
"ds_read_b64_tr_b16 a[204:205], v241 offset:9216\n" \
"v_accvgpr_read_b32 v158, a70\n" \
"v_mfma_f32_16x16x32_bf16 v[72:75], a[212:215], a[168:171], v[72:75]\n" \
"buffer_store_dwordx4 v[144:147], v248, s[32:35], s85 offen offset:0 sc1\n" \
"v_mfma_f32_16x16x32_bf16 v[104:107], a[212:215], a[172:175], 0\n" \
"s_add_u32 m0, s70, 58368\n" \
"ds_read_b64_tr_b16 a[206:207], v241 offset:11264\n" \
"v_accvgpr_read_b32 v159, a71\n" \
"v_mfma_f32_16x16x32_bf16 v[104:107], a[208:211], v[204:207], v[104:107]\n" \
"s_waitcnt lgkmcnt(9)\n" \
"v_max3_f32 v250, |v152|, |v153|, v250\n" \
"v_max3_f32 v250, |v154|, |v155|, v250\n" \
"v_mfma_f32_16x16x32_bf16 v[12:15], a[216:219], v[192:195], 0\n" \
"s_add_u32 s74, s69, 1835136\n" \
"buffer_load_dwordx4 v246, s[28:31], s74 offen lds\n" \
"v_mfma_f32_16x16x32_bf16 v[12:15], a[220:223], a[160:163], v[12:15]\n" \
"buffer_load_dwordx4 a[140:143], v248, s[32:35], s37 offen offset:1024 sc1\n" \
"v_max3_f32 v250, |v156|, |v157|, v250\n" \
"v_mfma_f32_16x16x32_bf16 v[44:47], a[220:223], a[164:167], 0\n" \
"buffer_load_dwordx4 a[144:147], v248, s[32:35], s79 offen offset:2048 sc1\n" \
"v_max3_f32 v250, |v158|, |v159|, v250\n" \
"v_mfma_f32_16x16x32_bf16 v[44:47], a[216:219], v[196:199], v[44:47]\n" \
"ds_read_b64_tr_b16 a[208:209], v240 offset:1536\n" \
"ds_read_b64_tr_b16 a[210:211], v240 offset:3584\n" \
"v_mfma_f32_16x16x32_bf16 v[76:79], a[216:219], v[200:203], 0\n" \
"ds_read_b64_tr_b16 a[212:213], v240 offset:9728\n" \
"v_cvt_pk_bf16_f32 v152, v152, v153\n" \
"v_mfma_f32_16x16x32_bf16 v[76:79], a[220:223], a[168:171], v[76:79]\n" \
"v_cvt_pk_bf16_f32 v153, v154, v155\n" \
"v_cvt_pk_bf16_f32 v154, v156, v157\n" \
"v_mfma_f32_16x16x32_bf16 v[108:111], a[220:223], a[172:175], 0\n" \
"ds_read_b64_tr_b16 a[214:215], v240 offset:11776\n" \
"v_cvt_pk_bf16_f32 v155, v158, v159\n" \
"v_mfma_f32_16x16x32_bf16 v[108:111], a[216:219], v[204:207], v[108:111]\n" \
"s_waitcnt lgkmcnt(9)\n" \
"v_accvgpr_write_b32 a64, v152\n" \
"v_accvgpr_write_b32 a65, v153\n" \
"v_mfma_f32_16x16x32_bf16 v[16:19], a[192:195], v[192:195], 0\n" \
"ds_read_b64_tr_b16 a[216:217], v241 offset:1536\n" \
"v_accvgpr_write_b32 a66, v154\n" \
"v_mfma_f32_16x16x32_bf16 v[16:19], a[196:199], a[160:163], v[16:19]\n" \
"v_accvgpr_write_b32 a67, v155\n" \
"v_mfma_f32_16x16x32_bf16 v[48:51], a[196:199], a[164:167], 0\n" \
"buffer_load_dwordx4 a[148:151], v248, s[32:35], s80 offen offset:2048 sc1\n" \
"v_mfma_f32_16x16x32_bf16 v[48:51], a[192:195], v[196:199], v[48:51]\n" \
"ds_read_b64_tr_b16 a[218:219], v241 offset:3584\n" \
"ds_read_b64_tr_b16 a[220:221], v241 offset:9728\n" \
"v_mfma_f32_16x16x32_bf16 v[80:83], a[192:195], v[200:203], 0\n" \
"ds_read_b64_tr_b16 a[222:223], v241 offset:11776\n" \
"v_mfma_f32_16x16x32_bf16 v[80:83], a[196:199], a[168:171], v[80:83]\n" \
"v_mfma_f32_16x16x32_bf16 v[112:115], a[196:199], a[172:175], 0\n" \
"v_mfma_f32_16x16x32_bf16 v[112:115], a[192:195], v[204:207], v[112:115]\n" \
"s_waitcnt lgkmcnt(8)\n" \
"v_mfma_f32_16x16x32_bf16 v[20:23], a[200:203], v[192:195], 0\n" \
"v_mfma_f32_16x16x32_bf16 v[20:23], a[204:207], a[160:163], v[20:23]\n" \
"v_mfma_f32_16x16x32_bf16 v[52:55], a[204:207], a[164:167], 0\n" \
"buffer_load_dwordx4 a[152:155], v248, s[32:35], s36 offen offset:2048 sc1\n" \
"v_mfma_f32_16x16x32_bf16 v[52:55], a[200:203], v[196:199], v[52:55]\n" \
"v_mfma_f32_16x16x32_bf16 v[84:87], a[200:203], v[200:203], 0\n" \
"s_waitcnt lgkmcnt(0)\n" \
"s_barrier\n" \
"s_add_u32 m0, s70, 65536\n" \
"v_mfma_f32_16x16x32_bf16 v[84:87], a[204:207], a[168:171], v[84:87]\n" \
"s_add_u32 s74, s69, 2097152\n" \
"ds_read_b64_tr_b16 a[192:193], v240 offset:16384\n" \
"ds_read_b64_tr_b16 a[194:195], v240 offset:18432\n" \
"v_mfma_f32_16x16x32_bf16 v[116:119], a[204:207], a[172:175], 0\n" \
"buffer_load_dwordx4 v246, s[28:31], s74 offen lds\n" \
"v_mfma_f32_16x16x32_bf16 v[116:119], a[200:203], v[204:207], v[116:119]\n" \
"ds_read_b64_tr_b16 a[196:197], v240 offset:24576\n" \
"ds_read_b64_tr_b16 a[198:199], v240 offset:26624\n" \
"v_mfma_f32_16x16x32_bf16 v[24:27], a[208:211], v[192:195], 0\n" \
"ds_read_b64_tr_b16 a[200:201], v241 offset:16384\n" \
"v_mfma_f32_16x16x32_bf16 v[24:27], a[212:215], a[160:163], v[24:27]\n" \
"v_mfma_f32_16x16x32_bf16 v[56:59], a[212:215], a[164:167], 0\n" \
"s_add_u32 s85, s94, 0\n" \
"buffer_load_dwordx4 a[156:159], v248, s[32:35], s37 offen offset:2048 sc1\n" \
"v_mfma_f32_16x16x32_bf16 v[56:59], a[208:211], v[196:199], v[56:59]\n" \
"ds_read_b64_tr_b16 a[202:203], v241 offset:18432\n" \
"v_mfma_f32_16x16x32_bf16 v[88:91], a[208:211], v[200:203], 0\n" \
"ds_read_b64_tr_b16 a[204:205], v241 offset:24576\n" \
"v_mfma_f32_16x16x32_bf16 v[88:91], a[212:215], a[168:171], v[88:91]\n" \
"buffer_store_dwordx4 v[160:163], v248, s[32:35], s85 offen offset:0 sc1\n" \
"v_mfma_f32_16x16x32_bf16 v[120:123], a[212:215], a[172:175], 0\n" \
"ds_read_b64_tr_b16 a[206:207], v241 offset:26624\n" \
"v_mfma_f32_16x16x32_bf16 v[120:123], a[208:211], v[204:207], v[120:123]\n" \
"v_mfma_f32_16x16x32_bf16 v[28:31], a[216:219], v[192:195], 0\n" \
"ds_read_b64_tr_b16 a[208:209], v240 offset:16896\n" \
"v_mfma_f32_16x16x32_bf16 v[28:31], a[220:223], a[160:163], v[28:31]\n" \
"v_mfma_f32_16x16x32_bf16 v[60:63], a[220:223], a[164:167], 0\n" \
"s_add_u32 m0, s70, 66560\n" \
"ds_read_b64_tr_b16 a[210:211], v240 offset:18944\n" \
"v_mfma_f32_16x16x32_bf16 v[60:63], a[216:219], v[196:199], v[60:63]\n" \
"s_add_u32 s74, s69, 2097280\n" \
"v_mfma_f32_16x16x32_bf16 v[92:95], a[216:219], v[200:203], 0\n" \
"buffer_load_dwordx4 v246, s[28:31], s74 offen lds\n" \
"v_mfma_f32_16x16x32_bf16 v[92:95], a[220:223], a[168:171], v[92:95]\n" \
"s_lshl_b32 s85, s68, 4\n" \
"ds_read_b64_tr_b16 a[212:213], v240 offset:25088\n" \
"v_accvgpr_read_b32 v144, a8\n" \
"v_mfma_f32_16x16x32_bf16 v[124:127], a[220:223], a[172:175], 0\n" \
"s_waitcnt vmcnt(21)\n" \
"ds_read_b64_tr_b16 a[214:215], v240 offset:27136\n" \
"v_mfma_f32_16x16x32_bf16 v[124:127], a[216:219], v[204:207], v[124:127]\n" \
"s_waitcnt lgkmcnt(8)\n" \
"v_accvgpr_read_b32 v145, a9\n" \
"v_accvgpr_read_b32 v146, a10\n" \
"v_mfma_f32_16x16x32_bf16 v[0:3], a[192:195], a[176:179], v[0:3]\n" \
"s_add_u32 s85, s85, s67\n" \
"ds_read_b64_tr_b16 a[216:217], v241 offset:16896\n" \
"v_accvgpr_read_b32 v147, a11\n" \
"v_mfma_f32_16x16x32_bf16 v[0:3], a[196:199], a[224:227], v[0:3]\n" \
"s_lshl2_add_u32 s85, s85, %[wid]\n" \
"buffer_load_dwordx4 v[192:195], v248, s[32:35], s79 offen offset:3072 sc1\n" \
"v_accvgpr_read_b32 v148, a12\n" \
"v_mfma_f32_16x16x32_bf16 v[32:35], a[196:199], a[228:231], v[32:35]\n" \
"s_lshl_b32 s85, s85, 2\n" \
"ds_read_b64_tr_b16 a[218:219], v241 offset:18944\n" \
"v_accvgpr_read_b32 v149, a13\n" \
"v_mfma_f32_16x16x32_bf16 v[32:35], a[192:195], a[180:183], v[32:35]\n" \
"s_waitcnt vmcnt(19)\n" \
"s_add_u32 s85, s85, 81920\n" \
"buffer_store_dword v239, off, s[32:35], s85 sc1\n" \
"v_mfma_f32_16x16x32_bf16 v[64:67], a[192:195], a[184:187], v[64:67]\n" \
"ds_read_b64_tr_b16 a[220:221], v241 offset:25088\n" \
"v_accvgpr_read_b32 v150, a14\n" \
"v_mfma_f32_16x16x32_bf16 v[64:67], a[196:199], a[232:235], v[64:67]\n" \
"v_accvgpr_read_b32 v151, a15\n" \
"v_max3_f32 v250, |v144|, |v145|, v250\n" \
"v_mfma_f32_16x16x32_bf16 v[96:99], a[196:199], a[236:239], v[96:99]\n" \
"ds_read_b64_tr_b16 a[222:223], v241 offset:27136\n" \
"v_max3_f32 v250, |v146|, |v147|, v250\n" \
"v_mfma_f32_16x16x32_bf16 v[96:99], a[192:195], a[188:191], v[96:99]\n" \
"s_waitcnt lgkmcnt(8)\n" \
"v_max3_f32 v250, |v148|, |v149|, v250\n" \
"v_max3_f32 v250, |v150|, |v151|, v250\n" \
"v_mfma_f32_16x16x32_bf16 v[4:7], a[200:203], a[176:179], v[4:7]\n" \
"ds_read_b64_tr_b16 a[192:193], v240 offset:17408\n" \
"v_cvt_pk_bf16_f32 v144, v144, v145\n" \
"v_mfma_f32_16x16x32_bf16 v[4:7], a[204:207], a[224:227], v[4:7]\n" \
"buffer_load_dwordx4 v[196:199], v248, s[32:35], s80 offen offset:3072 sc1\n" \
"v_cvt_pk_bf16_f32 v145, v146, v147\n" \
"v_mfma_f32_16x16x32_bf16 v[36:39], a[204:207], a[228:231], v[36:39]\n" \
"s_add_u32 m0, s70, 73728\n" \
"ds_read_b64_tr_b16 a[194:195], v240 offset:19456\n" \
"v_cvt_pk_bf16_f32 v146, v148, v149\n" \
"v_mfma_f32_16x16x32_bf16 v[36:39], a[200:203], a[180:183], v[36:39]\n" \
"s_add_u32 s74, s69, 2359296\n" \
"v_cvt_pk_bf16_f32 v147, v150, v151\n" \
"v_mfma_f32_16x16x32_bf16 v[68:71], a[200:203], a[184:187], v[68:71]\n" \
"buffer_load_dwordx4 v246, s[28:31], s74 offen lds\n" \
"v_mfma_f32_16x16x32_bf16 v[68:71], a[204:207], a[232:235], v[68:71]\n" \
"ds_read_b64_tr_b16 a[196:197], v240 offset:25600\n" \
"v_mfma_f32_16x16x32_bf16 v[100:103], a[204:207], a[236:239], v[100:103]\n" \
"ds_read_b64_tr_b16 a[198:199], v240 offset:27648\n" \
"v_mfma_f32_16x16x32_bf16 v[100:103], a[200:203], a[188:191], v[100:103]\n" \
"s_waitcnt lgkmcnt(8)\n" \
"ds_write_b128 v249, v[144:147] offset:1024\n" \
"v_accvgpr_read_b32 v152, a72\n" \
"v_mfma_f32_16x16x32_bf16 v[8:11], a[208:211], a[176:179], v[8:11]\n" \
"s_add_u32 s85, s71, 589824\n" \
"ds_read_b64_tr_b16 a[200:201], v241 offset:17408\n" \
"v_accvgpr_read_b32 v153, a73\n" \
"v_mfma_f32_16x16x32_bf16 v[8:11], a[212:215], a[224:227], v[8:11]\n" \
"buffer_load_dwordx4 v[200:203], v248, s[32:35], s36 offen offset:3072 sc1\n" \
"v_accvgpr_read_b32 v154, a74\n" \
"v_mfma_f32_16x16x32_bf16 v[40:43], a[212:215], a[228:231], v[40:43]\n" \
"ds_read_b64_tr_b16 a[202:203], v241 offset:19456\n" \
"v_accvgpr_read_b32 v155, a75\n" \
"v_mfma_f32_16x16x32_bf16 v[40:43], a[208:211], a[180:183], v[40:43]\n" \
"v_accvgpr_read_b32 v156, a76\n" \
"v_accvgpr_read_b32 v157, a77\n" \
"v_mfma_f32_16x16x32_bf16 v[72:75], a[208:211], a[184:187], v[72:75]\n" \
"buffer_store_dwordx4 v[144:147], v248, s[32:35], s85 offen offset:1024 sc1\n" \
"v_mfma_f32_16x16x32_bf16 v[72:75], a[212:215], a[232:235], v[72:75]\n" \
"ds_read_b64_tr_b16 a[204:205], v241 offset:25600\n" \
"v_accvgpr_read_b32 v158, a78\n" \
"v_mfma_f32_16x16x32_bf16 v[104:107], a[212:215], a[236:239], v[104:107]\n" \
"s_add_u32 m0, s70, 74752\n" \
"ds_read_b64_tr_b16 a[206:207], v241 offset:27648\n" \
"v_accvgpr_read_b32 v159, a79\n" \
"v_mfma_f32_16x16x32_bf16 v[104:107], a[208:211], a[188:191], v[104:107]\n" \
"s_waitcnt lgkmcnt(9)\n" \
"v_max3_f32 v250, |v152|, |v153|, v250\n" \
"v_max3_f32 v250, |v154|, |v155|, v250\n" \
"v_mfma_f32_16x16x32_bf16 v[12:15], a[216:219], a[176:179], v[12:15]\n" \
"s_add_u32 s74, s69, 2359424\n" \
"buffer_load_dwordx4 v246, s[28:31], s74 offen lds\n" \
"v_mfma_f32_16x16x32_bf16 v[12:15], a[220:223], a[224:227], v[12:15]\n" \
"buffer_load_dwordx4 v[204:207], v248, s[32:35], s37 offen offset:3072 sc1\n" \
"v_max3_f32 v250, |v156|, |v157|, v250\n" \
"v_mfma_f32_16x16x32_bf16 v[44:47], a[220:223], a[228:231], v[44:47]\n" \
"s_add_u32 s79, s79, 4096\n" \
"buffer_load_dwordx4 a[160:163], v248, s[32:35], s79 offen offset:0 sc1\n" \
"v_max3_f32 v250, |v158|, |v159|, v250\n" \
"v_mfma_f32_16x16x32_bf16 v[44:47], a[216:219], a[180:183], v[44:47]\n" \
"ds_read_b64_tr_b16 a[208:209], v240 offset:17920\n" \
"ds_read_b64_tr_b16 a[210:211], v240 offset:19968\n" \
"v_mfma_f32_16x16x32_bf16 v[76:79], a[216:219], a[184:187], v[76:79]\n" \
"ds_read_b64_tr_b16 a[212:213], v240 offset:26112\n" \
"v_cvt_pk_bf16_f32 v152, v152, v153\n" \
"v_mfma_f32_16x16x32_bf16 v[76:79], a[220:223], a[232:235], v[76:79]\n" \
"v_cvt_pk_bf16_f32 v153, v154, v155\n" \
"v_cvt_pk_bf16_f32 v154, v156, v157\n" \
"v_mfma_f32_16x16x32_bf16 v[108:111], a[220:223], a[236:239], v[108:111]\n" \
".Lst0_5_%=:\n" \
"s_cbranch_execz .Lst0_9_%=\n" \
"ds_read_b64_tr_b16 a[214:215], v240 offset:28160\n" \
"v_cvt_pk_bf16_f32 v155, v158, v159\n" \
"v_mfma_f32_16x16x32_bf16 v[108:111], a[216:219], a[188:191], v[108:111]\n" \
"s_waitcnt lgkmcnt(9)\n" \
"v_accvgpr_write_b32 a72, v152\n" \
"v_accvgpr_write_b32 a73, v153\n" \
"v_mfma_f32_16x16x32_bf16 v[16:19], a[192:195], a[176:179], v[16:19]\n" \
"ds_read_b64_tr_b16 a[216:217], v241 offset:17920\n" \
"v_accvgpr_write_b32 a74, v154\n" \
"v_mfma_f32_16x16x32_bf16 v[16:19], a[196:199], a[224:227], v[16:19]\n" \
"s_add_u32 s80, s80, 4096\n" \
"v_accvgpr_write_b32 a75, v155\n" \
"v_mfma_f32_16x16x32_bf16 v[48:51], a[196:199], a[228:231], v[48:51]\n" \
"buffer_load_dwordx4 a[164:167], v248, s[32:35], s80 offen offset:0 sc1\n" \
"v_mfma_f32_16x16x32_bf16 v[48:51], a[192:195], a[180:183], v[48:51]\n" \
"ds_read_b64_tr_b16 a[218:219], v241 offset:19968\n" \
"ds_read_b64_tr_b16 a[220:221], v241 offset:26112\n" \
"v_mfma_f32_16x16x32_bf16 v[80:83], a[192:195], a[184:187], v[80:83]\n" \
"ds_read_b64_tr_b16 a[222:223], v241 offset:28160\n" \
"v_xor_b32 v240, 0x30000, v240\n" \
"v_mfma_f32_16x16x32_bf16 v[80:83], a[196:199], a[232:235], v[80:83]\n" \
"v_xor_b32 v241, 0x30000, v241\n" \
"v_mfma_f32_16x16x32_bf16 v[112:115], a[196:199], a[236:239], v[112:115]\n" \
"v_mfma_f32_16x16x32_bf16 v[112:115], a[192:195], a[188:191], v[112:115]\n" \
"s_waitcnt lgkmcnt(8)\n" \
"v_mfma_f32_16x16x32_bf16 v[20:23], a[200:203], a[176:179], v[20:23]\n" \
"v_mfma_f32_16x16x32_bf16 v[20:23], a[204:207], a[224:227], v[20:23]\n" \
"s_add_u32 s36, s36, 4096\n" \
"v_mfma_f32_16x16x32_bf16 v[52:55], a[204:207], a[228:231], v[52:55]\n" \
"buffer_load_dwordx4 a[168:171], v248, s[32:35], s36 offen offset:0 sc1\n" \
"v_mfma_f32_16x16x32_bf16 v[52:55], a[200:203], a[180:183], v[52:55]\n" \
"v_mfma_f32_16x16x32_bf16 v[84:87], a[200:203], a[184:187], v[84:87]\n" \
"s_waitcnt lgkmcnt(0)\n" \
"s_barrier\n" \
"s_add_u32 m0, s70, 81920\n" \
"v_mfma_f32_16x16x32_bf16 v[84:87], a[204:207], a[232:235], v[84:87]\n" \
"s_add_u32 s74, s69, 2621440\n" \
"ds_read_b64_tr_b16 a[192:193], v240 offset:32768\n" \
"ds_read_b64_tr_b16 a[194:195], v240 offset:34816\n" \
"v_mfma_f32_16x16x32_bf16 v[116:119], a[204:207], a[236:239], v[116:119]\n" \
"s_add_u32 s85, s94, 0\n" \
"buffer_load_dwordx4 v246, s[28:31], s74 offen lds\n" \
"v_mfma_f32_16x16x32_bf16 v[116:119], a[200:203], a[188:191], v[116:119]\n" \
"ds_read_b64_tr_b16 a[196:197], v240 offset:40960\n" \
"ds_read_b64_tr_b16 a[198:199], v240 offset:43008\n" \
"v_mfma_f32_16x16x32_bf16 v[24:27], a[208:211], a[176:179], v[24:27]\n" \
"ds_read_b64_tr_b16 a[200:201], v241 offset:32768\n" \
"v_mfma_f32_16x16x32_bf16 v[24:27], a[212:215], a[224:227], v[24:27]\n" \
"s_add_u32 s37, s37, 4096\n" \
"buffer_store_dwordx4 v[164:167], v248, s[32:35], s85 offen offset:1024 sc1\n" \
"v_mfma_f32_16x16x32_bf16 v[56:59], a[212:215], a[228:231], v[56:59]\n" \
"buffer_load_dwordx4 a[172:175], v248, s[32:35], s37 offen offset:0 sc1\n" \
"v_mfma_f32_16x16x32_bf16 v[56:59], a[208:211], a[180:183], v[56:59]\n" \
"ds_read_b64_tr_b16 a[202:203], v241 offset:34816\n" \
"v_mfma_f32_16x16x32_bf16 v[88:91], a[208:211], a[184:187], v[88:91]\n" \
"ds_read_b64_tr_b16 a[204:205], v241 offset:40960\n" \
"v_mfma_f32_16x16x32_bf16 v[88:91], a[212:215], a[232:235], v[88:91]\n" \
"v_mfma_f32_16x16x32_bf16 v[120:123], a[212:215], a[236:239], v[120:123]\n" \
"ds_read_b64_tr_b16 a[206:207], v241 offset:43008\n" \
"v_mfma_f32_16x16x32_bf16 v[120:123], a[208:211], a[188:191], v[120:123]\n" \
"v_mfma_f32_16x16x32_bf16 v[28:31], a[216:219], a[176:179], v[28:31]\n" \
"ds_read_b64_tr_b16 a[208:209], v240 offset:33280\n" \
"v_mfma_f32_16x16x32_bf16 v[28:31], a[220:223], a[224:227], v[28:31]\n" \
"v_accvgpr_read_b32 v144, a16\n" \
"v_accvgpr_read_b32 v145, a17\n" \
"v_mfma_f32_16x16x32_bf16 v[60:63], a[220:223], a[228:231], v[60:63]\n" \
"s_add_u32 m0, s70, 82944\n" \
"ds_read_b64_tr_b16 a[210:211], v240 offset:35328\n" \
"v_accvgpr_read_b32 v146, a18\n" \
"v_mfma_f32_16x16x32_bf16 v[60:63], a[216:219], a[180:183], v[60:63]\n" \
"s_add_u32 s74, s69, 2621568\n" \
"v_accvgpr_read_b32 v147, a19\n" \
"v_accvgpr_read_b32 v148, a20\n" \
"v_mfma_f32_16x16x32_bf16 v[92:95], a[216:219], a[184:187], v[92:95]\n" \
"buffer_load_dwordx4 v246, s[28:31], s74 offen lds\n" \
"v_mfma_f32_16x16x32_bf16 v[92:95], a[220:223], a[232:235], v[92:95]\n" \
"ds_read_b64_tr_b16 a[212:213], v240 offset:41472\n" \
"v_accvgpr_read_b32 v149, a21\n" \
"v_mfma_f32_16x16x32_bf16 v[124:127], a[220:223], a[236:239], v[124:127]\n" \
"s_waitcnt vmcnt(22)\n" \
"ds_read_b64_tr_b16 a[214:215], v240 offset:43520\n" \
"v_mfma_f32_16x16x32_bf16 v[124:127], a[216:219], a[188:191], v[124:127]\n" \
"s_waitcnt lgkmcnt(8)\n" \
"v_accvgpr_read_b32 v150, a22\n" \
"v_accvgpr_read_b32 v151, a23\n" \
"v_mfma_f32_16x16x32_bf16 v[0:3], a[192:195], a[240:243], v[0:3]\n" \
"ds_read_b64_tr_b16 a[216:217], v241 offset:33280\n" \
"v_max3_f32 v250, |v144|, |v145|, v250\n" \
"v_mfma_f32_16x16x32_bf16 v[0:3], a[196:199], a[128:131], v[0:3]\n" \
"buffer_load_dwordx4 a[176:179], v248, s[32:35], s79 offen offset:1024 sc1\n" \
"v_max3_f32 v250, |v146|, |v147|, v250\n" \
"v_mfma_f32_16x16x32_bf16 v[32:35], a[196:199], a[132:135], v[32:35]\n" \
"ds_read_b64_tr_b16 a[218:219], v241 offset:35328\n" \
"v_max3_f32 v250, |v148|, |v149|, v250\n" \
"v_mfma_f32_16x16x32_bf16 v[32:35], a[192:195], a[244:247], v[32:35]\n" \
"v_max3_f32 v250, |v150|, |v151|, v250\n" \
"v_cvt_pk_bf16_f32 v144, v144, v145\n" \
"v_mfma_f32_16x16x32_bf16 v[64:67], a[192:195], a[248:251], v[64:67]\n" \
"ds_read_b64_tr_b16 a[220:221], v241 offset:41472\n" \
"v_cvt_pk_bf16_f32 v145, v146, v147\n" \
"v_mfma_f32_16x16x32_bf16 v[64:67], a[196:199], a[136:139], v[64:67]\n" \
"v_cvt_pk_bf16_f32 v146, v148, v149\n" \
"v_cvt_pk_bf16_f32 v147, v150, v151\n" \
"v_mfma_f32_16x16x32_bf16 v[96:99], a[196:199], a[140:143], v[96:99]\n" \
"ds_read_b64_tr_b16 a[222:223], v241 offset:43520\n" \
"v_mfma_f32_16x16x32_bf16 v[96:99], a[192:195], a[252:255], v[96:99]\n" \
"s_waitcnt lgkmcnt(8)\n" \
"ds_write_b128 v249, v[144:147] offset:2048\n" \
"v_accvgpr_read_b32 v152, a80\n" \
"v_mfma_f32_16x16x32_bf16 v[4:7], a[200:203], a[240:243], v[4:7]\n" \
"ds_read_b64_tr_b16 a[192:193], v240 offset:33792\n" \
"v_accvgpr_read_b32 v153, a81\n" \
"v_mfma_f32_16x16x32_bf16 v[4:7], a[204:207], a[128:131], v[4:7]\n" \
"buffer_load_dwordx4 a[180:183], v248, s[32:35], s80 offen offset:1024 sc1\n" \
"v_accvgpr_read_b32 v154, a82\n" \
"v_mfma_f32_16x16x32_bf16 v[36:39], a[204:207], a[132:135], v[36:39]\n" \
"s_add_u32 m0, s70, 90112\n" \
"ds_read_b64_tr_b16 a[194:195], v240 offset:35840\n" \
"v_accvgpr_read_b32 v155, a83\n" \
"v_mfma_f32_16x16x32_bf16 v[36:39], a[200:203], a[244:247], v[36:39]\n" \
"s_add_u32 s74, s69, 2883584\n" \
"v_accvgpr_read_b32 v156, a84\n" \
"v_accvgpr_read_b32 v157, a85\n" \
"v_mfma_f32_16x16x32_bf16 v[68:71], a[200:203], a[248:251], v[68:71]\n" \
"s_add_u32 s85, s71, 589824\n" \
"buffer_load_dwordx4 v246, s[28:31], s74 offen lds\n" \
"v_mfma_f32_16x16x32_bf16 v[68:71], a[204:207], a[136:139], v[68:71]\n" \
"ds_read_b64_tr_b16 a[196:197], v240 offset:41984\n" \
"v_accvgpr_read_b32 v158, a86\n" \
"v_mfma_f32_16x16x32_bf16 v[100:103], a[204:207], a[140:143], v[100:103]\n" \
"ds_read_b64_tr_b16 a[198:199], v240 offset:44032\n" \
"v_accvgpr_read_b32 v159, a87\n" \
"v_mfma_f32_16x16x32_bf16 v[100:103], a[200:203], a[252:255], v[100:103]\n" \
"s_waitcnt lgkmcnt(9)\n" \
"buffer_store_dwordx4 v[144:147], v248, s[32:35], s85 offen offset:2048 sc1\n" \
"v_mfma_f32_16x16x32_bf16 v[8:11], a[208:211], a[240:243], v[8:11]\n" \
"ds_read_b64_tr_b16 a[200:201], v241 offset:33792\n" \
"v_max3_f32 v250, |v152|, |v153|, v250\n" \
"v_mfma_f32_16x16x32_bf16 v[8:11], a[212:215], a[128:131], v[8:11]\n" \
"buffer_load_dwordx4 a[184:187], v248, s[32:35], s36 offen offset:1024 sc1\n" \
"v_max3_f32 v250, |v154|, |v155|, v250\n" \
"v_mfma_f32_16x16x32_bf16 v[40:43], a[212:215], a[132:135], v[40:43]\n" \
"ds_read_b64_tr_b16 a[202:203], v241 offset:35840\n" \
"v_max3_f32 v250, |v156|, |v157|, v250\n" \
"v_mfma_f32_16x16x32_bf16 v[40:43], a[208:211], a[244:247], v[40:43]\n" \
"v_max3_f32 v250, |v158|, |v159|, v250\n" \
"v_cvt_pk_bf16_f32 v152, v152, v153\n" \
"v_mfma_f32_16x16x32_bf16 v[72:75], a[208:211], a[248:251], v[72:75]\n" \
"ds_read_b64_tr_b16 a[204:205], v241 offset:41984\n" \
"v_cvt_pk_bf16_f32 v153, v154, v155\n" \
"v_mfma_f32_16x16x32_bf16 v[72:75], a[212:215], a[136:139], v[72:75]\n" \
"v_cvt_pk_bf16_f32 v154, v156, v157\n" \
"v_cvt_pk_bf16_f32 v155, v158, v159\n" \
"v_mfma_f32_16x16x32_bf16 v[104:107], a[212:215], a[140:143], v[104:107]\n" \
"s_add_u32 m0, s70, 91136\n" \
"ds_read_b64_tr_b16 a[206:207], v241 offset:44032\n" \
"v_accvgpr_write_b32 a80, v152\n" \
"v_mfma_f32_16x16x32_bf16 v[104:107], a[208:211], a[252:255], v[104:107]\n" \
"s_waitcnt lgkmcnt(9)\n" \
"v_accvgpr_write_b32 a81, v153\n" \
"v_accvgpr_write_b32 a82, v154\n" \
"v_mfma_f32_16x16x32_bf16 v[12:15], a[216:219], a[240:243], v[12:15]\n" \
"s_add_u32 s74, s69, 2883712\n" \
"buffer_load_dwordx4 v246, s[28:31], s74 offen lds\n" \
"v_mfma_f32_16x16x32_bf16 v[12:15], a[220:223], a[128:131], v[12:15]\n" \
"buffer_load_dwordx4 a[188:191], v248, s[32:35], s37 offen offset:1024 sc1\n" \
"v_accvgpr_write_b32 a83, v155\n" \
"v_mfma_f32_16x16x32_bf16 v[44:47], a[220:223], a[132:135], v[44:47]\n" \
"buffer_load_dwordx4 a[0:3], v248, s[32:35], s79 offen offset:2048 sc1\n" \
"v_mfma_f32_16x16x32_bf16 v[44:47], a[216:219], a[244:247], v[44:47]\n" \
"ds_read_b64_tr_b16 a[208:209], v240 offset:34304\n" \
"ds_read_b64_tr_b16 a[210:211], v240 offset:36352\n" \
"v_mfma_f32_16x16x32_bf16 v[76:79], a[216:219], a[248:251], v[76:79]\n" \
"ds_read_b64_tr_b16 a[212:213], v240 offset:42496\n" \
"v_mfma_f32_16x16x32_bf16 v[76:79], a[220:223], a[136:139], v[76:79]\n" \
"v_mfma_f32_16x16x32_bf16 v[108:111], a[220:223], a[140:143], v[108:111]\n" \
"ds_read_b64_tr_b16 a[214:215], v240 offset:44544\n" \
"v_mfma_f32_16x16x32_bf16 v[108:111], a[216:219], a[252:255], v[108:111]\n" \
"s_waitcnt lgkmcnt(8)\n" \
"v_mfma_f32_16x16x32_bf16 v[16:19], a[192:195], a[240:243], v[16:19]\n" \
"ds_read_b64_tr_b16 a[216:217], v241 offset:34304\n" \
"v_mfma_f32_16x16x32_bf16 v[16:19], a[196:199], a[128:131], v[16:19]\n" \
"v_mfma_f32_16x16x32_bf16 v[48:51], a[196:199], a[132:135], v[48:51]\n" \
"buffer_load_dwordx4 a[4:7], v248, s[32:35], s80 offen offset:2048 sc1\n" \
"v_mfma_f32_16x16x32_bf16 v[48:51], a[192:195], a[244:247], v[48:51]\n" \
"ds_read_b64_tr_b16 a[218:219], v241 offset:36352\n" \
"ds_read_b64_tr_b16 a[220:221], v241 offset:42496\n" \
"v_mfma_f32_16x16x32_bf16 v[80:83], a[192:195], a[248:251], v[80:83]\n" \
"ds_read_b64_tr_b16 a[222:223], v241 offset:44544\n" \
"v_mfma_f32_16x16x32_bf16 v[80:83], a[196:199], a[136:139], v[80:83]\n" \
"v_mfma_f32_16x16x32_bf16 v[112:115], a[196:199], a[140:143], v[112:115]\n" \
"v_mfma_f32_16x16x32_bf16 v[112:115], a[192:195], a[252:255], v[112:115]\n" \
"s_waitcnt lgkmcnt(8)\n" \
"v_mfma_f32_16x16x32_bf16 v[20:23], a[200:203], a[240:243], v[20:23]\n" \
"v_mfma_f32_16x16x32_bf16 v[20:23], a[204:207], a[128:131], v[20:23]\n" \
"v_mfma_f32_16x16x32_bf16 v[52:55], a[204:207], a[132:135], v[52:55]\n" \
"s_add_u32 s85, s94, 0\n" \
"buffer_load_dwordx4 a[8:11], v248, s[32:35], s36 offen offset:2048 sc1\n" \
"v_mfma_f32_16x16x32_bf16 v[52:55], a[200:203], a[244:247], v[52:55]\n" \
"v_mfma_f32_16x16x32_bf16 v[84:87], a[200:203], a[248:251], v[84:87]\n" \
"s_waitcnt lgkmcnt(0)\n" \
"s_barrier\n" \
"s_add_u32 m0, s70, 32768\n" \
"v_mfma_f32_16x16x32_bf16 v[84:87], a[204:207], a[136:139], v[84:87]\n" \
"s_add_u32 s74, s69, 3145728\n" \
"buffer_store_dwordx4 v[168:171], v248, s[32:35], s85 offen offset:2048 sc1\n" \
"v_mfma_f32_16x16x32_bf16 v[116:119], a[204:207], a[140:143], v[116:119]\n" \
"ds_read_b64_tr_b16 a[192:193], v240 offset:49152\n" \
"ds_read_b64_tr_b16 a[194:195], v240 offset:51200\n" \
"v_mfma_f32_16x16x32_bf16 v[116:119], a[200:203], a[252:255], v[116:119]\n" \
"ds_read_b64_tr_b16 a[196:197], v240 offset:57344\n" \
"ds_read_b64_tr_b16 a[198:199], v240 offset:59392\n" \
"v_mfma_f32_16x16x32_bf16 v[24:27], a[208:211], a[240:243], v[24:27]\n" \
"buffer_load_dwordx4 v246, s[28:31], s74 offen lds\n" \
"v_mfma_f32_16x16x32_bf16 v[24:27], a[212:215], a[128:131], v[24:27]\n" \
"ds_read_b64_tr_b16 a[200:201], v241 offset:49152\n" \
"v_mfma_f32_16x16x32_bf16 v[56:59], a[212:215], a[132:135], v[56:59]\n" \
"buffer_load_dwordx4 a[12:15], v248, s[32:35], s37 offen offset:2048 sc1\n" \
"v_mfma_f32_16x16x32_bf16 v[56:59], a[208:211], a[244:247], v[56:59]\n" \
"ds_read_b64_tr_b16 a[202:203], v241 offset:51200\n" \
"v_mfma_f32_16x16x32_bf16 v[88:91], a[208:211], a[248:251], v[88:91]\n" \
"ds_read_b64_tr_b16 a[204:205], v241 offset:57344\n" \
"v_mfma_f32_16x16x32_bf16 v[88:91], a[212:215], a[136:139], v[88:91]\n" \
"v_accvgpr_read_b32 v144, a24\n" \
"v_accvgpr_read_b32 v145, a25\n" \
"v_mfma_f32_16x16x32_bf16 v[120:123], a[212:215], a[140:143], v[120:123]\n" \
"ds_read_b64_tr_b16 a[206:207], v241 offset:59392\n" \
"v_accvgpr_read_b32 v146, a26\n" \
"v_mfma_f32_16x16x32_bf16 v[120:123], a[208:211], a[252:255], v[120:123]\n" \
"v_accvgpr_read_b32 v147, a27\n" \
"v_accvgpr_read_b32 v148, a28\n" \
"v_mfma_f32_16x16x32_bf16 v[28:31], a[216:219], a[240:243], v[28:31]\n" \
"ds_read_b64_tr_b16 a[208:209], v240 offset:49664\n" \
"v_accvgpr_read_b32 v149, a29\n" \
"v_mfma_f32_16x16x32_bf16 v[28:31], a[220:223], a[128:131], v[28:31]\n" \
"v_accvgpr_read_b32 v150, a30\n" \
"v_accvgpr_read_b32 v151, a31\n" \
"v_mfma_f32_16x16x32_bf16 v[60:63], a[220:223], a[132:135], v[60:63]\n" \
"ds_read_b64_tr_b16 a[210:211], v240 offset:51712\n" \
"v_max3_f32 v250, |v144|, |v145|, v250\n" \
"v_mfma_f32_16x16x32_bf16 v[60:63], a[216:219], a[244:247], v[60:63]\n" \
"v_max3_f32 v250, |v146|, |v147|, v250\n" \
"v_max3_f32 v250, |v148|, |v149|, v250\n" \
"v_mfma_f32_16x16x32_bf16 v[92:95], a[216:219], a[248:251], v[92:95]\n" \
"ds_read_b64_tr_b16 a[212:213], v240 offset:57856\n" \
"v_max3_f32 v250, |v150|, |v151|, v250\n" \
"v_mfma_f32_16x16x32_bf16 v[92:95], a[220:223], a[136:139], v[92:95]\n" \
"v_cvt_pk_bf16_f32 v144, v144, v145\n" \
"v_cvt_pk_bf16_f32 v145, v146, v147\n" \
"v_mfma_f32_16x16x32_bf16 v[124:127], a[220:223], a[140:143], v[124:127]\n" \
"s_waitcnt vmcnt(20)\n" \
"ds_read_b64_tr_b16 a[214:215], v240 offset:59904\n" \
"v_mfma_f32_16x16x32_bf16 v[124:127], a[216:219], a[252:255], v[124:127]\n" \
"s_waitcnt lgkmcnt(8)\n" \
"v_cvt_pk_bf16_f32 v146, v148, v149\n" \
"v_cvt_pk_bf16_f32 v147, v150, v151\n" \
"v_mfma_f32_16x16x32_bf16 v[0:3], a[192:195], a[144:147], v[0:3]\n" \
"ds_read_b64_tr_b16 a[216:217], v241 offset:49664\n" \
"v_mfma_f32_16x16x32_bf16 v[0:3], a[196:199], v[192:195], v[0:3]\n" \
"ds_write_b128 v249, v[144:147] offset:3072\n" \
"v_mfma_f32_16x16x32_bf16 v[32:35], a[196:199], v[196:199], v[32:35]\n" \
"buffer_load_dwordx4 a[16:19], v248, s[32:35], s79 offen offset:3072 sc1\n" \
"v_mfma_f32_16x16x32_bf16 v[32:35], a[192:195], a[148:151], v[32:35]\n" \
"s_add_u32 m0, s70, 33792\n" \
"ds_read_b64_tr_b16 a[218:219], v241 offset:51712\n" \
"v_accvgpr_read_b32 v152, a88\n" \
"v_mfma_f32_16x16x32_bf16 v[64:67], a[192:195], a[152:155], v[64:67]\n" \
"s_add_u32 s74, s69, 3145856\n" \
"ds_read_b64_tr_b16 a[220:221], v241 offset:57856\n" \
"v_accvgpr_read_b32 v153, a89\n" \
"v_mfma_f32_16x16x32_bf16 v[64:67], a[196:199], v[200:203], v[64:67]\n" \
"buffer_load_dwordx4 v246, s[28:31], s74 offen lds\n" \
"v_mfma_f32_16x16x32_bf16 v[96:99], a[196:199], v[204:207], v[96:99]\n" \
"buffer_load_dwordx4 a[20:23], v248, s[32:35], s80 offen offset:3072 sc1\n" \
"v_accvgpr_read_b32 v154, a90\n" \
"v_mfma_f32_16x16x32_bf16 v[96:99], a[192:195], a[156:159], v[96:99]\n" \
"s_waitcnt lgkmcnt(8)\n" \
"ds_read_b64_tr_b16 a[222:223], v241 offset:59904\n" \
"v_accvgpr_read_b32 v155, a91\n" \
"v_mfma_f32_16x16x32_bf16 v[4:7], a[200:203], a[144:147], v[4:7]\n" \
"s_add_u32 m0, s70, 40960\n" \
"ds_read_b64_tr_b16 a[192:193], v240 offset:50176\n" \
"v_accvgpr_read_b32 v156, a92\n" \
"v_mfma_f32_16x16x32_bf16 v[4:7], a[204:207], v[192:195], v[4:7]\n" \
"s_add_u32 s74, s69, 3407872\n" \
"v_accvgpr_read_b32 v157, a93\n" \
"v_accvgpr_read_b32 v158, a94\n" \
"v_mfma_f32_16x16x32_bf16 v[36:39], a[204:207], v[196:199], v[36:39]\n" \
"s_add_u32 s85, s71, 589824\n" \
"buffer_load_dwordx4 v246, s[28:31], s74 offen lds\n" \
"v_mfma_f32_16x16x32_bf16 v[36:39], a[200:203], a[148:151], v[36:39]\n" \
"buffer_load_dwordx4 a[24:27], v248, s[32:35], s36 offen offset:3072 sc1\n" \
"v_accvgpr_read_b32 v159, a95\n" \
"v_mfma_f32_16x16x32_bf16 v[68:71], a[200:203], a[152:155], v[68:71]\n" \
"ds_read_b64_tr_b16 a[194:195], v240 offset:52224\n" \
"ds_read_b64_tr_b16 a[196:197], v240 offset:58368\n" \
"v_mfma_f32_16x16x32_bf16 v[68:71], a[204:207], v[200:203], v[68:71]\n" \
"v_max3_f32 v250, |v152|, |v153|, v250\n" \
"v_max3_f32 v250, |v154|, |v155|, v250\n" \
"v_mfma_f32_16x16x32_bf16 v[100:103], a[204:207], v[204:207], v[100:103]\n" \
"buffer_store_dwordx4 v[144:147], v248, s[32:35], s85 offen offset:3072 sc1\n" \
"v_mfma_f32_16x16x32_bf16 v[100:103], a[200:203], a[156:159], v[100:103]\n" \
"s_waitcnt lgkmcnt(8)\n" \
"ds_read_b64_tr_b16 a[198:199], v240 offset:60416\n" \
"v_max3_f32 v250, |v156|, |v157|, v250\n" \
"v_mfma_f32_16x16x32_bf16 v[8:11], a[208:211], a[144:147], v[8:11]\n" \
"ds_read_b64_tr_b16 a[200:201], v241 offset:50176\n" \
"v_max3_f32 v250, |v158|, |v159|, v250\n" \
"v_mfma_f32_16x16x32_bf16 v[8:11], a[212:215], v[192:195], v[8:11]\n" \
"v_cvt_pk_bf16_f32 v152, v152, v153\n" \
"v_cvt_pk_bf16_f32 v153, v154, v155\n" \
"v_mfma_f32_16x16x32_bf16 v[40:43], a[212:215], v[196:199], v[40:43]\n" \
"buffer_load_dwordx4 a[28:31], v248, s[32:35], s37 offen offset:3072 sc1\n" \
"v_cvt_pk_bf16_f32 v154, v156, v157\n" \
"v_mfma_f32_16x16x32_bf16 v[40:43], a[208:211], a[148:151], v[40:43]\n" \
"ds_read_b64_tr_b16 a[202:203], v241 offset:52224\n" \
"v_cvt_pk_bf16_f32 v155, v158, v159\n" \
"v_mfma_f32_16x16x32_bf16 v[72:75], a[208:211], a[152:155], v[72:75]\n" \
"ds_read_b64_tr_b16 a[204:205], v241 offset:58368\n" \
"v_accvgpr_write_b32 a88, v152\n" \
"v_mfma_f32_16x16x32_bf16 v[72:75], a[212:215], v[200:203], v[72:75]\n" \
"v_accvgpr_write_b32 a89, v153\n" \
"v_accvgpr_write_b32 a90, v154\n" \
"v_mfma_f32_16x16x32_bf16 v[104:107], a[212:215], v[204:207], v[104:107]\n" \
"s_add_u32 m0, s70, 41984\n" \
"ds_read_b64_tr_b16 a[206:207], v241 offset:60416\n" \
"v_accvgpr_write_b32 a91, v155\n" \
"v_mfma_f32_16x16x32_bf16 v[104:107], a[208:211], a[156:159], v[104:107]\n" \
"s_waitcnt lgkmcnt(8)\n" \
"s_add_u32 s74, s69, 3408000\n" \
"v_mfma_f32_16x16x32_bf16 v[12:15], a[216:219], a[144:147], v[12:15]\n" \
"buffer_load_dwordx4 v246, s[28:31], s74 offen lds\n" \
"v_mfma_f32_16x16x32_bf16 v[12:15], a[220:223], v[192:195], v[12:15]\n" \
"s_add_u32 s79, s79, 4096\n" \
"ds_read_b64_tr_b16 a[208:209], v240 offset:50688\n" \
"v_mfma_f32_16x16x32_bf16 v[44:47], a[220:223], v[196:199], v[44:47]\n" \
"buffer_load_dwordx4 a[128:131], v248, s[32:35], s79 offen offset:0 sc1\n" \
"v_mfma_f32_16x16x32_bf16 v[44:47], a[216:219], a[148:151], v[44:47]\n" \
"ds_read_b64_tr_b16 a[210:211], v240 offset:52736\n" \
"v_mfma_f32_16x16x32_bf16 v[76:79], a[216:219], a[152:155], v[76:79]\n" \
"ds_read_b64_tr_b16 a[212:213], v240 offset:58880\n" \
"v_mfma_f32_16x16x32_bf16 v[76:79], a[220:223], v[200:203], v[76:79]\n" \
"v_mfma_f32_16x16x32_bf16 v[108:111], a[220:223], v[204:207], v[108:111]\n" \
"ds_read_b64_tr_b16 a[214:215], v240 offset:60928\n" \
"v_mfma_f32_16x16x32_bf16 v[108:111], a[216:219], a[156:159], v[108:111]\n" \
"s_waitcnt lgkmcnt(8)\n" \
"v_mfma_f32_16x16x32_bf16 v[16:19], a[192:195], a[144:147], v[16:19]\n" \
"ds_read_b64_tr_b16 a[216:217], v241 offset:50688\n" \
"v_mfma_f32_16x16x32_bf16 v[16:19], a[196:199], v[192:195], v[16:19]\n" \
"s_add_u32 s80, s80, 4096\n" \
"v_mfma_f32_16x16x32_bf16 v[48:51], a[196:199], v[196:199], v[48:51]\n" \
"buffer_load_dwordx4 a[132:135], v248, s[32:35], s80 offen offset:0 sc1\n" \
"v_mfma_f32_16x16x32_bf16 v[48:51], a[192:195], a[148:151], v[48:51]\n" \
"ds_read_b64_tr_b16 a[218:219], v241 offset:52736\n" \
"ds_read_b64_tr_b16 a[220:221], v241 offset:58880\n" \
"v_mfma_f32_16x16x32_bf16 v[80:83], a[192:195], a[152:155], v[80:83]\n" \
"ds_read_b64_tr_b16 a[222:223], v241 offset:60928\n" \
"v_xor_b32 v240, 0x30000, v240\n" \
"v_mfma_f32_16x16x32_bf16 v[80:83], a[196:199], v[200:203], v[80:83]\n" \
"v_xor_b32 v241, 0x30000, v241\n" \
"v_mfma_f32_16x16x32_bf16 v[112:115], a[196:199], v[204:207], v[112:115]\n" \
"s_add_u32 s85, s94, 0\n" \
"v_mfma_f32_16x16x32_bf16 v[112:115], a[192:195], a[156:159], v[112:115]\n" \
"s_waitcnt lgkmcnt(8)\n" \
"buffer_store_dwordx4 v[172:175], v248, s[32:35], s85 offen offset:3072 sc1\n" \
"v_mfma_f32_16x16x32_bf16 v[20:23], a[200:203], a[144:147], v[20:23]\n" \
"v_mfma_f32_16x16x32_bf16 v[20:23], a[204:207], v[192:195], v[20:23]\n" \
"s_add_u32 s36, s36, 4096\n" \
"v_mfma_f32_16x16x32_bf16 v[52:55], a[204:207], v[196:199], v[52:55]\n" \
"buffer_load_dwordx4 a[136:139], v248, s[32:35], s36 offen offset:0 sc1\n" \
"v_mfma_f32_16x16x32_bf16 v[52:55], a[200:203], a[148:151], v[52:55]\n" \
"v_mfma_f32_16x16x32_bf16 v[84:87], a[200:203], a[152:155], v[84:87]\n" \
"s_waitcnt lgkmcnt(0)\n" \
"s_barrier\n" \
"s_add_u32 m0, s70, 49152\n" \
"v_mfma_f32_16x16x32_bf16 v[84:87], a[204:207], v[200:203], v[84:87]\n" \
"s_add_u32 s74, s69, 3670016\n" \
"ds_read_b64_tr_b16 a[192:193], v240 offset:0\n" \
"ds_read_b64_tr_b16 a[194:195], v240 offset:2048\n" \
"v_mfma_f32_16x16x32_bf16 v[116:119], a[204:207], v[204:207], v[116:119]\n" \
"buffer_load_dwordx4 v246, s[28:31], s74 offen lds\n" \
"v_mfma_f32_16x16x32_bf16 v[116:119], a[200:203], a[156:159], v[116:119]\n" \
"ds_read_b64_tr_b16 a[196:197], v240 offset:8192\n" \
"ds_read_b64_tr_b16 a[198:199], v240 offset:10240\n" \
"v_mfma_f32_16x16x32_bf16 v[24:27], a[208:211], a[144:147], v[24:27]\n" \
"ds_read_b64_tr_b16 a[200:201], v241 offset:0\n" \
"v_mfma_f32_16x16x32_bf16 v[24:27], a[212:215], v[192:195], v[24:27]\n" \
"s_add_u32 s37, s37, 4096\n" \
"v_accvgpr_read_b32 v144, a32\n" \
"v_accvgpr_read_b32 v145, a33\n" \
"v_mfma_f32_16x16x32_bf16 v[56:59], a[212:215], v[196:199], v[56:59]\n" \
"buffer_load_dwordx4 a[140:143], v248, s[32:35], s37 offen offset:0 sc1\n" \
"v_accvgpr_read_b32 v146, a34\n" \
"v_mfma_f32_16x16x32_bf16 v[56:59], a[208:211], a[148:151], v[56:59]\n" \
"ds_read_b64_tr_b16 a[202:203], v241 offset:2048\n" \
"v_accvgpr_read_b32 v147, a35\n" \
"v_mfma_f32_16x16x32_bf16 v[88:91], a[208:211], a[152:155], v[88:91]\n" \
"ds_read_b64_tr_b16 a[204:205], v241 offset:8192\n" \
"v_accvgpr_read_b32 v148, a36\n" \
"v_mfma_f32_16x16x32_bf16 v[88:91], a[212:215], v[200:203], v[88:91]\n" \
"v_accvgpr_read_b32 v149, a37\n" \
"v_accvgpr_read_b32 v150, a38\n" \
"v_mfma_f32_16x16x32_bf16 v[120:123], a[212:215], v[204:207], v[120:123]\n" \
"ds_read_b64_tr_b16 a[206:207], v241 offset:10240\n" \
"v_accvgpr_read_b32 v151, a39\n" \
"v_mfma_f32_16x16x32_bf16 v[120:123], a[208:211], a[156:159], v[120:123]\n" \
"v_max3_f32 v250, |v144|, |v145|, v250\n" \
"v_max3_f32 v250, |v146|, |v147|, v250\n" \
"v_mfma_f32_16x16x32_bf16 v[28:31], a[216:219], a[144:147], v[28:31]\n" \
"ds_read_b64_tr_b16 a[208:209], v240 offset:512\n" \
"v_max3_f32 v250, |v148|, |v149|, v250\n" \
"v_mfma_f32_16x16x32_bf16 v[28:31], a[220:223], v[192:195], v[28:31]\n" \
"v_max3_f32 v250, |v150|, |v151|, v250\n" \
"v_cvt_pk_bf16_f32 v144, v144, v145\n" \
"v_mfma_f32_16x16x32_bf16 v[60:63], a[220:223], v[196:199], v[60:63]\n" \
"s_add_u32 m0, s70, 50176\n" \
"ds_read_b64_tr_b16 a[210:211], v240 offset:2560\n" \
"v_cvt_pk_bf16_f32 v145, v146, v147\n" \
"v_mfma_f32_16x16x32_bf16 v[60:63], a[216:219], a[148:151], v[60:63]\n" \
"s_add_u32 s74, s69, 3670144\n" \
"v_cvt_pk_bf16_f32 v146, v148, v149\n" \
"v_cvt_pk_bf16_f32 v147, v150, v151\n" \
"v_mfma_f32_16x16x32_bf16 v[92:95], a[216:219], a[152:155], v[92:95]\n" \
"buffer_load_dwordx4 v246, s[28:31], s74 offen lds\n" \
"v_mfma_f32_16x16x32_bf16 v[92:95], a[220:223], v[200:203], v[92:95]\n" \
"ds_read_b64_tr_b16 a[212:213], v240 offset:8704\n" \
"v_mfma_f32_16x16x32_bf16 v[124:127], a[220:223], v[204:207], v[124:127]\n" \
"s_waitcnt vmcnt(21)\n" \
"ds_read_b64_tr_b16 a[214:215], v240 offset:10752\n" \
"v_mfma_f32_16x16x32_bf16 v[124:127], a[216:219], a[156:159], v[124:127]\n" \
"s_waitcnt lgkmcnt(8)\n" \
"ds_write_b128 v249, v[144:147] offset:4096\n" \
"v_accvgpr_read_b32 v152, a96\n" \
"v_mfma_f32_16x16x32_bf16 v[0:3], a[192:195], a[160:163], v[0:3]\n" \
"ds_read_b64_tr_b16 a[216:217], v241 offset:512\n" \
"v_accvgpr_read_b32 v153, a97\n" \
"v_mfma_f32_16x16x32_bf16 v[0:3], a[196:199], a[176:179], v[0:3]\n" \
"buffer_load_dwordx4 a[144:147], v248, s[32:35], s79 offen offset:1024 sc1\n" \
"v_accvgpr_read_b32 v154, a98\n" \
"v_mfma_f32_16x16x32_bf16 v[32:35], a[196:199], a[180:183], v[32:35]\n" \
"ds_read_b64_tr_b16 a[218:219], v241 offset:2560\n" \
"v_accvgpr_read_b32 v155, a99\n" \
"v_mfma_f32_16x16x32_bf16 v[32:35], a[192:195], a[164:167], v[32:35]\n" \
"v_accvgpr_read_b32 v156, a100\n" \
"v_accvgpr_read_b32 v157, a101\n" \
"v_mfma_f32_16x16x32_bf16 v[64:67], a[192:195], a[168:171], v[64:67]\n" \
"s_add_u32 s85, s71, 593920\n" \
"ds_read_b64_tr_b16 a[220:221], v241 offset:8704\n" \
"v_accvgpr_read_b32 v158, a102\n" \
"v_mfma_f32_16x16x32_bf16 v[64:67], a[196:199], a[184:187], v[64:67]\n" \
"buffer_store_dwordx4 v[144:147], v248, s[32:35], s85 offen offset:0 sc1\n" \
"v_mfma_f32_16x16x32_bf16 v[96:99], a[196:199], a[188:191], v[96:99]\n" \
"ds_read_b64_tr_b16 a[222:223], v241 offset:10752\n" \
"v_accvgpr_read_b32 v159, a103\n" \
"v_mfma_f32_16x16x32_bf16 v[96:99], a[192:195], a[172:175], v[96:99]\n" \
"s_waitcnt lgkmcnt(9)\n" \
"v_max3_f32 v250, |v152|, |v153|, v250\n" \
"v_max3_f32 v250, |v154|, |v155|, v250\n" \
"v_mfma_f32_16x16x32_bf16 v[4:7], a[200:203], a[160:163], v[4:7]\n" \
"ds_read_b64_tr_b16 a[192:193], v240 offset:1024\n" \
"v_max3_f32 v250, |v156|, |v157|, v250\n" \
"v_mfma_f32_16x16x32_bf16 v[4:7], a[204:207], a[176:179], v[4:7]\n" \
"buffer_load_dwordx4 a[148:151], v248, s[32:35], s80 offen offset:1024 sc1\n" \
"v_max3_f32 v250, |v158|, |v159|, v250\n" \
"v_mfma_f32_16x16x32_bf16 v[36:39], a[204:207], a[180:183], v[36:39]\n" \
"s_add_u32 m0, s70, 57344\n" \
"ds_read_b64_tr_b16 a[194:195], v240 offset:3072\n" \
"v_cvt_pk_bf16_f32 v152, v152, v153\n" \
"v_mfma_f32_16x16x32_bf16 v[36:39], a[200:203], a[164:167], v[36:39]\n" \
"s_add_u32 s74, s69, 3932160\n" \
"v_cvt_pk_bf16_f32 v153, v154, v155\n" \
"v_cvt_pk_bf16_f32 v154, v156, v157\n" \
"v_mfma_f32_16x16x32_bf16 v[68:71], a[200:203], a[168:171], v[68:71]\n" \
"buffer_load_dwordx4 v246, s[28:31], s74 offen lds\n" \
"v_mfma_f32_16x16x32_bf16 v[68:71], a[204:207], a[184:187], v[68:71]\n" \
"ds_read_b64_tr_b16 a[196:197], v240 offset:9216\n" \
"v_cvt_pk_bf16_f32 v155, v158, v159\n" \
"v_mfma_f32_16x16x32_bf16 v[100:103], a[204:207], a[188:191], v[100:103]\n" \
"ds_read_b64_tr_b16 a[198:199], v240 offset:11264\n" \
"v_accvgpr_write_b32 a96, v152\n" \
"v_mfma_f32_16x16x32_bf16 v[100:103], a[200:203], a[172:175], v[100:103]\n" \
"s_waitcnt lgkmcnt(9)\n" \
"v_accvgpr_write_b32 a97, v153\n" \
"v_accvgpr_write_b32 a98, v154\n" \
"v_mfma_f32_16x16x32_bf16 v[8:11], a[208:211], a[160:163], v[8:11]\n" \
"ds_read_b64_tr_b16 a[200:201], v241 offset:1024\n" \
"v_accvgpr_write_b32 a99, v155\n" \
"v_mfma_f32_16x16x32_bf16 v[8:11], a[212:215], a[176:179], v[8:11]\n" \
"buffer_load_dwordx4 a[152:155], v248, s[32:35], s36 offen offset:1024 sc1\n" \
"v_mfma_f32_16x16x32_bf16 v[40:43], a[212:215], a[180:183], v[40:43]\n" \
"ds_read_b64_tr_b16 a[202:203], v241 offset:3072\n" \
"v_mfma_f32_16x16x32_bf16 v[40:43], a[208:211], a[164:167], v[40:43]\n" \
"v_mfma_f32_16x16x32_bf16 v[72:75], a[208:211], a[168:171], v[72:75]\n" \
"s_and_b32 s91, %[wid], 1\n" \
"s_lshl_b32 s91, s91, 4\n" \
"ds_read_b64_tr_b16 a[204:205], v241 offset:9216\n" \
"v_mfma_f32_16x16x32_bf16 v[72:75], a[212:215], a[184:187], v[72:75]\n" \
"v_and_b32 v254, 15, v251\n" \
"v_lshrrev_b32 v255, 4, v251\n" \
"v_mfma_f32_16x16x32_bf16 v[104:107], a[212:215], a[188:191], v[104:107]\n" \
"s_add_u32 m0, s70, 58368\n" \
"ds_read_b64_tr_b16 a[206:207], v241 offset:11264\n" \
"v_lshlrev_b32 v255, 2, v255\n" \
"v_mfma_f32_16x16x32_bf16 v[104:107], a[208:211], a[172:175], v[104:107]\n" \
"s_waitcnt lgkmcnt(8)\n" \
"v_sub_u32 v254, v254, v255\n" \
"v_add_u32 v254, s91, v254\n" \
"v_mfma_f32_16x16x32_bf16 v[12:15], a[216:219], a[160:163], v[12:15]\n" \
"s_add_u32 s74, s69, 3932288\n" \
"buffer_load_dwordx4 v246, s[28:31], s74 offen lds\n" \
"v_mfma_f32_16x16x32_bf16 v[12:15], a[220:223], a[176:179], v[12:15]\n" \
"buffer_load_dwordx4 a[156:159], v248, s[32:35], s37 offen offset:1024 sc1\n" \
"v_mov_b32 v255, 0xff000000\n" \
"v_mfma_f32_16x16x32_bf16 v[44:47], a[220:223], a[180:183], v[44:47]\n" \
"buffer_load_dwordx4 a[224:227], v248, s[32:35], s79 offen offset:2048 sc1\n" \
"v_cmp_gt_i32 s[96:97], 0, v254\n" \
"v_mfma_f32_16x16x32_bf16 v[44:47], a[216:219], a[164:167], v[44:47]\n" \
"ds_read_b64_tr_b16 a[208:209], v240 offset:1536\n" \
"ds_read_b64_tr_b16 a[210:211], v240 offset:3584\n" \
"v_mfma_f32_16x16x32_bf16 v[76:79], a[216:219], a[168:171], v[76:79]\n" \
"ds_read_b64_tr_b16 a[212:213], v240 offset:9728\n" \
"v_cmp_gt_i32 s[98:99], 1, v254\n" \
"v_mfma_f32_16x16x32_bf16 v[76:79], a[220:223], a[184:187], v[76:79]\n" \
"v_cndmask_b32 v224, 0, v255, s[96:97]\n" \
"v_mfma_f32_16x16x32_bf16 v[108:111], a[220:223], a[188:191], v[108:111]\n" \
"ds_read_b64_tr_b16 a[214:215], v240 offset:11776\n" \
"v_cndmask_b32 v225, 0, v255, s[98:99]\n" \
"v_mfma_f32_16x16x32_bf16 v[108:111], a[216:219], a[172:175], v[108:111]\n" \
".Lst0_6_%=:\n" \
"s_cbranch_execz .Lst0_10_%=\n" \
"s_waitcnt lgkmcnt(8)\n" \
"v_cmp_gt_i32 s[96:97], 2, v254\n" \
"v_cmp_gt_i32 s[98:99], 3, v254\n" \
"v_mfma_f32_16x16x32_bf16 v[16:19], a[192:195], a[160:163], v[16:19]\n" \
"ds_read_b64_tr_b16 a[216:217], v241 offset:1536\n" \
"v_mfma_f32_16x16x32_bf16 v[16:19], a[196:199], a[176:179], v[16:19]\n" \
"v_cndmask_b32 v226, 0, v255, s[96:97]\n" \
"v_cndmask_b32 v227, 0, v255, s[98:99]\n" \
"v_mfma_f32_16x16x32_bf16 v[48:51], a[196:199], a[180:183], v[48:51]\n" \
"s_add_u32 s85, s94, 4096\n" \
"buffer_load_dwordx4 a[228:231], v248, s[32:35], s80 offen offset:2048 sc1\n" \
"v_cmp_gt_i32 s[96:97], 16, v254\n" \
"v_mfma_f32_16x16x32_bf16 v[48:51], a[192:195], a[164:167], v[48:51]\n" \
"ds_read_b64_tr_b16 a[218:219], v241 offset:3584\n" \
"ds_read_b64_tr_b16 a[220:221], v241 offset:9728\n" \
"v_mfma_f32_16x16x32_bf16 v[80:83], a[192:195], a[168:171], v[80:83]\n" \
"ds_read_b64_tr_b16 a[222:223], v241 offset:11776\n" \
"v_cmp_gt_i32 s[98:99], 17, v254\n" \
"v_mfma_f32_16x16x32_bf16 v[80:83], a[196:199], a[184:187], v[80:83]\n" \
"buffer_store_dwordx4 v[176:179], v248, s[32:35], s85 offen offset:0 sc1\n" \
"v_mfma_f32_16x16x32_bf16 v[112:115], a[196:199], a[188:191], v[112:115]\n" \
"v_cndmask_b32 v228, 0, v255, s[96:97]\n" \
"v_cndmask_b32 v229, 0, v255, s[98:99]\n" \
"v_mfma_f32_16x16x32_bf16 v[112:115], a[192:195], a[172:175], v[112:115]\n" \
"s_waitcnt lgkmcnt(8)\n" \
"v_cmp_gt_i32 s[96:97], 18, v254\n" \
"v_cmp_gt_i32 s[98:99], 19, v254\n" \
"v_mfma_f32_16x16x32_bf16 v[20:23], a[200:203], a[160:163], v[20:23]\n" \
"v_mfma_f32_16x16x32_bf16 v[20:23], a[204:207], a[176:179], v[20:23]\n" \
"v_cndmask_b32 v230, 0, v255, s[96:97]\n" \
"v_cndmask_b32 v231, 0, v255, s[98:99]\n" \
"v_mfma_f32_16x16x32_bf16 v[52:55], a[204:207], a[180:183], v[52:55]\n" \
"s_lshl_b32 s91, s91, 1\n" \
"s_sub_u32 s91, 16, s91\n" \
"buffer_load_dwordx4 a[232:235], v248, s[32:35], s36 offen offset:2048 sc1\n" \
"v_mfma_f32_16x16x32_bf16 v[52:55], a[200:203], a[164:167], v[52:55]\n" \
"v_add_u32 v254, s91, v254\n" \
"v_cmp_gt_i32 s[96:97], 0, v254\n" \
"v_mfma_f32_16x16x32_bf16 v[84:87], a[200:203], a[168:171], v[84:87]\n" \
"s_waitcnt lgkmcnt(0)\n" \
"s_barrier\n" \
"v_cmp_gt_i32 s[98:99], 1, v254\n" \
"v_mfma_f32_16x16x32_bf16 v[84:87], a[204:207], a[184:187], v[84:87]\n" \
"ds_read_b64_tr_b16 a[192:193], v240 offset:16384\n" \
"ds_read_b64_tr_b16 a[194:195], v240 offset:18432\n" \
"v_mfma_f32_16x16x32_bf16 v[116:119], a[204:207], a[188:191], v[116:119]\n" \
"ds_read_b64_tr_b16 a[196:197], v240 offset:24576\n" \
"ds_read_b64_tr_b16 a[198:199], v240 offset:26624\n" \
"v_mfma_f32_16x16x32_bf16 v[116:119], a[200:203], a[172:175], v[116:119]\n" \
"v_cndmask_b32 v232, 0, v255, s[96:97]\n" \
"v_cndmask_b32 v233, 0, v255, s[98:99]\n" \
"v_mfma_f32_16x16x32_bf16 v[24:27], a[208:211], a[160:163], v[24:27]\n" \
"ds_read_b64_tr_b16 a[200:201], v241 offset:16384\n" \
"v_cmp_gt_i32 s[96:97], 2, v254\n" \
"v_mfma_f32_16x16x32_bf16 v[24:27], a[212:215], a[176:179], v[24:27]\n" \
"v_cmp_gt_i32 s[98:99], 3, v254\n" \
"v_accvgpr_read_b32 v144, a40\n" \
"v_mfma_f32_16x16x32_bf16 v[56:59], a[212:215], a[180:183], v[56:59]\n" \
"buffer_load_dwordx4 a[236:239], v248, s[32:35], s37 offen offset:2048 sc1\n" \
"v_cndmask_b32 v234, 0, v255, s[96:97]\n" \
"v_mfma_f32_16x16x32_bf16 v[56:59], a[208:211], a[164:167], v[56:59]\n" \
"ds_read_b64_tr_b16 a[202:203], v241 offset:18432\n" \
"v_cndmask_b32 v235, 0, v255, s[98:99]\n" \
"v_mfma_f32_16x16x32_bf16 v[88:91], a[208:211], a[168:171], v[88:91]\n" \
"ds_read_b64_tr_b16 a[204:205], v241 offset:24576\n" \
"v_cmp_gt_i32 s[96:97], 16, v254\n" \
"v_mfma_f32_16x16x32_bf16 v[88:91], a[212:215], a[184:187], v[88:91]\n" \
"v_cmp_gt_i32 s[98:99], 17, v254\n" \
"v_accvgpr_read_b32 v145, a41\n" \
"v_mfma_f32_16x16x32_bf16 v[120:123], a[212:215], a[188:191], v[120:123]\n" \
"ds_read_b64_tr_b16 a[206:207], v241 offset:26624\n" \
"v_cndmask_b32 v236, 0, v255, s[96:97]\n" \
"v_mfma_f32_16x16x32_bf16 v[120:123], a[208:211], a[172:175], v[120:123]\n" \
"v_cndmask_b32 v237, 0, v255, s[98:99]\n" \
"v_cmp_gt_i32 s[96:97], 18, v254\n" \
"v_mfma_f32_16x16x32_bf16 v[28:31], a[216:219], a[160:163], v[28:31]\n" \
"ds_read_b64_tr_b16 a[208:209], v240 offset:16896\n" \
"v_cmp_gt_i32 s[98:99], 19, v254\n" \
"v_mfma_f32_16x16x32_bf16 v[28:31], a[220:223], a[176:179], v[28:31]\n" \
"v_cndmask_b32 v238, 0, v255, s[96:97]\n" \
"v_accvgpr_read_b32 v146, a42\n" \
"v_mfma_f32_16x16x32_bf16 v[60:63], a[220:223], a[180:183], v[60:63]\n" \
"buffer_load_dwordx4 a[240:243], v248, s[32:35], s79 offen offset:3072 sc1\n" \
"v_cndmask_b32 v239, 0, v255, s[98:99]\n" \
"v_mfma_f32_16x16x32_bf16 v[60:63], a[216:219], a[164:167], v[60:63]\n" \
"ds_read_b64_tr_b16 a[210:211], v240 offset:18944\n" \
"v_accvgpr_read_b32 v147, a43\n" \
"v_mfma_f32_16x16x32_bf16 v[92:95], a[216:219], a[168:171], v[92:95]\n" \
"ds_read_b64_tr_b16 a[212:213], v240 offset:25088\n" \
"v_accvgpr_read_b32 v148, a44\n" \
"v_mfma_f32_16x16x32_bf16 v[92:95], a[220:223], a[184:187], v[92:95]\n" \
"v_accvgpr_read_b32 v149, a45\n" \
"v_accvgpr_read_b32 v150, a46\n" \
"v_mfma_f32_16x16x32_bf16 v[124:127], a[220:223], a[188:191], v[124:127]\n" \
"s_waitcnt vmcnt(21)\n" \
"ds_read_b64_tr_b16 a[214:215], v240 offset:27136\n" \
"v_mfma_f32_16x16x32_bf16 v[124:127], a[216:219], a[172:175], v[124:127]\n" \
"s_waitcnt lgkmcnt(8)\n" \
"v_accvgpr_read_b32 v151, a47\n" \
"v_max3_f32 v250, |v144|, |v145|, v250\n" \
"v_mfma_f32_16x16x32_bf16 v[0:3], a[192:195], a[0:3], v[0:3]\n" \
"s_add_u32 s85, s71, 593920\n" \
"ds_read_b64_tr_b16 a[216:217], v241 offset:16896\n" \
"v_max3_f32 v250, |v146|, |v147|, v250\n" \
"v_mfma_f32_16x16x32_bf16 v[0:3], a[196:199], a[16:19], v[0:3]\n" \
"ds_read_b128 a[160:163], v248 offset:8192\n" \
"v_max3_f32 v250, |v148|, |v149|, v250\n" \
"v_mfma_f32_16x16x32_bf16 v[32:35], a[196:199], a[20:23], v[32:35]\n" \
"buffer_load_dwordx4 a[244:247], v248, s[32:35], s80 offen offset:3072 sc1\n" \
"v_max3_f32 v250, |v150|, |v151|, v250\n" \
"v_mfma_f32_16x16x32_bf16 v[32:35], a[192:195], a[4:7], v[32:35]\n" \
"ds_read_b64_tr_b16 a[218:219], v241 offset:18944\n" \
"v_cvt_pk_bf16_f32 v144, v144, v145\n" \
"v_mfma_f32_16x16x32_bf16 v[64:67], a[192:195], a[8:11], v[64:67]\n" \
"ds_read_b64_tr_b16 a[220:221], v241 offset:25088\n" \
"ds_read_b128 a[164:167], v248 offset:9216\n" \
"v_mfma_f32_16x16x32_bf16 v[64:67], a[196:199], a[24:27], v[64:67]\n" \
"v_cvt_pk_bf16_f32 v145, v146, v147\n" \
"v_cvt_pk_bf16_f32 v146, v148, v149\n" \
"v_mfma_f32_16x16x32_bf16 v[96:99], a[196:199], a[28:31], v[96:99]\n" \
"ds_read_b64_tr_b16 a[222:223], v241 offset:27136\n" \
"v_cvt_pk_bf16_f32 v147, v150, v151\n" \
"v_mfma_f32_16x16x32_bf16 v[96:99], a[192:195], a[12:15], v[96:99]\n" \
"s_waitcnt lgkmcnt(10)\n" \
"buffer_store_dwordx4 v[144:147], v248, s[32:35], s85 offen offset:1024 sc1\n" \
"v_mfma_f32_16x16x32_bf16 v[4:7], a[200:203], a[0:3], v[4:7]\n" \
"ds_read_b64_tr_b16 a[192:193], v240 offset:17408\n" \
"ds_read_b128 a[168:171], v248 offset:10240\n" \
"v_mfma_f32_16x16x32_bf16 v[4:7], a[204:207], a[16:19], v[4:7]\n" \
"ds_write_b128 v249, v[144:147] offset:5120\n" \
"v_accvgpr_read_b32 v152, a104\n" \
"v_mfma_f32_16x16x32_bf16 v[36:39], a[204:207], a[20:23], v[36:39]\n" \
"buffer_load_dwordx4 a[248:251], v248, s[32:35], s36 offen offset:3072 sc1\n" \
"v_accvgpr_read_b32 v153, a105\n" \
"v_mfma_f32_16x16x32_bf16 v[36:39], a[200:203], a[4:7], v[36:39]\n" \
"ds_read_b64_tr_b16 a[194:195], v240 offset:19456\n" \
"ds_read_b128 a[172:175], v248 offset:11264\n" \
"v_mfma_f32_16x16x32_bf16 v[68:71], a[200:203], a[8:11], v[68:71]\n" \
"ds_read_b64_tr_b16 a[196:197], v240 offset:25600\n" \
"v_accvgpr_read_b32 v154, a106\n" \
"v_mfma_f32_16x16x32_bf16 v[68:71], a[204:207], a[24:27], v[68:71]\n" \
"ds_read_b128 a[188:191], v248 offset:15360\n" \
"v_accvgpr_read_b32 v155, a107\n" \
"v_mfma_f32_16x16x32_bf16 v[100:103], a[204:207], a[28:31], v[100:103]\n" \
"ds_read_b64_tr_b16 a[198:199], v240 offset:27648\n" \
"v_accvgpr_read_b32 v156, a108\n" \
"v_mfma_f32_16x16x32_bf16 v[100:103], a[200:203], a[12:15], v[100:103]\n" \
"s_waitcnt lgkmcnt(14)\n" \
"v_accvgpr_read_b32 v157, a109\n" \
"v_accvgpr_read_b32 v158, a110\n" \
"v_mfma_f32_16x16x32_bf16 v[8:11], a[208:211], a[0:3], v[8:11]\n" \
"ds_read_b64_tr_b16 a[200:201], v241 offset:17408\n" \
"ds_read_b128 a[184:187], v248 offset:14336\n" \
"v_mfma_f32_16x16x32_bf16 v[8:11], a[212:215], a[16:19], v[8:11]\n" \
"v_accvgpr_read_b32 v159, a111\n" \
"v_max3_f32 v250, |v152|, |v153|, v250\n" \
"v_mfma_f32_16x16x32_bf16 v[40:43], a[212:215], a[20:23], v[40:43]\n" \
"buffer_load_dwordx4 a[252:255], v248, s[32:35], s37 offen offset:3072 sc1\n" \
"v_max3_f32 v250, |v154|, |v155|, v250\n" \
"v_mfma_f32_16x16x32_bf16 v[40:43], a[208:211], a[4:7], v[40:43]\n" \
"ds_read_b64_tr_b16 a[202:203], v241 offset:19456\n" \
"ds_read_b128 a[180:183], v248 offset:13312\n" \
"v_mfma_f32_16x16x32_bf16 v[72:75], a[208:211], a[8:11], v[72:75]\n" \
"ds_read_b64_tr_b16 a[204:205], v241 offset:25600\n" \
"v_max3_f32 v250, |v156|, |v157|, v250\n" \
"v_mfma_f32_16x16x32_bf16 v[72:75], a[212:215], a[24:27], v[72:75]\n" \
"v_max3_f32 v250, |v158|, |v159|, v250\n" \
"v_cvt_pk_bf16_f32 v152, v152, v153\n" \
"v_mfma_f32_16x16x32_bf16 v[104:107], a[212:215], a[28:31], v[104:107]\n" \
"ds_read_b64_tr_b16 a[206:207], v241 offset:27648\n" \
"ds_read_b128 a[176:179], v248 offset:12288\n" \
"v_mfma_f32_16x16x32_bf16 v[104:107], a[208:211], a[12:15], v[104:107]\n" \
"s_waitcnt lgkmcnt(15)\n" \
"v_cvt_pk_bf16_f32 v153, v154, v155\n" \
"v_cvt_pk_bf16_f32 v154, v156, v157\n" \
"v_mfma_f32_16x16x32_bf16 v[12:15], a[216:219], a[0:3], v[12:15]\n" \
"ds_read_b64_tr_b16 a[208:209], v240 offset:17920\n" \
"v_cvt_pk_bf16_f32 v155, v158, v159\n" \
"v_mfma_f32_16x16x32_bf16 v[12:15], a[220:223], a[16:19], v[12:15]\n" \
"v_accvgpr_write_b32 a104, v152\n" \
"v_accvgpr_write_b32 a105, v153\n" \
"v_mfma_f32_16x16x32_bf16 v[44:47], a[220:223], a[20:23], v[44:47]\n" \
"ds_read_b64_tr_b16 a[210:211], v240 offset:19968\n" \
"v_accvgpr_write_b32 a106, v154\n" \
"v_mfma_f32_16x16x32_bf16 v[44:47], a[216:219], a[4:7], v[44:47]\n" \
"v_accvgpr_write_b32 a107, v155\n" \
"v_mfma_f32_16x16x32_bf16 v[76:79], a[216:219], a[8:11], v[76:79]\n" \
"ds_read_b64_tr_b16 a[212:213], v240 offset:26112\n" \
"v_mfma_f32_16x16x32_bf16 v[76:79], a[220:223], a[24:27], v[76:79]\n" \
"v_mfma_f32_16x16x32_bf16 v[108:111], a[220:223], a[28:31], v[108:111]\n" \
"s_add_u32 s85, s94, 4096\n" \
"ds_read_b64_tr_b16 a[214:215], v240 offset:28160\n" \
"v_mfma_f32_16x16x32_bf16 v[108:111], a[216:219], a[12:15], v[108:111]\n" \
"s_waitcnt lgkmcnt(11)\n" \
"buffer_store_dwordx4 v[180:183], v248, s[32:35], s85 offen offset:1024 sc1\n" \
"v_mfma_f32_16x16x32_bf16 v[16:19], a[192:195], a[0:3], v[16:19]\n" \
"ds_read_b64_tr_b16 a[216:217], v241 offset:17920\n" \
"v_mfma_f32_16x16x32_bf16 v[16:19], a[196:199], a[16:19], v[16:19]\n" \
"v_mfma_f32_16x16x32_bf16 v[48:51], a[196:199], a[20:23], v[48:51]\n" \
"ds_read_b64_tr_b16 a[218:219], v241 offset:19968\n" \
"v_mfma_f32_16x16x32_bf16 v[48:51], a[192:195], a[4:7], v[48:51]\n" \
"ds_read_b64_tr_b16 a[220:221], v241 offset:26112\n" \
"v_mfma_f32_16x16x32_bf16 v[80:83], a[192:195], a[8:11], v[80:83]\n" \
"ds_read_b64_tr_b16 a[222:223], v241 offset:28160\n" \
"v_xor_b32 v240, 0x30000, v240\n" \
"v_mfma_f32_16x16x32_bf16 v[80:83], a[196:199], a[24:27], v[80:83]\n" \
"v_xor_b32 v241, 0x30000, v241\n" \
"v_mfma_f32_16x16x32_bf16 v[112:115], a[196:199], a[28:31], v[112:115]\n" \
"v_mfma_f32_16x16x32_bf16 v[112:115], a[192:195], a[12:15], v[112:115]\n" \
"s_waitcnt lgkmcnt(9)\n" \
"v_mfma_f32_16x16x32_bf16 v[20:23], a[200:203], a[0:3], v[20:23]\n" \
"v_mfma_f32_16x16x32_bf16 v[20:23], a[204:207], a[16:19], v[20:23]\n" \
"v_accvgpr_read_b32 v144, a48\n" \
"v_accvgpr_read_b32 v145, a49\n" \
"v_mfma_f32_16x16x32_bf16 v[52:55], a[204:207], a[20:23], v[52:55]\n" \
"v_accvgpr_read_b32 v146, a50\n" \
"v_accvgpr_read_b32 v147, a51\n" \
"v_mfma_f32_16x16x32_bf16 v[52:55], a[200:203], a[4:7], v[52:55]\n" \
"v_accvgpr_read_b32 v148, a52\n" \
"v_accvgpr_read_b32 v149, a53\n" \
"v_mfma_f32_16x16x32_bf16 v[84:87], a[200:203], a[8:11], v[84:87]\n" \
"s_waitcnt vmcnt(25) lgkmcnt(0)\n" \
"s_barrier\n" \
"v_accvgpr_read_b32 v150, a54\n" \
"v_mfma_f32_16x16x32_bf16 v[84:87], a[204:207], a[24:27], v[84:87]\n" \
"ds_read_b64_tr_b16 a[192:193], v240 offset:32768\n" \
"ds_read_b64_tr_b16 a[194:195], v240 offset:34816\n" \
"v_mfma_f32_16x16x32_bf16 v[116:119], a[204:207], a[28:31], v[116:119]\n" \
"ds_read_b64_tr_b16 a[196:197], v240 offset:40960\n" \
"ds_read_b64_tr_b16 a[198:199], v240 offset:43008\n" \
"v_mfma_f32_16x16x32_bf16 v[116:119], a[200:203], a[12:15], v[116:119]\n" \
"v_accvgpr_read_b32 v151, a55\n" \
"v_max3_f32 v250, |v144|, |v145|, v250\n" \
"v_mfma_f32_16x16x32_bf16 v[24:27], a[208:211], a[0:3], v[24:27]\n" \
"ds_read_b64_tr_b16 a[200:201], v241 offset:32768\n" \
"v_max3_f32 v250, |v146|, |v147|, v250\n" \
"v_mfma_f32_16x16x32_bf16 v[24:27], a[212:215], a[16:19], v[24:27]\n" \
"v_max3_f32 v250, |v148|, |v149|, v250\n" \
"v_max3_f32 v250, |v150|, |v151|, v250\n" \
"v_mfma_f32_16x16x32_bf16 v[56:59], a[212:215], a[20:23], v[56:59]\n" \
"ds_read_b64_tr_b16 a[202:203], v241 offset:34816\n" \
"v_cvt_pk_bf16_f32 v144, v144, v145\n" \
"v_mfma_f32_16x16x32_bf16 v[56:59], a[208:211], a[4:7], v[56:59]\n" \
"v_cvt_pk_bf16_f32 v145, v146, v147\n" \
"v_cvt_pk_bf16_f32 v146, v148, v149\n" \
"v_mfma_f32_16x16x32_bf16 v[88:91], a[208:211], a[8:11], v[88:91]\n" \
"ds_read_b64_tr_b16 a[204:205], v241 offset:40960\n" \
"v_cvt_pk_bf16_f32 v147, v150, v151\n" \
"v_mfma_f32_16x16x32_bf16 v[88:91], a[212:215], a[24:27], v[88:91]\n" \
"ds_write_b128 v249, v[144:147] offset:6144\n" \
"v_mfma_f32_16x16x32_bf16 v[120:123], a[212:215], a[28:31], v[120:123]\n" \
"ds_read_b64_tr_b16 a[206:207], v241 offset:43008\n" \
"v_mfma_f32_16x16x32_bf16 v[120:123], a[208:211], a[12:15], v[120:123]\n" \
"v_accvgpr_read_b32 v152, a112\n" \
"v_accvgpr_read_b32 v153, a113\n" \
"v_mfma_f32_16x16x32_bf16 v[28:31], a[216:219], a[0:3], v[28:31]\n" \
"s_lshl2_add_u32 s76, s67, %[wid]\n" \
"ds_read_b64_tr_b16 a[208:209], v240 offset:33280\n" \
"v_accvgpr_read_b32 v154, a114\n" \
"v_mfma_f32_16x16x32_bf16 v[28:31], a[220:223], a[16:19], v[28:31]\n" \
"s_sub_u32 s77, 15, s67\n" \
"v_accvgpr_read_b32 v155, a115\n" \
"v_accvgpr_read_b32 v156, a116\n" \
"v_mfma_f32_16x16x32_bf16 v[60:63], a[220:223], a[20:23], v[60:63]\n" \
"s_lshl2_add_u32 s77, s77, %[wid]\n" \
"ds_read_b64_tr_b16 a[210:211], v240 offset:35328\n" \
"v_accvgpr_read_b32 v157, a117\n" \
"v_mfma_f32_16x16x32_bf16 v[60:63], a[216:219], a[4:7], v[60:63]\n" \
"s_lshl_b32 s28, %[wid], 12\n" \
"v_accvgpr_read_b32 v158, a118\n" \
"v_accvgpr_read_b32 v159, a119\n" \
"v_mfma_f32_16x16x32_bf16 v[92:95], a[216:219], a[8:11], v[92:95]\n" \
"s_add_u32 s85, s71, 593920\n" \
"ds_read_b64_tr_b16 a[212:213], v240 offset:41472\n" \
"v_max3_f32 v250, |v152|, |v153|, v250\n" \
"v_mfma_f32_16x16x32_bf16 v[92:95], a[220:223], a[24:27], v[92:95]\n" \
"s_add_u32 s88, s73, s28\n" \
"buffer_store_dwordx4 v[144:147], v248, s[32:35], s85 offen offset:2048 sc1\n" \
"v_mfma_f32_16x16x32_bf16 v[124:127], a[220:223], a[28:31], v[124:127]\n" \
"s_waitcnt vmcnt(12)\n" \
"ds_read_b64_tr_b16 a[214:215], v240 offset:43520\n" \
"v_mfma_f32_16x16x32_bf16 v[124:127], a[216:219], a[12:15], v[124:127]\n" \
"s_waitcnt lgkmcnt(9)\n" \
"v_max3_f32 v250, |v154|, |v155|, v250\n" \
"v_max3_f32 v250, |v156|, |v157|, v250\n" \
"v_mfma_f32_16x16x32_bf16 v[0:3], a[192:195], a[128:131], v[0:3]\n" \
"s_add_u32 s18, s88, 524288\n" \
"ds_read_b64_tr_b16 a[216:217], v241 offset:33280\n" \
"v_max3_f32 v250, |v158|, |v159|, v250\n" \
"v_mfma_f32_16x16x32_bf16 v[0:3], a[196:199], a[144:147], v[0:3]\n" \
"s_add_u32 s24, s88, 229376\n" \
"v_cvt_pk_bf16_f32 v152, v152, v153\n" \
"v_cvt_pk_bf16_f32 v153, v154, v155\n" \
"v_mfma_f32_16x16x32_bf16 v[32:35], a[196:199], a[148:151], v[32:35]\n" \
"s_lshl_b32 s16, s67, 15\n" \
"ds_read_b64_tr_b16 a[218:219], v241 offset:35328\n" \
"v_cvt_pk_bf16_f32 v154, v156, v157\n" \
"v_mfma_f32_16x16x32_bf16 v[32:35], a[192:195], a[132:135], v[32:35]\n" \
"s_add_u32 s16, s16, s88\n" \
"v_cvt_pk_bf16_f32 v155, v158, v159\n" \
"v_accvgpr_write_b32 a112, v152\n" \
"v_mfma_f32_16x16x32_bf16 v[64:67], a[192:195], a[136:139], v[64:67]\n" \
"s_add_u32 s21, s16, 0x8000\n" \
"ds_read_b64_tr_b16 a[220:221], v241 offset:41472\n" \
"v_accvgpr_write_b32 a113, v153\n" \
"v_mfma_f32_16x16x32_bf16 v[64:67], a[196:199], a[152:155], v[64:67]\n" \
"s_cmp_eq_u32 s88, s16\n" \
"s_cmov_b32 s88, s21\n" \
"v_accvgpr_write_b32 a114, v154\n" \
"v_mfma_f32_16x16x32_bf16 v[96:99], a[196:199], a[156:159], v[96:99]\n" \
"s_mov_b32 s19, s28\n" \
"ds_read_b64_tr_b16 a[222:223], v241 offset:43520\n" \
"v_accvgpr_write_b32 a115, v155\n" \
"v_mfma_f32_16x16x32_bf16 v[96:99], a[192:195], a[140:143], v[96:99]\n" \
"s_waitcnt lgkmcnt(8)\n" \
"s_add_u32 s20, s28, 0x10000\n" \
"s_lshl_b32 s36, s64, 6\n" \
"v_mfma_f32_16x16x32_bf16 v[4:7], a[200:203], a[128:131], v[4:7]\n" \
"s_add_u32 s36, s36, s77\n" \
"s_lshl2_add_u32 s36, s36, s66\n" \
"ds_read_b64_tr_b16 a[192:193], v240 offset:33792\n" \
"v_mfma_f32_16x16x32_bf16 v[4:7], a[204:207], a[144:147], v[4:7]\n" \
"s_lshl_b32 s36, s36, 13\n" \
"s_add_u32 s36, s36, 59768832\n" \
"s_lshl_b32 s17, s64, 19\n" \
"v_mfma_f32_16x16x32_bf16 v[36:39], a[204:207], a[148:151], v[36:39]\n" \
"s_lshl_b32 s83, s66, 8\n" \
"s_add_u32 s17, s17, s83\n" \
"ds_read_b64_tr_b16 a[194:195], v240 offset:35840\n" \
"v_mfma_f32_16x16x32_bf16 v[36:39], a[200:203], a[132:135], v[36:39]\n" \
"s_lshr_b32 s83, %[wid], 1\n" \
"s_lshl1_add_u32 s93, s66, s83\n" \
"s_add_u32 s93, s93, 6\n" \
"v_mfma_f32_16x16x32_bf16 v[68:71], a[200:203], a[136:139], v[68:71]\n" \
"s_and_b32 s93, s93, 7\n" \
"s_and_b32 s84, %[wid], 1\n" \
"ds_read_b64_tr_b16 a[196:197], v240 offset:41984\n" \
"v_mfma_f32_16x16x32_bf16 v[68:71], a[204:207], a[152:155], v[68:71]\n" \
"s_lshl1_add_u32 s93, s93, s84\n" \
"s_lshl_b32 s93, s93, 15\n" \
"s_add_u32 s93, s93, s17\n" \
"v_mfma_f32_16x16x32_bf16 v[100:103], a[204:207], a[156:159], v[100:103]\n" \
"s_lshl_b32 s83, s83, 15\n" \
"s_add_u32 s17, s17, s83\n" \
"ds_read_b64_tr_b16 a[198:199], v240 offset:44032\n" \
"v_mfma_f32_16x16x32_bf16 v[100:103], a[200:203], a[140:143], v[100:103]\n" \
"s_waitcnt lgkmcnt(8)\n" \
"s_lshl_b32 s84, s84, 7\n" \
"s_add_u32 s17, s17, s84\n" \
"v_mfma_f32_16x16x32_bf16 v[8:11], a[208:211], a[128:131], v[8:11]\n" \
"s_lshl_b32 s87, %[wid], 13\n" \
"s_add_u32 s87, s87, 0x20000\n" \
"ds_read_b64_tr_b16 a[200:201], v241 offset:33792\n" \
"v_mfma_f32_16x16x32_bf16 v[8:11], a[212:215], a[144:147], v[8:11]\n" \
"s_mov_b32 s25, 1073741824\n" \
"s_lshl_b32 s26, s68, 7\n" \
"s_add_u32 s26, s26, 655360\n" \
"v_mfma_f32_16x16x32_bf16 v[40:43], a[212:215], a[148:151], v[40:43]\n" \
"s_lshl_b32 s81, s68, 8\n" \
"s_add_u32 s81, s81, 81920\n" \
"ds_read_b64_tr_b16 a[202:203], v241 offset:35840\n" \
"v_mfma_f32_16x16x32_bf16 v[40:43], a[208:211], a[132:135], v[40:43]\n" \
"s_lshl_b32 s84, s64, 15\n" \
"s_add_u32 s84, s84, 41943040\n" \
"s_lshl_b32 s29, s76, 18\n" \
"v_mfma_f32_16x16x32_bf16 v[72:75], a[208:211], a[136:139], v[72:75]\n" \
"s_add_u32 s29, s29, s84\n" \
"s_lshl_b32 s37, s77, 18\n" \
"ds_read_b64_tr_b16 a[204:205], v241 offset:41984\n" \
"v_mfma_f32_16x16x32_bf16 v[72:75], a[212:215], a[152:155], v[72:75]\n" \
"s_add_u32 s37, s37, s84\n" \
"s_lshl_b32 s83, s66, 13\n" \
"s_add_u32 s30, s29, s83\n" \
"v_mfma_f32_16x16x32_bf16 v[104:107], a[212:215], a[156:159], v[104:107]\n" \
"s_lshl3_add_u32 s83, s67, s64\n" \
"s_lshl3_add_u32 s83, s83, %[wid]\n" \
"ds_read_b64_tr_b16 a[206:207], v241 offset:44032\n" \
"v_mfma_f32_16x16x32_bf16 v[104:107], a[208:211], a[140:143], v[104:107]\n" \
"s_waitcnt lgkmcnt(8)\n" \
"s_lshl_b32 s83, s83, 4\n" \
"s_add_u32 s31, s83, 73728\n" \
"v_mfma_f32_16x16x32_bf16 v[12:15], a[216:219], a[128:131], v[12:15]\n" \
"s_mul_i32 s69, s64, 28\n" \
"ds_read_b64_tr_b16 a[208:209], v240 offset:34304\n" \
"v_mfma_f32_16x16x32_bf16 v[12:15], a[220:223], a[144:147], v[12:15]\n" \
"s_cmp_lt_u32 s65, 28\n" \
"s_cselect_b32 s83, s65, 0\n" \
"s_add_u32 s69, s69, s83\n" \
"v_mfma_f32_16x16x32_bf16 v[44:47], a[220:223], a[148:151], v[44:47]\n" \
"s_add_u32 s85, s94, 4096\n" \
"s_lshl_b32 s79, s69, 13\n" \
"ds_read_b64_tr_b16 a[210:211], v240 offset:36352\n" \
"v_mfma_f32_16x16x32_bf16 v[44:47], a[216:219], a[132:135], v[44:47]\n" \
"s_lshl_b32 s83, %[wid], 8\n" \
"buffer_store_dwordx4 v[184:187], v248, s[32:35], s85 offen offset:2048 sc1\n" \
"v_mfma_f32_16x16x32_bf16 v[76:79], a[216:219], a[136:139], v[76:79]\n" \
"s_add_u32 s79, s79, s83\n" \
"s_add_u32 s80, s79, 4096\n" \
"ds_read_b64_tr_b16 a[212:213], v240 offset:42496\n" \
"v_mfma_f32_16x16x32_bf16 v[76:79], a[220:223], a[152:155], v[76:79]\n" \
"s_mov_b64 s[42:43], s[54:55]\n" \
"v_mfma_f32_16x16x32_bf16 v[108:111], a[220:223], a[156:159], v[108:111]\n" \
"ds_read_b64_tr_b16 a[214:215], v240 offset:44544\n" \
"v_mfma_f32_16x16x32_bf16 v[108:111], a[216:219], a[140:143], v[108:111]\n" \
"s_waitcnt lgkmcnt(8)\n" \
"v_mfma_f32_16x16x32_bf16 v[16:19], a[192:195], a[128:131], v[16:19]\n" \
"ds_read_b64_tr_b16 a[216:217], v241 offset:34304\n" \
"v_accvgpr_read_b32 v144, a56\n" \
"v_mfma_f32_16x16x32_bf16 v[16:19], a[196:199], a[144:147], v[16:19]\n" \
"s_bitcmp1_b32 %[bid], 6\n" \
"s_cselect_b64 s[40:41], s[56:57], s[52:53]\n" \
"s_cselect_b32 s39, 2048, 0\n" \
"v_mfma_f32_16x16x32_bf16 v[48:51], a[196:199], a[148:151], v[48:51]\n" \
"s_bfe_u32 s83, %[bid], 0x40002\n" \
"ds_read_b64_tr_b16 a[218:219], v241 offset:36352\n" \
"v_accvgpr_read_b32 v145, a57\n" \
"v_mfma_f32_16x16x32_bf16 v[48:51], a[192:195], a[132:135], v[48:51]\n" \
"s_and_b32 s84, %[bid], 3\n" \
"ds_read_b64_tr_b16 a[220:221], v241 offset:42496\n" \
"v_accvgpr_read_b32 v146, a58\n" \
"v_mfma_f32_16x16x32_bf16 v[80:83], a[192:195], a[136:139], v[80:83]\n" \
"s_lshl_b32 s22, %[wid], 3\n" \
"ds_read_b64_tr_b16 a[222:223], v241 offset:44544\n" \
"v_accvgpr_read_b32 v147, a59\n" \
"v_mfma_f32_16x16x32_bf16 v[80:83], a[196:199], a[152:155], v[80:83]\n" \
"s_and_b32 s22, s22, 16\n" \
"v_accvgpr_read_b32 v148, a60\n" \
"v_accvgpr_read_b32 v149, a61\n" \
"v_mfma_f32_16x16x32_bf16 v[112:115], a[196:199], a[156:159], v[112:115]\n" \
"s_and_b32 s23, %[wid], 1\n" \
"v_accvgpr_read_b32 v150, a62\n" \
"v_accvgpr_read_b32 v151, a63\n" \
"v_mfma_f32_16x16x32_bf16 v[112:115], a[192:195], a[140:143], v[112:115]\n" \
"s_waitcnt lgkmcnt(8)\n" \
"v_max3_f32 v250, |v144|, |v145|, v250\n" \
"v_max3_f32 v250, |v146|, |v147|, v250\n" \
"v_mfma_f32_16x16x32_bf16 v[20:23], a[200:203], a[128:131], v[20:23]\n" \
"s_add_u32 s38, s22, s23\n" \
"v_max3_f32 v250, |v148|, |v149|, v250\n" \
"v_max3_f32 v250, |v150|, |v151|, v250\n" \
"v_mfma_f32_16x16x32_bf16 v[20:23], a[204:207], a[144:147], v[20:23]\n" \
"s_lshl_b32 s72, s83, 5\n" \
"v_cvt_pk_bf16_f32 v144, v144, v145\n" \
"v_cvt_pk_bf16_f32 v145, v146, v147\n" \
"v_mfma_f32_16x16x32_bf16 v[52:55], a[204:207], a[148:151], v[52:55]\n" \
"s_add_u32 s38, s38, s72\n" \
"v_cvt_pk_bf16_f32 v146, v148, v149\n" \
"v_cvt_pk_bf16_f32 v147, v150, v151\n" \
"v_mfma_f32_16x16x32_bf16 v[52:55], a[200:203], a[132:135], v[52:55]\n" \
"s_mul_i32 s38, s38, 3584\n" \
"ds_write_b128 v249, v[144:147] offset:7168\n" \
"v_lshlrev_b32 v252, 2, v251\n" \
"v_mfma_f32_16x16x32_bf16 v[84:87], a[200:203], a[136:139], v[84:87]\n" \
"s_waitcnt lgkmcnt(1)\n" \
"s_barrier\n" \
"v_mov_b32 v254, 0x3f803f80\n" \
"v_mfma_f32_16x16x32_bf16 v[84:87], a[204:207], a[152:155], v[84:87]\n" \
"s_lshl_b32 s72, s84, 7\n" \
"ds_read_b64_tr_b16 a[192:193], v240 offset:49152\n" \
"ds_read_b64_tr_b16 a[194:195], v240 offset:51200\n" \
"v_mfma_f32_16x16x32_bf16 v[116:119], a[204:207], a[156:159], v[116:119]\n" \
"s_add_u32 s38, s38, s72\n" \
"ds_read_b64_tr_b16 a[196:197], v240 offset:57344\n" \
"ds_read_b64_tr_b16 a[198:199], v240 offset:59392\n" \
"v_mfma_f32_16x16x32_bf16 v[116:119], a[200:203], a[140:143], v[116:119]\n" \
"s_lshl3_add_u32 s39, s22, s39\n" \
"v_accvgpr_write_b32 a48, v254\n" \
"v_accvgpr_write_b32 a49, v254\n" \
"v_mfma_f32_16x16x32_bf16 v[24:27], a[208:211], a[128:131], v[24:27]\n" \
"s_lshl_b32 s23, s23, 10\n" \
"ds_read_b64_tr_b16 a[200:201], v241 offset:49152\n" \
"v_accvgpr_write_b32 a50, v254\n" \
"v_mfma_f32_16x16x32_bf16 v[24:27], a[212:215], a[144:147], v[24:27]\n" \
"s_add_u32 s39, s39, s23\n" \
"v_accvgpr_write_b32 a51, v254\n" \
"v_accvgpr_write_b32 a56, 1\n" \
"v_mfma_f32_16x16x32_bf16 v[56:59], a[212:215], a[148:151], v[56:59]\n" \
"s_lshl_b32 s22, s84, 17\n" \
"ds_read_b64_tr_b16 a[202:203], v241 offset:51200\n" \
"v_accvgpr_read_b32 v152, a120\n" \
"v_mfma_f32_16x16x32_bf16 v[56:59], a[208:211], a[132:135], v[56:59]\n" \
"s_add_u32 s85, s71, 593920\n" \
"v_accvgpr_read_b32 v153, a121\n" \
"v_accvgpr_read_b32 v154, a122\n" \
"v_mfma_f32_16x16x32_bf16 v[88:91], a[208:211], a[136:139], v[88:91]\n" \
"s_add_u32 s39, s39, s22\n" \
"buffer_store_dwordx4 v[144:147], v248, s[32:35], s85 offen offset:3072 sc1\n" \
"v_mfma_f32_16x16x32_bf16 v[88:91], a[212:215], a[152:155], v[88:91]\n" \
"s_lshr_b32 s22, s83, 2\n" \
"ds_read_b64_tr_b16 a[204:205], v241 offset:57344\n" \
"v_and_b32 v244, 31, v251\n" \
"v_mfma_f32_16x16x32_bf16 v[120:123], a[212:215], a[156:159], v[120:123]\n" \
"s_lshl_b32 s22, s22, 12\n" \
"ds_read_b64_tr_b16 a[206:207], v241 offset:59392\n" \
"v_lshlrev_b32 v244, 2, v244\n" \
"v_mfma_f32_16x16x32_bf16 v[120:123], a[208:211], a[140:143], v[120:123]\n" \
"s_add_u32 s39, s39, s22\n" \
"v_accvgpr_read_b32 v155, a123\n" \
"v_accvgpr_read_b32 v156, a124\n" \
"v_mfma_f32_16x16x32_bf16 v[28:31], a[216:219], a[128:131], v[28:31]\n" \
"s_and_b32 s22, s83, 3\n" \
"ds_read_b64_tr_b16 a[208:209], v240 offset:49664\n" \
"v_accvgpr_read_b32 v157, a125\n" \
"v_mfma_f32_16x16x32_bf16 v[28:31], a[220:223], a[144:147], v[28:31]\n" \
"s_lshl_b32 s22, s22, 8\n" \
"v_accvgpr_read_b32 v158, a126\n" \
"v_accvgpr_read_b32 v159, a127\n" \
"v_mfma_f32_16x16x32_bf16 v[60:63], a[220:223], a[148:151], v[60:63]\n" \
"s_add_u32 s39, s39, s22\n" \
"ds_read_b64_tr_b16 a[210:211], v240 offset:51712\n" \
"v_max3_f32 v250, |v152|, |v153|, v250\n" \
"v_mfma_f32_16x16x32_bf16 v[60:63], a[216:219], a[132:135], v[60:63]\n" \
"s_add_u32 s39, s39, 77070336\n" \
"v_max3_f32 v250, |v154|, |v155|, v250\n" \
"v_max3_f32 v250, |v156|, |v157|, v250\n" \
"v_mfma_f32_16x16x32_bf16 v[92:95], a[216:219], a[136:139], v[92:95]\n" \
"s_and_b32 s83, %[bid], 127\n" \
"ds_read_b64_tr_b16 a[212:213], v240 offset:57856\n" \
"v_max3_f32 v250, |v158|, |v159|, v250\n" \
"v_mfma_f32_16x16x32_bf16 v[92:95], a[220:223], a[152:155], v[92:95]\n" \
"s_lshl_b32 s78, %[wid], 7\n" \
"v_cvt_pk_bf16_f32 v152, v152, v153\n" \
"v_cvt_pk_bf16_f32 v153, v154, v155\n" \
"v_mfma_f32_16x16x32_bf16 v[124:127], a[220:223], a[156:159], v[124:127]\n" \
"s_waitcnt vmcnt(4)\n" \
"ds_read_b64_tr_b16 a[214:215], v240 offset:59904\n" \
"v_mfma_f32_16x16x32_bf16 v[124:127], a[216:219], a[140:143], v[124:127]\n" \
"s_waitcnt lgkmcnt(8)\n" \
"v_cvt_pk_bf16_f32 v154, v156, v157\n" \
"v_cvt_pk_bf16_f32 v155, v158, v159\n" \
"v_mfma_f32_16x16x32_bf16 v[0:3], a[192:195], a[224:227], v[0:3]\n" \
"s_add_u32 s78, s78, s83\n" \
"ds_read_b64_tr_b16 a[216:217], v241 offset:49664\n" \
"v_accvgpr_write_b32 a120, v152\n" \
"v_mfma_f32_16x16x32_bf16 v[0:3], a[196:199], a[240:243], v[0:3]\n" \
"s_lshl_b32 s78, s78, 2\n" \
"ds_read_b64_tr_b16 a[218:219], v241 offset:51712\n" \
"ds_read_b64_tr_b16 a[220:221], v241 offset:57856\n" \
"v_mfma_f32_16x16x32_bf16 v[32:35], a[196:199], a[244:247], v[32:35]\n" \
"s_add_u32 s78, s78, 659456\n" \
"ds_read_b64_tr_b16 a[222:223], v241 offset:59904\n" \
"ds_read_b128 a[128:131], v248 offset:0\n" \
"v_mfma_f32_16x16x32_bf16 v[32:35], a[192:195], a[228:231], v[32:35]\n" \
"s_waitcnt lgkmcnt(9)\n" \
"s_cmp_lt_u32 s67, 4\n" \
"s_cselect_b32 s78, 1073741824, s78\n" \
"v_mfma_f32_16x16x32_bf16 v[4:7], a[200:203], a[224:227], v[4:7]\n" \
"s_lshl2_add_u32 s72, s83, %[wid]\n" \
"ds_read_b64_tr_b16 a[192:193], v240 offset:50176\n" \
"ds_read_b128 a[132:135], v248 offset:1024\n" \
"v_mfma_f32_16x16x32_bf16 v[4:7], a[204:207], a[240:243], v[4:7]\n" \
"s_mul_i32 s72, s72, 3584\n" \
"ds_read_b64_tr_b16 a[194:195], v240 offset:52224\n" \
"ds_read_b64_tr_b16 a[196:197], v240 offset:58368\n" \
"v_mfma_f32_16x16x32_bf16 v[36:39], a[204:207], a[244:247], v[36:39]\n" \
"s_mov_b32 s76, 0\n" \
"ds_read_b64_tr_b16 a[198:199], v240 offset:60416\n" \
"v_accvgpr_write_b32 a121, v153\n" \
"v_mfma_f32_16x16x32_bf16 v[36:39], a[200:203], a[228:231], v[36:39]\n" \
"s_waitcnt lgkmcnt(10)\n" \
"ds_read_b128 a[136:139], v248 offset:2048\n" \
"v_accvgpr_write_b32 a122, v154\n" \
"v_mfma_f32_16x16x32_bf16 v[8:11], a[208:211], a[224:227], v[8:11]\n" \
"s_add_u32 s85, s94, 4096\n" \
"buffer_store_dwordx4 v[188:191], v248, s[32:35], s85 offen offset:3072 sc1\n" \
"v_mfma_f32_16x16x32_bf16 v[8:11], a[212:215], a[240:243], v[8:11]\n" \
"s_lshl_b32 s84, s67, 1\n" \
"ds_read_b64_tr_b16 a[200:201], v241 offset:50176\n" \
"ds_read_b64_tr_b16 a[202:203], v241 offset:52224\n" \
"v_mfma_f32_16x16x32_bf16 v[40:43], a[212:215], a[244:247], v[40:43]\n" \
".Lst0_7_%=:\n" \
"s_cbranch_execz .Lst0_11_%=\n" \
"s_sub_u32 s84, s84, 11\n" \
"ds_read_b64_tr_b16 a[204:205], v241 offset:58368\n" \
"ds_read_b64_tr_b16 a[206:207], v241 offset:60416\n" \
"v_mfma_f32_16x16x32_bf16 v[40:43], a[208:211], a[228:231], v[40:43]\n" \
"s_waitcnt lgkmcnt(11)\n" \
"ds_read_b128 a[140:143], v248 offset:3072\n" \
"v_accvgpr_write_b32 a123, v155\n" \
"v_mfma_f32_16x16x32_bf16 v[12:15], a[216:219], a[224:227], v[12:15]\n" \
"s_max_i32 s84, s84, 0\n" \
"ds_read_b64_tr_b16 a[208:209], v240 offset:50688\n" \
"v_max_u32_dpp v250, v250, v250 quad_perm:[1,0,3,2] row_mask:0xf bank_mask:0xf\n" \
"v_mfma_f32_16x16x32_bf16 v[12:15], a[220:223], a[240:243], v[12:15]\n" \
"s_add_u32 s83, s84, s67\n" \
"ds_read_b64_tr_b16 a[210:211], v240 offset:52736\n" \
"ds_read_b64_tr_b16 a[212:213], v240 offset:58880\n" \
"v_mfma_f32_16x16x32_bf16 v[44:47], a[220:223], a[244:247], v[44:47]\n" \
"s_and_b32 s83, s83, 1\n" \
"ds_read_b64_tr_b16 a[214:215], v240 offset:60928\n" \
"ds_read_b128 a[156:159], v248 offset:7168\n" \
"v_mfma_f32_16x16x32_bf16 v[44:47], a[216:219], a[228:231], v[44:47]\n" \
"s_waitcnt lgkmcnt(11)\n" \
"v_max_u32_dpp v250, v250, v250 quad_perm:[2,3,0,1] row_mask:0xf bank_mask:0xf\n" \
"v_cvt_pk_bf16_f32 v192, v0, v1\n" \
"v_mfma_f32_16x16x32_bf16 v[16:19], a[192:195], a[224:227], v[16:19]\n" \
"s_lshl_b32 s83, s83, 15\n" \
"ds_read_b64_tr_b16 a[216:217], v241 offset:50688\n" \
"ds_read_b128 a[152:155], v248 offset:6144\n" \
"v_mfma_f32_16x16x32_bf16 v[16:19], a[196:199], a[240:243], v[16:19]\n" \
"s_movk_i32 s27, 0x4000\n" \
"ds_read_b64_tr_b16 a[218:219], v241 offset:52736\n" \
"ds_read_b64_tr_b16 a[220:221], v241 offset:58880\n" \
"v_mfma_f32_16x16x32_bf16 v[48:51], a[196:199], a[244:247], v[48:51]\n" \
"s_mov_b32 s92, s21\n" \
"ds_read_b64_tr_b16 a[222:223], v241 offset:60928\n" \
"v_max_u32_dpp v250, v250, v250 row_half_mirror row_mask:0xf bank_mask:0xf\n" \
"v_mfma_f32_16x16x32_bf16 v[48:51], a[192:195], a[228:231], v[48:51]\n" \
"s_waitcnt lgkmcnt(11)\n" \
"ds_read_b128 a[148:151], v248 offset:5120\n" \
"v_cvt_pk_bf16_f32 v193, v2, v3\n" \
"v_mfma_f32_16x16x32_bf16 v[20:23], a[200:203], a[224:227], v[20:23]\n" \
"s_sub_u32 s22, s18, 524288\n" \
"ds_read_b64_tr_b16 a[192:193], v240 offset:49152\n" \
"v_max_u32_dpp v250, v250, v250 row_mirror row_mask:0xf bank_mask:0xf\n" \
"v_mfma_f32_16x16x32_bf16 v[20:23], a[204:207], a[240:243], v[20:23]\n" \
"s_lshl_b32 s77, s84, 16\n" \
"ds_read_b64_tr_b16 a[194:195], v240 offset:51200\n" \
"ds_read_b64_tr_b16 a[196:197], v240 offset:57344\n" \
"v_mfma_f32_16x16x32_bf16 v[52:55], a[204:207], a[244:247], v[52:55]\n" \
"s_add_u32 s77, s77, s22\n" \
"ds_read_b64_tr_b16 a[198:199], v240 offset:59392\n" \
"ds_read_b128 a[144:147], v248 offset:4096\n" \
"v_mfma_f32_16x16x32_bf16 v[52:55], a[200:203], a[228:231], v[52:55]\n" \
"s_waitcnt lgkmcnt(12)\n" \
"v_mov_b32 v152, v250\n" \
"v_cvt_pk_bf16_f32 v194, v4, v5\n" \
"v_mfma_f32_16x16x32_bf16 v[24:27], a[208:211], a[224:227], v[24:27]\n" \
"s_movk_i32 s91, 0x4000\n" \
"ds_read_b64_tr_b16 a[200:201], v241 offset:49152\n" \
"v_cvt_pk_bf16_f32 v195, v6, v7\n" \
"v_mfma_f32_16x16x32_bf16 v[24:27], a[212:215], a[240:243], v[24:27]\n" \
"ds_read_b64_tr_b16 a[202:203], v241 offset:51200\n" \
"ds_read_b64_tr_b16 a[204:205], v241 offset:57344\n" \
"v_mfma_f32_16x16x32_bf16 v[56:59], a[212:215], a[244:247], v[56:59]\n" \
"ds_read_b64_tr_b16 a[206:207], v241 offset:59392\n" \
"v_cvt_pk_bf16_f32 v208, v32, v33\n" \
"v_mfma_f32_16x16x32_bf16 v[56:59], a[208:211], a[228:231], v[56:59]\n" \
"s_waitcnt lgkmcnt(10)\n" \
"v_permlane16_swap_b32 v250, v152\n" \
"v_mfma_f32_16x16x32_bf16 v[28:31], a[216:219], a[224:227], v[28:31]\n" \
"ds_read_b64_tr_b16 a[208:209], v240 offset:49664\n" \
"v_max_u32 v250, v250, v152\n" \
"v_mfma_f32_16x16x32_bf16 v[28:31], a[220:223], a[240:243], v[28:31]\n" \
"ds_read_b64_tr_b16 a[210:211], v240 offset:51712\n" \
"ds_read_b64_tr_b16 a[212:213], v240 offset:57856\n" \
"v_mfma_f32_16x16x32_bf16 v[60:63], a[220:223], a[244:247], v[60:63]\n" \
"ds_read_b64_tr_b16 a[214:215], v240 offset:59904\n" \
"v_mov_b32 v152, v250\n" \
"v_mfma_f32_16x16x32_bf16 v[60:63], a[216:219], a[228:231], v[60:63]\n" \
"s_waitcnt lgkmcnt(9)\n" \
"v_cvt_pk_bf16_f32 v209, v34, v35\n" \
"v_cvt_pk_bf16_f32 v210, v36, v37\n" \
"v_mfma_f32_16x16x32_bf16 v[64:67], a[192:195], a[232:235], v[64:67]\n" \
"ds_read_b64_tr_b16 a[216:217], v241 offset:49664\n" \
"v_cvt_pk_bf16_f32 v211, v38, v39\n" \
"v_mfma_f32_16x16x32_bf16 v[64:67], a[196:199], a[248:251], v[64:67]\n" \
"ds_read_b64_tr_b16 a[218:219], v241 offset:51712\n" \
"ds_read_b64_tr_b16 a[220:221], v241 offset:57856\n" \
"v_mfma_f32_16x16x32_bf16 v[96:99], a[196:199], a[252:255], v[96:99]\n" \
"ds_read_b64_tr_b16 a[222:223], v241 offset:59904\n" \
"v_cvt_pk_bf16_f32 v196, v8, v9\n" \
"v_mfma_f32_16x16x32_bf16 v[96:99], a[192:195], a[236:239], v[96:99]\n" \
"s_waitcnt lgkmcnt(8)\n" \
"v_permlane32_swap_b32 v250, v152\n" \
"v_mfma_f32_16x16x32_bf16 v[68:71], a[200:203], a[232:235], v[68:71]\n" \
"ds_read_b64_tr_b16 a[192:193], v240 offset:50176\n" \
"v_max_u32 v250, v250, v152\n" \
"v_mfma_f32_16x16x32_bf16 v[68:71], a[204:207], a[248:251], v[68:71]\n" \
"ds_read_b64_tr_b16 a[194:195], v240 offset:52224\n" \
"ds_read_b64_tr_b16 a[196:197], v240 offset:58368\n" \
"v_mfma_f32_16x16x32_bf16 v[100:103], a[204:207], a[252:255], v[100:103]\n" \
"ds_read_b64_tr_b16 a[198:199], v240 offset:60416\n" \
"v_max_u32 v250, 1, v250\n" \
"v_mfma_f32_16x16x32_bf16 v[100:103], a[200:203], a[236:239], v[100:103]\n" \
"s_waitcnt lgkmcnt(8)\n" \
"v_cvt_pk_bf16_f32 v197, v10, v11\n" \
"v_cvt_pk_bf16_f32 v198, v12, v13\n" \
"v_mfma_f32_16x16x32_bf16 v[72:75], a[208:211], a[232:235], v[72:75]\n" \
"ds_read_b64_tr_b16 a[200:201], v241 offset:50176\n" \
"v_cvt_pk_bf16_f32 v199, v14, v15\n" \
"v_mfma_f32_16x16x32_bf16 v[72:75], a[212:215], a[248:251], v[72:75]\n" \
"ds_read_b64_tr_b16 a[202:203], v241 offset:52224\n" \
"ds_read_b64_tr_b16 a[204:205], v241 offset:58368\n" \
"v_mfma_f32_16x16x32_bf16 v[104:107], a[212:215], a[252:255], v[104:107]\n" \
"ds_read_b64_tr_b16 a[206:207], v241 offset:60416\n" \
"v_cvt_pk_bf16_f32 v212, v40, v41\n" \
"v_mfma_f32_16x16x32_bf16 v[104:107], a[208:211], a[236:239], v[104:107]\n" \
"s_waitcnt lgkmcnt(8)\n" \
"v_cvt_pk_bf16_f32 v213, v42, v43\n" \
"v_cvt_pk_bf16_f32 v214, v44, v45\n" \
"v_mfma_f32_16x16x32_bf16 v[76:79], a[216:219], a[232:235], v[76:79]\n" \
"ds_read_b64_tr_b16 a[208:209], v240 offset:50688\n" \
"v_cvt_pk_bf16_f32 v215, v46, v47\n" \
"v_mfma_f32_16x16x32_bf16 v[76:79], a[220:223], a[248:251], v[76:79]\n" \
"ds_read_b64_tr_b16 a[210:211], v240 offset:52736\n" \
"ds_read_b64_tr_b16 a[212:213], v240 offset:58880\n" \
"v_mfma_f32_16x16x32_bf16 v[108:111], a[220:223], a[252:255], v[108:111]\n" \
"ds_read_b64_tr_b16 a[214:215], v240 offset:60928\n" \
"v_cvt_pk_bf16_f32 v200, v16, v17\n" \
"v_mfma_f32_16x16x32_bf16 v[108:111], a[216:219], a[236:239], v[108:111]\n" \
"s_waitcnt lgkmcnt(8)\n" \
"v_cvt_pk_bf16_f32 v201, v18, v19\n" \
"v_cvt_pk_bf16_f32 v202, v20, v21\n" \
"v_mfma_f32_16x16x32_bf16 v[80:83], a[192:195], a[232:235], v[80:83]\n" \
"ds_read_b64_tr_b16 a[216:217], v241 offset:50688\n" \
"v_cvt_pk_bf16_f32 v203, v22, v23\n" \
"v_mfma_f32_16x16x32_bf16 v[80:83], a[196:199], a[248:251], v[80:83]\n" \
"ds_read_b64_tr_b16 a[218:219], v241 offset:52736\n" \
"ds_read_b64_tr_b16 a[220:221], v241 offset:58880\n" \
"v_mfma_f32_16x16x32_bf16 v[112:115], a[196:199], a[252:255], v[112:115]\n" \
"ds_read_b64_tr_b16 a[222:223], v241 offset:60928\n" \
"v_cvt_pk_bf16_f32 v216, v48, v49\n" \
"v_mfma_f32_16x16x32_bf16 v[112:115], a[192:195], a[236:239], v[112:115]\n" \
"s_waitcnt lgkmcnt(8)\n" \
"v_cvt_pk_bf16_f32 v217, v50, v51\n" \
"v_cvt_pk_bf16_f32 v218, v52, v53\n" \
"v_mfma_f32_16x16x32_bf16 v[84:87], a[200:203], a[232:235], v[84:87]\n" \
"s_cmp_lg_u32 s84, 0\n" \
"s_cmov_b32 s21, s22\n" \
"s_cmov_b32 s91, 0x8000\n" \
"v_mfma_f32_16x16x32_bf16 v[84:87], a[204:207], a[248:251], v[84:87]\n" \
"v_cvt_pk_bf16_f32 v219, v54, v55\n" \
"v_cvt_pk_bf16_f32 v204, v24, v25\n" \
"v_mfma_f32_16x16x32_bf16 v[116:119], a[204:207], a[252:255], v[116:119]\n" \
"v_cvt_pk_bf16_f32 v205, v26, v27\n" \
"v_cvt_pk_bf16_f32 v206, v28, v29\n" \
"v_mfma_f32_16x16x32_bf16 v[116:119], a[200:203], a[236:239], v[116:119]\n" \
"s_waitcnt lgkmcnt(4)\n" \
"v_cvt_pk_bf16_f32 v207, v30, v31\n" \
"v_cvt_pk_bf16_f32 v220, v56, v57\n" \
"v_mfma_f32_16x16x32_bf16 v[88:91], a[208:211], a[232:235], v[88:91]\n" \
"v_cvt_pk_bf16_f32 v221, v58, v59\n" \
"v_cvt_pk_bf16_f32 v222, v60, v61\n" \
"v_mfma_f32_16x16x32_bf16 v[88:91], a[212:215], a[248:251], v[88:91]\n" \
"v_cvt_pk_bf16_f32 v223, v62, v63\n" \
"v_cvt_pk_bf16_f32 v160, v64, v65\n" \
"v_mfma_f32_16x16x32_bf16 v[120:123], a[212:215], a[252:255], v[120:123]\n" \
"v_cvt_pk_bf16_f32 v161, v66, v67\n" \
"v_cvt_pk_bf16_f32 v162, v68, v69\n" \
"v_mfma_f32_16x16x32_bf16 v[120:123], a[208:211], a[236:239], v[120:123]\n" \
"s_waitcnt lgkmcnt(0)\n" \
"v_cvt_pk_bf16_f32 v163, v70, v71\n" \
"v_cvt_pk_bf16_f32 v164, v72, v73\n" \
"v_mfma_f32_16x16x32_bf16 v[92:95], a[216:219], a[232:235], v[92:95]\n" \
"v_cvt_pk_bf16_f32 v165, v74, v75\n" \
"v_cvt_pk_bf16_f32 v166, v76, v77\n" \
"v_mfma_f32_16x16x32_bf16 v[92:95], a[220:223], a[248:251], v[92:95]\n" \
"v_cvt_pk_bf16_f32 v167, v78, v79\n" \
"v_cvt_pk_bf16_f32 v168, v80, v81\n" \
"v_mfma_f32_16x16x32_bf16 v[124:127], a[220:223], a[252:255], v[124:127]\n" \
"v_cvt_pk_bf16_f32 v169, v82, v83\n" \
"v_cvt_pk_bf16_f32 v170, v84, v85\n" \
"v_mfma_f32_16x16x32_bf16 v[124:127], a[216:219], a[236:239], v[124:127]\n" \
"s_lshl_b32 s85, s68, 4\n" \
"v_cvt_pk_bf16_f32 v171, v86, v87\n" \
"v_add_u32 v242, 0x10000, v248\n" \
  \
".Lat_b_%=:\n" \
"s_cmp_ge_u32 %[wid], 2\n" \
"s_cbranch_scc1 .Ldhi_%=\n" \
"v_mfma_f32_16x16x32_bf16 v[128:131], a[128:131], v[192:195], v[224:227]\n" \
"s_add_u32 s85, s85, s67\n" \
"v_xor_b32 v249, 0x4000, v249\n" \
"v_mfma_f32_16x16x32_bf16 v[128:131], a[132:135], v[196:199], v[128:131]\n" \
"s_barrier\n" \
"s_lshl2_add_u32 s85, s85, %[wid]\n" \
"v_mfma_f32_16x16x32_bf16 v[128:131], a[136:139], v[200:203], v[128:131]\n" \
"s_lshl_b32 s85, s85, 2\n" \
"ds_read_b128 a[192:195], v242 offset:0\n" \
"v_cvt_pk_bf16_f32 v172, v88, v89\n" \
"v_mfma_f32_16x16x32_bf16 v[128:131], a[140:143], v[204:207], v[128:131]\n" \
"s_add_u32 s85, s85, 82048\n" \
"ds_read_b128 a[196:199], v242 offset:1024\n" \
"v_cvt_pk_bf16_f32 v173, v90, v91\n" \
"v_mfma_f32_16x16x32_bf16 v[132:135], a[156:159], v[204:207], v[228:231]\n" \
"s_waitcnt vmcnt(1)\n" \
"buffer_store_dword v250, off, s[32:35], s85 sc1\n" \
"v_cvt_pk_bf16_f32 v174, v92, v93\n" \
"v_mfma_f32_16x16x32_bf16 v[132:135], a[152:155], v[200:203], v[132:135]\n" \
"ds_read_b128 a[200:203], v242 offset:2048\n" \
"ds_read_b128 a[204:207], v242 offset:3072\n" \
"v_mfma_f32_16x16x32_bf16 v[132:135], a[148:151], v[196:199], v[132:135]\n" \
"v_exp_f32 v128, v128\n" \
"v_mfma_f32_16x16x32_bf16 v[132:135], a[144:147], v[192:195], v[132:135]\n" \
"s_add_u32 s85, s94, 589824\n" \
"ds_read_b128 a[208:211], v242 offset:4096\n" \
"ds_read_b128 a[212:215], v242 offset:5120\n" \
"v_mfma_f32_16x16x32_bf16 v[144:147], a[128:131], v[208:211], 0\n" \
"v_exp_f32 v129, v129\n" \
"v_mfma_f32_16x16x32_bf16 v[144:147], a[132:135], v[212:215], v[144:147]\n" \
"buffer_store_dwordx4 a[64:67], v248, s[32:35], s85 offen offset:0 sc1\n" \
"v_mfma_f32_16x16x32_bf16 v[144:147], a[136:139], v[216:219], v[144:147]\n" \
"v_exp_f32 v130, v130\n" \
"v_mfma_f32_16x16x32_bf16 v[144:147], a[140:143], v[220:223], v[144:147]\n" \
"ds_read_b128 a[216:219], v242 offset:6144\n" \
"v_cvt_pk_bf16_f32 v128, v128, v129\n" \
"v_mfma_f32_16x16x32_bf16 v[148:151], a[156:159], v[220:223], 0\n" \
"v_exp_f32 v131, v131\n" \
"v_mfma_f32_16x16x32_bf16 v[148:151], a[152:155], v[216:219], v[148:151]\n" \
"v_exp_f32 v132, v132\n" \
"v_mfma_f32_16x16x32_bf16 v[148:151], a[148:151], v[212:215], v[148:151]\n" \
"ds_read_b128 a[220:223], v242 offset:7168\n" \
"v_cvt_pk_bf16_f32 v129, v130, v131\n" \
"v_mfma_f32_16x16x32_bf16 v[148:151], a[144:147], v[208:211], v[148:151]\n" \
"v_exp_f32 v133, v133\n" \
"v_mfma_f32_16x16x32_bf16 v[152:155], a[160:163], v[208:211], 0\n" \
"ds_read_b128 a[224:227], v242 offset:8192\n" \
"v_cvt_pk_bf16_f32 v130, v132, v133\n" \
"v_mfma_f32_16x16x32_bf16 v[152:155], a[164:167], v[212:215], v[152:155]\n" \
"v_exp_f32 v134, v134\n" \
"v_mfma_f32_16x16x32_bf16 v[152:155], a[168:171], v[216:219], v[152:155]\n" \
"ds_read_b128 a[228:231], v242 offset:9216\n" \
"ds_read_b128 a[232:235], v242 offset:10240\n" \
"v_mfma_f32_16x16x32_bf16 v[152:155], a[172:175], v[220:223], v[152:155]\n" \
"v_exp_f32 v135, v135\n" \
"v_mfma_f32_16x16x32_bf16 v[156:159], a[188:191], v[220:223], 0\n" \
"v_exp_f32 v144, v144\n" \
"v_mfma_f32_16x16x32_bf16 v[156:159], a[184:187], v[216:219], v[156:159]\n" \
"v_exp_f32 v145, v145\n" \
"v_mfma_f32_16x16x32_bf16 v[156:159], a[180:183], v[212:215], v[156:159]\n" \
"v_exp_f32 v146, v146\n" \
"v_cvt_pk_bf16_f32 v131, v134, v135\n" \
"v_mfma_f32_16x16x32_bf16 v[156:159], a[176:179], v[208:211], v[156:159]\n" \
"v_exp_f32 v147, v147\n" \
"v_mfma_f32_16x16x32_bf16 a[32:35], a[48:51], v[128:131], 0\n" \
"s_waitcnt lgkmcnt(7)\n" \
"v_exp_f32 v148, v148\n" \
"v_mfma_f32_16x16x32_bf16 v[0:3], a[192:195], v[128:131], 0\n" \
"s_add_u32 s85, s94, 589824\n" \
"v_exp_f32 v149, v149\n" \
"v_mfma_f32_16x16x32_bf16 v[4:7], a[196:199], v[128:131], 0\n" \
"v_exp_f32 v150, v150\n" \
"v_mfma_f32_16x16x32_bf16 v[8:11], a[200:203], v[128:131], 0\n" \
"buffer_store_dwordx4 a[72:75], v248, s[32:35], s85 offen offset:1024 sc1\n" \
"v_mfma_f32_16x16x32_bf16 v[12:15], a[204:207], v[128:131], 0\n" \
"s_waitcnt lgkmcnt(3)\n" \
"v_exp_f32 v151, v151\n" \
"v_mfma_f32_16x16x32_bf16 v[16:19], a[208:211], v[128:131], 0\n" \
"v_cvt_pk_bf16_f32 v144, v144, v145\n" \
"v_cvt_pk_bf16_f32 v145, v146, v147\n" \
"v_mfma_f32_16x16x32_bf16 v[20:23], a[212:215], v[128:131], 0\n" \
"v_exp_f32 v152, v152\n" \
"v_mfma_f32_16x16x32_bf16 v[24:27], a[216:219], v[128:131], 0\n" \
"v_cvt_pk_bf16_f32 v146, v148, v149\n" \
"v_cvt_pk_bf16_f32 v147, v150, v151\n" \
"v_mfma_f32_16x16x32_bf16 v[28:31], a[220:223], v[128:131], 0\n" \
"v_exp_f32 v153, v153\n" \
"v_mfma_f32_16x16x32_bf16 v[128:131], a[128:131], v[160:163], 0\n" \
"ds_read_b128 a[236:239], v242 offset:11264\n" \
"v_cvt_pk_bf16_f32 v152, v152, v153\n" \
"v_mfma_f32_16x16x32_bf16 v[128:131], a[132:135], v[164:167], v[128:131]\n" \
"v_exp_f32 v154, v154\n" \
"v_cvt_pk_bf16_f32 v175, v94, v95\n" \
"v_mfma_f32_16x16x32_bf16 v[128:131], a[136:139], v[168:171], v[128:131]\n" \
"ds_read_b128 a[240:243], v242 offset:12288\n" \
"ds_read_b128 a[244:247], v242 offset:13312\n" \
"v_mfma_f32_16x16x32_bf16 v[128:131], a[140:143], v[172:175], v[128:131]\n" \
"v_exp_f32 v155, v155\n" \
"v_mfma_f32_16x16x32_bf16 v[132:135], a[156:159], v[172:175], 0\n" \
"ds_read_b128 a[248:251], v242 offset:14336\n" \
"v_cvt_pk_bf16_f32 v153, v154, v155\n" \
"v_mfma_f32_16x16x32_bf16 v[132:135], a[152:155], v[168:171], v[132:135]\n" \
"v_exp_f32 v156, v156\n" \
"v_mfma_f32_16x16x32_bf16 v[132:135], a[148:151], v[164:167], v[132:135]\n" \
"ds_read_b128 a[252:255], v242 offset:15360\n" \
"v_cvt_pk_bf16_f32 v176, v96, v97\n" \
"v_mfma_f32_16x16x32_bf16 v[132:135], a[144:147], v[160:163], v[132:135]\n" \
"v_exp_f32 v157, v157\n" \
"v_mfma_f32_16x16x32_bf16 v[136:139], a[160:163], v[160:163], 0\n" \
"v_cvt_pk_bf16_f32 v154, v156, v157\n" \
"v_cvt_pk_bf16_f32 v177, v98, v99\n" \
"v_mfma_f32_16x16x32_bf16 v[136:139], a[164:167], v[164:167], v[136:139]\n" \
"v_exp_f32 v158, v158\n" \
"v_mfma_f32_16x16x32_bf16 v[136:139], a[168:171], v[168:171], v[136:139]\n" \
"s_add_u32 s85, s94, 589824\n" \
"v_cvt_pk_bf16_f32 v178, v100, v101\n" \
"v_cvt_pk_bf16_f32 v179, v102, v103\n" \
"v_mfma_f32_16x16x32_bf16 v[136:139], a[172:175], v[172:175], v[136:139]\n" \
"v_exp_f32 v159, v159\n" \
"v_mfma_f32_16x16x32_bf16 v[140:143], a[188:191], v[172:175], 0\n" \
"buffer_store_dwordx4 a[80:83], v248, s[32:35], s85 offen offset:2048 sc1\n" \
"v_mfma_f32_16x16x32_bf16 v[140:143], a[184:187], v[168:171], v[140:143]\n" \
"v_cvt_pk_bf16_f32 v155, v158, v159\n" \
"v_cvt_pk_bf16_f32 v180, v104, v105\n" \
"v_mfma_f32_16x16x32_bf16 v[140:143], a[180:183], v[164:167], v[140:143]\n" \
"v_exp_f32 v128, v128\n" \
"v_mfma_f32_16x16x32_bf16 v[140:143], a[176:179], v[160:163], v[140:143]\n" \
"v_cvt_pk_bf16_f32 v181, v106, v107\n" \
"v_cvt_pk_bf16_f32 v182, v108, v109\n" \
"v_mfma_f32_16x16x32_bf16 a[36:39], a[48:51], v[144:147], 0\n" \
"v_exp_f32 v129, v129\n" \
"v_mfma_f32_16x16x32_bf16 a[36:39], a[48:51], v[152:155], a[36:39]\n" \
"s_waitcnt lgkmcnt(4)\n" \
"ds_write_b128 v249, a[64:67] offset:32768\n" \
"v_cvt_pk_bf16_f32 v128, v128, v129\n" \
"v_mfma_f32_16x16x32_bf16 v[32:35], a[224:227], v[152:155], 0\n" \
"v_exp_f32 v130, v130\n" \
"v_mfma_f32_16x16x32_bf16 v[32:35], a[192:195], v[144:147], v[32:35]\n" \
"v_cvt_pk_bf16_f32 v183, v110, v111\n" \
"v_cvt_pk_bf16_f32 v184, v112, v113\n" \
"v_mfma_f32_16x16x32_bf16 v[36:39], a[196:199], v[144:147], 0\n" \
"v_exp_f32 v131, v131\n" \
"v_mfma_f32_16x16x32_bf16 v[36:39], a[228:231], v[152:155], v[36:39]\n" \
"v_cvt_pk_bf16_f32 v129, v130, v131\n" \
"v_cvt_pk_bf16_f32 v185, v114, v115\n" \
"v_mfma_f32_16x16x32_bf16 v[40:43], a[232:235], v[152:155], 0\n" \
"v_exp_f32 v132, v132\n" \
"v_mfma_f32_16x16x32_bf16 v[40:43], a[200:203], v[144:147], v[40:43]\n" \
"v_cvt_pk_bf16_f32 v186, v116, v117\n" \
"v_cvt_pk_bf16_f32 v187, v118, v119\n" \
"v_mfma_f32_16x16x32_bf16 v[44:47], a[204:207], v[144:147], 0\n" \
"v_exp_f32 v133, v133\n" \
"v_mfma_f32_16x16x32_bf16 v[44:47], a[236:239], v[152:155], v[44:47]\n" \
"s_waitcnt lgkmcnt(1)\n" \
"v_cvt_pk_bf16_f32 v130, v132, v133\n" \
"v_cvt_pk_bf16_f32 v188, v120, v121\n" \
"v_mfma_f32_16x16x32_bf16 v[48:51], a[240:243], v[152:155], 0\n" \
"v_exp_f32 v134, v134\n" \
"v_mfma_f32_16x16x32_bf16 v[48:51], a[208:211], v[144:147], v[48:51]\n" \
"v_cvt_pk_bf16_f32 v189, v122, v123\n" \
"v_cvt_pk_bf16_f32 v190, v124, v125\n" \
"v_mfma_f32_16x16x32_bf16 v[52:55], a[212:215], v[144:147], 0\n" \
"s_add_u32 s85, s94, 589824\n" \
"v_exp_f32 v135, v135\n" \
"v_mfma_f32_16x16x32_bf16 v[52:55], a[244:247], v[152:155], v[52:55]\n" \
"buffer_store_dwordx4 a[88:91], v248, s[32:35], s85 offen offset:3072 sc1\n" \
"v_mfma_f32_16x16x32_bf16 v[56:59], a[248:251], v[152:155], 0\n" \
"ds_write_b128 v249, a[72:75] offset:33792\n" \
"v_cvt_pk_bf16_f32 v131, v134, v135\n" \
"v_mfma_f32_16x16x32_bf16 v[56:59], a[216:219], v[144:147], v[56:59]\n" \
"v_exp_f32 v136, v136\n" \
"v_mfma_f32_16x16x32_bf16 v[60:63], a[220:223], v[144:147], 0\n" \
"v_cvt_pk_bf16_f32 v191, v126, v127\n" \
"v_mfma_f32_16x16x32_bf16 v[60:63], a[252:255], v[152:155], v[60:63]\n" \
"v_exp_f32 v137, v137\n" \
"v_mfma_f32_16x16x32_bf16 v[144:147], a[128:131], v[176:179], 0\n" \
"v_cvt_pk_bf16_f32 v136, v136, v137\n" \
"v_mfma_f32_16x16x32_bf16 v[144:147], a[132:135], v[180:183], v[144:147]\n" \
"v_exp_f32 v138, v138\n" \
"v_mfma_f32_16x16x32_bf16 v[144:147], a[136:139], v[184:187], v[144:147]\n" \
"ds_read_b128 a[128:131], v248 offset:16384\n" \
"ds_read_b128 a[132:135], v248 offset:17408\n" \
"v_mfma_f32_16x16x32_bf16 v[144:147], a[140:143], v[188:191], v[144:147]\n" \
"v_exp_f32 v139, v139\n" \
"v_mfma_f32_16x16x32_bf16 v[148:151], a[156:159], v[188:191], 0\n" \
"ds_read_b128 a[136:139], v248 offset:18432\n" \
"v_cvt_pk_bf16_f32 v137, v138, v139\n" \
"v_mfma_f32_16x16x32_bf16 v[148:151], a[152:155], v[184:187], v[148:151]\n" \
"v_exp_f32 v140, v140\n" \
"v_mfma_f32_16x16x32_bf16 v[148:151], a[148:151], v[180:183], v[148:151]\n" \
"ds_read_b128 a[140:143], v248 offset:19456\n" \
"v_mfma_f32_16x16x32_bf16 v[148:151], a[144:147], v[176:179], v[148:151]\n" \
"v_exp_f32 v141, v141\n" \
"v_mfma_f32_16x16x32_bf16 v[152:155], a[160:163], v[176:179], 0\n" \
"ds_read_b128 a[156:159], v248 offset:23552\n" \
"v_cvt_pk_bf16_f32 v138, v140, v141\n" \
"v_mfma_f32_16x16x32_bf16 v[152:155], a[164:167], v[180:183], v[152:155]\n" \
"v_exp_f32 v142, v142\n" \
"v_mfma_f32_16x16x32_bf16 v[152:155], a[168:171], v[184:187], v[152:155]\n" \
"ds_write_b128 v249, a[80:83] offset:34816\n" \
"v_mfma_f32_16x16x32_bf16 v[152:155], a[172:175], v[188:191], v[152:155]\n" \
"v_exp_f32 v143, v143\n" \
"v_mfma_f32_16x16x32_bf16 v[156:159], a[188:191], v[188:191], 0\n" \
"s_add_u32 s85, s94, 593920\n" \
"ds_read_b128 a[152:155], v248 offset:22528\n" \
"v_cvt_pk_bf16_f32 v139, v142, v143\n" \
"v_mfma_f32_16x16x32_bf16 v[156:159], a[184:187], v[184:187], v[156:159]\n" \
"buffer_store_dwordx4 a[96:99], v248, s[32:35], s85 offen offset:0 sc1\n" \
"v_mfma_f32_16x16x32_bf16 v[156:159], a[180:183], v[180:183], v[156:159]\n" \
"v_exp_f32 v144, v144\n" \
"v_mfma_f32_16x16x32_bf16 v[156:159], a[176:179], v[176:179], v[156:159]\n" \
"ds_read_b128 a[148:151], v248 offset:21504\n" \
"ds_read_b128 a[144:147], v248 offset:20480\n" \
"v_mfma_f32_16x16x32_bf16 a[40:43], a[48:51], v[128:131], 0\n" \
"v_exp_f32 v145, v145\n" \
"v_mfma_f32_16x16x32_bf16 a[40:43], a[48:51], v[136:139], a[40:43]\n" \
"ds_read_b128 a[160:163], v248 offset:24576\n" \
"v_cvt_pk_bf16_f32 v144, v144, v145\n" \
"v_mfma_f32_16x16x32_bf16 v[64:67], a[224:227], v[136:139], 0\n" \
"v_exp_f32 v146, v146\n" \
"v_mfma_f32_16x16x32_bf16 v[64:67], a[192:195], v[128:131], v[64:67]\n" \
"ds_read_b128 a[164:167], v248 offset:25600\n" \
"ds_read_b128 a[168:171], v248 offset:26624\n" \
"v_mfma_f32_16x16x32_bf16 v[68:71], a[196:199], v[128:131], 0\n" \
"v_exp_f32 v147, v147\n" \
"v_mfma_f32_16x16x32_bf16 v[68:71], a[228:231], v[136:139], v[68:71]\n" \
"ds_read_b128 a[172:175], v248 offset:27648\n" \
"v_cvt_pk_bf16_f32 v145, v146, v147\n" \
"v_mfma_f32_16x16x32_bf16 v[72:75], a[232:235], v[136:139], 0\n" \
"v_exp_f32 v148, v148\n" \
"v_mfma_f32_16x16x32_bf16 v[72:75], a[200:203], v[128:131], v[72:75]\n" \
"ds_write_b128 v249, a[88:91] offset:35840\n" \
"v_mfma_f32_16x16x32_bf16 v[76:79], a[204:207], v[128:131], 0\n" \
"v_exp_f32 v149, v149\n" \
"v_mfma_f32_16x16x32_bf16 v[76:79], a[236:239], v[136:139], v[76:79]\n" \
"ds_read_b128 a[188:191], v248 offset:31744\n" \
"v_cvt_pk_bf16_f32 v146, v148, v149\n" \
"v_mfma_f32_16x16x32_bf16 v[80:83], a[240:243], v[136:139], 0\n" \
"v_exp_f32 v150, v150\n" \
"v_mfma_f32_16x16x32_bf16 v[80:83], a[208:211], v[128:131], v[80:83]\n" \
"ds_read_b128 a[184:187], v248 offset:30720\n" \
"ds_read_b128 a[180:183], v248 offset:29696\n" \
"v_mfma_f32_16x16x32_bf16 v[84:87], a[212:215], v[128:131], 0\n" \
"v_exp_f32 v151, v151\n" \
"v_mfma_f32_16x16x32_bf16 v[84:87], a[244:247], v[136:139], v[84:87]\n" \
"ds_read_b128 a[176:179], v248 offset:28672\n" \
"v_cvt_pk_bf16_f32 v147, v150, v151\n" \
"v_mfma_f32_16x16x32_bf16 v[88:91], a[248:251], v[136:139], 0\n" \
"s_add_u32 s85, s94, 593920\n" \
"v_mfma_f32_16x16x32_bf16 v[88:91], a[216:219], v[128:131], v[88:91]\n" \
"v_exp_f32 v152, v152\n" \
"v_mfma_f32_16x16x32_bf16 v[92:95], a[220:223], v[128:131], 0\n" \
"buffer_store_dwordx4 a[104:107], v248, s[32:35], s85 offen offset:1024 sc1\n" \
"v_mfma_f32_16x16x32_bf16 v[92:95], a[252:255], v[136:139], v[92:95]\n" \
"s_waitcnt lgkmcnt(14)\n" \
"v_exp_f32 v153, v153\n" \
"v_mfma_f32_16x16x32_bf16 v[128:131], a[128:131], v[208:211], 0\n" \
"v_mov_b32 v240, v248\n" \
"v_cvt_pk_bf16_f32 v152, v152, v153\n" \
"v_mfma_f32_16x16x32_bf16 v[128:131], a[132:135], v[212:215], v[128:131]\n" \
"v_exp_f32 v154, v154\n" \
"v_mfma_f32_16x16x32_bf16 v[128:131], a[136:139], v[216:219], v[128:131]\n" \
"ds_write_b128 v249, a[96:99] offset:36864\n" \
"v_mfma_f32_16x16x32_bf16 v[128:131], a[140:143], v[220:223], v[128:131]\n" \
"s_waitcnt lgkmcnt(10)\n" \
"v_exp_f32 v155, v155\n" \
"v_mfma_f32_16x16x32_bf16 v[132:135], a[156:159], v[220:223], 0\n" \
"buffer_load_dword v253, v252, s[32:35], s81 offen sc1\n" \
"v_cvt_pk_bf16_f32 v153, v154, v155\n" \
"v_mfma_f32_16x16x32_bf16 v[132:135], a[152:155], v[216:219], v[132:135]\n" \
"v_exp_f32 v156, v156\n" \
"v_mfma_f32_16x16x32_bf16 v[132:135], a[148:151], v[212:215], v[132:135]\n" \
"v_mfma_f32_16x16x32_bf16 v[132:135], a[144:147], v[208:211], v[132:135]\n" \
"s_waitcnt lgkmcnt(6)\n" \
"v_exp_f32 v157, v157\n" \
"v_mfma_f32_16x16x32_bf16 v[136:139], a[160:163], v[208:211], v[232:235]\n" \
"v_cvt_pk_bf16_f32 v154, v156, v157\n" \
"v_mfma_f32_16x16x32_bf16 v[136:139], a[164:167], v[212:215], v[136:139]\n" \
"v_exp_f32 v158, v158\n" \
"v_mfma_f32_16x16x32_bf16 v[136:139], a[168:171], v[216:219], v[136:139]\n" \
"v_mfma_f32_16x16x32_bf16 v[136:139], a[172:175], v[220:223], v[136:139]\n" \
"s_waitcnt lgkmcnt(1)\n" \
"v_exp_f32 v159, v159\n" \
"v_mfma_f32_16x16x32_bf16 v[140:143], a[188:191], v[220:223], v[236:239]\n" \
"v_cvt_pk_bf16_f32 v155, v158, v159\n" \
"v_mfma_f32_16x16x32_bf16 v[140:143], a[184:187], v[216:219], v[140:143]\n" \
"v_mfma_f32_16x16x32_bf16 v[140:143], a[180:183], v[212:215], v[140:143]\n" \
"s_add_u32 s85, s94, 593920\n" \
"v_exp_f32 v128, v128\n" \
"v_mfma_f32_16x16x32_bf16 v[140:143], a[176:179], v[208:211], v[140:143]\n" \
"buffer_store_dwordx4 a[112:115], v248, s[32:35], s85 offen offset:2048 sc1\n" \
"v_mfma_f32_16x16x32_bf16 a[44:47], a[48:51], v[144:147], 0\n" \
"v_exp_f32 v129, v129\n" \
"v_mfma_f32_16x16x32_bf16 a[44:47], a[48:51], v[152:155], a[44:47]\n" \
"ds_write_b128 v249, a[104:107] offset:37888\n" \
"v_cvt_pk_bf16_f32 v128, v128, v129\n" \
"v_mfma_f32_16x16x32_bf16 v[96:99], a[224:227], v[152:155], 0\n" \
"v_exp_f32 v130, v130\n" \
"v_mfma_f32_16x16x32_bf16 v[96:99], a[192:195], v[144:147], v[96:99]\n" \
"ds_read_b128 a[224:227], v242 offset:24576\n" \
"v_mfma_f32_16x16x32_bf16 v[100:103], a[196:199], v[144:147], 0\n" \
"v_exp_f32 v131, v131\n" \
"v_mfma_f32_16x16x32_bf16 v[100:103], a[228:231], v[152:155], v[100:103]\n" \
"ds_read_b128 a[192:195], v242 offset:16384\n" \
"v_cvt_pk_bf16_f32 v129, v130, v131\n" \
"v_mfma_f32_16x16x32_bf16 v[104:107], a[232:235], v[152:155], 0\n" \
"v_exp_f32 v132, v132\n" \
"v_mfma_f32_16x16x32_bf16 v[104:107], a[200:203], v[144:147], v[104:107]\n" \
"ds_read_b128 a[196:199], v242 offset:17408\n" \
"ds_read_b128 a[228:231], v242 offset:25600\n" \
"v_mfma_f32_16x16x32_bf16 v[108:111], a[204:207], v[144:147], 0\n" \
"v_exp_f32 v133, v133\n" \
"v_mfma_f32_16x16x32_bf16 v[108:111], a[236:239], v[152:155], v[108:111]\n" \
"ds_read_b128 a[232:235], v242 offset:26624\n" \
"v_cvt_pk_bf16_f32 v130, v132, v133\n" \
"v_mfma_f32_16x16x32_bf16 v[112:115], a[240:243], v[152:155], 0\n" \
"v_exp_f32 v134, v134\n" \
"v_mfma_f32_16x16x32_bf16 v[112:115], a[208:211], v[144:147], v[112:115]\n" \
"ds_write_b128 v249, a[112:115] offset:38912\n" \
"v_mfma_f32_16x16x32_bf16 v[116:119], a[212:215], v[144:147], 0\n" \
"v_exp_f32 v135, v135\n" \
"v_mfma_f32_16x16x32_bf16 v[116:119], a[244:247], v[152:155], v[116:119]\n" \
"ds_read_b128 a[200:203], v242 offset:18432\n" \
"v_cvt_pk_bf16_f32 v131, v134, v135\n" \
"v_mfma_f32_16x16x32_bf16 v[120:123], a[248:251], v[152:155], 0\n" \
"ds_read_b128 a[204:207], v242 offset:19456\n" \
"ds_read_b128 a[236:239], v242 offset:27648\n" \
"v_mfma_f32_16x16x32_bf16 v[120:123], a[216:219], v[144:147], v[120:123]\n" \
"v_exp_f32 v136, v136\n" \
"v_mfma_f32_16x16x32_bf16 v[124:127], a[220:223], v[144:147], 0\n" \
"s_add_u32 s85, s94, 593920\n" \
"ds_read_b128 a[240:243], v242 offset:28672\n" \
"v_mfma_f32_16x16x32_bf16 v[124:127], a[252:255], v[152:155], v[124:127]\n" \
"v_exp_f32 v137, v137\n" \
"v_mfma_f32_16x16x32_bf16 v[144:147], a[128:131], v[160:163], 0\n" \
"buffer_store_dwordx4 a[120:123], v248, s[32:35], s85 offen offset:3072 sc1\n" \
"v_mfma_f32_16x16x32_bf16 v[144:147], a[132:135], v[164:167], v[144:147]\n" \
"v_exp_f32 v138, v138\n" \
"v_mfma_f32_16x16x32_bf16 v[144:147], a[136:139], v[168:171], v[144:147]\n" \
"ds_read_b128 a[208:211], v242 offset:20480\n" \
"v_cvt_pk_bf16_f32 v136, v136, v137\n" \
"v_mfma_f32_16x16x32_bf16 v[144:147], a[140:143], v[172:175], v[144:147]\n" \
"v_exp_f32 v139, v139\n" \
"v_mfma_f32_16x16x32_bf16 v[148:151], a[156:159], v[172:175], 0\n" \
"ds_read_b128 a[212:215], v242 offset:21504\n" \
"v_cvt_pk_bf16_f32 v137, v138, v139\n" \
"v_mfma_f32_16x16x32_bf16 v[148:151], a[152:155], v[168:171], v[148:151]\n" \
"v_exp_f32 v140, v140\n" \
"v_mfma_f32_16x16x32_bf16 v[148:151], a[148:151], v[164:167], v[148:151]\n" \
"ds_write_b128 v249, a[120:123] offset:39936\n" \
"v_mfma_f32_16x16x32_bf16 v[148:151], a[144:147], v[160:163], v[148:151]\n" \
"v_exp_f32 v141, v141\n" \
"v_mfma_f32_16x16x32_bf16 v[152:155], a[160:163], v[160:163], 0\n" \
"ds_read_b128 a[244:247], v242 offset:29696\n" \
"v_cvt_pk_bf16_f32 v138, v140, v141\n" \
"v_mfma_f32_16x16x32_bf16 v[152:155], a[164:167], v[164:167], v[152:155]\n" \
"v_exp_f32 v142, v142\n" \
"v_mfma_f32_16x16x32_bf16 v[152:155], a[168:171], v[168:171], v[152:155]\n" \
"ds_read_b128 a[248:251], v242 offset:30720\n" \
"ds_read_b128 a[216:219], v242 offset:22528\n" \
"v_mfma_f32_16x16x32_bf16 v[152:155], a[172:175], v[172:175], v[152:155]\n" \
"v_exp_f32 v143, v143\n" \
"v_mfma_f32_16x16x32_bf16 v[156:159], a[188:191], v[172:175], 0\n" \
"ds_read_b128 a[220:223], v242 offset:23552\n" \
"v_cvt_pk_bf16_f32 v139, v142, v143\n" \
"v_mfma_f32_16x16x32_bf16 v[156:159], a[184:187], v[168:171], v[156:159]\n" \
"ds_read_b128 a[252:255], v242 offset:31744\n" \
"v_mfma_f32_16x16x32_bf16 v[156:159], a[180:183], v[164:167], v[156:159]\n" \
"v_exp_f32 v144, v144\n" \
"v_mfma_f32_16x16x32_bf16 v[156:159], a[176:179], v[160:163], v[156:159]\n" \
"v_mfma_f32_16x16x32_bf16 a[36:39], a[48:51], v[128:131], a[36:39]\n" \
"v_exp_f32 v145, v145\n" \
"v_mfma_f32_16x16x32_bf16 a[36:39], a[48:51], v[136:139], a[36:39]\n" \
"s_waitcnt lgkmcnt(14)\n" \
"v_cvt_pk_bf16_f32 v144, v144, v145\n" \
"v_mfma_f32_16x16x32_bf16 v[32:35], a[224:227], v[136:139], v[32:35]\n" \
"v_exp_f32 v146, v146\n" \
"v_mfma_f32_16x16x32_bf16 v[32:35], a[192:195], v[128:131], v[32:35]\n" \
"v_mfma_f32_16x16x32_bf16 v[36:39], a[196:199], v[128:131], v[36:39]\n" \
"v_exp_f32 v147, v147\n" \
"v_mfma_f32_16x16x32_bf16 v[36:39], a[228:231], v[136:139], v[36:39]\n" \
"s_waitcnt lgkmcnt(9)\n" \
"v_cvt_pk_bf16_f32 v145, v146, v147\n" \
"v_mfma_f32_16x16x32_bf16 v[40:43], a[232:235], v[136:139], v[40:43]\n" \
"v_exp_f32 v148, v148\n" \
"v_mfma_f32_16x16x32_bf16 v[40:43], a[200:203], v[128:131], v[40:43]\n" \
".Lst0_8_%=:\n" \
"s_cbranch_execz .Lst0_12_%=\n" \
"v_mfma_f32_16x16x32_bf16 v[44:47], a[204:207], v[128:131], v[44:47]\n" \
"v_exp_f32 v149, v149\n" \
"v_mfma_f32_16x16x32_bf16 v[44:47], a[236:239], v[136:139], v[44:47]\n" \
"s_waitcnt lgkmcnt(4)\n" \
"v_cvt_pk_bf16_f32 v146, v148, v149\n" \
"v_mfma_f32_16x16x32_bf16 v[48:51], a[240:243], v[136:139], v[48:51]\n" \
"s_waitcnt vmcnt(2)\n" \
"v_cmp_eq_u32 vcc, 0, v253\n" \
"v_mfma_f32_16x16x32_bf16 v[48:51], a[208:211], v[128:131], v[48:51]\n" \
"v_exp_f32 v150, v150\n" \
"v_mfma_f32_16x16x32_bf16 v[52:55], a[212:215], v[128:131], v[52:55]\n" \
"v_exp_f32 v151, v151\n" \
"v_mfma_f32_16x16x32_bf16 v[52:55], a[244:247], v[136:139], v[52:55]\n" \
"s_waitcnt lgkmcnt(0)\n" \
"s_cbranch_vccnz .Lspv_%=\n" \
".Lsbv_%=:\n" \
"v_mfma_f32_16x16x32_bf16 v[56:59], a[248:251], v[136:139], v[56:59]\n" \
"s_mov_b32 m0, s19\n" \
"v_cmp_gt_u32 s[98:99], 32, v251\n" \
"v_cvt_pk_bf16_f32 v147, v150, v151\n" \
"v_mfma_f32_16x16x32_bf16 v[56:59], a[216:219], v[128:131], v[56:59]\n" \
"s_barrier\n" \
"v_exp_f32 v152, v152\n" \
"v_mfma_f32_16x16x32_bf16 v[60:63], a[220:223], v[128:131], v[60:63]\n" \
"buffer_load_dwordx4 v248, s[32:35], s88 offen offset:0 sc1 lds\n" \
"v_mfma_f32_16x16x32_bf16 v[60:63], a[252:255], v[136:139], v[60:63]\n" \
"v_exp_f32 v153, v153\n" \
"v_mfma_f32_16x16x32_bf16 v[128:131], a[128:131], v[176:179], 0\n" \
"v_cndmask_b32 v253, v253, 0, s[98:99]\n" \
"v_cvt_pk_bf16_f32 v152, v152, v153\n" \
"v_mfma_f32_16x16x32_bf16 v[128:131], a[132:135], v[180:183], v[128:131]\n" \
"v_exp_f32 v154, v154\n" \
"v_mfma_f32_16x16x32_bf16 v[128:131], a[136:139], v[184:187], v[128:131]\n" \
"buffer_load_dwordx4 v248, s[32:35], s88 offen offset:1024 sc1 lds\n" \
"v_mfma_f32_16x16x32_bf16 v[128:131], a[140:143], v[188:191], v[128:131]\n" \
"v_exp_f32 v155, v155\n" \
"v_mfma_f32_16x16x32_bf16 v[132:135], a[156:159], v[188:191], 0\n" \
"v_max_u32_dpp v253, v253, v253 quad_perm:[1,0,3,2] row_mask:0xf bank_mask:0xf\n" \
"v_cvt_pk_bf16_f32 v153, v154, v155\n" \
"v_mfma_f32_16x16x32_bf16 v[132:135], a[152:155], v[184:187], v[132:135]\n" \
"v_exp_f32 v156, v156\n" \
"v_mfma_f32_16x16x32_bf16 v[132:135], a[148:151], v[180:183], v[132:135]\n" \
"buffer_load_dwordx4 v248, s[32:35], s88 offen offset:2048 sc1 lds\n" \
"v_mfma_f32_16x16x32_bf16 v[132:135], a[144:147], v[176:179], v[132:135]\n" \
"v_exp_f32 v157, v157\n" \
"v_mfma_f32_16x16x32_bf16 v[136:139], a[160:163], v[176:179], 0\n" \
"v_max_u32_dpp v253, v253, v253 quad_perm:[2,3,0,1] row_mask:0xf bank_mask:0xf\n" \
"v_cvt_pk_bf16_f32 v154, v156, v157\n" \
"v_mfma_f32_16x16x32_bf16 v[136:139], a[164:167], v[180:183], v[136:139]\n" \
"v_exp_f32 v158, v158\n" \
"v_mfma_f32_16x16x32_bf16 v[136:139], a[168:171], v[184:187], v[136:139]\n" \
"buffer_load_dwordx4 v248, s[32:35], s88 offen offset:3072 sc1 lds\n" \
"v_mfma_f32_16x16x32_bf16 v[136:139], a[172:175], v[188:191], v[136:139]\n" \
"s_add_u32 s89, s88, 589824\n" \
"v_exp_f32 v159, v159\n" \
"v_mfma_f32_16x16x32_bf16 v[140:143], a[188:191], v[188:191], 0\n" \
"s_add_u32 s88, s88, s27\n" \
"v_max_u32_dpp v253, v253, v253 row_half_mirror row_mask:0xf bank_mask:0xf\n" \
"v_cvt_pk_bf16_f32 v155, v158, v159\n" \
"v_mfma_f32_16x16x32_bf16 v[140:143], a[184:187], v[184:187], v[140:143]\n" \
"s_add_u32 s19, s19, 0x4000\n" \
"ds_read_b128 a[128:131], v248 offset:32768\n" \
"ds_read_b128 a[132:135], v248 offset:33792\n" \
"v_mfma_f32_16x16x32_bf16 v[140:143], a[180:183], v[180:183], v[140:143]\n" \
"s_bitset0_b32 s19, 16\n" \
"v_exp_f32 v128, v128\n" \
"v_mfma_f32_16x16x32_bf16 v[140:143], a[176:179], v[176:179], v[140:143]\n" \
"s_cmp_eq_u32 s88, s16\n" \
"v_max_u32_dpp v253, v253, v253 row_mirror row_mask:0xf bank_mask:0xf\n" \
"v_mov_b32 v254, v253\n" \
"v_mfma_f32_16x16x32_bf16 a[40:43], a[48:51], v[144:147], a[40:43]\n" \
"s_cmov_b32 s88, s21\n" \
"v_exp_f32 v129, v129\n" \
"v_mfma_f32_16x16x32_bf16 a[40:43], a[48:51], v[152:155], a[40:43]\n" \
"s_cmov_b32 s27, s91\n" \
"v_permlane16_swap_b32 v253, v254\n" \
"v_mfma_f32_16x16x32_bf16 v[64:67], a[224:227], v[152:155], v[64:67]\n" \
"s_cmov_b32 s16, s77\n" \
"v_exp_f32 v130, v130\n" \
"v_mfma_f32_16x16x32_bf16 v[64:67], a[192:195], v[144:147], v[64:67]\n" \
"s_cmov_b32 s21, s92\n" \
"v_max_u32 v253, v253, v254\n" \
"v_mov_b32 v254, v253\n" \
"v_mfma_f32_16x16x32_bf16 v[68:71], a[196:199], v[144:147], v[68:71]\n" \
"s_cmovk_i32 s91, 0x4000\n" \
"v_exp_f32 v131, v131\n" \
"v_mfma_f32_16x16x32_bf16 v[68:71], a[228:231], v[152:155], v[68:71]\n" \
"s_mov_b32 m0, s20\n" \
"v_permlane32_swap_b32 v253, v254\n" \
"v_mfma_f32_16x16x32_bf16 v[72:75], a[232:235], v[152:155], v[72:75]\n" \
"v_exp_f32 v132, v132\n" \
"v_mfma_f32_16x16x32_bf16 v[72:75], a[200:203], v[144:147], v[72:75]\n" \
"buffer_load_dwordx4 v248, s[32:35], s89 offen offset:0 sc1 lds\n" \
"v_mfma_f32_16x16x32_bf16 v[76:79], a[204:207], v[144:147], v[76:79]\n" \
"v_exp_f32 v133, v133\n" \
"v_mfma_f32_16x16x32_bf16 v[76:79], a[236:239], v[152:155], v[76:79]\n" \
"buffer_load_dwordx4 v248, s[32:35], s89 offen offset:1024 sc1 lds\n" \
"v_mfma_f32_16x16x32_bf16 v[80:83], a[240:243], v[152:155], v[80:83]\n" \
"v_exp_f32 v134, v134\n" \
"v_mfma_f32_16x16x32_bf16 v[80:83], a[208:211], v[144:147], v[80:83]\n" \
"buffer_load_dwordx4 v248, s[32:35], s89 offen offset:2048 sc1 lds\n" \
"ds_read_b128 a[136:139], v248 offset:34816\n" \
"ds_read_b128 a[140:143], v248 offset:35840\n" \
"v_mfma_f32_16x16x32_bf16 v[84:87], a[212:215], v[144:147], v[84:87]\n" \
"v_exp_f32 v135, v135\n" \
"v_mfma_f32_16x16x32_bf16 v[84:87], a[244:247], v[152:155], v[84:87]\n" \
"buffer_load_dwordx4 v248, s[32:35], s89 offen offset:3072 sc1 lds\n" \
"v_mfma_f32_16x16x32_bf16 v[88:91], a[248:251], v[152:155], v[88:91]\n" \
"s_add_u32 s20, s20, 0x4000\n" \
"v_max_u32 v253, v253, v254\n" \
"v_max_f32 v254, 1.0, v253\n" \
"v_mfma_f32_16x16x32_bf16 v[88:91], a[216:219], v[144:147], v[88:91]\n" \
"s_and_b32 s20, s20, 0xffff\n" \
"ds_read_b128 a[156:159], v248 offset:39936\n" \
"ds_read_b128 a[152:155], v248 offset:38912\n" \
"ds_read_b128 a[148:151], v248 offset:37888\n" \
"ds_read_b128 a[144:147], v248 offset:36864\n" \
"v_exp_f32 v136, v136\n" \
"v_mfma_f32_16x16x32_bf16 v[92:95], a[220:223], v[144:147], v[92:95]\n" \
"s_bitset1_b32 s20, 16\n" \
"v_min_f32 v253, 1.0, v253\n" \
"v_cvt_pk_bf16_f32 v128, v128, v129\n" \
"v_mfma_f32_16x16x32_bf16 v[92:95], a[252:255], v[152:155], v[92:95]\n" \
"s_waitcnt lgkmcnt(4)\n" \
"v_exp_f32 v137, v137\n" \
"v_mfma_f32_16x16x32_bf16 v[144:147], a[128:131], v[176:179], 0\n" \
"s_mov_b32 m0, s19\n" \
"v_rcp_f32 v254, v254\n" \
"v_mfma_f32_16x16x32_bf16 v[144:147], a[132:135], v[180:183], v[144:147]\n" \
"ds_read_b128 a[160:163], v248 offset:40960\n" \
"ds_read_b128 a[164:167], v248 offset:41984\n" \
"ds_read_b128 a[168:171], v248 offset:43008\n" \
"ds_read_b128 a[172:175], v248 offset:44032\n" \
"v_exp_f32 v138, v138\n" \
"v_mfma_f32_16x16x32_bf16 v[144:147], a[136:139], v[184:187], v[144:147]\n" \
"buffer_load_dwordx4 v248, s[32:35], s88 offen offset:0 sc1 lds\n" \
"v_mfma_f32_16x16x32_bf16 v[144:147], a[140:143], v[188:191], v[144:147]\n" \
"s_waitcnt lgkmcnt(4)\n" \
"v_exp_f32 v139, v139\n" \
"v_mfma_f32_16x16x32_bf16 v[148:151], a[156:159], v[188:191], 0\n" \
"buffer_load_dwordx4 v248, s[32:35], s88 offen offset:1024 sc1 lds\n" \
"v_mfma_f32_16x16x32_bf16 v[148:151], a[152:155], v[184:187], v[148:151]\n" \
"ds_read_b128 a[188:191], v248 offset:48128\n" \
"ds_read_b128 a[184:187], v248 offset:47104\n" \
"ds_read_b128 a[180:183], v248 offset:46080\n" \
"ds_read_b128 a[176:179], v248 offset:45056\n" \
"v_exp_f32 v140, v140\n" \
"v_mfma_f32_16x16x32_bf16 v[148:151], a[148:151], v[180:183], v[148:151]\n" \
"s_lshl_b32 s85, s68, 3\n" \
"buffer_load_dwordx4 v248, s[32:35], s88 offen offset:2048 sc1 lds\n" \
"v_mfma_f32_16x16x32_bf16 v[148:151], a[144:147], v[176:179], v[148:151]\n" \
"s_waitcnt lgkmcnt(4)\n" \
"v_exp_f32 v141, v141\n" \
"v_mfma_f32_16x16x32_bf16 v[152:155], a[160:163], v[176:179], v[232:235]\n" \
"s_add_u32 s85, s85, s67\n" \
"v_rcp_f32 v253, v253\n" \
"v_mfma_f32_16x16x32_bf16 v[152:155], a[164:167], v[180:183], v[152:155]\n" \
"s_lshl2_add_u32 s85, s85, %[wid]\n" \
"v_exp_f32 v142, v142\n" \
"v_mfma_f32_16x16x32_bf16 v[152:155], a[168:171], v[184:187], v[152:155]\n" \
"s_lshl_b32 s85, s85, 2\n" \
"buffer_load_dwordx4 v248, s[32:35], s88 offen offset:3072 sc1 lds\n" \
"v_mfma_f32_16x16x32_bf16 v[152:155], a[172:175], v[188:191], v[152:155]\n" \
"s_waitcnt lgkmcnt(0)\n" \
"v_exp_f32 v143, v143\n" \
"v_mfma_f32_16x16x32_bf16 v[156:159], a[188:191], v[188:191], v[236:239]\n" \
"s_add_u32 s89, s88, 589824\n" \
"v_exp_f32 v144, v144\n" \
"v_mfma_f32_16x16x32_bf16 v[156:159], a[184:187], v[184:187], v[156:159]\n" \
"s_add_u32 s88, s88, s27\n" \
"v_exp_f32 v145, v145\n" \
"v_mfma_f32_16x16x32_bf16 v[156:159], a[180:183], v[180:183], v[156:159]\n" \
"s_add_u32 s19, s19, 0x4000\n" \
"v_exp_f32 v146, v146\n" \
"v_cvt_pk_bf16_f32 v129, v130, v131\n" \
"v_cvt_pk_bf16_f32 v130, v132, v133\n" \
"v_cvt_pk_bf16_f32 v131, v134, v135\n" \
"v_mfma_f32_16x16x32_bf16 v[156:159], a[176:179], v[176:179], v[156:159]\n" \
"s_bitset0_b32 s19, 16\n" \
"v_exp_f32 v147, v147\n" \
"v_cvt_pk_bf16_f32 v136, v136, v137\n" \
"v_cvt_pk_bf16_f32 v137, v138, v139\n" \
"v_cvt_pk_bf16_f32 v138, v140, v141\n" \
"v_cvt_pk_bf16_f32 v139, v142, v143\n" \
"v_mfma_f32_16x16x32_bf16 a[44:47], a[48:51], v[128:131], a[44:47]\n" \
"s_barrier\n" \
"v_exp_f32 v148, v148\n" \
"v_mfma_f32_16x16x32_bf16 a[44:47], a[48:51], v[136:139], a[44:47]\n" \
"s_cmp_eq_u32 s88, s16\n" \
"v_exp_f32 v149, v149\n" \
"v_mfma_f32_16x16x32_bf16 v[96:99], a[224:227], v[136:139], v[96:99]\n" \
"s_cmov_b32 s88, s21\n" \
"v_exp_f32 v150, v150\n" \
"v_mfma_f32_16x16x32_bf16 v[96:99], a[192:195], v[128:131], v[96:99]\n" \
"s_cmov_b32 s27, s91\n" \
"v_mul_f32 v250, 0x7e000000, v254\n" \
"v_mul_f32 v246, 0x0d800000, v253\n" \
"v_mfma_f32_16x16x32_bf16 v[100:103], a[196:199], v[128:131], v[100:103]\n" \
"s_cmov_b32 s16, s77\n" \
"v_exp_f32 v151, v151\n" \
"v_mfma_f32_16x16x32_bf16 v[100:103], a[228:231], v[136:139], v[100:103]\n" \
"s_cmov_b32 s21, s92\n" \
"v_cvt_pk_bf16_f32 v144, v144, v145\n" \
"v_cvt_pk_bf16_f32 v145, v146, v147\n" \
"v_mfma_f32_16x16x32_bf16 v[104:107], a[232:235], v[136:139], v[104:107]\n" \
"s_cmovk_i32 s91, 0x4000\n" \
"v_cvt_pk_bf16_f32 v146, v148, v149\n" \
"v_cvt_pk_bf16_f32 v147, v150, v151\n" \
"v_mfma_f32_16x16x32_bf16 v[104:107], a[200:203], v[128:131], v[104:107]\n" \
"s_mov_b32 m0, s20\n" \
"v_exp_f32 v152, v152\n" \
"v_mfma_f32_16x16x32_bf16 v[108:111], a[204:207], v[128:131], v[108:111]\n" \
"s_add_u32 s85, s85, 655360\n" \
"buffer_load_dwordx4 v248, s[32:35], s89 offen offset:0 sc1 lds\n" \
"v_mfma_f32_16x16x32_bf16 v[108:111], a[236:239], v[136:139], v[108:111]\n" \
"v_exp_f32 v153, v153\n" \
"v_mfma_f32_16x16x32_bf16 v[112:115], a[240:243], v[136:139], v[112:115]\n" \
"buffer_load_dwordx4 v248, s[32:35], s89 offen offset:1024 sc1 lds\n" \
"v_mfma_f32_16x16x32_bf16 v[112:115], a[208:211], v[128:131], v[112:115]\n" \
"ds_read_b128 a[128:131], v248 offset:49152\n" \
"ds_read_b128 a[132:135], v248 offset:50176\n" \
"ds_read_b128 a[136:139], v248 offset:51200\n" \
"ds_read_b128 a[140:143], v248 offset:52224\n" \
"v_exp_f32 v154, v154\n" \
"v_mfma_f32_16x16x32_bf16 v[116:119], a[212:215], v[128:131], v[116:119]\n" \
"buffer_load_dwordx4 v248, s[32:35], s89 offen offset:2048 sc1 lds\n" \
"v_mfma_f32_16x16x32_bf16 v[116:119], a[244:247], v[136:139], v[116:119]\n" \
"v_exp_f32 v155, v155\n" \
"v_mfma_f32_16x16x32_bf16 v[120:123], a[248:251], v[136:139], v[120:123]\n" \
"s_waitcnt vmcnt(15)\n" \
"buffer_store_dword a56, off, s[32:35], s85 sc1\n" \
"v_cvt_pk_bf16_f32 v152, v152, v153\n" \
"v_mfma_f32_16x16x32_bf16 v[120:123], a[216:219], v[128:131], v[120:123]\n" \
"ds_read_b128 a[156:159], v248 offset:56320\n" \
"ds_read_b128 a[152:155], v248 offset:55296\n" \
"ds_read_b128 a[148:151], v248 offset:54272\n" \
"ds_read_b128 a[144:147], v248 offset:53248\n" \
"v_exp_f32 v156, v156\n" \
"v_mfma_f32_16x16x32_bf16 v[124:127], a[220:223], v[128:131], v[124:127]\n" \
"ds_read_b128 a[224:227], v242 offset:40960\n" \
"v_cvt_pk_bf16_f32 v153, v154, v155\n" \
"v_mfma_f32_16x16x32_bf16 v[124:127], a[252:255], v[136:139], v[124:127]\n" \
"s_waitcnt lgkmcnt(5)\n" \
"v_exp_f32 v157, v157\n" \
"v_mfma_f32_16x16x32_bf16 v[128:131], a[128:131], v[160:163], v[224:227]\n" \
"buffer_load_dwordx4 v248, s[32:35], s89 offen offset:3072 sc1 lds\n" \
"v_mfma_f32_16x16x32_bf16 v[128:131], a[132:135], v[164:167], v[128:131]\n" \
"s_add_u32 s20, s20, 0x4000\n" \
"v_exp_f32 v158, v158\n" \
"v_mfma_f32_16x16x32_bf16 v[128:131], a[136:139], v[168:171], v[128:131]\n" \
"s_and_b32 s20, s20, 0xffff\n" \
"ds_read_b128 a[192:195], v242 offset:32768\n" \
"v_cvt_pk_bf16_f32 v154, v156, v157\n" \
"v_mfma_f32_16x16x32_bf16 v[128:131], a[140:143], v[172:175], v[128:131]\n" \
"s_waitcnt lgkmcnt(2)\n" \
"ds_read_b128 a[196:199], v242 offset:33792\n" \
"ds_read_b128 a[228:231], v242 offset:41984\n" \
"v_exp_f32 v159, v159\n" \
"v_mfma_f32_16x16x32_bf16 v[132:135], a[156:159], v[172:175], v[228:231]\n" \
"s_bitset1_b32 s20, 16\n" \
"ds_read_b128 a[232:235], v242 offset:43008\n" \
"v_cvt_pk_bf16_f32 v155, v158, v159\n" \
"v_mfma_f32_16x16x32_bf16 v[132:135], a[152:155], v[168:171], v[132:135]\n" \
"s_mov_b32 m0, s19\n" \
"ds_read_b128 a[200:203], v242 offset:34816\n" \
"ds_read_b128 a[204:207], v242 offset:35840\n" \
"v_mfma_f32_16x16x32_bf16 v[132:135], a[148:151], v[164:167], v[132:135]\n" \
"v_exp_f32 v128, v128\n" \
"v_mfma_f32_16x16x32_bf16 v[132:135], a[144:147], v[160:163], v[132:135]\n" \
"buffer_load_dwordx4 v248, s[32:35], s88 offen offset:0 sc1 lds\n" \
"ds_read_b128 a[236:239], v242 offset:44032\n" \
"v_mfma_f32_16x16x32_bf16 a[44:47], a[48:51], v[144:147], a[44:47]\n" \
"ds_read_b128 a[160:163], v248 offset:57344\n" \
"ds_read_b128 a[240:243], v242 offset:45056\n" \
"v_mfma_f32_16x16x32_bf16 a[44:47], a[48:51], v[152:155], a[44:47]\n" \
"s_waitcnt lgkmcnt(6)\n" \
"buffer_load_dwordx4 v248, s[32:35], s88 offen offset:1024 sc1 lds\n" \
"v_mfma_f32_16x16x32_bf16 v[96:99], a[224:227], v[152:155], v[96:99]\n" \
"v_exp_f32 v129, v129\n" \
"v_mfma_f32_16x16x32_bf16 v[96:99], a[192:195], v[144:147], v[96:99]\n" \
"ds_read_b128 a[164:167], v248 offset:58368\n" \
"ds_read_b128 a[208:211], v242 offset:36864\n" \
"ds_read_b128 a[168:171], v248 offset:59392\n" \
"ds_read_b128 a[172:175], v248 offset:60416\n" \
"ds_read_b128 a[212:215], v242 offset:37888\n" \
"ds_read_b128 a[244:247], v242 offset:46080\n" \
"v_cvt_pk_bf16_f32 v128, v128, v129\n" \
"v_mfma_f32_16x16x32_bf16 v[100:103], a[196:199], v[144:147], v[100:103]\n" \
"ds_read_b128 a[188:191], v248 offset:64512\n" \
"ds_read_b128 a[248:251], v242 offset:47104\n" \
"v_mfma_f32_16x16x32_bf16 v[100:103], a[228:231], v[152:155], v[100:103]\n" \
"s_waitcnt lgkmcnt(10)\n" \
"ds_read_b128 a[184:187], v248 offset:63488\n" \
"ds_read_b128 a[216:219], v242 offset:38912\n" \
"v_mfma_f32_16x16x32_bf16 v[104:107], a[232:235], v[152:155], v[104:107]\n" \
"buffer_load_dwordx4 v248, s[32:35], s88 offen offset:2048 sc1 lds\n" \
"v_mfma_f32_16x16x32_bf16 v[104:107], a[200:203], v[144:147], v[104:107]\n" \
"ds_read_b128 a[180:183], v248 offset:62464\n" \
"ds_read_b128 a[220:223], v242 offset:39936\n" \
"ds_read_b128 a[176:179], v248 offset:61440\n" \
"ds_read_b128 a[252:255], v242 offset:48128\n" \
"v_exp_f32 v130, v130\n" \
"v_mfma_f32_16x16x32_bf16 v[108:111], a[204:207], v[144:147], v[108:111]\n" \
"ds_read_b128 a[192:195], v242 offset:49152\n" \
"ds_read_b128 a[196:199], v242 offset:50176\n" \
"v_mfma_f32_16x16x32_bf16 v[108:111], a[236:239], v[152:155], v[108:111]\n" \
"s_waitcnt lgkmcnt(10)\n" \
"ds_read_b128 a[200:203], v242 offset:51200\n" \
"ds_read_b128 a[204:207], v242 offset:52224\n" \
"v_mfma_f32_16x16x32_bf16 v[112:115], a[240:243], v[152:155], v[112:115]\n" \
"v_mfma_f32_16x16x32_bf16 v[112:115], a[208:211], v[144:147], v[112:115]\n" \
"v_exp_f32 v131, v131\n" \
"v_mfma_f32_16x16x32_bf16 v[116:119], a[212:215], v[144:147], v[116:119]\n" \
"buffer_load_dwordx4 v248, s[32:35], s88 offen offset:3072 sc1 lds\n" \
"v_mfma_f32_16x16x32_bf16 v[116:119], a[244:247], v[152:155], v[116:119]\n" \
"s_waitcnt lgkmcnt(4)\n" \
"ds_read_b128 a[208:211], v242 offset:53248\n" \
"v_cvt_pk_bf16_f32 v129, v130, v131\n" \
"v_mfma_f32_16x16x32_bf16 v[120:123], a[248:251], v[152:155], v[120:123]\n" \
"s_add_u32 s89, s88, 589824\n" \
"s_add_u32 s88, s88, s27\n" \
"ds_read_b128 a[212:215], v242 offset:54272\n" \
"v_mfma_f32_16x16x32_bf16 v[120:123], a[216:219], v[144:147], v[120:123]\n" \
"s_add_u32 s19, s19, 0x4000\n" \
"s_bitset0_b32 s19, 16\n" \
"s_cmp_eq_u32 s88, s16\n" \
"v_mfma_f32_16x16x32_bf16 v[124:127], a[220:223], v[144:147], v[124:127]\n" \
"s_cmov_b32 s88, s21\n" \
"v_exp_f32 v132, v132\n" \
"v_mfma_f32_16x16x32_bf16 v[124:127], a[252:255], v[152:155], v[124:127]\n" \
"s_cmov_b32 s27, s91\n" \
"ds_read_b128 a[216:219], v242 offset:55296\n" \
"ds_read_b128 a[220:223], v242 offset:56320\n" \
"v_mfma_f32_16x16x32_bf16 v[144:147], a[128:131], v[176:179], 0\n" \
"s_waitcnt vmcnt(13)\n" \
"s_barrier\n" \
"v_and_b32 v230, 7, v251\n" \
"v_mfma_f32_16x16x32_bf16 v[144:147], a[132:135], v[180:183], v[144:147]\n" \
"s_cmov_b32 s16, s77\n" \
"ds_read_b128 a[128:131], v248 offset:0\n" \
"v_lshrrev_b32 v232, 3, v251\n" \
"v_mfma_f32_16x16x32_bf16 v[144:147], a[136:139], v[184:187], v[144:147]\n" \
"s_cmov_b32 s21, s92\n" \
"v_exp_f32 v133, v133\n" \
"v_mfma_f32_16x16x32_bf16 v[144:147], a[140:143], v[188:191], v[144:147]\n" \
"s_cmovk_i32 s91, 0x4000\n" \
"ds_read_b128 a[132:135], v248 offset:1024\n" \
"v_cvt_pk_bf16_f32 v130, v132, v133\n" \
"v_mfma_f32_16x16x32_bf16 v[148:151], a[156:159], v[188:191], 0\n" \
"s_mov_b32 m0, s20\n" \
"ds_read_b128 a[224:227], v242 offset:57344\n" \
"ds_read_b128 a[136:139], v248 offset:2048\n" \
"v_mfma_f32_16x16x32_bf16 v[148:151], a[152:155], v[184:187], v[148:151]\n" \
"buffer_load_dwordx4 v248, s[32:35], s89 offen offset:0 sc1 lds\n" \
"v_mfma_f32_16x16x32_bf16 v[148:151], a[148:151], v[180:183], v[148:151]\n" \
"ds_read_b128 a[228:231], v242 offset:58368\n" \
"ds_read_b128 a[140:143], v248 offset:3072\n" \
"v_mfma_f32_16x16x32_bf16 v[148:151], a[144:147], v[176:179], v[148:151]\n" \
"v_exp_f32 v134, v134\n" \
"v_mfma_f32_16x16x32_bf16 v[152:155], a[160:163], v[176:179], 0\n" \
"buffer_load_dwordx4 v248, s[32:35], s89 offen offset:1024 sc1 lds\n" \
"v_mfma_f32_16x16x32_bf16 v[152:155], a[164:167], v[180:183], v[152:155]\n" \
"ds_read_b128 a[232:235], v242 offset:59392\n" \
"ds_read_b128 a[236:239], v242 offset:60416\n" \
"v_mfma_f32_16x16x32_bf16 v[152:155], a[168:171], v[184:187], v[152:155]\n" \
"ds_read_b128 a[240:243], v242 offset:61440\n" \
"ds_read_b128 a[244:247], v242 offset:62464\n" \
"v_mfma_f32_16x16x32_bf16 v[152:155], a[172:175], v[188:191], v[152:155]\n" \
"v_exp_f32 v135, v135\n" \
"v_mfma_f32_16x16x32_bf16 v[156:159], a[188:191], v[188:191], 0\n" \
"v_exp_f32 v144, v144\n" \
"v_mfma_f32_16x16x32_bf16 v[156:159], a[184:187], v[184:187], v[156:159]\n" \
"v_exp_f32 v145, v145\n" \
"v_mfma_f32_16x16x32_bf16 v[156:159], a[180:183], v[180:183], v[156:159]\n" \
"v_exp_f32 v146, v146\n" \
"v_cvt_pk_bf16_f32 v131, v134, v135\n" \
"v_mfma_f32_16x16x32_bf16 v[156:159], a[176:179], v[176:179], v[156:159]\n" \
"v_exp_f32 v147, v147\n" \
"v_mfma_f32_16x16x32_bf16 a[40:43], a[48:51], v[128:131], a[40:43]\n" \
"s_waitcnt lgkmcnt(14)\n" \
"v_exp_f32 v148, v148\n" \
"v_mfma_f32_16x16x32_bf16 v[64:67], a[192:195], v[128:131], v[64:67]\n" \
"v_exp_f32 v149, v149\n" \
"v_mfma_f32_16x16x32_bf16 v[68:71], a[196:199], v[128:131], v[68:71]\n" \
"v_exp_f32 v150, v150\n" \
"v_mfma_f32_16x16x32_bf16 v[72:75], a[200:203], v[128:131], v[72:75]\n" \
"buffer_load_dwordx4 v248, s[32:35], s89 offen offset:2048 sc1 lds\n" \
"v_mfma_f32_16x16x32_bf16 v[76:79], a[204:207], v[128:131], v[76:79]\n" \
"s_waitcnt lgkmcnt(10)\n" \
"v_exp_f32 v151, v151\n" \
"v_mfma_f32_16x16x32_bf16 v[80:83], a[208:211], v[128:131], v[80:83]\n" \
"buffer_load_dwordx4 v248, s[32:35], s89 offen offset:3072 sc1 lds\n" \
"v_mfma_f32_16x16x32_bf16 v[84:87], a[212:215], v[128:131], v[84:87]\n" \
"s_add_u32 s20, s20, 0x4000\n" \
"ds_read_b128 a[156:159], v248 offset:7168\n" \
"ds_read_b128 a[248:251], v242 offset:63488\n" \
"ds_read_b128 a[152:155], v248 offset:6144\n" \
"ds_read_b128 a[252:255], v242 offset:64512\n" \
"ds_read_b128 a[148:151], v248 offset:5120\n" \
"ds_read_b128 a[144:147], v248 offset:4096\n" \
"v_exp_f32 v152, v152\n" \
"v_mfma_f32_16x16x32_bf16 v[88:91], a[216:219], v[128:131], v[88:91]\n" \
"s_and_b32 s20, s20, 0xffff\n" \
"v_exp_f32 v153, v153\n" \
"v_mfma_f32_16x16x32_bf16 v[92:95], a[220:223], v[128:131], v[92:95]\n" \
"s_waitcnt lgkmcnt(10)\n" \
"ds_read_b128 a[160:163], v248 offset:8192\n" \
"ds_read_b128 a[164:167], v248 offset:9216\n" \
"ds_read_b128 a[168:171], v248 offset:10240\n" \
"ds_read_b128 a[172:175], v248 offset:11264\n" \
"v_cvt_pk_bf16_f32 v144, v144, v145\n" \
"v_cvt_pk_bf16_f32 v145, v146, v147\n" \
"v_cvt_pk_bf16_f32 v146, v148, v149\n" \
"v_cvt_pk_bf16_f32 v147, v150, v151\n" \
"s_bitset1_b32 s20, 16\n" \
"v_lshlrev_b32 v231, 4, v230\n" \
"v_mul_u32_u24 v230, 7168, v230\n" \
"v_lshl_add_u32 v230, v232, 4, v230\n" \
"v_lshl_or_b32 v232, v232, 14, v231\n" \
"v_and_b32 v231, 3, v251\n" \
"v_lshlrev_b32 v231, 7, v231\n" \
"v_bfe_u32 v237, v251, 4, 1\n" \
"v_lshlrev_b32 v237, 1, v237\n" \
"v_and_b32 v238, 3, v251\n" \
"v_xor_b32 v237, v237, v238\n" \
"v_lshrrev_b32 v238, 5, v251\n" \
"v_lshl_or_b32 v237, v238, 2, v237\n" \
"v_bfe_u32 v238, v251, 2, 3\n" \
"v_lshl_or_b32 v237, v238, 6, v237\n" \
"v_lshlrev_b32 v237, 4, v237\n" \
"v_and_b32 v238, 7, v251\n" \
"v_lshlrev_b32 v238, 7, v238\n" \
".Ldj_%=:\n" \
"s_mov_b32 s86, 0\n" \
"s_and_b32 s85, s65, 15\n" \
"s_lshl2_add_u32 s85, s85, %[wid]\n" \
"s_lshl_b32 s22, s64, 6\n" \
"s_add_u32 s85, s85, s22\n" \
"s_lshl_b32 s85, s85, 13\n" \
"s_lshl_b32 s95, s67, 1\n" \
"s_cmp_lt_u32 s67, 4\n" \
"s_cbranch_scc1 .Lld_%=\n" \
"s_lshl_b32 s95, s84, 1\n" \
"s_sub_u32 s95, s95, 1\n" \
"s_cbranch_scc1 .Ll0_%=\n" \
".p2align 3\n" \
".Lloop0_%=:\n" \
  \
"v_mfma_f32_16x16x32_bf16 v[128:131], a[128:131], v[192:195], 0\n" \
"s_add_u32 s86, s86, 0x4000\n" \
"ds_read_b128 a[188:191], v240 offset:15360\n" \
"v_cvt_pk_bf16_f32 v152, v152, v153\n" \
"v_mfma_f32_16x16x32_bf16 v[128:131], a[132:135], v[196:199], v[128:131]\n" \
"s_bitset0_b32 s86, 16\n" \
"v_exp_f32 v154, v154\n" \
"v_mfma_f32_16x16x32_bf16 v[128:131], a[136:139], v[200:203], v[128:131]\n" \
"ds_read_b128 a[184:187], v240 offset:14336\n" \
"v_add_u32 v242, 0x10000, v240\n" \
"v_mfma_f32_16x16x32_bf16 v[128:131], a[140:143], v[204:207], v[128:131]\n" \
"s_waitcnt lgkmcnt(6)\n" \
"v_exp_f32 v155, v155\n" \
"v_mfma_f32_16x16x32_bf16 v[132:135], a[156:159], v[204:207], 0\n" \
"s_mov_b32 m0, s19\n" \
"ds_read_b128 a[180:183], v240 offset:13312\n" \
"v_cvt_pk_bf16_f32 v153, v154, v155\n" \
"v_mfma_f32_16x16x32_bf16 v[132:135], a[152:155], v[200:203], v[132:135]\n" \
"ds_read_b128 a[176:179], v240 offset:12288\n" \
"v_exp_f32 v156, v156\n" \
"v_mfma_f32_16x16x32_bf16 v[132:135], a[148:151], v[196:199], v[132:135]\n" \
"buffer_load_dwordx4 v248, s[32:35], s88 offen offset:0 sc1 lds\n" \
"v_mfma_f32_16x16x32_bf16 v[132:135], a[144:147], v[192:195], v[132:135]\n" \
"s_waitcnt lgkmcnt(4)\n" \
"v_exp_f32 v157, v157\n" \
"v_mfma_f32_16x16x32_bf16 v[136:139], a[160:163], v[192:195], 0\n" \
"v_cvt_pk_bf16_f32 v154, v156, v157\n" \
"v_mfma_f32_16x16x32_bf16 v[136:139], a[164:167], v[196:199], v[136:139]\n" \
"v_exp_f32 v158, v158\n" \
"v_mfma_f32_16x16x32_bf16 v[136:139], a[168:171], v[200:203], v[136:139]\n" \
"buffer_load_dwordx4 v248, s[32:35], s88 offen offset:1024 sc1 lds\n" \
"v_mfma_f32_16x16x32_bf16 v[136:139], a[172:175], v[204:207], v[136:139]\n" \
"s_waitcnt lgkmcnt(0)\n" \
"v_exp_f32 v159, v159\n" \
"v_mfma_f32_16x16x32_bf16 v[140:143], a[188:191], v[204:207], 0\n" \
"v_add_u32 v240, s86, v248\n" \
"v_cvt_pk_bf16_f32 v155, v158, v159\n" \
"v_mfma_f32_16x16x32_bf16 v[140:143], a[184:187], v[200:203], v[140:143]\n" \
"buffer_load_dwordx4 v248, s[32:35], s88 offen offset:2048 sc1 lds\n" \
"v_mfma_f32_16x16x32_bf16 v[140:143], a[180:183], v[196:199], v[140:143]\n" \
"v_exp_f32 v128, v128\n" \
"v_mfma_f32_16x16x32_bf16 v[140:143], a[176:179], v[192:195], v[140:143]\n" \
"buffer_load_dwordx4 v248, s[32:35], s88 offen offset:3072 sc1 lds\n" \
"v_mfma_f32_16x16x32_bf16 a[44:47], a[48:51], v[144:147], a[44:47]\n" \
"s_add_u32 s89, s88, 589824\n" \
"v_exp_f32 v129, v129\n" \
"v_mfma_f32_16x16x32_bf16 a[44:47], a[48:51], v[152:155], a[44:47]\n" \
"s_add_u32 s88, s88, s27\n" \
"s_add_u32 s19, s19, 0x4000\n" \
"v_cvt_pk_bf16_f32 v128, v128, v129\n" \
"v_mfma_f32_16x16x32_bf16 v[96:99], a[224:227], v[152:155], v[96:99]\n" \
"s_bitset0_b32 s19, 16\n" \
"v_exp_f32 v130, v130\n" \
"v_mfma_f32_16x16x32_bf16 v[96:99], a[192:195], v[144:147], v[96:99]\n" \
"s_cmp_eq_u32 s88, s16\n" \
"s_cmov_b32 s88, s21\n" \
"ds_read_b128 a[224:227], v242 offset:8192\n" \
"v_mfma_f32_16x16x32_bf16 v[100:103], a[196:199], v[144:147], v[100:103]\n" \
"s_cmov_b32 s27, s91\n" \
"v_exp_f32 v131, v131\n" \
"v_mfma_f32_16x16x32_bf16 v[100:103], a[228:231], v[152:155], v[100:103]\n" \
"s_cmov_b32 s16, s77\n" \
"ds_read_b128 a[192:195], v242 offset:0\n" \
"v_cvt_pk_bf16_f32 v129, v130, v131\n" \
"v_mfma_f32_16x16x32_bf16 v[104:107], a[232:235], v[152:155], v[104:107]\n" \
"s_cmov_b32 s21, s92\n" \
"v_exp_f32 v132, v132\n" \
"v_mfma_f32_16x16x32_bf16 v[104:107], a[200:203], v[144:147], v[104:107]\n" \
"s_cmovk_i32 s91, 0x4000\n" \
"ds_read_b128 a[196:199], v242 offset:1024\n" \
"ds_read_b128 a[228:231], v242 offset:9216\n" \
"v_mfma_f32_16x16x32_bf16 v[108:111], a[204:207], v[144:147], v[108:111]\n" \
"v_exp_f32 v133, v133\n" \
"v_mfma_f32_16x16x32_bf16 v[108:111], a[236:239], v[152:155], v[108:111]\n" \
"ds_read_b128 a[232:235], v242 offset:10240\n" \
"v_cvt_pk_bf16_f32 v130, v132, v133\n" \
"v_mfma_f32_16x16x32_bf16 v[112:115], a[240:243], v[152:155], v[112:115]\n" \
"v_exp_f32 v134, v134\n" \
"v_mfma_f32_16x16x32_bf16 v[112:115], a[208:211], v[144:147], v[112:115]\n" \
"ds_read_b128 a[200:203], v242 offset:2048\n" \
"v_mfma_f32_16x16x32_bf16 v[116:119], a[212:215], v[144:147], v[116:119]\n" \
"v_exp_f32 v135, v135\n" \
"v_mfma_f32_16x16x32_bf16 v[116:119], a[244:247], v[152:155], v[116:119]\n" \
"ds_read_b128 a[204:207], v242 offset:3072\n" \
"v_cvt_pk_bf16_f32 v131, v134, v135\n" \
"v_mfma_f32_16x16x32_bf16 v[120:123], a[248:251], v[152:155], v[120:123]\n" \
"ds_read_b128 a[236:239], v242 offset:11264\n" \
"v_mfma_f32_16x16x32_bf16 v[120:123], a[216:219], v[144:147], v[120:123]\n" \
"v_exp_f32 v136, v136\n" \
"v_mfma_f32_16x16x32_bf16 v[124:127], a[220:223], v[144:147], v[124:127]\n" \
"ds_read_b128 a[240:243], v242 offset:12288\n" \
"v_mfma_f32_16x16x32_bf16 v[124:127], a[252:255], v[152:155], v[124:127]\n" \
"s_waitcnt vmcnt(12)\n" \
"v_exp_f32 v137, v137\n" \
"v_mfma_f32_16x16x32_bf16 v[144:147], a[128:131], v[208:211], 0\n" \
"s_barrier\n" \
"ds_read_b128 a[208:211], v242 offset:4096\n" \
"v_cvt_pk_bf16_f32 v136, v136, v137\n" \
"v_mfma_f32_16x16x32_bf16 v[144:147], a[132:135], v[212:215], v[144:147]\n" \
"s_mov_b32 m0, s20\n" \
"v_exp_f32 v138, v138\n" \
"v_mfma_f32_16x16x32_bf16 v[144:147], a[136:139], v[216:219], v[144:147]\n" \
"buffer_load_dwordx4 v248, s[32:35], s89 offen offset:0 sc1 lds\n" \
"v_mfma_f32_16x16x32_bf16 v[144:147], a[140:143], v[220:223], v[144:147]\n" \
"v_exp_f32 v139, v139\n" \
"v_mfma_f32_16x16x32_bf16 v[148:151], a[156:159], v[220:223], 0\n" \
"ds_read_b128 a[212:215], v242 offset:5120\n" \
"v_cvt_pk_bf16_f32 v137, v138, v139\n" \
"v_mfma_f32_16x16x32_bf16 v[148:151], a[152:155], v[216:219], v[148:151]\n" \
"v_exp_f32 v140, v140\n" \
"v_mfma_f32_16x16x32_bf16 v[148:151], a[148:151], v[212:215], v[148:151]\n" \
"ds_read_b128 a[244:247], v242 offset:13312\n" \
"ds_read_b128 a[248:251], v242 offset:14336\n" \
"v_mfma_f32_16x16x32_bf16 v[148:151], a[144:147], v[208:211], v[148:151]\n" \
"v_exp_f32 v141, v141\n" \
"v_mfma_f32_16x16x32_bf16 v[152:155], a[160:163], v[208:211], 0\n" \
"buffer_load_dwordx4 v248, s[32:35], s89 offen offset:1024 sc1 lds\n" \
"v_mfma_f32_16x16x32_bf16 v[152:155], a[164:167], v[212:215], v[152:155]\n" \
"v_exp_f32 v142, v142\n" \
"v_mfma_f32_16x16x32_bf16 v[152:155], a[168:171], v[216:219], v[152:155]\n" \
"ds_read_b128 a[216:219], v242 offset:6144\n" \
"v_cvt_pk_bf16_f32 v138, v140, v141\n" \
"v_mfma_f32_16x16x32_bf16 v[152:155], a[172:175], v[220:223], v[152:155]\n" \
"v_exp_f32 v143, v143\n" \
"v_mfma_f32_16x16x32_bf16 v[156:159], a[188:191], v[220:223], 0\n" \
"ds_read_b128 a[128:131], v240 offset:0\n" \
"v_cvt_pk_bf16_f32 v139, v142, v143\n" \
"v_mfma_f32_16x16x32_bf16 v[156:159], a[184:187], v[216:219], v[156:159]\n" \
"ds_read_b128 a[132:135], v240 offset:1024\n" \
"ds_read_b128 a[220:223], v242 offset:7168\n" \
"v_mfma_f32_16x16x32_bf16 v[156:159], a[180:183], v[212:215], v[156:159]\n" \
"v_exp_f32 v144, v144\n" \
"v_mfma_f32_16x16x32_bf16 v[156:159], a[176:179], v[208:211], v[156:159]\n" \
"buffer_load_dwordx4 v248, s[32:35], s89 offen offset:2048 sc1 lds\n" \
"v_mfma_f32_16x16x32_bf16 a[32:35], a[48:51], v[128:131], a[32:35]\n" \
"v_exp_f32 v145, v145\n" \
"v_mfma_f32_16x16x32_bf16 a[32:35], a[48:51], v[136:139], a[32:35]\n" \
"s_waitcnt lgkmcnt(13)\n" \
"ds_read_b128 a[136:139], v240 offset:2048\n" \
"v_cvt_pk_bf16_f32 v144, v144, v145\n" \
"v_mfma_f32_16x16x32_bf16 v[0:3], a[224:227], v[136:139], v[0:3]\n" \
"v_exp_f32 v146, v146\n" \
"v_mfma_f32_16x16x32_bf16 v[0:3], a[192:195], v[128:131], v[0:3]\n" \
"ds_read_b128 a[252:255], v242 offset:15360\n" \
"ds_read_b128 a[140:143], v240 offset:3072\n" \
"v_mfma_f32_16x16x32_bf16 v[4:7], a[196:199], v[128:131], v[4:7]\n" \
"v_exp_f32 v147, v147\n" \
"v_mfma_f32_16x16x32_bf16 v[4:7], a[228:231], v[136:139], v[4:7]\n" \
"s_waitcnt lgkmcnt(12)\n" \
"buffer_load_dwordx4 v248, s[32:35], s89 offen offset:3072 sc1 lds\n" \
"v_mfma_f32_16x16x32_bf16 v[8:11], a[232:235], v[136:139], v[8:11]\n" \
"s_add_u32 s20, s20, 0x4000\n" \
"v_exp_f32 v148, v148\n" \
"v_mfma_f32_16x16x32_bf16 v[8:11], a[200:203], v[128:131], v[8:11]\n" \
"s_and_b32 s20, s20, 0xffff\n" \
"ds_read_b128 a[156:159], v240 offset:7168\n" \
"v_cvt_pk_bf16_f32 v145, v146, v147\n" \
"v_mfma_f32_16x16x32_bf16 v[12:15], a[204:207], v[128:131], v[12:15]\n" \
"s_bitset1_b32 s20, 16\n" \
"v_exp_f32 v149, v149\n" \
"v_mfma_f32_16x16x32_bf16 v[12:15], a[236:239], v[136:139], v[12:15]\n" \
"s_waitcnt lgkmcnt(9)\n" \
"ds_read_b128 a[152:155], v240 offset:6144\n" \
"v_cvt_pk_bf16_f32 v146, v148, v149\n" \
"v_mfma_f32_16x16x32_bf16 v[16:19], a[240:243], v[136:139], v[16:19]\n" \
"v_exp_f32 v150, v150\n" \
"v_mfma_f32_16x16x32_bf16 v[16:19], a[208:211], v[128:131], v[16:19]\n" \
"ds_read_b128 a[148:151], v240 offset:5120\n" \
"ds_read_b128 a[144:147], v240 offset:4096\n" \
"v_mfma_f32_16x16x32_bf16 v[20:23], a[212:215], v[128:131], v[20:23]\n" \
"v_exp_f32 v151, v151\n" \
"v_mfma_f32_16x16x32_bf16 v[20:23], a[244:247], v[136:139], v[20:23]\n" \
".Lst0_9_%=:\n" \
"s_cbranch_execz .Lst0_13_%=\n" \
"s_waitcnt lgkmcnt(5)\n" \
"ds_read_b128 a[160:163], v240 offset:8192\n" \
"v_cvt_pk_bf16_f32 v147, v150, v151\n" \
"v_mfma_f32_16x16x32_bf16 v[24:27], a[248:251], v[136:139], v[24:27]\n" \
"ds_read_b128 a[164:167], v240 offset:9216\n" \
"ds_read_b128 a[168:171], v240 offset:10240\n" \
"v_mfma_f32_16x16x32_bf16 v[24:27], a[216:219], v[128:131], v[24:27]\n" \
"v_exp_f32 v152, v152\n" \
"v_mfma_f32_16x16x32_bf16 v[28:31], a[220:223], v[128:131], v[28:31]\n" \
"ds_read_b128 a[172:175], v240 offset:11264\n" \
"ds_read_b128 a[188:191], v240 offset:15360\n" \
"v_mfma_f32_16x16x32_bf16 v[28:31], a[252:255], v[136:139], v[28:31]\n" \
"s_waitcnt lgkmcnt(9)\n" \
"v_exp_f32 v153, v153\n" \
"v_mfma_f32_16x16x32_bf16 v[128:131], a[128:131], v[192:195], 0\n" \
"s_add_u32 s86, s86, 0x4000\n" \
"ds_read_b128 a[184:187], v240 offset:14336\n" \
"v_cvt_pk_bf16_f32 v152, v152, v153\n" \
"v_mfma_f32_16x16x32_bf16 v[128:131], a[132:135], v[196:199], v[128:131]\n" \
"s_bitset0_b32 s86, 16\n" \
"v_exp_f32 v154, v154\n" \
"v_mfma_f32_16x16x32_bf16 v[128:131], a[136:139], v[200:203], v[128:131]\n" \
"ds_read_b128 a[180:183], v240 offset:13312\n" \
"v_add_u32 v242, 0x10000, v240\n" \
"v_mfma_f32_16x16x32_bf16 v[128:131], a[140:143], v[204:207], v[128:131]\n" \
"s_waitcnt lgkmcnt(7)\n" \
"v_exp_f32 v155, v155\n" \
"v_mfma_f32_16x16x32_bf16 v[132:135], a[156:159], v[204:207], 0\n" \
"s_mov_b32 m0, s19\n" \
"ds_read_b128 a[176:179], v240 offset:12288\n" \
"v_cvt_pk_bf16_f32 v153, v154, v155\n" \
"v_mfma_f32_16x16x32_bf16 v[132:135], a[152:155], v[200:203], v[132:135]\n" \
"v_exp_f32 v156, v156\n" \
"v_mfma_f32_16x16x32_bf16 v[132:135], a[148:151], v[196:199], v[132:135]\n" \
"buffer_load_dwordx4 v248, s[32:35], s88 offen offset:0 sc1 lds\n" \
"v_mfma_f32_16x16x32_bf16 v[132:135], a[144:147], v[192:195], v[132:135]\n" \
"s_waitcnt lgkmcnt(4)\n" \
"v_exp_f32 v157, v157\n" \
"v_mfma_f32_16x16x32_bf16 v[136:139], a[160:163], v[192:195], 0\n" \
"v_cvt_pk_bf16_f32 v154, v156, v157\n" \
"v_mfma_f32_16x16x32_bf16 v[136:139], a[164:167], v[196:199], v[136:139]\n" \
"v_exp_f32 v158, v158\n" \
"v_mfma_f32_16x16x32_bf16 v[136:139], a[168:171], v[200:203], v[136:139]\n" \
"v_add_u32 v240, s86, v248\n" \
"v_mfma_f32_16x16x32_bf16 v[136:139], a[172:175], v[204:207], v[136:139]\n" \
"s_waitcnt lgkmcnt(0)\n" \
"v_exp_f32 v159, v159\n" \
"v_mfma_f32_16x16x32_bf16 v[140:143], a[188:191], v[204:207], 0\n" \
"v_cvt_pk_bf16_f32 v155, v158, v159\n" \
"v_mfma_f32_16x16x32_bf16 v[140:143], a[184:187], v[200:203], v[140:143]\n" \
"buffer_load_dwordx4 v248, s[32:35], s88 offen offset:1024 sc1 lds\n" \
"v_mfma_f32_16x16x32_bf16 v[140:143], a[180:183], v[196:199], v[140:143]\n" \
"v_exp_f32 v128, v128\n" \
"v_mfma_f32_16x16x32_bf16 v[140:143], a[176:179], v[192:195], v[140:143]\n" \
"v_mfma_f32_16x16x32_bf16 a[36:39], a[48:51], v[144:147], a[36:39]\n" \
"v_exp_f32 v129, v129\n" \
"v_mfma_f32_16x16x32_bf16 a[36:39], a[48:51], v[152:155], a[36:39]\n" \
"v_cvt_pk_bf16_f32 v128, v128, v129\n" \
"v_mfma_f32_16x16x32_bf16 v[32:35], a[224:227], v[152:155], v[32:35]\n" \
"v_exp_f32 v130, v130\n" \
"v_mfma_f32_16x16x32_bf16 v[32:35], a[192:195], v[144:147], v[32:35]\n" \
"buffer_load_dwordx4 v248, s[32:35], s88 offen offset:2048 sc1 lds\n" \
"v_mfma_f32_16x16x32_bf16 v[36:39], a[196:199], v[144:147], v[36:39]\n" \
"v_exp_f32 v131, v131\n" \
"v_mfma_f32_16x16x32_bf16 v[36:39], a[228:231], v[152:155], v[36:39]\n" \
"ds_read_b128 a[224:227], v242 offset:8192\n" \
"v_cvt_pk_bf16_f32 v129, v130, v131\n" \
"v_mfma_f32_16x16x32_bf16 v[40:43], a[232:235], v[152:155], v[40:43]\n" \
"v_exp_f32 v132, v132\n" \
"v_mfma_f32_16x16x32_bf16 v[40:43], a[200:203], v[144:147], v[40:43]\n" \
"ds_read_b128 a[192:195], v242 offset:0\n" \
"ds_read_b128 a[196:199], v242 offset:1024\n" \
"v_mfma_f32_16x16x32_bf16 v[44:47], a[204:207], v[144:147], v[44:47]\n" \
"v_exp_f32 v133, v133\n" \
"v_mfma_f32_16x16x32_bf16 v[44:47], a[236:239], v[152:155], v[44:47]\n" \
"buffer_load_dwordx4 v248, s[32:35], s88 offen offset:3072 sc1 lds\n" \
"v_mfma_f32_16x16x32_bf16 v[48:51], a[240:243], v[152:155], v[48:51]\n" \
"s_mov_b32 m0, s87\n" \
"v_exp_f32 v134, v134\n" \
"v_mfma_f32_16x16x32_bf16 v[48:51], a[208:211], v[144:147], v[48:51]\n" \
"s_add_u32 s89, s88, 589824\n" \
"ds_read_b128 a[228:231], v242 offset:9216\n" \
"v_cvt_pk_bf16_f32 v130, v132, v133\n" \
"v_mfma_f32_16x16x32_bf16 v[52:55], a[212:215], v[144:147], v[52:55]\n" \
"s_add_u32 s88, s88, s27\n" \
"buffer_load_dwordx4 v237, s[48:51], s93 offen lds\n" \
"v_mfma_f32_16x16x32_bf16 v[52:55], a[244:247], v[152:155], v[52:55]\n" \
"s_add_u32 s93, s93, 8192\n" \
"v_exp_f32 v135, v135\n" \
"v_mfma_f32_16x16x32_bf16 v[56:59], a[248:251], v[152:155], v[56:59]\n" \
"s_add_u32 s87, s87, 1024\n" \
"ds_read_b128 a[232:235], v242 offset:10240\n" \
"v_cvt_pk_bf16_f32 v131, v134, v135\n" \
"v_mfma_f32_16x16x32_bf16 v[56:59], a[216:219], v[144:147], v[56:59]\n" \
"s_and_b32 s22, s87, 0xfff\n" \
"v_exp_f32 v136, v136\n" \
"v_mfma_f32_16x16x32_bf16 v[60:63], a[220:223], v[144:147], v[60:63]\n" \
"s_cselect_b32 s22, 0, 0xffff8080\n" \
"ds_read_b128 a[200:203], v242 offset:2048\n" \
"ds_read_b128 a[204:207], v242 offset:3072\n" \
"v_mfma_f32_16x16x32_bf16 v[60:63], a[252:255], v[152:155], v[60:63]\n" \
"s_add_u32 s93, s93, s22\n" \
"v_exp_f32 v137, v137\n" \
"v_mfma_f32_16x16x32_bf16 v[144:147], a[128:131], v[208:211], 0\n" \
"s_add_u32 s19, s19, 0x4000\n" \
"buffer_load_dword v245, v230, s[40:43], s38 offen offset:512\n" \
"v_cvt_pk_bf16_f32 v136, v136, v137\n" \
"v_mfma_f32_16x16x32_bf16 v[144:147], a[132:135], v[212:215], v[144:147]\n" \
"s_bitset0_b32 s19, 16\n" \
"v_exp_f32 v138, v138\n" \
"v_mfma_f32_16x16x32_bf16 v[144:147], a[136:139], v[216:219], v[144:147]\n" \
"s_cmp_eq_u32 s88, s16\n" \
"ds_read_b128 a[236:239], v242 offset:11264\n" \
"ds_read_b128 a[240:243], v242 offset:12288\n" \
"v_mfma_f32_16x16x32_bf16 v[144:147], a[140:143], v[220:223], v[144:147]\n" \
"s_cmov_b32 s88, s21\n" \
"v_exp_f32 v139, v139\n" \
"v_mfma_f32_16x16x32_bf16 v[148:151], a[156:159], v[220:223], 0\n" \
"s_cmov_b32 s27, s91\n" \
"ds_read_b128 a[208:211], v242 offset:4096\n" \
"v_cvt_pk_bf16_f32 v137, v138, v139\n" \
"v_mfma_f32_16x16x32_bf16 v[148:151], a[152:155], v[216:219], v[148:151]\n" \
"s_cmov_b32 s16, s77\n" \
"v_exp_f32 v140, v140\n" \
"v_mfma_f32_16x16x32_bf16 v[148:151], a[148:151], v[212:215], v[148:151]\n" \
"s_cmov_b32 s21, s92\n" \
"s_cmovk_i32 s91, 0x4000\n" \
"buffer_load_dword v245, v238, s[48:51], s85 offen\n" \
"v_mfma_f32_16x16x32_bf16 v[148:151], a[144:147], v[208:211], v[148:151]\n" \
"v_exp_f32 v141, v141\n" \
"v_mfma_f32_16x16x32_bf16 v[152:155], a[160:163], v[208:211], 0\n" \
"ds_read_b128 a[212:215], v242 offset:5120\n" \
"v_cvt_pk_bf16_f32 v138, v140, v141\n" \
"v_mfma_f32_16x16x32_bf16 v[152:155], a[164:167], v[212:215], v[152:155]\n" \
"v_exp_f32 v142, v142\n" \
"v_mfma_f32_16x16x32_bf16 v[152:155], a[168:171], v[216:219], v[152:155]\n" \
"ds_read_b128 a[244:247], v242 offset:13312\n" \
"ds_read_b128 a[248:251], v242 offset:14336\n" \
"v_mfma_f32_16x16x32_bf16 v[152:155], a[172:175], v[220:223], v[152:155]\n" \
"v_exp_f32 v143, v143\n" \
"v_mfma_f32_16x16x32_bf16 v[156:159], a[188:191], v[220:223], 0\n" \
"buffer_load_dword v245, v231, s[60:63], s72 offen\n" \
"v_cvt_pk_bf16_f32 v139, v142, v143\n" \
"v_mfma_f32_16x16x32_bf16 v[156:159], a[184:187], v[216:219], v[156:159]\n" \
"s_add_u32 s85, s85, 1024\n" \
"ds_read_b128 a[216:219], v242 offset:6144\n" \
"ds_read_b128 a[220:223], v242 offset:7168\n" \
"v_mfma_f32_16x16x32_bf16 v[156:159], a[180:183], v[212:215], v[156:159]\n" \
"s_add_u32 s72, s72, 512\n" \
"v_exp_f32 v144, v144\n" \
"v_mfma_f32_16x16x32_bf16 v[156:159], a[176:179], v[208:211], v[156:159]\n" \
"ds_read_b128 a[252:255], v242 offset:15360\n" \
"v_mfma_f32_16x16x32_bf16 a[32:35], a[48:51], v[128:131], a[32:35]\n" \
"v_exp_f32 v145, v145\n" \
"v_mfma_f32_16x16x32_bf16 a[32:35], a[48:51], v[136:139], a[32:35]\n" \
"s_waitcnt lgkmcnt(12)\n" \
"v_cvt_pk_bf16_f32 v144, v144, v145\n" \
"v_mfma_f32_16x16x32_bf16 v[0:3], a[224:227], v[136:139], v[0:3]\n" \
"v_exp_f32 v146, v146\n" \
"v_mfma_f32_16x16x32_bf16 v[0:3], a[192:195], v[128:131], v[0:3]\n" \
"v_mfma_f32_16x16x32_bf16 v[4:7], a[196:199], v[128:131], v[4:7]\n" \
"v_exp_f32 v147, v147\n" \
"v_mfma_f32_16x16x32_bf16 v[4:7], a[228:231], v[136:139], v[4:7]\n" \
"s_waitcnt lgkmcnt(8)\n" \
"v_cvt_pk_bf16_f32 v145, v146, v147\n" \
"v_mfma_f32_16x16x32_bf16 v[8:11], a[232:235], v[136:139], v[8:11]\n" \
"v_exp_f32 v148, v148\n" \
"v_mfma_f32_16x16x32_bf16 v[8:11], a[200:203], v[128:131], v[8:11]\n" \
"v_mfma_f32_16x16x32_bf16 v[12:15], a[204:207], v[128:131], v[12:15]\n" \
"v_exp_f32 v149, v149\n" \
"v_mfma_f32_16x16x32_bf16 v[12:15], a[236:239], v[136:139], v[12:15]\n" \
"s_waitcnt lgkmcnt(4)\n" \
"v_cvt_pk_bf16_f32 v146, v148, v149\n" \
"v_mfma_f32_16x16x32_bf16 v[16:19], a[240:243], v[136:139], v[16:19]\n" \
"v_exp_f32 v150, v150\n" \
"v_mfma_f32_16x16x32_bf16 v[16:19], a[208:211], v[128:131], v[16:19]\n" \
"v_mfma_f32_16x16x32_bf16 v[20:23], a[212:215], v[128:131], v[20:23]\n" \
"v_exp_f32 v151, v151\n" \
"v_mfma_f32_16x16x32_bf16 v[20:23], a[244:247], v[136:139], v[20:23]\n" \
"s_waitcnt lgkmcnt(0)\n" \
"v_cvt_pk_bf16_f32 v147, v150, v151\n" \
"v_mfma_f32_16x16x32_bf16 v[24:27], a[248:251], v[136:139], v[24:27]\n" \
"v_mfma_f32_16x16x32_bf16 v[24:27], a[216:219], v[128:131], v[24:27]\n" \
"v_exp_f32 v152, v152\n" \
"v_mfma_f32_16x16x32_bf16 v[28:31], a[220:223], v[128:131], v[28:31]\n" \
"v_mfma_f32_16x16x32_bf16 v[28:31], a[252:255], v[136:139], v[28:31]\n" \
"v_exp_f32 v153, v153\n" \
"v_mfma_f32_16x16x32_bf16 v[128:131], a[128:131], v[160:163], 0\n" \
"v_cvt_pk_bf16_f32 v152, v152, v153\n" \
"v_mfma_f32_16x16x32_bf16 v[128:131], a[132:135], v[164:167], v[128:131]\n" \
"v_exp_f32 v154, v154\n" \
"v_mfma_f32_16x16x32_bf16 v[128:131], a[136:139], v[168:171], v[128:131]\n" \
"v_mfma_f32_16x16x32_bf16 v[128:131], a[140:143], v[172:175], v[128:131]\n" \
"v_exp_f32 v155, v155\n" \
"v_mfma_f32_16x16x32_bf16 v[132:135], a[156:159], v[172:175], 0\n" \
"v_cvt_pk_bf16_f32 v153, v154, v155\n" \
"v_mfma_f32_16x16x32_bf16 v[132:135], a[152:155], v[168:171], v[132:135]\n" \
"v_exp_f32 v156, v156\n" \
"v_mfma_f32_16x16x32_bf16 v[132:135], a[148:151], v[164:167], v[132:135]\n" \
"v_mfma_f32_16x16x32_bf16 v[132:135], a[144:147], v[160:163], v[132:135]\n" \
"v_exp_f32 v157, v157\n" \
"v_mfma_f32_16x16x32_bf16 v[136:139], a[160:163], v[160:163], 0\n" \
"v_cvt_pk_bf16_f32 v154, v156, v157\n" \
"v_mfma_f32_16x16x32_bf16 v[136:139], a[164:167], v[164:167], v[136:139]\n" \
"v_exp_f32 v158, v158\n" \
"v_mfma_f32_16x16x32_bf16 v[136:139], a[168:171], v[168:171], v[136:139]\n" \
"v_mfma_f32_16x16x32_bf16 v[136:139], a[172:175], v[172:175], v[136:139]\n" \
"v_exp_f32 v159, v159\n" \
"v_mfma_f32_16x16x32_bf16 v[140:143], a[188:191], v[172:175], 0\n" \
"v_cvt_pk_bf16_f32 v155, v158, v159\n" \
"v_mfma_f32_16x16x32_bf16 v[140:143], a[184:187], v[168:171], v[140:143]\n" \
"v_mfma_f32_16x16x32_bf16 v[140:143], a[180:183], v[164:167], v[140:143]\n" \
"v_exp_f32 v128, v128\n" \
"v_mfma_f32_16x16x32_bf16 v[140:143], a[176:179], v[160:163], v[140:143]\n" \
"v_mfma_f32_16x16x32_bf16 a[36:39], a[48:51], v[144:147], a[36:39]\n" \
"s_waitcnt vmcnt(16)\n" \
"v_exp_f32 v129, v129\n" \
"v_mfma_f32_16x16x32_bf16 a[36:39], a[48:51], v[152:155], a[36:39]\n" \
"buffer_store_dwordx4 a[60:63], v232, s[32:35], s39 offen sc1\n" \
"v_mfma_f32_16x16x32_bf16 v[32:35], a[224:227], v[152:155], v[32:35]\n" \
"v_exp_f32 v130, v130\n" \
"v_mfma_f32_16x16x32_bf16 v[32:35], a[192:195], v[144:147], v[32:35]\n" \
"v_cvt_pk_bf16_f32 v128, v128, v129\n" \
"v_mfma_f32_16x16x32_bf16 v[36:39], a[196:199], v[144:147], v[36:39]\n" \
"v_exp_f32 v131, v131\n" \
"v_mfma_f32_16x16x32_bf16 v[36:39], a[228:231], v[152:155], v[36:39]\n" \
"buffer_load_dwordx4 a[60:63], v230, s[40:43], s38 offen\n" \
"v_cvt_pk_bf16_f32 v129, v130, v131\n" \
"v_mfma_f32_16x16x32_bf16 v[40:43], a[232:235], v[152:155], v[40:43]\n" \
"s_add_u32 s38, s38, 512\n" \
"v_exp_f32 v132, v132\n" \
"v_mfma_f32_16x16x32_bf16 v[40:43], a[200:203], v[144:147], v[40:43]\n" \
"s_add_u32 s39, s39, 0x80000\n" \
"v_mfma_f32_16x16x32_bf16 v[44:47], a[204:207], v[144:147], v[44:47]\n" \
"v_exp_f32 v133, v133\n" \
"v_mfma_f32_16x16x32_bf16 v[44:47], a[236:239], v[152:155], v[44:47]\n" \
"v_cvt_pk_bf16_f32 v130, v132, v133\n" \
"v_mfma_f32_16x16x32_bf16 v[48:51], a[240:243], v[152:155], v[48:51]\n" \
"v_exp_f32 v134, v134\n" \
"v_mfma_f32_16x16x32_bf16 v[48:51], a[208:211], v[144:147], v[48:51]\n" \
"v_mfma_f32_16x16x32_bf16 v[52:55], a[212:215], v[144:147], v[52:55]\n" \
"v_exp_f32 v135, v135\n" \
"v_mfma_f32_16x16x32_bf16 v[52:55], a[244:247], v[152:155], v[52:55]\n" \
"v_cvt_pk_bf16_f32 v131, v134, v135\n" \
"v_mfma_f32_16x16x32_bf16 v[56:59], a[248:251], v[152:155], v[56:59]\n" \
"v_mfma_f32_16x16x32_bf16 v[56:59], a[216:219], v[144:147], v[56:59]\n" \
"v_exp_f32 v136, v136\n" \
"v_mfma_f32_16x16x32_bf16 v[60:63], a[220:223], v[144:147], v[60:63]\n" \
"v_mfma_f32_16x16x32_bf16 v[60:63], a[252:255], v[152:155], v[60:63]\n" \
"s_waitcnt vmcnt(18)\n" \
"v_exp_f32 v137, v137\n" \
"v_mfma_f32_16x16x32_bf16 v[144:147], a[128:131], v[176:179], 0\n" \
"s_barrier\n" \
"s_mov_b32 m0, s20\n" \
"v_cvt_pk_bf16_f32 v136, v136, v137\n" \
"v_mfma_f32_16x16x32_bf16 v[144:147], a[132:135], v[180:183], v[144:147]\n" \
"v_exp_f32 v138, v138\n" \
"v_mfma_f32_16x16x32_bf16 v[144:147], a[136:139], v[184:187], v[144:147]\n" \
"buffer_load_dwordx4 v248, s[32:35], s89 offen offset:0 sc1 lds\n" \
"v_mfma_f32_16x16x32_bf16 v[144:147], a[140:143], v[188:191], v[144:147]\n" \
"v_exp_f32 v139, v139\n" \
"v_mfma_f32_16x16x32_bf16 v[148:151], a[156:159], v[188:191], 0\n" \
"ds_read_b128 a[128:131], v240 offset:0\n" \
"v_cvt_pk_bf16_f32 v137, v138, v139\n" \
"v_mfma_f32_16x16x32_bf16 v[148:151], a[152:155], v[184:187], v[148:151]\n" \
"v_exp_f32 v140, v140\n" \
"v_mfma_f32_16x16x32_bf16 v[148:151], a[148:151], v[180:183], v[148:151]\n" \
"ds_read_b128 a[132:135], v240 offset:1024\n" \
"ds_read_b128 a[136:139], v240 offset:2048\n" \
"v_mfma_f32_16x16x32_bf16 v[148:151], a[144:147], v[176:179], v[148:151]\n" \
"v_exp_f32 v141, v141\n" \
"v_mfma_f32_16x16x32_bf16 v[152:155], a[160:163], v[176:179], 0\n" \
"buffer_load_dwordx4 v248, s[32:35], s89 offen offset:1024 sc1 lds\n" \
"v_mfma_f32_16x16x32_bf16 v[152:155], a[164:167], v[180:183], v[152:155]\n" \
"v_exp_f32 v142, v142\n" \
"v_mfma_f32_16x16x32_bf16 v[152:155], a[168:171], v[184:187], v[152:155]\n" \
"ds_read_b128 a[140:143], v240 offset:3072\n" \
"v_cvt_pk_bf16_f32 v138, v140, v141\n" \
"v_mfma_f32_16x16x32_bf16 v[152:155], a[172:175], v[188:191], v[152:155]\n" \
"v_exp_f32 v143, v143\n" \
"v_mfma_f32_16x16x32_bf16 v[156:159], a[188:191], v[188:191], 0\n" \
"ds_read_b128 a[156:159], v240 offset:7168\n" \
"v_cvt_pk_bf16_f32 v139, v142, v143\n" \
"v_mfma_f32_16x16x32_bf16 v[156:159], a[184:187], v[184:187], v[156:159]\n" \
"ds_read_b128 a[152:155], v240 offset:6144\n" \
"ds_read_b128 a[148:151], v240 offset:5120\n" \
"v_mfma_f32_16x16x32_bf16 v[156:159], a[180:183], v[180:183], v[156:159]\n" \
"v_exp_f32 v144, v144\n" \
"v_mfma_f32_16x16x32_bf16 v[156:159], a[176:179], v[176:179], v[156:159]\n" \
"buffer_load_dwordx4 v248, s[32:35], s89 offen offset:2048 sc1 lds\n" \
"v_mfma_f32_16x16x32_bf16 a[40:43], a[48:51], v[128:131], a[40:43]\n" \
"v_exp_f32 v145, v145\n" \
"v_mfma_f32_16x16x32_bf16 a[40:43], a[48:51], v[136:139], a[40:43]\n" \
"ds_read_b128 a[144:147], v240 offset:4096\n" \
"v_cvt_pk_bf16_f32 v144, v144, v145\n" \
"v_mfma_f32_16x16x32_bf16 v[64:67], a[224:227], v[136:139], v[64:67]\n" \
"v_exp_f32 v146, v146\n" \
"v_mfma_f32_16x16x32_bf16 v[64:67], a[192:195], v[128:131], v[64:67]\n" \
"ds_read_b128 a[160:163], v240 offset:8192\n" \
"ds_read_b128 a[164:167], v240 offset:9216\n" \
"v_mfma_f32_16x16x32_bf16 v[68:71], a[196:199], v[128:131], v[68:71]\n" \
"v_exp_f32 v147, v147\n" \
"v_mfma_f32_16x16x32_bf16 v[68:71], a[228:231], v[136:139], v[68:71]\n" \
"buffer_load_dwordx4 v248, s[32:35], s89 offen offset:3072 sc1 lds\n" \
"v_mfma_f32_16x16x32_bf16 v[72:75], a[232:235], v[136:139], v[72:75]\n" \
"s_add_u32 s20, s20, 0x4000\n" \
"v_exp_f32 v148, v148\n" \
"v_mfma_f32_16x16x32_bf16 v[72:75], a[200:203], v[128:131], v[72:75]\n" \
"s_and_b32 s20, s20, 0xffff\n" \
"ds_read_b128 a[168:171], v240 offset:10240\n" \
"v_cvt_pk_bf16_f32 v145, v146, v147\n" \
"v_mfma_f32_16x16x32_bf16 v[76:79], a[204:207], v[128:131], v[76:79]\n" \
"s_bitset1_b32 s20, 16\n" \
"v_exp_f32 v149, v149\n" \
"v_mfma_f32_16x16x32_bf16 v[76:79], a[236:239], v[136:139], v[76:79]\n" \
"ds_read_b128 a[172:175], v240 offset:11264\n" \
"v_cvt_pk_bf16_f32 v146, v148, v149\n" \
"v_mfma_f32_16x16x32_bf16 v[80:83], a[240:243], v[136:139], v[80:83]\n" \
"v_exp_f32 v150, v150\n" \
"v_mfma_f32_16x16x32_bf16 v[80:83], a[208:211], v[128:131], v[80:83]\n" \
"v_mfma_f32_16x16x32_bf16 v[84:87], a[212:215], v[128:131], v[84:87]\n" \
"s_sub_u32 s95, s95, 1\n" \
"v_exp_f32 v151, v151\n" \
"v_mfma_f32_16x16x32_bf16 v[84:87], a[244:247], v[136:139], v[84:87]\n" \
"v_cvt_pk_bf16_f32 v147, v150, v151\n" \
"v_mfma_f32_16x16x32_bf16 v[88:91], a[248:251], v[136:139], v[88:91]\n" \
"v_mfma_f32_16x16x32_bf16 v[88:91], a[216:219], v[128:131], v[88:91]\n" \
"v_exp_f32 v152, v152\n" \
"v_mfma_f32_16x16x32_bf16 v[92:95], a[220:223], v[128:131], v[92:95]\n" \
"s_waitcnt lgkmcnt(8)\n" \
"v_exp_f32 v153, v153\n" \
"v_mfma_f32_16x16x32_bf16 v[92:95], a[252:255], v[136:139], v[92:95]\n" \
"s_cbranch_scc0 .Lloop0_%=\n" \
".Ll0_%=:\n" \
".Lst0_10_%=:\n" \
"s_cbranch_execz .Lst0_14_%=\n" \
"s_lshl_b32 s95, s84, 1\n" \
"s_sub_u32 s95, 7, s95\n" \
".p2align 3\n" \
".Lloop_%=:\n" \
  \
"v_mfma_f32_16x16x32_bf16 v[128:131], a[128:131], v[192:195], 0\n" \
"s_add_u32 s86, s86, 0x4000\n" \
"ds_read_b128 a[188:191], v240 offset:15360\n" \
"v_cvt_pk_bf16_f32 v152, v152, v153\n" \
"v_mfma_f32_16x16x32_bf16 v[128:131], a[132:135], v[196:199], v[128:131]\n" \
"s_bitset0_b32 s86, 16\n" \
"v_exp_f32 v154, v154\n" \
"v_mfma_f32_16x16x32_bf16 v[128:131], a[136:139], v[200:203], v[128:131]\n" \
"ds_read_b128 a[184:187], v240 offset:14336\n" \
"v_add_u32 v242, 0x10000, v240\n" \
"v_mfma_f32_16x16x32_bf16 v[128:131], a[140:143], v[204:207], v[128:131]\n" \
"s_waitcnt lgkmcnt(6)\n" \
"v_exp_f32 v155, v155\n" \
"v_mfma_f32_16x16x32_bf16 v[132:135], a[156:159], v[204:207], 0\n" \
"s_mov_b32 m0, s19\n" \
"ds_read_b128 a[180:183], v240 offset:13312\n" \
"v_cvt_pk_bf16_f32 v153, v154, v155\n" \
"v_mfma_f32_16x16x32_bf16 v[132:135], a[152:155], v[200:203], v[132:135]\n" \
"ds_read_b128 a[176:179], v240 offset:12288\n" \
"v_exp_f32 v156, v156\n" \
"v_mfma_f32_16x16x32_bf16 v[132:135], a[148:151], v[196:199], v[132:135]\n" \
"buffer_load_dwordx4 v248, s[32:35], s88 offen offset:0 sc1 lds\n" \
"v_mfma_f32_16x16x32_bf16 v[132:135], a[144:147], v[192:195], v[132:135]\n" \
"s_waitcnt lgkmcnt(4)\n" \
"v_exp_f32 v157, v157\n" \
"v_mfma_f32_16x16x32_bf16 v[136:139], a[160:163], v[192:195], 0\n" \
"v_cvt_pk_bf16_f32 v154, v156, v157\n" \
"v_mfma_f32_16x16x32_bf16 v[136:139], a[164:167], v[196:199], v[136:139]\n" \
"v_exp_f32 v158, v158\n" \
"v_mfma_f32_16x16x32_bf16 v[136:139], a[168:171], v[200:203], v[136:139]\n" \
"v_add_u32 v240, s86, v248\n" \
"v_mfma_f32_16x16x32_bf16 v[136:139], a[172:175], v[204:207], v[136:139]\n" \
"s_waitcnt lgkmcnt(0)\n" \
"v_exp_f32 v159, v159\n" \
"v_mfma_f32_16x16x32_bf16 v[140:143], a[188:191], v[204:207], 0\n" \
"v_cvt_pk_bf16_f32 v155, v158, v159\n" \
"v_mfma_f32_16x16x32_bf16 v[140:143], a[184:187], v[200:203], v[140:143]\n" \
"buffer_load_dwordx4 v248, s[32:35], s88 offen offset:1024 sc1 lds\n" \
"v_mfma_f32_16x16x32_bf16 v[140:143], a[180:183], v[196:199], v[140:143]\n" \
"v_exp_f32 v128, v128\n" \
"v_mfma_f32_16x16x32_bf16 v[140:143], a[176:179], v[192:195], v[140:143]\n" \
"v_mfma_f32_16x16x32_bf16 a[44:47], a[48:51], v[144:147], a[44:47]\n" \
"v_exp_f32 v129, v129\n" \
"v_mfma_f32_16x16x32_bf16 a[44:47], a[48:51], v[152:155], a[44:47]\n" \
"v_cvt_pk_bf16_f32 v128, v128, v129\n" \
"v_mfma_f32_16x16x32_bf16 v[96:99], a[224:227], v[152:155], v[96:99]\n" \
"v_exp_f32 v130, v130\n" \
"v_mfma_f32_16x16x32_bf16 v[96:99], a[192:195], v[144:147], v[96:99]\n" \
"buffer_load_dwordx4 v248, s[32:35], s88 offen offset:2048 sc1 lds\n" \
"v_mfma_f32_16x16x32_bf16 v[100:103], a[196:199], v[144:147], v[100:103]\n" \
"v_exp_f32 v131, v131\n" \
"v_mfma_f32_16x16x32_bf16 v[100:103], a[228:231], v[152:155], v[100:103]\n" \
"ds_read_b128 a[224:227], v242 offset:8192\n" \
"v_cvt_pk_bf16_f32 v129, v130, v131\n" \
"v_mfma_f32_16x16x32_bf16 v[104:107], a[232:235], v[152:155], v[104:107]\n" \
"v_exp_f32 v132, v132\n" \
"v_mfma_f32_16x16x32_bf16 v[104:107], a[200:203], v[144:147], v[104:107]\n" \
"ds_read_b128 a[192:195], v242 offset:0\n" \
"ds_read_b128 a[196:199], v242 offset:1024\n" \
"v_mfma_f32_16x16x32_bf16 v[108:111], a[204:207], v[144:147], v[108:111]\n" \
"v_exp_f32 v133, v133\n" \
"v_mfma_f32_16x16x32_bf16 v[108:111], a[236:239], v[152:155], v[108:111]\n" \
"buffer_load_dwordx4 v248, s[32:35], s88 offen offset:3072 sc1 lds\n" \
"v_mfma_f32_16x16x32_bf16 v[112:115], a[240:243], v[152:155], v[112:115]\n" \
"s_mov_b32 m0, s87\n" \
"v_exp_f32 v134, v134\n" \
"v_mfma_f32_16x16x32_bf16 v[112:115], a[208:211], v[144:147], v[112:115]\n" \
"s_add_u32 s89, s88, 589824\n" \
"ds_read_b128 a[228:231], v242 offset:9216\n" \
"v_cvt_pk_bf16_f32 v130, v132, v133\n" \
"v_mfma_f32_16x16x32_bf16 v[116:119], a[212:215], v[144:147], v[116:119]\n" \
"s_add_u32 s88, s88, s27\n" \
"buffer_load_dwordx4 v237, s[48:51], s93 offen lds\n" \
"v_mfma_f32_16x16x32_bf16 v[116:119], a[244:247], v[152:155], v[116:119]\n" \
"s_add_u32 s93, s93, 8192\n" \
"v_exp_f32 v135, v135\n" \
"v_mfma_f32_16x16x32_bf16 v[120:123], a[248:251], v[152:155], v[120:123]\n" \
"s_add_u32 s87, s87, 1024\n" \
"ds_read_b128 a[232:235], v242 offset:10240\n" \
"v_cvt_pk_bf16_f32 v131, v134, v135\n" \
"v_mfma_f32_16x16x32_bf16 v[120:123], a[216:219], v[144:147], v[120:123]\n" \
"s_and_b32 s22, s87, 0xfff\n" \
"v_exp_f32 v136, v136\n" \
"v_mfma_f32_16x16x32_bf16 v[124:127], a[220:223], v[144:147], v[124:127]\n" \
"s_cselect_b32 s22, 0, 0xffff8080\n" \
"ds_read_b128 a[200:203], v242 offset:2048\n" \
"ds_read_b128 a[204:207], v242 offset:3072\n" \
"v_mfma_f32_16x16x32_bf16 v[124:127], a[252:255], v[152:155], v[124:127]\n" \
"s_add_u32 s93, s93, s22\n" \
"v_exp_f32 v137, v137\n" \
"v_mfma_f32_16x16x32_bf16 v[144:147], a[128:131], v[208:211], 0\n" \
"s_add_u32 s19, s19, 0x4000\n" \
"buffer_load_dword v245, v230, s[40:43], s38 offen offset:512\n" \
"v_cvt_pk_bf16_f32 v136, v136, v137\n" \
"v_mfma_f32_16x16x32_bf16 v[144:147], a[132:135], v[212:215], v[144:147]\n" \
"s_bitset0_b32 s19, 16\n" \
"v_exp_f32 v138, v138\n" \
"v_mfma_f32_16x16x32_bf16 v[144:147], a[136:139], v[216:219], v[144:147]\n" \
"s_cmp_eq_u32 s88, s16\n" \
"ds_read_b128 a[236:239], v242 offset:11264\n" \
"ds_read_b128 a[240:243], v242 offset:12288\n" \
"v_mfma_f32_16x16x32_bf16 v[144:147], a[140:143], v[220:223], v[144:147]\n" \
"s_cmov_b32 s88, s21\n" \
"v_exp_f32 v139, v139\n" \
"v_mfma_f32_16x16x32_bf16 v[148:151], a[156:159], v[220:223], 0\n" \
"s_cmov_b32 s27, s91\n" \
"ds_read_b128 a[208:211], v242 offset:4096\n" \
"v_cvt_pk_bf16_f32 v137, v138, v139\n" \
"v_mfma_f32_16x16x32_bf16 v[148:151], a[152:155], v[216:219], v[148:151]\n" \
"s_cmov_b32 s16, s77\n" \
"v_exp_f32 v140, v140\n" \
"v_mfma_f32_16x16x32_bf16 v[148:151], a[148:151], v[212:215], v[148:151]\n" \
"s_cmov_b32 s21, s92\n" \
"s_cmovk_i32 s91, 0x4000\n" \
"buffer_load_dword v245, v238, s[48:51], s85 offen\n" \
"v_mfma_f32_16x16x32_bf16 v[148:151], a[144:147], v[208:211], v[148:151]\n" \
"v_exp_f32 v141, v141\n" \
"v_mfma_f32_16x16x32_bf16 v[152:155], a[160:163], v[208:211], 0\n" \
"ds_read_b128 a[212:215], v242 offset:5120\n" \
"v_cvt_pk_bf16_f32 v138, v140, v141\n" \
"v_mfma_f32_16x16x32_bf16 v[152:155], a[164:167], v[212:215], v[152:155]\n" \
"v_exp_f32 v142, v142\n" \
"v_mfma_f32_16x16x32_bf16 v[152:155], a[168:171], v[216:219], v[152:155]\n" \
"ds_read_b128 a[244:247], v242 offset:13312\n" \
"ds_read_b128 a[248:251], v242 offset:14336\n" \
"v_mfma_f32_16x16x32_bf16 v[152:155], a[172:175], v[220:223], v[152:155]\n" \
"v_exp_f32 v143, v143\n" \
"v_mfma_f32_16x16x32_bf16 v[156:159], a[188:191], v[220:223], 0\n" \
"buffer_load_dword v245, v231, s[60:63], s72 offen\n" \
"v_cvt_pk_bf16_f32 v139, v142, v143\n" \
"v_mfma_f32_16x16x32_bf16 v[156:159], a[184:187], v[216:219], v[156:159]\n" \
"s_add_u32 s85, s85, 1024\n" \
"ds_read_b128 a[216:219], v242 offset:6144\n" \
"ds_read_b128 a[220:223], v242 offset:7168\n" \
"v_mfma_f32_16x16x32_bf16 v[156:159], a[180:183], v[212:215], v[156:159]\n" \
"s_add_u32 s72, s72, 512\n" \
"v_exp_f32 v144, v144\n" \
"v_mfma_f32_16x16x32_bf16 v[156:159], a[176:179], v[208:211], v[156:159]\n" \
"ds_read_b128 a[252:255], v242 offset:15360\n" \
"v_mfma_f32_16x16x32_bf16 a[32:35], a[48:51], v[128:131], a[32:35]\n" \
"v_exp_f32 v145, v145\n" \
"v_mfma_f32_16x16x32_bf16 a[32:35], a[48:51], v[136:139], a[32:35]\n" \
"s_waitcnt lgkmcnt(12)\n" \
"v_cvt_pk_bf16_f32 v144, v144, v145\n" \
"v_mfma_f32_16x16x32_bf16 v[0:3], a[224:227], v[136:139], v[0:3]\n" \
"v_exp_f32 v146, v146\n" \
"v_mfma_f32_16x16x32_bf16 v[0:3], a[192:195], v[128:131], v[0:3]\n" \
"v_mfma_f32_16x16x32_bf16 v[4:7], a[196:199], v[128:131], v[4:7]\n" \
"v_exp_f32 v147, v147\n" \
"v_mfma_f32_16x16x32_bf16 v[4:7], a[228:231], v[136:139], v[4:7]\n" \
"s_waitcnt lgkmcnt(8)\n" \
"v_cvt_pk_bf16_f32 v145, v146, v147\n" \
"v_mfma_f32_16x16x32_bf16 v[8:11], a[232:235], v[136:139], v[8:11]\n" \
"v_exp_f32 v148, v148\n" \
"v_mfma_f32_16x16x32_bf16 v[8:11], a[200:203], v[128:131], v[8:11]\n" \
"v_mfma_f32_16x16x32_bf16 v[12:15], a[204:207], v[128:131], v[12:15]\n" \
"v_exp_f32 v149, v149\n" \
"v_mfma_f32_16x16x32_bf16 v[12:15], a[236:239], v[136:139], v[12:15]\n" \
"s_waitcnt lgkmcnt(4)\n" \
"v_cvt_pk_bf16_f32 v146, v148, v149\n" \
"v_mfma_f32_16x16x32_bf16 v[16:19], a[240:243], v[136:139], v[16:19]\n" \
"v_exp_f32 v150, v150\n" \
"v_mfma_f32_16x16x32_bf16 v[16:19], a[208:211], v[128:131], v[16:19]\n" \
"v_mfma_f32_16x16x32_bf16 v[20:23], a[212:215], v[128:131], v[20:23]\n" \
"v_exp_f32 v151, v151\n" \
"v_mfma_f32_16x16x32_bf16 v[20:23], a[244:247], v[136:139], v[20:23]\n" \
"s_waitcnt lgkmcnt(0)\n" \
"v_cvt_pk_bf16_f32 v147, v150, v151\n" \
"v_mfma_f32_16x16x32_bf16 v[24:27], a[248:251], v[136:139], v[24:27]\n" \
"v_mfma_f32_16x16x32_bf16 v[24:27], a[216:219], v[128:131], v[24:27]\n" \
"v_exp_f32 v152, v152\n" \
"v_mfma_f32_16x16x32_bf16 v[28:31], a[220:223], v[128:131], v[28:31]\n" \
"v_mfma_f32_16x16x32_bf16 v[28:31], a[252:255], v[136:139], v[28:31]\n" \
"v_exp_f32 v153, v153\n" \
"v_mfma_f32_16x16x32_bf16 v[128:131], a[128:131], v[160:163], 0\n" \
"v_cvt_pk_bf16_f32 v152, v152, v153\n" \
"v_mfma_f32_16x16x32_bf16 v[128:131], a[132:135], v[164:167], v[128:131]\n" \
"v_exp_f32 v154, v154\n" \
"v_mfma_f32_16x16x32_bf16 v[128:131], a[136:139], v[168:171], v[128:131]\n" \
"v_mfma_f32_16x16x32_bf16 v[128:131], a[140:143], v[172:175], v[128:131]\n" \
"v_exp_f32 v155, v155\n" \
"v_mfma_f32_16x16x32_bf16 v[132:135], a[156:159], v[172:175], 0\n" \
"v_cvt_pk_bf16_f32 v153, v154, v155\n" \
"v_mfma_f32_16x16x32_bf16 v[132:135], a[152:155], v[168:171], v[132:135]\n" \
"v_exp_f32 v156, v156\n" \
"v_mfma_f32_16x16x32_bf16 v[132:135], a[148:151], v[164:167], v[132:135]\n" \
"v_mfma_f32_16x16x32_bf16 v[132:135], a[144:147], v[160:163], v[132:135]\n" \
"v_exp_f32 v157, v157\n" \
"v_mfma_f32_16x16x32_bf16 v[136:139], a[160:163], v[160:163], 0\n" \
"v_cvt_pk_bf16_f32 v154, v156, v157\n" \
"v_mfma_f32_16x16x32_bf16 v[136:139], a[164:167], v[164:167], v[136:139]\n" \
"v_exp_f32 v158, v158\n" \
"v_mfma_f32_16x16x32_bf16 v[136:139], a[168:171], v[168:171], v[136:139]\n" \
"v_mfma_f32_16x16x32_bf16 v[136:139], a[172:175], v[172:175], v[136:139]\n" \
"v_exp_f32 v159, v159\n" \
"v_mfma_f32_16x16x32_bf16 v[140:143], a[188:191], v[172:175], 0\n" \
"v_cvt_pk_bf16_f32 v155, v158, v159\n" \
"v_mfma_f32_16x16x32_bf16 v[140:143], a[184:187], v[168:171], v[140:143]\n" \
"v_mfma_f32_16x16x32_bf16 v[140:143], a[180:183], v[164:167], v[140:143]\n" \
"v_exp_f32 v128, v128\n" \
"v_mfma_f32_16x16x32_bf16 v[140:143], a[176:179], v[160:163], v[140:143]\n" \
"v_mfma_f32_16x16x32_bf16 a[36:39], a[48:51], v[144:147], a[36:39]\n" \
"s_waitcnt vmcnt(8)\n" \
"v_exp_f32 v129, v129\n" \
"v_mfma_f32_16x16x32_bf16 a[36:39], a[48:51], v[152:155], a[36:39]\n" \
"buffer_store_dwordx4 a[60:63], v232, s[32:35], s39 offen sc1\n" \
"v_mfma_f32_16x16x32_bf16 v[32:35], a[224:227], v[152:155], v[32:35]\n" \
"v_exp_f32 v130, v130\n" \
"v_mfma_f32_16x16x32_bf16 v[32:35], a[192:195], v[144:147], v[32:35]\n" \
"v_cvt_pk_bf16_f32 v128, v128, v129\n" \
"v_mfma_f32_16x16x32_bf16 v[36:39], a[196:199], v[144:147], v[36:39]\n" \
"v_exp_f32 v131, v131\n" \
"v_mfma_f32_16x16x32_bf16 v[36:39], a[228:231], v[152:155], v[36:39]\n" \
"buffer_load_dwordx4 a[60:63], v230, s[40:43], s38 offen\n" \
"v_cvt_pk_bf16_f32 v129, v130, v131\n" \
"v_mfma_f32_16x16x32_bf16 v[40:43], a[232:235], v[152:155], v[40:43]\n" \
"s_add_u32 s38, s38, 512\n" \
"v_exp_f32 v132, v132\n" \
"v_mfma_f32_16x16x32_bf16 v[40:43], a[200:203], v[144:147], v[40:43]\n" \
"s_add_u32 s39, s39, 0x80000\n" \
"v_mfma_f32_16x16x32_bf16 v[44:47], a[204:207], v[144:147], v[44:47]\n" \
"v_exp_f32 v133, v133\n" \
"v_mfma_f32_16x16x32_bf16 v[44:47], a[236:239], v[152:155], v[44:47]\n" \
"v_cvt_pk_bf16_f32 v130, v132, v133\n" \
"v_mfma_f32_16x16x32_bf16 v[48:51], a[240:243], v[152:155], v[48:51]\n" \
"v_exp_f32 v134, v134\n" \
"v_mfma_f32_16x16x32_bf16 v[48:51], a[208:211], v[144:147], v[48:51]\n" \
"v_mfma_f32_16x16x32_bf16 v[52:55], a[212:215], v[144:147], v[52:55]\n" \
"v_exp_f32 v135, v135\n" \
"v_mfma_f32_16x16x32_bf16 v[52:55], a[244:247], v[152:155], v[52:55]\n" \
"v_cvt_pk_bf16_f32 v131, v134, v135\n" \
"v_mfma_f32_16x16x32_bf16 v[56:59], a[248:251], v[152:155], v[56:59]\n" \
"v_mfma_f32_16x16x32_bf16 v[56:59], a[216:219], v[144:147], v[56:59]\n" \
"v_exp_f32 v136, v136\n" \
"v_mfma_f32_16x16x32_bf16 v[60:63], a[220:223], v[144:147], v[60:63]\n" \
"v_mfma_f32_16x16x32_bf16 v[60:63], a[252:255], v[152:155], v[60:63]\n" \
"s_waitcnt vmcnt(18)\n" \
"v_exp_f32 v137, v137\n" \
"v_mfma_f32_16x16x32_bf16 v[144:147], a[128:131], v[176:179], 0\n" \
"s_barrier\n" \
"s_mov_b32 m0, s20\n" \
"v_cvt_pk_bf16_f32 v136, v136, v137\n" \
"v_mfma_f32_16x16x32_bf16 v[144:147], a[132:135], v[180:183], v[144:147]\n" \
"v_exp_f32 v138, v138\n" \
"v_mfma_f32_16x16x32_bf16 v[144:147], a[136:139], v[184:187], v[144:147]\n" \
"buffer_load_dwordx4 v248, s[32:35], s89 offen offset:0 sc1 lds\n" \
"v_mfma_f32_16x16x32_bf16 v[144:147], a[140:143], v[188:191], v[144:147]\n" \
"v_exp_f32 v139, v139\n" \
"v_mfma_f32_16x16x32_bf16 v[148:151], a[156:159], v[188:191], 0\n" \
"ds_read_b128 a[128:131], v240 offset:0\n" \
"v_cvt_pk_bf16_f32 v137, v138, v139\n" \
"v_mfma_f32_16x16x32_bf16 v[148:151], a[152:155], v[184:187], v[148:151]\n" \
"v_exp_f32 v140, v140\n" \
"v_mfma_f32_16x16x32_bf16 v[148:151], a[148:151], v[180:183], v[148:151]\n" \
"ds_read_b128 a[132:135], v240 offset:1024\n" \
"ds_read_b128 a[136:139], v240 offset:2048\n" \
"v_mfma_f32_16x16x32_bf16 v[148:151], a[144:147], v[176:179], v[148:151]\n" \
"v_exp_f32 v141, v141\n" \
"v_mfma_f32_16x16x32_bf16 v[152:155], a[160:163], v[176:179], 0\n" \
"buffer_load_dwordx4 v248, s[32:35], s89 offen offset:1024 sc1 lds\n" \
"v_mfma_f32_16x16x32_bf16 v[152:155], a[164:167], v[180:183], v[152:155]\n" \
"v_exp_f32 v142, v142\n" \
"v_mfma_f32_16x16x32_bf16 v[152:155], a[168:171], v[184:187], v[152:155]\n" \
".Lst0_11_%=:\n" \
"s_cbranch_execz .Lst0_15_%=\n" \
"ds_read_b128 a[140:143], v240 offset:3072\n" \
"v_cvt_pk_bf16_f32 v138, v140, v141\n" \
"v_mfma_f32_16x16x32_bf16 v[152:155], a[172:175], v[188:191], v[152:155]\n" \
"v_exp_f32 v143, v143\n" \
"v_mfma_f32_16x16x32_bf16 v[156:159], a[188:191], v[188:191], 0\n" \
"ds_read_b128 a[156:159], v240 offset:7168\n" \
"v_cvt_pk_bf16_f32 v139, v142, v143\n" \
"v_mfma_f32_16x16x32_bf16 v[156:159], a[184:187], v[184:187], v[156:159]\n" \
"ds_read_b128 a[152:155], v240 offset:6144\n" \
"ds_read_b128 a[148:151], v240 offset:5120\n" \
"v_mfma_f32_16x16x32_bf16 v[156:159], a[180:183], v[180:183], v[156:159]\n" \
"v_exp_f32 v144, v144\n" \
"v_mfma_f32_16x16x32_bf16 v[156:159], a[176:179], v[176:179], v[156:159]\n" \
"buffer_load_dwordx4 v248, s[32:35], s89 offen offset:2048 sc1 lds\n" \
"v_mfma_f32_16x16x32_bf16 a[40:43], a[48:51], v[128:131], a[40:43]\n" \
"v_exp_f32 v145, v145\n" \
"v_mfma_f32_16x16x32_bf16 a[40:43], a[48:51], v[136:139], a[40:43]\n" \
"ds_read_b128 a[144:147], v240 offset:4096\n" \
"v_cvt_pk_bf16_f32 v144, v144, v145\n" \
"v_mfma_f32_16x16x32_bf16 v[64:67], a[224:227], v[136:139], v[64:67]\n" \
"v_exp_f32 v146, v146\n" \
"v_mfma_f32_16x16x32_bf16 v[64:67], a[192:195], v[128:131], v[64:67]\n" \
"ds_read_b128 a[160:163], v240 offset:8192\n" \
"ds_read_b128 a[164:167], v240 offset:9216\n" \
"v_mfma_f32_16x16x32_bf16 v[68:71], a[196:199], v[128:131], v[68:71]\n" \
"v_exp_f32 v147, v147\n" \
"v_mfma_f32_16x16x32_bf16 v[68:71], a[228:231], v[136:139], v[68:71]\n" \
"buffer_load_dwordx4 v248, s[32:35], s89 offen offset:3072 sc1 lds\n" \
"v_mfma_f32_16x16x32_bf16 v[72:75], a[232:235], v[136:139], v[72:75]\n" \
"s_add_u32 s20, s20, 0x4000\n" \
"v_exp_f32 v148, v148\n" \
"v_mfma_f32_16x16x32_bf16 v[72:75], a[200:203], v[128:131], v[72:75]\n" \
"s_and_b32 s20, s20, 0xffff\n" \
"ds_read_b128 a[168:171], v240 offset:10240\n" \
"v_cvt_pk_bf16_f32 v145, v146, v147\n" \
"v_mfma_f32_16x16x32_bf16 v[76:79], a[204:207], v[128:131], v[76:79]\n" \
"s_bitset1_b32 s20, 16\n" \
"v_exp_f32 v149, v149\n" \
"v_mfma_f32_16x16x32_bf16 v[76:79], a[236:239], v[136:139], v[76:79]\n" \
"ds_read_b128 a[172:175], v240 offset:11264\n" \
"v_cvt_pk_bf16_f32 v146, v148, v149\n" \
"v_mfma_f32_16x16x32_bf16 v[80:83], a[240:243], v[136:139], v[80:83]\n" \
"v_exp_f32 v150, v150\n" \
"v_mfma_f32_16x16x32_bf16 v[80:83], a[208:211], v[128:131], v[80:83]\n" \
"v_mfma_f32_16x16x32_bf16 v[84:87], a[212:215], v[128:131], v[84:87]\n" \
"s_sub_u32 s95, s95, 1\n" \
"v_exp_f32 v151, v151\n" \
"v_mfma_f32_16x16x32_bf16 v[84:87], a[244:247], v[136:139], v[84:87]\n" \
"v_cvt_pk_bf16_f32 v147, v150, v151\n" \
"v_mfma_f32_16x16x32_bf16 v[88:91], a[248:251], v[136:139], v[88:91]\n" \
"v_mfma_f32_16x16x32_bf16 v[88:91], a[216:219], v[128:131], v[88:91]\n" \
"v_exp_f32 v152, v152\n" \
"v_mfma_f32_16x16x32_bf16 v[92:95], a[220:223], v[128:131], v[92:95]\n" \
"s_waitcnt lgkmcnt(8)\n" \
"v_exp_f32 v153, v153\n" \
"v_mfma_f32_16x16x32_bf16 v[92:95], a[252:255], v[136:139], v[92:95]\n" \
"s_cbranch_scc0 .Lloop_%=\n" \
"s_sub_u32 s95, s67, s84\n" \
"s_lshl_b32 s95, s95, 1\n" \
"s_sub_u32 s95, s95, 8\n" \
"s_mov_b32 s76, 1\n" \
".Lld_%=:\n" \
".Lst0_12_%=:\n" \
"s_cbranch_execz .Lstb0_%=\n" \
"s_sub_u32 s95, s95, 1\n" \
"s_cbranch_scc1 .Lhook_%=\n" \
".p2align 3\n" \
".Lloop1_%=:\n" \
  \
"v_mfma_f32_16x16x32_bf16 v[128:131], a[128:131], v[192:195], 0\n" \
"s_add_u32 s86, s86, 0x4000\n" \
"ds_read_b128 a[188:191], v240 offset:15360\n" \
"v_cvt_pk_bf16_f32 v152, v152, v153\n" \
"v_mfma_f32_16x16x32_bf16 v[128:131], a[132:135], v[196:199], v[128:131]\n" \
"s_bitset0_b32 s86, 16\n" \
"v_exp_f32 v154, v154\n" \
"v_mfma_f32_16x16x32_bf16 v[128:131], a[136:139], v[200:203], v[128:131]\n" \
"s_cmp_eq_u32 s88, s24\n" \
"s_cbranch_scc1 .Lchd_%=\n" \
".Lsbhd_%=:\n" \
"v_mfma_f32_16x16x32_bf16 v[128:131], a[140:143], v[204:207], v[128:131]\n" \
"s_waitcnt lgkmcnt(5)\n" \
"v_exp_f32 v155, v155\n" \
"v_mfma_f32_16x16x32_bf16 v[132:135], a[156:159], v[204:207], 0\n" \
"s_mov_b32 m0, s19\n" \
"v_add_u32 v242, 0x10000, v240\n" \
"v_cvt_pk_bf16_f32 v153, v154, v155\n" \
"v_mfma_f32_16x16x32_bf16 v[132:135], a[152:155], v[200:203], v[132:135]\n" \
"ds_read_b128 a[184:187], v240 offset:14336\n" \
"ds_read_b128 a[180:183], v240 offset:13312\n" \
"ds_read_b128 a[176:179], v240 offset:12288\n" \
"v_exp_f32 v156, v156\n" \
"v_mfma_f32_16x16x32_bf16 v[132:135], a[148:151], v[196:199], v[132:135]\n" \
"buffer_load_dwordx4 v248, s[32:35], s88 offen offset:0 sc1 lds\n" \
"v_mfma_f32_16x16x32_bf16 v[132:135], a[144:147], v[192:195], v[132:135]\n" \
"s_waitcnt lgkmcnt(4)\n" \
"v_exp_f32 v157, v157\n" \
"v_mfma_f32_16x16x32_bf16 v[136:139], a[160:163], v[192:195], 0\n" \
"v_cvt_pk_bf16_f32 v154, v156, v157\n" \
"v_mfma_f32_16x16x32_bf16 v[136:139], a[164:167], v[196:199], v[136:139]\n" \
"v_exp_f32 v158, v158\n" \
"v_mfma_f32_16x16x32_bf16 v[136:139], a[168:171], v[200:203], v[136:139]\n" \
"v_add_u32 v240, s86, v248\n" \
"v_mfma_f32_16x16x32_bf16 v[136:139], a[172:175], v[204:207], v[136:139]\n" \
"s_waitcnt lgkmcnt(0)\n" \
"v_exp_f32 v159, v159\n" \
"v_mfma_f32_16x16x32_bf16 v[140:143], a[188:191], v[204:207], 0\n" \
"v_cvt_pk_bf16_f32 v155, v158, v159\n" \
"v_mfma_f32_16x16x32_bf16 v[140:143], a[184:187], v[200:203], v[140:143]\n" \
"buffer_load_dwordx4 v248, s[32:35], s88 offen offset:1024 sc1 lds\n" \
"v_mfma_f32_16x16x32_bf16 v[140:143], a[180:183], v[196:199], v[140:143]\n" \
"v_exp_f32 v128, v128\n" \
"v_mfma_f32_16x16x32_bf16 v[140:143], a[176:179], v[192:195], v[140:143]\n" \
"v_mfma_f32_16x16x32_bf16 a[44:47], a[48:51], v[144:147], a[44:47]\n" \
"v_exp_f32 v129, v129\n" \
"v_mfma_f32_16x16x32_bf16 a[44:47], a[48:51], v[152:155], a[44:47]\n" \
"v_cvt_pk_bf16_f32 v128, v128, v129\n" \
"v_mfma_f32_16x16x32_bf16 v[96:99], a[224:227], v[152:155], v[96:99]\n" \
"v_exp_f32 v130, v130\n" \
"v_mfma_f32_16x16x32_bf16 v[96:99], a[192:195], v[144:147], v[96:99]\n" \
"ds_read_b128 a[224:227], v242 offset:8192\n" \
"v_mfma_f32_16x16x32_bf16 v[100:103], a[196:199], v[144:147], v[100:103]\n" \
"v_exp_f32 v131, v131\n" \
"v_mfma_f32_16x16x32_bf16 v[100:103], a[228:231], v[152:155], v[100:103]\n" \
"ds_read_b128 a[192:195], v242 offset:0\n" \
"v_cvt_pk_bf16_f32 v129, v130, v131\n" \
"v_mfma_f32_16x16x32_bf16 v[104:107], a[232:235], v[152:155], v[104:107]\n" \
"v_exp_f32 v132, v132\n" \
"v_mfma_f32_16x16x32_bf16 v[104:107], a[200:203], v[144:147], v[104:107]\n" \
"ds_read_b128 a[196:199], v242 offset:1024\n" \
"ds_read_b128 a[228:231], v242 offset:9216\n" \
"v_mfma_f32_16x16x32_bf16 v[108:111], a[204:207], v[144:147], v[108:111]\n" \
"v_exp_f32 v133, v133\n" \
"v_mfma_f32_16x16x32_bf16 v[108:111], a[236:239], v[152:155], v[108:111]\n" \
"ds_read_b128 a[232:235], v242 offset:10240\n" \
"v_cvt_pk_bf16_f32 v130, v132, v133\n" \
"v_mfma_f32_16x16x32_bf16 v[112:115], a[240:243], v[152:155], v[112:115]\n" \
"v_exp_f32 v134, v134\n" \
"v_mfma_f32_16x16x32_bf16 v[112:115], a[208:211], v[144:147], v[112:115]\n" \
"ds_read_b128 a[200:203], v242 offset:2048\n" \
"v_mfma_f32_16x16x32_bf16 v[116:119], a[212:215], v[144:147], v[116:119]\n" \
"v_exp_f32 v135, v135\n" \
"v_mfma_f32_16x16x32_bf16 v[116:119], a[244:247], v[152:155], v[116:119]\n" \
"ds_read_b128 a[204:207], v242 offset:3072\n" \
"v_cvt_pk_bf16_f32 v131, v134, v135\n" \
"v_mfma_f32_16x16x32_bf16 v[120:123], a[248:251], v[152:155], v[120:123]\n" \
"ds_read_b128 a[236:239], v242 offset:11264\n" \
"v_mfma_f32_16x16x32_bf16 v[120:123], a[216:219], v[144:147], v[120:123]\n" \
"v_exp_f32 v136, v136\n" \
"v_mfma_f32_16x16x32_bf16 v[124:127], a[220:223], v[144:147], v[124:127]\n" \
"ds_read_b128 a[240:243], v242 offset:12288\n" \
"v_mfma_f32_16x16x32_bf16 v[124:127], a[252:255], v[152:155], v[124:127]\n" \
"v_exp_f32 v137, v137\n" \
"v_mfma_f32_16x16x32_bf16 v[144:147], a[128:131], v[208:211], 0\n" \
"ds_read_b128 a[208:211], v242 offset:4096\n" \
"v_cvt_pk_bf16_f32 v136, v136, v137\n" \
"v_mfma_f32_16x16x32_bf16 v[144:147], a[132:135], v[212:215], v[144:147]\n" \
"v_exp_f32 v138, v138\n" \
"v_mfma_f32_16x16x32_bf16 v[144:147], a[136:139], v[216:219], v[144:147]\n" \
"ds_read_b128 a[212:215], v242 offset:5120\n" \
"ds_read_b128 a[244:247], v242 offset:13312\n" \
"v_mfma_f32_16x16x32_bf16 v[144:147], a[140:143], v[220:223], v[144:147]\n" \
"v_exp_f32 v139, v139\n" \
"v_mfma_f32_16x16x32_bf16 v[148:151], a[156:159], v[220:223], 0\n" \
"ds_read_b128 a[248:251], v242 offset:14336\n" \
"v_cvt_pk_bf16_f32 v137, v138, v139\n" \
"v_mfma_f32_16x16x32_bf16 v[148:151], a[152:155], v[216:219], v[148:151]\n" \
"v_exp_f32 v140, v140\n" \
"v_mfma_f32_16x16x32_bf16 v[148:151], a[148:151], v[212:215], v[148:151]\n" \
"ds_read_b128 a[216:219], v242 offset:6144\n" \
"v_mfma_f32_16x16x32_bf16 v[148:151], a[144:147], v[208:211], v[148:151]\n" \
"v_exp_f32 v141, v141\n" \
"v_mfma_f32_16x16x32_bf16 v[152:155], a[160:163], v[208:211], 0\n" \
"ds_read_b128 a[220:223], v242 offset:7168\n" \
"v_cvt_pk_bf16_f32 v138, v140, v141\n" \
"v_mfma_f32_16x16x32_bf16 v[152:155], a[164:167], v[212:215], v[152:155]\n" \
"v_exp_f32 v142, v142\n" \
"v_mfma_f32_16x16x32_bf16 v[152:155], a[168:171], v[216:219], v[152:155]\n" \
"ds_read_b128 a[252:255], v242 offset:15360\n" \
"v_mfma_f32_16x16x32_bf16 v[152:155], a[172:175], v[220:223], v[152:155]\n" \
"v_exp_f32 v143, v143\n" \
"v_mfma_f32_16x16x32_bf16 v[156:159], a[188:191], v[220:223], 0\n" \
"v_cvt_pk_bf16_f32 v139, v142, v143\n" \
"v_mfma_f32_16x16x32_bf16 v[156:159], a[184:187], v[216:219], v[156:159]\n" \
"buffer_load_dwordx4 v248, s[32:35], s88 offen offset:2048 sc1 lds\n" \
"v_mfma_f32_16x16x32_bf16 v[156:159], a[180:183], v[212:215], v[156:159]\n" \
"v_exp_f32 v144, v144\n" \
"v_mfma_f32_16x16x32_bf16 v[156:159], a[176:179], v[208:211], v[156:159]\n" \
"v_mfma_f32_16x16x32_bf16 a[32:35], a[48:51], v[128:131], a[32:35]\n" \
"v_exp_f32 v145, v145\n" \
"v_mfma_f32_16x16x32_bf16 a[32:35], a[48:51], v[136:139], a[32:35]\n" \
"s_waitcnt lgkmcnt(12)\n" \
"v_cvt_pk_bf16_f32 v144, v144, v145\n" \
"v_mfma_f32_16x16x32_bf16 v[0:3], a[224:227], v[136:139], v[0:3]\n" \
"v_exp_f32 v146, v146\n" \
"v_mfma_f32_16x16x32_bf16 v[0:3], a[192:195], v[128:131], v[0:3]\n" \
"v_mfma_f32_16x16x32_bf16 v[4:7], a[196:199], v[128:131], v[4:7]\n" \
"v_exp_f32 v147, v147\n" \
"v_mfma_f32_16x16x32_bf16 v[4:7], a[228:231], v[136:139], v[4:7]\n" \
"s_waitcnt lgkmcnt(8)\n" \
"v_cvt_pk_bf16_f32 v145, v146, v147\n" \
"v_mfma_f32_16x16x32_bf16 v[8:11], a[232:235], v[136:139], v[8:11]\n" \
"v_exp_f32 v148, v148\n" \
"v_mfma_f32_16x16x32_bf16 v[8:11], a[200:203], v[128:131], v[8:11]\n" \
"v_mfma_f32_16x16x32_bf16 v[12:15], a[204:207], v[128:131], v[12:15]\n" \
"v_exp_f32 v149, v149\n" \
"v_mfma_f32_16x16x32_bf16 v[12:15], a[236:239], v[136:139], v[12:15]\n" \
"s_waitcnt lgkmcnt(4)\n" \
"v_cvt_pk_bf16_f32 v146, v148, v149\n" \
"v_mfma_f32_16x16x32_bf16 v[16:19], a[240:243], v[136:139], v[16:19]\n" \
"v_exp_f32 v150, v150\n" \
"v_mfma_f32_16x16x32_bf16 v[16:19], a[208:211], v[128:131], v[16:19]\n" \
"v_mfma_f32_16x16x32_bf16 v[20:23], a[212:215], v[128:131], v[20:23]\n" \
"v_exp_f32 v151, v151\n" \
"v_mfma_f32_16x16x32_bf16 v[20:23], a[244:247], v[136:139], v[20:23]\n" \
"s_waitcnt lgkmcnt(0)\n" \
"v_cvt_pk_bf16_f32 v147, v150, v151\n" \
"v_mfma_f32_16x16x32_bf16 v[24:27], a[248:251], v[136:139], v[24:27]\n" \
"buffer_load_dwordx4 v248, s[32:35], s88 offen offset:3072 sc1 lds\n" \
"v_mfma_f32_16x16x32_bf16 v[24:27], a[216:219], v[128:131], v[24:27]\n" \
"s_add_u32 s89, s88, 589824\n" \
"v_exp_f32 v152, v152\n" \
"v_mfma_f32_16x16x32_bf16 v[28:31], a[220:223], v[128:131], v[28:31]\n" \
"s_add_u32 s88, s88, s27\n" \
"s_add_u32 s19, s19, 0x4000\n" \
"s_bitset0_b32 s19, 16\n" \
"v_mfma_f32_16x16x32_bf16 v[28:31], a[252:255], v[136:139], v[28:31]\n" \
"s_cmp_eq_u32 s88, s16\n" \
"v_exp_f32 v153, v153\n" \
"v_mfma_f32_16x16x32_bf16 v[128:131], a[128:131], v[160:163], 0\n" \
"s_cmov_b32 s88, s21\n" \
"s_cmov_b32 s27, s91\n" \
"v_cvt_pk_bf16_f32 v152, v152, v153\n" \
"v_mfma_f32_16x16x32_bf16 v[128:131], a[132:135], v[164:167], v[128:131]\n" \
"s_cmov_b32 s16, s77\n" \
"v_exp_f32 v154, v154\n" \
"v_mfma_f32_16x16x32_bf16 v[128:131], a[136:139], v[168:171], v[128:131]\n" \
"s_cmov_b32 s21, s92\n" \
"s_cmovk_i32 s91, 0x4000\n" \
"s_cmp_lg_u32 s76, 0\n" \
"v_mfma_f32_16x16x32_bf16 v[128:131], a[140:143], v[172:175], v[128:131]\n" \
"v_exp_f32 v155, v155\n" \
"v_mfma_f32_16x16x32_bf16 v[132:135], a[156:159], v[172:175], 0\n" \
"s_cbranch_scc1 .Land_%=\n" \
".Labd_%=:\n" \
"v_cvt_pk_bf16_f32 v153, v154, v155\n" \
"v_mfma_f32_16x16x32_bf16 v[132:135], a[152:155], v[168:171], v[132:135]\n" \
"v_exp_f32 v156, v156\n" \
"v_mfma_f32_16x16x32_bf16 v[132:135], a[148:151], v[164:167], v[132:135]\n" \
"v_mfma_f32_16x16x32_bf16 v[132:135], a[144:147], v[160:163], v[132:135]\n" \
"v_exp_f32 v157, v157\n" \
"v_mfma_f32_16x16x32_bf16 v[136:139], a[160:163], v[160:163], 0\n" \
"v_cvt_pk_bf16_f32 v154, v156, v157\n" \
"v_mfma_f32_16x16x32_bf16 v[136:139], a[164:167], v[164:167], v[136:139]\n" \
"v_exp_f32 v158, v158\n" \
"v_mfma_f32_16x16x32_bf16 v[136:139], a[168:171], v[168:171], v[136:139]\n" \
"v_mfma_f32_16x16x32_bf16 v[136:139], a[172:175], v[172:175], v[136:139]\n" \
"v_exp_f32 v159, v159\n" \
"v_mfma_f32_16x16x32_bf16 v[140:143], a[188:191], v[172:175], 0\n" \
"v_cvt_pk_bf16_f32 v155, v158, v159\n" \
"v_mfma_f32_16x16x32_bf16 v[140:143], a[184:187], v[168:171], v[140:143]\n" \
"v_mfma_f32_16x16x32_bf16 v[140:143], a[180:183], v[164:167], v[140:143]\n" \
"v_exp_f32 v128, v128\n" \
"v_mfma_f32_16x16x32_bf16 v[140:143], a[176:179], v[160:163], v[140:143]\n" \
"v_mfma_f32_16x16x32_bf16 a[36:39], a[48:51], v[144:147], a[36:39]\n" \
"v_exp_f32 v129, v129\n" \
"v_mfma_f32_16x16x32_bf16 a[36:39], a[48:51], v[152:155], a[36:39]\n" \
"v_cvt_pk_bf16_f32 v128, v128, v129\n" \
"v_mfma_f32_16x16x32_bf16 v[32:35], a[224:227], v[152:155], v[32:35]\n" \
"v_exp_f32 v130, v130\n" \
"v_mfma_f32_16x16x32_bf16 v[32:35], a[192:195], v[144:147], v[32:35]\n" \
"v_mfma_f32_16x16x32_bf16 v[36:39], a[196:199], v[144:147], v[36:39]\n" \
"v_exp_f32 v131, v131\n" \
"v_mfma_f32_16x16x32_bf16 v[36:39], a[228:231], v[152:155], v[36:39]\n" \
"v_cvt_pk_bf16_f32 v129, v130, v131\n" \
"v_mfma_f32_16x16x32_bf16 v[40:43], a[232:235], v[152:155], v[40:43]\n" \
"v_exp_f32 v132, v132\n" \
"v_mfma_f32_16x16x32_bf16 v[40:43], a[200:203], v[144:147], v[40:43]\n" \
"v_mfma_f32_16x16x32_bf16 v[44:47], a[204:207], v[144:147], v[44:47]\n" \
"v_exp_f32 v133, v133\n" \
"v_mfma_f32_16x16x32_bf16 v[44:47], a[236:239], v[152:155], v[44:47]\n" \
"v_cvt_pk_bf16_f32 v130, v132, v133\n" \
"v_mfma_f32_16x16x32_bf16 v[48:51], a[240:243], v[152:155], v[48:51]\n" \
"v_exp_f32 v134, v134\n" \
"v_mfma_f32_16x16x32_bf16 v[48:51], a[208:211], v[144:147], v[48:51]\n" \
"v_mfma_f32_16x16x32_bf16 v[52:55], a[212:215], v[144:147], v[52:55]\n" \
"v_exp_f32 v135, v135\n" \
"v_mfma_f32_16x16x32_bf16 v[52:55], a[244:247], v[152:155], v[52:55]\n" \
"v_cvt_pk_bf16_f32 v131, v134, v135\n" \
"v_mfma_f32_16x16x32_bf16 v[56:59], a[248:251], v[152:155], v[56:59]\n" \
"v_mfma_f32_16x16x32_bf16 v[56:59], a[216:219], v[144:147], v[56:59]\n" \
"v_exp_f32 v136, v136\n" \
"v_mfma_f32_16x16x32_bf16 v[60:63], a[220:223], v[144:147], v[60:63]\n" \
"v_mfma_f32_16x16x32_bf16 v[60:63], a[252:255], v[152:155], v[60:63]\n" \
"s_waitcnt vmcnt(12)\n" \
"v_exp_f32 v137, v137\n" \
"v_mfma_f32_16x16x32_bf16 v[144:147], a[128:131], v[176:179], 0\n" \
"s_barrier\n" \
"s_mov_b32 m0, s20\n" \
"v_cvt_pk_bf16_f32 v136, v136, v137\n" \
"v_mfma_f32_16x16x32_bf16 v[144:147], a[132:135], v[180:183], v[144:147]\n" \
"v_exp_f32 v138, v138\n" \
"v_mfma_f32_16x16x32_bf16 v[144:147], a[136:139], v[184:187], v[144:147]\n" \
"buffer_load_dwordx4 v248, s[32:35], s89 offen offset:0 sc1 lds\n" \
"v_mfma_f32_16x16x32_bf16 v[144:147], a[140:143], v[188:191], v[144:147]\n" \
"v_exp_f32 v139, v139\n" \
"v_mfma_f32_16x16x32_bf16 v[148:151], a[156:159], v[188:191], 0\n" \
"ds_read_b128 a[128:131], v240 offset:0\n" \
"v_cvt_pk_bf16_f32 v137, v138, v139\n" \
"v_mfma_f32_16x16x32_bf16 v[148:151], a[152:155], v[184:187], v[148:151]\n" \
"v_exp_f32 v140, v140\n" \
"v_mfma_f32_16x16x32_bf16 v[148:151], a[148:151], v[180:183], v[148:151]\n" \
"ds_read_b128 a[132:135], v240 offset:1024\n" \
"ds_read_b128 a[136:139], v240 offset:2048\n" \
"v_mfma_f32_16x16x32_bf16 v[148:151], a[144:147], v[176:179], v[148:151]\n" \
"v_exp_f32 v141, v141\n" \
"v_mfma_f32_16x16x32_bf16 v[152:155], a[160:163], v[176:179], 0\n" \
"buffer_load_dwordx4 v248, s[32:35], s89 offen offset:1024 sc1 lds\n" \
"v_mfma_f32_16x16x32_bf16 v[152:155], a[164:167], v[180:183], v[152:155]\n" \
"v_exp_f32 v142, v142\n" \
"v_mfma_f32_16x16x32_bf16 v[152:155], a[168:171], v[184:187], v[152:155]\n" \
"ds_read_b128 a[140:143], v240 offset:3072\n" \
"v_cvt_pk_bf16_f32 v138, v140, v141\n" \
"v_mfma_f32_16x16x32_bf16 v[152:155], a[172:175], v[188:191], v[152:155]\n" \
"v_exp_f32 v143, v143\n" \
"v_mfma_f32_16x16x32_bf16 v[156:159], a[188:191], v[188:191], 0\n" \
"ds_read_b128 a[156:159], v240 offset:7168\n" \
"v_cvt_pk_bf16_f32 v139, v142, v143\n" \
"v_mfma_f32_16x16x32_bf16 v[156:159], a[184:187], v[184:187], v[156:159]\n" \
"ds_read_b128 a[152:155], v240 offset:6144\n" \
"ds_read_b128 a[148:151], v240 offset:5120\n" \
"v_mfma_f32_16x16x32_bf16 v[156:159], a[180:183], v[180:183], v[156:159]\n" \
"v_exp_f32 v144, v144\n" \
"v_mfma_f32_16x16x32_bf16 v[156:159], a[176:179], v[176:179], v[156:159]\n" \
"buffer_load_dwordx4 v248, s[32:35], s89 offen offset:2048 sc1 lds\n" \
"v_mfma_f32_16x16x32_bf16 a[40:43], a[48:51], v[128:131], a[40:43]\n" \
"v_exp_f32 v145, v145\n" \
"v_mfma_f32_16x16x32_bf16 a[40:43], a[48:51], v[136:139], a[40:43]\n" \
"ds_read_b128 a[144:147], v240 offset:4096\n" \
"v_cvt_pk_bf16_f32 v144, v144, v145\n" \
"v_mfma_f32_16x16x32_bf16 v[64:67], a[224:227], v[136:139], v[64:67]\n" \
"v_exp_f32 v146, v146\n" \
"v_mfma_f32_16x16x32_bf16 v[64:67], a[192:195], v[128:131], v[64:67]\n" \
"ds_read_b128 a[160:163], v240 offset:8192\n" \
"ds_read_b128 a[164:167], v240 offset:9216\n" \
"v_mfma_f32_16x16x32_bf16 v[68:71], a[196:199], v[128:131], v[68:71]\n" \
"v_exp_f32 v147, v147\n" \
"v_mfma_f32_16x16x32_bf16 v[68:71], a[228:231], v[136:139], v[68:71]\n" \
"buffer_load_dwordx4 v248, s[32:35], s89 offen offset:3072 sc1 lds\n" \
"v_mfma_f32_16x16x32_bf16 v[72:75], a[232:235], v[136:139], v[72:75]\n" \
"s_add_u32 s20, s20, 0x4000\n" \
"v_exp_f32 v148, v148\n" \
"v_mfma_f32_16x16x32_bf16 v[72:75], a[200:203], v[128:131], v[72:75]\n" \
"s_and_b32 s20, s20, 0xffff\n" \
"ds_read_b128 a[168:171], v240 offset:10240\n" \
"v_cvt_pk_bf16_f32 v145, v146, v147\n" \
"v_mfma_f32_16x16x32_bf16 v[76:79], a[204:207], v[128:131], v[76:79]\n" \
"s_bitset1_b32 s20, 16\n" \
"v_exp_f32 v149, v149\n" \
"v_mfma_f32_16x16x32_bf16 v[76:79], a[236:239], v[136:139], v[76:79]\n" \
"ds_read_b128 a[172:175], v240 offset:11264\n" \
"v_cvt_pk_bf16_f32 v146, v148, v149\n" \
"v_mfma_f32_16x16x32_bf16 v[80:83], a[240:243], v[136:139], v[80:83]\n" \
"v_exp_f32 v150, v150\n" \
"v_mfma_f32_16x16x32_bf16 v[80:83], a[208:211], v[128:131], v[80:83]\n" \
"v_mfma_f32_16x16x32_bf16 v[84:87], a[212:215], v[128:131], v[84:87]\n" \
"s_sub_u32 s95, s95, 1\n" \
"v_exp_f32 v151, v151\n" \
"v_mfma_f32_16x16x32_bf16 v[84:87], a[244:247], v[136:139], v[84:87]\n" \
"v_cvt_pk_bf16_f32 v147, v150, v151\n" \
"v_mfma_f32_16x16x32_bf16 v[88:91], a[248:251], v[136:139], v[88:91]\n" \
"v_mfma_f32_16x16x32_bf16 v[88:91], a[216:219], v[128:131], v[88:91]\n" \
"v_exp_f32 v152, v152\n" \
"v_mfma_f32_16x16x32_bf16 v[92:95], a[220:223], v[128:131], v[92:95]\n" \
"s_waitcnt lgkmcnt(8)\n" \
"v_exp_f32 v153, v153\n" \
"v_mfma_f32_16x16x32_bf16 v[92:95], a[252:255], v[136:139], v[92:95]\n" \
"s_cbranch_scc0 .Lloop1_%=\n" \
".Lhook_%=:\n" \
".Lst0_13_%=:\n" \
"s_cbranch_execz .Lstb0_%=\n" \
  \
"s_load_dwordx4 s[52:55], %[ka], 192\n" \
"s_load_dwordx2 s[96:97], %[ka], 208\n" \
"v_accvgpr_read_b32 v252, a32\n" \
"v_accvgpr_read_b32 v254, a36\n" \
"v_min_f32 v253, v252, v254\n" \
"v_max_f32 v255, v252, v254\n" \
"v_cmp_nle_f32 s[98:99], v246, v253\n" \
"v_cmp_nle_f32 vcc, v255, v250\n" \
"v_cmp_u_f32 s[14:15], v252, v254\n" \
"s_nop 1\n" \
"s_or_b64 vcc, vcc, s[98:99]\n" \
"s_or_b64 vcc, vcc, s[14:15]\n" \
"s_cbranch_vccz .Loka_%=\n" \
"s_call_b64 s[98:99], .Lredo0_%=\n" \
"v_accvgpr_read_b32 v252, a32\n" \
"v_accvgpr_read_b32 v254, a36\n" \
".Loka_%=:\n" \
"s_add_u32 s23, s30, 4096\n" \
"v_rcp_f32 v252, v252\n" \
"v_rcp_f32 v254, v254\n" \
"s_nop 0\n" \
"v_mov_b32 v253, v252\n" \
"v_mov_b32 v255, v254\n" \
"v_pk_mul_f32 v[0:1], v[0:1], v[252:253]\n" \
"v_pk_mul_f32 v[2:3], v[2:3], v[252:253]\n" \
"v_pk_mul_f32 v[4:5], v[4:5], v[252:253]\n" \
"v_pk_mul_f32 v[6:7], v[6:7], v[252:253]\n" \
"v_cvt_pk_bf16_f32 v0, v0, v1\n" \
"v_cvt_pk_bf16_f32 v1, v2, v3\n" \
"v_cvt_pk_bf16_f32 v2, v4, v5\n" \
"v_cvt_pk_bf16_f32 v3, v6, v7\n" \
"buffer_store_dwordx4 v[0:3], v248, s[32:35], s30 offen offset:0 sc1\n" \
"v_pk_mul_f32 v[32:33], v[32:33], v[254:255]\n" \
"v_pk_mul_f32 v[34:35], v[34:35], v[254:255]\n" \
"v_pk_mul_f32 v[36:37], v[36:37], v[254:255]\n" \
"v_pk_mul_f32 v[38:39], v[38:39], v[254:255]\n" \
"v_cvt_pk_bf16_f32 v4, v32, v33\n" \
"v_cvt_pk_bf16_f32 v5, v34, v35\n" \
"v_cvt_pk_bf16_f32 v6, v36, v37\n" \
"v_cvt_pk_bf16_f32 v7, v38, v39\n" \
"buffer_store_dwordx4 v[4:7], v248, s[32:35], s30 offen offset:1024 sc1\n" \
"v_pk_mul_f32 v[8:9], v[8:9], v[252:253]\n" \
"v_pk_mul_f32 v[10:11], v[10:11], v[252:253]\n" \
"v_pk_mul_f32 v[12:13], v[12:13], v[252:253]\n" \
"v_pk_mul_f32 v[14:15], v[14:15], v[252:253]\n" \
"v_cvt_pk_bf16_f32 v8, v8, v9\n" \
"v_cvt_pk_bf16_f32 v9, v10, v11\n" \
"v_cvt_pk_bf16_f32 v10, v12, v13\n" \
"v_cvt_pk_bf16_f32 v11, v14, v15\n" \
"buffer_store_dwordx4 v[8:11], v248, s[32:35], s30 offen offset:2048 sc1\n" \
"v_pk_mul_f32 v[40:41], v[40:41], v[254:255]\n" \
"v_pk_mul_f32 v[42:43], v[42:43], v[254:255]\n" \
"v_pk_mul_f32 v[44:45], v[44:45], v[254:255]\n" \
"v_pk_mul_f32 v[46:47], v[46:47], v[254:255]\n" \
"v_cvt_pk_bf16_f32 v12, v40, v41\n" \
"v_cvt_pk_bf16_f32 v13, v42, v43\n" \
"v_cvt_pk_bf16_f32 v14, v44, v45\n" \
"v_cvt_pk_bf16_f32 v15, v46, v47\n" \
"buffer_store_dwordx4 v[12:15], v248, s[32:35], s30 offen offset:3072 sc1\n" \
"s_mov_b64 exec, 0\n" \
"s_cmp_eq_u32 %[wid], 0\n" \
"s_cbranch_scc1 .Lst1_0_%=\n" \
"s_cmp_eq_u32 %[wid], 1\n" \
"s_cbranch_scc1 .Lst1_1_%=\n" \
"s_cmp_eq_u32 %[wid], 2\n" \
"s_cbranch_scc1 .Lst1_2_%=\n" \
"s_branch .Lst1_3_%=\n" \
".Lstb1_%=:\n" \
"s_mov_b64 exec, -1\n" \
"v_pk_mul_f32 v[16:17], v[16:17], v[252:253]\n" \
"v_pk_mul_f32 v[18:19], v[18:19], v[252:253]\n" \
"v_pk_mul_f32 v[20:21], v[20:21], v[252:253]\n" \
"v_pk_mul_f32 v[22:23], v[22:23], v[252:253]\n" \
"v_cvt_pk_bf16_f32 v16, v16, v17\n" \
"v_cvt_pk_bf16_f32 v17, v18, v19\n" \
"v_cvt_pk_bf16_f32 v18, v20, v21\n" \
"v_cvt_pk_bf16_f32 v19, v22, v23\n" \
"buffer_store_dwordx4 v[16:19], v248, s[32:35], s23 offen offset:0 sc1\n" \
"v_pk_mul_f32 v[48:49], v[48:49], v[254:255]\n" \
"v_pk_mul_f32 v[50:51], v[50:51], v[254:255]\n" \
"v_pk_mul_f32 v[52:53], v[52:53], v[254:255]\n" \
"v_pk_mul_f32 v[54:55], v[54:55], v[254:255]\n" \
"v_cvt_pk_bf16_f32 v20, v48, v49\n" \
"v_cvt_pk_bf16_f32 v21, v50, v51\n" \
"v_cvt_pk_bf16_f32 v22, v52, v53\n" \
"v_cvt_pk_bf16_f32 v23, v54, v55\n" \
"buffer_store_dwordx4 v[20:23], v248, s[32:35], s23 offen offset:1024 sc1\n" \
"v_pk_mul_f32 v[24:25], v[24:25], v[252:253]\n" \
"v_pk_mul_f32 v[26:27], v[26:27], v[252:253]\n" \
"v_pk_mul_f32 v[28:29], v[28:29], v[252:253]\n" \
"v_pk_mul_f32 v[30:31], v[30:31], v[252:253]\n" \
"v_cvt_pk_bf16_f32 v24, v24, v25\n" \
"v_cvt_pk_bf16_f32 v25, v26, v27\n" \
"v_cvt_pk_bf16_f32 v26, v28, v29\n" \
"v_cvt_pk_bf16_f32 v27, v30, v31\n" \
"buffer_store_dwordx4 v[24:27], v248, s[32:35], s23 offen offset:2048 sc1\n" \
"v_pk_mul_f32 v[56:57], v[56:57], v[254:255]\n" \
"v_pk_mul_f32 v[58:59], v[58:59], v[254:255]\n" \
"v_pk_mul_f32 v[60:61], v[60:61], v[254:255]\n" \
"v_pk_mul_f32 v[62:63], v[62:63], v[254:255]\n" \
"v_cvt_pk_bf16_f32 v28, v56, v57\n" \
"v_cvt_pk_bf16_f32 v29, v58, v59\n" \
"v_cvt_pk_bf16_f32 v30, v60, v61\n" \
"v_cvt_pk_bf16_f32 v31, v62, v63\n" \
"buffer_store_dwordx4 v[28:31], v248, s[32:35], s23 offen offset:3072 sc1\n" \
"s_lshl2_add_u32 s25, s66, s31\n" \
"s_mov_b32 s76, 1\n" \
"s_lshl_b32 s95, s67, 1\n" \
"s_cmp_lt_u32 s67, 4\n" \
"s_cselect_b32 s22, 7, 11\n" \
"s_sub_u32 s95, s22, s95\n" \
"s_max_i32 s95, s95, 0\n" \
"s_waitcnt lgkmcnt(0)\n" \
".p2align 3\n" \
".Lloop2_%=:\n" \
  \
"v_mfma_f32_16x16x32_bf16 v[128:131], a[128:131], v[160:163], 0\n" \
"s_add_u32 s86, s86, 0x4000\n" \
"ds_read_b128 a[188:191], v240 offset:15360\n" \
"v_cvt_pk_bf16_f32 v152, v152, v153\n" \
"v_mfma_f32_16x16x32_bf16 v[128:131], a[132:135], v[164:167], v[128:131]\n" \
"s_bitset0_b32 s86, 16\n" \
"v_exp_f32 v154, v154\n" \
"v_mfma_f32_16x16x32_bf16 v[128:131], a[136:139], v[168:171], v[128:131]\n" \
"ds_read_b128 a[184:187], v240 offset:14336\n" \
"v_add_u32 v242, 0x10000, v240\n" \
"v_mfma_f32_16x16x32_bf16 v[128:131], a[140:143], v[172:175], v[128:131]\n" \
"s_waitcnt lgkmcnt(6)\n" \
"v_exp_f32 v155, v155\n" \
"v_mfma_f32_16x16x32_bf16 v[132:135], a[156:159], v[172:175], 0\n" \
"s_mov_b32 m0, s19\n" \
"ds_read_b128 a[180:183], v240 offset:13312\n" \
"v_cvt_pk_bf16_f32 v153, v154, v155\n" \
"v_mfma_f32_16x16x32_bf16 v[132:135], a[152:155], v[168:171], v[132:135]\n" \
"ds_read_b128 a[176:179], v240 offset:12288\n" \
"v_exp_f32 v156, v156\n" \
"v_mfma_f32_16x16x32_bf16 v[132:135], a[148:151], v[164:167], v[132:135]\n" \
"buffer_load_dwordx4 v248, s[32:35], s88 offen offset:0 sc1 lds\n" \
"v_mfma_f32_16x16x32_bf16 v[132:135], a[144:147], v[160:163], v[132:135]\n" \
"s_waitcnt lgkmcnt(4)\n" \
"v_exp_f32 v157, v157\n" \
"v_mfma_f32_16x16x32_bf16 v[136:139], a[160:163], v[160:163], 0\n" \
"v_cvt_pk_bf16_f32 v154, v156, v157\n" \
"v_mfma_f32_16x16x32_bf16 v[136:139], a[164:167], v[164:167], v[136:139]\n" \
"v_exp_f32 v158, v158\n" \
"v_mfma_f32_16x16x32_bf16 v[136:139], a[168:171], v[168:171], v[136:139]\n" \
"buffer_load_dwordx4 v248, s[32:35], s88 offen offset:1024 sc1 lds\n" \
"v_mfma_f32_16x16x32_bf16 v[136:139], a[172:175], v[172:175], v[136:139]\n" \
"s_waitcnt lgkmcnt(0)\n" \
"v_exp_f32 v159, v159\n" \
"v_mfma_f32_16x16x32_bf16 v[140:143], a[188:191], v[172:175], 0\n" \
"v_add_u32 v240, s86, v248\n" \
"v_cvt_pk_bf16_f32 v155, v158, v159\n" \
"v_mfma_f32_16x16x32_bf16 v[140:143], a[184:187], v[168:171], v[140:143]\n" \
"buffer_load_dwordx4 v248, s[32:35], s88 offen offset:2048 sc1 lds\n" \
"v_mfma_f32_16x16x32_bf16 v[140:143], a[180:183], v[164:167], v[140:143]\n" \
"v_exp_f32 v128, v128\n" \
"v_mfma_f32_16x16x32_bf16 v[140:143], a[176:179], v[160:163], v[140:143]\n" \
"buffer_load_dwordx4 v248, s[32:35], s88 offen offset:3072 sc1 lds\n" \
"v_mfma_f32_16x16x32_bf16 a[44:47], a[48:51], v[144:147], a[44:47]\n" \
"s_add_u32 s89, s88, 589824\n" \
"v_exp_f32 v129, v129\n" \
"v_mfma_f32_16x16x32_bf16 a[44:47], a[48:51], v[152:155], a[44:47]\n" \
"s_add_u32 s88, s88, s27\n" \
"s_add_u32 s19, s19, 0x4000\n" \
"v_cvt_pk_bf16_f32 v128, v128, v129\n" \
"v_mfma_f32_16x16x32_bf16 v[96:99], a[224:227], v[152:155], v[96:99]\n" \
"s_bitset0_b32 s19, 16\n" \
"v_exp_f32 v130, v130\n" \
"v_mfma_f32_16x16x32_bf16 v[96:99], a[192:195], v[144:147], v[96:99]\n" \
"s_cmp_eq_u32 s88, s16\n" \
"s_cmov_b32 s88, s21\n" \
"ds_read_b128 a[224:227], v242 offset:8192\n" \
"v_mfma_f32_16x16x32_bf16 v[100:103], a[196:199], v[144:147], v[100:103]\n" \
"s_cmov_b32 s27, s91\n" \
"v_exp_f32 v131, v131\n" \
"v_mfma_f32_16x16x32_bf16 v[100:103], a[228:231], v[152:155], v[100:103]\n" \
"s_cmov_b32 s16, s77\n" \
"ds_read_b128 a[192:195], v242 offset:0\n" \
"v_cvt_pk_bf16_f32 v129, v130, v131\n" \
"v_mfma_f32_16x16x32_bf16 v[104:107], a[232:235], v[152:155], v[104:107]\n" \
"s_cmov_b32 s21, s92\n" \
"v_exp_f32 v132, v132\n" \
"v_mfma_f32_16x16x32_bf16 v[104:107], a[200:203], v[144:147], v[104:107]\n" \
"s_cmovk_i32 s91, 0x4000\n" \
"ds_read_b128 a[196:199], v242 offset:1024\n" \
"ds_read_b128 a[228:231], v242 offset:9216\n" \
"v_mfma_f32_16x16x32_bf16 v[108:111], a[204:207], v[144:147], v[108:111]\n" \
"v_exp_f32 v133, v133\n" \
"v_mfma_f32_16x16x32_bf16 v[108:111], a[236:239], v[152:155], v[108:111]\n" \
"ds_read_b128 a[232:235], v242 offset:10240\n" \
"v_cvt_pk_bf16_f32 v130, v132, v133\n" \
"v_mfma_f32_16x16x32_bf16 v[112:115], a[240:243], v[152:155], v[112:115]\n" \
"v_exp_f32 v134, v134\n" \
"v_mfma_f32_16x16x32_bf16 v[112:115], a[208:211], v[144:147], v[112:115]\n" \
"ds_read_b128 a[200:203], v242 offset:2048\n" \
"v_mfma_f32_16x16x32_bf16 v[116:119], a[212:215], v[144:147], v[116:119]\n" \
"v_exp_f32 v135, v135\n" \
"v_mfma_f32_16x16x32_bf16 v[116:119], a[244:247], v[152:155], v[116:119]\n" \
"ds_read_b128 a[204:207], v242 offset:3072\n" \
"v_cvt_pk_bf16_f32 v131, v134, v135\n" \
"v_mfma_f32_16x16x32_bf16 v[120:123], a[248:251], v[152:155], v[120:123]\n" \
"ds_read_b128 a[236:239], v242 offset:11264\n" \
"v_mfma_f32_16x16x32_bf16 v[120:123], a[216:219], v[144:147], v[120:123]\n" \
"v_exp_f32 v136, v136\n" \
"v_mfma_f32_16x16x32_bf16 v[124:127], a[220:223], v[144:147], v[124:127]\n" \
"ds_read_b128 a[240:243], v242 offset:12288\n" \
"v_mfma_f32_16x16x32_bf16 v[124:127], a[252:255], v[152:155], v[124:127]\n" \
"s_waitcnt vmcnt(12)\n" \
"v_exp_f32 v137, v137\n" \
"v_mfma_f32_16x16x32_bf16 v[144:147], a[128:131], v[176:179], 0\n" \
"s_barrier\n" \
"ds_read_b128 a[208:211], v242 offset:4096\n" \
"v_cvt_pk_bf16_f32 v136, v136, v137\n" \
"v_mfma_f32_16x16x32_bf16 v[144:147], a[132:135], v[180:183], v[144:147]\n" \
"s_mov_b32 m0, s20\n" \
"v_exp_f32 v138, v138\n" \
"v_mfma_f32_16x16x32_bf16 v[144:147], a[136:139], v[184:187], v[144:147]\n" \
"buffer_load_dwordx4 v248, s[32:35], s89 offen offset:0 sc1 lds\n" \
"v_mfma_f32_16x16x32_bf16 v[144:147], a[140:143], v[188:191], v[144:147]\n" \
"v_exp_f32 v139, v139\n" \
"v_mfma_f32_16x16x32_bf16 v[148:151], a[156:159], v[188:191], 0\n" \
"ds_read_b128 a[212:215], v242 offset:5120\n" \
"v_cvt_pk_bf16_f32 v137, v138, v139\n" \
"v_mfma_f32_16x16x32_bf16 v[148:151], a[152:155], v[184:187], v[148:151]\n" \
"v_exp_f32 v140, v140\n" \
"v_mfma_f32_16x16x32_bf16 v[148:151], a[148:151], v[180:183], v[148:151]\n" \
"ds_read_b128 a[244:247], v242 offset:13312\n" \
"ds_read_b128 a[248:251], v242 offset:14336\n" \
"v_mfma_f32_16x16x32_bf16 v[148:151], a[144:147], v[176:179], v[148:151]\n" \
"v_exp_f32 v141, v141\n" \
"v_mfma_f32_16x16x32_bf16 v[152:155], a[160:163], v[176:179], 0\n" \
"buffer_load_dwordx4 v248, s[32:35], s89 offen offset:1024 sc1 lds\n" \
"v_mfma_f32_16x16x32_bf16 v[152:155], a[164:167], v[180:183], v[152:155]\n" \
"v_exp_f32 v142, v142\n" \
"v_mfma_f32_16x16x32_bf16 v[152:155], a[168:171], v[184:187], v[152:155]\n" \
".Lst1_0_%=:\n" \
"s_cbranch_execz .Lst1_4_%=\n" \
"ds_read_b128 a[216:219], v242 offset:6144\n" \
"v_cvt_pk_bf16_f32 v138, v140, v141\n" \
"v_mfma_f32_16x16x32_bf16 v[152:155], a[172:175], v[188:191], v[152:155]\n" \
"v_exp_f32 v143, v143\n" \
"v_mfma_f32_16x16x32_bf16 v[156:159], a[188:191], v[188:191], 0\n" \
"ds_read_b128 a[128:131], v240 offset:0\n" \
"v_cvt_pk_bf16_f32 v139, v142, v143\n" \
"v_mfma_f32_16x16x32_bf16 v[156:159], a[184:187], v[184:187], v[156:159]\n" \
"ds_read_b128 a[132:135], v240 offset:1024\n" \
"ds_read_b128 a[220:223], v242 offset:7168\n" \
"v_mfma_f32_16x16x32_bf16 v[156:159], a[180:183], v[180:183], v[156:159]\n" \
"v_exp_f32 v144, v144\n" \
"v_mfma_f32_16x16x32_bf16 v[156:159], a[176:179], v[176:179], v[156:159]\n" \
"buffer_load_dwordx4 v248, s[32:35], s89 offen offset:2048 sc1 lds\n" \
"v_mfma_f32_16x16x32_bf16 a[40:43], a[48:51], v[128:131], a[40:43]\n" \
"v_exp_f32 v145, v145\n" \
"v_mfma_f32_16x16x32_bf16 a[40:43], a[48:51], v[136:139], a[40:43]\n" \
"s_waitcnt lgkmcnt(13)\n" \
"ds_read_b128 a[136:139], v240 offset:2048\n" \
"v_cvt_pk_bf16_f32 v144, v144, v145\n" \
"v_mfma_f32_16x16x32_bf16 v[64:67], a[224:227], v[136:139], v[64:67]\n" \
"v_exp_f32 v146, v146\n" \
"v_mfma_f32_16x16x32_bf16 v[64:67], a[192:195], v[128:131], v[64:67]\n" \
"ds_read_b128 a[252:255], v242 offset:15360\n" \
"ds_read_b128 a[140:143], v240 offset:3072\n" \
"v_mfma_f32_16x16x32_bf16 v[68:71], a[196:199], v[128:131], v[68:71]\n" \
"v_exp_f32 v147, v147\n" \
"v_mfma_f32_16x16x32_bf16 v[68:71], a[228:231], v[136:139], v[68:71]\n" \
"s_waitcnt lgkmcnt(12)\n" \
"buffer_load_dwordx4 v248, s[32:35], s89 offen offset:3072 sc1 lds\n" \
"v_mfma_f32_16x16x32_bf16 v[72:75], a[232:235], v[136:139], v[72:75]\n" \
"s_add_u32 s20, s20, 0x4000\n" \
"v_exp_f32 v148, v148\n" \
"v_mfma_f32_16x16x32_bf16 v[72:75], a[200:203], v[128:131], v[72:75]\n" \
"s_and_b32 s20, s20, 0xffff\n" \
"ds_read_b128 a[156:159], v240 offset:7168\n" \
"v_cvt_pk_bf16_f32 v145, v146, v147\n" \
"v_mfma_f32_16x16x32_bf16 v[76:79], a[204:207], v[128:131], v[76:79]\n" \
"s_bitset1_b32 s20, 16\n" \
"v_exp_f32 v149, v149\n" \
"v_mfma_f32_16x16x32_bf16 v[76:79], a[236:239], v[136:139], v[76:79]\n" \
"s_waitcnt lgkmcnt(9)\n" \
"ds_read_b128 a[152:155], v240 offset:6144\n" \
"v_cvt_pk_bf16_f32 v146, v148, v149\n" \
"v_mfma_f32_16x16x32_bf16 v[80:83], a[240:243], v[136:139], v[80:83]\n" \
"v_exp_f32 v150, v150\n" \
"v_mfma_f32_16x16x32_bf16 v[80:83], a[208:211], v[128:131], v[80:83]\n" \
"ds_read_b128 a[148:151], v240 offset:5120\n" \
"ds_read_b128 a[144:147], v240 offset:4096\n" \
"v_mfma_f32_16x16x32_bf16 v[84:87], a[212:215], v[128:131], v[84:87]\n" \
"v_exp_f32 v151, v151\n" \
"v_mfma_f32_16x16x32_bf16 v[84:87], a[244:247], v[136:139], v[84:87]\n" \
"s_waitcnt lgkmcnt(5)\n" \
"ds_read_b128 a[160:163], v240 offset:8192\n" \
"v_cvt_pk_bf16_f32 v147, v150, v151\n" \
"v_mfma_f32_16x16x32_bf16 v[88:91], a[248:251], v[136:139], v[88:91]\n" \
"ds_read_b128 a[164:167], v240 offset:9216\n" \
"ds_read_b128 a[168:171], v240 offset:10240\n" \
"v_mfma_f32_16x16x32_bf16 v[88:91], a[216:219], v[128:131], v[88:91]\n" \
"v_exp_f32 v152, v152\n" \
"v_mfma_f32_16x16x32_bf16 v[92:95], a[220:223], v[128:131], v[92:95]\n" \
"ds_read_b128 a[172:175], v240 offset:11264\n" \
"ds_read_b128 a[188:191], v240 offset:15360\n" \
"v_mfma_f32_16x16x32_bf16 v[92:95], a[252:255], v[136:139], v[92:95]\n" \
"s_waitcnt lgkmcnt(9)\n" \
"v_exp_f32 v153, v153\n" \
"v_mfma_f32_16x16x32_bf16 v[128:131], a[128:131], v[160:163], 0\n" \
"s_add_u32 s86, s86, 0x4000\n" \
"ds_read_b128 a[184:187], v240 offset:14336\n" \
"v_cvt_pk_bf16_f32 v152, v152, v153\n" \
"v_mfma_f32_16x16x32_bf16 v[128:131], a[132:135], v[164:167], v[128:131]\n" \
"s_bitset0_b32 s86, 16\n" \
"v_exp_f32 v154, v154\n" \
"v_mfma_f32_16x16x32_bf16 v[128:131], a[136:139], v[168:171], v[128:131]\n" \
"s_cmp_eq_u32 s88, s24\n" \
"s_cbranch_scc1 .Lcht_%=\n" \
".Lsbht_%=:\n" \
"v_mfma_f32_16x16x32_bf16 v[128:131], a[140:143], v[172:175], v[128:131]\n" \
"s_waitcnt lgkmcnt(6)\n" \
"v_exp_f32 v155, v155\n" \
"v_mfma_f32_16x16x32_bf16 v[132:135], a[156:159], v[172:175], 0\n" \
"s_mov_b32 m0, s19\n" \
"v_add_u32 v242, 0x10000, v240\n" \
"v_cvt_pk_bf16_f32 v153, v154, v155\n" \
"v_mfma_f32_16x16x32_bf16 v[132:135], a[152:155], v[168:171], v[132:135]\n" \
"s_cmp_lg_u32 s76, 0\n" \
"ds_read_b128 a[180:183], v240 offset:13312\n" \
"ds_read_b128 a[176:179], v240 offset:12288\n" \
"v_exp_f32 v156, v156\n" \
"v_mfma_f32_16x16x32_bf16 v[132:135], a[148:151], v[164:167], v[132:135]\n" \
"buffer_load_dwordx4 v248, s[32:35], s88 offen offset:0 sc1 lds\n" \
"v_mfma_f32_16x16x32_bf16 v[132:135], a[144:147], v[160:163], v[132:135]\n" \
"s_waitcnt lgkmcnt(4)\n" \
"v_exp_f32 v157, v157\n" \
"v_mfma_f32_16x16x32_bf16 v[136:139], a[160:163], v[160:163], 0\n" \
"s_cbranch_scc1 .Lant_%=\n" \
".Labt_%=:\n" \
"v_cvt_pk_bf16_f32 v154, v156, v157\n" \
"v_mfma_f32_16x16x32_bf16 v[136:139], a[164:167], v[164:167], v[136:139]\n" \
"v_exp_f32 v158, v158\n" \
"v_mfma_f32_16x16x32_bf16 v[136:139], a[168:171], v[168:171], v[136:139]\n" \
"buffer_load_dwordx4 v248, s[32:35], s88 offen offset:1024 sc1 lds\n" \
"v_mfma_f32_16x16x32_bf16 v[136:139], a[172:175], v[172:175], v[136:139]\n" \
"s_waitcnt lgkmcnt(0)\n" \
"v_exp_f32 v159, v159\n" \
"v_mfma_f32_16x16x32_bf16 v[140:143], a[188:191], v[172:175], 0\n" \
"v_add_u32 v240, s86, v248\n" \
"v_cvt_pk_bf16_f32 v155, v158, v159\n" \
"v_mfma_f32_16x16x32_bf16 v[140:143], a[184:187], v[168:171], v[140:143]\n" \
"buffer_load_dwordx4 v248, s[32:35], s88 offen offset:2048 sc1 lds\n" \
"v_mfma_f32_16x16x32_bf16 v[140:143], a[180:183], v[164:167], v[140:143]\n" \
"v_exp_f32 v128, v128\n" \
"v_mfma_f32_16x16x32_bf16 v[140:143], a[176:179], v[160:163], v[140:143]\n" \
"buffer_load_dwordx4 v248, s[32:35], s88 offen offset:3072 sc1 lds\n" \
"v_mfma_f32_16x16x32_bf16 a[44:47], a[48:51], v[144:147], a[44:47]\n" \
"s_add_u32 s89, s88, 589824\n" \
"v_exp_f32 v129, v129\n" \
"v_mfma_f32_16x16x32_bf16 a[44:47], a[48:51], v[152:155], a[44:47]\n" \
"s_add_u32 s88, s88, s27\n" \
"s_add_u32 s19, s19, 0x4000\n" \
"v_cvt_pk_bf16_f32 v128, v128, v129\n" \
"v_mfma_f32_16x16x32_bf16 v[96:99], a[224:227], v[152:155], v[96:99]\n" \
"s_bitset0_b32 s19, 16\n" \
"v_exp_f32 v130, v130\n" \
"v_mfma_f32_16x16x32_bf16 v[96:99], a[192:195], v[144:147], v[96:99]\n" \
"s_cmp_eq_u32 s88, s16\n" \
"s_cmov_b32 s88, s21\n" \
"ds_read_b128 a[224:227], v242 offset:8192\n" \
"v_mfma_f32_16x16x32_bf16 v[100:103], a[196:199], v[144:147], v[100:103]\n" \
"s_cmov_b32 s27, s91\n" \
"v_exp_f32 v131, v131\n" \
"v_mfma_f32_16x16x32_bf16 v[100:103], a[228:231], v[152:155], v[100:103]\n" \
"s_cmov_b32 s16, s77\n" \
"ds_read_b128 a[192:195], v242 offset:0\n" \
"v_cvt_pk_bf16_f32 v129, v130, v131\n" \
"v_mfma_f32_16x16x32_bf16 v[104:107], a[232:235], v[152:155], v[104:107]\n" \
"s_cmov_b32 s21, s92\n" \
"v_exp_f32 v132, v132\n" \
"v_mfma_f32_16x16x32_bf16 v[104:107], a[200:203], v[144:147], v[104:107]\n" \
"s_cmovk_i32 s91, 0x4000\n" \
"ds_read_b128 a[196:199], v242 offset:1024\n" \
"ds_read_b128 a[228:231], v242 offset:9216\n" \
"v_mfma_f32_16x16x32_bf16 v[108:111], a[204:207], v[144:147], v[108:111]\n" \
"v_exp_f32 v133, v133\n" \
"v_mfma_f32_16x16x32_bf16 v[108:111], a[236:239], v[152:155], v[108:111]\n" \
"ds_read_b128 a[232:235], v242 offset:10240\n" \
"v_cvt_pk_bf16_f32 v130, v132, v133\n" \
"v_mfma_f32_16x16x32_bf16 v[112:115], a[240:243], v[152:155], v[112:115]\n" \
"v_exp_f32 v134, v134\n" \
"v_mfma_f32_16x16x32_bf16 v[112:115], a[208:211], v[144:147], v[112:115]\n" \
"ds_read_b128 a[200:203], v242 offset:2048\n" \
"v_mfma_f32_16x16x32_bf16 v[116:119], a[212:215], v[144:147], v[116:119]\n" \
"v_exp_f32 v135, v135\n" \
"v_mfma_f32_16x16x32_bf16 v[116:119], a[244:247], v[152:155], v[116:119]\n" \
"ds_read_b128 a[204:207], v242 offset:3072\n" \
"v_cvt_pk_bf16_f32 v131, v134, v135\n" \
"v_mfma_f32_16x16x32_bf16 v[120:123], a[248:251], v[152:155], v[120:123]\n" \
"ds_read_b128 a[236:239], v242 offset:11264\n" \
"v_mfma_f32_16x16x32_bf16 v[120:123], a[216:219], v[144:147], v[120:123]\n" \
"v_exp_f32 v136, v136\n" \
"v_mfma_f32_16x16x32_bf16 v[124:127], a[220:223], v[144:147], v[124:127]\n" \
"ds_read_b128 a[240:243], v242 offset:12288\n" \
"v_mfma_f32_16x16x32_bf16 v[124:127], a[252:255], v[152:155], v[124:127]\n" \
"s_waitcnt vmcnt(12)\n" \
"v_exp_f32 v137, v137\n" \
"v_mfma_f32_16x16x32_bf16 v[144:147], a[128:131], v[176:179], 0\n" \
"s_barrier\n" \
"ds_read_b128 a[208:211], v242 offset:4096\n" \
"v_cvt_pk_bf16_f32 v136, v136, v137\n" \
"v_mfma_f32_16x16x32_bf16 v[144:147], a[132:135], v[180:183], v[144:147]\n" \
"s_mov_b32 m0, s20\n" \
"v_exp_f32 v138, v138\n" \
"v_mfma_f32_16x16x32_bf16 v[144:147], a[136:139], v[184:187], v[144:147]\n" \
"buffer_load_dwordx4 v248, s[32:35], s89 offen offset:0 sc1 lds\n" \
"v_mfma_f32_16x16x32_bf16 v[144:147], a[140:143], v[188:191], v[144:147]\n" \
"v_exp_f32 v139, v139\n" \
"v_mfma_f32_16x16x32_bf16 v[148:151], a[156:159], v[188:191], 0\n" \
"ds_read_b128 a[212:215], v242 offset:5120\n" \
"v_cvt_pk_bf16_f32 v137, v138, v139\n" \
"v_mfma_f32_16x16x32_bf16 v[148:151], a[152:155], v[184:187], v[148:151]\n" \
"v_exp_f32 v140, v140\n" \
"v_mfma_f32_16x16x32_bf16 v[148:151], a[148:151], v[180:183], v[148:151]\n" \
"ds_read_b128 a[244:247], v242 offset:13312\n" \
"ds_read_b128 a[248:251], v242 offset:14336\n" \
"v_mfma_f32_16x16x32_bf16 v[148:151], a[144:147], v[176:179], v[148:151]\n" \
"v_exp_f32 v141, v141\n" \
"v_mfma_f32_16x16x32_bf16 v[152:155], a[160:163], v[176:179], 0\n" \
"buffer_load_dwordx4 v248, s[32:35], s89 offen offset:1024 sc1 lds\n" \
"v_mfma_f32_16x16x32_bf16 v[152:155], a[164:167], v[180:183], v[152:155]\n" \
"v_exp_f32 v142, v142\n" \
"v_mfma_f32_16x16x32_bf16 v[152:155], a[168:171], v[184:187], v[152:155]\n" \
"ds_read_b128 a[216:219], v242 offset:6144\n" \
"v_cvt_pk_bf16_f32 v138, v140, v141\n" \
"v_mfma_f32_16x16x32_bf16 v[152:155], a[172:175], v[188:191], v[152:155]\n" \
"v_exp_f32 v143, v143\n" \
"v_mfma_f32_16x16x32_bf16 v[156:159], a[188:191], v[188:191], 0\n" \
"ds_read_b128 a[128:131], v240 offset:0\n" \
"v_cvt_pk_bf16_f32 v139, v142, v143\n" \
"v_mfma_f32_16x16x32_bf16 v[156:159], a[184:187], v[184:187], v[156:159]\n" \
"ds_read_b128 a[132:135], v240 offset:1024\n" \
"ds_read_b128 a[220:223], v242 offset:7168\n" \
"v_mfma_f32_16x16x32_bf16 v[156:159], a[180:183], v[180:183], v[156:159]\n" \
"v_exp_f32 v144, v144\n" \
"v_mfma_f32_16x16x32_bf16 v[156:159], a[176:179], v[176:179], v[156:159]\n" \
"buffer_load_dwordx4 v248, s[32:35], s89 offen offset:2048 sc1 lds\n" \
"v_mfma_f32_16x16x32_bf16 a[40:43], a[48:51], v[128:131], a[40:43]\n" \
"v_exp_f32 v145, v145\n" \
"v_mfma_f32_16x16x32_bf16 a[40:43], a[48:51], v[136:139], a[40:43]\n" \
"s_waitcnt lgkmcnt(13)\n" \
"ds_read_b128 a[136:139], v240 offset:2048\n" \
"v_cvt_pk_bf16_f32 v144, v144, v145\n" \
"v_mfma_f32_16x16x32_bf16 v[64:67], a[224:227], v[136:139], v[64:67]\n" \
"v_exp_f32 v146, v146\n" \
"v_mfma_f32_16x16x32_bf16 v[64:67], a[192:195], v[128:131], v[64:67]\n" \
"ds_read_b128 a[252:255], v242 offset:15360\n" \
"ds_read_b128 a[140:143], v240 offset:3072\n" \
"v_mfma_f32_16x16x32_bf16 v[68:71], a[196:199], v[128:131], v[68:71]\n" \
"v_exp_f32 v147, v147\n" \
"v_mfma_f32_16x16x32_bf16 v[68:71], a[228:231], v[136:139], v[68:71]\n" \
"s_waitcnt lgkmcnt(12)\n" \
"buffer_load_dwordx4 v248, s[32:35], s89 offen offset:3072 sc1 lds\n" \
"v_mfma_f32_16x16x32_bf16 v[72:75], a[232:235], v[136:139], v[72:75]\n" \
"s_add_u32 s20, s20, 0x4000\n" \
"v_exp_f32 v148, v148\n" \
"v_mfma_f32_16x16x32_bf16 v[72:75], a[200:203], v[128:131], v[72:75]\n" \
"s_and_b32 s20, s20, 0xffff\n" \
"ds_read_b128 a[156:159], v240 offset:7168\n" \
"v_cvt_pk_bf16_f32 v145, v146, v147\n" \
"v_mfma_f32_16x16x32_bf16 v[76:79], a[204:207], v[128:131], v[76:79]\n" \
"s_bitset1_b32 s20, 16\n" \
"v_exp_f32 v149, v149\n" \
"v_mfma_f32_16x16x32_bf16 v[76:79], a[236:239], v[136:139], v[76:79]\n" \
"s_waitcnt lgkmcnt(9)\n" \
"ds_read_b128 a[152:155], v240 offset:6144\n" \
"v_cvt_pk_bf16_f32 v146, v148, v149\n" \
"v_mfma_f32_16x16x32_bf16 v[80:83], a[240:243], v[136:139], v[80:83]\n" \
"v_exp_f32 v150, v150\n" \
"v_mfma_f32_16x16x32_bf16 v[80:83], a[208:211], v[128:131], v[80:83]\n" \
"ds_read_b128 a[148:151], v240 offset:5120\n" \
"ds_read_b128 a[144:147], v240 offset:4096\n" \
"v_mfma_f32_16x16x32_bf16 v[84:87], a[212:215], v[128:131], v[84:87]\n" \
"s_sub_u32 s95, s95, 1\n" \
"v_exp_f32 v151, v151\n" \
"v_mfma_f32_16x16x32_bf16 v[84:87], a[244:247], v[136:139], v[84:87]\n" \
"s_waitcnt lgkmcnt(5)\n" \
"ds_read_b128 a[160:163], v240 offset:8192\n" \
"v_cvt_pk_bf16_f32 v147, v150, v151\n" \
"v_mfma_f32_16x16x32_bf16 v[88:91], a[248:251], v[136:139], v[88:91]\n" \
"ds_read_b128 a[164:167], v240 offset:9216\n" \
"ds_read_b128 a[168:171], v240 offset:10240\n" \
"v_mfma_f32_16x16x32_bf16 v[88:91], a[216:219], v[128:131], v[88:91]\n" \
"v_exp_f32 v152, v152\n" \
"v_mfma_f32_16x16x32_bf16 v[92:95], a[220:223], v[128:131], v[92:95]\n" \
"s_waitcnt lgkmcnt(7)\n" \
"ds_read_b128 a[172:175], v240 offset:11264\n" \
"v_exp_f32 v153, v153\n" \
"v_mfma_f32_16x16x32_bf16 v[92:95], a[252:255], v[136:139], v[92:95]\n" \
"s_cbranch_scc0 .Lloop2_%=\n" \
"s_cmp_lt_u32 s67, 4\n" \
"s_cbranch_scc0 .Le1_%=\n" \
"s_mov_b32 s95, 3\n" \
".p2align 3\n" \
".Lloop3_%=:\n" \
".Lst1_1_%=:\n" \
"s_cbranch_execz .Lst1_5_%=\n" \
  \
"v_mfma_f32_16x16x32_bf16 v[128:131], a[128:131], v[160:163], 0\n" \
"s_add_u32 s86, s86, 0x4000\n" \
"ds_read_b128 a[188:191], v240 offset:15360\n" \
"v_cvt_pk_bf16_f32 v152, v152, v153\n" \
"v_mfma_f32_16x16x32_bf16 v[128:131], a[132:135], v[164:167], v[128:131]\n" \
"s_bitset0_b32 s86, 16\n" \
"v_exp_f32 v154, v154\n" \
"v_mfma_f32_16x16x32_bf16 v[128:131], a[136:139], v[168:171], v[128:131]\n" \
"ds_read_b128 a[184:187], v240 offset:14336\n" \
"v_add_u32 v242, 0x10000, v240\n" \
"v_mfma_f32_16x16x32_bf16 v[128:131], a[140:143], v[172:175], v[128:131]\n" \
"s_waitcnt lgkmcnt(6)\n" \
"v_exp_f32 v155, v155\n" \
"v_mfma_f32_16x16x32_bf16 v[132:135], a[156:159], v[172:175], 0\n" \
"s_mov_b32 m0, s19\n" \
"ds_read_b128 a[180:183], v240 offset:13312\n" \
"v_cvt_pk_bf16_f32 v153, v154, v155\n" \
"v_mfma_f32_16x16x32_bf16 v[132:135], a[152:155], v[168:171], v[132:135]\n" \
"ds_read_b128 a[176:179], v240 offset:12288\n" \
"v_exp_f32 v156, v156\n" \
"v_mfma_f32_16x16x32_bf16 v[132:135], a[148:151], v[164:167], v[132:135]\n" \
"buffer_load_dwordx4 v248, s[32:35], s88 offen offset:0 sc1 lds\n" \
"v_mfma_f32_16x16x32_bf16 v[132:135], a[144:147], v[160:163], v[132:135]\n" \
"s_waitcnt lgkmcnt(4)\n" \
"v_exp_f32 v157, v157\n" \
"v_mfma_f32_16x16x32_bf16 v[136:139], a[160:163], v[160:163], 0\n" \
"v_cvt_pk_bf16_f32 v154, v156, v157\n" \
"v_mfma_f32_16x16x32_bf16 v[136:139], a[164:167], v[164:167], v[136:139]\n" \
"v_exp_f32 v158, v158\n" \
"v_mfma_f32_16x16x32_bf16 v[136:139], a[168:171], v[168:171], v[136:139]\n" \
"buffer_load_dwordx4 v248, s[32:35], s88 offen offset:1024 sc1 lds\n" \
"v_mfma_f32_16x16x32_bf16 v[136:139], a[172:175], v[172:175], v[136:139]\n" \
"s_waitcnt lgkmcnt(0)\n" \
"v_exp_f32 v159, v159\n" \
"v_mfma_f32_16x16x32_bf16 v[140:143], a[188:191], v[172:175], 0\n" \
"v_add_u32 v240, s86, v248\n" \
"v_cvt_pk_bf16_f32 v155, v158, v159\n" \
"v_mfma_f32_16x16x32_bf16 v[140:143], a[184:187], v[168:171], v[140:143]\n" \
"buffer_load_dwordx4 v248, s[32:35], s88 offen offset:2048 sc1 lds\n" \
"v_mfma_f32_16x16x32_bf16 v[140:143], a[180:183], v[164:167], v[140:143]\n" \
"v_exp_f32 v128, v128\n" \
"v_mfma_f32_16x16x32_bf16 v[140:143], a[176:179], v[160:163], v[140:143]\n" \
"buffer_load_dwordx4 v248, s[32:35], s88 offen offset:3072 sc1 lds\n" \
"v_mfma_f32_16x16x32_bf16 a[44:47], a[48:51], v[144:147], a[44:47]\n" \
"s_add_u32 s89, s88, 589824\n" \
"v_exp_f32 v129, v129\n" \
"v_mfma_f32_16x16x32_bf16 a[44:47], a[48:51], v[152:155], a[44:47]\n" \
"s_add_u32 s88, s88, s27\n" \
"s_add_u32 s19, s19, 0x4000\n" \
"v_cvt_pk_bf16_f32 v128, v128, v129\n" \
"v_mfma_f32_16x16x32_bf16 v[96:99], a[224:227], v[152:155], v[96:99]\n" \
"s_bitset0_b32 s19, 16\n" \
"v_exp_f32 v130, v130\n" \
"v_mfma_f32_16x16x32_bf16 v[96:99], a[192:195], v[144:147], v[96:99]\n" \
"s_cmp_eq_u32 s88, s16\n" \
"s_cmov_b32 s88, s21\n" \
"ds_read_b128 a[224:227], v242 offset:8192\n" \
"v_mfma_f32_16x16x32_bf16 v[100:103], a[196:199], v[144:147], v[100:103]\n" \
"s_cmov_b32 s27, s91\n" \
"v_exp_f32 v131, v131\n" \
"v_mfma_f32_16x16x32_bf16 v[100:103], a[228:231], v[152:155], v[100:103]\n" \
"s_cmov_b32 s16, s77\n" \
"ds_read_b128 a[192:195], v242 offset:0\n" \
"v_cvt_pk_bf16_f32 v129, v130, v131\n" \
"v_mfma_f32_16x16x32_bf16 v[104:107], a[232:235], v[152:155], v[104:107]\n" \
"s_cmov_b32 s21, s92\n" \
"v_exp_f32 v132, v132\n" \
"v_mfma_f32_16x16x32_bf16 v[104:107], a[200:203], v[144:147], v[104:107]\n" \
"s_cmovk_i32 s91, 0x4000\n" \
"ds_read_b128 a[196:199], v242 offset:1024\n" \
"ds_read_b128 a[228:231], v242 offset:9216\n" \
"v_mfma_f32_16x16x32_bf16 v[108:111], a[204:207], v[144:147], v[108:111]\n" \
"s_mov_b32 m0, s87\n" \
"v_exp_f32 v133, v133\n" \
"v_mfma_f32_16x16x32_bf16 v[108:111], a[236:239], v[152:155], v[108:111]\n" \
"ds_read_b128 a[232:235], v242 offset:10240\n" \
"v_cvt_pk_bf16_f32 v130, v132, v133\n" \
"v_mfma_f32_16x16x32_bf16 v[112:115], a[240:243], v[152:155], v[112:115]\n" \
"buffer_load_dwordx4 v237, s[48:51], s93 offen lds\n" \
"v_mfma_f32_16x16x32_bf16 v[112:115], a[208:211], v[144:147], v[112:115]\n" \
"s_add_u32 s93, s93, 8192\n" \
"v_exp_f32 v134, v134\n" \
"v_mfma_f32_16x16x32_bf16 v[116:119], a[212:215], v[144:147], v[116:119]\n" \
"s_add_u32 s87, s87, 1024\n" \
"v_exp_f32 v135, v135\n" \
"v_mfma_f32_16x16x32_bf16 v[116:119], a[244:247], v[152:155], v[116:119]\n" \
"s_and_b32 s22, s87, 0xfff\n" \
"ds_read_b128 a[200:203], v242 offset:2048\n" \
"v_cvt_pk_bf16_f32 v131, v134, v135\n" \
"v_mfma_f32_16x16x32_bf16 v[120:123], a[248:251], v[152:155], v[120:123]\n" \
"s_cselect_b32 s22, 0, 0xffff8080\n" \
"ds_read_b128 a[204:207], v242 offset:3072\n" \
"ds_read_b128 a[236:239], v242 offset:11264\n" \
"v_mfma_f32_16x16x32_bf16 v[120:123], a[216:219], v[144:147], v[120:123]\n" \
"s_add_u32 s93, s93, s22\n" \
"v_exp_f32 v136, v136\n" \
"v_mfma_f32_16x16x32_bf16 v[124:127], a[220:223], v[144:147], v[124:127]\n" \
"ds_read_b128 a[240:243], v242 offset:12288\n" \
"v_mfma_f32_16x16x32_bf16 v[124:127], a[252:255], v[152:155], v[124:127]\n" \
"s_waitcnt vmcnt(13)\n" \
"v_exp_f32 v137, v137\n" \
"v_mfma_f32_16x16x32_bf16 v[144:147], a[128:131], v[176:179], 0\n" \
"s_barrier\n" \
"ds_read_b128 a[208:211], v242 offset:4096\n" \
"v_cvt_pk_bf16_f32 v136, v136, v137\n" \
"v_mfma_f32_16x16x32_bf16 v[144:147], a[132:135], v[180:183], v[144:147]\n" \
"s_mov_b32 m0, s20\n" \
"v_exp_f32 v138, v138\n" \
"v_mfma_f32_16x16x32_bf16 v[144:147], a[136:139], v[184:187], v[144:147]\n" \
"buffer_load_dwordx4 v248, s[32:35], s89 offen offset:0 sc1 lds\n" \
"v_mfma_f32_16x16x32_bf16 v[144:147], a[140:143], v[188:191], v[144:147]\n" \
"v_exp_f32 v139, v139\n" \
"v_mfma_f32_16x16x32_bf16 v[148:151], a[156:159], v[188:191], 0\n" \
"ds_read_b128 a[212:215], v242 offset:5120\n" \
"v_cvt_pk_bf16_f32 v137, v138, v139\n" \
"v_mfma_f32_16x16x32_bf16 v[148:151], a[152:155], v[184:187], v[148:151]\n" \
"v_exp_f32 v140, v140\n" \
"v_mfma_f32_16x16x32_bf16 v[148:151], a[148:151], v[180:183], v[148:151]\n" \
"ds_read_b128 a[244:247], v242 offset:13312\n" \
"ds_read_b128 a[248:251], v242 offset:14336\n" \
"v_mfma_f32_16x16x32_bf16 v[148:151], a[144:147], v[176:179], v[148:151]\n" \
"v_exp_f32 v141, v141\n" \
"v_mfma_f32_16x16x32_bf16 v[152:155], a[160:163], v[176:179], 0\n" \
"buffer_load_dwordx4 v248, s[32:35], s89 offen offset:1024 sc1 lds\n" \
"v_mfma_f32_16x16x32_bf16 v[152:155], a[164:167], v[180:183], v[152:155]\n" \
"v_exp_f32 v142, v142\n" \
"v_mfma_f32_16x16x32_bf16 v[152:155], a[168:171], v[184:187], v[152:155]\n" \
"ds_read_b128 a[216:219], v242 offset:6144\n" \
"v_cvt_pk_bf16_f32 v138, v140, v141\n" \
"v_mfma_f32_16x16x32_bf16 v[152:155], a[172:175], v[188:191], v[152:155]\n" \
"v_exp_f32 v143, v143\n" \
"v_mfma_f32_16x16x32_bf16 v[156:159], a[188:191], v[188:191], 0\n" \
"ds_read_b128 a[128:131], v240 offset:0\n" \
"v_cvt_pk_bf16_f32 v139, v142, v143\n" \
"v_mfma_f32_16x16x32_bf16 v[156:159], a[184:187], v[184:187], v[156:159]\n" \
"ds_read_b128 a[132:135], v240 offset:1024\n" \
"ds_read_b128 a[220:223], v242 offset:7168\n" \
"v_mfma_f32_16x16x32_bf16 v[156:159], a[180:183], v[180:183], v[156:159]\n" \
"v_exp_f32 v144, v144\n" \
"v_mfma_f32_16x16x32_bf16 v[156:159], a[176:179], v[176:179], v[156:159]\n" \
"buffer_load_dwordx4 v248, s[32:35], s89 offen offset:2048 sc1 lds\n" \
"v_mfma_f32_16x16x32_bf16 a[40:43], a[48:51], v[128:131], a[40:43]\n" \
"v_exp_f32 v145, v145\n" \
"v_mfma_f32_16x16x32_bf16 a[40:43], a[48:51], v[136:139], a[40:43]\n" \
"s_waitcnt lgkmcnt(13)\n" \
"ds_read_b128 a[136:139], v240 offset:2048\n" \
"v_cvt_pk_bf16_f32 v144, v144, v145\n" \
"v_mfma_f32_16x16x32_bf16 v[64:67], a[224:227], v[136:139], v[64:67]\n" \
"v_exp_f32 v146, v146\n" \
"v_mfma_f32_16x16x32_bf16 v[64:67], a[192:195], v[128:131], v[64:67]\n" \
"ds_read_b128 a[252:255], v242 offset:15360\n" \
"ds_read_b128 a[140:143], v240 offset:3072\n" \
"v_mfma_f32_16x16x32_bf16 v[68:71], a[196:199], v[128:131], v[68:71]\n" \
"v_exp_f32 v147, v147\n" \
"v_mfma_f32_16x16x32_bf16 v[68:71], a[228:231], v[136:139], v[68:71]\n" \
"s_waitcnt lgkmcnt(12)\n" \
"buffer_load_dwordx4 v248, s[32:35], s89 offen offset:3072 sc1 lds\n" \
"v_mfma_f32_16x16x32_bf16 v[72:75], a[232:235], v[136:139], v[72:75]\n" \
"s_add_u32 s20, s20, 0x4000\n" \
"v_exp_f32 v148, v148\n" \
"v_mfma_f32_16x16x32_bf16 v[72:75], a[200:203], v[128:131], v[72:75]\n" \
"s_and_b32 s20, s20, 0xffff\n" \
"ds_read_b128 a[156:159], v240 offset:7168\n" \
"v_cvt_pk_bf16_f32 v145, v146, v147\n" \
"v_mfma_f32_16x16x32_bf16 v[76:79], a[204:207], v[128:131], v[76:79]\n" \
"s_bitset1_b32 s20, 16\n" \
"v_exp_f32 v149, v149\n" \
"v_mfma_f32_16x16x32_bf16 v[76:79], a[236:239], v[136:139], v[76:79]\n" \
"s_waitcnt lgkmcnt(9)\n" \
"ds_read_b128 a[152:155], v240 offset:6144\n" \
"v_cvt_pk_bf16_f32 v146, v148, v149\n" \
"v_mfma_f32_16x16x32_bf16 v[80:83], a[240:243], v[136:139], v[80:83]\n" \
"v_exp_f32 v150, v150\n" \
"v_mfma_f32_16x16x32_bf16 v[80:83], a[208:211], v[128:131], v[80:83]\n" \
"ds_read_b128 a[148:151], v240 offset:5120\n" \
"ds_read_b128 a[144:147], v240 offset:4096\n" \
"v_mfma_f32_16x16x32_bf16 v[84:87], a[212:215], v[128:131], v[84:87]\n" \
"v_exp_f32 v151, v151\n" \
"v_mfma_f32_16x16x32_bf16 v[84:87], a[244:247], v[136:139], v[84:87]\n" \
"s_waitcnt lgkmcnt(5)\n" \
"ds_read_b128 a[160:163], v240 offset:8192\n" \
"v_cvt_pk_bf16_f32 v147, v150, v151\n" \
"v_mfma_f32_16x16x32_bf16 v[88:91], a[248:251], v[136:139], v[88:91]\n" \
"ds_read_b128 a[164:167], v240 offset:9216\n" \
"ds_read_b128 a[168:171], v240 offset:10240\n" \
"v_mfma_f32_16x16x32_bf16 v[88:91], a[216:219], v[128:131], v[88:91]\n" \
"v_exp_f32 v152, v152\n" \
"v_mfma_f32_16x16x32_bf16 v[92:95], a[220:223], v[128:131], v[92:95]\n" \
"ds_read_b128 a[172:175], v240 offset:11264\n" \
"ds_read_b128 a[188:191], v240 offset:15360\n" \
"v_mfma_f32_16x16x32_bf16 v[92:95], a[252:255], v[136:139], v[92:95]\n" \
"s_waitcnt lgkmcnt(9)\n" \
"v_exp_f32 v153, v153\n" \
"v_mfma_f32_16x16x32_bf16 v[128:131], a[128:131], v[160:163], 0\n" \
"s_add_u32 s86, s86, 0x4000\n" \
"ds_read_b128 a[184:187], v240 offset:14336\n" \
"v_cvt_pk_bf16_f32 v152, v152, v153\n" \
"v_mfma_f32_16x16x32_bf16 v[128:131], a[132:135], v[164:167], v[128:131]\n" \
"s_bitset0_b32 s86, 16\n" \
"v_exp_f32 v154, v154\n" \
"v_mfma_f32_16x16x32_bf16 v[128:131], a[136:139], v[168:171], v[128:131]\n" \
"s_cmp_eq_u32 s88, s24\n" \
"s_cbranch_scc1 .Lchtx_%=\n" \
".Lsbhtx_%=:\n" \
"v_mfma_f32_16x16x32_bf16 v[128:131], a[140:143], v[172:175], v[128:131]\n" \
"s_waitcnt lgkmcnt(6)\n" \
"v_exp_f32 v155, v155\n" \
"v_mfma_f32_16x16x32_bf16 v[132:135], a[156:159], v[172:175], 0\n" \
"s_mov_b32 m0, s19\n" \
"v_add_u32 v242, 0x10000, v240\n" \
"v_cvt_pk_bf16_f32 v153, v154, v155\n" \
"v_mfma_f32_16x16x32_bf16 v[132:135], a[152:155], v[168:171], v[132:135]\n" \
"s_cmp_lg_u32 s76, 0\n" \
"ds_read_b128 a[180:183], v240 offset:13312\n" \
"ds_read_b128 a[176:179], v240 offset:12288\n" \
"v_exp_f32 v156, v156\n" \
"v_mfma_f32_16x16x32_bf16 v[132:135], a[148:151], v[164:167], v[132:135]\n" \
"buffer_load_dwordx4 v248, s[32:35], s88 offen offset:0 sc1 lds\n" \
"v_mfma_f32_16x16x32_bf16 v[132:135], a[144:147], v[160:163], v[132:135]\n" \
"s_waitcnt lgkmcnt(4)\n" \
"v_exp_f32 v157, v157\n" \
"v_mfma_f32_16x16x32_bf16 v[136:139], a[160:163], v[160:163], 0\n" \
"s_cbranch_scc1 .Lantx_%=\n" \
".Labtx_%=:\n" \
"v_cvt_pk_bf16_f32 v154, v156, v157\n" \
"v_mfma_f32_16x16x32_bf16 v[136:139], a[164:167], v[164:167], v[136:139]\n" \
"v_exp_f32 v158, v158\n" \
"v_mfma_f32_16x16x32_bf16 v[136:139], a[168:171], v[168:171], v[136:139]\n" \
"buffer_load_dwordx4 v248, s[32:35], s88 offen offset:1024 sc1 lds\n" \
"v_mfma_f32_16x16x32_bf16 v[136:139], a[172:175], v[172:175], v[136:139]\n" \
"s_waitcnt lgkmcnt(0)\n" \
"v_exp_f32 v159, v159\n" \
"v_mfma_f32_16x16x32_bf16 v[140:143], a[188:191], v[172:175], 0\n" \
"v_add_u32 v240, s86, v248\n" \
"v_cvt_pk_bf16_f32 v155, v158, v159\n" \
"v_mfma_f32_16x16x32_bf16 v[140:143], a[184:187], v[168:171], v[140:143]\n" \
"buffer_load_dwordx4 v248, s[32:35], s88 offen offset:2048 sc1 lds\n" \
"v_mfma_f32_16x16x32_bf16 v[140:143], a[180:183], v[164:167], v[140:143]\n" \
"v_exp_f32 v128, v128\n" \
"v_mfma_f32_16x16x32_bf16 v[140:143], a[176:179], v[160:163], v[140:143]\n" \
"buffer_load_dwordx4 v248, s[32:35], s88 offen offset:3072 sc1 lds\n" \
"v_mfma_f32_16x16x32_bf16 a[44:47], a[48:51], v[144:147], a[44:47]\n" \
"s_add_u32 s89, s88, 589824\n" \
"v_exp_f32 v129, v129\n" \
"v_mfma_f32_16x16x32_bf16 a[44:47], a[48:51], v[152:155], a[44:47]\n" \
"s_add_u32 s88, s88, s27\n" \
"s_add_u32 s19, s19, 0x4000\n" \
"v_cvt_pk_bf16_f32 v128, v128, v129\n" \
"v_mfma_f32_16x16x32_bf16 v[96:99], a[224:227], v[152:155], v[96:99]\n" \
"s_bitset0_b32 s19, 16\n" \
"v_exp_f32 v130, v130\n" \
"v_mfma_f32_16x16x32_bf16 v[96:99], a[192:195], v[144:147], v[96:99]\n" \
"s_cmp_eq_u32 s88, s16\n" \
"s_cmov_b32 s88, s21\n" \
"ds_read_b128 a[224:227], v242 offset:8192\n" \
"v_mfma_f32_16x16x32_bf16 v[100:103], a[196:199], v[144:147], v[100:103]\n" \
"s_cmov_b32 s27, s91\n" \
"v_exp_f32 v131, v131\n" \
"v_mfma_f32_16x16x32_bf16 v[100:103], a[228:231], v[152:155], v[100:103]\n" \
"s_cmov_b32 s16, s77\n" \
"ds_read_b128 a[192:195], v242 offset:0\n" \
"v_cvt_pk_bf16_f32 v129, v130, v131\n" \
"v_mfma_f32_16x16x32_bf16 v[104:107], a[232:235], v[152:155], v[104:107]\n" \
"s_cmov_b32 s21, s92\n" \
"v_exp_f32 v132, v132\n" \
"v_mfma_f32_16x16x32_bf16 v[104:107], a[200:203], v[144:147], v[104:107]\n" \
"s_cmovk_i32 s91, 0x4000\n" \
"ds_read_b128 a[196:199], v242 offset:1024\n" \
"ds_read_b128 a[228:231], v242 offset:9216\n" \
"v_mfma_f32_16x16x32_bf16 v[108:111], a[204:207], v[144:147], v[108:111]\n" \
"s_mov_b32 m0, s87\n" \
"v_exp_f32 v133, v133\n" \
"v_mfma_f32_16x16x32_bf16 v[108:111], a[236:239], v[152:155], v[108:111]\n" \
"ds_read_b128 a[232:235], v242 offset:10240\n" \
"v_cvt_pk_bf16_f32 v130, v132, v133\n" \
"v_mfma_f32_16x16x32_bf16 v[112:115], a[240:243], v[152:155], v[112:115]\n" \
"buffer_load_dwordx4 v237, s[48:51], s93 offen lds\n" \
"v_mfma_f32_16x16x32_bf16 v[112:115], a[208:211], v[144:147], v[112:115]\n" \
"s_add_u32 s93, s93, 8192\n" \
"v_exp_f32 v134, v134\n" \
"v_mfma_f32_16x16x32_bf16 v[116:119], a[212:215], v[144:147], v[116:119]\n" \
"s_add_u32 s87, s87, 1024\n" \
"v_exp_f32 v135, v135\n" \
"v_mfma_f32_16x16x32_bf16 v[116:119], a[244:247], v[152:155], v[116:119]\n" \
"s_and_b32 s22, s87, 0xfff\n" \
"ds_read_b128 a[200:203], v242 offset:2048\n" \
"v_cvt_pk_bf16_f32 v131, v134, v135\n" \
"v_mfma_f32_16x16x32_bf16 v[120:123], a[248:251], v[152:155], v[120:123]\n" \
"s_cselect_b32 s22, 0, 0xffff8080\n" \
"ds_read_b128 a[204:207], v242 offset:3072\n" \
"ds_read_b128 a[236:239], v242 offset:11264\n" \
"v_mfma_f32_16x16x32_bf16 v[120:123], a[216:219], v[144:147], v[120:123]\n" \
"s_add_u32 s93, s93, s22\n" \
"v_exp_f32 v136, v136\n" \
"v_mfma_f32_16x16x32_bf16 v[124:127], a[220:223], v[144:147], v[124:127]\n" \
"ds_read_b128 a[240:243], v242 offset:12288\n" \
"v_mfma_f32_16x16x32_bf16 v[124:127], a[252:255], v[152:155], v[124:127]\n" \
"s_waitcnt vmcnt(14)\n" \
"v_exp_f32 v137, v137\n" \
"v_mfma_f32_16x16x32_bf16 v[144:147], a[128:131], v[176:179], 0\n" \
"s_barrier\n" \
"ds_read_b128 a[208:211], v242 offset:4096\n" \
"v_cvt_pk_bf16_f32 v136, v136, v137\n" \
"v_mfma_f32_16x16x32_bf16 v[144:147], a[132:135], v[180:183], v[144:147]\n" \
"s_mov_b32 m0, s20\n" \
"v_exp_f32 v138, v138\n" \
"v_mfma_f32_16x16x32_bf16 v[144:147], a[136:139], v[184:187], v[144:147]\n" \
"buffer_load_dwordx4 v248, s[32:35], s89 offen offset:0 sc1 lds\n" \
"v_mfma_f32_16x16x32_bf16 v[144:147], a[140:143], v[188:191], v[144:147]\n" \
"v_exp_f32 v139, v139\n" \
"v_mfma_f32_16x16x32_bf16 v[148:151], a[156:159], v[188:191], 0\n" \
"ds_read_b128 a[212:215], v242 offset:5120\n" \
"v_cvt_pk_bf16_f32 v137, v138, v139\n" \
"v_mfma_f32_16x16x32_bf16 v[148:151], a[152:155], v[184:187], v[148:151]\n" \
"v_exp_f32 v140, v140\n" \
"v_mfma_f32_16x16x32_bf16 v[148:151], a[148:151], v[180:183], v[148:151]\n" \
"ds_read_b128 a[244:247], v242 offset:13312\n" \
"ds_read_b128 a[248:251], v242 offset:14336\n" \
"v_mfma_f32_16x16x32_bf16 v[148:151], a[144:147], v[176:179], v[148:151]\n" \
"v_exp_f32 v141, v141\n" \
"v_mfma_f32_16x16x32_bf16 v[152:155], a[160:163], v[176:179], 0\n" \
"buffer_load_dwordx4 v248, s[32:35], s89 offen offset:1024 sc1 lds\n" \
"v_mfma_f32_16x16x32_bf16 v[152:155], a[164:167], v[180:183], v[152:155]\n" \
"v_exp_f32 v142, v142\n" \
"v_mfma_f32_16x16x32_bf16 v[152:155], a[168:171], v[184:187], v[152:155]\n" \
"ds_read_b128 a[216:219], v242 offset:6144\n" \
"v_cvt_pk_bf16_f32 v138, v140, v141\n" \
"v_mfma_f32_16x16x32_bf16 v[152:155], a[172:175], v[188:191], v[152:155]\n" \
"v_exp_f32 v143, v143\n" \
"v_mfma_f32_16x16x32_bf16 v[156:159], a[188:191], v[188:191], 0\n" \
"ds_read_b128 a[128:131], v240 offset:0\n" \
"v_cvt_pk_bf16_f32 v139, v142, v143\n" \
"v_mfma_f32_16x16x32_bf16 v[156:159], a[184:187], v[184:187], v[156:159]\n" \
"ds_read_b128 a[132:135], v240 offset:1024\n" \
"ds_read_b128 a[220:223], v242 offset:7168\n" \
"v_mfma_f32_16x16x32_bf16 v[156:159], a[180:183], v[180:183], v[156:159]\n" \
"v_exp_f32 v144, v144\n" \
"v_mfma_f32_16x16x32_bf16 v[156:159], a[176:179], v[176:179], v[156:159]\n" \
"buffer_load_dwordx4 v248, s[32:35], s89 offen offset:2048 sc1 lds\n" \
"v_mfma_f32_16x16x32_bf16 a[40:43], a[48:51], v[128:131], a[40:43]\n" \
"v_exp_f32 v145, v145\n" \
"v_mfma_f32_16x16x32_bf16 a[40:43], a[48:51], v[136:139], a[40:43]\n" \
"s_waitcnt lgkmcnt(13)\n" \
"ds_read_b128 a[136:139], v240 offset:2048\n" \
"v_cvt_pk_bf16_f32 v144, v144, v145\n" \
"v_mfma_f32_16x16x32_bf16 v[64:67], a[224:227], v[136:139], v[64:67]\n" \
"v_exp_f32 v146, v146\n" \
"v_mfma_f32_16x16x32_bf16 v[64:67], a[192:195], v[128:131], v[64:67]\n" \
".Lst1_2_%=:\n" \
"s_cbranch_execz .Lst1_6_%=\n" \
"ds_read_b128 a[252:255], v242 offset:15360\n" \
"ds_read_b128 a[140:143], v240 offset:3072\n" \
"v_mfma_f32_16x16x32_bf16 v[68:71], a[196:199], v[128:131], v[68:71]\n" \
"v_exp_f32 v147, v147\n" \
"v_mfma_f32_16x16x32_bf16 v[68:71], a[228:231], v[136:139], v[68:71]\n" \
"s_waitcnt lgkmcnt(12)\n" \
"buffer_load_dwordx4 v248, s[32:35], s89 offen offset:3072 sc1 lds\n" \
"v_mfma_f32_16x16x32_bf16 v[72:75], a[232:235], v[136:139], v[72:75]\n" \
"s_add_u32 s20, s20, 0x4000\n" \
"v_exp_f32 v148, v148\n" \
"v_mfma_f32_16x16x32_bf16 v[72:75], a[200:203], v[128:131], v[72:75]\n" \
"s_and_b32 s20, s20, 0xffff\n" \
"ds_read_b128 a[156:159], v240 offset:7168\n" \
"v_cvt_pk_bf16_f32 v145, v146, v147\n" \
"v_mfma_f32_16x16x32_bf16 v[76:79], a[204:207], v[128:131], v[76:79]\n" \
"s_bitset1_b32 s20, 16\n" \
"v_exp_f32 v149, v149\n" \
"v_mfma_f32_16x16x32_bf16 v[76:79], a[236:239], v[136:139], v[76:79]\n" \
"s_waitcnt lgkmcnt(9)\n" \
"ds_read_b128 a[152:155], v240 offset:6144\n" \
"v_cvt_pk_bf16_f32 v146, v148, v149\n" \
"v_mfma_f32_16x16x32_bf16 v[80:83], a[240:243], v[136:139], v[80:83]\n" \
"v_exp_f32 v150, v150\n" \
"v_mfma_f32_16x16x32_bf16 v[80:83], a[208:211], v[128:131], v[80:83]\n" \
"ds_read_b128 a[148:151], v240 offset:5120\n" \
"ds_read_b128 a[144:147], v240 offset:4096\n" \
"v_mfma_f32_16x16x32_bf16 v[84:87], a[212:215], v[128:131], v[84:87]\n" \
"s_sub_u32 s95, s95, 1\n" \
"v_exp_f32 v151, v151\n" \
"v_mfma_f32_16x16x32_bf16 v[84:87], a[244:247], v[136:139], v[84:87]\n" \
"s_waitcnt lgkmcnt(5)\n" \
"ds_read_b128 a[160:163], v240 offset:8192\n" \
"v_cvt_pk_bf16_f32 v147, v150, v151\n" \
"v_mfma_f32_16x16x32_bf16 v[88:91], a[248:251], v[136:139], v[88:91]\n" \
"ds_read_b128 a[164:167], v240 offset:9216\n" \
"ds_read_b128 a[168:171], v240 offset:10240\n" \
"v_mfma_f32_16x16x32_bf16 v[88:91], a[216:219], v[128:131], v[88:91]\n" \
"v_exp_f32 v152, v152\n" \
"v_mfma_f32_16x16x32_bf16 v[92:95], a[220:223], v[128:131], v[92:95]\n" \
"s_waitcnt lgkmcnt(7)\n" \
"ds_read_b128 a[172:175], v240 offset:11264\n" \
"v_exp_f32 v153, v153\n" \
"v_mfma_f32_16x16x32_bf16 v[92:95], a[252:255], v[136:139], v[92:95]\n" \
"s_cbranch_scc0 .Lloop3_%=\n" \
".Le1_%=:\n" \
".Lst1_3_%=:\n" \
"s_cbranch_execz .Lst1_7_%=\n" \
  \
"v_mfma_f32_16x16x32_bf16 v[128:131], a[128:131], v[160:163], 0\n" \
"s_add_u32 s86, s86, 0x4000\n" \
"v_and_b32 v255, 3, v251\n" \
"v_lshlrev_b32 v255, 2, v255\n" \
"v_mfma_f32_16x16x32_bf16 v[128:131], a[132:135], v[164:167], v[128:131]\n" \
"s_bitset0_b32 s86, 16\n" \
"v_exp_f32 v154, v154\n" \
"v_mfma_f32_16x16x32_bf16 v[128:131], a[136:139], v[168:171], v[128:131]\n" \
"buffer_load_dword v247, v255, s[32:35], s31 offen sc1\n" \
"v_add_u32 v242, 0x10000, v240\n" \
"v_mfma_f32_16x16x32_bf16 v[128:131], a[140:143], v[172:175], v[128:131]\n" \
"s_waitcnt lgkmcnt(4)\n" \
"v_exp_f32 v155, v155\n" \
"v_mfma_f32_16x16x32_bf16 v[132:135], a[156:159], v[172:175], 0\n" \
"s_mov_b32 m0, s19\n" \
"v_cvt_pk_bf16_f32 v152, v152, v153\n" \
"v_cvt_pk_bf16_f32 v153, v154, v155\n" \
"v_mfma_f32_16x16x32_bf16 v[132:135], a[152:155], v[168:171], v[132:135]\n" \
"ds_read_b128 a[188:191], v240 offset:15360\n" \
"ds_read_b128 a[184:187], v240 offset:14336\n" \
"ds_read_b128 a[180:183], v240 offset:13312\n" \
"ds_read_b128 a[176:179], v240 offset:12288\n" \
"v_exp_f32 v156, v156\n" \
"v_mfma_f32_16x16x32_bf16 v[132:135], a[148:151], v[164:167], v[132:135]\n" \
"buffer_load_dwordx4 v248, s[32:35], s88 offen offset:0 sc1 lds\n" \
"v_mfma_f32_16x16x32_bf16 v[132:135], a[144:147], v[160:163], v[132:135]\n" \
"s_waitcnt lgkmcnt(4)\n" \
"v_exp_f32 v157, v157\n" \
"v_mfma_f32_16x16x32_bf16 v[136:139], a[160:163], v[160:163], 0\n" \
"v_cvt_pk_bf16_f32 v154, v156, v157\n" \
"v_mfma_f32_16x16x32_bf16 v[136:139], a[164:167], v[164:167], v[136:139]\n" \
"v_exp_f32 v158, v158\n" \
"v_mfma_f32_16x16x32_bf16 v[136:139], a[168:171], v[168:171], v[136:139]\n" \
"buffer_load_dwordx4 v248, s[32:35], s88 offen offset:1024 sc1 lds\n" \
"v_mfma_f32_16x16x32_bf16 v[136:139], a[172:175], v[172:175], v[136:139]\n" \
"s_waitcnt lgkmcnt(0)\n" \
"v_exp_f32 v159, v159\n" \
"v_mfma_f32_16x16x32_bf16 v[140:143], a[188:191], v[172:175], 0\n" \
"v_add_u32 v240, s86, v248\n" \
"v_cvt_pk_bf16_f32 v155, v158, v159\n" \
"v_mfma_f32_16x16x32_bf16 v[140:143], a[184:187], v[168:171], v[140:143]\n" \
"buffer_load_dwordx4 v248, s[32:35], s88 offen offset:2048 sc1 lds\n" \
"v_mfma_f32_16x16x32_bf16 v[140:143], a[180:183], v[164:167], v[140:143]\n" \
"v_exp_f32 v128, v128\n" \
"v_mfma_f32_16x16x32_bf16 v[140:143], a[176:179], v[160:163], v[140:143]\n" \
"buffer_load_dwordx4 v248, s[32:35], s88 offen offset:3072 sc1 lds\n" \
"v_mfma_f32_16x16x32_bf16 a[44:47], a[48:51], v[144:147], a[44:47]\n" \
"s_add_u32 s89, s88, 589824\n" \
"v_exp_f32 v129, v129\n" \
"v_mfma_f32_16x16x32_bf16 a[44:47], a[48:51], v[152:155], a[44:47]\n" \
"s_add_u32 s88, s88, s27\n" \
"s_add_u32 s19, s19, 0x4000\n" \
"v_cvt_pk_bf16_f32 v128, v128, v129\n" \
"v_mfma_f32_16x16x32_bf16 v[96:99], a[224:227], v[152:155], v[96:99]\n" \
"s_bitset0_b32 s19, 16\n" \
"v_exp_f32 v130, v130\n" \
"v_mfma_f32_16x16x32_bf16 v[96:99], a[192:195], v[144:147], v[96:99]\n" \
"s_cmp_eq_u32 s88, s16\n" \
"s_cmov_b32 s88, s21\n" \
"ds_read_b128 a[224:227], v242 offset:8192\n" \
"v_mfma_f32_16x16x32_bf16 v[100:103], a[196:199], v[144:147], v[100:103]\n" \
"s_cmov_b32 s27, s91\n" \
"v_exp_f32 v131, v131\n" \
"v_mfma_f32_16x16x32_bf16 v[100:103], a[228:231], v[152:155], v[100:103]\n" \
"s_cmov_b32 s16, s77\n" \
"ds_read_b128 a[192:195], v242 offset:0\n" \
"v_cvt_pk_bf16_f32 v129, v130, v131\n" \
"v_mfma_f32_16x16x32_bf16 v[104:107], a[232:235], v[152:155], v[104:107]\n" \
"s_cmov_b32 s21, s92\n" \
"v_exp_f32 v132, v132\n" \
"v_mfma_f32_16x16x32_bf16 v[104:107], a[200:203], v[144:147], v[104:107]\n" \
"s_cmovk_i32 s91, 0x4000\n" \
"ds_read_b128 a[196:199], v242 offset:1024\n" \
"ds_read_b128 a[228:231], v242 offset:9216\n" \
"v_mfma_f32_16x16x32_bf16 v[108:111], a[204:207], v[144:147], v[108:111]\n" \
"v_exp_f32 v133, v133\n" \
"v_mfma_f32_16x16x32_bf16 v[108:111], a[236:239], v[152:155], v[108:111]\n" \
"ds_read_b128 a[232:235], v242 offset:10240\n" \
"v_cvt_pk_bf16_f32 v130, v132, v133\n" \
"v_mfma_f32_16x16x32_bf16 v[112:115], a[240:243], v[152:155], v[112:115]\n" \
"v_exp_f32 v134, v134\n" \
"v_mfma_f32_16x16x32_bf16 v[112:115], a[208:211], v[144:147], v[112:115]\n" \
"ds_read_b128 a[200:203], v242 offset:2048\n" \
"v_mfma_f32_16x16x32_bf16 v[116:119], a[212:215], v[144:147], v[116:119]\n" \
"v_exp_f32 v135, v135\n" \
"v_mfma_f32_16x16x32_bf16 v[116:119], a[244:247], v[152:155], v[116:119]\n" \
"ds_read_b128 a[204:207], v242 offset:3072\n" \
"v_cvt_pk_bf16_f32 v131, v134, v135\n" \
"v_mfma_f32_16x16x32_bf16 v[120:123], a[248:251], v[152:155], v[120:123]\n" \
"s_waitcnt vmcnt(4)\n" \
"ds_read_b128 a[236:239], v242 offset:11264\n" \
"v_cmp_eq_u32 vcc, 0, v247\n" \
"v_mfma_f32_16x16x32_bf16 v[120:123], a[216:219], v[144:147], v[120:123]\n" \
"v_exp_f32 v136, v136\n" \
"v_mfma_f32_16x16x32_bf16 v[124:127], a[220:223], v[144:147], v[124:127]\n" \
"ds_read_b128 a[240:243], v242 offset:12288\n" \
"v_mfma_f32_16x16x32_bf16 v[124:127], a[252:255], v[152:155], v[124:127]\n" \
"s_waitcnt vmcnt(13)\n" \
"v_exp_f32 v137, v137\n" \
"v_mfma_f32_16x16x32_bf16 v[144:147], a[128:131], v[176:179], 0\n" \
"s_barrier\n" \
"s_cbranch_vccnz .Lspl_%=\n" \
".Lsbl_%=:\n" \
"v_mfma_f32_16x16x32_bf16 v[144:147], a[132:135], v[180:183], v[144:147]\n" \
"s_mov_b32 m0, s20\n" \
"v_exp_f32 v138, v138\n" \
"v_mfma_f32_16x16x32_bf16 v[144:147], a[136:139], v[184:187], v[144:147]\n" \
"s_add_u32 s38, s66, 1\n" \
"buffer_load_dwordx4 v248, s[32:35], s89 offen offset:0 sc1 lds\n" \
"v_mfma_f32_16x16x32_bf16 v[144:147], a[140:143], v[188:191], v[144:147]\n" \
"s_and_b32 s38, s38, 3\n" \
"v_exp_f32 v139, v139\n" \
"v_mfma_f32_16x16x32_bf16 v[148:151], a[156:159], v[188:191], 0\n" \
"s_lshl_b32 s38, s38, 13\n" \
"v_cvt_pk_bf16_f32 v136, v136, v137\n" \
"v_cvt_pk_bf16_f32 v137, v138, v139\n" \
"v_mfma_f32_16x16x32_bf16 v[148:151], a[152:155], v[184:187], v[148:151]\n" \
"s_add_u32 s38, s38, s29\n" \
"v_exp_f32 v140, v140\n" \
"v_mfma_f32_16x16x32_bf16 v[148:151], a[148:151], v[180:183], v[148:151]\n" \
"buffer_load_dwordx4 v[32:35], v248, s[32:35], s38 offen offset:0 sc1\n" \
"v_mfma_f32_16x16x32_bf16 v[148:151], a[144:147], v[176:179], v[148:151]\n" \
"v_exp_f32 v141, v141\n" \
"v_mfma_f32_16x16x32_bf16 v[152:155], a[160:163], v[176:179], 0\n" \
"buffer_load_dwordx4 v248, s[32:35], s89 offen offset:1024 sc1 lds\n" \
"v_mfma_f32_16x16x32_bf16 v[152:155], a[164:167], v[180:183], v[152:155]\n" \
"v_exp_f32 v142, v142\n" \
"v_mfma_f32_16x16x32_bf16 v[152:155], a[168:171], v[184:187], v[152:155]\n" \
"ds_read_b128 a[208:211], v242 offset:4096\n" \
"v_cvt_pk_bf16_f32 v138, v140, v141\n" \
"v_mfma_f32_16x16x32_bf16 v[152:155], a[172:175], v[188:191], v[152:155]\n" \
"v_exp_f32 v143, v143\n" \
"v_mfma_f32_16x16x32_bf16 v[156:159], a[188:191], v[188:191], 0\n" \
"buffer_load_dwordx4 v[36:39], v248, s[32:35], s38 offen offset:1024 sc1\n" \
"v_cvt_pk_bf16_f32 v139, v142, v143\n" \
"v_mfma_f32_16x16x32_bf16 v[156:159], a[184:187], v[184:187], v[156:159]\n" \
"ds_read_b128 a[212:215], v242 offset:5120\n" \
"ds_read_b128 a[244:247], v242 offset:13312\n" \
"v_mfma_f32_16x16x32_bf16 v[156:159], a[180:183], v[180:183], v[156:159]\n" \
"v_exp_f32 v144, v144\n" \
"v_mfma_f32_16x16x32_bf16 v[156:159], a[176:179], v[176:179], v[156:159]\n" \
"buffer_load_dwordx4 v248, s[32:35], s89 offen offset:2048 sc1 lds\n" \
"v_mfma_f32_16x16x32_bf16 a[40:43], a[48:51], v[128:131], a[40:43]\n" \
"v_exp_f32 v145, v145\n" \
"v_mfma_f32_16x16x32_bf16 a[40:43], a[48:51], v[136:139], a[40:43]\n" \
"s_waitcnt lgkmcnt(8)\n" \
"ds_read_b128 a[248:251], v242 offset:14336\n" \
"v_cvt_pk_bf16_f32 v144, v144, v145\n" \
"v_mfma_f32_16x16x32_bf16 v[64:67], a[224:227], v[136:139], v[64:67]\n" \
"v_exp_f32 v146, v146\n" \
"v_mfma_f32_16x16x32_bf16 v[64:67], a[192:195], v[128:131], v[64:67]\n" \
"ds_read_b128 a[216:219], v242 offset:6144\n" \
"ds_read_b128 a[128:131], v240 offset:0\n" \
"v_mfma_f32_16x16x32_bf16 v[68:71], a[196:199], v[128:131], v[68:71]\n" \
"v_exp_f32 v147, v147\n" \
"v_mfma_f32_16x16x32_bf16 v[68:71], a[228:231], v[136:139], v[68:71]\n" \
"s_waitcnt lgkmcnt(7)\n" \
"buffer_load_dwordx4 v248, s[32:35], s89 offen offset:3072 sc1 lds\n" \
"v_mfma_f32_16x16x32_bf16 v[72:75], a[232:235], v[136:139], v[72:75]\n" \
"s_add_u32 s20, s20, 0x4000\n" \
"v_exp_f32 v148, v148\n" \
"v_mfma_f32_16x16x32_bf16 v[72:75], a[200:203], v[128:131], v[72:75]\n" \
"s_and_b32 s20, s20, 0xffff\n" \
"buffer_load_dwordx4 v[40:43], v248, s[32:35], s38 offen offset:2048 sc1\n" \
"ds_read_b128 a[132:135], v240 offset:1024\n" \
"ds_read_b128 a[220:223], v242 offset:7168\n" \
"ds_read_b128 a[136:139], v240 offset:2048\n" \
"ds_read_b128 a[252:255], v242 offset:15360\n" \
"v_cvt_pk_bf16_f32 v145, v146, v147\n" \
"v_mfma_f32_16x16x32_bf16 v[76:79], a[204:207], v[128:131], v[76:79]\n" \
"s_bitset1_b32 s20, 16\n" \
"v_exp_f32 v149, v149\n" \
"v_mfma_f32_16x16x32_bf16 v[76:79], a[236:239], v[136:139], v[76:79]\n" \
"s_waitcnt lgkmcnt(7)\n" \
"ds_read_b128 a[140:143], v240 offset:3072\n" \
"v_cvt_pk_bf16_f32 v146, v148, v149\n" \
"v_mfma_f32_16x16x32_bf16 v[80:83], a[240:243], v[136:139], v[80:83]\n" \
"v_exp_f32 v150, v150\n" \
"v_mfma_f32_16x16x32_bf16 v[80:83], a[208:211], v[128:131], v[80:83]\n" \
"ds_read_b128 a[156:159], v240 offset:7168\n" \
"ds_read_b128 a[152:155], v240 offset:6144\n" \
"v_mfma_f32_16x16x32_bf16 v[84:87], a[212:215], v[128:131], v[84:87]\n" \
"v_exp_f32 v151, v151\n" \
"v_mfma_f32_16x16x32_bf16 v[84:87], a[244:247], v[136:139], v[84:87]\n" \
"s_waitcnt lgkmcnt(3)\n" \
"buffer_load_dwordx4 v[44:47], v248, s[32:35], s38 offen offset:3072 sc1\n" \
"v_cvt_pk_bf16_f32 v147, v150, v151\n" \
"v_mfma_f32_16x16x32_bf16 v[88:91], a[248:251], v[136:139], v[88:91]\n" \
"ds_read_b128 a[148:151], v240 offset:5120\n" \
"ds_read_b128 a[144:147], v240 offset:4096\n" \
"v_mfma_f32_16x16x32_bf16 v[88:91], a[216:219], v[128:131], v[88:91]\n" \
"v_exp_f32 v152, v152\n" \
"v_mfma_f32_16x16x32_bf16 v[92:95], a[220:223], v[128:131], v[92:95]\n" \
"ds_read_b128 a[160:163], v240 offset:8192\n" \
"ds_read_b128 a[164:167], v240 offset:9216\n" \
"v_mfma_f32_16x16x32_bf16 v[92:95], a[252:255], v[136:139], v[92:95]\n" \
"s_waitcnt lgkmcnt(6)\n" \
"v_exp_f32 v153, v153\n" \
"v_mfma_f32_16x16x32_bf16 v[128:131], a[128:131], v[160:163], 0\n" \
"s_add_u32 s86, s86, 0x4000\n" \
"ds_read_b128 a[168:171], v240 offset:10240\n" \
"v_cvt_pk_bf16_f32 v152, v152, v153\n" \
"v_mfma_f32_16x16x32_bf16 v[128:131], a[132:135], v[164:167], v[128:131]\n" \
"s_bitset0_b32 s86, 16\n" \
"ds_read_b128 a[172:175], v240 offset:11264\n" \
"v_exp_f32 v154, v154\n" \
"v_mfma_f32_16x16x32_bf16 v[128:131], a[136:139], v[168:171], v[128:131]\n" \
"s_lshl1_add_u32 s22, s66, 0\n" \
"ds_read_b128 a[188:191], v240 offset:15360\n" \
"v_add_u32 v242, 0x10000, v240\n" \
"v_mfma_f32_16x16x32_bf16 v[128:131], a[140:143], v[172:175], v[128:131]\n" \
"s_waitcnt lgkmcnt(5)\n" \
"v_exp_f32 v155, v155\n" \
"v_mfma_f32_16x16x32_bf16 v[132:135], a[156:159], v[172:175], 0\n" \
"s_and_b32 s22, s22, 7\n" \
"s_add_u32 s38, s38, 4096\n" \
"buffer_load_dwordx4 v[48:51], v248, s[32:35], s38 offen offset:0 sc1\n" \
"v_mfma_f32_16x16x32_bf16 v[132:135], a[152:155], v[168:171], v[132:135]\n" \
"s_lshl_b32 s22, s22, 16\n" \
"ds_read_b128 a[184:187], v240 offset:14336\n" \
"ds_read_b128 a[180:183], v240 offset:13312\n" \
"ds_read_b128 a[176:179], v240 offset:12288\n" \
"v_exp_f32 v156, v156\n" \
"v_mfma_f32_16x16x32_bf16 v[132:135], a[148:151], v[164:167], v[132:135]\n" \
"s_add_u32 s22, s22, s17\n" \
"s_mov_b32 m0, s19\n" \
"v_cvt_pk_bf16_f32 v153, v154, v155\n" \
"v_mfma_f32_16x16x32_bf16 v[132:135], a[144:147], v[160:163], v[132:135]\n" \
"s_waitcnt lgkmcnt(4)\n" \
"v_exp_f32 v157, v157\n" \
"v_mfma_f32_16x16x32_bf16 v[136:139], a[160:163], v[160:163], 0\n" \
"buffer_load_dwordx4 v237, s[48:51], s22 offen offset:0 lds\n" \
"v_mfma_f32_16x16x32_bf16 v[136:139], a[164:167], v[164:167], v[136:139]\n" \
"s_add_u32 s22, s22, 7168\n" \
"v_exp_f32 v158, v158\n" \
"v_mfma_f32_16x16x32_bf16 v[136:139], a[168:171], v[168:171], v[136:139]\n" \
"buffer_load_dwordx4 v237, s[48:51], s22 offen offset:1024 lds\n" \
"v_mfma_f32_16x16x32_bf16 v[136:139], a[172:175], v[172:175], v[136:139]\n" \
"s_waitcnt lgkmcnt(0)\n" \
"v_exp_f32 v159, v159\n" \
"v_mfma_f32_16x16x32_bf16 v[140:143], a[188:191], v[172:175], 0\n" \
"s_add_u32 s22, s22, 7168\n" \
"buffer_load_dwordx4 v[52:55], v248, s[32:35], s38 offen offset:1024 sc1\n" \
"v_add_u32 v240, s86, v248\n" \
"v_mfma_f32_16x16x32_bf16 v[140:143], a[184:187], v[168:171], v[140:143]\n" \
"buffer_load_dwordx4 v237, s[48:51], s22 offen offset:2048 lds\n" \
"v_mfma_f32_16x16x32_bf16 v[140:143], a[180:183], v[164:167], v[140:143]\n" \
"s_add_u32 s22, s22, 7168\n" \
"v_exp_f32 v128, v128\n" \
"v_mfma_f32_16x16x32_bf16 v[140:143], a[176:179], v[160:163], v[140:143]\n" \
"buffer_load_dwordx4 v237, s[48:51], s22 offen offset:3072 lds\n" \
"v_cvt_pk_bf16_f32 v154, v156, v157\n" \
"v_cvt_pk_bf16_f32 v155, v158, v159\n" \
"v_mfma_f32_16x16x32_bf16 a[44:47], a[48:51], v[144:147], a[44:47]\n" \
"s_add_u32 s19, s19, 0x4000\n" \
"v_exp_f32 v129, v129\n" \
"v_mfma_f32_16x16x32_bf16 a[44:47], a[48:51], v[152:155], a[44:47]\n" \
"s_bitset0_b32 s19, 16\n" \
"v_cvt_pk_bf16_f32 v128, v128, v129\n" \
"v_mfma_f32_16x16x32_bf16 v[96:99], a[224:227], v[152:155], v[96:99]\n" \
"v_exp_f32 v130, v130\n" \
"v_mfma_f32_16x16x32_bf16 v[96:99], a[192:195], v[144:147], v[96:99]\n" \
"buffer_load_dwordx4 v[56:59], v248, s[32:35], s38 offen offset:2048 sc1\n" \
"v_mfma_f32_16x16x32_bf16 v[100:103], a[196:199], v[144:147], v[100:103]\n" \
"v_exp_f32 v131, v131\n" \
"v_mfma_f32_16x16x32_bf16 v[100:103], a[228:231], v[152:155], v[100:103]\n" \
"ds_read_b128 a[224:227], v242 offset:8192\n" \
"v_cvt_pk_bf16_f32 v129, v130, v131\n" \
"v_mfma_f32_16x16x32_bf16 v[104:107], a[232:235], v[152:155], v[104:107]\n" \
"v_exp_f32 v132, v132\n" \
"v_mfma_f32_16x16x32_bf16 v[104:107], a[200:203], v[144:147], v[104:107]\n" \
"ds_read_b128 a[192:195], v242 offset:0\n" \
"ds_read_b128 a[196:199], v242 offset:1024\n" \
"v_mfma_f32_16x16x32_bf16 v[108:111], a[204:207], v[144:147], v[108:111]\n" \
"v_exp_f32 v133, v133\n" \
"v_mfma_f32_16x16x32_bf16 v[108:111], a[236:239], v[152:155], v[108:111]\n" \
"ds_read_b128 a[228:231], v242 offset:9216\n" \
"v_cvt_pk_bf16_f32 v130, v132, v133\n" \
"v_mfma_f32_16x16x32_bf16 v[112:115], a[240:243], v[152:155], v[112:115]\n" \
"v_exp_f32 v134, v134\n" \
"v_mfma_f32_16x16x32_bf16 v[112:115], a[208:211], v[144:147], v[112:115]\n" \
"buffer_load_dwordx4 v[60:63], v248, s[32:35], s38 offen offset:3072 sc1\n" \
"v_mfma_f32_16x16x32_bf16 v[116:119], a[212:215], v[144:147], v[116:119]\n" \
"v_exp_f32 v135, v135\n" \
"v_mfma_f32_16x16x32_bf16 v[116:119], a[244:247], v[152:155], v[116:119]\n" \
"ds_read_b128 a[232:235], v242 offset:10240\n" \
"v_cvt_pk_bf16_f32 v131, v134, v135\n" \
"v_mfma_f32_16x16x32_bf16 v[120:123], a[248:251], v[152:155], v[120:123]\n" \
"s_lshl1_add_u32 s22, s66, 2\n" \
"ds_read_b128 a[200:203], v242 offset:2048\n" \
"ds_read_b128 a[204:207], v242 offset:3072\n" \
"v_mfma_f32_16x16x32_bf16 v[120:123], a[216:219], v[144:147], v[120:123]\n" \
"s_and_b32 s22, s22, 7\n" \
"v_exp_f32 v136, v136\n" \
"v_mfma_f32_16x16x32_bf16 v[124:127], a[220:223], v[144:147], v[124:127]\n" \
"s_lshl_b32 s22, s22, 16\n" \
"ds_read_b128 a[236:239], v242 offset:11264\n" \
"ds_read_b128 a[240:243], v242 offset:12288\n" \
"v_mfma_f32_16x16x32_bf16 v[124:127], a[252:255], v[152:155], v[124:127]\n" \
"s_waitcnt vmcnt(21)\n" \
"v_exp_f32 v137, v137\n" \
"v_mfma_f32_16x16x32_bf16 v[144:147], a[128:131], v[176:179], 0\n" \
"s_barrier\n" \
"ds_read_b128 a[208:211], v242 offset:4096\n" \
"v_cvt_pk_bf16_f32 v136, v136, v137\n" \
"v_mfma_f32_16x16x32_bf16 v[144:147], a[132:135], v[180:183], v[144:147]\n" \
"s_add_u32 s22, s22, s17\n" \
"v_exp_f32 v138, v138\n" \
"v_mfma_f32_16x16x32_bf16 v[144:147], a[136:139], v[184:187], v[144:147]\n" \
"s_mov_b32 m0, s20\n" \
"s_add_u32 s38, s66, 2\n" \
"s_and_b32 s38, s38, 3\n" \
"s_lshl_b32 s38, s38, 13\n" \
"s_add_u32 s38, s38, s29\n" \
"v_mfma_f32_16x16x32_bf16 v[144:147], a[140:143], v[188:191], v[144:147]\n" \
"buffer_load_dwordx4 a[64:67], v248, s[32:35], s38 offen offset:0 sc1\n" \
"v_exp_f32 v139, v139\n" \
"v_mfma_f32_16x16x32_bf16 v[148:151], a[156:159], v[188:191], 0\n" \
"buffer_load_dwordx4 v237, s[48:51], s22 offen offset:0 lds\n" \
"v_mfma_f32_16x16x32_bf16 v[148:151], a[152:155], v[184:187], v[148:151]\n" \
"s_add_u32 s22, s22, 7168\n" \
"v_exp_f32 v140, v140\n" \
"v_mfma_f32_16x16x32_bf16 v[148:151], a[148:151], v[180:183], v[148:151]\n" \
"ds_read_b128 a[212:215], v242 offset:5120\n" \
"v_cvt_pk_bf16_f32 v137, v138, v139\n" \
"v_mfma_f32_16x16x32_bf16 v[148:151], a[144:147], v[176:179], v[148:151]\n" \
"v_exp_f32 v141, v141\n" \
"v_mfma_f32_16x16x32_bf16 v[152:155], a[160:163], v[176:179], 0\n" \
"buffer_load_dwordx4 v237, s[48:51], s22 offen offset:1024 lds\n" \
"v_mfma_f32_16x16x32_bf16 v[152:155], a[164:167], v[180:183], v[152:155]\n" \
"s_add_u32 s22, s22, 7168\n" \
"v_exp_f32 v142, v142\n" \
"v_mfma_f32_16x16x32_bf16 v[152:155], a[168:171], v[184:187], v[152:155]\n" \
"buffer_load_dwordx4 a[72:75], v248, s[32:35], s38 offen offset:1024 sc1\n" \
"v_cvt_pk_bf16_f32 v138, v140, v141\n" \
"v_mfma_f32_16x16x32_bf16 v[152:155], a[172:175], v[188:191], v[152:155]\n" \
"v_exp_f32 v143, v143\n" \
"v_mfma_f32_16x16x32_bf16 v[156:159], a[188:191], v[188:191], 0\n" \
"ds_read_b128 a[244:247], v242 offset:13312\n" \
"v_cvt_pk_bf16_f32 v139, v142, v143\n" \
"v_mfma_f32_16x16x32_bf16 v[156:159], a[184:187], v[184:187], v[156:159]\n" \
"ds_read_b128 a[248:251], v242 offset:14336\n" \
"ds_read_b128 a[216:219], v242 offset:6144\n" \
"v_mfma_f32_16x16x32_bf16 v[156:159], a[180:183], v[180:183], v[156:159]\n" \
"v_exp_f32 v144, v144\n" \
"v_mfma_f32_16x16x32_bf16 v[156:159], a[176:179], v[176:179], v[156:159]\n" \
"buffer_load_dwordx4 v237, s[48:51], s22 offen offset:2048 lds\n" \
"v_mfma_f32_16x16x32_bf16 a[40:43], a[48:51], v[128:131], a[40:43]\n" \
"s_add_u32 s22, s22, 7168\n" \
"v_exp_f32 v145, v145\n" \
"v_mfma_f32_16x16x32_bf16 a[40:43], a[48:51], v[136:139], a[40:43]\n" \
"s_waitcnt lgkmcnt(10)\n" \
"buffer_load_dwordx4 a[80:83], v248, s[32:35], s38 offen offset:2048 sc1\n" \
"v_cvt_pk_bf16_f32 v144, v144, v145\n" \
"v_mfma_f32_16x16x32_bf16 v[64:67], a[224:227], v[136:139], v[64:67]\n" \
"v_exp_f32 v146, v146\n" \
"v_mfma_f32_16x16x32_bf16 v[64:67], a[192:195], v[128:131], v[64:67]\n" \
"ds_read_b128 a[128:131], v240 offset:0\n" \
"ds_read_b128 a[132:135], v240 offset:1024\n" \
"v_mfma_f32_16x16x32_bf16 v[68:71], a[196:199], v[128:131], v[68:71]\n" \
"v_exp_f32 v147, v147\n" \
"v_mfma_f32_16x16x32_bf16 v[68:71], a[228:231], v[136:139], v[68:71]\n" \
"s_waitcnt lgkmcnt(8)\n" \
"buffer_load_dwordx4 v237, s[48:51], s22 offen offset:3072 lds\n" \
"v_mfma_f32_16x16x32_bf16 v[72:75], a[232:235], v[136:139], v[72:75]\n" \
"s_add_u32 s20, s20, 0x4000\n" \
"v_exp_f32 v148, v148\n" \
"v_mfma_f32_16x16x32_bf16 v[72:75], a[200:203], v[128:131], v[72:75]\n" \
"s_and_b32 s20, s20, 0xffff\n" \
"ds_read_b128 a[220:223], v242 offset:7168\n" \
"ds_read_b128 a[136:139], v240 offset:2048\n" \
"ds_read_b128 a[252:255], v242 offset:15360\n" \
"v_cvt_pk_bf16_f32 v145, v146, v147\n" \
"v_mfma_f32_16x16x32_bf16 v[76:79], a[204:207], v[128:131], v[76:79]\n" \
"s_bitset1_b32 s20, 16\n" \
"v_exp_f32 v149, v149\n" \
"v_mfma_f32_16x16x32_bf16 v[76:79], a[236:239], v[136:139], v[76:79]\n" \
"s_waitcnt lgkmcnt(7)\n" \
"buffer_load_dwordx4 a[88:91], v248, s[32:35], s38 offen offset:3072 sc1\n" \
"v_cvt_pk_bf16_f32 v146, v148, v149\n" \
"v_mfma_f32_16x16x32_bf16 v[80:83], a[240:243], v[136:139], v[80:83]\n" \
"v_exp_f32 v150, v150\n" \
"v_mfma_f32_16x16x32_bf16 v[80:83], a[208:211], v[128:131], v[80:83]\n" \
"ds_read_b128 a[140:143], v240 offset:3072\n" \
"ds_read_b128 a[156:159], v240 offset:7168\n" \
"v_mfma_f32_16x16x32_bf16 v[84:87], a[212:215], v[128:131], v[84:87]\n" \
"v_exp_f32 v151, v151\n" \
"v_mfma_f32_16x16x32_bf16 v[84:87], a[244:247], v[136:139], v[84:87]\n" \
"s_waitcnt lgkmcnt(2)\n" \
"ds_read_b128 a[152:155], v240 offset:6144\n" \
"v_cvt_pk_bf16_f32 v147, v150, v151\n" \
"v_mfma_f32_16x16x32_bf16 v[88:91], a[248:251], v[136:139], v[88:91]\n" \
"ds_read_b128 a[148:151], v240 offset:5120\n" \
"ds_read_b128 a[144:147], v240 offset:4096\n" \
"v_mfma_f32_16x16x32_bf16 v[88:91], a[216:219], v[128:131], v[88:91]\n" \
"v_exp_f32 v152, v152\n" \
"v_mfma_f32_16x16x32_bf16 v[92:95], a[220:223], v[128:131], v[92:95]\n" \
"v_exp_f32 v153, v153\n" \
"v_mfma_f32_16x16x32_bf16 v[92:95], a[252:255], v[136:139], v[92:95]\n" \
"s_waitcnt lgkmcnt(4)\n" \
"ds_read_b128 a[160:163], v240 offset:8192\n" \
"ds_read_b128 a[164:167], v240 offset:9216\n" \
"ds_read_b128 a[168:171], v240 offset:10240\n" \
"ds_read_b128 a[172:175], v240 offset:11264\n" \
".Llast_%=:\n" \
".Lst1_4_%=:\n" \
"s_cbranch_execz .Lst1_8_%=\n" \
  \
"v_mfma_f32_16x16x32_bf16 v[128:131], a[128:131], v[160:163], 0\n" \
"s_add_u32 s86, s86, 0x4000\n" \
"ds_read_b128 a[188:191], v240 offset:15360\n" \
"v_cvt_pk_bf16_f32 v152, v152, v153\n" \
"v_mfma_f32_16x16x32_bf16 v[128:131], a[132:135], v[164:167], v[128:131]\n" \
"s_bitset0_b32 s86, 16\n" \
"v_exp_f32 v154, v154\n" \
"v_mfma_f32_16x16x32_bf16 v[128:131], a[136:139], v[168:171], v[128:131]\n" \
"s_lshl1_add_u32 s22, s66, 1\n" \
"ds_read_b128 a[184:187], v240 offset:14336\n" \
"v_add_u32 v242, 0x10000, v240\n" \
"v_mfma_f32_16x16x32_bf16 v[128:131], a[140:143], v[172:175], v[128:131]\n" \
"s_waitcnt lgkmcnt(6)\n" \
"v_exp_f32 v155, v155\n" \
"v_mfma_f32_16x16x32_bf16 v[132:135], a[156:159], v[172:175], 0\n" \
"s_and_b32 s22, s22, 7\n" \
"ds_read_b128 a[180:183], v240 offset:13312\n" \
"v_cvt_pk_bf16_f32 v153, v154, v155\n" \
"v_mfma_f32_16x16x32_bf16 v[132:135], a[152:155], v[168:171], v[132:135]\n" \
"s_lshl_b32 s22, s22, 16\n" \
"ds_read_b128 a[176:179], v240 offset:12288\n" \
"v_exp_f32 v156, v156\n" \
"v_mfma_f32_16x16x32_bf16 v[132:135], a[148:151], v[164:167], v[132:135]\n" \
"s_add_u32 s22, s22, s17\n" \
"s_mov_b32 m0, s19\n" \
"v_mfma_f32_16x16x32_bf16 v[132:135], a[144:147], v[160:163], v[132:135]\n" \
"s_waitcnt lgkmcnt(4)\n" \
"v_exp_f32 v157, v157\n" \
"v_mfma_f32_16x16x32_bf16 v[136:139], a[160:163], v[160:163], 0\n" \
"buffer_load_dwordx4 v237, s[48:51], s22 offen offset:0 lds\n" \
"v_mfma_f32_16x16x32_bf16 v[136:139], a[164:167], v[164:167], v[136:139]\n" \
"s_add_u32 s22, s22, 7168\n" \
"v_exp_f32 v158, v158\n" \
"v_mfma_f32_16x16x32_bf16 v[136:139], a[168:171], v[168:171], v[136:139]\n" \
"buffer_load_dwordx4 v237, s[48:51], s22 offen offset:1024 lds\n" \
"v_mfma_f32_16x16x32_bf16 v[136:139], a[172:175], v[172:175], v[136:139]\n" \
"s_waitcnt lgkmcnt(0)\n" \
"v_exp_f32 v159, v159\n" \
"v_mfma_f32_16x16x32_bf16 v[140:143], a[188:191], v[172:175], 0\n" \
"s_add_u32 s22, s22, 7168\n" \
"v_add_u32 v240, s86, v248\n" \
"v_cvt_pk_bf16_f32 v154, v156, v157\n" \
"v_mfma_f32_16x16x32_bf16 v[140:143], a[184:187], v[168:171], v[140:143]\n" \
"buffer_load_dwordx4 v237, s[48:51], s22 offen offset:2048 lds\n" \
"v_mfma_f32_16x16x32_bf16 v[140:143], a[180:183], v[164:167], v[140:143]\n" \
"s_add_u32 s22, s22, 7168\n" \
"v_exp_f32 v128, v128\n" \
"v_mfma_f32_16x16x32_bf16 v[140:143], a[176:179], v[160:163], v[140:143]\n" \
"buffer_load_dwordx4 v237, s[48:51], s22 offen offset:3072 lds\n" \
"v_cvt_pk_bf16_f32 v155, v158, v159\n" \
"v_mfma_f32_16x16x32_bf16 a[44:47], a[48:51], v[144:147], a[44:47]\n" \
"s_add_u32 s19, s19, 0x4000\n" \
"v_exp_f32 v129, v129\n" \
"v_mfma_f32_16x16x32_bf16 a[44:47], a[48:51], v[152:155], a[44:47]\n" \
"s_bitset0_b32 s19, 16\n" \
"s_add_u32 s38, s38, 4096\n" \
"v_cvt_pk_bf16_f32 v128, v128, v129\n" \
"v_mfma_f32_16x16x32_bf16 v[96:99], a[224:227], v[152:155], v[96:99]\n" \
"v_exp_f32 v130, v130\n" \
"v_mfma_f32_16x16x32_bf16 v[96:99], a[192:195], v[144:147], v[96:99]\n" \
"ds_read_b128 a[224:227], v242 offset:8192\n" \
"v_mfma_f32_16x16x32_bf16 v[100:103], a[196:199], v[144:147], v[100:103]\n" \
"v_exp_f32 v131, v131\n" \
"v_mfma_f32_16x16x32_bf16 v[100:103], a[228:231], v[152:155], v[100:103]\n" \
"ds_read_b128 a[192:195], v242 offset:0\n" \
"buffer_load_dwordx4 a[96:99], v248, s[32:35], s38 offen offset:0 sc1\n" \
"v_cvt_pk_bf16_f32 v129, v130, v131\n" \
"v_mfma_f32_16x16x32_bf16 v[104:107], a[232:235], v[152:155], v[104:107]\n" \
"v_exp_f32 v132, v132\n" \
"v_mfma_f32_16x16x32_bf16 v[104:107], a[200:203], v[144:147], v[104:107]\n" \
"ds_read_b128 a[196:199], v242 offset:1024\n" \
"ds_read_b128 a[228:231], v242 offset:9216\n" \
"v_mfma_f32_16x16x32_bf16 v[108:111], a[204:207], v[144:147], v[108:111]\n" \
"v_exp_f32 v133, v133\n" \
"v_mfma_f32_16x16x32_bf16 v[108:111], a[236:239], v[152:155], v[108:111]\n" \
"ds_read_b128 a[232:235], v242 offset:10240\n" \
"v_cvt_pk_bf16_f32 v130, v132, v133\n" \
"v_mfma_f32_16x16x32_bf16 v[112:115], a[240:243], v[152:155], v[112:115]\n" \
"v_exp_f32 v134, v134\n" \
"v_mfma_f32_16x16x32_bf16 v[112:115], a[208:211], v[144:147], v[112:115]\n" \
"ds_read_b128 a[200:203], v242 offset:2048\n" \
"v_mfma_f32_16x16x32_bf16 v[116:119], a[212:215], v[144:147], v[116:119]\n" \
"v_exp_f32 v135, v135\n" \
"v_mfma_f32_16x16x32_bf16 v[116:119], a[244:247], v[152:155], v[116:119]\n" \
"ds_read_b128 a[204:207], v242 offset:3072\n" \
"v_cvt_pk_bf16_f32 v131, v134, v135\n" \
"v_mfma_f32_16x16x32_bf16 v[120:123], a[248:251], v[152:155], v[120:123]\n" \
"s_lshl1_add_u32 s22, s66, 3\n" \
"s_and_b32 s22, s22, 7\n" \
"ds_read_b128 a[236:239], v242 offset:11264\n" \
"v_mfma_f32_16x16x32_bf16 v[120:123], a[216:219], v[144:147], v[120:123]\n" \
"s_lshl_b32 s22, s22, 16\n" \
"v_exp_f32 v136, v136\n" \
"v_mfma_f32_16x16x32_bf16 v[124:127], a[220:223], v[144:147], v[124:127]\n" \
"s_add_u32 s22, s22, s17\n" \
"ds_read_b128 a[240:243], v242 offset:12288\n" \
"v_mfma_f32_16x16x32_bf16 v[124:127], a[252:255], v[152:155], v[124:127]\n" \
"s_waitcnt vmcnt(13)\n" \
"v_exp_f32 v137, v137\n" \
"v_mfma_f32_16x16x32_bf16 v[144:147], a[128:131], v[176:179], 0\n" \
"s_barrier\n" \
"ds_read_b128 a[208:211], v242 offset:4096\n" \
"v_cvt_pk_bf16_f32 v136, v136, v137\n" \
"v_mfma_f32_16x16x32_bf16 v[144:147], a[132:135], v[180:183], v[144:147]\n" \
"s_mov_b32 m0, s20\n" \
"buffer_load_dwordx4 a[104:107], v248, s[32:35], s38 offen offset:1024 sc1\n" \
"v_exp_f32 v138, v138\n" \
"v_mfma_f32_16x16x32_bf16 v[144:147], a[136:139], v[184:187], v[144:147]\n" \
"buffer_load_dwordx4 v237, s[48:51], s22 offen offset:0 lds\n" \
"v_mfma_f32_16x16x32_bf16 v[144:147], a[140:143], v[188:191], v[144:147]\n" \
"s_add_u32 s22, s22, 7168\n" \
"v_exp_f32 v139, v139\n" \
"v_mfma_f32_16x16x32_bf16 v[148:151], a[156:159], v[188:191], 0\n" \
"ds_read_b128 a[212:215], v242 offset:5120\n" \
"v_cvt_pk_bf16_f32 v137, v138, v139\n" \
"v_mfma_f32_16x16x32_bf16 v[148:151], a[152:155], v[184:187], v[148:151]\n" \
"v_exp_f32 v140, v140\n" \
"v_mfma_f32_16x16x32_bf16 v[148:151], a[148:151], v[180:183], v[148:151]\n" \
"ds_read_b128 a[244:247], v242 offset:13312\n" \
"ds_read_b128 a[248:251], v242 offset:14336\n" \
"v_mfma_f32_16x16x32_bf16 v[148:151], a[144:147], v[176:179], v[148:151]\n" \
"v_exp_f32 v141, v141\n" \
"v_mfma_f32_16x16x32_bf16 v[152:155], a[160:163], v[176:179], 0\n" \
"buffer_load_dwordx4 v237, s[48:51], s22 offen offset:1024 lds\n" \
"v_mfma_f32_16x16x32_bf16 v[152:155], a[164:167], v[180:183], v[152:155]\n" \
"s_add_u32 s22, s22, 7168\n" \
"v_exp_f32 v142, v142\n" \
"v_mfma_f32_16x16x32_bf16 v[152:155], a[168:171], v[184:187], v[152:155]\n" \
"ds_read_b128 a[216:219], v242 offset:6144\n" \
"v_cvt_pk_bf16_f32 v138, v140, v141\n" \
"v_mfma_f32_16x16x32_bf16 v[152:155], a[172:175], v[188:191], v[152:155]\n" \
"v_exp_f32 v143, v143\n" \
"v_mfma_f32_16x16x32_bf16 v[156:159], a[188:191], v[188:191], 0\n" \
"ds_read_b128 a[128:131], v240 offset:0\n" \
"v_cvt_pk_bf16_f32 v139, v142, v143\n" \
"v_mfma_f32_16x16x32_bf16 v[156:159], a[184:187], v[184:187], v[156:159]\n" \
"ds_read_b128 a[132:135], v240 offset:1024\n" \
"ds_read_b128 a[220:223], v242 offset:7168\n" \
"v_mfma_f32_16x16x32_bf16 v[156:159], a[180:183], v[180:183], v[156:159]\n" \
"v_exp_f32 v144, v144\n" \
"v_mfma_f32_16x16x32_bf16 v[156:159], a[176:179], v[176:179], v[156:159]\n" \
"buffer_load_dwordx4 v237, s[48:51], s22 offen offset:2048 lds\n" \
"buffer_load_dwordx4 a[112:115], v248, s[32:35], s38 offen offset:2048 sc1\n" \
"v_mfma_f32_16x16x32_bf16 a[40:43], a[48:51], v[128:131], a[40:43]\n" \
"s_add_u32 s22, s22, 7168\n" \
"v_exp_f32 v145, v145\n" \
"v_mfma_f32_16x16x32_bf16 a[40:43], a[48:51], v[136:139], a[40:43]\n" \
"s_waitcnt lgkmcnt(13)\n" \
"ds_read_b128 a[136:139], v240 offset:2048\n" \
"v_cvt_pk_bf16_f32 v144, v144, v145\n" \
"v_mfma_f32_16x16x32_bf16 v[64:67], a[224:227], v[136:139], v[64:67]\n" \
"v_exp_f32 v146, v146\n" \
"v_mfma_f32_16x16x32_bf16 v[64:67], a[192:195], v[128:131], v[64:67]\n" \
"ds_read_b128 a[252:255], v242 offset:15360\n" \
"ds_read_b128 a[140:143], v240 offset:3072\n" \
"v_mfma_f32_16x16x32_bf16 v[68:71], a[196:199], v[128:131], v[68:71]\n" \
".Lst1_5_%=:\n" \
"s_cbranch_execz .Lst1_9_%=\n" \
"v_exp_f32 v147, v147\n" \
"v_mfma_f32_16x16x32_bf16 v[68:71], a[228:231], v[136:139], v[68:71]\n" \
"s_waitcnt lgkmcnt(12)\n" \
"buffer_load_dwordx4 v237, s[48:51], s22 offen offset:3072 lds\n" \
"v_mfma_f32_16x16x32_bf16 v[72:75], a[232:235], v[136:139], v[72:75]\n" \
"s_add_u32 s20, s20, 0x4000\n" \
"v_exp_f32 v148, v148\n" \
"v_mfma_f32_16x16x32_bf16 v[72:75], a[200:203], v[128:131], v[72:75]\n" \
"s_and_b32 s20, s20, 0xffff\n" \
"ds_read_b128 a[156:159], v240 offset:7168\n" \
"v_cvt_pk_bf16_f32 v145, v146, v147\n" \
"v_mfma_f32_16x16x32_bf16 v[76:79], a[204:207], v[128:131], v[76:79]\n" \
"s_bitset1_b32 s20, 16\n" \
"v_exp_f32 v149, v149\n" \
"v_mfma_f32_16x16x32_bf16 v[76:79], a[236:239], v[136:139], v[76:79]\n" \
"s_waitcnt lgkmcnt(9)\n" \
"ds_read_b128 a[152:155], v240 offset:6144\n" \
"v_cvt_pk_bf16_f32 v146, v148, v149\n" \
"v_mfma_f32_16x16x32_bf16 v[80:83], a[240:243], v[136:139], v[80:83]\n" \
"v_exp_f32 v150, v150\n" \
"v_mfma_f32_16x16x32_bf16 v[80:83], a[208:211], v[128:131], v[80:83]\n" \
"ds_read_b128 a[148:151], v240 offset:5120\n" \
"ds_read_b128 a[144:147], v240 offset:4096\n" \
"v_mfma_f32_16x16x32_bf16 v[84:87], a[212:215], v[128:131], v[84:87]\n" \
"v_exp_f32 v151, v151\n" \
"v_mfma_f32_16x16x32_bf16 v[84:87], a[244:247], v[136:139], v[84:87]\n" \
"s_waitcnt lgkmcnt(5)\n" \
"ds_read_b128 a[160:163], v240 offset:8192\n" \
"buffer_load_dwordx4 a[116:119], v248, s[32:35], s38 offen offset:3072 sc1\n" \
"v_cvt_pk_bf16_f32 v147, v150, v151\n" \
"v_mfma_f32_16x16x32_bf16 v[88:91], a[248:251], v[136:139], v[88:91]\n" \
"ds_read_b128 a[164:167], v240 offset:9216\n" \
"ds_read_b128 a[168:171], v240 offset:10240\n" \
"v_mfma_f32_16x16x32_bf16 v[88:91], a[216:219], v[128:131], v[88:91]\n" \
"v_exp_f32 v152, v152\n" \
"v_mfma_f32_16x16x32_bf16 v[92:95], a[220:223], v[128:131], v[92:95]\n" \
"ds_read_b128 a[172:175], v240 offset:11264\n" \
"ds_read_b128 a[188:191], v240 offset:15360\n" \
"v_mfma_f32_16x16x32_bf16 v[92:95], a[252:255], v[136:139], v[92:95]\n" \
"s_waitcnt lgkmcnt(9)\n" \
"v_exp_f32 v153, v153\n" \
"v_mfma_f32_16x16x32_bf16 v[128:131], a[128:131], v[160:163], 0\n" \
"s_lshl1_add_u32 s22, s66, 4\n" \
"ds_read_b128 a[184:187], v240 offset:14336\n" \
"v_cvt_pk_bf16_f32 v152, v152, v153\n" \
"v_mfma_f32_16x16x32_bf16 v[128:131], a[132:135], v[164:167], v[128:131]\n" \
"s_and_b32 s22, s22, 7\n" \
"v_exp_f32 v154, v154\n" \
"v_mfma_f32_16x16x32_bf16 v[128:131], a[136:139], v[168:171], v[128:131]\n" \
"s_lshl_b32 s22, s22, 16\n" \
"ds_read_b128 a[180:183], v240 offset:13312\n" \
"v_add_u32 v242, 0x10000, v240\n" \
"v_mfma_f32_16x16x32_bf16 v[128:131], a[140:143], v[172:175], v[128:131]\n" \
"s_waitcnt lgkmcnt(7)\n" \
"v_exp_f32 v155, v155\n" \
"v_mfma_f32_16x16x32_bf16 v[132:135], a[156:159], v[172:175], 0\n" \
"s_add_u32 s22, s22, s17\n" \
"ds_read_b128 a[176:179], v240 offset:12288\n" \
"v_cvt_pk_bf16_f32 v153, v154, v155\n" \
"v_mfma_f32_16x16x32_bf16 v[132:135], a[152:155], v[168:171], v[132:135]\n" \
"s_mov_b32 m0, s19\n" \
"v_exp_f32 v156, v156\n" \
"v_mfma_f32_16x16x32_bf16 v[132:135], a[148:151], v[164:167], v[132:135]\n" \
"s_add_u32 s38, s66, 3\n" \
"buffer_load_dwordx4 v237, s[48:51], s22 offen offset:0 lds\n" \
"v_mfma_f32_16x16x32_bf16 v[132:135], a[144:147], v[160:163], v[132:135]\n" \
"s_waitcnt lgkmcnt(4)\n" \
"v_exp_f32 v157, v157\n" \
"v_mfma_f32_16x16x32_bf16 v[136:139], a[160:163], v[160:163], 0\n" \
"s_add_u32 s22, s22, 7168\n" \
"s_and_b32 s38, s38, 3\n" \
"s_lshl_b32 s38, s38, 13\n" \
"s_add_u32 s38, s38, s29\n" \
"v_cvt_pk_bf16_f32 v154, v156, v157\n" \
"v_mfma_f32_16x16x32_bf16 v[136:139], a[164:167], v[164:167], v[136:139]\n" \
"buffer_load_dwordx4 a[120:123], v248, s[32:35], s38 offen offset:0 sc1\n" \
"v_exp_f32 v158, v158\n" \
"v_mfma_f32_16x16x32_bf16 v[136:139], a[168:171], v[168:171], v[136:139]\n" \
"buffer_load_dwordx4 v237, s[48:51], s22 offen offset:1024 lds\n" \
"v_mfma_f32_16x16x32_bf16 v[136:139], a[172:175], v[172:175], v[136:139]\n" \
"s_waitcnt lgkmcnt(0)\n" \
"v_exp_f32 v159, v159\n" \
"v_mfma_f32_16x16x32_bf16 v[140:143], a[188:191], v[172:175], 0\n" \
"s_add_u32 s22, s22, 7168\n" \
"v_cvt_pk_bf16_f32 v155, v158, v159\n" \
"v_mfma_f32_16x16x32_bf16 v[140:143], a[184:187], v[168:171], v[140:143]\n" \
"buffer_load_dwordx4 v237, s[48:51], s22 offen offset:2048 lds\n" \
"v_mfma_f32_16x16x32_bf16 v[140:143], a[180:183], v[164:167], v[140:143]\n" \
"s_add_u32 s22, s22, 7168\n" \
"v_exp_f32 v128, v128\n" \
"v_mfma_f32_16x16x32_bf16 v[140:143], a[176:179], v[160:163], v[140:143]\n" \
"buffer_load_dwordx4 v237, s[48:51], s22 offen offset:3072 lds\n" \
"v_mfma_f32_16x16x32_bf16 a[44:47], a[48:51], v[144:147], a[44:47]\n" \
"v_exp_f32 v129, v129\n" \
"v_mfma_f32_16x16x32_bf16 a[44:47], a[48:51], v[152:155], a[44:47]\n" \
"v_cvt_pk_bf16_f32 v128, v128, v129\n" \
"v_lshlrev_b32 v232, 3, v251\n" \
"v_mfma_f32_16x16x32_bf16 v[96:99], a[224:227], v[152:155], v[96:99]\n" \
"s_lshl_b32 s70, %[wid], 9\n" \
"v_exp_f32 v130, v130\n" \
"v_mfma_f32_16x16x32_bf16 v[96:99], a[192:195], v[144:147], v[96:99]\n" \
"s_add_u32 s70, s70, 659456\n" \
"ds_read_b128 a[224:227], v242 offset:8192\n" \
"v_mov_b32 v234, 1\n" \
"v_mfma_f32_16x16x32_bf16 v[100:103], a[196:199], v[144:147], v[100:103]\n" \
"v_exp_f32 v131, v131\n" \
"v_mfma_f32_16x16x32_bf16 v[100:103], a[228:231], v[152:155], v[100:103]\n" \
"ds_read_b128 a[192:195], v242 offset:0\n" \
"v_cvt_pk_bf16_f32 v129, v130, v131\n" \
"v_mfma_f32_16x16x32_bf16 v[104:107], a[232:235], v[152:155], v[104:107]\n" \
"v_exp_f32 v132, v132\n" \
"v_mfma_f32_16x16x32_bf16 v[104:107], a[200:203], v[144:147], v[104:107]\n" \
"ds_read_b128 a[196:199], v242 offset:1024\n" \
"ds_read_b128 a[228:231], v242 offset:9216\n" \
"buffer_load_dwordx4 a[124:127], v248, s[32:35], s38 offen offset:1024 sc1\n" \
"v_mfma_f32_16x16x32_bf16 v[108:111], a[204:207], v[144:147], v[108:111]\n" \
"v_exp_f32 v133, v133\n" \
"v_mfma_f32_16x16x32_bf16 v[108:111], a[236:239], v[152:155], v[108:111]\n" \
"ds_read_b128 a[232:235], v242 offset:10240\n" \
"v_cvt_pk_bf16_f32 v130, v132, v133\n" \
"v_mfma_f32_16x16x32_bf16 v[112:115], a[240:243], v[152:155], v[112:115]\n" \
"v_exp_f32 v134, v134\n" \
"v_mfma_f32_16x16x32_bf16 v[112:115], a[208:211], v[144:147], v[112:115]\n" \
"ds_read_b128 a[200:203], v242 offset:2048\n" \
"v_mov_b32 v235, 1\n" \
"v_mfma_f32_16x16x32_bf16 v[116:119], a[212:215], v[144:147], v[116:119]\n" \
"v_exp_f32 v135, v135\n" \
"v_mfma_f32_16x16x32_bf16 v[116:119], a[244:247], v[152:155], v[116:119]\n" \
"ds_read_b128 a[204:207], v242 offset:3072\n" \
"v_cvt_pk_bf16_f32 v131, v134, v135\n" \
"v_mfma_f32_16x16x32_bf16 v[120:123], a[248:251], v[152:155], v[120:123]\n" \
"s_lshl1_add_u32 s22, s66, 5\n" \
"s_and_b32 s22, s22, 7\n" \
"ds_read_b128 a[236:239], v242 offset:11264\n" \
"v_mfma_f32_16x16x32_bf16 v[120:123], a[216:219], v[144:147], v[120:123]\n" \
"s_lshl_b32 s22, s22, 16\n" \
"v_exp_f32 v136, v136\n" \
"v_mfma_f32_16x16x32_bf16 v[124:127], a[220:223], v[144:147], v[124:127]\n" \
"s_add_u32 s22, s22, s17\n" \
"ds_read_b128 a[240:243], v242 offset:12288\n" \
"v_mfma_f32_16x16x32_bf16 v[124:127], a[252:255], v[152:155], v[124:127]\n" \
"s_waitcnt vmcnt(7)\n" \
"v_exp_f32 v137, v137\n" \
"v_mfma_f32_16x16x32_bf16 v[144:147], a[128:131], v[176:179], 0\n" \
"s_barrier\n" \
"ds_read_b128 a[208:211], v242 offset:4096\n" \
"v_cvt_pk_bf16_f32 v136, v136, v137\n" \
"v_mfma_f32_16x16x32_bf16 v[144:147], a[132:135], v[180:183], v[144:147]\n" \
"s_mov_b32 m0, s20\n" \
"v_exp_f32 v138, v138\n" \
"v_mfma_f32_16x16x32_bf16 v[144:147], a[136:139], v[184:187], v[144:147]\n" \
"buffer_load_dwordx4 v237, s[48:51], s22 offen offset:0 lds\n" \
"v_mfma_f32_16x16x32_bf16 v[144:147], a[140:143], v[188:191], v[144:147]\n" \
"s_add_u32 s22, s22, 7168\n" \
"v_exp_f32 v139, v139\n" \
"v_mfma_f32_16x16x32_bf16 v[148:151], a[156:159], v[188:191], 0\n" \
"ds_read_b128 a[212:215], v242 offset:5120\n" \
"v_cvt_pk_bf16_f32 v137, v138, v139\n" \
"v_mfma_f32_16x16x32_bf16 v[148:151], a[152:155], v[184:187], v[148:151]\n" \
"v_exp_f32 v140, v140\n" \
"v_mfma_f32_16x16x32_bf16 v[148:151], a[148:151], v[180:183], v[148:151]\n" \
"ds_read_b128 a[244:247], v242 offset:13312\n" \
"ds_read_b128 a[248:251], v242 offset:14336\n" \
"v_mfma_f32_16x16x32_bf16 v[148:151], a[144:147], v[176:179], v[148:151]\n" \
"v_exp_f32 v141, v141\n" \
"v_mfma_f32_16x16x32_bf16 v[152:155], a[160:163], v[176:179], 0\n" \
"buffer_load_dwordx4 v237, s[48:51], s22 offen offset:1024 lds\n" \
"v_mfma_f32_16x16x32_bf16 v[152:155], a[164:167], v[180:183], v[152:155]\n" \
"s_add_u32 s22, s22, 7168\n" \
"v_exp_f32 v142, v142\n" \
"v_mfma_f32_16x16x32_bf16 v[152:155], a[168:171], v[184:187], v[152:155]\n" \
"ds_read_b128 a[216:219], v242 offset:6144\n" \
"v_cvt_pk_bf16_f32 v138, v140, v141\n" \
"v_mfma_f32_16x16x32_bf16 v[152:155], a[172:175], v[188:191], v[152:155]\n" \
"v_exp_f32 v143, v143\n" \
"v_mfma_f32_16x16x32_bf16 v[156:159], a[188:191], v[188:191], 0\n" \
"v_exp_f32 v144, v144\n" \
"v_mfma_f32_16x16x32_bf16 v[156:159], a[184:187], v[184:187], v[156:159]\n" \
"v_exp_f32 v145, v145\n" \
"v_mfma_f32_16x16x32_bf16 v[156:159], a[180:183], v[180:183], v[156:159]\n" \
"v_exp_f32 v146, v146\n" \
"v_mfma_f32_16x16x32_bf16 v[156:159], a[176:179], v[176:179], v[156:159]\n" \
"v_exp_f32 v147, v147\n" \
"v_cvt_pk_bf16_f32 v139, v142, v143\n" \
"v_mfma_f32_16x16x32_bf16 a[40:43], a[48:51], v[128:131], a[40:43]\n" \
"v_exp_f32 v148, v148\n" \
"v_cvt_pk_bf16_f32 v144, v144, v145\n" \
"v_mfma_f32_16x16x32_bf16 a[40:43], a[48:51], v[136:139], a[40:43]\n" \
"s_waitcnt lgkmcnt(10)\n" \
"v_exp_f32 v149, v149\n" \
"v_cvt_pk_bf16_f32 v145, v146, v147\n" \
"v_mfma_f32_16x16x32_bf16 v[64:67], a[224:227], v[136:139], v[64:67]\n" \
"v_exp_f32 v150, v150\n" \
"v_cvt_pk_bf16_f32 v146, v148, v149\n" \
"v_mfma_f32_16x16x32_bf16 v[64:67], a[192:195], v[128:131], v[64:67]\n" \
"ds_read_b128 a[220:223], v242 offset:7168\n" \
"v_exp_f32 v151, v151\n" \
"v_mfma_f32_16x16x32_bf16 v[68:71], a[196:199], v[128:131], v[68:71]\n" \
"buffer_load_dwordx4 a[128:131], v248, s[32:35], s38 offen offset:2048 sc1\n" \
"v_exp_f32 v152, v152\n" \
"v_cvt_pk_bf16_f32 v147, v150, v151\n" \
"v_mfma_f32_16x16x32_bf16 v[68:71], a[228:231], v[136:139], v[68:71]\n" \
"s_waitcnt lgkmcnt(7)\n" \
"ds_read_b128 a[252:255], v242 offset:15360\n" \
"v_exp_f32 v153, v153\n" \
"v_mfma_f32_16x16x32_bf16 v[72:75], a[232:235], v[136:139], v[72:75]\n" \
"v_exp_f32 v154, v154\n" \
"v_cvt_pk_bf16_f32 v152, v152, v153\n" \
"v_mfma_f32_16x16x32_bf16 v[72:75], a[200:203], v[128:131], v[72:75]\n" \
"buffer_load_dwordx4 a[132:135], v248, s[32:35], s38 offen offset:3072 sc1\n" \
"v_exp_f32 v155, v155\n" \
"v_mfma_f32_16x16x32_bf16 v[76:79], a[204:207], v[128:131], v[76:79]\n" \
"v_exp_f32 v156, v156\n" \
"v_cvt_pk_bf16_f32 v153, v154, v155\n" \
"v_mfma_f32_16x16x32_bf16 v[76:79], a[236:239], v[136:139], v[76:79]\n" \
"s_waitcnt lgkmcnt(4)\n" \
"buffer_load_dwordx4 v237, s[48:51], s22 offen offset:2048 lds\n" \
"v_accvgpr_read_b32 v224, a40\n" \
"v_mfma_f32_16x16x32_bf16 v[80:83], a[240:243], v[136:139], v[80:83]\n" \
"s_add_u32 s22, s22, 7168\n" \
"buffer_load_dwordx2 v[230:231], v232, s[32:35], s70 offen sc1\n" \
"v_exp_f32 v157, v157\n" \
"v_mfma_f32_16x16x32_bf16 v[80:83], a[208:211], v[128:131], v[80:83]\n" \
"v_exp_f32 v158, v158\n" \
"v_cvt_pk_bf16_f32 v154, v156, v157\n" \
"v_mfma_f32_16x16x32_bf16 v[84:87], a[212:215], v[128:131], v[84:87]\n" \
"s_add_u32 s38, s38, 4096\n" \
"buffer_load_dwordx4 v237, s[48:51], s22 offen offset:3072 lds\n" \
"v_mfma_f32_16x16x32_bf16 v[84:87], a[244:247], v[136:139], v[84:87]\n" \
"s_waitcnt lgkmcnt(0)\n" \
"buffer_load_dwordx4 a[136:139], v248, s[32:35], s38 offen offset:0 sc1\n" \
"v_exp_f32 v159, v159\n" \
"v_mfma_f32_16x16x32_bf16 v[88:91], a[248:251], v[136:139], v[88:91]\n" \
"v_cvt_pk_bf16_f32 v155, v158, v159\n" \
"v_bfe_u32 v239, v251, 4, 1\n" \
"v_mul_u32_u24 v239, 0x120, v239\n" \
"v_mfma_f32_16x16x32_bf16 v[88:91], a[216:219], v[128:131], v[88:91]\n" \
"v_lshrrev_b32 v249, 5, v251\n" \
"v_lshl_or_b32 v239, v249, 10, v239\n" \
"v_bfe_u32 v249, v251, 2, 2\n" \
"v_mfma_f32_16x16x32_bf16 v[92:95], a[220:223], v[128:131], v[92:95]\n" \
"v_lshl_or_b32 v239, v249, 6, v239\n" \
"v_and_b32 v249, 3, v251\n" \
"v_lshl_or_b32 v239, v249, 3, v239\n" \
"v_mfma_f32_16x16x32_bf16 v[92:95], a[252:255], v[136:139], v[92:95]\n" \
"v_add_u32 v240, s83, v239\n" \
"v_xor_b32 v236, 32, v240\n" \
"v_add_u32 v241, 0x10000, v240\n" \
"v_mfma_f32_16x16x32_bf16 a[44:47], a[48:51], v[144:147], a[44:47]\n" \
"v_xor_b32 v244, 0x8000, v240\n" \
"v_xor_b32 v249, 0x8000, v241\n" \
"v_add_u32 v239, 0x20000, v239\n" \
"v_mfma_f32_16x16x32_bf16 a[44:47], a[48:51], v[152:155], a[44:47]\n" \
"s_lshl_b32 s39, s66, 13\n" \
"buffer_load_dwordx4 a[140:143], v248, s[32:35], s38 offen offset:1024 sc1\n" \
"v_rcp_f32 v224, v224\n" \
"v_mfma_f32_16x16x32_bf16 v[96:99], a[224:227], v[152:155], v[96:99]\n" \
"s_add_u32 s39, s39, s37\n" \
"v_mul_f32 v64, v64, v224\n" \
"v_mul_f32 v65, v65, v224\n" \
"v_mul_f32 v66, v66, v224\n" \
"v_mfma_f32_16x16x32_bf16 v[96:99], a[192:195], v[144:147], v[96:99]\n" \
"s_add_u32 s19, s39, 4096\n" \
"v_mul_f32 v67, v67, v224\n" \
"v_mul_f32 v68, v68, v224\n" \
"v_mul_f32 v69, v69, v224\n" \
"v_mfma_f32_16x16x32_bf16 v[100:103], a[196:199], v[144:147], v[100:103]\n" \
"ds_read_b64_tr_b16 a[0:1], v240 offset:0\n" \
"v_mul_f32 v70, v70, v224\n" \
"v_mul_f32 v71, v71, v224\n" \
"v_mfma_f32_16x16x32_bf16 v[100:103], a[228:231], v[152:155], v[100:103]\n" \
"ds_read_b64_tr_b16 a[2:3], v240 offset:2048\n" \
"v_cvt_pk_bf16_f32 v192, v64, v65\n" \
"v_cvt_pk_bf16_f32 v193, v66, v67\n" \
"v_mfma_f32_16x16x32_bf16 v[104:107], a[232:235], v[152:155], v[104:107]\n" \
"v_accvgpr_read_b32 v228, a40\n" \
"v_accvgpr_read_b32 v229, a44\n" \
"v_min_f32 v253, v228, v229\n" \
"v_mfma_f32_16x16x32_bf16 v[104:107], a[200:203], v[144:147], v[104:107]\n" \
"v_max_f32 v252, v228, v229\n" \
"v_cmp_nle_f32 s[98:99], v246, v253\n" \
"v_cmp_nle_f32 vcc, v252, v250\n" \
"v_mfma_f32_16x16x32_bf16 v[108:111], a[204:207], v[144:147], v[108:111]\n" \
"ds_read_b64_tr_b16 a[4:5], v240 offset:8192\n" \
"ds_read_b64_tr_b16 a[6:7], v240 offset:10240\n" \
"v_cmp_u_f32 s[14:15], v228, v229\n" \
"v_mfma_f32_16x16x32_bf16 v[108:111], a[236:239], v[152:155], v[108:111]\n" \
"ds_read_b64_tr_b16 a[8:9], v236 offset:0\n" \
"ds_read_b64_tr_b16 a[10:11], v236 offset:2048\n" \
"v_cvt_pk_bf16_f32 v194, v68, v69\n" \
"v_mfma_f32_16x16x32_bf16 v[112:115], a[240:243], v[152:155], v[112:115]\n" \
"s_or_b64 vcc, vcc, s[98:99]\n" \
"ds_read_b64_tr_b16 a[12:13], v236 offset:8192\n" \
"buffer_load_dwordx4 a[144:147], v248, s[32:35], s38 offen offset:2048 sc1\n" \
"v_cvt_pk_bf16_f32 v195, v70, v71\n" \
"v_mfma_f32_16x16x32_bf16 v[112:115], a[208:211], v[144:147], v[112:115]\n" \
"s_or_b64 vcc, vcc, s[14:15]\n" \
"ds_read_b64_tr_b16 a[14:15], v236 offset:10240\n" \
"v_mul_f32 v72, v72, v224\n" \
"v_mul_f32 v73, v73, v224\n" \
"v_mfma_f32_16x16x32_bf16 v[116:119], a[212:215], v[144:147], v[116:119]\n" \
"ds_read_b64_tr_b16 a[16:17], v240 offset:512\n" \
"v_mul_f32 v74, v74, v224\n" \
"v_mul_f32 v75, v75, v224\n" \
"v_mfma_f32_16x16x32_bf16 v[116:119], a[244:247], v[152:155], v[116:119]\n" \
"ds_read_b64_tr_b16 a[18:19], v240 offset:2560\n" \
"v_mul_f32 v76, v76, v224\n" \
"v_mul_f32 v77, v77, v224\n" \
"v_mfma_f32_16x16x32_bf16 v[120:123], a[248:251], v[152:155], v[120:123]\n" \
"ds_read_b64_tr_b16 a[20:21], v240 offset:8704\n" \
"v_mul_f32 v78, v78, v224\n" \
"v_mul_f32 v79, v79, v224\n" \
"v_mfma_f32_16x16x32_bf16 v[120:123], a[216:219], v[144:147], v[120:123]\n" \
"ds_read_b64_tr_b16 a[22:23], v240 offset:10752\n" \
"v_cvt_pk_bf16_f32 v200, v72, v73\n" \
"v_cvt_pk_bf16_f32 v201, v74, v75\n" \
"v_mfma_f32_16x16x32_bf16 v[124:127], a[220:223], v[144:147], v[124:127]\n" \
"buffer_store_dwordx4 v[192:195], v248, s[32:35], s39 offen offset:0 sc1\n" \
"buffer_load_dwordx4 a[148:151], v248, s[32:35], s38 offen offset:3072 sc1\n" \
"v_mfma_f32_16x16x32_bf16 v[124:127], a[252:255], v[152:155], v[124:127]\n" \
"v_cvt_pk_bf16_f32 v202, v76, v77\n" \
"v_cvt_pk_bf16_f32 v203, v78, v79\n" \
"v_mul_f32 v80, v80, v224\n" \
"s_cbranch_vccnz .Lrh_%=\n" \
".Lrhb_%=:\n" \
  \
"s_waitcnt lgkmcnt(10)\n" \
"v_mfma_f32_16x16x32_bf16 v[128:131], a[0:3], v[0:3], 0\n" \
"ds_read_b64_tr_b16 a[24:25], v236 offset:512\n" \
"ds_read_b64_tr_b16 a[26:27], v236 offset:2560\n" \
"v_mul_f32 v81, v81, v224\n" \
"v_mul_f32 v82, v82, v224\n" \
"s_waitcnt lgkmcnt(10)\n" \
"v_mfma_f32_16x16x32_bf16 v[128:131], a[4:7], v[8:11], v[128:131]\n" \
"buffer_store_dwordx4 v[200:203], v248, s[32:35], s39 offen offset:2048 sc1\n" \
"v_mul_f32 v83, v83, v224\n" \
"v_mul_f32 v84, v84, v224\n" \
"v_mfma_f32_16x16x32_bf16 v[136:139], a[4:7], v[12:15], 0\n" \
"ds_read_b64_tr_b16 a[28:29], v236 offset:8704\n" \
"ds_read_b64_tr_b16 a[30:31], v236 offset:10752\n" \
"v_mul_f32 v85, v85, v224\n" \
"v_mul_f32 v86, v86, v224\n" \
"v_mfma_f32_16x16x32_bf16 v[136:139], a[0:3], v[4:7], v[136:139]\n" \
"s_waitcnt lgkmcnt(8)\n" \
"v_mul_f32 v87, v87, v224\n" \
"v_cvt_pk_bf16_f32 v208, v80, v81\n" \
"v_cvt_pk_bf16_f32 v209, v82, v83\n" \
"v_cvt_pk_bf16_f32 v210, v84, v85\n" \
"v_mfma_f32_16x16x32_bf16 v[132:135], a[8:11], v[0:3], 0\n" \
"ds_read_b64_tr_b16 a[0:1], v240 offset:4096\n" \
"ds_read_b64_tr_b16 a[2:3], v240 offset:6144\n" \
"v_cvt_pk_bf16_f32 v211, v86, v87\n" \
"v_mul_f32 v88, v88, v224\n" \
"v_mfma_f32_16x16x32_bf16 v[132:135], a[12:15], v[8:11], v[132:135]\n" \
"buffer_store_dwordx4 v[208:211], v248, s[32:35], s19 offen offset:0 sc1\n" \
"v_mul_f32 v89, v89, v224\n" \
"v_mul_f32 v90, v90, v224\n" \
"v_mfma_f32_16x16x32_bf16 v[140:143], a[12:15], v[12:15], 0\n" \
"ds_read_b64_tr_b16 a[4:5], v240 offset:12288\n" \
"ds_read_b64_tr_b16 a[6:7], v240 offset:14336\n" \
"v_mul_f32 v91, v91, v224\n" \
"v_mul_f32 v92, v92, v224\n" \
"v_mfma_f32_16x16x32_bf16 v[140:143], a[8:11], v[4:7], v[140:143]\n" \
"s_waitcnt lgkmcnt(8)\n" \
"v_mul_f32 v93, v93, v224\n" \
"v_mul_f32 v94, v94, v224\n" \
"v_mul_f32 v95, v95, v224\n" \
"v_cvt_pk_bf16_f32 v216, v88, v89\n" \
"v_mfma_f32_16x16x32_bf16 v[144:147], a[16:19], v[0:3], 0\n" \
"ds_read_b64_tr_b16 a[8:9], v236 offset:4096\n" \
"ds_read_b64_tr_b16 a[10:11], v236 offset:6144\n" \
"v_cvt_pk_bf16_f32 v217, v90, v91\n" \
"v_cvt_pk_bf16_f32 v218, v92, v93\n" \
"v_mfma_f32_16x16x32_bf16 v[144:147], a[20:23], v[8:11], v[144:147]\n" \
"v_cvt_pk_bf16_f32 v219, v94, v95\n" \
"v_accvgpr_read_b32 v225, a44\n" \
"v_rcp_f32 v225, v225\n" \
"v_mfma_f32_16x16x32_bf16 v[152:155], a[20:23], v[12:15], 0\n" \
"ds_read_b64_tr_b16 a[12:13], v236 offset:12288\n" \
"ds_read_b64_tr_b16 a[14:15], v236 offset:14336\n" \
"v_mul_f32 v96, v96, v225\n" \
"v_mul_f32 v97, v97, v225\n" \
"v_mfma_f32_16x16x32_bf16 v[152:155], a[16:19], v[4:7], v[152:155]\n" \
"s_waitcnt lgkmcnt(8)\n" \
"buffer_store_dwordx4 v[216:219], v248, s[32:35], s19 offen offset:2048 sc1\n" \
"v_mul_f32 v98, v98, v225\n" \
"v_mul_f32 v99, v99, v225\n" \
"v_mfma_f32_16x16x32_bf16 v[148:151], a[24:27], v[0:3], 0\n" \
"ds_read_b64_tr_b16 a[16:17], v240 offset:4608\n" \
"ds_read_b64_tr_b16 a[18:19], v240 offset:6656\n" \
"v_mul_f32 v100, v100, v225\n" \
"v_mul_f32 v101, v101, v225\n" \
"v_mfma_f32_16x16x32_bf16 v[148:151], a[28:31], v[8:11], v[148:151]\n" \
"v_mul_f32 v102, v102, v225\n" \
"v_mul_f32 v103, v103, v225\n" \
"v_cvt_pk_bf16_f32 v196, v96, v97\n" \
"v_cvt_pk_bf16_f32 v197, v98, v99\n" \
"v_mfma_f32_16x16x32_bf16 v[156:159], a[28:31], v[12:15], 0\n" \
"ds_read_b64_tr_b16 a[20:21], v240 offset:12800\n" \
"ds_read_b64_tr_b16 a[22:23], v240 offset:14848\n" \
"v_cvt_pk_bf16_f32 v198, v100, v101\n" \
"v_cvt_pk_bf16_f32 v199, v102, v103\n" \
"v_mfma_f32_16x16x32_bf16 v[156:159], a[24:27], v[4:7], v[156:159]\n" \
"s_waitcnt lgkmcnt(8)\n" \
"buffer_store_dwordx4 v[196:199], v248, s[32:35], s39 offen offset:1024 sc1\n" \
"v_mul_f32 v104, v104, v225\n" \
"v_mul_f32 v105, v105, v225\n" \
"v_mfma_f32_16x16x32_bf16 v[160:163], a[0:3], v[0:3], 0\n" \
"ds_read_b64_tr_b16 a[24:25], v236 offset:4608\n" \
"ds_read_b64_tr_b16 a[26:27], v236 offset:6656\n" \
"v_mul_f32 v106, v106, v225\n" \
"v_mul_f32 v107, v107, v225\n" \
"v_mfma_f32_16x16x32_bf16 v[160:163], a[4:7], v[8:11], v[160:163]\n" \
"v_mul_f32 v108, v108, v225\n" \
"v_mul_f32 v109, v109, v225\n" \
"v_mul_f32 v110, v110, v225\n" \
"v_mul_f32 v111, v111, v225\n" \
"v_mfma_f32_16x16x32_bf16 v[168:171], a[4:7], v[12:15], 0\n" \
"ds_read_b64_tr_b16 a[28:29], v236 offset:12800\n" \
"ds_read_b64_tr_b16 a[30:31], v236 offset:14848\n" \
"v_cvt_pk_bf16_f32 v204, v104, v105\n" \
"v_cvt_pk_bf16_f32 v205, v106, v107\n" \
"v_mfma_f32_16x16x32_bf16 v[168:171], a[0:3], v[4:7], v[168:171]\n" \
"s_waitcnt lgkmcnt(8)\n" \
"v_cvt_pk_bf16_f32 v206, v108, v109\n" \
"v_cvt_pk_bf16_f32 v207, v110, v111\n" \
"v_mul_f32 v112, v112, v225\n" \
"v_mul_f32 v113, v113, v225\n" \
"v_mfma_f32_16x16x32_bf16 v[164:167], a[8:11], v[0:3], 0\n" \
"ds_read_b64_tr_b16 a[0:1], v240 offset:16384\n" \
"ds_read_b64_tr_b16 a[2:3], v240 offset:18432\n" \
"v_mul_f32 v114, v114, v225\n" \
"v_mul_f32 v115, v115, v225\n" \
"v_mfma_f32_16x16x32_bf16 v[164:167], a[12:15], v[8:11], v[164:167]\n" \
"buffer_store_dwordx4 v[204:207], v248, s[32:35], s39 offen offset:3072 sc1\n" \
"v_mul_f32 v116, v116, v225\n" \
"v_mul_f32 v117, v117, v225\n" \
"v_mfma_f32_16x16x32_bf16 v[172:175], a[12:15], v[12:15], 0\n" \
"s_waitcnt vmcnt(11)\n" \
"ds_read_b64_tr_b16 a[4:5], v240 offset:24576\n" \
"ds_read_b64_tr_b16 a[6:7], v240 offset:26624\n" \
"v_cmp_ne_u64 vcc, v[230:231], v[234:235]\n" \
"v_mul_f32 v118, v118, v225\n" \
"v_mfma_f32_16x16x32_bf16 v[172:175], a[8:11], v[4:7], v[172:175]\n" \
"s_waitcnt lgkmcnt(8)\n" \
"v_mul_f32 v119, v119, v225\n" \
"v_cvt_pk_bf16_f32 v212, v112, v113\n" \
"v_cvt_pk_bf16_f32 v213, v114, v115\n" \
"v_cvt_pk_bf16_f32 v214, v116, v117\n" \
"v_mfma_f32_16x16x32_bf16 v[176:179], a[16:19], v[0:3], 0\n" \
"ds_read_b64_tr_b16 a[8:9], v236 offset:16384\n" \
"ds_read_b64_tr_b16 a[10:11], v236 offset:18432\n" \
"v_cvt_pk_bf16_f32 v215, v118, v119\n" \
"v_mul_f32 v120, v120, v225\n" \
"v_mfma_f32_16x16x32_bf16 v[176:179], a[20:23], v[8:11], v[176:179]\n" \
"s_cbranch_vccnz .Lspg_%=\n" \
".Lsbg_%=:\n" \
"buffer_store_dwordx4 v[212:215], v248, s[32:35], s19 offen offset:1024 sc1\n" \
"v_mul_f32 v121, v121, v225\n" \
"v_mfma_f32_16x16x32_bf16 v[184:187], a[20:23], v[12:15], 0\n" \
"ds_read_b64_tr_b16 a[12:13], v236 offset:24576\n" \
"ds_read_b64_tr_b16 a[14:15], v236 offset:26624\n" \
"v_mul_f32 v122, v122, v225\n" \
"v_mul_f32 v123, v123, v225\n" \
"v_mfma_f32_16x16x32_bf16 v[184:187], a[16:19], v[4:7], v[184:187]\n" \
"s_waitcnt lgkmcnt(8)\n" \
"v_mul_f32 v124, v124, v225\n" \
"v_mul_f32 v125, v125, v225\n" \
"v_mul_f32 v126, v126, v225\n" \
"v_mul_f32 v127, v127, v225\n" \
"v_mfma_f32_16x16x32_bf16 v[180:183], a[24:27], v[0:3], 0\n" \
"ds_read_b64_tr_b16 a[16:17], v240 offset:16896\n" \
"ds_read_b64_tr_b16 a[18:19], v240 offset:18944\n" \
"v_cvt_pk_bf16_f32 v220, v120, v121\n" \
"v_cvt_pk_bf16_f32 v221, v122, v123\n" \
"v_mfma_f32_16x16x32_bf16 v[180:183], a[28:31], v[8:11], v[180:183]\n" \
"v_cvt_pk_bf16_f32 v222, v124, v125\n" \
"v_cvt_pk_bf16_f32 v223, v126, v127\n" \
"v_mov_b32 v252, s66\n" \
"v_lshlrev_b32 v252, 2, v252\n" \
"v_mfma_f32_16x16x32_bf16 v[188:191], a[28:31], v[12:15], 0\n" \
"ds_read_b64_tr_b16 a[20:21], v240 offset:25088\n" \
"ds_read_b64_tr_b16 a[22:23], v240 offset:27136\n" \
"v_mfma_f32_16x16x32_bf16 v[188:191], a[24:27], v[4:7], v[188:191]\n" \
"s_waitcnt lgkmcnt(8)\n" \
"buffer_store_dwordx4 v[220:223], v248, s[32:35], s19 offen offset:3072 sc1\n" \
"v_mfma_f32_16x16x32_bf16 v[128:131], a[0:3], v[16:19], v[128:131]\n" \
"ds_read_b64_tr_b16 a[24:25], v236 offset:16896\n" \
"ds_read_b64_tr_b16 a[26:27], v236 offset:18944\n" \
"v_mfma_f32_16x16x32_bf16 v[128:131], a[4:7], v[24:27], v[128:131]\n" \
"v_mov_b32 v253, 1\n" \
"v_and_b32 v254, 15, v251\n" \
"v_mfma_f32_16x16x32_bf16 v[136:139], a[4:7], v[28:31], v[136:139]\n" \
"ds_read_b64_tr_b16 a[28:29], v236 offset:25088\n" \
"ds_read_b64_tr_b16 a[30:31], v236 offset:27136\n" \
"v_mfma_f32_16x16x32_bf16 v[136:139], a[0:3], v[20:23], v[136:139]\n" \
"s_waitcnt lgkmcnt(8)\n" \
"v_lshlrev_b32 v254, 6, v254\n" \
"v_and_or_b32 v250, v251, 48, v254\n" \
"v_mfma_f32_16x16x32_bf16 v[132:135], a[8:11], v[16:19], v[132:135]\n" \
"ds_read_b64_tr_b16 a[0:1], v240 offset:20480\n" \
"ds_read_b64_tr_b16 a[2:3], v240 offset:22528\n" \
"v_mfma_f32_16x16x32_bf16 v[132:135], a[12:15], v[24:27], v[132:135]\n" \
"v_min_u32 v254, 8, v255\n" \
"v_add3_u32 v254, v254, v252, 4\n" \
"v_mfma_f32_16x16x32_bf16 v[140:143], a[12:15], v[28:31], v[140:143]\n" \
"ds_read_b64_tr_b16 a[4:5], v240 offset:28672\n" \
"ds_read_b64_tr_b16 a[6:7], v240 offset:30720\n" \
"v_mfma_f32_16x16x32_bf16 v[140:143], a[8:11], v[20:23], v[140:143]\n" \
"s_waitcnt lgkmcnt(8)\n" \
"v_and_b32 v254, 12, v254\n" \
"v_xor_b32 v235, 32, v241\n" \
"v_mfma_f32_16x16x32_bf16 v[144:147], a[16:19], v[16:19], v[144:147]\n" \
"ds_read_b64_tr_b16 a[8:9], v236 offset:20480\n" \
"ds_read_b64_tr_b16 a[10:11], v236 offset:22528\n" \
"v_mfma_f32_16x16x32_bf16 v[144:147], a[20:23], v[24:27], v[144:147]\n" \
"v_xor_b32 v234, 32, v244\n" \
"v_xor_b32 v233, 32, v249\n" \
"v_mfma_f32_16x16x32_bf16 v[152:155], a[20:23], v[28:31], v[152:155]\n" \
"ds_read_b64_tr_b16 a[12:13], v236 offset:28672\n" \
"ds_read_b64_tr_b16 a[14:15], v236 offset:30720\n" \
"v_mfma_f32_16x16x32_bf16 v[152:155], a[16:19], v[20:23], v[152:155]\n" \
"s_waitcnt lgkmcnt(8)\n" \
"v_xor_b32 v232, 32, v239\n" \
"v_mfma_f32_16x16x32_bf16 v[148:151], a[24:27], v[16:19], v[148:151]\n" \
"ds_read_b64_tr_b16 a[16:17], v240 offset:20992\n" \
"ds_read_b64_tr_b16 a[18:19], v240 offset:23040\n" \
"v_mfma_f32_16x16x32_bf16 v[148:151], a[28:31], v[24:27], v[148:151]\n" \
"v_mfma_f32_16x16x32_bf16 v[156:159], a[28:31], v[28:31], v[156:159]\n" \
"ds_read_b64_tr_b16 a[20:21], v240 offset:29184\n" \
"ds_read_b64_tr_b16 a[22:23], v240 offset:31232\n" \
"v_mfma_f32_16x16x32_bf16 v[156:159], a[24:27], v[20:23], v[156:159]\n" \
"s_waitcnt lgkmcnt(8)\n" \
"v_mfma_f32_16x16x32_bf16 v[160:163], a[0:3], v[16:19], v[160:163]\n" \
"s_waitcnt vmcnt(12)\n" \
"s_barrier\n" \
"ds_read_b64_tr_b16 a[24:25], v236 offset:20992\n" \
"v_mfma_f32_16x16x32_bf16 v[160:163], a[4:7], v[24:27], v[160:163]\n" \
"s_lshl2_add_u32 s70, s69, %[wid]\n" \
"s_lshl_b32 s70, s70, 12\n" \
"ds_read_b64_tr_b16 a[26:27], v236 offset:23040\n" \
"v_mfma_f32_16x16x32_bf16 v[168:171], a[4:7], v[28:31], v[168:171]\n" \
"s_add_u32 s70, s70, 77594624\n" \
"ds_read_b64_tr_b16 a[28:29], v236 offset:29184\n" \
"ds_read_b64_tr_b16 a[30:31], v236 offset:31232\n" \
"v_mfma_f32_16x16x32_bf16 v[168:171], a[0:3], v[20:23], v[168:171]\n" \
"s_waitcnt lgkmcnt(8)\n" \
"buffer_load_dwordx4 a[68:71], v248, s[32:35], s70 offen offset:0 sc1\n" \
"v_mfma_f32_16x16x32_bf16 v[164:167], a[8:11], v[16:19], v[164:167]\n" \
"ds_read_b64_tr_b16 a[0:1], v241 offset:0\n" \
"ds_read_b64_tr_b16 a[2:3], v241 offset:2048\n" \
"v_mfma_f32_16x16x32_bf16 v[164:167], a[12:15], v[24:27], v[164:167]\n" \
"v_mfma_f32_16x16x32_bf16 v[172:175], a[12:15], v[28:31], v[172:175]\n" \
"ds_read_b64_tr_b16 a[4:5], v241 offset:8192\n" \
"ds_read_b64_tr_b16 a[6:7], v241 offset:10240\n" \
"v_mfma_f32_16x16x32_bf16 v[172:175], a[8:11], v[20:23], v[172:175]\n" \
"s_waitcnt lgkmcnt(8)\n" \
"buffer_load_dwordx4 a[76:79], v248, s[32:35], s70 offen offset:1024 sc1\n" \
"v_mfma_f32_16x16x32_bf16 v[176:179], a[16:19], v[16:19], v[176:179]\n" \
"ds_read_b64_tr_b16 a[8:9], v235 offset:0\n" \
"ds_read_b64_tr_b16 a[10:11], v235 offset:2048\n" \
"v_mfma_f32_16x16x32_bf16 v[176:179], a[20:23], v[24:27], v[176:179]\n" \
"v_mfma_f32_16x16x32_bf16 v[184:187], a[20:23], v[28:31], v[184:187]\n" \
"ds_read_b64_tr_b16 a[12:13], v235 offset:8192\n" \
"ds_read_b64_tr_b16 a[14:15], v235 offset:10240\n" \
"v_mfma_f32_16x16x32_bf16 v[184:187], a[16:19], v[20:23], v[184:187]\n" \
"s_waitcnt lgkmcnt(8)\n" \
"buffer_load_dwordx4 a[84:87], v248, s[32:35], s70 offen offset:2048 sc1\n" \
"v_mfma_f32_16x16x32_bf16 v[180:183], a[24:27], v[16:19], v[180:183]\n" \
"ds_read_b64_tr_b16 a[16:17], v241 offset:512\n" \
"ds_read_b64_tr_b16 a[18:19], v241 offset:2560\n" \
"v_mfma_f32_16x16x32_bf16 v[180:183], a[28:31], v[24:27], v[180:183]\n" \
"v_mfma_f32_16x16x32_bf16 v[188:191], a[28:31], v[28:31], v[188:191]\n" \
"ds_read_b64_tr_b16 a[20:21], v241 offset:8704\n" \
"ds_read_b64_tr_b16 a[22:23], v241 offset:10752\n" \
"v_mfma_f32_16x16x32_bf16 v[188:191], a[24:27], v[20:23], v[188:191]\n" \
"s_waitcnt lgkmcnt(8)\n" \
"buffer_load_dwordx4 a[92:95], v248, s[32:35], s70 offen offset:3072 sc1\n" \
"v_lshlrev_b32 v231, 2, v251\n" \
"v_mfma_f32_16x16x32_bf16 v[128:131], a[0:3], v[32:35], v[128:131]\n" \
"ds_read_b64_tr_b16 a[24:25], v235 offset:512\n" \
"ds_read_b64_tr_b16 a[26:27], v235 offset:2560\n" \
"v_mfma_f32_16x16x32_bf16 v[128:131], a[4:7], v[40:43], v[128:131]\n" \
"v_mfma_f32_16x16x32_bf16 v[136:139], a[4:7], v[44:47], v[136:139]\n" \
"ds_read_b64_tr_b16 a[28:29], v235 offset:8704\n" \
"ds_read_b64_tr_b16 a[30:31], v235 offset:10752\n" \
"v_mfma_f32_16x16x32_bf16 v[136:139], a[0:3], v[36:39], v[136:139]\n" \
"s_waitcnt lgkmcnt(8)\n" \
"buffer_load_dword a100, v231, s[60:63], s79 offen offset:0\n" \
"v_mfma_f32_16x16x32_bf16 v[132:135], a[8:11], v[32:35], v[132:135]\n" \
"ds_read_b64_tr_b16 a[0:1], v241 offset:16384\n" \
"ds_read_b64_tr_b16 a[2:3], v241 offset:18432\n" \
"v_mfma_f32_16x16x32_bf16 v[132:135], a[12:15], v[40:43], v[132:135]\n" \
"v_mfma_f32_16x16x32_bf16 v[140:143], a[12:15], v[44:47], v[140:143]\n" \
"ds_read_b64_tr_b16 a[4:5], v241 offset:24576\n" \
"ds_read_b64_tr_b16 a[6:7], v241 offset:26624\n" \
"v_mfma_f32_16x16x32_bf16 v[140:143], a[8:11], v[36:39], v[140:143]\n" \
"s_waitcnt lgkmcnt(8)\n" \
"buffer_load_dword a101, v231, s[60:63], s79 offen offset:1024\n" \
"v_mfma_f32_16x16x32_bf16 v[144:147], a[16:19], v[32:35], v[144:147]\n" \
"ds_read_b64_tr_b16 a[8:9], v235 offset:16384\n" \
"ds_read_b64_tr_b16 a[10:11], v235 offset:18432\n" \
"v_mfma_f32_16x16x32_bf16 v[144:147], a[20:23], v[40:43], v[144:147]\n" \
"v_mfma_f32_16x16x32_bf16 v[152:155], a[20:23], v[44:47], v[152:155]\n" \
"ds_read_b64_tr_b16 a[12:13], v235 offset:24576\n" \
"ds_read_b64_tr_b16 a[14:15], v235 offset:26624\n" \
"v_mfma_f32_16x16x32_bf16 v[152:155], a[16:19], v[36:39], v[152:155]\n" \
"s_waitcnt lgkmcnt(8)\n" \
"buffer_load_dword a102, v231, s[60:63], s79 offen offset:2048\n" \
"v_mfma_f32_16x16x32_bf16 v[148:151], a[24:27], v[32:35], v[148:151]\n" \
".Lst1_6_%=:\n" \
"s_cbranch_execz .Lst1_10_%=\n" \
"ds_read_b64_tr_b16 a[16:17], v241 offset:16896\n" \
"ds_read_b64_tr_b16 a[18:19], v241 offset:18944\n" \
"v_mfma_f32_16x16x32_bf16 v[148:151], a[28:31], v[40:43], v[148:151]\n" \
"v_mfma_f32_16x16x32_bf16 v[156:159], a[28:31], v[44:47], v[156:159]\n" \
"ds_read_b64_tr_b16 a[20:21], v241 offset:25088\n" \
"ds_read_b64_tr_b16 a[22:23], v241 offset:27136\n" \
"v_mfma_f32_16x16x32_bf16 v[156:159], a[24:27], v[36:39], v[156:159]\n" \
"s_waitcnt lgkmcnt(8)\n" \
"s_sub_u32 s41, s31, 8192\n" \
"buffer_load_dword a103, v231, s[60:63], s79 offen offset:3072\n" \
"v_mfma_f32_16x16x32_bf16 v[128:131], a[0:3], v[48:51], v[128:131]\n" \
"ds_read_b64_tr_b16 a[24:25], v235 offset:16896\n" \
"ds_read_b64_tr_b16 a[26:27], v235 offset:18944\n" \
"v_mfma_f32_16x16x32_bf16 v[128:131], a[4:7], v[56:59], v[128:131]\n" \
"s_waitcnt vmcnt(8)\n" \
"buffer_store_dword v253, v252, s[32:35], s41 offen sc1\n" \
"v_mfma_f32_16x16x32_bf16 v[136:139], a[4:7], v[60:63], v[136:139]\n" \
"ds_read_b64_tr_b16 a[28:29], v235 offset:25088\n" \
"ds_read_b64_tr_b16 a[30:31], v235 offset:27136\n" \
"v_mfma_f32_16x16x32_bf16 v[136:139], a[0:3], v[52:55], v[136:139]\n" \
"s_waitcnt lgkmcnt(8)\n" \
"v_mfma_f32_16x16x32_bf16 v[132:135], a[8:11], v[48:51], v[132:135]\n" \
"ds_read_b64_tr_b16 a[0:1], v244 offset:0\n" \
"ds_read_b64_tr_b16 a[2:3], v244 offset:2048\n" \
"v_mfma_f32_16x16x32_bf16 v[132:135], a[12:15], v[56:59], v[132:135]\n" \
"buffer_load_dword a108, v231, s[60:63], s80 offen offset:0\n" \
"v_mfma_f32_16x16x32_bf16 v[140:143], a[12:15], v[60:63], v[140:143]\n" \
"ds_read_b64_tr_b16 a[4:5], v244 offset:8192\n" \
"ds_read_b64_tr_b16 a[6:7], v244 offset:10240\n" \
"v_mfma_f32_16x16x32_bf16 v[140:143], a[8:11], v[52:55], v[140:143]\n" \
"s_waitcnt lgkmcnt(8)\n" \
"v_mfma_f32_16x16x32_bf16 v[144:147], a[16:19], v[48:51], v[144:147]\n" \
"ds_read_b64_tr_b16 a[8:9], v234 offset:0\n" \
"ds_read_b64_tr_b16 a[10:11], v234 offset:2048\n" \
"v_mfma_f32_16x16x32_bf16 v[144:147], a[20:23], v[56:59], v[144:147]\n" \
"buffer_load_dword a109, v231, s[60:63], s80 offen offset:1024\n" \
"v_mfma_f32_16x16x32_bf16 v[152:155], a[20:23], v[60:63], v[152:155]\n" \
"s_lshl_b32 s17, s67, 18\n" \
"ds_read_b64_tr_b16 a[12:13], v234 offset:8192\n" \
"ds_read_b64_tr_b16 a[14:15], v234 offset:10240\n" \
"v_mfma_f32_16x16x32_bf16 v[152:155], a[16:19], v[52:55], v[152:155]\n" \
"s_waitcnt lgkmcnt(8)\n" \
"s_sub_u32 s17, 1966080, s17\n" \
"s_sub_u32 s17, s36, s17\n" \
"v_mfma_f32_16x16x32_bf16 v[148:151], a[24:27], v[48:51], v[148:151]\n" \
"s_add_u32 s16, s17, 4096\n" \
"ds_read_b64_tr_b16 a[16:17], v244 offset:512\n" \
"ds_read_b64_tr_b16 a[18:19], v244 offset:2560\n" \
"v_mfma_f32_16x16x32_bf16 v[148:151], a[28:31], v[56:59], v[148:151]\n" \
"s_add_u32 s21, s36, 4096\n" \
"buffer_load_dword a110, v231, s[60:63], s80 offen offset:2048\n" \
"v_and_b32 v246, 7, v251\n" \
"v_mfma_f32_16x16x32_bf16 v[156:159], a[28:31], v[60:63], v[156:159]\n" \
"ds_read_b64_tr_b16 a[20:21], v244 offset:8704\n" \
"ds_read_b64_tr_b16 a[22:23], v244 offset:10752\n" \
"v_mfma_f32_16x16x32_bf16 v[156:159], a[24:27], v[52:55], v[156:159]\n" \
"s_waitcnt lgkmcnt(8)\n" \
"v_lshlrev_b32 v246, 8, v246\n" \
"v_lshlrev_b32 v255, 2, v251\n" \
"v_mfma_f32_16x16x32_bf16 v[128:131], a[0:3], a[64:67], v[128:131]\n" \
"ds_read_b64_tr_b16 a[24:25], v234 offset:512\n" \
"ds_read_b64_tr_b16 a[26:27], v234 offset:2560\n" \
"v_mfma_f32_16x16x32_bf16 v[128:131], a[4:7], a[80:83], v[128:131]\n" \
"buffer_load_dword a111, v231, s[60:63], s80 offen offset:3072\n" \
"v_and_b32 v252, 0x7c, v255\n" \
"v_mfma_f32_16x16x32_bf16 v[136:139], a[4:7], a[88:91], v[136:139]\n" \
"ds_read_b64_tr_b16 a[28:29], v234 offset:8704\n" \
"ds_read_b64_tr_b16 a[30:31], v234 offset:10752\n" \
"v_mfma_f32_16x16x32_bf16 v[136:139], a[0:3], a[72:75], v[136:139]\n" \
"s_waitcnt lgkmcnt(8)\n" \
"v_mfma_f32_16x16x32_bf16 v[132:135], a[8:11], a[64:67], v[132:135]\n" \
"ds_read_b64_tr_b16 a[0:1], v249 offset:0\n" \
"ds_read_b64_tr_b16 a[2:3], v249 offset:2048\n" \
"v_mfma_f32_16x16x32_bf16 v[132:135], a[12:15], a[80:83], v[132:135]\n" \
"v_mfma_f32_16x16x32_bf16 v[140:143], a[12:15], a[88:91], v[140:143]\n" \
"ds_read_b64_tr_b16 a[4:5], v249 offset:8192\n" \
"ds_read_b64_tr_b16 a[6:7], v249 offset:10240\n" \
"v_mfma_f32_16x16x32_bf16 v[140:143], a[8:11], a[72:75], v[140:143]\n" \
"s_waitcnt lgkmcnt(8)\n" \
"v_mfma_f32_16x16x32_bf16 v[144:147], a[16:19], a[64:67], v[144:147]\n" \
"ds_read_b64_tr_b16 a[8:9], v233 offset:0\n" \
"ds_read_b64_tr_b16 a[10:11], v233 offset:2048\n" \
"v_mfma_f32_16x16x32_bf16 v[144:147], a[20:23], a[80:83], v[144:147]\n" \
"v_mfma_f32_16x16x32_bf16 v[152:155], a[20:23], a[88:91], v[152:155]\n" \
"ds_read_b64_tr_b16 a[12:13], v233 offset:8192\n" \
"ds_read_b64_tr_b16 a[14:15], v233 offset:10240\n" \
"v_mfma_f32_16x16x32_bf16 v[152:155], a[16:19], a[72:75], v[152:155]\n" \
"s_waitcnt lgkmcnt(8)\n" \
"v_mfma_f32_16x16x32_bf16 v[148:151], a[24:27], a[64:67], v[148:151]\n" \
"ds_read_b64_tr_b16 a[16:17], v249 offset:512\n" \
"ds_read_b64_tr_b16 a[18:19], v249 offset:2560\n" \
"v_mfma_f32_16x16x32_bf16 v[148:151], a[28:31], a[80:83], v[148:151]\n" \
"v_mfma_f32_16x16x32_bf16 v[156:159], a[28:31], a[88:91], v[156:159]\n" \
"ds_read_b64_tr_b16 a[20:21], v249 offset:8704\n" \
"ds_read_b64_tr_b16 a[22:23], v249 offset:10752\n" \
"v_mfma_f32_16x16x32_bf16 v[156:159], a[24:27], a[72:75], v[156:159]\n" \
"s_waitcnt lgkmcnt(8)\n" \
"v_mfma_f32_16x16x32_bf16 v[128:131], a[0:3], a[96:99], v[128:131]\n" \
"ds_read_b64_tr_b16 a[24:25], v233 offset:512\n" \
"ds_read_b64_tr_b16 a[26:27], v233 offset:2560\n" \
"v_mfma_f32_16x16x32_bf16 v[128:131], a[4:7], a[112:115], v[128:131]\n" \
"v_mfma_f32_16x16x32_bf16 v[136:139], a[4:7], a[116:119], v[136:139]\n" \
"ds_read_b64_tr_b16 a[28:29], v233 offset:8704\n" \
"ds_read_b64_tr_b16 a[30:31], v233 offset:10752\n" \
"v_mfma_f32_16x16x32_bf16 v[136:139], a[0:3], a[104:107], v[136:139]\n" \
"s_waitcnt lgkmcnt(8)\n" \
"v_mfma_f32_16x16x32_bf16 v[132:135], a[8:11], a[96:99], v[132:135]\n" \
"ds_read_b64_tr_b16 a[0:1], v239 offset:0\n" \
"ds_read_b64_tr_b16 a[2:3], v239 offset:2048\n" \
"v_mfma_f32_16x16x32_bf16 v[132:135], a[12:15], a[112:115], v[132:135]\n" \
"v_mfma_f32_16x16x32_bf16 v[140:143], a[12:15], a[116:119], v[140:143]\n" \
"ds_read_b64_tr_b16 a[4:5], v239 offset:8192\n" \
"ds_read_b64_tr_b16 a[6:7], v239 offset:10240\n" \
"v_mfma_f32_16x16x32_bf16 v[140:143], a[8:11], a[104:107], v[140:143]\n" \
"s_waitcnt lgkmcnt(8)\n" \
"v_mfma_f32_16x16x32_bf16 v[144:147], a[16:19], a[96:99], v[144:147]\n" \
"ds_read_b64_tr_b16 a[8:9], v232 offset:0\n" \
"ds_read_b64_tr_b16 a[10:11], v232 offset:2048\n" \
"v_mfma_f32_16x16x32_bf16 v[144:147], a[20:23], a[112:115], v[144:147]\n" \
"v_mfma_f32_16x16x32_bf16 v[152:155], a[20:23], a[116:119], v[152:155]\n" \
"ds_read_b64_tr_b16 a[12:13], v232 offset:8192\n" \
"ds_read_b64_tr_b16 a[14:15], v232 offset:10240\n" \
"v_mfma_f32_16x16x32_bf16 v[152:155], a[16:19], a[104:107], v[152:155]\n" \
"s_waitcnt lgkmcnt(8)\n" \
"v_mfma_f32_16x16x32_bf16 v[148:151], a[24:27], a[96:99], v[148:151]\n" \
"ds_read_b64_tr_b16 a[16:17], v239 offset:512\n" \
"ds_read_b64_tr_b16 a[18:19], v239 offset:2560\n" \
"v_mfma_f32_16x16x32_bf16 v[148:151], a[28:31], a[112:115], v[148:151]\n" \
"v_mfma_f32_16x16x32_bf16 v[156:159], a[28:31], a[116:119], v[156:159]\n" \
"ds_read_b64_tr_b16 a[20:21], v239 offset:8704\n" \
"ds_read_b64_tr_b16 a[22:23], v239 offset:10752\n" \
"v_mfma_f32_16x16x32_bf16 v[156:159], a[24:27], a[104:107], v[156:159]\n" \
"s_waitcnt lgkmcnt(8)\n" \
"v_mfma_f32_16x16x32_bf16 v[128:131], a[0:3], a[120:123], v[128:131]\n" \
"ds_read_b64_tr_b16 a[24:25], v232 offset:512\n" \
"ds_read_b64_tr_b16 a[26:27], v232 offset:2560\n" \
"v_mfma_f32_16x16x32_bf16 v[128:131], a[4:7], a[128:131], v[128:131]\n" \
"v_mfma_f32_16x16x32_bf16 v[136:139], a[4:7], a[132:135], v[136:139]\n" \
"ds_read_b64_tr_b16 a[28:29], v232 offset:8704\n" \
"ds_read_b64_tr_b16 a[30:31], v232 offset:10752\n" \
"v_mfma_f32_16x16x32_bf16 v[136:139], a[0:3], a[124:127], v[136:139]\n" \
"s_waitcnt lgkmcnt(8)\n" \
"v_mfma_f32_16x16x32_bf16 v[132:135], a[8:11], a[120:123], v[132:135]\n" \
"ds_read_b64_tr_b16 a[0:1], v239 offset:16384\n" \
"ds_read_b64_tr_b16 a[2:3], v239 offset:18432\n" \
"v_mfma_f32_16x16x32_bf16 v[132:135], a[12:15], a[128:131], v[132:135]\n" \
"v_mfma_f32_16x16x32_bf16 v[140:143], a[12:15], a[132:135], v[140:143]\n" \
"ds_read_b64_tr_b16 a[4:5], v239 offset:24576\n" \
"ds_read_b64_tr_b16 a[6:7], v239 offset:26624\n" \
"v_mfma_f32_16x16x32_bf16 v[140:143], a[8:11], a[124:127], v[140:143]\n" \
"s_waitcnt lgkmcnt(8)\n" \
"v_mfma_f32_16x16x32_bf16 v[144:147], a[16:19], a[120:123], v[144:147]\n" \
"ds_read_b64_tr_b16 a[8:9], v232 offset:16384\n" \
"ds_read_b64_tr_b16 a[10:11], v232 offset:18432\n" \
"v_mfma_f32_16x16x32_bf16 v[144:147], a[20:23], a[128:131], v[144:147]\n" \
"v_mfma_f32_16x16x32_bf16 v[152:155], a[20:23], a[132:135], v[152:155]\n" \
"ds_read_b64_tr_b16 a[12:13], v232 offset:24576\n" \
"ds_read_b64_tr_b16 a[14:15], v232 offset:26624\n" \
"v_mfma_f32_16x16x32_bf16 v[152:155], a[16:19], a[124:127], v[152:155]\n" \
"s_waitcnt lgkmcnt(8)\n" \
"v_mfma_f32_16x16x32_bf16 v[148:151], a[24:27], a[120:123], v[148:151]\n" \
"ds_read_b64_tr_b16 a[16:17], v239 offset:16896\n" \
"ds_read_b64_tr_b16 a[18:19], v239 offset:18944\n" \
"v_mfma_f32_16x16x32_bf16 v[148:151], a[28:31], a[128:131], v[148:151]\n" \
"v_mfma_f32_16x16x32_bf16 v[156:159], a[28:31], a[132:135], v[156:159]\n" \
"ds_read_b64_tr_b16 a[20:21], v239 offset:25088\n" \
"ds_read_b64_tr_b16 a[22:23], v239 offset:27136\n" \
"v_mfma_f32_16x16x32_bf16 v[156:159], a[24:27], a[124:127], v[156:159]\n" \
"s_waitcnt lgkmcnt(8)\n" \
"v_mfma_f32_16x16x32_bf16 v[128:131], a[0:3], a[136:139], v[128:131]\n" \
"ds_read_b64_tr_b16 a[24:25], v232 offset:16896\n" \
"ds_read_b64_tr_b16 a[26:27], v232 offset:18944\n" \
"v_mfma_f32_16x16x32_bf16 v[128:131], a[4:7], a[144:147], v[128:131]\n" \
"v_mfma_f32_16x16x32_bf16 v[136:139], a[4:7], a[148:151], v[136:139]\n" \
"ds_read_b64_tr_b16 a[28:29], v232 offset:25088\n" \
"ds_read_b64_tr_b16 a[30:31], v232 offset:27136\n" \
"v_mfma_f32_16x16x32_bf16 v[136:139], a[0:3], a[140:143], v[136:139]\n" \
"s_waitcnt lgkmcnt(8)\n" \
"v_mfma_f32_16x16x32_bf16 v[132:135], a[8:11], a[136:139], v[132:135]\n" \
"s_lshl2_add_u32 s18, s67, %[wid]\n" \
"ds_read_b64_tr_b16 a[0:1], v241 offset:4096\n" \
"ds_read_b64_tr_b16 a[2:3], v241 offset:6144\n" \
"v_mfma_f32_16x16x32_bf16 v[132:135], a[12:15], a[144:147], v[132:135]\n" \
"s_lshl_b32 s18, s18, 11\n" \
"s_add_u32 s18, s18, 589824\n" \
"s_lshl2_add_u32 s39, s68, s18\n" \
"v_mfma_f32_16x16x32_bf16 v[140:143], a[12:15], a[148:151], v[140:143]\n" \
"s_lshl_b32 s20, s64, 8\n" \
"ds_read_b64_tr_b16 a[4:5], v241 offset:12288\n" \
"ds_read_b64_tr_b16 a[6:7], v241 offset:14336\n" \
"v_mfma_f32_16x16x32_bf16 v[140:143], a[8:11], a[140:143], v[140:143]\n" \
"s_waitcnt lgkmcnt(8)\n" \
"s_add_u32 s18, s18, s20\n" \
"v_mfma_f32_16x16x32_bf16 v[144:147], a[16:19], a[136:139], v[144:147]\n" \
"ds_read_b64_tr_b16 a[8:9], v235 offset:4096\n" \
"ds_read_b64_tr_b16 a[10:11], v235 offset:6144\n" \
"v_mfma_f32_16x16x32_bf16 v[144:147], a[20:23], a[144:147], v[144:147]\n" \
"v_mfma_f32_16x16x32_bf16 v[152:155], a[20:23], a[148:151], v[152:155]\n" \
"ds_read_b64_tr_b16 a[12:13], v235 offset:12288\n" \
"ds_read_b64_tr_b16 a[14:15], v235 offset:14336\n" \
"v_mfma_f32_16x16x32_bf16 v[152:155], a[16:19], a[140:143], v[152:155]\n" \
"s_waitcnt lgkmcnt(8)\n" \
"v_mfma_f32_16x16x32_bf16 v[148:151], a[24:27], a[136:139], v[148:151]\n" \
"ds_read_b64_tr_b16 a[16:17], v241 offset:20480\n" \
"ds_read_b64_tr_b16 a[18:19], v241 offset:22528\n" \
"v_mfma_f32_16x16x32_bf16 v[148:151], a[28:31], a[144:147], v[148:151]\n" \
"v_cvt_pk_bf16_f32 v128, v128, v129\n" \
"v_cvt_pk_bf16_f32 v129, v130, v131\n" \
"v_mfma_f32_16x16x32_bf16 v[156:159], a[28:31], a[148:151], v[156:159]\n" \
"ds_read_b64_tr_b16 a[20:21], v241 offset:28672\n" \
"ds_read_b64_tr_b16 a[22:23], v241 offset:30720\n" \
"v_mfma_f32_16x16x32_bf16 v[156:159], a[24:27], a[140:143], v[156:159]\n" \
"s_waitcnt lgkmcnt(8)\n" \
"v_cvt_pk_bf16_f32 v130, v132, v133\n" \
"v_cvt_pk_bf16_f32 v131, v134, v135\n" \
"v_mfma_f32_16x16x32_bf16 v[160:163], a[0:3], v[32:35], v[160:163]\n" \
"ds_read_b64_tr_b16 a[24:25], v235 offset:20480\n" \
"ds_read_b64_tr_b16 a[26:27], v235 offset:22528\n" \
"v_mfma_f32_16x16x32_bf16 v[160:163], a[4:7], v[40:43], v[160:163]\n" \
"v_mfma_f32_16x16x32_bf16 v[168:171], a[4:7], v[44:47], v[168:171]\n" \
"ds_read_b64_tr_b16 a[28:29], v235 offset:28672\n" \
"ds_read_b64_tr_b16 a[30:31], v235 offset:30720\n" \
"v_mfma_f32_16x16x32_bf16 v[168:171], a[0:3], v[36:39], v[168:171]\n" \
"s_waitcnt lgkmcnt(8)\n" \
"buffer_store_dwordx4 v[128:131], v250, s[32:35], s17 offen offset:0 sc1\n" \
"v_mfma_f32_16x16x32_bf16 v[164:167], a[8:11], v[32:35], v[164:167]\n" \
"ds_read_b64_tr_b16 a[0:1], v244 offset:4096\n" \
"ds_read_b64_tr_b16 a[2:3], v244 offset:6144\n" \
"v_mfma_f32_16x16x32_bf16 v[164:167], a[12:15], v[40:43], v[164:167]\n" \
"buffer_load_dword v247, v254, s[32:35], s41 offen sc1\n" \
"v_mfma_f32_16x16x32_bf16 v[172:175], a[12:15], v[44:47], v[172:175]\n" \
"ds_read_b64_tr_b16 a[4:5], v244 offset:12288\n" \
"ds_read_b64_tr_b16 a[6:7], v244 offset:14336\n" \
"v_mfma_f32_16x16x32_bf16 v[172:175], a[8:11], v[36:39], v[172:175]\n" \
"s_waitcnt lgkmcnt(8)\n" \
"v_cvt_pk_bf16_f32 v136, v136, v137\n" \
"v_cvt_pk_bf16_f32 v137, v138, v139\n" \
"v_mfma_f32_16x16x32_bf16 v[160:163], a[16:19], v[48:51], v[160:163]\n" \
"ds_read_b64_tr_b16 a[8:9], v234 offset:4096\n" \
"ds_read_b64_tr_b16 a[10:11], v234 offset:6144\n" \
"v_mfma_f32_16x16x32_bf16 v[160:163], a[20:23], v[56:59], v[160:163]\n" \
"v_cvt_pk_bf16_f32 v138, v140, v141\n" \
"v_cvt_pk_bf16_f32 v139, v142, v143\n" \
"v_mfma_f32_16x16x32_bf16 v[168:171], a[20:23], v[60:63], v[168:171]\n" \
"ds_read_b64_tr_b16 a[12:13], v234 offset:12288\n" \
"ds_read_b64_tr_b16 a[14:15], v234 offset:14336\n" \
"v_mfma_f32_16x16x32_bf16 v[168:171], a[16:19], v[52:55], v[168:171]\n" \
"s_waitcnt lgkmcnt(8)\n" \
"v_mfma_f32_16x16x32_bf16 v[164:167], a[24:27], v[48:51], v[164:167]\n" \
"ds_read_b64_tr_b16 a[16:17], v249 offset:4096\n" \
"ds_read_b64_tr_b16 a[18:19], v249 offset:6144\n" \
"v_mfma_f32_16x16x32_bf16 v[164:167], a[28:31], v[56:59], v[164:167]\n" \
"v_mfma_f32_16x16x32_bf16 v[172:175], a[28:31], v[60:63], v[172:175]\n" \
"ds_read_b64_tr_b16 a[20:21], v249 offset:12288\n" \
"ds_read_b64_tr_b16 a[22:23], v249 offset:14336\n" \
"v_mfma_f32_16x16x32_bf16 v[172:175], a[24:27], v[52:55], v[172:175]\n" \
"s_waitcnt lgkmcnt(8)\n" \
"buffer_store_dwordx4 v[136:139], v250, s[32:35], s16 offen offset:0 sc1\n" \
"v_mfma_f32_16x16x32_bf16 v[160:163], a[0:3], a[64:67], v[160:163]\n" \
"ds_read_b64_tr_b16 a[24:25], v233 offset:4096\n" \
"ds_read_b64_tr_b16 a[26:27], v233 offset:6144\n" \
"v_mfma_f32_16x16x32_bf16 v[160:163], a[4:7], a[80:83], v[160:163]\n" \
"v_mfma_f32_16x16x32_bf16 v[168:171], a[4:7], a[88:91], v[168:171]\n" \
"ds_read_b64_tr_b16 a[28:29], v233 offset:12288\n" \
"ds_read_b64_tr_b16 a[30:31], v233 offset:14336\n" \
"v_mfma_f32_16x16x32_bf16 v[168:171], a[0:3], a[72:75], v[168:171]\n" \
"s_waitcnt lgkmcnt(8)\n" \
"v_cvt_pk_bf16_f32 v144, v144, v145\n" \
"v_cvt_pk_bf16_f32 v145, v146, v147\n" \
"v_mfma_f32_16x16x32_bf16 v[164:167], a[8:11], a[64:67], v[164:167]\n" \
"ds_read_b64_tr_b16 a[0:1], v239 offset:4096\n" \
"ds_read_b64_tr_b16 a[2:3], v239 offset:6144\n" \
"v_mfma_f32_16x16x32_bf16 v[164:167], a[12:15], a[80:83], v[164:167]\n" \
"v_cvt_pk_bf16_f32 v146, v148, v149\n" \
"v_cvt_pk_bf16_f32 v147, v150, v151\n" \
"v_mfma_f32_16x16x32_bf16 v[172:175], a[12:15], a[88:91], v[172:175]\n" \
"ds_read_b64_tr_b16 a[4:5], v239 offset:12288\n" \
"ds_read_b64_tr_b16 a[6:7], v239 offset:14336\n" \
"v_mfma_f32_16x16x32_bf16 v[172:175], a[8:11], a[72:75], v[172:175]\n" \
"s_waitcnt lgkmcnt(8)\n" \
"v_mfma_f32_16x16x32_bf16 v[160:163], a[16:19], a[96:99], v[160:163]\n" \
"ds_read_b64_tr_b16 a[8:9], v232 offset:4096\n" \
"ds_read_b64_tr_b16 a[10:11], v232 offset:6144\n" \
"v_mfma_f32_16x16x32_bf16 v[160:163], a[20:23], a[112:115], v[160:163]\n" \
"v_mfma_f32_16x16x32_bf16 v[168:171], a[20:23], a[116:119], v[168:171]\n" \
"ds_read_b64_tr_b16 a[12:13], v232 offset:12288\n" \
"ds_read_b64_tr_b16 a[14:15], v232 offset:14336\n" \
"v_mfma_f32_16x16x32_bf16 v[168:171], a[16:19], a[104:107], v[168:171]\n" \
"s_waitcnt lgkmcnt(8)\n" \
"buffer_store_dwordx4 v[144:147], v250, s[32:35], s17 offen offset:1024 sc1\n" \
"v_mfma_f32_16x16x32_bf16 v[164:167], a[24:27], a[96:99], v[164:167]\n" \
"ds_read_b64_tr_b16 a[16:17], v239 offset:20480\n" \
"ds_read_b64_tr_b16 a[18:19], v239 offset:22528\n" \
"v_mfma_f32_16x16x32_bf16 v[164:167], a[28:31], a[112:115], v[164:167]\n" \
"v_mfma_f32_16x16x32_bf16 v[172:175], a[28:31], a[116:119], v[172:175]\n" \
"ds_read_b64_tr_b16 a[20:21], v239 offset:28672\n" \
"ds_read_b64_tr_b16 a[22:23], v239 offset:30720\n" \
"v_mfma_f32_16x16x32_bf16 v[172:175], a[24:27], a[104:107], v[172:175]\n" \
"s_waitcnt lgkmcnt(8)\n" \
"v_cvt_pk_bf16_f32 v152, v152, v153\n" \
"v_cvt_pk_bf16_f32 v153, v154, v155\n" \
"v_mfma_f32_16x16x32_bf16 v[160:163], a[0:3], a[120:123], v[160:163]\n" \
"ds_read_b64_tr_b16 a[24:25], v232 offset:20480\n" \
"ds_read_b64_tr_b16 a[26:27], v232 offset:22528\n" \
"v_mfma_f32_16x16x32_bf16 v[160:163], a[4:7], a[128:131], v[160:163]\n" \
"v_cvt_pk_bf16_f32 v154, v156, v157\n" \
"v_cvt_pk_bf16_f32 v155, v158, v159\n" \
"v_mfma_f32_16x16x32_bf16 v[168:171], a[4:7], a[132:135], v[168:171]\n" \
"ds_read_b64_tr_b16 a[28:29], v232 offset:28672\n" \
"ds_read_b64_tr_b16 a[30:31], v232 offset:30720\n" \
"v_mfma_f32_16x16x32_bf16 v[168:171], a[0:3], a[124:127], v[168:171]\n" \
"s_waitcnt lgkmcnt(8)\n" \
"v_mfma_f32_16x16x32_bf16 v[164:167], a[8:11], a[120:123], v[164:167]\n" \
"ds_read_b64_tr_b16 a[0:1], v241 offset:4608\n" \
"ds_read_b64_tr_b16 a[2:3], v241 offset:6656\n" \
"v_mfma_f32_16x16x32_bf16 v[164:167], a[12:15], a[128:131], v[164:167]\n" \
"v_mfma_f32_16x16x32_bf16 v[172:175], a[12:15], a[132:135], v[172:175]\n" \
"ds_read_b64_tr_b16 a[4:5], v241 offset:12800\n" \
"ds_read_b64_tr_b16 a[6:7], v241 offset:14848\n" \
"v_mfma_f32_16x16x32_bf16 v[172:175], a[8:11], a[124:127], v[172:175]\n" \
"s_waitcnt lgkmcnt(8)\n" \
"buffer_store_dwordx4 v[152:155], v250, s[32:35], s16 offen offset:1024 sc1\n" \
"v_mfma_f32_16x16x32_bf16 v[160:163], a[16:19], a[136:139], v[160:163]\n" \
"ds_read_b64_tr_b16 a[8:9], v235 offset:4608\n" \
"ds_read_b64_tr_b16 a[10:11], v235 offset:6656\n" \
"v_mfma_f32_16x16x32_bf16 v[160:163], a[20:23], a[144:147], v[160:163]\n" \
"v_mfma_f32_16x16x32_bf16 v[168:171], a[20:23], a[148:151], v[168:171]\n" \
"ds_read_b64_tr_b16 a[12:13], v235 offset:12800\n" \
"ds_read_b64_tr_b16 a[14:15], v235 offset:14848\n" \
"v_mfma_f32_16x16x32_bf16 v[168:171], a[16:19], a[140:143], v[168:171]\n" \
"s_waitcnt lgkmcnt(8)\n" \
"v_mfma_f32_16x16x32_bf16 v[164:167], a[24:27], a[136:139], v[164:167]\n" \
"ds_read_b64_tr_b16 a[16:17], v241 offset:20992\n" \
"ds_read_b64_tr_b16 a[18:19], v241 offset:23040\n" \
"v_mfma_f32_16x16x32_bf16 v[164:167], a[28:31], a[144:147], v[164:167]\n" \
"v_mfma_f32_16x16x32_bf16 v[172:175], a[28:31], a[148:151], v[172:175]\n" \
"ds_read_b64_tr_b16 a[20:21], v241 offset:29184\n" \
"ds_read_b64_tr_b16 a[22:23], v241 offset:31232\n" \
"v_mfma_f32_16x16x32_bf16 v[172:175], a[24:27], a[140:143], v[172:175]\n" \
"s_waitcnt lgkmcnt(8)\n" \
"v_mfma_f32_16x16x32_bf16 v[176:179], a[0:3], v[32:35], v[176:179]\n" \
"ds_read_b64_tr_b16 a[24:25], v235 offset:20992\n" \
"ds_read_b64_tr_b16 a[26:27], v235 offset:23040\n" \
"v_mfma_f32_16x16x32_bf16 v[176:179], a[4:7], v[40:43], v[176:179]\n" \
"v_mfma_f32_16x16x32_bf16 v[184:187], a[4:7], v[44:47], v[184:187]\n" \
"ds_read_b64_tr_b16 a[28:29], v235 offset:29184\n" \
"ds_read_b64_tr_b16 a[30:31], v235 offset:31232\n" \
"v_mfma_f32_16x16x32_bf16 v[184:187], a[0:3], v[36:39], v[184:187]\n" \
"s_waitcnt lgkmcnt(8)\n" \
"v_mfma_f32_16x16x32_bf16 v[180:183], a[8:11], v[32:35], v[180:183]\n" \
"ds_read_b64_tr_b16 a[0:1], v244 offset:4608\n" \
"ds_read_b64_tr_b16 a[2:3], v244 offset:6656\n" \
"v_mfma_f32_16x16x32_bf16 v[180:183], a[12:15], v[40:43], v[180:183]\n" \
"v_cvt_pk_bf16_f32 v160, v160, v161\n" \
"v_cvt_pk_bf16_f32 v161, v162, v163\n" \
"v_mfma_f32_16x16x32_bf16 v[188:191], a[12:15], v[44:47], v[188:191]\n" \
"ds_read_b64_tr_b16 a[4:5], v244 offset:12800\n" \
"ds_read_b64_tr_b16 a[6:7], v244 offset:14848\n" \
"v_mfma_f32_16x16x32_bf16 v[188:191], a[8:11], v[36:39], v[188:191]\n" \
"s_waitcnt lgkmcnt(8)\n" \
"v_cvt_pk_bf16_f32 v162, v164, v165\n" \
"v_cvt_pk_bf16_f32 v163, v166, v167\n" \
"v_mfma_f32_16x16x32_bf16 v[176:179], a[16:19], v[48:51], v[176:179]\n" \
"s_waitcnt vmcnt(3)\n" \
"ds_read_b64_tr_b16 a[8:9], v234 offset:4608\n" \
"v_cmp_eq_u32 vcc, 0, v247\n" \
"v_mfma_f32_16x16x32_bf16 v[176:179], a[20:23], v[56:59], v[176:179]\n" \
"s_add_u32 s38, s66, 1\n" \
"s_and_b32 s38, s38, 3\n" \
"ds_read_b64_tr_b16 a[10:11], v234 offset:6656\n" \
"v_mfma_f32_16x16x32_bf16 v[184:187], a[20:23], v[60:63], v[184:187]\n" \
"s_lshl_b32 s38, s38, 13\n" \
"ds_read_b64_tr_b16 a[12:13], v234 offset:12800\n" \
"ds_read_b64_tr_b16 a[14:15], v234 offset:14848\n" \
"v_mfma_f32_16x16x32_bf16 v[184:187], a[16:19], v[52:55], v[184:187]\n" \
"s_waitcnt lgkmcnt(8)\n" \
"s_cbranch_vccnz .LspcA_%=\n" \
".LsbcA_%=:\n" \
"v_mfma_f32_16x16x32_bf16 v[180:183], a[24:27], v[48:51], v[180:183]\n" \
"s_add_u32 s38, s38, s37\n" \
"ds_read_b64_tr_b16 a[16:17], v249 offset:4608\n" \
"ds_read_b64_tr_b16 a[18:19], v249 offset:6656\n" \
"v_mfma_f32_16x16x32_bf16 v[180:183], a[28:31], v[56:59], v[180:183]\n" \
"buffer_store_dwordx4 v[160:163], v250, s[32:35], s17 offen offset:2048 sc1\n" \
"v_mfma_f32_16x16x32_bf16 v[188:191], a[28:31], v[60:63], v[188:191]\n" \
"ds_read_b64_tr_b16 a[20:21], v249 offset:12800\n" \
"ds_read_b64_tr_b16 a[22:23], v249 offset:14848\n" \
"v_mfma_f32_16x16x32_bf16 v[188:191], a[24:27], v[52:55], v[188:191]\n" \
"s_waitcnt lgkmcnt(8)\n" \
"v_cvt_pk_bf16_f32 v168, v168, v169\n" \
"v_cvt_pk_bf16_f32 v169, v170, v171\n" \
"v_mfma_f32_16x16x32_bf16 v[176:179], a[0:3], a[64:67], v[176:179]\n" \
"ds_read_b64_tr_b16 a[24:25], v233 offset:4608\n" \
"ds_read_b64_tr_b16 a[26:27], v233 offset:6656\n" \
"v_mfma_f32_16x16x32_bf16 v[176:179], a[4:7], a[80:83], v[176:179]\n" \
"buffer_load_dwordx4 a[192:195], v248, s[32:35], s38 offen offset:0 sc1\n" \
"v_cvt_pk_bf16_f32 v170, v172, v173\n" \
"v_mfma_f32_16x16x32_bf16 v[184:187], a[4:7], a[88:91], v[184:187]\n" \
"ds_read_b64_tr_b16 a[28:29], v233 offset:12800\n" \
"ds_read_b64_tr_b16 a[30:31], v233 offset:14848\n" \
"v_mfma_f32_16x16x32_bf16 v[184:187], a[0:3], a[72:75], v[184:187]\n" \
"s_waitcnt lgkmcnt(8)\n" \
"v_cvt_pk_bf16_f32 v171, v174, v175\n" \
"v_mfma_f32_16x16x32_bf16 v[180:183], a[8:11], a[64:67], v[180:183]\n" \
"ds_read_b64_tr_b16 a[0:1], v239 offset:4608\n" \
"ds_read_b64_tr_b16 a[2:3], v239 offset:6656\n" \
"v_mfma_f32_16x16x32_bf16 v[180:183], a[12:15], a[80:83], v[180:183]\n" \
"buffer_load_dwordx4 a[196:199], v248, s[32:35], s38 offen offset:1024 sc1\n" \
"v_mfma_f32_16x16x32_bf16 v[188:191], a[12:15], a[88:91], v[188:191]\n" \
"ds_read_b64_tr_b16 a[4:5], v239 offset:12800\n" \
"ds_read_b64_tr_b16 a[6:7], v239 offset:14848\n" \
"v_mfma_f32_16x16x32_bf16 v[188:191], a[8:11], a[72:75], v[188:191]\n" \
"s_waitcnt lgkmcnt(8)\n" \
"buffer_store_dwordx4 v[168:171], v250, s[32:35], s16 offen offset:2048 sc1\n" \
"v_mfma_f32_16x16x32_bf16 v[176:179], a[16:19], a[96:99], v[176:179]\n" \
"ds_read_b64_tr_b16 a[8:9], v232 offset:4608\n" \
"ds_read_b64_tr_b16 a[10:11], v232 offset:6656\n" \
"v_mfma_f32_16x16x32_bf16 v[176:179], a[20:23], a[112:115], v[176:179]\n" \
"v_mfma_f32_16x16x32_bf16 v[184:187], a[20:23], a[116:119], v[184:187]\n" \
"ds_read_b64_tr_b16 a[12:13], v232 offset:12800\n" \
"ds_read_b64_tr_b16 a[14:15], v232 offset:14848\n" \
"v_mfma_f32_16x16x32_bf16 v[184:187], a[16:19], a[104:107], v[184:187]\n" \
"s_waitcnt lgkmcnt(8)\n" \
"buffer_load_dwordx4 a[200:203], v248, s[32:35], s38 offen offset:2048 sc1\n" \
"v_mfma_f32_16x16x32_bf16 v[180:183], a[24:27], a[96:99], v[180:183]\n" \
"ds_read_b64_tr_b16 a[16:17], v239 offset:20992\n" \
"ds_read_b64_tr_b16 a[18:19], v239 offset:23040\n" \
"v_mfma_f32_16x16x32_bf16 v[180:183], a[28:31], a[112:115], v[180:183]\n" \
"v_mfma_f32_16x16x32_bf16 v[188:191], a[28:31], a[116:119], v[188:191]\n" \
"ds_read_b64_tr_b16 a[20:21], v239 offset:29184\n" \
"ds_read_b64_tr_b16 a[22:23], v239 offset:31232\n" \
"v_mfma_f32_16x16x32_bf16 v[188:191], a[24:27], a[104:107], v[188:191]\n" \
"s_waitcnt lgkmcnt(8)\n" \
"buffer_load_dwordx4 a[204:207], v248, s[32:35], s38 offen offset:3072 sc1\n" \
"v_mfma_f32_16x16x32_bf16 v[176:179], a[0:3], a[120:123], v[176:179]\n" \
"s_add_u32 s38, s38, 4096\n" \
"ds_read_b64_tr_b16 a[24:25], v232 offset:20992\n" \
"ds_read_b64_tr_b16 a[26:27], v232 offset:23040\n" \
"v_mfma_f32_16x16x32_bf16 v[176:179], a[4:7], a[128:131], v[176:179]\n" \
"v_mfma_f32_16x16x32_bf16 v[184:187], a[4:7], a[132:135], v[184:187]\n" \
"ds_read_b64_tr_b16 a[28:29], v232 offset:29184\n" \
"ds_read_b64_tr_b16 a[30:31], v232 offset:31232\n" \
"v_mfma_f32_16x16x32_bf16 v[184:187], a[0:3], a[124:127], v[184:187]\n" \
"s_waitcnt lgkmcnt(8)\n" \
"buffer_load_dwordx4 a[208:211], v248, s[32:35], s38 offen offset:0 sc1\n" \
"v_mfma_f32_16x16x32_bf16 v[180:183], a[8:11], a[120:123], v[180:183]\n" \
"ds_read_b64_tr_b16 a[0:1], v240 offset:0\n" \
"ds_read_b64_tr_b16 a[2:3], v240 offset:2048\n" \
"v_mfma_f32_16x16x32_bf16 v[180:183], a[12:15], a[128:131], v[180:183]\n" \
"v_mfma_f32_16x16x32_bf16 v[188:191], a[12:15], a[132:135], v[188:191]\n" \
"ds_read_b64_tr_b16 a[4:5], v240 offset:8192\n" \
"ds_read_b64_tr_b16 a[6:7], v240 offset:10240\n" \
"v_mfma_f32_16x16x32_bf16 v[188:191], a[8:11], a[124:127], v[188:191]\n" \
"s_waitcnt lgkmcnt(8)\n" \
"buffer_load_dwordx4 a[212:215], v248, s[32:35], s38 offen offset:1024 sc1\n" \
"v_mfma_f32_16x16x32_bf16 v[176:179], a[16:19], a[136:139], v[176:179]\n" \
"ds_read_b64_tr_b16 a[8:9], v236 offset:0\n" \
"ds_read_b64_tr_b16 a[10:11], v236 offset:2048\n" \
"v_mfma_f32_16x16x32_bf16 v[176:179], a[20:23], a[144:147], v[176:179]\n" \
"v_mfma_f32_16x16x32_bf16 v[184:187], a[20:23], a[148:151], v[184:187]\n" \
"ds_read_b64_tr_b16 a[12:13], v236 offset:8192\n" \
"ds_read_b64_tr_b16 a[14:15], v236 offset:10240\n" \
"v_mfma_f32_16x16x32_bf16 v[184:187], a[16:19], a[140:143], v[184:187]\n" \
"s_waitcnt lgkmcnt(8)\n" \
"buffer_load_dwordx4 a[216:219], v248, s[32:35], s38 offen offset:2048 sc1\n" \
"v_mfma_f32_16x16x32_bf16 v[180:183], a[24:27], a[136:139], v[180:183]\n" \
"ds_read_b64_tr_b16 a[16:17], v240 offset:512\n" \
"ds_read_b64_tr_b16 a[18:19], v240 offset:2560\n" \
"v_mfma_f32_16x16x32_bf16 v[180:183], a[28:31], a[144:147], v[180:183]\n" \
"v_mfma_f32_16x16x32_bf16 v[188:191], a[28:31], a[148:151], v[188:191]\n" \
"ds_read_b64_tr_b16 a[20:21], v240 offset:8704\n" \
"ds_read_b64_tr_b16 a[22:23], v240 offset:10752\n" \
"v_mfma_f32_16x16x32_bf16 v[188:191], a[24:27], a[140:143], v[188:191]\n" \
"s_waitcnt lgkmcnt(8)\n" \
"buffer_load_dwordx4 a[220:223], v248, s[32:35], s38 offen offset:3072 sc1\n" \
"v_mfma_f32_16x16x32_bf16 v[64:67], a[0:3], v[192:195], 0\n" \
"s_add_u32 s38, s66, 2\n" \
"ds_read_b64_tr_b16 a[24:25], v236 offset:512\n" \
"ds_read_b64_tr_b16 a[26:27], v236 offset:2560\n" \
"v_mfma_f32_16x16x32_bf16 v[64:67], a[4:7], v[200:203], v[64:67]\n" \
"s_and_b32 s38, s38, 3\n" \
"s_lshl_b32 s38, s38, 13\n" \
"s_add_u32 s38, s38, s37\n" \
"v_mfma_f32_16x16x32_bf16 v[72:75], a[4:7], v[204:207], 0\n" \
"ds_read_b64_tr_b16 a[28:29], v236 offset:8704\n" \
"ds_read_b64_tr_b16 a[30:31], v236 offset:10752\n" \
"v_mfma_f32_16x16x32_bf16 v[72:75], a[0:3], v[196:199], v[72:75]\n" \
"s_waitcnt lgkmcnt(8)\n" \
"buffer_load_dwordx4 a[224:227], v248, s[32:35], s38 offen offset:0 sc1\n" \
"v_mfma_f32_16x16x32_bf16 v[68:71], a[8:11], v[192:195], 0\n" \
"ds_read_b64_tr_b16 a[0:1], v240 offset:4096\n" \
"ds_read_b64_tr_b16 a[2:3], v240 offset:6144\n" \
"v_mfma_f32_16x16x32_bf16 v[68:71], a[12:15], v[200:203], v[68:71]\n" \
"v_cvt_pk_bf16_f32 v176, v176, v177\n" \
"v_cvt_pk_bf16_f32 v177, v178, v179\n" \
"v_mfma_f32_16x16x32_bf16 v[76:79], a[12:15], v[204:207], 0\n" \
"ds_read_b64_tr_b16 a[4:5], v240 offset:12288\n" \
"ds_read_b64_tr_b16 a[6:7], v240 offset:14336\n" \
"v_mfma_f32_16x16x32_bf16 v[76:79], a[8:11], v[196:199], v[76:79]\n" \
"s_waitcnt lgkmcnt(8)\n" \
"buffer_load_dwordx4 a[228:231], v248, s[32:35], s38 offen offset:1024 sc1\n" \
"v_cvt_pk_bf16_f32 v178, v180, v181\n" \
"v_mfma_f32_16x16x32_bf16 v[80:83], a[16:19], v[192:195], 0\n" \
"ds_read_b64_tr_b16 a[8:9], v236 offset:4096\n" \
"ds_read_b64_tr_b16 a[10:11], v236 offset:6144\n" \
"v_mfma_f32_16x16x32_bf16 v[80:83], a[20:23], v[200:203], v[80:83]\n" \
"v_cvt_pk_bf16_f32 v179, v182, v183\n" \
"v_mfma_f32_16x16x32_bf16 v[88:91], a[20:23], v[204:207], 0\n" \
"ds_read_b64_tr_b16 a[12:13], v236 offset:12288\n" \
"ds_read_b64_tr_b16 a[14:15], v236 offset:14336\n" \
"v_mfma_f32_16x16x32_bf16 v[88:91], a[16:19], v[196:199], v[88:91]\n" \
"s_waitcnt lgkmcnt(8)\n" \
"buffer_store_dwordx4 v[176:179], v250, s[32:35], s17 offen offset:3072 sc1\n" \
"v_mfma_f32_16x16x32_bf16 v[84:87], a[24:27], v[192:195], 0\n" \
"ds_read_b64_tr_b16 a[16:17], v240 offset:4608\n" \
"ds_read_b64_tr_b16 a[18:19], v240 offset:6656\n" \
"v_mfma_f32_16x16x32_bf16 v[84:87], a[28:31], v[200:203], v[84:87]\n" \
"v_mfma_f32_16x16x32_bf16 v[92:95], a[28:31], v[204:207], 0\n" \
"ds_read_b64_tr_b16 a[20:21], v240 offset:12800\n" \
"ds_read_b64_tr_b16 a[22:23], v240 offset:14848\n" \
"v_mfma_f32_16x16x32_bf16 v[92:95], a[24:27], v[196:199], v[92:95]\n" \
"s_waitcnt lgkmcnt(8)\n" \
"buffer_load_dwordx4 a[232:235], v248, s[32:35], s38 offen offset:2048 sc1\n" \
"v_cvt_pk_bf16_f32 v184, v184, v185\n" \
"v_mfma_f32_16x16x32_bf16 v[96:99], a[0:3], v[192:195], 0\n" \
"ds_read_b64_tr_b16 a[24:25], v236 offset:4608\n" \
"ds_read_b64_tr_b16 a[26:27], v236 offset:6656\n" \
"v_mfma_f32_16x16x32_bf16 v[96:99], a[4:7], v[200:203], v[96:99]\n" \
"v_cvt_pk_bf16_f32 v185, v186, v187\n" \
"v_cvt_pk_bf16_f32 v186, v188, v189\n" \
"v_mfma_f32_16x16x32_bf16 v[104:107], a[4:7], v[204:207], 0\n" \
"ds_read_b64_tr_b16 a[28:29], v236 offset:12800\n" \
"ds_read_b64_tr_b16 a[30:31], v236 offset:14848\n" \
"v_mfma_f32_16x16x32_bf16 v[104:107], a[0:3], v[196:199], v[104:107]\n" \
"s_waitcnt lgkmcnt(8)\n" \
"buffer_load_dwordx4 a[236:239], v248, s[32:35], s38 offen offset:3072 sc1\n" \
"v_cvt_pk_bf16_f32 v187, v190, v191\n" \
"v_mfma_f32_16x16x32_bf16 v[100:103], a[8:11], v[192:195], 0\n" \
"s_add_u32 s38, s38, 4096\n" \
"ds_read_b64_tr_b16 a[0:1], v240 offset:16384\n" \
"ds_read_b64_tr_b16 a[2:3], v240 offset:18432\n" \
"v_mfma_f32_16x16x32_bf16 v[100:103], a[12:15], v[200:203], v[100:103]\n" \
"v_mfma_f32_16x16x32_bf16 v[108:111], a[12:15], v[204:207], 0\n" \
"ds_read_b64_tr_b16 a[4:5], v240 offset:24576\n" \
"ds_read_b64_tr_b16 a[6:7], v240 offset:26624\n" \
"v_mfma_f32_16x16x32_bf16 v[108:111], a[8:11], v[196:199], v[108:111]\n" \
"s_waitcnt lgkmcnt(8)\n" \
"buffer_store_dwordx4 v[184:187], v250, s[32:35], s16 offen offset:3072 sc1\n" \
"v_mfma_f32_16x16x32_bf16 v[112:115], a[16:19], v[192:195], 0\n" \
".Lst1_7_%=:\n" \
"s_cbranch_execz .Lst1_11_%=\n" \
"ds_read_b64_tr_b16 a[8:9], v236 offset:16384\n" \
"ds_read_b64_tr_b16 a[10:11], v236 offset:18432\n" \
"v_mfma_f32_16x16x32_bf16 v[112:115], a[20:23], v[200:203], v[112:115]\n" \
"v_lshrrev_b32 v188, 2, v251\n" \
"v_lshlrev_b32 v188, 6, v188\n" \
"v_mfma_f32_16x16x32_bf16 v[120:123], a[20:23], v[204:207], 0\n" \
"ds_read_b64_tr_b16 a[12:13], v236 offset:24576\n" \
"ds_read_b64_tr_b16 a[14:15], v236 offset:26624\n" \
"v_mfma_f32_16x16x32_bf16 v[120:123], a[16:19], v[196:199], v[120:123]\n" \
"s_waitcnt lgkmcnt(8)\n" \
"buffer_load_dwordx4 a[240:243], v248, s[32:35], s38 offen offset:0 sc1\n" \
"v_and_b32 v189, 3, v251\n" \
"v_mfma_f32_16x16x32_bf16 v[116:119], a[24:27], v[192:195], 0\n" \
"ds_read_b64_tr_b16 a[16:17], v240 offset:16896\n" \
"ds_read_b64_tr_b16 a[18:19], v240 offset:18944\n" \
"v_mfma_f32_16x16x32_bf16 v[116:119], a[28:31], v[200:203], v[116:119]\n" \
"v_lshl_or_b32 v188, v189, 3, v188\n" \
"v_mfma_f32_16x16x32_bf16 v[124:127], a[28:31], v[204:207], 0\n" \
"s_lshl_b32 s19, %[wid], 6\n" \
"ds_read_b64_tr_b16 a[20:21], v240 offset:25088\n" \
"ds_read_b64_tr_b16 a[22:23], v240 offset:27136\n" \
"v_mfma_f32_16x16x32_bf16 v[124:127], a[24:27], v[196:199], v[124:127]\n" \
"s_waitcnt lgkmcnt(8)\n" \
"buffer_load_dwordx4 a[244:247], v248, s[32:35], s38 offen offset:1024 sc1\n" \
"v_add_u32 v191, s19, v251\n" \
"v_mfma_f32_16x16x32_bf16 v[64:67], a[0:3], v[208:211], v[64:67]\n" \
"ds_read_b64_tr_b16 a[24:25], v236 offset:16896\n" \
"ds_read_b64_tr_b16 a[26:27], v236 offset:18944\n" \
"v_mfma_f32_16x16x32_bf16 v[64:67], a[4:7], v[216:219], v[64:67]\n" \
"v_lshlrev_b32 v190, 3, v191\n" \
"v_mfma_f32_16x16x32_bf16 v[72:75], a[4:7], v[220:223], v[72:75]\n" \
"ds_read_b64_tr_b16 a[28:29], v236 offset:25088\n" \
"ds_read_b64_tr_b16 a[30:31], v236 offset:27136\n" \
"v_mfma_f32_16x16x32_bf16 v[72:75], a[0:3], v[212:215], v[72:75]\n" \
"s_waitcnt lgkmcnt(8)\n" \
"buffer_load_dwordx4 a[248:251], v248, s[32:35], s38 offen offset:2048 sc1\n" \
"v_mfma_f32_16x16x32_bf16 v[68:71], a[8:11], v[208:211], v[68:71]\n" \
"ds_read_b64_tr_b16 a[0:1], v240 offset:20480\n" \
"ds_read_b64_tr_b16 a[2:3], v240 offset:22528\n" \
"v_mfma_f32_16x16x32_bf16 v[68:71], a[12:15], v[216:219], v[68:71]\n" \
"v_mfma_f32_16x16x32_bf16 v[76:79], a[12:15], v[220:223], v[76:79]\n" \
"ds_read_b64_tr_b16 a[4:5], v240 offset:28672\n" \
"ds_read_b64_tr_b16 a[6:7], v240 offset:30720\n" \
"v_mfma_f32_16x16x32_bf16 v[76:79], a[8:11], v[212:215], v[76:79]\n" \
"s_waitcnt lgkmcnt(8)\n" \
"buffer_load_dwordx4 a[252:255], v248, s[32:35], s38 offen offset:3072 sc1\n" \
"v_mfma_f32_16x16x32_bf16 v[80:83], a[16:19], v[208:211], v[80:83]\n" \
"s_add_u32 s38, s66, 3\n" \
"ds_read_b64_tr_b16 a[8:9], v236 offset:20480\n" \
"ds_read_b64_tr_b16 a[10:11], v236 offset:22528\n" \
"v_mfma_f32_16x16x32_bf16 v[80:83], a[20:23], v[216:219], v[80:83]\n" \
"s_and_b32 s38, s38, 3\n" \
"s_lshl_b32 s38, s38, 13\n" \
"s_add_u32 s38, s38, s37\n" \
"v_mfma_f32_16x16x32_bf16 v[88:91], a[20:23], v[220:223], v[88:91]\n" \
"ds_read_b64_tr_b16 a[12:13], v236 offset:28672\n" \
"ds_read_b64_tr_b16 a[14:15], v236 offset:30720\n" \
"v_mfma_f32_16x16x32_bf16 v[88:91], a[16:19], v[212:215], v[88:91]\n" \
"s_waitcnt lgkmcnt(8)\n" \
"buffer_load_dwordx4 a[64:67], v248, s[32:35], s38 offen offset:0 sc1\n" \
"v_mfma_f32_16x16x32_bf16 v[84:87], a[24:27], v[208:211], v[84:87]\n" \
"ds_read_b64_tr_b16 a[16:17], v240 offset:20992\n" \
"ds_read_b64_tr_b16 a[18:19], v240 offset:23040\n" \
"v_mfma_f32_16x16x32_bf16 v[84:87], a[28:31], v[216:219], v[84:87]\n" \
"v_mfma_f32_16x16x32_bf16 v[92:95], a[28:31], v[220:223], v[92:95]\n" \
"ds_read_b64_tr_b16 a[20:21], v240 offset:29184\n" \
"ds_read_b64_tr_b16 a[22:23], v240 offset:31232\n" \
"v_mfma_f32_16x16x32_bf16 v[92:95], a[24:27], v[212:215], v[92:95]\n" \
"s_waitcnt lgkmcnt(8)\n" \
"buffer_load_dwordx4 a[72:75], v248, s[32:35], s38 offen offset:1024 sc1\n" \
"v_mfma_f32_16x16x32_bf16 v[96:99], a[0:3], v[208:211], v[96:99]\n" \
"ds_read_b64_tr_b16 a[24:25], v236 offset:20992\n" \
"ds_read_b64_tr_b16 a[26:27], v236 offset:23040\n" \
"v_mfma_f32_16x16x32_bf16 v[96:99], a[4:7], v[216:219], v[96:99]\n" \
"v_mfma_f32_16x16x32_bf16 v[104:107], a[4:7], v[220:223], v[104:107]\n" \
"ds_read_b64_tr_b16 a[28:29], v236 offset:29184\n" \
"ds_read_b64_tr_b16 a[30:31], v236 offset:31232\n" \
"v_mfma_f32_16x16x32_bf16 v[104:107], a[0:3], v[212:215], v[104:107]\n" \
"s_waitcnt lgkmcnt(8)\n" \
"buffer_load_dwordx4 a[80:83], v248, s[32:35], s38 offen offset:2048 sc1\n" \
"v_mfma_f32_16x16x32_bf16 v[100:103], a[8:11], v[208:211], v[100:103]\n" \
"ds_read_b64_tr_b16 a[0:1], v241 offset:0\n" \
"ds_read_b64_tr_b16 a[2:3], v241 offset:2048\n" \
"v_mfma_f32_16x16x32_bf16 v[100:103], a[12:15], v[216:219], v[100:103]\n" \
"v_mfma_f32_16x16x32_bf16 v[108:111], a[12:15], v[220:223], v[108:111]\n" \
"ds_read_b64_tr_b16 a[4:5], v241 offset:8192\n" \
"ds_read_b64_tr_b16 a[6:7], v241 offset:10240\n" \
"v_mfma_f32_16x16x32_bf16 v[108:111], a[8:11], v[212:215], v[108:111]\n" \
"s_waitcnt lgkmcnt(8)\n" \
"buffer_load_dwordx4 a[88:91], v248, s[32:35], s38 offen offset:3072 sc1\n" \
"v_mfma_f32_16x16x32_bf16 v[112:115], a[16:19], v[208:211], v[112:115]\n" \
"s_add_u32 s38, s38, 4096\n" \
"ds_read_b64_tr_b16 a[8:9], v235 offset:0\n" \
"ds_read_b64_tr_b16 a[10:11], v235 offset:2048\n" \
"v_mfma_f32_16x16x32_bf16 v[112:115], a[20:23], v[216:219], v[112:115]\n" \
"v_mfma_f32_16x16x32_bf16 v[120:123], a[20:23], v[220:223], v[120:123]\n" \
"ds_read_b64_tr_b16 a[12:13], v235 offset:8192\n" \
"ds_read_b64_tr_b16 a[14:15], v235 offset:10240\n" \
"v_mfma_f32_16x16x32_bf16 v[120:123], a[16:19], v[212:215], v[120:123]\n" \
"s_waitcnt lgkmcnt(8)\n" \
"buffer_load_dwordx4 a[96:99], v248, s[32:35], s38 offen offset:0 sc1\n" \
"v_mfma_f32_16x16x32_bf16 v[116:119], a[24:27], v[208:211], v[116:119]\n" \
"ds_read_b64_tr_b16 a[16:17], v241 offset:512\n" \
"ds_read_b64_tr_b16 a[18:19], v241 offset:2560\n" \
"v_mfma_f32_16x16x32_bf16 v[116:119], a[28:31], v[216:219], v[116:119]\n" \
"v_mfma_f32_16x16x32_bf16 v[124:127], a[28:31], v[220:223], v[124:127]\n" \
"s_waitcnt vmcnt(19)\n" \
"ds_read_b64_tr_b16 a[20:21], v241 offset:8704\n" \
"v_mfma_f32_16x16x32_bf16 v[124:127], a[24:27], v[212:215], v[124:127]\n" \
"s_waitcnt lgkmcnt(7)\n" \
"ds_read_b64_tr_b16 a[22:23], v241 offset:10752\n" \
"v_mfma_f32_16x16x32_bf16 v[64:67], a[0:3], a[192:195], v[64:67]\n" \
"ds_read_b64_tr_b16 a[24:25], v235 offset:512\n" \
"ds_read_b64_tr_b16 a[26:27], v235 offset:2560\n" \
"v_mfma_f32_16x16x32_bf16 v[64:67], a[4:7], a[200:203], v[64:67]\n" \
"buffer_load_dwordx4 a[104:107], v248, s[32:35], s38 offen offset:1024 sc1\n" \
"v_mfma_f32_16x16x32_bf16 v[72:75], a[4:7], a[204:207], v[72:75]\n" \
"ds_read_b64_tr_b16 a[28:29], v235 offset:8704\n" \
"ds_read_b64_tr_b16 a[30:31], v235 offset:10752\n" \
"v_mfma_f32_16x16x32_bf16 v[72:75], a[0:3], a[196:199], v[72:75]\n" \
"s_waitcnt vmcnt(10) lgkmcnt(8)\n" \
"buffer_store_dword v253, v246, s[32:35], s39 offen sc1\n" \
"v_mfma_f32_16x16x32_bf16 v[68:71], a[8:11], a[192:195], v[68:71]\n" \
"ds_read_b64_tr_b16 a[0:1], v241 offset:16384\n" \
"ds_read_b64_tr_b16 a[2:3], v241 offset:18432\n" \
"v_mfma_f32_16x16x32_bf16 v[68:71], a[12:15], a[200:203], v[68:71]\n" \
"v_mfma_f32_16x16x32_bf16 v[76:79], a[12:15], a[204:207], v[76:79]\n" \
"ds_read_b64_tr_b16 a[4:5], v241 offset:24576\n" \
"ds_read_b64_tr_b16 a[6:7], v241 offset:26624\n" \
"v_mfma_f32_16x16x32_bf16 v[76:79], a[8:11], a[196:199], v[76:79]\n" \
"s_waitcnt lgkmcnt(8)\n" \
"buffer_load_dwordx4 a[112:115], v248, s[32:35], s38 offen offset:2048 sc1\n" \
"v_lshrrev_b32 v214, 4, v251\n" \
"v_mfma_f32_16x16x32_bf16 v[80:83], a[16:19], a[192:195], v[80:83]\n" \
"ds_read_b64_tr_b16 a[8:9], v235 offset:16384\n" \
"ds_read_b64_tr_b16 a[10:11], v235 offset:18432\n" \
"v_mfma_f32_16x16x32_bf16 v[80:83], a[20:23], a[200:203], v[80:83]\n" \
"v_lshlrev_b32 v214, 13, v214\n" \
"v_and_or_b32 v214, v248, 48, v214\n" \
"v_mfma_f32_16x16x32_bf16 v[88:91], a[20:23], a[204:207], v[88:91]\n" \
"ds_read_b64_tr_b16 a[12:13], v235 offset:24576\n" \
"ds_read_b64_tr_b16 a[14:15], v235 offset:26624\n" \
"v_mfma_f32_16x16x32_bf16 v[88:91], a[16:19], a[196:199], v[88:91]\n" \
"s_waitcnt lgkmcnt(8)\n" \
"buffer_load_dwordx4 a[116:119], v248, s[32:35], s38 offen offset:3072 sc1\n" \
"v_bfe_u32 v208, v251, 2, 2\n" \
"v_mfma_f32_16x16x32_bf16 v[84:87], a[24:27], a[192:195], v[84:87]\n" \
"ds_read_b64_tr_b16 a[16:17], v241 offset:16896\n" \
"ds_read_b64_tr_b16 a[18:19], v241 offset:18944\n" \
"v_mfma_f32_16x16x32_bf16 v[84:87], a[28:31], a[200:203], v[84:87]\n" \
"v_lshl_or_b32 v214, v208, 10, v214\n" \
"v_mov_b32 v208, 0\n" \
"v_mfma_f32_16x16x32_bf16 v[92:95], a[28:31], a[204:207], v[92:95]\n" \
"ds_read_b64_tr_b16 a[20:21], v241 offset:25088\n" \
"ds_read_b64_tr_b16 a[22:23], v241 offset:27136\n" \
"v_mfma_f32_16x16x32_bf16 v[92:95], a[24:27], a[196:199], v[92:95]\n" \
"s_waitcnt lgkmcnt(8)\n" \
"buffer_load_dwordx4 v[172:175], v248, s[32:35], 0 offen offset:0 sc1\n" \
"v_mov_b32 v209, 0\n" \
"v_mfma_f32_16x16x32_bf16 v[64:67], a[0:3], a[208:211], v[64:67]\n" \
"ds_read_b64_tr_b16 a[24:25], v235 offset:16896\n" \
"ds_read_b64_tr_b16 a[26:27], v235 offset:18944\n" \
"v_mfma_f32_16x16x32_bf16 v[64:67], a[4:7], a[216:219], v[64:67]\n" \
"v_mov_b32 v212, 0\n" \
"v_mov_b32 v213, 0\n" \
"v_mfma_f32_16x16x32_bf16 v[72:75], a[4:7], a[220:223], v[72:75]\n" \
"ds_read_b64_tr_b16 a[28:29], v235 offset:25088\n" \
"ds_read_b64_tr_b16 a[30:31], v235 offset:27136\n" \
"v_mfma_f32_16x16x32_bf16 v[72:75], a[0:3], a[212:215], v[72:75]\n" \
"s_waitcnt lgkmcnt(8)\n" \
"buffer_load_dwordx4 v[176:179], v248, s[32:35], 0 offen offset:1024 sc1\n" \
"v_bfe_u32 v210, v251, 2, 2\n" \
"v_mfma_f32_16x16x32_bf16 v[68:71], a[8:11], a[208:211], v[68:71]\n" \
"ds_read_b64_tr_b16 a[0:1], v244 offset:0\n" \
"ds_read_b64_tr_b16 a[2:3], v244 offset:2048\n" \
"v_mfma_f32_16x16x32_bf16 v[68:71], a[12:15], a[216:219], v[68:71]\n" \
"v_lshrrev_b32 v211, 4, v251\n" \
"v_cmp_eq_u32 s[24:25], v210, v211\n" \
"v_mfma_f32_16x16x32_bf16 v[76:79], a[12:15], a[220:223], v[76:79]\n" \
"ds_read_b64_tr_b16 a[4:5], v244 offset:8192\n" \
"ds_read_b64_tr_b16 a[6:7], v244 offset:10240\n" \
"v_mfma_f32_16x16x32_bf16 v[76:79], a[8:11], a[212:215], v[76:79]\n" \
"s_waitcnt lgkmcnt(8)\n" \
"buffer_load_dwordx4 v[180:183], v248, s[32:35], 0 offen offset:2048 sc1\n" \
"v_and_b32 v210, 1, v251\n" \
"v_mfma_f32_16x16x32_bf16 v[80:83], a[16:19], a[208:211], v[80:83]\n" \
"ds_read_b64_tr_b16 a[8:9], v234 offset:0\n" \
"ds_read_b64_tr_b16 a[10:11], v234 offset:2048\n" \
"v_mfma_f32_16x16x32_bf16 v[80:83], a[20:23], a[216:219], v[80:83]\n" \
"v_lshlrev_b32 v210, 4, v210\n" \
"v_mov_b32 v211, 0x3f80\n" \
"v_mfma_f32_16x16x32_bf16 v[88:91], a[20:23], a[220:223], v[88:91]\n" \
"ds_read_b64_tr_b16 a[12:13], v234 offset:8192\n" \
"ds_read_b64_tr_b16 a[14:15], v234 offset:10240\n" \
"v_mfma_f32_16x16x32_bf16 v[88:91], a[16:19], a[212:215], v[88:91]\n" \
"s_waitcnt lgkmcnt(8)\n" \
"buffer_load_dwordx4 v[184:187], v248, s[32:35], 0 offen offset:3072 sc1\n" \
"v_lshlrev_b32 v210, v210, v211\n" \
"v_mfma_f32_16x16x32_bf16 v[84:87], a[24:27], a[208:211], v[84:87]\n" \
"ds_read_b64_tr_b16 a[16:17], v244 offset:512\n" \
"ds_read_b64_tr_b16 a[18:19], v244 offset:2560\n" \
"v_mfma_f32_16x16x32_bf16 v[84:87], a[28:31], a[216:219], v[84:87]\n" \
"v_cndmask_b32 v210, 0, v210, s[24:25]\n" \
"v_bfe_i32 v215, v251, 1, 1\n" \
"v_mfma_f32_16x16x32_bf16 v[92:95], a[28:31], a[220:223], v[92:95]\n" \
"ds_read_b64_tr_b16 a[20:21], v244 offset:8704\n" \
"ds_read_b64_tr_b16 a[22:23], v244 offset:10752\n" \
"v_mfma_f32_16x16x32_bf16 v[92:95], a[24:27], a[212:215], v[92:95]\n" \
"s_waitcnt lgkmcnt(8)\n" \
"global_load_dwordx2 v[168:169], v188, s[96:97] offset:0\n" \
"v_and_b32 v211, v215, v210\n" \
"v_mfma_f32_16x16x32_bf16 v[64:67], a[0:3], a[224:227], v[64:67]\n" \
"ds_read_b64_tr_b16 a[24:25], v234 offset:512\n" \
"ds_read_b64_tr_b16 a[26:27], v234 offset:2560\n" \
"v_mfma_f32_16x16x32_bf16 v[64:67], a[4:7], a[232:235], v[64:67]\n" \
"v_xor_b32 v210, v210, v211\n" \
"v_mfma_f32_16x16x32_bf16 v[72:75], a[4:7], a[236:239], v[72:75]\n" \
"ds_read_b64_tr_b16 a[28:29], v234 offset:8704\n" \
"ds_read_b64_tr_b16 a[30:31], v234 offset:10752\n" \
"v_mfma_f32_16x16x32_bf16 v[72:75], a[0:3], a[228:231], v[72:75]\n" \
"s_waitcnt lgkmcnt(8)\n" \
"global_load_dwordx2 v[170:171], v188, s[96:97] offset:32\n" \
"v_mfma_f32_16x16x32_bf16 v[68:71], a[8:11], a[224:227], v[68:71]\n" \
"ds_read_b64_tr_b16 a[0:1], v249 offset:0\n" \
"ds_read_b64_tr_b16 a[2:3], v249 offset:2048\n" \
"v_mfma_f32_16x16x32_bf16 v[68:71], a[12:15], a[232:235], v[68:71]\n" \
"v_mfma_f32_16x16x32_bf16 v[76:79], a[12:15], a[236:239], v[76:79]\n" \
"ds_read_b64_tr_b16 a[4:5], v249 offset:8192\n" \
"ds_read_b64_tr_b16 a[6:7], v249 offset:10240\n" \
"v_mfma_f32_16x16x32_bf16 v[76:79], a[8:11], a[228:231], v[76:79]\n" \
"s_waitcnt lgkmcnt(8)\n" \
"v_mfma_f32_16x16x32_bf16 v[80:83], a[16:19], a[224:227], v[80:83]\n" \
"ds_read_b64_tr_b16 a[8:9], v233 offset:0\n" \
"ds_read_b64_tr_b16 a[10:11], v233 offset:2048\n" \
"v_mfma_f32_16x16x32_bf16 v[80:83], a[20:23], a[232:235], v[80:83]\n" \
"v_mfma_f32_16x16x32_bf16 v[88:91], a[20:23], a[236:239], v[88:91]\n" \
"ds_read_b64_tr_b16 a[12:13], v233 offset:8192\n" \
"ds_read_b64_tr_b16 a[14:15], v233 offset:10240\n" \
"v_mfma_f32_16x16x32_bf16 v[88:91], a[16:19], a[228:231], v[88:91]\n" \
"s_waitcnt lgkmcnt(8)\n" \
"v_mfma_f32_16x16x32_bf16 v[84:87], a[24:27], a[224:227], v[84:87]\n" \
"ds_read_b64_tr_b16 a[16:17], v249 offset:512\n" \
"ds_read_b64_tr_b16 a[18:19], v249 offset:2560\n" \
"v_mfma_f32_16x16x32_bf16 v[84:87], a[28:31], a[232:235], v[84:87]\n" \
"v_mfma_f32_16x16x32_bf16 v[92:95], a[28:31], a[236:239], v[92:95]\n" \
"s_waitcnt vmcnt(15)\n" \
"ds_read_b64_tr_b16 a[20:21], v249 offset:8704\n" \
"v_mfma_f32_16x16x32_bf16 v[92:95], a[24:27], a[228:231], v[92:95]\n" \
"s_waitcnt lgkmcnt(7)\n" \
"ds_read_b64_tr_b16 a[22:23], v249 offset:10752\n" \
"v_mfma_f32_16x16x32_bf16 v[64:67], a[0:3], a[240:243], v[64:67]\n" \
"ds_read_b64_tr_b16 a[24:25], v233 offset:512\n" \
"ds_read_b64_tr_b16 a[26:27], v233 offset:2560\n" \
"v_mfma_f32_16x16x32_bf16 v[64:67], a[4:7], a[248:251], v[64:67]\n" \
"v_mfma_f32_16x16x32_bf16 v[72:75], a[4:7], a[252:255], v[72:75]\n" \
"ds_read_b64_tr_b16 a[28:29], v233 offset:8704\n" \
"ds_read_b64_tr_b16 a[30:31], v233 offset:10752\n" \
"v_mfma_f32_16x16x32_bf16 v[72:75], a[0:3], a[244:247], v[72:75]\n" \
"s_waitcnt lgkmcnt(8)\n" \
"v_mfma_f32_16x16x32_bf16 v[68:71], a[8:11], a[240:243], v[68:71]\n" \
"ds_read_b64_tr_b16 a[0:1], v239 offset:0\n" \
"ds_read_b64_tr_b16 a[2:3], v239 offset:2048\n" \
"v_mfma_f32_16x16x32_bf16 v[68:71], a[12:15], a[248:251], v[68:71]\n" \
"v_mfma_f32_16x16x32_bf16 v[76:79], a[12:15], a[252:255], v[76:79]\n" \
"ds_read_b64_tr_b16 a[4:5], v239 offset:8192\n" \
"ds_read_b64_tr_b16 a[6:7], v239 offset:10240\n" \
"v_mfma_f32_16x16x32_bf16 v[76:79], a[8:11], a[244:247], v[76:79]\n" \
"s_waitcnt lgkmcnt(8)\n" \
"v_mfma_f32_16x16x32_bf16 v[80:83], a[16:19], a[240:243], v[80:83]\n" \
"ds_read_b64_tr_b16 a[8:9], v232 offset:0\n" \
"ds_read_b64_tr_b16 a[10:11], v232 offset:2048\n" \
"v_mfma_f32_16x16x32_bf16 v[80:83], a[20:23], a[248:251], v[80:83]\n" \
"v_mfma_f32_16x16x32_bf16 v[88:91], a[20:23], a[252:255], v[88:91]\n" \
"ds_read_b64_tr_b16 a[12:13], v232 offset:8192\n" \
"ds_read_b64_tr_b16 a[14:15], v232 offset:10240\n" \
"v_mfma_f32_16x16x32_bf16 v[88:91], a[16:19], a[244:247], v[88:91]\n" \
"s_waitcnt lgkmcnt(8)\n" \
"v_mfma_f32_16x16x32_bf16 v[84:87], a[24:27], a[240:243], v[84:87]\n" \
"ds_read_b64_tr_b16 a[16:17], v239 offset:512\n" \
"ds_read_b64_tr_b16 a[18:19], v239 offset:2560\n" \
"v_mfma_f32_16x16x32_bf16 v[84:87], a[28:31], a[248:251], v[84:87]\n" \
"v_mfma_f32_16x16x32_bf16 v[92:95], a[28:31], a[252:255], v[92:95]\n" \
"s_waitcnt vmcnt(11)\n" \
"ds_read_b64_tr_b16 a[20:21], v239 offset:8704\n" \
"v_mfma_f32_16x16x32_bf16 v[92:95], a[24:27], a[244:247], v[92:95]\n" \
"s_waitcnt lgkmcnt(7)\n" \
"ds_read_b64_tr_b16 a[22:23], v239 offset:10752\n" \
"v_mfma_f32_16x16x32_bf16 v[64:67], a[0:3], a[64:67], v[64:67]\n" \
"ds_read_b64_tr_b16 a[24:25], v232 offset:512\n" \
"ds_read_b64_tr_b16 a[26:27], v232 offset:2560\n" \
"v_mfma_f32_16x16x32_bf16 v[64:67], a[4:7], a[80:83], v[64:67]\n" \
"v_mfma_f32_16x16x32_bf16 v[72:75], a[4:7], a[88:91], v[72:75]\n" \
"ds_read_b64_tr_b16 a[28:29], v232 offset:8704\n" \
"ds_read_b64_tr_b16 a[30:31], v232 offset:10752\n" \
"v_mfma_f32_16x16x32_bf16 v[72:75], a[0:3], a[72:75], v[72:75]\n" \
"s_waitcnt lgkmcnt(8)\n" \
"v_mfma_f32_16x16x32_bf16 v[68:71], a[8:11], a[64:67], v[68:71]\n" \
"ds_read_b64_tr_b16 a[0:1], v239 offset:16384\n" \
"ds_read_b64_tr_b16 a[2:3], v239 offset:18432\n" \
"v_mfma_f32_16x16x32_bf16 v[68:71], a[12:15], a[80:83], v[68:71]\n" \
"v_mfma_f32_16x16x32_bf16 v[76:79], a[12:15], a[88:91], v[76:79]\n" \
"ds_read_b64_tr_b16 a[4:5], v239 offset:24576\n" \
"ds_read_b64_tr_b16 a[6:7], v239 offset:26624\n" \
"v_mfma_f32_16x16x32_bf16 v[76:79], a[8:11], a[72:75], v[76:79]\n" \
"s_waitcnt lgkmcnt(8)\n" \
"v_mfma_f32_16x16x32_bf16 v[80:83], a[16:19], a[64:67], v[80:83]\n" \
"ds_read_b64_tr_b16 a[8:9], v232 offset:16384\n" \
"ds_read_b64_tr_b16 a[10:11], v232 offset:18432\n" \
"v_mfma_f32_16x16x32_bf16 v[80:83], a[20:23], a[80:83], v[80:83]\n" \
"v_mfma_f32_16x16x32_bf16 v[88:91], a[20:23], a[88:91], v[88:91]\n" \
"ds_read_b64_tr_b16 a[12:13], v232 offset:24576\n" \
"ds_read_b64_tr_b16 a[14:15], v232 offset:26624\n" \
"v_mfma_f32_16x16x32_bf16 v[88:91], a[16:19], a[72:75], v[88:91]\n" \
"s_waitcnt lgkmcnt(8)\n" \
"s_lshl2_add_u32 s74, s67, %[wid]\n" \
"s_lshl_b32 s74, s74, 7\n" \
"v_mfma_f32_16x16x32_bf16 v[84:87], a[24:27], a[64:67], v[84:87]\n" \
"s_add_u32 s74, s74, 94208\n" \
"ds_read_b64_tr_b16 a[16:17], v239 offset:16896\n" \
"ds_read_b64_tr_b16 a[18:19], v239 offset:18944\n" \
"v_mfma_f32_16x16x32_bf16 v[84:87], a[28:31], a[80:83], v[84:87]\n" \
"v_mov_b32 v238, 1\n" \
"v_mfma_f32_16x16x32_bf16 v[92:95], a[28:31], a[88:91], v[92:95]\n" \
"s_waitcnt vmcnt(6)\n" \
"ds_read_b64_tr_b16 a[20:21], v239 offset:25088\n" \
"v_mfma_f32_16x16x32_bf16 v[92:95], a[24:27], a[72:75], v[92:95]\n" \
"s_waitcnt lgkmcnt(7)\n" \
"ds_read_b64_tr_b16 a[22:23], v239 offset:27136\n" \
"v_mfma_f32_16x16x32_bf16 v[64:67], a[0:3], a[96:99], v[64:67]\n" \
"ds_read_b64_tr_b16 a[24:25], v232 offset:16896\n" \
"ds_read_b64_tr_b16 a[26:27], v232 offset:18944\n" \
"v_mfma_f32_16x16x32_bf16 v[64:67], a[4:7], a[112:115], v[64:67]\n" \
"v_mfma_f32_16x16x32_bf16 v[72:75], a[4:7], a[116:119], v[72:75]\n" \
"ds_read_b64_tr_b16 a[28:29], v232 offset:25088\n" \
"ds_read_b64_tr_b16 a[30:31], v232 offset:27136\n" \
"v_mfma_f32_16x16x32_bf16 v[72:75], a[0:3], a[104:107], v[72:75]\n" \
"s_waitcnt lgkmcnt(8)\n" \
"v_mfma_f32_16x16x32_bf16 v[68:71], a[8:11], a[96:99], v[68:71]\n" \
"ds_read_b64_tr_b16 a[0:1], v241 offset:4096\n" \
"ds_read_b64_tr_b16 a[2:3], v241 offset:6144\n" \
"v_mfma_f32_16x16x32_bf16 v[68:71], a[12:15], a[112:115], v[68:71]\n" \
"v_mfma_f32_16x16x32_bf16 v[76:79], a[12:15], a[116:119], v[76:79]\n" \
"ds_read_b64_tr_b16 a[4:5], v241 offset:12288\n" \
"ds_read_b64_tr_b16 a[6:7], v241 offset:14336\n" \
"v_mfma_f32_16x16x32_bf16 v[76:79], a[8:11], a[104:107], v[76:79]\n" \
"s_waitcnt lgkmcnt(8)\n" \
"v_mfma_f32_16x16x32_bf16 v[80:83], a[16:19], a[96:99], v[80:83]\n" \
"ds_read_b64_tr_b16 a[8:9], v235 offset:4096\n" \
"ds_read_b64_tr_b16 a[10:11], v235 offset:6144\n" \
"v_mfma_f32_16x16x32_bf16 v[80:83], a[20:23], a[112:115], v[80:83]\n" \
"v_mfma_f32_16x16x32_bf16 v[88:91], a[20:23], a[116:119], v[88:91]\n" \
"ds_read_b64_tr_b16 a[12:13], v235 offset:12288\n" \
"ds_read_b64_tr_b16 a[14:15], v235 offset:14336\n" \
"v_mfma_f32_16x16x32_bf16 v[88:91], a[16:19], a[104:107], v[88:91]\n" \
"s_waitcnt lgkmcnt(8)\n" \
"v_mfma_f32_16x16x32_bf16 v[84:87], a[24:27], a[96:99], v[84:87]\n" \
"ds_read_b64_tr_b16 a[16:17], v241 offset:20480\n" \
"ds_read_b64_tr_b16 a[18:19], v241 offset:22528\n" \
"v_mfma_f32_16x16x32_bf16 v[84:87], a[28:31], a[112:115], v[84:87]\n" \
"v_cvt_pk_bf16_f32 v64, v64, v65\n" \
"v_cvt_pk_bf16_f32 v65, v66, v67\n" \
"v_mfma_f32_16x16x32_bf16 v[92:95], a[28:31], a[116:119], v[92:95]\n" \
"ds_read_b64_tr_b16 a[20:21], v241 offset:28672\n" \
"ds_read_b64_tr_b16 a[22:23], v241 offset:30720\n" \
"v_mfma_f32_16x16x32_bf16 v[92:95], a[24:27], a[104:107], v[92:95]\n" \
"s_waitcnt lgkmcnt(8)\n" \
"v_cvt_pk_bf16_f32 v66, v68, v69\n" \
"v_cvt_pk_bf16_f32 v67, v70, v71\n" \
"v_mfma_f32_16x16x32_bf16 v[96:99], a[0:3], a[192:195], v[96:99]\n" \
"ds_read_b64_tr_b16 a[24:25], v235 offset:20480\n" \
"ds_read_b64_tr_b16 a[26:27], v235 offset:22528\n" \
"v_mfma_f32_16x16x32_bf16 v[96:99], a[4:7], a[200:203], v[96:99]\n" \
"s_mov_b64 exec, 1\n" \
"buffer_atomic_add v238, off, s[32:35], s74 sc0 sc1\n" \
"s_mov_b64 exec, -1\n" \
"v_mfma_f32_16x16x32_bf16 v[104:107], a[4:7], a[204:207], v[104:107]\n" \
"ds_read_b64_tr_b16 a[28:29], v235 offset:28672\n" \
"ds_read_b64_tr_b16 a[30:31], v235 offset:30720\n" \
"v_mfma_f32_16x16x32_bf16 v[104:107], a[0:3], a[196:199], v[104:107]\n" \
"s_waitcnt lgkmcnt(8)\n" \
"buffer_load_dword v245, v255, s[32:35], s18 offen sc1\n" \
"v_mfma_f32_16x16x32_bf16 v[100:103], a[8:11], a[192:195], v[100:103]\n" \
"ds_read_b64_tr_b16 a[0:1], v244 offset:4096\n" \
"ds_read_b64_tr_b16 a[2:3], v244 offset:6144\n" \
"v_mfma_f32_16x16x32_bf16 v[100:103], a[12:15], a[200:203], v[100:103]\n" \
"buffer_store_dwordx4 v[64:67], v250, s[32:35], s36 offen offset:0 sc1\n" \
"v_mfma_f32_16x16x32_bf16 v[108:111], a[12:15], a[204:207], v[108:111]\n" \
"ds_read_b64_tr_b16 a[4:5], v244 offset:12288\n" \
"ds_read_b64_tr_b16 a[6:7], v244 offset:14336\n" \
"v_mfma_f32_16x16x32_bf16 v[108:111], a[8:11], a[196:199], v[108:111]\n" \
"s_waitcnt lgkmcnt(8)\n" \
"v_cvt_pk_bf16_f32 v72, v72, v73\n" \
"v_cvt_pk_bf16_f32 v73, v74, v75\n" \
"v_mfma_f32_16x16x32_bf16 v[96:99], a[16:19], a[208:211], v[96:99]\n" \
"ds_read_b64_tr_b16 a[8:9], v234 offset:4096\n" \
"ds_read_b64_tr_b16 a[10:11], v234 offset:6144\n" \
"v_mfma_f32_16x16x32_bf16 v[96:99], a[20:23], a[216:219], v[96:99]\n" \
"v_cvt_pk_bf16_f32 v74, v76, v77\n" \
"v_cvt_pk_bf16_f32 v75, v78, v79\n" \
"v_mfma_f32_16x16x32_bf16 v[104:107], a[20:23], a[220:223], v[104:107]\n" \
"ds_read_b64_tr_b16 a[12:13], v234 offset:12288\n" \
"ds_read_b64_tr_b16 a[14:15], v234 offset:14336\n" \
"v_mfma_f32_16x16x32_bf16 v[104:107], a[16:19], a[212:215], v[104:107]\n" \
"s_waitcnt lgkmcnt(8)\n" \
"v_mfma_f32_16x16x32_bf16 v[100:103], a[24:27], a[208:211], v[100:103]\n" \
"ds_read_b64_tr_b16 a[16:17], v249 offset:4096\n" \
"ds_read_b64_tr_b16 a[18:19], v249 offset:6144\n" \
"v_mfma_f32_16x16x32_bf16 v[100:103], a[28:31], a[216:219], v[100:103]\n" \
"buffer_store_dwordx4 v[72:75], v250, s[32:35], s21 offen offset:0 sc1\n" \
"v_mfma_f32_16x16x32_bf16 v[108:111], a[28:31], a[220:223], v[108:111]\n" \
"ds_read_b64_tr_b16 a[20:21], v249 offset:12288\n" \
"ds_read_b64_tr_b16 a[22:23], v249 offset:14336\n" \
"v_mfma_f32_16x16x32_bf16 v[108:111], a[24:27], a[212:215], v[108:111]\n" \
"s_waitcnt lgkmcnt(8)\n" \
"v_cvt_pk_bf16_f32 v80, v80, v81\n" \
"v_cvt_pk_bf16_f32 v81, v82, v83\n" \
"v_mfma_f32_16x16x32_bf16 v[96:99], a[0:3], a[224:227], v[96:99]\n" \
"ds_read_b64_tr_b16 a[24:25], v233 offset:4096\n" \
"ds_read_b64_tr_b16 a[26:27], v233 offset:6144\n" \
"v_mfma_f32_16x16x32_bf16 v[96:99], a[4:7], a[232:235], v[96:99]\n" \
"v_cvt_pk_bf16_f32 v82, v84, v85\n" \
"v_cvt_pk_bf16_f32 v83, v86, v87\n" \
"v_mfma_f32_16x16x32_bf16 v[104:107], a[4:7], a[236:239], v[104:107]\n" \
"ds_read_b64_tr_b16 a[28:29], v233 offset:12288\n" \
"ds_read_b64_tr_b16 a[30:31], v233 offset:14336\n" \
"v_mfma_f32_16x16x32_bf16 v[104:107], a[0:3], a[228:231], v[104:107]\n" \
"s_waitcnt lgkmcnt(8)\n" \
"v_mfma_f32_16x16x32_bf16 v[100:103], a[8:11], a[224:227], v[100:103]\n" \
"ds_read_b64_tr_b16 a[0:1], v239 offset:4096\n" \
"ds_read_b64_tr_b16 a[2:3], v239 offset:6144\n" \
"v_mfma_f32_16x16x32_bf16 v[100:103], a[12:15], a[232:235], v[100:103]\n" \
"v_mfma_f32_16x16x32_bf16 v[108:111], a[12:15], a[236:239], v[108:111]\n" \
"ds_read_b64_tr_b16 a[4:5], v239 offset:12288\n" \
"ds_read_b64_tr_b16 a[6:7], v239 offset:14336\n" \
"v_mfma_f32_16x16x32_bf16 v[108:111], a[8:11], a[228:231], v[108:111]\n" \
"s_waitcnt lgkmcnt(8)\n" \
"buffer_store_dwordx4 v[80:83], v250, s[32:35], s36 offen offset:1024 sc1\n" \
"v_mfma_f32_16x16x32_bf16 v[96:99], a[16:19], a[240:243], v[96:99]\n" \
"ds_read_b64_tr_b16 a[8:9], v232 offset:4096\n" \
"ds_read_b64_tr_b16 a[10:11], v232 offset:6144\n" \
"v_mfma_f32_16x16x32_bf16 v[96:99], a[20:23], a[248:251], v[96:99]\n" \
"v_cvt_pk_bf16_f32 v88, v88, v89\n" \
"v_cvt_pk_bf16_f32 v89, v90, v91\n" \
"v_mfma_f32_16x16x32_bf16 v[104:107], a[20:23], a[252:255], v[104:107]\n" \
"ds_read_b64_tr_b16 a[12:13], v232 offset:12288\n" \
"ds_read_b64_tr_b16 a[14:15], v232 offset:14336\n" \
"v_mfma_f32_16x16x32_bf16 v[104:107], a[16:19], a[244:247], v[104:107]\n" \
"s_waitcnt lgkmcnt(8)\n" \
"v_cvt_pk_bf16_f32 v90, v92, v93\n" \
"v_cvt_pk_bf16_f32 v91, v94, v95\n" \
"v_mfma_f32_16x16x32_bf16 v[100:103], a[24:27], a[240:243], v[100:103]\n" \
"ds_read_b64_tr_b16 a[16:17], v239 offset:20480\n" \
"ds_read_b64_tr_b16 a[18:19], v239 offset:22528\n" \
"v_mfma_f32_16x16x32_bf16 v[100:103], a[28:31], a[248:251], v[100:103]\n" \
"v_mfma_f32_16x16x32_bf16 v[108:111], a[28:31], a[252:255], v[108:111]\n" \
"ds_read_b64_tr_b16 a[20:21], v239 offset:28672\n" \
"ds_read_b64_tr_b16 a[22:23], v239 offset:30720\n" \
"v_mfma_f32_16x16x32_bf16 v[108:111], a[24:27], a[244:247], v[108:111]\n" \
"s_waitcnt lgkmcnt(8)\n" \
"v_mfma_f32_16x16x32_bf16 v[96:99], a[0:3], a[64:67], v[96:99]\n" \
"ds_read_b64_tr_b16 a[24:25], v232 offset:20480\n" \
"ds_read_b64_tr_b16 a[26:27], v232 offset:22528\n" \
"v_mfma_f32_16x16x32_bf16 v[96:99], a[4:7], a[80:83], v[96:99]\n" \
"buffer_store_dwordx4 v[88:91], v250, s[32:35], s21 offen offset:1024 sc1\n" \
"v_mfma_f32_16x16x32_bf16 v[104:107], a[4:7], a[88:91], v[104:107]\n" \
"ds_read_b64_tr_b16 a[28:29], v232 offset:28672\n" \
"ds_read_b64_tr_b16 a[30:31], v232 offset:30720\n" \
"v_mfma_f32_16x16x32_bf16 v[104:107], a[0:3], a[72:75], v[104:107]\n" \
"s_waitcnt lgkmcnt(8)\n" \
"v_mfma_f32_16x16x32_bf16 v[100:103], a[8:11], a[64:67], v[100:103]\n" \
"ds_read_b64_tr_b16 a[0:1], v241 offset:4608\n" \
"ds_read_b64_tr_b16 a[2:3], v241 offset:6656\n" \
"v_mfma_f32_16x16x32_bf16 v[100:103], a[12:15], a[80:83], v[100:103]\n" \
"v_mfma_f32_16x16x32_bf16 v[108:111], a[12:15], a[88:91], v[108:111]\n" \
"ds_read_b64_tr_b16 a[4:5], v241 offset:12800\n" \
"ds_read_b64_tr_b16 a[6:7], v241 offset:14848\n" \
"v_mfma_f32_16x16x32_bf16 v[108:111], a[8:11], a[72:75], v[108:111]\n" \
"s_waitcnt lgkmcnt(8)\n" \
"v_mfma_f32_16x16x32_bf16 v[96:99], a[16:19], a[96:99], v[96:99]\n" \
"ds_read_b64_tr_b16 a[8:9], v235 offset:4608\n" \
"ds_read_b64_tr_b16 a[10:11], v235 offset:6656\n" \
"v_mfma_f32_16x16x32_bf16 v[96:99], a[20:23], a[112:115], v[96:99]\n" \
"v_mfma_f32_16x16x32_bf16 v[104:107], a[20:23], a[116:119], v[104:107]\n" \
"ds_read_b64_tr_b16 a[12:13], v235 offset:12800\n" \
"ds_read_b64_tr_b16 a[14:15], v235 offset:14848\n" \
"v_mfma_f32_16x16x32_bf16 v[104:107], a[16:19], a[104:107], v[104:107]\n" \
"s_waitcnt lgkmcnt(8)\n" \
"v_mfma_f32_16x16x32_bf16 v[100:103], a[24:27], a[96:99], v[100:103]\n" \
"ds_read_b64_tr_b16 a[16:17], v241 offset:20992\n" \
"ds_read_b64_tr_b16 a[18:19], v241 offset:23040\n" \
"v_mfma_f32_16x16x32_bf16 v[100:103], a[28:31], a[112:115], v[100:103]\n" \
"v_mfma_f32_16x16x32_bf16 v[108:111], a[28:31], a[116:119], v[108:111]\n" \
"ds_read_b64_tr_b16 a[20:21], v241 offset:29184\n" \
"ds_read_b64_tr_b16 a[22:23], v241 offset:31232\n" \
"v_mfma_f32_16x16x32_bf16 v[108:111], a[24:27], a[104:107], v[108:111]\n" \
"s_waitcnt vmcnt(6) lgkmcnt(8)\n" \
"v_lshlrev_b32 v136, 16, v172\n" \
"v_mfma_f32_16x16x32_bf16 v[112:115], a[0:3], a[192:195], v[112:115]\n" \
"ds_read_b64_tr_b16 a[24:25], v235 offset:20992\n" \
"ds_read_b64_tr_b16 a[26:27], v235 offset:23040\n" \
"v_mfma_f32_16x16x32_bf16 v[112:115], a[4:7], a[200:203], v[112:115]\n" \
"v_and_b32 v137, 0xffff0000, v172\n" \
"v_lshlrev_b32 v138, 16, v173\n" \
"v_mfma_f32_16x16x32_bf16 v[120:123], a[4:7], a[204:207], v[120:123]\n" \
"ds_read_b64_tr_b16 a[28:29], v235 offset:29184\n" \
"ds_read_b64_tr_b16 a[30:31], v235 offset:31232\n" \
"v_mfma_f32_16x16x32_bf16 v[120:123], a[0:3], a[196:199], v[120:123]\n" \
"s_waitcnt lgkmcnt(8)\n" \
"v_and_b32 v139, 0xffff0000, v173\n" \
"v_lshlrev_b32 v140, 16, v174\n" \
"v_mfma_f32_16x16x32_bf16 v[116:119], a[8:11], a[192:195], v[116:119]\n" \
"ds_read_b64_tr_b16 a[0:1], v244 offset:4608\n" \
"ds_read_b64_tr_b16 a[2:3], v244 offset:6656\n" \
"v_mfma_f32_16x16x32_bf16 v[116:119], a[12:15], a[200:203], v[116:119]\n" \
"v_cvt_pk_bf16_f32 v96, v96, v97\n" \
"v_cvt_pk_bf16_f32 v97, v98, v99\n" \
"v_mfma_f32_16x16x32_bf16 v[124:127], a[12:15], a[204:207], v[124:127]\n" \
"ds_read_b64_tr_b16 a[4:5], v244 offset:12800\n" \
"ds_read_b64_tr_b16 a[6:7], v244 offset:14848\n" \
"v_mfma_f32_16x16x32_bf16 v[124:127], a[8:11], a[196:199], v[124:127]\n" \
"s_waitcnt lgkmcnt(8)\n" \
"v_cvt_pk_bf16_f32 v98, v100, v101\n" \
"v_cvt_pk_bf16_f32 v99, v102, v103\n" \
"v_mfma_f32_16x16x32_bf16 v[112:115], a[16:19], a[208:211], v[112:115]\n" \
"ds_read_b64_tr_b16 a[8:9], v234 offset:4608\n" \
"ds_read_b64_tr_b16 a[10:11], v234 offset:6656\n" \
"v_mfma_f32_16x16x32_bf16 v[112:115], a[20:23], a[216:219], v[112:115]\n" \
"v_and_b32 v141, 0xffff0000, v174\n" \
"v_lshlrev_b32 v142, 16, v175\n" \
"v_mfma_f32_16x16x32_bf16 v[120:123], a[20:23], a[220:223], v[120:123]\n" \
"ds_read_b64_tr_b16 a[12:13], v234 offset:12800\n" \
"ds_read_b64_tr_b16 a[14:15], v234 offset:14848\n" \
"v_mfma_f32_16x16x32_bf16 v[120:123], a[16:19], a[212:215], v[120:123]\n" \
"s_waitcnt lgkmcnt(8)\n" \
"buffer_store_dwordx4 v[96:99], v250, s[32:35], s36 offen offset:2048 sc1\n" \
"v_mfma_f32_16x16x32_bf16 v[116:119], a[24:27], a[208:211], v[116:119]\n" \
"ds_read_b64_tr_b16 a[16:17], v249 offset:4608\n" \
"ds_read_b64_tr_b16 a[18:19], v249 offset:6656\n" \
"v_mfma_f32_16x16x32_bf16 v[116:119], a[28:31], a[216:219], v[116:119]\n" \
"v_cvt_pk_bf16_f32 v104, v104, v105\n" \
"v_cvt_pk_bf16_f32 v105, v106, v107\n" \
"v_mfma_f32_16x16x32_bf16 v[124:127], a[28:31], a[220:223], v[124:127]\n" \
"ds_read_b64_tr_b16 a[20:21], v249 offset:12800\n" \
"ds_read_b64_tr_b16 a[22:23], v249 offset:14848\n" \
"v_mfma_f32_16x16x32_bf16 v[124:127], a[24:27], a[212:215], v[124:127]\n" \
"s_waitcnt lgkmcnt(8)\n" \
"v_cvt_pk_bf16_f32 v106, v108, v109\n" \
"v_cvt_pk_bf16_f32 v107, v110, v111\n" \
"v_mfma_f32_16x16x32_bf16 v[112:115], a[0:3], a[224:227], v[112:115]\n" \
"ds_read_b64_tr_b16 a[24:25], v233 offset:4608\n" \
"ds_read_b64_tr_b16 a[26:27], v233 offset:6656\n" \
"v_mfma_f32_16x16x32_bf16 v[112:115], a[4:7], a[232:235], v[112:115]\n" \
"v_and_b32 v143, 0xffff0000, v175\n" \
"v_lshlrev_b32 v144, 16, v176\n" \
"v_mfma_f32_16x16x32_bf16 v[120:123], a[4:7], a[236:239], v[120:123]\n" \
".Lst1_8_%=:\n" \
"s_cbranch_execz .Lst1_12_%=\n" \
"ds_read_b64_tr_b16 a[28:29], v233 offset:12800\n" \
"ds_read_b64_tr_b16 a[30:31], v233 offset:14848\n" \
"v_mfma_f32_16x16x32_bf16 v[120:123], a[0:3], a[228:231], v[120:123]\n" \
"s_waitcnt lgkmcnt(8)\n" \
"v_and_b32 v145, 0xffff0000, v176\n" \
"v_lshlrev_b32 v146, 16, v177\n" \
"v_mfma_f32_16x16x32_bf16 v[116:119], a[8:11], a[224:227], v[116:119]\n" \
"ds_read_b64_tr_b16 a[0:1], v239 offset:4608\n" \
"ds_read_b64_tr_b16 a[2:3], v239 offset:6656\n" \
"v_mfma_f32_16x16x32_bf16 v[116:119], a[12:15], a[232:235], v[116:119]\n" \
"buffer_store_dwordx4 v[104:107], v250, s[32:35], s21 offen offset:2048 sc1\n" \
"v_mfma_f32_16x16x32_bf16 v[124:127], a[12:15], a[236:239], v[124:127]\n" \
"ds_read_b64_tr_b16 a[4:5], v239 offset:12800\n" \
"ds_read_b64_tr_b16 a[6:7], v239 offset:14848\n" \
"v_mfma_f32_16x16x32_bf16 v[124:127], a[8:11], a[228:231], v[124:127]\n" \
"s_waitcnt lgkmcnt(8)\n" \
"v_and_b32 v147, 0xffff0000, v177\n" \
"v_lshlrev_b32 v148, 16, v178\n" \
"v_mfma_f32_16x16x32_bf16 v[112:115], a[16:19], a[240:243], v[112:115]\n" \
"ds_read_b64_tr_b16 a[8:9], v232 offset:4608\n" \
"ds_read_b64_tr_b16 a[10:11], v232 offset:6656\n" \
"v_mfma_f32_16x16x32_bf16 v[112:115], a[20:23], a[248:251], v[112:115]\n" \
"v_and_b32 v149, 0xffff0000, v178\n" \
"v_lshlrev_b32 v150, 16, v179\n" \
"v_mfma_f32_16x16x32_bf16 v[120:123], a[20:23], a[252:255], v[120:123]\n" \
"ds_read_b64_tr_b16 a[12:13], v232 offset:12800\n" \
"ds_read_b64_tr_b16 a[14:15], v232 offset:14848\n" \
"v_mfma_f32_16x16x32_bf16 v[120:123], a[16:19], a[244:247], v[120:123]\n" \
"s_waitcnt lgkmcnt(8)\n" \
"v_and_b32 v151, 0xffff0000, v179\n" \
"v_lshlrev_b32 v152, 16, v180\n" \
"v_mfma_f32_16x16x32_bf16 v[116:119], a[24:27], a[240:243], v[116:119]\n" \
"ds_read_b64_tr_b16 a[16:17], v239 offset:20992\n" \
"ds_read_b64_tr_b16 a[18:19], v239 offset:23040\n" \
"v_mfma_f32_16x16x32_bf16 v[116:119], a[28:31], a[248:251], v[116:119]\n" \
"v_and_b32 v153, 0xffff0000, v180\n" \
"v_lshlrev_b32 v154, 16, v181\n" \
"v_mfma_f32_16x16x32_bf16 v[124:127], a[28:31], a[252:255], v[124:127]\n" \
"ds_read_b64_tr_b16 a[20:21], v239 offset:29184\n" \
"ds_read_b64_tr_b16 a[22:23], v239 offset:31232\n" \
"v_mfma_f32_16x16x32_bf16 v[124:127], a[24:27], a[244:247], v[124:127]\n" \
"s_waitcnt lgkmcnt(8)\n" \
"v_and_b32 v155, 0xffff0000, v181\n" \
"v_lshlrev_b32 v156, 16, v182\n" \
"v_mfma_f32_16x16x32_bf16 v[112:115], a[0:3], a[64:67], v[112:115]\n" \
"ds_read_b64_tr_b16 a[24:25], v232 offset:20992\n" \
"ds_read_b64_tr_b16 a[26:27], v232 offset:23040\n" \
"v_mfma_f32_16x16x32_bf16 v[112:115], a[4:7], a[80:83], v[112:115]\n" \
"v_and_b32 v157, 0xffff0000, v182\n" \
"v_lshlrev_b32 v158, 16, v183\n" \
"v_mfma_f32_16x16x32_bf16 v[120:123], a[4:7], a[88:91], v[120:123]\n" \
"ds_read_b64_tr_b16 a[28:29], v232 offset:29184\n" \
"ds_read_b64_tr_b16 a[30:31], v232 offset:31232\n" \
"v_mfma_f32_16x16x32_bf16 v[120:123], a[0:3], a[72:75], v[120:123]\n" \
"s_waitcnt lgkmcnt(8)\n" \
"v_and_b32 v159, 0xffff0000, v183\n" \
"v_lshlrev_b32 v160, 16, v184\n" \
"v_mfma_f32_16x16x32_bf16 v[116:119], a[8:11], a[64:67], v[116:119]\n" \
"v_and_b32 v161, 0xffff0000, v184\n" \
"v_lshlrev_b32 v162, 16, v185\n" \
"v_mfma_f32_16x16x32_bf16 v[116:119], a[12:15], a[80:83], v[116:119]\n" \
"v_and_b32 v163, 0xffff0000, v185\n" \
"v_lshlrev_b32 v164, 16, v186\n" \
"v_mfma_f32_16x16x32_bf16 v[124:127], a[12:15], a[88:91], v[124:127]\n" \
"v_and_b32 v165, 0xffff0000, v186\n" \
"v_lshlrev_b32 v166, 16, v187\n" \
"v_mfma_f32_16x16x32_bf16 v[124:127], a[8:11], a[72:75], v[124:127]\n" \
"s_waitcnt lgkmcnt(4)\n" \
"v_and_b32 v167, 0xffff0000, v187\n" \
"v_mfma_f32_16x16x32_bf16 v[112:115], a[16:19], a[96:99], v[112:115]\n" \
"s_waitcnt vmcnt(7)\n" \
"v_readfirstlane_b32 s81, v238\n" \
"v_mfma_f32_16x16x32_bf16 v[112:115], a[20:23], a[112:115], v[112:115]\n" \
"v_mfma_f32_16x16x32_bf16 v[120:123], a[20:23], a[116:119], v[120:123]\n" \
"s_mov_b32 s40, s81\n" \
"s_sub_u32 s86, 15, s67\n" \
"s_and_b32 s25, s40, 15\n" \
"v_mfma_f32_16x16x32_bf16 v[120:123], a[16:19], a[104:107], v[120:123]\n" \
"s_waitcnt lgkmcnt(0)\n" \
"s_lshl_b32 s25, s25, 1\n" \
"v_mfma_f32_16x16x32_bf16 v[116:119], a[24:27], a[96:99], v[116:119]\n" \
"s_cmp_lt_u32 s40, 16\n" \
"s_cselect_b32 s86, s67, s86\n" \
"s_lshl_b32 s95, s86, 2\n" \
"v_mfma_f32_16x16x32_bf16 v[116:119], a[28:31], a[112:115], v[116:119]\n" \
"s_add_u32 s95, s95, %[wid]\n" \
"s_lshl_b32 s95, s95, 3\n" \
"s_lshr_b32 s26, s25, 4\n" \
"v_mfma_f32_16x16x32_bf16 v[124:127], a[28:31], a[116:119], v[124:127]\n" \
"s_add_u32 s95, s95, s26\n" \
"s_lshl_b32 s95, s95, 6\n" \
"s_and_b32 s27, s25, 15\n" \
"v_mfma_f32_16x16x32_bf16 v[124:127], a[24:27], a[104:107], v[124:127]\n" \
"s_add_u32 s95, s95, s27\n" \
"s_lshl_b32 s95, s95, 6\n" \
"s_add_u32 s95, s95, 59768832\n" \
"v_cvt_pk_bf16_f32 v112, v112, v113\n" \
"v_cvt_pk_bf16_f32 v113, v114, v115\n" \
"v_cvt_pk_bf16_f32 v114, v116, v117\n" \
"v_cvt_pk_bf16_f32 v115, v118, v119\n" \
"buffer_store_dwordx4 v[112:115], v250, s[32:35], s36 offen offset:3072 sc1\n" \
"v_cvt_pk_bf16_f32 v120, v120, v121\n" \
"v_cvt_pk_bf16_f32 v121, v122, v123\n" \
"v_cvt_pk_bf16_f32 v122, v124, v125\n" \
"v_cvt_pk_bf16_f32 v123, v126, v127\n" \
"buffer_store_dwordx4 v[120:123], v250, s[32:35], s21 offen offset:3072 sc1\n" \
"s_waitcnt vmcnt(0)\n" \
"buffer_store_dword v253, v246, s[32:35], s39 offen offset:128 sc1\n" \
"s_mul_i32 s26, s26, 7\n" \
"s_xor_b32 s26, s26, %[wid]\n" \
"s_lshl3_add_u32 s85, s86, s26\n" \
"s_lshl4_add_u32 s85, s85, s27\n" \
"s_and_b32 s84, s40, 16\n" \
"s_lshl_b32 s84, s84, 3\n" \
"v_cmp_ne_u32 vcc, 0, v245\n" \
"s_lshl_b32 s38, s85, 10\n" \
"s_cmp_eq_u32 s84, 0\n" \
"s_cselect_b32 s75, vcc_lo, vcc_hi\n" \
"buffer_load_dwordx2 v[128:129], v188, s[44:47], s38 offen offset:0\n" \
"s_lshr_b32 s76, s75, 1\n" \
"buffer_load_dwordx2 v[130:131], v188, s[44:47], s38 offen offset:32\n" \
"s_and_b32 s76, s76, s75\n" \
"buffer_load_dwordx2 v[132:133], v188, s[44:47], s38 offen offset:1024\n" \
"s_lshr_b32 s77, s76, 2\n" \
"buffer_load_dwordx2 v[134:135], v188, s[44:47], s38 offen offset:1056\n" \
"s_and_b32 s76, s76, s77\n" \
"s_and_b32 s76, s76, 0x11111111\n" \
"s_mov_b32 s74, s76\n" \
"s_add_u32 s18, s18, s84\n" \
"s_add_u32 s22, s95, 0\n" \
"s_bitcmp1_b32 s76, 0\n" \
"s_cselect_b32 s22, s22, 1073741824\n" \
"buffer_load_dwordx4 a[128:131], v214, s[32:35], s22 offen sc1\n" \
"buffer_load_dwordx4 a[160:163], v214, s[32:35], s22 offen offset:64 sc1\n" \
"s_add_u32 s22, s95, 2097152\n" \
"s_bitcmp1_b32 s76, 4\n" \
"s_cselect_b32 s22, s22, 1073741824\n" \
"buffer_load_dwordx4 a[132:135], v214, s[32:35], s22 offen sc1\n" \
"buffer_load_dwordx4 a[164:167], v214, s[32:35], s22 offen offset:64 sc1\n" \
"s_add_u32 s22, s95, 4194304\n" \
"s_bitcmp1_b32 s76, 8\n" \
"s_cselect_b32 s22, s22, 1073741824\n" \
"buffer_load_dwordx4 a[136:139], v214, s[32:35], s22 offen sc1\n" \
"buffer_load_dwordx4 a[168:171], v214, s[32:35], s22 offen offset:64 sc1\n" \
"s_add_u32 s22, s95, 6291456\n" \
"s_bitcmp1_b32 s76, 12\n" \
"s_cselect_b32 s22, s22, 1073741824\n" \
"buffer_load_dwordx4 a[140:143], v214, s[32:35], s22 offen sc1\n" \
"buffer_load_dwordx4 a[172:175], v214, s[32:35], s22 offen offset:64 sc1\n" \
"s_add_u32 s22, s95, 8388608\n" \
"s_bitcmp1_b32 s76, 16\n" \
"s_cselect_b32 s22, s22, 1073741824\n" \
"buffer_load_dwordx4 a[144:147], v214, s[32:35], s22 offen sc1\n" \
"buffer_load_dwordx4 a[176:179], v214, s[32:35], s22 offen offset:64 sc1\n" \
"s_add_u32 s22, s95, 10485760\n" \
"s_bitcmp1_b32 s76, 20\n" \
"s_cselect_b32 s22, s22, 1073741824\n" \
"buffer_load_dwordx4 a[148:151], v214, s[32:35], s22 offen sc1\n" \
"buffer_load_dwordx4 a[180:183], v214, s[32:35], s22 offen offset:64 sc1\n" \
"s_add_u32 s22, s95, 12582912\n" \
"s_bitcmp1_b32 s76, 24\n" \
"s_cselect_b32 s22, s22, 1073741824\n" \
"buffer_load_dwordx4 a[152:155], v214, s[32:35], s22 offen sc1\n" \
"buffer_load_dwordx4 a[184:187], v214, s[32:35], s22 offen offset:64 sc1\n" \
"s_add_u32 s22, s95, 14680064\n" \
"s_bitcmp1_b32 s76, 28\n" \
"s_cselect_b32 s22, s22, 1073741824\n" \
"buffer_load_dwordx4 a[156:159], v214, s[32:35], s22 offen sc1\n" \
"buffer_load_dwordx4 a[188:191], v214, s[32:35], s22 offen offset:64 sc1\n" \
"v_lshlrev_b32 v236, 2, v191\n" \
"v_lshrrev_b32 v100, 4, v191\n" \
"v_bfe_u32 v101, v191, 1, 2\n" \
"v_lshlrev_b32 v101, 4, v101\n" \
"v_and_or_b32 v101, v191, 8, v101\n" \
"v_and_or_b32 v101, v236, 4, v101\n" \
"v_and_b32 v102, 3, v100\n" \
"v_lshl_or_b32 v239, v102, 10, v101\n" \
"v_lshrrev_b32 v102, 2, v100\n" \
"v_lshl_or_b32 v239, v102, 13, v239\n" \
"v_lshl_or_b32 v101, v100, 6, v101\n" \
"buffer_load_dword v172, v101, s[32:35], 0 offen offset:0 sc1\n" \
"buffer_load_dword v174, v101, s[32:35], 0 offen offset:1024 sc1\n" \
"buffer_load_dword v176, v101, s[32:35], 0 offen offset:2048 sc1\n" \
"buffer_load_dword v178, v101, s[32:35], 0 offen offset:3072 sc1\n" \
  \
".Ljoin_%=:\n" \
"s_nop 1\n" \
"buffer_load_dword v245, v252, s[32:35], s18 offen sc1\n" \
"s_xor_b32 s94, s83, 0xc000\n" \
"s_lshl3_add_u32 s24, %[wid], s94\n" \
"v_mov_b32 v232, s24\n" \
"v_mov_b32 v233, s94\n" \
"v_lshlrev_b32 v234, 11, v251\n" \
"v_cmp_gt_u32 vcc, 8, v251\n" \
"v_mov_b32 v235, 1073741824\n" \
"s_lshl2_add_u32 s38, s64, %[wid]\n" \
"v_cndmask_b32 v234, v235, v234, vcc\n" \
"v_lshlrev_b32 v235, 3, v251\n" \
"s_lshl_b32 s38, s38, 9\n" \
"s_add_u32 s38, s38, 557056\n" \
"s_lshl_b64 s[42:43], 1, %[bid]\n" \
"s_lshr_b32 s24, %[bid], 6\n" \
"s_cmp_eq_u32 s24, %[wid]\n" \
"s_cselect_b64 s[42:43], s[42:43], 0\n" \
"s_call_b64 s[98:99], .Lch3_%=\n" \
"s_mov_b32 s82, 0x100000\n" \
".Lg0_%=:\n" \
"s_waitcnt vmcnt(0)\n" \
"s_cmp_eq_u32 s74, 0x11111111\n" \
"s_cbranch_scc0 .Lga_%=\n" \
"v_mfma_f32_16x16x32_bf16 v[92:95], v[210:213], a[128:131], 0\n" \
"v_lshlrev_b32 v48, 16, v128\n" \
"v_mfma_f32_16x16x32_bf16 v[96:99], v[208:211], a[128:131], 0\n" \
"v_and_b32 v49, 0xffff0000, v128\n" \
"v_mfma_f32_16x16x32_bf16 v[92:95], v[210:213], a[132:135], v[92:95]\n" \
"v_lshlrev_b32 v50, 16, v129\n" \
"v_mfma_f32_16x16x32_bf16 v[96:99], v[208:211], a[132:135], v[96:99]\n" \
"v_and_b32 v51, 0xffff0000, v129\n" \
"v_mfma_f32_16x16x32_bf16 v[92:95], v[210:213], a[136:139], v[92:95]\n" \
"v_lshlrev_b32 v52, 16, v130\n" \
"v_mfma_f32_16x16x32_bf16 v[96:99], v[208:211], a[136:139], v[96:99]\n" \
"v_and_b32 v53, 0xffff0000, v130\n" \
"v_mfma_f32_16x16x32_bf16 v[92:95], v[210:213], a[140:143], v[92:95]\n" \
"v_lshlrev_b32 v54, 16, v131\n" \
"v_mfma_f32_16x16x32_bf16 v[96:99], v[208:211], a[140:143], v[96:99]\n" \
"v_and_b32 v55, 0xffff0000, v131\n" \
"v_mfma_f32_16x16x32_bf16 v[92:95], v[210:213], a[144:147], v[92:95]\n" \
"v_lshlrev_b32 v56, 16, v132\n" \
"v_mfma_f32_16x16x32_bf16 v[96:99], v[208:211], a[144:147], v[96:99]\n" \
"v_and_b32 v57, 0xffff0000, v132\n" \
"v_mfma_f32_16x16x32_bf16 v[92:95], v[210:213], a[148:151], v[92:95]\n" \
"v_lshlrev_b32 v58, 16, v133\n" \
"v_mfma_f32_16x16x32_bf16 v[96:99], v[208:211], a[148:151], v[96:99]\n" \
"v_and_b32 v59, 0xffff0000, v133\n" \
"v_mfma_f32_16x16x32_bf16 v[92:95], v[210:213], a[152:155], v[92:95]\n" \
"v_lshlrev_b32 v60, 16, v134\n" \
"v_mfma_f32_16x16x32_bf16 v[96:99], v[208:211], a[152:155], v[96:99]\n" \
"v_and_b32 v61, 0xffff0000, v134\n" \
"v_mfma_f32_16x16x32_bf16 v[92:95], v[210:213], a[156:159], v[92:95]\n" \
"v_lshlrev_b32 v62, 16, v135\n" \
"v_mfma_f32_16x16x32_bf16 v[96:99], v[208:211], a[156:159], v[96:99]\n" \
"v_and_b32 v63, 0xffff0000, v135\n" \
"s_nop 4\n" \
"v_pk_fma_f32 v[92:93], v[92:93], v[136:137], v[48:49]\n" \
"v_mfma_f32_16x16x32_bf16 v[64:67], v[210:213], a[160:163], 0\n" \
"v_pk_fma_f32 v[94:95], v[94:95], v[138:139], v[50:51]\n" \
"v_mfma_f32_16x16x32_bf16 v[68:71], v[208:211], a[160:163], 0\n" \
"v_pk_fma_f32 v[96:97], v[96:97], v[140:141], v[52:53]\n" \
"v_mfma_f32_16x16x32_bf16 v[64:67], v[210:213], a[164:167], v[64:67]\n" \
"v_pk_fma_f32 v[98:99], v[98:99], v[142:143], v[54:55]\n" \
"v_mfma_f32_16x16x32_bf16 v[68:71], v[208:211], a[164:167], v[68:71]\n" \
"v_cvt_pk_bf16_f32 v100, v92, v93\n" \
"v_mfma_f32_16x16x32_bf16 v[64:67], v[210:213], a[168:171], v[64:67]\n" \
"v_cvt_pk_bf16_f32 v101, v94, v95\n" \
"v_mfma_f32_16x16x32_bf16 v[68:71], v[208:211], a[168:171], v[68:71]\n" \
"v_cvt_pk_bf16_f32 v102, v96, v97\n" \
"v_mfma_f32_16x16x32_bf16 v[64:67], v[210:213], a[172:175], v[64:67]\n" \
"v_cvt_pk_bf16_f32 v103, v98, v99\n" \
"v_mfma_f32_16x16x32_bf16 v[68:71], v[208:211], a[172:175], v[68:71]\n" \
"v_lshlrev_b32 v92, 16, v100\n" \
"v_mfma_f32_16x16x32_bf16 v[64:67], v[210:213], a[176:179], v[64:67]\n" \
"v_and_b32 v93, 0xffff0000, v100\n" \
"v_mfma_f32_16x16x32_bf16 v[68:71], v[208:211], a[176:179], v[68:71]\n" \
"v_lshlrev_b32 v94, 16, v101\n" \
"v_mfma_f32_16x16x32_bf16 v[64:67], v[210:213], a[180:183], v[64:67]\n" \
"v_readfirstlane_b32 s78, v92\n" \
"v_mfma_f32_16x16x32_bf16 v[68:71], v[208:211], a[180:183], v[68:71]\n" \
"v_and_b32 v95, 0xffff0000, v101\n" \
"v_mfma_f32_16x16x32_bf16 v[64:67], v[210:213], a[184:187], v[64:67]\n" \
"v_lshlrev_b32 v96, 16, v102\n" \
"v_mfma_f32_16x16x32_bf16 v[68:71], v[208:211], a[184:187], v[68:71]\n" \
"s_mov_b32 s79, s78\n" \
"v_mfma_f32_16x16x32_bf16 v[64:67], v[210:213], a[188:191], v[64:67]\n" \
"v_and_b32 v97, 0xffff0000, v102\n" \
"v_mfma_f32_16x16x32_bf16 v[68:71], v[208:211], a[188:191], v[68:71]\n" \
"v_lshlrev_b32 v98, 16, v103\n" \
"s_nop 2\n" \
"v_and_b32 v99, 0xffff0000, v103\n" \
"s_nop 2\n" \
"v_pk_add_f32 v[104:105], v[92:93], s[78:79] neg_lo:[0,1] neg_hi:[0,1]\n" \
"v_pk_fma_f32 v[64:65], v[64:65], v[136:137], v[56:57]\n" \
"v_pk_add_f32 v[106:107], v[94:95], s[78:79] neg_lo:[0,1] neg_hi:[0,1]\n" \
"v_pk_fma_f32 v[66:67], v[66:67], v[138:139], v[58:59]\n" \
"v_pk_add_f32 v[108:109], v[96:97], s[78:79] neg_lo:[0,1] neg_hi:[0,1]\n" \
"v_pk_fma_f32 v[68:69], v[68:69], v[140:141], v[60:61]\n" \
"v_pk_add_f32 v[110:111], v[98:99], s[78:79] neg_lo:[0,1] neg_hi:[0,1]\n" \
"v_pk_fma_f32 v[70:71], v[70:71], v[142:143], v[62:63]\n" \
"v_pk_add_f32 v[112:113], v[104:105], v[106:107]\n" \
"v_cvt_pk_bf16_f32 v72, v64, v65\n" \
"v_pk_mul_f32 v[114:115], v[104:105], v[104:105]\n" \
"v_cvt_pk_bf16_f32 v73, v66, v67\n" \
"v_pk_add_f32 v[116:117], v[108:109], v[110:111]\n" \
"v_cvt_pk_bf16_f32 v74, v68, v69\n" \
"v_pk_mul_f32 v[118:119], v[108:109], v[108:109]\n" \
"v_cvt_pk_bf16_f32 v75, v70, v71\n" \
"v_pk_fma_f32 v[114:115], v[106:107], v[106:107], v[114:115]\n" \
"v_lshlrev_b32 v64, 16, v72\n" \
"v_pk_fma_f32 v[118:119], v[110:111], v[110:111], v[118:119]\n" \
"v_and_b32 v65, 0xffff0000, v72\n" \
"v_pk_add_f32 v[112:113], v[112:113], v[116:117]\n" \
"v_lshlrev_b32 v66, 16, v73\n" \
"v_pk_add_f32 v[114:115], v[114:115], v[118:119]\n" \
"v_readfirstlane_b32 s24, v64\n" \
"v_add_f32 v112, v112, v113\n" \
"v_and_b32 v67, 0xffff0000, v73\n" \
"v_add_f32 v114, v114, v115\n" \
"v_lshlrev_b32 v68, 16, v74\n" \
"s_nop 0\n" \
"v_permlane32_swap_b32 v112, v114\n" \
"s_mov_b32 s25, s24\n" \
"v_add_f32 v112, v112, v114\n" \
"v_and_b32 v69, 0xffff0000, v74\n" \
"s_nop 0\n" \
"v_add_f32_dpp v112, v112, v112 quad_perm:[1,0,3,2] row_mask:0xf bank_mask:0xf\n" \
"v_lshlrev_b32 v70, 16, v75\n" \
"s_nop 0\n" \
"v_add_f32_dpp v112, v112, v112 quad_perm:[2,3,0,1] row_mask:0xf bank_mask:0xf\n" \
"v_and_b32 v71, 0xffff0000, v75\n" \
"s_nop 0\n" \
"v_add_f32_dpp v112, v112, v112 row_half_mirror row_mask:0xf bank_mask:0xf\n" \
"v_pk_add_f32 v[76:77], v[64:65], s[24:25] neg_lo:[0,1] neg_hi:[0,1]\n" \
"s_nop 0\n" \
"v_add_f32_dpp v112, v112, v112 row_mirror row_mask:0xf bank_mask:0xf\n" \
"v_pk_add_f32 v[78:79], v[66:67], s[24:25] neg_lo:[0,1] neg_hi:[0,1]\n" \
"s_nop 0\n" \
"v_add_f32_dpp v112, v112, v112 row_bcast:15 row_mask:0xa bank_mask:0xf\n" \
"v_pk_add_f32 v[80:81], v[68:69], s[24:25] neg_lo:[0,1] neg_hi:[0,1]\n" \
"v_mul_f32 v112, 0x3b000000, v112\n" \
"v_pk_add_f32 v[82:83], v[70:71], s[24:25] neg_lo:[0,1] neg_hi:[0,1]\n" \
"v_readlane_b32 s80, v112, 31\n" \
"v_pk_add_f32 v[84:85], v[76:77], v[78:79]\n" \
"v_readlane_b32 s81, v112, 63\n" \
"v_pk_mul_f32 v[86:87], v[76:77], v[76:77]\n" \
"v_mov_b32 v113, s78\n" \
"v_pk_add_f32 v[88:89], v[80:81], v[82:83]\n" \
"v_mov_b32 v114, s81\n" \
"v_pk_mul_f32 v[90:91], v[80:81], v[80:81]\n" \
"v_add_f32 v113, s80, v113\n" \
"v_pk_fma_f32 v[86:87], v[78:79], v[78:79], v[86:87]\n" \
"v_fma_f32 v114, -s80, s80, v114\n" \
"v_pk_fma_f32 v[90:91], v[82:83], v[82:83], v[90:91]\n" \
"v_max_f32 v115, 0, v114\n" \
"v_pk_add_f32 v[84:85], v[84:85], v[88:89]\n" \
"v_cmp_u_f32 s[22:23], v114, v114\n" \
"v_pk_add_f32 v[86:87], v[86:87], v[90:91]\n" \
"s_nop 0\n" \
"v_cndmask_b32 v114, v115, v114, s[22:23]\n" \
"v_add_f32 v84, v84, v85\n" \
"v_cvt_pk_bf16_f32 v116, v114, v113\n" \
"v_add_f32 v86, v86, v87\n" \
"v_lshlrev_b32 v114, 16, v116\n" \
"s_nop 0\n" \
"v_permlane32_swap_b32 v84, v86\n" \
"v_and_b32 v112, 0xffff0000, v116\n" \
"v_add_f32 v84, v84, v86\n" \
"v_add_f32 v114, 0x3727c5ac, v114\n" \
"s_nop 0\n" \
"v_add_f32_dpp v84, v84, v84 quad_perm:[1,0,3,2] row_mask:0xf bank_mask:0xf\n" \
"v_cvt_pk_bf16_f32 v114, v114, v114\n" \
"s_nop 0\n" \
"v_add_f32_dpp v84, v84, v84 quad_perm:[2,3,0,1] row_mask:0xf bank_mask:0xf\n" \
"v_pk_add_f32 v[104:105], v[92:93], v[112:113] op_sel_hi:[1,0] neg_lo:[0,1] neg_hi:[0,1]\n" \
"s_nop 0\n" \
"v_add_f32_dpp v84, v84, v84 row_half_mirror row_mask:0xf bank_mask:0xf\n" \
"v_lshlrev_b32 v114, 16, v114\n" \
"s_nop 0\n" \
"v_add_f32_dpp v84, v84, v84 row_mirror row_mask:0xf bank_mask:0xf\n" \
"v_pk_add_f32 v[106:107], v[94:95], v[112:113] op_sel_hi:[1,0] neg_lo:[0,1] neg_hi:[0,1]\n" \
"s_nop 0\n" \
"v_add_f32_dpp v84, v84, v84 row_bcast:15 row_mask:0xa bank_mask:0xf\n" \
"v_sqrt_f32 v114, v114\n" \
"v_mul_f32 v84, 0x3b000000, v84\n" \
"v_pk_add_f32 v[108:109], v[96:97], v[112:113] op_sel_hi:[1,0] neg_lo:[0,1] neg_hi:[0,1]\n" \
"v_readlane_b32 s26, v84, 31\n" \
"v_pk_add_f32 v[110:111], v[98:99], v[112:113] op_sel_hi:[1,0] neg_lo:[0,1] neg_hi:[0,1]\n" \
"v_readlane_b32 s27, v84, 63\n" \
"v_cvt_pk_bf16_f32 v116, v114, v112\n" \
"v_mov_b32 v85, s24\n" \
"v_pk_mul_f32 v[104:105], v[104:105], v[152:153]\n" \
"v_mov_b32 v86, s27\n" \
"v_lshlrev_b32 v114, 16, v116\n" \
"v_add_f32 v85, s26, v85\n" \
"v_pk_mul_f32 v[106:107], v[106:107], v[154:155]\n" \
"v_fma_f32 v86, -s26, s26, v86\n" \
"v_rcp_f32 v114, v114\n" \
"v_max_f32 v87, 0, v86\n" \
"v_pk_mul_f32 v[108:109], v[108:109], v[156:157]\n" \
"v_cmp_u_f32 s[20:21], v86, v86\n" \
"v_pk_mul_f32 v[110:111], v[110:111], v[158:159]\n" \
"s_nop 0\n" \
"v_cndmask_b32 v86, v87, v86, s[20:21]\n" \
"v_readfirstlane_b32 s72, v116\n" \
"v_cvt_pk_bf16_f32 v88, v86, v85\n" \
"v_pk_fma_f32 v[104:105], v[104:105], v[114:115], v[144:145] op_sel_hi:[1,0,1]\n" \
"v_lshlrev_b32 v86, 16, v88\n" \
"v_pk_fma_f32 v[106:107], v[106:107], v[114:115], v[146:147] op_sel_hi:[1,0,1]\n" \
"v_and_b32 v84, 0xffff0000, v88\n" \
"v_pk_fma_f32 v[108:109], v[108:109], v[114:115], v[148:149] op_sel_hi:[1,0,1]\n" \
"v_add_f32 v86, 0x3727c5ac, v86\n" \
"v_pk_fma_f32 v[110:111], v[110:111], v[114:115], v[150:151] op_sel_hi:[1,0,1]\n" \
"v_cvt_pk_bf16_f32 v86, v86, v86\n" \
"v_cvt_pk_bf16_f32 v220, v104, v105\n" \
"v_pk_add_f32 v[76:77], v[64:65], v[84:85] op_sel_hi:[1,0] neg_lo:[0,1] neg_hi:[0,1]\n" \
"v_cvt_pk_bf16_f32 v221, v106, v107\n" \
"v_lshlrev_b32 v86, 16, v86\n" \
"v_cvt_pk_bf16_f32 v222, v108, v109\n" \
"v_pk_add_f32 v[78:79], v[66:67], v[84:85] op_sel_hi:[1,0] neg_lo:[0,1] neg_hi:[0,1]\n" \
"v_cvt_pk_bf16_f32 v223, v110, v111\n" \
"v_sqrt_f32 v86, v86\n" \
"v_dot2_f32_bf16 v112, v220, v168, 0\n" \
"v_pk_add_f32 v[80:81], v[68:69], v[84:85] op_sel_hi:[1,0] neg_lo:[0,1] neg_hi:[0,1]\n" \
"v_dot2_f32_bf16 v113, v221, v169, 0\n" \
"v_pk_add_f32 v[82:83], v[70:71], v[84:85] op_sel_hi:[1,0] neg_lo:[0,1] neg_hi:[0,1]\n" \
"v_dot2_f32_bf16 v112, v222, v170, v112\n" \
"v_cvt_pk_bf16_f32 v88, v86, v84\n" \
"v_dot2_f32_bf16 v113, v223, v171, v113\n" \
"v_pk_mul_f32 v[76:77], v[76:77], v[152:153]\n" \
"s_nop 2\n" \
"v_lshlrev_b32 v86, 16, v88\n" \
"v_add_f32 v112, v112, v113\n" \
"v_pk_mul_f32 v[78:79], v[78:79], v[154:155]\n" \
"s_nop 0\n" \
"v_add_f32_dpp v112, v112, v112 quad_perm:[1,0,3,2] row_mask:0xf bank_mask:0xf\n" \
"v_rcp_f32 v86, v86\n" \
"s_nop 0\n" \
"v_add_f32_dpp v112, v112, v112 quad_perm:[2,3,0,1] row_mask:0xf bank_mask:0xf\n" \
"v_pk_mul_f32 v[80:81], v[80:81], v[156:157]\n" \
"s_nop 0\n" \
"v_add_f32_dpp v112, v112, v112 row_half_mirror row_mask:0xf bank_mask:0xf\n" \
"v_pk_mul_f32 v[82:83], v[82:83], v[158:159]\n" \
"s_nop 0\n" \
"v_add_f32_dpp v112, v112, v112 row_mirror row_mask:0xf bank_mask:0xf\n" \
"v_readfirstlane_b32 s73, v88\n" \
"s_nop 0\n" \
"v_add_f32_dpp v112, v112, v112 row_bcast:15 row_mask:0xa bank_mask:0xf\n" \
"v_pk_fma_f32 v[76:77], v[76:77], v[86:87], v[144:145] op_sel_hi:[1,0,1]\n" \
"s_nop 0\n" \
"v_add_f32_dpp v112, v112, v112 row_bcast:31 row_mask:0xc bank_mask:0xf\n" \
"v_pk_fma_f32 v[78:79], v[78:79], v[86:87], v[146:147] op_sel_hi:[1,0,1]\n" \
"v_cvt_pk_bf16_f32 v112, v112, v112\n" \
"v_pk_fma_f32 v[80:81], v[80:81], v[86:87], v[148:149] op_sel_hi:[1,0,1]\n" \
"s_sub_u32 s79, 2047, s85\n" \
"v_pk_fma_f32 v[82:83], v[82:83], v[86:87], v[150:151] op_sel_hi:[1,0,1]\n" \
"s_lshl_b32 s79, s79, 5\n" \
"v_cvt_pk_bf16_f32 v216, v76, v77\n" \
"s_or_b32 s79, s79, 3\n" \
"v_cvt_pk_bf16_f32 v217, v78, v79\n" \
"v_readlane_b32 s78, v112, 63\n" \
"v_cvt_pk_bf16_f32 v218, v80, v81\n" \
"s_and_b32 s78, s78, 0xffff\n" \
"v_cvt_pk_bf16_f32 v219, v82, v83\n" \
"s_or_b32 s80, s78, 0x8000\n" \
"v_dot2_f32_bf16 v84, v216, v168, 0\n" \
"s_andn2_b32 s81, 0xffff, s78\n" \
"v_dot2_f32_bf16 v85, v217, v169, 0\n" \
"s_bitcmp1_b32 s78, 15\n" \
"s_cselect_b32 s80, s81, s80\n" \
"v_dot2_f32_bf16 v84, v218, v170, v84\n" \
"s_and_b32 s78, s78, 0x7fff\n" \
"v_dot2_f32_bf16 v85, v219, v171, v85\n" \
"s_cmp_gt_u32 s78, 0x7f80\n" \
"s_cselect_b32 s80, 0, s80\n" \
"s_nop 2\n" \
"s_pack_ll_b32_b16 s87, s79, s80\n" \
"v_add_f32 v84, v84, v85\n" \
"s_nop 1\n" \
"v_add_f32_dpp v84, v84, v84 quad_perm:[1,0,3,2] row_mask:0xf bank_mask:0xf\n" \
"s_nop 1\n" \
"v_add_f32_dpp v84, v84, v84 quad_perm:[2,3,0,1] row_mask:0xf bank_mask:0xf\n" \
"s_nop 1\n" \
"v_add_f32_dpp v84, v84, v84 row_half_mirror row_mask:0xf bank_mask:0xf\n" \
"s_nop 1\n" \
"v_add_f32_dpp v84, v84, v84 row_mirror row_mask:0xf bank_mask:0xf\n" \
"s_nop 1\n" \
"v_add_f32_dpp v84, v84, v84 row_bcast:15 row_mask:0xa bank_mask:0xf\n" \
"s_nop 1\n" \
"v_add_f32_dpp v84, v84, v84 row_bcast:31 row_mask:0xc bank_mask:0xf\n" \
"v_cvt_pk_bf16_f32 v84, v84, v84\n" \
"s_sub_u32 s25, 2046, s85\n" \
"s_lshl_b32 s25, s25, 5\n" \
"s_or_b32 s25, s25, 3\n" \
"v_readlane_b32 s24, v84, 63\n" \
"s_and_b32 s24, s24, 0xffff\n" \
"s_or_b32 s26, s24, 0x8000\n" \
"s_andn2_b32 s27, 0xffff, s24\n" \
"s_bitcmp1_b32 s24, 15\n" \
"s_cselect_b32 s26, s27, s26\n" \
"s_and_b32 s24, s24, 0x7fff\n" \
"s_cmp_gt_u32 s24, 0x7f80\n" \
"s_cselect_b32 s26, 0, s26\n" \
"s_pack_ll_b32_b16 s88, s25, s26\n" \
"s_cmp_gt_u32 s87, s88\n" \
"s_cselect_b32 s25, s87, s88\n" \
"s_cselect_b32 s24, s72, s73\n" \
  \
"v_mov_b32 v85, s25\n" \
"v_mov_b32 v84, s24\n" \
"ds_write_b64 v232, v[84:85]\n" \
"s_waitcnt lgkmcnt(0)\n" \
"s_barrier\n" \
"ds_read_b128 v[84:87], v233\n" \
"ds_read_b128 v[88:91], v233 offset:16\n" \
"s_lshl_b32 s26, %[bid], 3\n" \
"s_add_u32 s26, s26, 557056\n" \
"s_cmp_eq_u32 %[wid], 0\n" \
"s_cselect_b32 s26, s26, 1073741824\n" \
"s_waitcnt lgkmcnt(0)\n" \
"v_cmp_gt_u32 vcc, v85, v87\n" \
"v_max_u32 v85, v85, v87\n" \
"s_nop 0\n" \
"v_cndmask_b32 v84, v86, v84, vcc\n" \
"v_cmp_gt_u32 vcc, v89, v91\n" \
"v_max_u32 v89, v89, v91\n" \
"s_nop 0\n" \
"v_cndmask_b32 v88, v90, v88, vcc\n" \
"v_cmp_gt_u32 vcc, v85, v89\n" \
"v_max_u32 v85, v85, v89\n" \
"s_nop 0\n" \
"v_cndmask_b32 v84, v88, v84, vcc\n" \
"buffer_store_dwordx2 v[84:85], v234, s[32:35], s26 offen sc1\n" \
"buffer_load_dwordx2 v[86:87], v235, s[32:35], s38 offen sc1\n" \
"v_readfirstlane_b32 s92, v85\n" \
"v_lshlrev_b32 v104, 16, v220\n" \
"v_lshlrev_b32 v76, 16, v216\n" \
"v_and_b32 v105, 0xffff0000, v220\n" \
"v_and_b32 v77, 0xffff0000, v216\n" \
"v_lshlrev_b32 v106, 16, v221\n" \
"v_lshlrev_b32 v78, 16, v217\n" \
"v_and_b32 v107, 0xffff0000, v221\n" \
"v_and_b32 v79, 0xffff0000, v217\n" \
"v_lshlrev_b32 v108, 16, v222\n" \
"v_lshlrev_b32 v80, 16, v218\n" \
"v_and_b32 v109, 0xffff0000, v222\n" \
"v_and_b32 v81, 0xffff0000, v218\n" \
"v_lshlrev_b32 v110, 16, v223\n" \
"v_lshlrev_b32 v82, 16, v219\n" \
"v_and_b32 v111, 0xffff0000, v223\n" \
"v_and_b32 v83, 0xffff0000, v219\n" \
"v_pk_fma_f32 v[104:105], v[104:105], v[160:161], v[92:93]\n" \
"v_pk_fma_f32 v[76:77], v[76:77], v[160:161], v[64:65]\n" \
"v_pk_fma_f32 v[106:107], v[106:107], v[162:163], v[94:95]\n" \
"v_pk_fma_f32 v[78:79], v[78:79], v[162:163], v[66:67]\n" \
"v_pk_fma_f32 v[108:109], v[108:109], v[164:165], v[96:97]\n" \
"v_pk_fma_f32 v[80:81], v[80:81], v[164:165], v[68:69]\n" \
"v_pk_fma_f32 v[110:111], v[110:111], v[166:167], v[98:99]\n" \
"v_pk_fma_f32 v[82:83], v[82:83], v[166:167], v[70:71]\n" \
"v_cvt_pk_bf16_f32 v100, v104, v105\n" \
"v_cvt_pk_bf16_f32 v72, v76, v77\n" \
"v_cvt_pk_bf16_f32 v101, v106, v107\n" \
"v_cvt_pk_bf16_f32 v73, v78, v79\n" \
"v_cvt_pk_bf16_f32 v102, v108, v109\n" \
"v_cvt_pk_bf16_f32 v74, v80, v81\n" \
"v_cvt_pk_bf16_f32 v103, v110, v111\n" \
"v_cvt_pk_bf16_f32 v75, v82, v83\n" \
"s_mov_b32 vcc_lo, 0xcccccccc\n" \
"s_mov_b32 vcc_lo, 0xcccccccc\n" \
"s_mov_b32 vcc_hi, 0xcccccccc\n" \
"s_mov_b32 vcc_hi, 0xcccccccc\n" \
"v_mov_b32_dpp v104, v102 quad_perm:[0,2,0,2] row_mask:0xf bank_mask:0xf\n" \
"v_mov_b32_dpp v76, v74 quad_perm:[0,2,0,2] row_mask:0xf bank_mask:0xf\n" \
"v_mov_b32_dpp v105, v103 quad_perm:[0,2,0,2] row_mask:0xf bank_mask:0xf\n" \
"v_mov_b32_dpp v77, v75 quad_perm:[0,2,0,2] row_mask:0xf bank_mask:0xf\n" \
"v_mov_b32_dpp v106, v102 quad_perm:[1,3,1,3] row_mask:0xf bank_mask:0xf\n" \
"v_mov_b32_dpp v78, v74 quad_perm:[1,3,1,3] row_mask:0xf bank_mask:0xf\n" \
"v_mov_b32_dpp v107, v103 quad_perm:[1,3,1,3] row_mask:0xf bank_mask:0xf\n" \
"v_mov_b32_dpp v79, v75 quad_perm:[1,3,1,3] row_mask:0xf bank_mask:0xf\n" \
"v_cndmask_b32_dpp v224, v100, v104, vcc quad_perm:[0,2,0,2] row_mask:0xf bank_mask:0xf\n" \
"v_cndmask_b32_dpp v228, v72, v76, vcc quad_perm:[0,2,0,2] row_mask:0xf bank_mask:0xf\n" \
"v_cndmask_b32_dpp v225, v101, v105, vcc quad_perm:[0,2,0,2] row_mask:0xf bank_mask:0xf\n" \
"v_cndmask_b32_dpp v229, v73, v77, vcc quad_perm:[0,2,0,2] row_mask:0xf bank_mask:0xf\n" \
"v_cndmask_b32_dpp v226, v100, v106, vcc quad_perm:[1,3,1,3] row_mask:0xf bank_mask:0xf\n" \
"v_cndmask_b32_dpp v230, v72, v78, vcc quad_perm:[1,3,1,3] row_mask:0xf bank_mask:0xf\n" \
"v_cndmask_b32_dpp v227, v101, v107, vcc quad_perm:[1,3,1,3] row_mask:0xf bank_mask:0xf\n" \
"v_cndmask_b32_dpp v231, v73, v79, vcc quad_perm:[1,3,1,3] row_mask:0xf bank_mask:0xf\n" \
"v_accvgpr_read_b32 v180, a100\n" \
"v_accvgpr_read_b32 v181, a101\n" \
"v_accvgpr_read_b32 v182, a102\n" \
"v_accvgpr_read_b32 v183, a103\n" \
"v_accvgpr_read_b32 v184, a108\n" \
"v_accvgpr_read_b32 v185, a109\n" \
"v_accvgpr_read_b32 v186, a110\n" \
"v_accvgpr_read_b32 v187, a111\n" \
"s_mov_b32 s24, 0x05040100\n" \
"s_mov_b32 s25, 0x07060302\n" \
"v_accvgpr_read_b32 v32, a68\n" \
"v_accvgpr_read_b32 v33, a69\n" \
"v_accvgpr_read_b32 v34, a70\n" \
"v_accvgpr_read_b32 v35, a71\n" \
"v_accvgpr_read_b32 v36, a76\n" \
"v_accvgpr_read_b32 v37, a77\n" \
"v_accvgpr_read_b32 v38, a78\n" \
"v_accvgpr_read_b32 v39, a79\n" \
"v_accvgpr_read_b32 v40, a84\n" \
"v_accvgpr_read_b32 v41, a85\n" \
"v_accvgpr_read_b32 v42, a86\n" \
"v_accvgpr_read_b32 v43, a87\n" \
"v_accvgpr_read_b32 v44, a92\n" \
"v_accvgpr_read_b32 v45, a93\n" \
"v_accvgpr_read_b32 v46, a94\n" \
"v_accvgpr_read_b32 v47, a95\n" \
"v_perm_b32 v0, v36, v32, s24\n" \
"v_perm_b32 v1, v36, v32, s25\n" \
"v_perm_b32 v2, v37, v33, s24\n" \
"v_perm_b32 v3, v37, v33, s25\n" \
"v_perm_b32 v4, v38, v34, s24\n" \
"v_perm_b32 v5, v38, v34, s25\n" \
"v_perm_b32 v6, v39, v35, s24\n" \
"v_perm_b32 v7, v39, v35, s25\n" \
"v_perm_b32 v8, v44, v40, s24\n" \
"v_perm_b32 v9, v44, v40, s25\n" \
"v_perm_b32 v10, v45, v41, s24\n" \
"v_perm_b32 v11, v45, v41, s25\n" \
"v_perm_b32 v12, v46, v42, s24\n" \
"v_perm_b32 v13, v46, v42, s25\n" \
"v_perm_b32 v14, v47, v43, s24\n" \
"v_perm_b32 v15, v47, v43, s25\n" \
"v_lshlrev_b32 v16, 16, v180\n" \
"v_and_b32 v17, 0xffff0000, v180\n" \
"v_lshlrev_b32 v18, 16, v181\n" \
"v_and_b32 v19, 0xffff0000, v181\n" \
"v_lshlrev_b32 v20, 16, v182\n" \
"v_and_b32 v21, 0xffff0000, v182\n" \
"v_lshlrev_b32 v22, 16, v183\n" \
"v_and_b32 v23, 0xffff0000, v183\n" \
"v_lshlrev_b32 v24, 16, v184\n" \
"v_and_b32 v25, 0xffff0000, v184\n" \
"v_lshlrev_b32 v26, 16, v185\n" \
"v_and_b32 v27, 0xffff0000, v185\n" \
"v_lshlrev_b32 v28, 16, v186\n" \
"v_and_b32 v29, 0xffff0000, v186\n" \
"v_lshlrev_b32 v30, 16, v187\n" \
"v_and_b32 v31, 0xffff0000, v187\n" \
"v_and_b32 v173, 0xffff0000, v172\n" \
".Lst3_0_%=:\n" \
"s_cbranch_execz .Lst3_4_%=\n" \
"v_lshlrev_b32 v172, 16, v172\n" \
"v_and_b32 v175, 0xffff0000, v174\n" \
"v_lshlrev_b32 v174, 16, v174\n" \
"v_and_b32 v177, 0xffff0000, v176\n" \
"v_lshlrev_b32 v176, 16, v176\n" \
"v_and_b32 v179, 0xffff0000, v178\n" \
"v_lshlrev_b32 v178, 16, v178\n" \
"v_lshrrev_b32 v100, 4, v191\n" \
"v_lshl_add_u32 v180, v100, 4, s94\n" \
"v_add_u32 v180, 256, v180\n" \
"v_and_b32 v101, 28, v236\n" \
"v_add_u32 v181, s94, v101\n" \
"v_add_u32 v181, 256, v181\n" \
"v_lshrrev_b32 v101, 1, v191\n" \
"v_and_b32 v102, 1, v191\n" \
"v_lshlrev_b32 v102, 4, v102\n" \
"v_lshl_or_b32 v182, v101, 11, v102\n" \
"s_lshl_b32 s24, %[wid], 5\n" \
"s_add_u32 s24, s24, s94\n" \
"s_add_u32 s24, s24, 512\n" \
"v_bfe_u32 v101, v251, 4, 2\n" \
"v_lshl_add_u32 v183, v101, 2, v102\n" \
"v_add_u32 v183, s24, v183\n" \
"v_and_b32 v101, 3, v191\n" \
"s_add_u32 s24, s94, 512\n" \
"v_lshl_add_u32 v238, v101, 3, s24\n" \
"s_lshl3_add_u32 s24, s64, s65\n" \
"v_lshrrev_b32 v101, 2, v191\n" \
"v_cmp_eq_u32 vcc, s24, v101\n" \
"v_mov_b32 v237, 1073741824\n" \
"s_lshl_b32 s24, s24, 5\n" \
"s_add_u32 s37, s24, 98304\n" \
"v_cndmask_b32 v237, v237, v236, vcc\n" \
"s_add_u32 s29, s37, 262144\n" \
"s_cmp_lt_u32 %[wid], 3\n" \
"s_cselect_b32 s29, s29, 1073741824\n" \
"s_lshl_b32 s24, s69, 11\n" \
"s_add_u32 s24, s24, 98304\n" \
"s_cmp_lt_u32 s65, 28\n" \
"s_cselect_b32 s41, s24, 1073741824\n" \
"s_lshl_b32 s85, s85, 10\n" \
"s_cmp_eq_u32 s87, s92\n" \
"s_cselect_b32 s24, 1073741824, s85\n" \
"buffer_store_dwordx4 v[224:227], v248, s[52:55], s24 offen offset:0 sc1\n" \
"s_cmp_eq_u32 s88, s92\n" \
"s_cselect_b32 s24, 1073741824, s85\n" \
"buffer_store_dwordx4 v[228:231], v248, s[52:55], s24 offen offset:1024 sc1\n" \
"s_mov_b32 s82, 0x100000\n" \
"s_mov_b32 s93, 0\n" \
"s_waitcnt vmcnt(2)\n" \
".Lkp_%=:\n" \
"v_cndmask_b32 v87, v87, v85, s[42:43]\n" \
"v_cndmask_b32 v86, v86, v84, s[42:43]\n" \
"v_cmp_eq_u32 vcc, 0, v87\n" \
"v_mov_b32 v88, v87\n" \
"s_nop 1\n" \
"s_cbranch_vccnz .Lkr_%=\n" \
".Lkb_%=:\n" \
"v_max_u32_dpp v88, v88, v88 quad_perm:[1,0,3,2] row_mask:0xf bank_mask:0xf\n" \
"s_nop 1\n" \
"v_max_u32_dpp v88, v88, v88 quad_perm:[2,3,0,1] row_mask:0xf bank_mask:0xf\n" \
"s_nop 1\n" \
"v_max_u32_dpp v88, v88, v88 row_half_mirror row_mask:0xf bank_mask:0xf\n" \
"s_nop 1\n" \
"v_max_u32_dpp v88, v88, v88 row_mirror row_mask:0xf bank_mask:0xf\n" \
"s_nop 1\n" \
"v_max_u32_dpp v88, v88, v88 row_bcast:15 row_mask:0xa bank_mask:0xf\n" \
"s_nop 1\n" \
"v_max_u32_dpp v88, v88, v88 row_bcast:31 row_mask:0xc bank_mask:0xf\n" \
"s_nop 0\n" \
"v_readlane_b32 s25, v88, 63\n" \
"s_nop 1\n" \
"v_cmp_eq_u32 vcc, s25, v87\n" \
"v_mov_b32 v89, s25\n" \
"s_nop 2\n" \
"s_ff1_i32_b64 s26, vcc\n" \
"s_and_b32 s26, s26, 63\n" \
"v_readlane_b32 s24, v86, s26\n" \
"s_nop 1\n" \
"v_mov_b32 v88, s24\n" \
"ds_write_b64 v232, v[88:89] offset:64\n" \
"s_waitcnt lgkmcnt(0)\n" \
"s_barrier\n" \
"ds_read_b128 v[84:87], v233 offset:64\n" \
"ds_read_b128 v[88:91], v233 offset:80\n" \
"s_waitcnt lgkmcnt(0)\n" \
"v_cmp_gt_u32 vcc, v85, v87\n" \
"v_max_u32 v85, v85, v87\n" \
"s_nop 0\n" \
"v_cndmask_b32 v84, v86, v84, vcc\n" \
"v_cmp_gt_u32 vcc, v89, v91\n" \
"v_max_u32 v89, v89, v91\n" \
"s_nop 0\n" \
"v_cndmask_b32 v88, v90, v88, vcc\n" \
"v_cmp_gt_u32 vcc, v85, v89\n" \
"v_max_u32 v85, v85, v89\n" \
"s_nop 0\n" \
"v_cndmask_b32 v84, v88, v84, vcc\n" \
"v_readfirstlane_b32 s93, v85\n" \
"s_bfe_u32 s30, s93, 0xb0005\n" \
"s_sub_u32 s30, 2047, s30\n" \
"s_mov_b32 s22, 0x18100800\n" \
"s_mov_b32 s23, 0x01091119\n" \
"s_bfe_u32 s24, s30, 0x30004\n" \
"s_lshl_b32 s24, s24, 3\n" \
"s_lshr_b64 s[22:23], s[22:23], s24\n" \
"s_and_b32 s22, s22, 0xff\n" \
"s_lshr_b32 s25, s30, 7\n" \
"s_lshl_b32 s25, s25, 5\n" \
"s_add_u32 s25, s25, s22\n" \
"s_lshl_b32 s25, s25, 6\n" \
"s_and_b32 s24, s30, 15\n" \
"s_add_u32 s25, s25, s24\n" \
"s_lshl_b32 s25, s25, 6\n" \
"s_add_u32 s25, s25, 59768832\n" \
"s_lshl_b32 s30, s30, 10\n" \
  \
"s_add_u32 s24, s25, 0\n" \
"buffer_load_dword v32, v239, s[32:35], s24 offen sc1\n" \
"s_add_u32 s24, s25, 2097152\n" \
"buffer_load_dword v33, v239, s[32:35], s24 offen sc1\n" \
"s_add_u32 s24, s25, 4194304\n" \
"buffer_load_dword v34, v239, s[32:35], s24 offen sc1\n" \
"s_add_u32 s24, s25, 6291456\n" \
"buffer_load_dword v35, v239, s[32:35], s24 offen sc1\n" \
"s_add_u32 s24, s25, 8388608\n" \
"buffer_load_dword v36, v239, s[32:35], s24 offen sc1\n" \
"s_add_u32 s24, s25, 10485760\n" \
"buffer_load_dword v37, v239, s[32:35], s24 offen sc1\n" \
"s_add_u32 s24, s25, 12582912\n" \
"buffer_load_dword v38, v239, s[32:35], s24 offen sc1\n" \
"s_add_u32 s24, s25, 14680064\n" \
"buffer_load_dword v39, v239, s[32:35], s24 offen sc1\n" \
"buffer_load_dword v40, v236, s[44:47], s30 offen\n" \
"s_cmp_eq_u32 s93, s92\n" \
"s_cselect_b32 s92, 0, s92\n" \
"s_cmp_eq_u32 s87, s92\n" \
"s_cselect_b32 s24, s85, 1073741824\n" \
"buffer_store_dwordx4 v[224:227], v248, s[52:55], s24 offen offset:0 sc1\n" \
"s_cmp_eq_u32 s88, s92\n" \
"s_cselect_b32 s24, s85, 1073741824\n" \
"buffer_store_dwordx4 v[228:231], v248, s[52:55], s24 offen offset:1024 sc1\n" \
"v_lshlrev_b32 v86, 16, v84\n" \
"v_and_b32 v88, 0xffff0000, v84\n" \
"v_rcp_f32 v86, v86\n" \
"s_lshr_b32 s24, s93, 16\n" \
"s_and_b32 s25, s24, 0x7fff\n" \
"s_andn2_b32 s26, 0xffff, s24\n" \
"s_bitcmp1_b32 s24, 15\n" \
"s_cselect_b32 s24, s25, s26\n" \
"s_lshl_b32 s24, s24, 16\n" \
"v_mov_b32 v90, 0xbfb8aa3b\n" \
"v_mul_f32 v90, s24, v90\n" \
"v_exp_f32 v90, v90\n" \
"s_nop 0\n" \
"v_add_f32 v90, 1.0, v90\n" \
"v_rcp_f32 v90, v90\n" \
"s_nop 0\n" \
"v_cvt_pk_bf16_f32 v90, v90, v90\n" \
"v_lshlrev_b32 v90, 16, v90\n" \
"s_waitcnt vmcnt(2)\n" \
"v_lshlrev_b32 v44, 16, v32\n" \
"v_and_b32 v45, 0xffff0000, v32\n" \
"v_lshlrev_b32 v46, 16, v33\n" \
"v_and_b32 v47, 0xffff0000, v33\n" \
"v_lshlrev_b32 v48, 16, v34\n" \
"v_and_b32 v49, 0xffff0000, v34\n" \
"v_lshlrev_b32 v50, 16, v35\n" \
"v_and_b32 v51, 0xffff0000, v35\n" \
"v_lshlrev_b32 v52, 16, v36\n" \
"v_and_b32 v53, 0xffff0000, v36\n" \
"v_lshlrev_b32 v54, 16, v37\n" \
"v_and_b32 v55, 0xffff0000, v37\n" \
"v_lshlrev_b32 v56, 16, v38\n" \
"v_and_b32 v57, 0xffff0000, v38\n" \
"v_lshlrev_b32 v58, 16, v39\n" \
"v_and_b32 v59, 0xffff0000, v39\n" \
"v_pk_add_f32 v[44:45], v[44:45], v[46:47]\n" \
"v_pk_add_f32 v[48:49], v[48:49], v[50:51]\n" \
"v_pk_add_f32 v[52:53], v[52:53], v[54:55]\n" \
"v_pk_add_f32 v[56:57], v[56:57], v[58:59]\n" \
"v_pk_add_f32 v[44:45], v[44:45], v[48:49]\n" \
"v_pk_add_f32 v[52:53], v[52:53], v[56:57]\n" \
"v_lshlrev_b32 v60, 16, v40\n" \
"v_pk_add_f32 v[44:45], v[44:45], v[52:53]\n" \
"v_and_b32 v61, 0xffff0000, v40\n" \
"v_pk_fma_f32 v[60:61], v[44:45], v[172:173], v[60:61]\n" \
"v_cvt_pk_bf16_f32 v46, v60, v61\n" \
"v_lshlrev_b32 v60, 16, v46\n" \
"v_and_b32 v61, 0xffff0000, v46\n" \
"v_pk_add_f32 v[44:45], v[60:61], v[88:89] op_sel_hi:[1,0] neg_lo:[0,1] neg_hi:[0,1]\n" \
"v_pk_mul_f32 v[44:45], v[44:45], v[176:177]\n" \
"v_pk_fma_f32 v[62:63], v[44:45], v[86:87], v[174:175] op_sel_hi:[1,0,1]\n" \
"v_cvt_pk_bf16_f32 v46, v62, v63\n" \
"v_lshlrev_b32 v62, 16, v46\n" \
"v_and_b32 v63, 0xffff0000, v46\n" \
"v_dot2_f32_bf16 v64, v0, v46, 0\n" \
"v_dot2_f32_bf16 v65, v1, v46, 0\n" \
"v_dot2_f32_bf16 v66, v2, v46, 0\n" \
"v_dot2_f32_bf16 v67, v3, v46, 0\n" \
"v_dot2_f32_bf16 v68, v4, v46, 0\n" \
"v_dot2_f32_bf16 v69, v5, v46, 0\n" \
"v_dot2_f32_bf16 v70, v6, v46, 0\n" \
"v_dot2_f32_bf16 v71, v7, v46, 0\n" \
"v_dot2_f32_bf16 v72, v8, v46, 0\n" \
"v_dot2_f32_bf16 v73, v9, v46, 0\n" \
"v_dot2_f32_bf16 v74, v10, v46, 0\n" \
"v_dot2_f32_bf16 v75, v11, v46, 0\n" \
"v_dot2_f32_bf16 v76, v12, v46, 0\n" \
"v_dot2_f32_bf16 v77, v13, v46, 0\n" \
"v_dot2_f32_bf16 v78, v14, v46, 0\n" \
"v_dot2_f32_bf16 v79, v15, v46, 0\n" \
"s_nop 1\n" \
"v_permlane32_swap_b32 v64, v72\n" \
"v_permlane32_swap_b32 v65, v73\n" \
"v_permlane32_swap_b32 v66, v74\n" \
"v_permlane32_swap_b32 v67, v75\n" \
"v_permlane32_swap_b32 v68, v76\n" \
"v_permlane32_swap_b32 v69, v77\n" \
"v_permlane32_swap_b32 v70, v78\n" \
"v_permlane32_swap_b32 v71, v79\n" \
"v_pk_add_f32 v[64:65], v[64:65], v[72:73]\n" \
"v_pk_add_f32 v[66:67], v[66:67], v[74:75]\n" \
"v_pk_add_f32 v[68:69], v[68:69], v[76:77]\n" \
"v_pk_add_f32 v[70:71], v[70:71], v[78:79]\n" \
"s_nop 0\n" \
"v_permlane16_swap_b32 v64, v68\n" \
"v_permlane16_swap_b32 v65, v69\n" \
"v_permlane16_swap_b32 v66, v70\n" \
"v_permlane16_swap_b32 v67, v71\n" \
"v_pk_add_f32 v[64:65], v[64:65], v[68:69]\n" \
"v_pk_add_f32 v[66:67], v[66:67], v[70:71]\n" \
"s_nop 0\n" \
"v_add_f32_dpp v64, v64, v64 quad_perm:[1,0,3,2] row_mask:0xf bank_mask:0xf\n" \
"v_add_f32_dpp v65, v65, v65 quad_perm:[1,0,3,2] row_mask:0xf bank_mask:0xf\n" \
"v_add_f32_dpp v66, v66, v66 quad_perm:[1,0,3,2] row_mask:0xf bank_mask:0xf\n" \
"v_add_f32_dpp v67, v67, v67 quad_perm:[1,0,3,2] row_mask:0xf bank_mask:0xf\n" \
"v_add_f32_dpp v64, v64, v64 quad_perm:[2,3,0,1] row_mask:0xf bank_mask:0xf\n" \
"v_add_f32_dpp v65, v65, v65 quad_perm:[2,3,0,1] row_mask:0xf bank_mask:0xf\n" \
"v_add_f32_dpp v66, v66, v66 quad_perm:[2,3,0,1] row_mask:0xf bank_mask:0xf\n" \
"v_add_f32_dpp v67, v67, v67 quad_perm:[2,3,0,1] row_mask:0xf bank_mask:0xf\n" \
"v_add_f32_dpp v64, v64, v64 row_half_mirror row_mask:0xf bank_mask:0xf\n" \
"v_add_f32_dpp v65, v65, v65 row_half_mirror row_mask:0xf bank_mask:0xf\n" \
"v_add_f32_dpp v66, v66, v66 row_half_mirror row_mask:0xf bank_mask:0xf\n" \
"v_add_f32_dpp v67, v67, v67 row_half_mirror row_mask:0xf bank_mask:0xf\n" \
"v_add_f32_dpp v64, v64, v64 row_mirror row_mask:0xf bank_mask:0xf\n" \
"v_add_f32_dpp v65, v65, v65 row_mirror row_mask:0xf bank_mask:0xf\n" \
"v_add_f32_dpp v66, v66, v66 row_mirror row_mask:0xf bank_mask:0xf\n" \
"v_add_f32_dpp v67, v67, v67 row_mirror row_mask:0xf bank_mask:0xf\n" \
"ds_write_b128 v180, v[64:67]\n" \
"s_waitcnt lgkmcnt(0)\n" \
"s_barrier\n" \
"ds_read2_b32 v[64:65], v181 offset0:0 offset1:8\n" \
"ds_read2_b32 v[66:67], v181 offset0:16 offset1:24\n" \
"ds_read2_b32 v[68:69], v181 offset0:32 offset1:40\n" \
"ds_read2_b32 v[70:71], v181 offset0:48 offset1:56\n" \
"s_waitcnt lgkmcnt(0)\n" \
"v_pk_add_f32 v[64:65], v[64:65], v[66:67]\n" \
"v_pk_add_f32 v[68:69], v[68:69], v[70:71]\n" \
"v_pk_add_f32 v[64:65], v[64:65], v[68:69]\n" \
"v_cvt_pk_bf16_f32 v66, v64, v65\n" \
"v_lshlrev_b32 v64, 16, v66\n" \
"v_and_b32 v65, 0xffff0000, v66\n" \
"v_mul_f32 v66, 0xbfb8aa3b, v64\n" \
"v_exp_f32 v66, v66\n" \
"s_nop 0\n" \
"v_add_f32 v66, 1.0, v66\n" \
"v_rcp_f32 v66, v66\n" \
"s_nop 0\n" \
"v_mul_f32 v64, v64, v66\n" \
"v_cvt_pk_bf16_f32 v64, v64, v64\n" \
"v_lshlrev_b32 v64, 16, v64\n" \
"v_mul_f32 v64, v64, v65\n" \
"v_cvt_pk_bf16_f32 v64, v64, v64\n" \
"v_lshlrev_b32 v64, 16, v64\n" \
"s_nop 0\n" \
"v_readlane_b32 s16, v64, 0\n" \
"v_readlane_b32 s17, v64, 1\n" \
"v_readlane_b32 s18, v64, 2\n" \
"v_readlane_b32 s19, v64, 3\n" \
"v_readlane_b32 s20, v64, 4\n" \
"v_readlane_b32 s21, v64, 5\n" \
"v_readlane_b32 s22, v64, 6\n" \
"v_readlane_b32 s23, v64, 7\n" \
"v_mul_f32 v66, s16, v16\n" \
"v_mul_f32 v67, s16, v17\n" \
"v_mul_f32 v68, s17, v18\n" \
"v_mul_f32 v69, s17, v19\n" \
"v_fmac_f32 v66, s18, v20\n" \
"v_fmac_f32 v67, s18, v21\n" \
"v_fmac_f32 v68, s19, v22\n" \
"v_fmac_f32 v69, s19, v23\n" \
"v_fmac_f32 v66, s20, v24\n" \
"v_fmac_f32 v67, s20, v25\n" \
"v_fmac_f32 v68, s21, v26\n" \
"v_fmac_f32 v69, s21, v27\n" \
"v_fmac_f32 v66, s22, v28\n" \
"v_fmac_f32 v67, s22, v29\n" \
"v_fmac_f32 v68, s23, v30\n" \
"v_fmac_f32 v69, s23, v31\n" \
"v_pk_add_f32 v[66:67], v[66:67], v[68:69]\n" \
"v_min_u32 v66, -2, v66\n" \
"v_min_u32 v67, -2, v67\n" \
"buffer_store_dwordx2 v[66:67], v190, s[32:35], s41 offen sc1\n" \
  \
"s_cmp_ge_u32 %[bid], 64\n" \
"s_cbranch_scc1 .Lend_%=\n" \
"s_mov_b32 s82, 0x100000\n" \
"s_sleep 14\n" \
".Lfp_%=:\n" \
"buffer_load_dwordx4 v[64:67], v182, s[32:35], s37 offen sc1\n" \
"buffer_load_dwordx4 v[68:71], v182, s[32:35], s29 offen sc1\n" \
"s_waitcnt vmcnt(0)\n" \
"v_max3_u32 v72, v64, v65, v66\n" \
"v_max3_u32 v73, v67, v68, v69\n" \
"v_max3_u32 v72, v72, v70, v71\n" \
"v_max_u32 v72, v72, v73\n" \
"v_cmp_eq_u32 vcc, -1, v72\n" \
"v_pk_add_f32 v[64:65], v[64:65], v[68:69]\n" \
"v_pk_add_f32 v[66:67], v[66:67], v[70:71]\n" \
"s_nop 0\n" \
"s_cbranch_vccnz .Lfr_%=\n" \
".Lfb_%=:\n" \
"v_permlane32_swap_b32 v64, v66\n" \
"v_permlane32_swap_b32 v65, v67\n" \
"v_pk_add_f32 v[64:65], v[64:65], v[66:67]\n" \
"s_nop 1\n" \
"v_permlane16_swap_b32 v64, v65\n" \
"v_add_f32 v64, v64, v65\n" \
"s_nop 1\n" \
"v_add_f32_dpp v64, v64, v64 quad_perm:[2,3,0,1] row_mask:0xf bank_mask:0xf\n" \
"s_nop 1\n" \
"v_add_f32_dpp v64, v64, v64 row_ror:4 row_mask:0xf bank_mask:0xf\n" \
"s_nop 1\n" \
"v_add_f32_dpp v64, v64, v64 row_ror:8 row_mask:0xf bank_mask:0xf\n" \
"ds_write_b32 v183, v64\n" \
"s_waitcnt lgkmcnt(0)\n" \
"s_barrier\n" \
"ds_read2_b64 v[64:67], v238 offset0:0 offset1:4\n" \
"ds_read2_b64 v[68:71], v238 offset0:8 offset1:12\n" \
"s_waitcnt lgkmcnt(0)\n" \
"v_pk_add_f32 v[64:65], v[64:65], v[66:67]\n" \
"v_pk_add_f32 v[68:69], v[68:69], v[70:71]\n" \
"v_pk_add_f32 v[64:65], v[64:65], v[68:69]\n" \
"v_cvt_pk_bf16_f32 v46, v64, v65\n" \
"v_lshlrev_b32 v64, 16, v46\n" \
"v_and_b32 v65, 0xffff0000, v46\n" \
"v_pk_mul_f32 v[64:65], v[64:65], v[90:91] op_sel_hi:[1,0]\n" \
"v_cvt_pk_bf16_f32 v46, v64, v65\n" \
"v_lshlrev_b32 v64, 16, v46\n" \
"v_and_b32 v65, 0xffff0000, v46\n" \
"v_pk_add_f32 v[64:65], v[64:65], v[62:63]\n" \
"v_cvt_pk_bf16_f32 v46, v64, v65\n" \
"v_lshlrev_b32 v64, 16, v46\n" \
"v_and_b32 v65, 0xffff0000, v46\n" \
"v_pk_mul_f32 v[64:65], v[64:65], v[178:179]\n" \
"v_cvt_pk_bf16_f32 v46, v64, v65\n" \
"v_lshlrev_b32 v64, 16, v46\n" \
"v_and_b32 v65, 0xffff0000, v46\n" \
"v_pk_add_f32 v[64:65], v[64:65], v[60:61]\n" \
"v_cvt_pk_bf16_f32 v64, v64, v65\n" \
"buffer_store_dword v64, v237, s[52:55], s30 offen sc1\n" \
"s_branch .Lend_%=\n" \
".Lrare_%=:\n" \
  \
".Ldhi_%=:\n" \
".Lst0_14_%=:\n" \
"s_cbranch_execz .Lstb0_%=\n" \
"v_mfma_f32_16x16x32_bf16 v[128:131], a[128:131], v[192:195], 0\n" \
"s_add_u32 s85, s85, s67\n" \
"v_xor_b32 v249, 0x4000, v249\n" \
"v_mfma_f32_16x16x32_bf16 v[128:131], a[132:135], v[196:199], v[128:131]\n" \
"s_barrier\n" \
"s_lshl2_add_u32 s85, s85, %[wid]\n" \
"v_mfma_f32_16x16x32_bf16 v[128:131], a[136:139], v[200:203], v[128:131]\n" \
"s_lshl_b32 s85, s85, 2\n" \
"ds_read_b128 a[224:227], v242 offset:8192\n" \
"v_cvt_pk_bf16_f32 v172, v88, v89\n" \
"v_mfma_f32_16x16x32_bf16 v[128:131], a[140:143], v[204:207], v[128:131]\n" \
"s_add_u32 s85, s85, 82048\n" \
"ds_read_b128 a[192:195], v242 offset:0\n" \
"v_cvt_pk_bf16_f32 v173, v90, v91\n" \
"v_mfma_f32_16x16x32_bf16 v[132:135], a[156:159], v[204:207], 0\n" \
"s_waitcnt vmcnt(1)\n" \
"buffer_store_dword v250, off, s[32:35], s85 sc1\n" \
"v_cvt_pk_bf16_f32 v174, v92, v93\n" \
"v_mfma_f32_16x16x32_bf16 v[132:135], a[152:155], v[200:203], v[132:135]\n" \
"ds_read_b128 a[196:199], v242 offset:1024\n" \
"ds_read_b128 a[228:231], v242 offset:9216\n" \
"v_mfma_f32_16x16x32_bf16 v[132:135], a[148:151], v[196:199], v[132:135]\n" \
"v_exp_f32 v128, v128\n" \
"v_mfma_f32_16x16x32_bf16 v[132:135], a[144:147], v[192:195], v[132:135]\n" \
"s_add_u32 s85, s94, 589824\n" \
"v_exp_f32 v129, v129\n" \
"v_mfma_f32_16x16x32_bf16 v[136:139], a[160:163], v[192:195], v[224:227]\n" \
"buffer_store_dwordx4 a[64:67], v248, s[32:35], s85 offen offset:0 sc1\n" \
"v_mfma_f32_16x16x32_bf16 v[136:139], a[164:167], v[196:199], v[136:139]\n" \
"v_exp_f32 v130, v130\n" \
"v_mfma_f32_16x16x32_bf16 v[136:139], a[168:171], v[200:203], v[136:139]\n" \
"v_exp_f32 v131, v131\n" \
"v_mfma_f32_16x16x32_bf16 v[136:139], a[172:175], v[204:207], v[136:139]\n" \
"v_cvt_pk_bf16_f32 v128, v128, v129\n" \
"v_cvt_pk_bf16_f32 v129, v130, v131\n" \
"v_mfma_f32_16x16x32_bf16 v[140:143], a[188:191], v[204:207], v[228:231]\n" \
"v_exp_f32 v132, v132\n" \
"v_mfma_f32_16x16x32_bf16 v[140:143], a[184:187], v[200:203], v[140:143]\n" \
"v_exp_f32 v133, v133\n" \
"v_mfma_f32_16x16x32_bf16 v[140:143], a[180:183], v[196:199], v[140:143]\n" \
"v_exp_f32 v134, v134\n" \
"v_mfma_f32_16x16x32_bf16 v[140:143], a[176:179], v[192:195], v[140:143]\n" \
"ds_read_b128 a[232:235], v242 offset:10240\n" \
"v_cvt_pk_bf16_f32 v130, v132, v133\n" \
"v_mfma_f32_16x16x32_bf16 v[144:147], a[128:131], v[208:211], 0\n" \
"v_exp_f32 v135, v135\n" \
"v_mfma_f32_16x16x32_bf16 v[144:147], a[132:135], v[212:215], v[144:147]\n" \
"v_exp_f32 v136, v136\n" \
"v_mfma_f32_16x16x32_bf16 v[144:147], a[136:139], v[216:219], v[144:147]\n" \
"ds_read_b128 a[200:203], v242 offset:2048\n" \
"v_cvt_pk_bf16_f32 v131, v134, v135\n" \
"v_mfma_f32_16x16x32_bf16 v[144:147], a[140:143], v[220:223], v[144:147]\n" \
"v_exp_f32 v137, v137\n" \
"v_mfma_f32_16x16x32_bf16 v[148:151], a[156:159], v[220:223], 0\n" \
"v_exp_f32 v138, v138\n" \
"v_mfma_f32_16x16x32_bf16 v[148:151], a[152:155], v[216:219], v[148:151]\n" \
"v_exp_f32 v139, v139\n" \
"v_mfma_f32_16x16x32_bf16 v[148:151], a[148:151], v[212:215], v[148:151]\n" \
"v_cvt_pk_bf16_f32 v136, v136, v137\n" \
"v_cvt_pk_bf16_f32 v137, v138, v139\n" \
"v_mfma_f32_16x16x32_bf16 v[148:151], a[144:147], v[208:211], v[148:151]\n" \
"v_exp_f32 v140, v140\n" \
"v_mfma_f32_16x16x32_bf16 v[152:155], a[160:163], v[208:211], 0\n" \
"v_exp_f32 v141, v141\n" \
"v_mfma_f32_16x16x32_bf16 v[152:155], a[164:167], v[212:215], v[152:155]\n" \
"s_add_u32 s85, s94, 589824\n" \
"ds_read_b128 a[204:207], v242 offset:3072\n" \
"v_cvt_pk_bf16_f32 v138, v140, v141\n" \
"v_mfma_f32_16x16x32_bf16 v[152:155], a[168:171], v[216:219], v[152:155]\n" \
"v_exp_f32 v142, v142\n" \
"v_mfma_f32_16x16x32_bf16 v[152:155], a[172:175], v[220:223], v[152:155]\n" \
"v_exp_f32 v143, v143\n" \
"v_mfma_f32_16x16x32_bf16 v[156:159], a[188:191], v[220:223], 0\n" \
"buffer_store_dwordx4 a[72:75], v248, s[32:35], s85 offen offset:1024 sc1\n" \
"v_mfma_f32_16x16x32_bf16 v[156:159], a[184:187], v[216:219], v[156:159]\n" \
"ds_read_b128 a[236:239], v242 offset:11264\n" \
"v_cvt_pk_bf16_f32 v139, v142, v143\n" \
"v_mfma_f32_16x16x32_bf16 v[156:159], a[180:183], v[212:215], v[156:159]\n" \
"v_exp_f32 v144, v144\n" \
"v_mfma_f32_16x16x32_bf16 v[156:159], a[176:179], v[208:211], v[156:159]\n" \
"ds_read_b128 a[240:243], v242 offset:12288\n" \
"ds_read_b128 a[208:211], v242 offset:4096\n" \
"v_mfma_f32_16x16x32_bf16 a[32:35], a[48:51], v[128:131], 0\n" \
"v_exp_f32 v145, v145\n" \
"v_mfma_f32_16x16x32_bf16 a[32:35], a[48:51], v[136:139], a[32:35]\n" \
"s_waitcnt lgkmcnt(6)\n" \
"ds_read_b128 a[212:215], v242 offset:5120\n" \
"v_cvt_pk_bf16_f32 v144, v144, v145\n" \
"v_mfma_f32_16x16x32_bf16 v[0:3], a[224:227], v[136:139], 0\n" \
"v_exp_f32 v146, v146\n" \
"v_cvt_pk_bf16_f32 v175, v94, v95\n" \
"v_mfma_f32_16x16x32_bf16 v[0:3], a[192:195], v[128:131], v[0:3]\n" \
"ds_read_b128 a[244:247], v242 offset:13312\n" \
"ds_read_b128 a[248:251], v242 offset:14336\n" \
"v_mfma_f32_16x16x32_bf16 v[4:7], a[196:199], v[128:131], 0\n" \
"v_exp_f32 v147, v147\n" \
"v_mfma_f32_16x16x32_bf16 v[4:7], a[228:231], v[136:139], v[4:7]\n" \
"s_waitcnt lgkmcnt(5)\n" \
"ds_read_b128 a[216:219], v242 offset:6144\n" \
"v_cvt_pk_bf16_f32 v145, v146, v147\n" \
"v_mfma_f32_16x16x32_bf16 v[8:11], a[232:235], v[136:139], 0\n" \
"v_exp_f32 v148, v148\n" \
"v_mfma_f32_16x16x32_bf16 v[8:11], a[200:203], v[128:131], v[8:11]\n" \
"ds_read_b128 a[220:223], v242 offset:7168\n" \
"ds_read_b128 a[252:255], v242 offset:15360\n" \
"v_mfma_f32_16x16x32_bf16 v[12:15], a[204:207], v[128:131], 0\n" \
"v_exp_f32 v149, v149\n" \
"v_mfma_f32_16x16x32_bf16 v[12:15], a[236:239], v[136:139], v[12:15]\n" \
"s_waitcnt lgkmcnt(4)\n" \
"v_cvt_pk_bf16_f32 v146, v148, v149\n" \
"v_cvt_pk_bf16_f32 v176, v96, v97\n" \
"v_mfma_f32_16x16x32_bf16 v[16:19], a[240:243], v[136:139], 0\n" \
"v_exp_f32 v150, v150\n" \
"v_mfma_f32_16x16x32_bf16 v[16:19], a[208:211], v[128:131], v[16:19]\n" \
"s_add_u32 s85, s94, 589824\n" \
"v_cvt_pk_bf16_f32 v177, v98, v99\n" \
"v_cvt_pk_bf16_f32 v178, v100, v101\n" \
"v_mfma_f32_16x16x32_bf16 v[20:23], a[212:215], v[128:131], 0\n" \
"v_exp_f32 v151, v151\n" \
"v_mfma_f32_16x16x32_bf16 v[20:23], a[244:247], v[136:139], v[20:23]\n" \
"s_waitcnt lgkmcnt(0)\n" \
"buffer_store_dwordx4 a[80:83], v248, s[32:35], s85 offen offset:2048 sc1\n" \
"v_mfma_f32_16x16x32_bf16 v[24:27], a[248:251], v[136:139], 0\n" \
"v_cvt_pk_bf16_f32 v147, v150, v151\n" \
"v_cvt_pk_bf16_f32 v179, v102, v103\n" \
"v_mfma_f32_16x16x32_bf16 v[24:27], a[216:219], v[128:131], v[24:27]\n" \
"v_exp_f32 v152, v152\n" \
"v_mfma_f32_16x16x32_bf16 v[28:31], a[220:223], v[128:131], 0\n" \
"v_cvt_pk_bf16_f32 v180, v104, v105\n" \
"v_cvt_pk_bf16_f32 v181, v106, v107\n" \
"v_mfma_f32_16x16x32_bf16 v[28:31], a[252:255], v[136:139], v[28:31]\n" \
"v_exp_f32 v153, v153\n" \
"v_mfma_f32_16x16x32_bf16 v[128:131], a[128:131], v[160:163], 0\n" \
"ds_write_b128 v249, a[64:67] offset:32768\n" \
"v_cvt_pk_bf16_f32 v152, v152, v153\n" \
"v_mfma_f32_16x16x32_bf16 v[128:131], a[132:135], v[164:167], v[128:131]\n" \
"v_exp_f32 v154, v154\n" \
"v_mfma_f32_16x16x32_bf16 v[128:131], a[136:139], v[168:171], v[128:131]\n" \
"v_cvt_pk_bf16_f32 v182, v108, v109\n" \
"v_cvt_pk_bf16_f32 v183, v110, v111\n" \
"v_mfma_f32_16x16x32_bf16 v[128:131], a[140:143], v[172:175], v[128:131]\n" \
"v_exp_f32 v155, v155\n" \
"v_mfma_f32_16x16x32_bf16 v[132:135], a[156:159], v[172:175], 0\n" \
"v_cvt_pk_bf16_f32 v153, v154, v155\n" \
"v_cvt_pk_bf16_f32 v184, v112, v113\n" \
"v_mfma_f32_16x16x32_bf16 v[132:135], a[152:155], v[168:171], v[132:135]\n" \
"v_exp_f32 v156, v156\n" \
"v_mfma_f32_16x16x32_bf16 v[132:135], a[148:151], v[164:167], v[132:135]\n" \
"v_cvt_pk_bf16_f32 v185, v114, v115\n" \
"v_cvt_pk_bf16_f32 v186, v116, v117\n" \
"v_mfma_f32_16x16x32_bf16 v[132:135], a[144:147], v[160:163], v[132:135]\n" \
"v_exp_f32 v157, v157\n" \
"v_mfma_f32_16x16x32_bf16 v[136:139], a[160:163], v[160:163], 0\n" \
"v_cvt_pk_bf16_f32 v154, v156, v157\n" \
"v_cvt_pk_bf16_f32 v187, v118, v119\n" \
"v_mfma_f32_16x16x32_bf16 v[136:139], a[164:167], v[164:167], v[136:139]\n" \
"v_exp_f32 v158, v158\n" \
"v_mfma_f32_16x16x32_bf16 v[136:139], a[168:171], v[168:171], v[136:139]\n" \
"v_cvt_pk_bf16_f32 v188, v120, v121\n" \
"v_cvt_pk_bf16_f32 v189, v122, v123\n" \
"v_mfma_f32_16x16x32_bf16 v[136:139], a[172:175], v[172:175], v[136:139]\n" \
"s_add_u32 s85, s94, 589824\n" \
"v_exp_f32 v159, v159\n" \
"v_mfma_f32_16x16x32_bf16 v[140:143], a[188:191], v[172:175], 0\n" \
"buffer_store_dwordx4 a[88:91], v248, s[32:35], s85 offen offset:3072 sc1\n" \
"v_mfma_f32_16x16x32_bf16 v[140:143], a[184:187], v[168:171], v[140:143]\n" \
"ds_write_b128 v249, a[72:75] offset:33792\n" \
"v_cvt_pk_bf16_f32 v155, v158, v159\n" \
"v_mfma_f32_16x16x32_bf16 v[140:143], a[180:183], v[164:167], v[140:143]\n" \
"v_exp_f32 v128, v128\n" \
"v_mfma_f32_16x16x32_bf16 v[140:143], a[176:179], v[160:163], v[140:143]\n" \
"v_cvt_pk_bf16_f32 v190, v124, v125\n" \
"v_cvt_pk_bf16_f32 v191, v126, v127\n" \
"v_mfma_f32_16x16x32_bf16 a[36:39], a[48:51], v[144:147], 0\n" \
"v_exp_f32 v129, v129\n" \
"v_mfma_f32_16x16x32_bf16 a[36:39], a[48:51], v[152:155], a[36:39]\n" \
"v_cvt_pk_bf16_f32 v128, v128, v129\n" \
"v_mfma_f32_16x16x32_bf16 v[32:35], a[224:227], v[152:155], 0\n" \
"v_exp_f32 v130, v130\n" \
"v_mfma_f32_16x16x32_bf16 v[32:35], a[192:195], v[144:147], v[32:35]\n" \
"v_mfma_f32_16x16x32_bf16 v[36:39], a[196:199], v[144:147], 0\n" \
"v_exp_f32 v131, v131\n" \
"v_mfma_f32_16x16x32_bf16 v[36:39], a[228:231], v[152:155], v[36:39]\n" \
"v_cvt_pk_bf16_f32 v129, v130, v131\n" \
"v_mfma_f32_16x16x32_bf16 v[40:43], a[232:235], v[152:155], 0\n" \
"v_exp_f32 v132, v132\n" \
"v_mfma_f32_16x16x32_bf16 v[40:43], a[200:203], v[144:147], v[40:43]\n" \
"v_mfma_f32_16x16x32_bf16 v[44:47], a[204:207], v[144:147], 0\n" \
"v_exp_f32 v133, v133\n" \
"v_mfma_f32_16x16x32_bf16 v[44:47], a[236:239], v[152:155], v[44:47]\n" \
"v_cvt_pk_bf16_f32 v130, v132, v133\n" \
"v_mfma_f32_16x16x32_bf16 v[48:51], a[240:243], v[152:155], 0\n" \
"v_exp_f32 v134, v134\n" \
"v_mfma_f32_16x16x32_bf16 v[48:51], a[208:211], v[144:147], v[48:51]\n" \
"ds_write_b128 v249, a[80:83] offset:34816\n" \
"v_mfma_f32_16x16x32_bf16 v[52:55], a[212:215], v[144:147], 0\n" \
"v_exp_f32 v135, v135\n" \
"v_mfma_f32_16x16x32_bf16 v[52:55], a[244:247], v[152:155], v[52:55]\n" \
"s_add_u32 s85, s94, 593920\n" \
"v_cvt_pk_bf16_f32 v131, v134, v135\n" \
"v_mfma_f32_16x16x32_bf16 v[56:59], a[248:251], v[152:155], 0\n" \
"buffer_store_dwordx4 a[96:99], v248, s[32:35], s85 offen offset:0 sc1\n" \
"v_mfma_f32_16x16x32_bf16 v[56:59], a[216:219], v[144:147], v[56:59]\n" \
"v_exp_f32 v136, v136\n" \
"v_mfma_f32_16x16x32_bf16 v[60:63], a[220:223], v[144:147], 0\n" \
"v_mfma_f32_16x16x32_bf16 v[60:63], a[252:255], v[152:155], v[60:63]\n" \
"v_exp_f32 v137, v137\n" \
"v_mfma_f32_16x16x32_bf16 v[144:147], a[128:131], v[176:179], 0\n" \
"v_cvt_pk_bf16_f32 v136, v136, v137\n" \
"v_mfma_f32_16x16x32_bf16 v[144:147], a[132:135], v[180:183], v[144:147]\n" \
"v_exp_f32 v138, v138\n" \
"v_mfma_f32_16x16x32_bf16 v[144:147], a[136:139], v[184:187], v[144:147]\n" \
"ds_read_b128 a[128:131], v248 offset:16384\n" \
"ds_read_b128 a[132:135], v248 offset:17408\n" \
"v_mfma_f32_16x16x32_bf16 v[144:147], a[140:143], v[188:191], v[144:147]\n" \
"v_exp_f32 v139, v139\n" \
"v_mfma_f32_16x16x32_bf16 v[148:151], a[156:159], v[188:191], 0\n" \
"ds_read_b128 a[136:139], v248 offset:18432\n" \
"v_cvt_pk_bf16_f32 v137, v138, v139\n" \
"v_mfma_f32_16x16x32_bf16 v[148:151], a[152:155], v[184:187], v[148:151]\n" \
"v_exp_f32 v140, v140\n" \
"v_mfma_f32_16x16x32_bf16 v[148:151], a[148:151], v[180:183], v[148:151]\n" \
"ds_write_b128 v249, a[88:91] offset:35840\n" \
"v_mfma_f32_16x16x32_bf16 v[148:151], a[144:147], v[176:179], v[148:151]\n" \
"v_exp_f32 v141, v141\n" \
"v_mfma_f32_16x16x32_bf16 v[152:155], a[160:163], v[176:179], 0\n" \
"ds_read_b128 a[140:143], v248 offset:19456\n" \
"v_cvt_pk_bf16_f32 v138, v140, v141\n" \
"v_mfma_f32_16x16x32_bf16 v[152:155], a[164:167], v[180:183], v[152:155]\n" \
"v_exp_f32 v142, v142\n" \
"v_mfma_f32_16x16x32_bf16 v[152:155], a[168:171], v[184:187], v[152:155]\n" \
"ds_read_b128 a[156:159], v248 offset:23552\n" \
"ds_read_b128 a[152:155], v248 offset:22528\n" \
"v_mfma_f32_16x16x32_bf16 v[152:155], a[172:175], v[188:191], v[152:155]\n" \
"v_exp_f32 v143, v143\n" \
"v_mfma_f32_16x16x32_bf16 v[156:159], a[188:191], v[188:191], 0\n" \
"v_exp_f32 v144, v144\n" \
"v_mfma_f32_16x16x32_bf16 v[156:159], a[184:187], v[184:187], v[156:159]\n" \
"s_add_u32 s85, s94, 593920\n" \
"v_exp_f32 v145, v145\n" \
"v_mfma_f32_16x16x32_bf16 v[156:159], a[180:183], v[180:183], v[156:159]\n" \
"v_exp_f32 v146, v146\n" \
"v_mfma_f32_16x16x32_bf16 v[156:159], a[176:179], v[176:179], v[156:159]\n" \
"v_exp_f32 v147, v147\n" \
"v_cvt_pk_bf16_f32 v139, v142, v143\n" \
"v_mfma_f32_16x16x32_bf16 a[40:43], a[48:51], v[128:131], 0\n" \
"v_exp_f32 v148, v148\n" \
"v_mfma_f32_16x16x32_bf16 a[40:43], a[48:51], v[136:139], a[40:43]\n" \
"v_exp_f32 v149, v149\n" \
"v_mfma_f32_16x16x32_bf16 v[64:67], a[224:227], v[136:139], 0\n" \
"v_exp_f32 v150, v150\n" \
"v_mfma_f32_16x16x32_bf16 v[64:67], a[192:195], v[128:131], v[64:67]\n" \
"buffer_store_dwordx4 a[104:107], v248, s[32:35], s85 offen offset:1024 sc1\n" \
"v_mfma_f32_16x16x32_bf16 v[68:71], a[196:199], v[128:131], 0\n" \
"v_exp_f32 v151, v151\n" \
"v_mfma_f32_16x16x32_bf16 v[68:71], a[228:231], v[136:139], v[68:71]\n" \
"buffer_load_dword v253, v252, s[32:35], s81 offen sc1\n" \
"v_mov_b32 v240, v248\n" \
"v_mfma_f32_16x16x32_bf16 v[72:75], a[232:235], v[136:139], 0\n" \
"ds_write_b128 v249, a[96:99] offset:36864\n" \
"v_cvt_pk_bf16_f32 v144, v144, v145\n" \
"v_mfma_f32_16x16x32_bf16 v[72:75], a[200:203], v[128:131], v[72:75]\n" \
"v_exp_f32 v152, v152\n" \
"v_mfma_f32_16x16x32_bf16 v[76:79], a[204:207], v[128:131], 0\n" \
"v_cvt_pk_bf16_f32 v145, v146, v147\n" \
"v_cvt_pk_bf16_f32 v146, v148, v149\n" \
"v_mfma_f32_16x16x32_bf16 v[76:79], a[236:239], v[136:139], v[76:79]\n" \
"v_exp_f32 v153, v153\n" \
"v_mfma_f32_16x16x32_bf16 v[80:83], a[240:243], v[136:139], 0\n" \
"v_cvt_pk_bf16_f32 v147, v150, v151\n" \
"v_cvt_pk_bf16_f32 v152, v152, v153\n" \
"v_mfma_f32_16x16x32_bf16 v[80:83], a[208:211], v[128:131], v[80:83]\n" \
".Lst0_15_%=:\n" \
"s_cbranch_execz .Lstb0_%=\n" \
"v_exp_f32 v154, v154\n" \
"v_mfma_f32_16x16x32_bf16 v[84:87], a[212:215], v[128:131], 0\n" \
"ds_read_b128 a[148:151], v248 offset:21504\n" \
"ds_read_b128 a[144:147], v248 offset:20480\n" \
"v_mfma_f32_16x16x32_bf16 v[84:87], a[244:247], v[136:139], v[84:87]\n" \
"v_exp_f32 v155, v155\n" \
"v_mfma_f32_16x16x32_bf16 v[88:91], a[248:251], v[136:139], 0\n" \
"ds_read_b128 a[160:163], v248 offset:24576\n" \
"v_cvt_pk_bf16_f32 v153, v154, v155\n" \
"v_mfma_f32_16x16x32_bf16 v[88:91], a[216:219], v[128:131], v[88:91]\n" \
"s_add_u32 s85, s94, 593920\n" \
"v_exp_f32 v156, v156\n" \
"v_mfma_f32_16x16x32_bf16 v[92:95], a[220:223], v[128:131], 0\n" \
"buffer_store_dwordx4 a[112:115], v248, s[32:35], s85 offen offset:2048 sc1\n" \
"v_mfma_f32_16x16x32_bf16 v[92:95], a[252:255], v[136:139], v[92:95]\n" \
"s_waitcnt lgkmcnt(6)\n" \
"v_exp_f32 v157, v157\n" \
"v_mfma_f32_16x16x32_bf16 v[128:131], a[128:131], v[208:211], v[232:235]\n" \
"ds_write_b128 v249, a[104:107] offset:37888\n" \
"v_cvt_pk_bf16_f32 v154, v156, v157\n" \
"v_mfma_f32_16x16x32_bf16 v[128:131], a[132:135], v[212:215], v[128:131]\n" \
"v_exp_f32 v158, v158\n" \
"v_mfma_f32_16x16x32_bf16 v[128:131], a[136:139], v[216:219], v[128:131]\n" \
"ds_read_b128 a[164:167], v248 offset:25600\n" \
"ds_read_b128 a[168:171], v248 offset:26624\n" \
"v_mfma_f32_16x16x32_bf16 v[128:131], a[140:143], v[220:223], v[128:131]\n" \
"s_waitcnt lgkmcnt(4)\n" \
"v_exp_f32 v159, v159\n" \
"v_mfma_f32_16x16x32_bf16 v[132:135], a[156:159], v[220:223], v[236:239]\n" \
"ds_read_b128 a[172:175], v248 offset:27648\n" \
"v_cvt_pk_bf16_f32 v155, v158, v159\n" \
"v_mfma_f32_16x16x32_bf16 v[132:135], a[152:155], v[216:219], v[132:135]\n" \
"ds_read_b128 a[188:191], v248 offset:31744\n" \
"ds_read_b128 a[184:187], v248 offset:30720\n" \
"v_mfma_f32_16x16x32_bf16 v[132:135], a[148:151], v[212:215], v[132:135]\n" \
"v_exp_f32 v128, v128\n" \
"v_mfma_f32_16x16x32_bf16 v[132:135], a[144:147], v[208:211], v[132:135]\n" \
"ds_read_b128 a[180:183], v248 offset:29696\n" \
"ds_read_b128 a[176:179], v248 offset:28672\n" \
"v_mfma_f32_16x16x32_bf16 a[44:47], a[48:51], v[144:147], 0\n" \
"v_mfma_f32_16x16x32_bf16 a[44:47], a[48:51], v[152:155], a[44:47]\n" \
"v_mfma_f32_16x16x32_bf16 v[96:99], a[224:227], v[152:155], 0\n" \
"v_exp_f32 v129, v129\n" \
"v_mfma_f32_16x16x32_bf16 v[96:99], a[192:195], v[144:147], v[96:99]\n" \
"ds_write_b128 v249, a[112:115] offset:38912\n" \
"v_cvt_pk_bf16_f32 v128, v128, v129\n" \
"v_mfma_f32_16x16x32_bf16 v[100:103], a[196:199], v[144:147], 0\n" \
"ds_read_b128 a[192:195], v242 offset:16384\n" \
"v_mfma_f32_16x16x32_bf16 v[100:103], a[228:231], v[152:155], v[100:103]\n" \
"ds_read_b128 a[196:199], v242 offset:17408\n" \
"v_mfma_f32_16x16x32_bf16 v[104:107], a[232:235], v[152:155], 0\n" \
"v_mfma_f32_16x16x32_bf16 v[104:107], a[200:203], v[144:147], v[104:107]\n" \
"s_add_u32 s85, s94, 593920\n" \
"v_exp_f32 v130, v130\n" \
"v_mfma_f32_16x16x32_bf16 v[108:111], a[204:207], v[144:147], 0\n" \
"buffer_store_dwordx4 a[120:123], v248, s[32:35], s85 offen offset:3072 sc1\n" \
"v_mfma_f32_16x16x32_bf16 v[108:111], a[236:239], v[152:155], v[108:111]\n" \
"ds_read_b128 a[200:203], v242 offset:18432\n" \
"ds_read_b128 a[204:207], v242 offset:19456\n" \
"v_mfma_f32_16x16x32_bf16 v[112:115], a[240:243], v[152:155], 0\n" \
"v_mfma_f32_16x16x32_bf16 v[112:115], a[208:211], v[144:147], v[112:115]\n" \
"v_exp_f32 v131, v131\n" \
"v_mfma_f32_16x16x32_bf16 v[116:119], a[212:215], v[144:147], 0\n" \
"ds_read_b128 a[208:211], v242 offset:20480\n" \
"v_cvt_pk_bf16_f32 v129, v130, v131\n" \
"v_mfma_f32_16x16x32_bf16 v[116:119], a[244:247], v[152:155], v[116:119]\n" \
"ds_read_b128 a[212:215], v242 offset:21504\n" \
"v_mfma_f32_16x16x32_bf16 v[120:123], a[248:251], v[152:155], 0\n" \
"v_mfma_f32_16x16x32_bf16 v[120:123], a[216:219], v[144:147], v[120:123]\n" \
"ds_write_b128 v249, a[120:123] offset:39936\n" \
"v_mfma_f32_16x16x32_bf16 v[124:127], a[220:223], v[144:147], 0\n" \
"v_exp_f32 v132, v132\n" \
"v_mfma_f32_16x16x32_bf16 v[124:127], a[252:255], v[152:155], v[124:127]\n" \
"ds_read_b128 a[216:219], v242 offset:22528\n" \
"ds_read_b128 a[220:223], v242 offset:23552\n" \
"v_mfma_f32_16x16x32_bf16 v[144:147], a[128:131], v[160:163], 0\n" \
"ds_read_b128 a[224:227], v242 offset:24576\n" \
"ds_read_b128 a[228:231], v242 offset:25600\n" \
"v_mfma_f32_16x16x32_bf16 v[144:147], a[132:135], v[164:167], v[144:147]\n" \
"ds_read_b128 a[232:235], v242 offset:26624\n" \
"v_mfma_f32_16x16x32_bf16 v[144:147], a[136:139], v[168:171], v[144:147]\n" \
"v_exp_f32 v133, v133\n" \
"v_mfma_f32_16x16x32_bf16 v[144:147], a[140:143], v[172:175], v[144:147]\n" \
"ds_read_b128 a[236:239], v242 offset:27648\n" \
"v_cvt_pk_bf16_f32 v130, v132, v133\n" \
"v_mfma_f32_16x16x32_bf16 v[148:151], a[156:159], v[172:175], 0\n" \
"ds_read_b128 a[240:243], v242 offset:28672\n" \
"v_mfma_f32_16x16x32_bf16 v[148:151], a[152:155], v[168:171], v[148:151]\n" \
"ds_read_b128 a[244:247], v242 offset:29696\n" \
"v_mfma_f32_16x16x32_bf16 v[148:151], a[148:151], v[164:167], v[148:151]\n" \
"v_mfma_f32_16x16x32_bf16 v[148:151], a[144:147], v[160:163], v[148:151]\n" \
"s_waitcnt lgkmcnt(15)\n" \
"v_exp_f32 v134, v134\n" \
"v_mfma_f32_16x16x32_bf16 v[152:155], a[160:163], v[160:163], 0\n" \
"ds_read_b128 a[248:251], v242 offset:30720\n" \
"v_mfma_f32_16x16x32_bf16 v[152:155], a[164:167], v[164:167], v[152:155]\n" \
"ds_read_b128 a[252:255], v242 offset:31744\n" \
"v_mfma_f32_16x16x32_bf16 v[152:155], a[168:171], v[168:171], v[152:155]\n" \
"v_mfma_f32_16x16x32_bf16 v[152:155], a[172:175], v[172:175], v[152:155]\n" \
"v_exp_f32 v135, v135\n" \
"v_mfma_f32_16x16x32_bf16 v[156:159], a[188:191], v[172:175], 0\n" \
"v_exp_f32 v144, v144\n" \
"v_mfma_f32_16x16x32_bf16 v[156:159], a[184:187], v[168:171], v[156:159]\n" \
"v_exp_f32 v145, v145\n" \
"v_mfma_f32_16x16x32_bf16 v[156:159], a[180:183], v[164:167], v[156:159]\n" \
"v_exp_f32 v146, v146\n" \
"v_cvt_pk_bf16_f32 v131, v134, v135\n" \
"v_mfma_f32_16x16x32_bf16 v[156:159], a[176:179], v[160:163], v[156:159]\n" \
"v_exp_f32 v147, v147\n" \
"v_mfma_f32_16x16x32_bf16 a[36:39], a[48:51], v[128:131], a[36:39]\n" \
"s_waitcnt lgkmcnt(13)\n" \
"v_exp_f32 v148, v148\n" \
"v_mfma_f32_16x16x32_bf16 v[32:35], a[192:195], v[128:131], v[32:35]\n" \
"s_waitcnt vmcnt(2)\n" \
"v_cmp_eq_u32 vcc, 0, v253\n" \
"v_cvt_pk_bf16_f32 v144, v144, v145\n" \
"v_mfma_f32_16x16x32_bf16 v[36:39], a[196:199], v[128:131], v[36:39]\n" \
"v_exp_f32 v149, v149\n" \
"v_mfma_f32_16x16x32_bf16 v[40:43], a[200:203], v[128:131], v[40:43]\n" \
"v_exp_f32 v150, v150\n" \
"v_mfma_f32_16x16x32_bf16 v[44:47], a[204:207], v[128:131], v[44:47]\n" \
"s_waitcnt lgkmcnt(8)\n" \
"s_cbranch_vccnz .Lspw_%=\n" \
".Lsbw_%=:\n" \
"v_mfma_f32_16x16x32_bf16 v[48:51], a[208:211], v[128:131], v[48:51]\n" \
"s_mov_b32 m0, s19\n" \
"v_exp_f32 v151, v151\n" \
"v_mfma_f32_16x16x32_bf16 v[52:55], a[212:215], v[128:131], v[52:55]\n" \
"s_barrier\n" \
"v_exp_f32 v152, v152\n" \
"v_mfma_f32_16x16x32_bf16 v[56:59], a[216:219], v[128:131], v[56:59]\n" \
"buffer_load_dwordx4 v248, s[32:35], s88 offen offset:0 sc1 lds\n" \
"v_mfma_f32_16x16x32_bf16 v[60:63], a[220:223], v[128:131], v[60:63]\n" \
"v_exp_f32 v153, v153\n" \
"v_mfma_f32_16x16x32_bf16 v[128:131], a[128:131], v[176:179], 0\n" \
"v_cmp_gt_u32 s[98:99], 32, v251\n" \
"v_cvt_pk_bf16_f32 v145, v146, v147\n" \
"v_mfma_f32_16x16x32_bf16 v[128:131], a[132:135], v[180:183], v[128:131]\n" \
"v_exp_f32 v154, v154\n" \
"v_mfma_f32_16x16x32_bf16 v[128:131], a[136:139], v[184:187], v[128:131]\n" \
"buffer_load_dwordx4 v248, s[32:35], s88 offen offset:1024 sc1 lds\n" \
"v_mfma_f32_16x16x32_bf16 v[128:131], a[140:143], v[188:191], v[128:131]\n" \
"v_exp_f32 v155, v155\n" \
"v_mfma_f32_16x16x32_bf16 v[132:135], a[156:159], v[188:191], 0\n" \
"v_cndmask_b32 v253, v253, 0, s[98:99]\n" \
"v_cvt_pk_bf16_f32 v146, v148, v149\n" \
"v_mfma_f32_16x16x32_bf16 v[132:135], a[152:155], v[184:187], v[132:135]\n" \
"v_exp_f32 v156, v156\n" \
"v_mfma_f32_16x16x32_bf16 v[132:135], a[148:151], v[180:183], v[132:135]\n" \
"buffer_load_dwordx4 v248, s[32:35], s88 offen offset:2048 sc1 lds\n" \
"v_mfma_f32_16x16x32_bf16 v[132:135], a[144:147], v[176:179], v[132:135]\n" \
"v_exp_f32 v157, v157\n" \
"v_mfma_f32_16x16x32_bf16 v[136:139], a[160:163], v[176:179], 0\n" \
"v_max_u32_dpp v253, v253, v253 quad_perm:[1,0,3,2] row_mask:0xf bank_mask:0xf\n" \
"v_cvt_pk_bf16_f32 v147, v150, v151\n" \
"v_mfma_f32_16x16x32_bf16 v[136:139], a[164:167], v[180:183], v[136:139]\n" \
"v_exp_f32 v158, v158\n" \
"v_mfma_f32_16x16x32_bf16 v[136:139], a[168:171], v[184:187], v[136:139]\n" \
"buffer_load_dwordx4 v248, s[32:35], s88 offen offset:3072 sc1 lds\n" \
"v_mfma_f32_16x16x32_bf16 v[136:139], a[172:175], v[188:191], v[136:139]\n" \
"s_add_u32 s89, s88, 589824\n" \
"v_exp_f32 v159, v159\n" \
"v_mfma_f32_16x16x32_bf16 v[140:143], a[188:191], v[188:191], 0\n" \
"s_add_u32 s88, s88, s27\n" \
"v_exp_f32 v128, v128\n" \
"v_mfma_f32_16x16x32_bf16 v[140:143], a[184:187], v[184:187], v[140:143]\n" \
"s_add_u32 s19, s19, 0x4000\n" \
"v_exp_f32 v129, v129\n" \
"v_mfma_f32_16x16x32_bf16 v[140:143], a[180:183], v[180:183], v[140:143]\n" \
"s_bitset0_b32 s19, 16\n" \
"v_exp_f32 v130, v130\n" \
"v_mfma_f32_16x16x32_bf16 v[140:143], a[176:179], v[176:179], v[140:143]\n" \
"s_cmp_eq_u32 s88, s16\n" \
"v_exp_f32 v131, v131\n" \
"v_cvt_pk_bf16_f32 v152, v152, v153\n" \
"v_cvt_pk_bf16_f32 v153, v154, v155\n" \
"v_cvt_pk_bf16_f32 v154, v156, v157\n" \
"v_cvt_pk_bf16_f32 v155, v158, v159\n" \
"v_mfma_f32_16x16x32_bf16 a[40:43], a[48:51], v[144:147], a[40:43]\n" \
"s_cmov_b32 s88, s21\n" \
"v_exp_f32 v132, v132\n" \
"v_mfma_f32_16x16x32_bf16 a[40:43], a[48:51], v[152:155], a[40:43]\n" \
"s_waitcnt lgkmcnt(4)\n" \
"v_exp_f32 v133, v133\n" \
"v_mfma_f32_16x16x32_bf16 v[64:67], a[224:227], v[152:155], v[64:67]\n" \
"s_cmov_b32 s27, s91\n" \
"v_exp_f32 v134, v134\n" \
"v_mfma_f32_16x16x32_bf16 v[64:67], a[192:195], v[144:147], v[64:67]\n" \
"s_cmov_b32 s16, s77\n" \
"v_max_u32_dpp v253, v253, v253 quad_perm:[2,3,0,1] row_mask:0xf bank_mask:0xf\n" \
"v_cvt_pk_bf16_f32 v128, v128, v129\n" \
"v_mfma_f32_16x16x32_bf16 v[68:71], a[196:199], v[144:147], v[68:71]\n" \
"s_cmov_b32 s21, s92\n" \
"v_exp_f32 v135, v135\n" \
"v_mfma_f32_16x16x32_bf16 v[68:71], a[228:231], v[152:155], v[68:71]\n" \
"s_cmovk_i32 s91, 0x4000\n" \
"v_max_u32_dpp v253, v253, v253 row_half_mirror row_mask:0xf bank_mask:0xf\n" \
"v_cvt_pk_bf16_f32 v129, v130, v131\n" \
"v_mfma_f32_16x16x32_bf16 v[72:75], a[232:235], v[152:155], v[72:75]\n" \
"s_mov_b32 m0, s20\n" \
"v_cvt_pk_bf16_f32 v130, v132, v133\n" \
"v_cvt_pk_bf16_f32 v131, v134, v135\n" \
"v_mfma_f32_16x16x32_bf16 v[72:75], a[200:203], v[144:147], v[72:75]\n" \
"v_exp_f32 v136, v136\n" \
"v_mfma_f32_16x16x32_bf16 v[76:79], a[204:207], v[144:147], v[76:79]\n" \
"buffer_load_dwordx4 v248, s[32:35], s89 offen offset:0 sc1 lds\n" \
"v_mfma_f32_16x16x32_bf16 v[76:79], a[236:239], v[152:155], v[76:79]\n" \
"s_waitcnt lgkmcnt(0)\n" \
"v_exp_f32 v137, v137\n" \
"v_mfma_f32_16x16x32_bf16 v[80:83], a[240:243], v[152:155], v[80:83]\n" \
"buffer_load_dwordx4 v248, s[32:35], s89 offen offset:1024 sc1 lds\n" \
"v_mfma_f32_16x16x32_bf16 v[80:83], a[208:211], v[144:147], v[80:83]\n" \
"ds_read_b128 a[128:131], v248 offset:32768\n" \
"ds_read_b128 a[132:135], v248 offset:33792\n" \
"ds_read_b128 a[136:139], v248 offset:34816\n" \
"ds_read_b128 a[140:143], v248 offset:35840\n" \
"v_exp_f32 v138, v138\n" \
"v_mfma_f32_16x16x32_bf16 v[84:87], a[212:215], v[144:147], v[84:87]\n" \
"buffer_load_dwordx4 v248, s[32:35], s89 offen offset:2048 sc1 lds\n" \
"v_mfma_f32_16x16x32_bf16 v[84:87], a[244:247], v[152:155], v[84:87]\n" \
"v_exp_f32 v139, v139\n" \
"v_mfma_f32_16x16x32_bf16 v[88:91], a[248:251], v[152:155], v[88:91]\n" \
"buffer_load_dwordx4 v248, s[32:35], s89 offen offset:3072 sc1 lds\n" \
"v_mfma_f32_16x16x32_bf16 v[88:91], a[216:219], v[144:147], v[88:91]\n" \
"s_add_u32 s20, s20, 0x4000\n" \
"ds_read_b128 a[156:159], v248 offset:39936\n" \
"ds_read_b128 a[152:155], v248 offset:38912\n" \
"ds_read_b128 a[148:151], v248 offset:37888\n" \
"ds_read_b128 a[144:147], v248 offset:36864\n" \
"v_exp_f32 v140, v140\n" \
"v_mfma_f32_16x16x32_bf16 v[92:95], a[220:223], v[144:147], v[92:95]\n" \
"s_and_b32 s20, s20, 0xffff\n" \
"v_max_u32_dpp v253, v253, v253 row_mirror row_mask:0xf bank_mask:0xf\n" \
"v_mov_b32 v254, v253\n" \
"v_mfma_f32_16x16x32_bf16 v[92:95], a[252:255], v[152:155], v[92:95]\n" \
"s_waitcnt lgkmcnt(4)\n" \
"v_exp_f32 v141, v141\n" \
"v_mfma_f32_16x16x32_bf16 v[144:147], a[128:131], v[176:179], v[232:235]\n" \
"s_bitset1_b32 s20, 16\n" \
"v_permlane16_swap_b32 v253, v254\n" \
"v_mfma_f32_16x16x32_bf16 v[144:147], a[132:135], v[180:183], v[144:147]\n" \
"s_mov_b32 m0, s19\n" \
"v_exp_f32 v142, v142\n" \
"v_mfma_f32_16x16x32_bf16 v[144:147], a[136:139], v[184:187], v[144:147]\n" \
"buffer_load_dwordx4 v248, s[32:35], s88 offen offset:0 sc1 lds\n" \
"v_mfma_f32_16x16x32_bf16 v[144:147], a[140:143], v[188:191], v[144:147]\n" \
"s_waitcnt lgkmcnt(0)\n" \
"v_exp_f32 v143, v143\n" \
"v_mfma_f32_16x16x32_bf16 v[148:151], a[156:159], v[188:191], v[236:239]\n" \
"buffer_load_dwordx4 v248, s[32:35], s88 offen offset:1024 sc1 lds\n" \
"v_mfma_f32_16x16x32_bf16 v[148:151], a[152:155], v[184:187], v[148:151]\n" \
"buffer_load_dwordx4 v248, s[32:35], s88 offen offset:2048 sc1 lds\n" \
"v_mfma_f32_16x16x32_bf16 v[148:151], a[148:151], v[180:183], v[148:151]\n" \
"s_lshl_b32 s85, s68, 3\n" \
"v_exp_f32 v144, v144\n" \
"v_mfma_f32_16x16x32_bf16 v[148:151], a[144:147], v[176:179], v[148:151]\n" \
"s_add_u32 s85, s85, s67\n" \
"buffer_load_dwordx4 v248, s[32:35], s88 offen offset:3072 sc1 lds\n" \
"v_cvt_pk_bf16_f32 v136, v136, v137\n" \
"v_cvt_pk_bf16_f32 v137, v138, v139\n" \
"v_cvt_pk_bf16_f32 v138, v140, v141\n" \
"v_cvt_pk_bf16_f32 v139, v142, v143\n" \
"v_mfma_f32_16x16x32_bf16 a[44:47], a[48:51], v[128:131], a[44:47]\n" \
"s_barrier\n" \
"v_max_u32 v253, v253, v254\n" \
"v_mov_b32 v254, v253\n" \
"v_mfma_f32_16x16x32_bf16 a[44:47], a[48:51], v[136:139], a[44:47]\n" \
"s_add_u32 s89, s88, 589824\n" \
"ds_read_b128 a[128:131], v248 offset:49152\n" \
"ds_read_b128 a[132:135], v248 offset:50176\n" \
"v_mfma_f32_16x16x32_bf16 v[96:99], a[224:227], v[136:139], v[96:99]\n" \
"s_add_u32 s88, s88, s27\n" \
"v_exp_f32 v145, v145\n" \
"v_mfma_f32_16x16x32_bf16 v[96:99], a[192:195], v[128:131], v[96:99]\n" \
"s_add_u32 s19, s19, 0x4000\n" \
"v_permlane32_swap_b32 v253, v254\n" \
"v_mfma_f32_16x16x32_bf16 v[100:103], a[196:199], v[128:131], v[100:103]\n" \
"s_bitset0_b32 s19, 16\n" \
"v_max_u32 v253, v253, v254\n" \
"v_max_f32 v254, 1.0, v253\n" \
"v_mfma_f32_16x16x32_bf16 v[100:103], a[228:231], v[136:139], v[100:103]\n" \
"s_cmp_eq_u32 s88, s16\n" \
"v_min_f32 v253, 1.0, v253\n" \
"v_cvt_pk_bf16_f32 v144, v144, v145\n" \
"v_mfma_f32_16x16x32_bf16 v[104:107], a[232:235], v[136:139], v[104:107]\n" \
"s_cmov_b32 s88, s21\n" \
"v_rcp_f32 v254, v254\n" \
"v_mfma_f32_16x16x32_bf16 v[104:107], a[200:203], v[128:131], v[104:107]\n" \
"s_cmov_b32 s27, s91\n" \
"v_exp_f32 v146, v146\n" \
"v_mfma_f32_16x16x32_bf16 v[108:111], a[204:207], v[128:131], v[108:111]\n" \
"s_cmov_b32 s16, s77\n" \
"v_rcp_f32 v253, v253\n" \
"v_mfma_f32_16x16x32_bf16 v[108:111], a[236:239], v[136:139], v[108:111]\n" \
"s_cmov_b32 s21, s92\n" \
"v_mul_f32 v250, 0x7e000000, v254\n" \
"v_mul_f32 v246, 0x0d800000, v253\n" \
"v_mfma_f32_16x16x32_bf16 v[112:115], a[240:243], v[136:139], v[112:115]\n" \
"s_cmovk_i32 s91, 0x4000\n" \
"ds_read_b128 a[136:139], v248 offset:51200\n" \
"ds_read_b128 a[140:143], v248 offset:52224\n" \
"v_mfma_f32_16x16x32_bf16 v[112:115], a[208:211], v[128:131], v[112:115]\n" \
"s_mov_b32 m0, s20\n" \
"v_exp_f32 v147, v147\n" \
"v_mfma_f32_16x16x32_bf16 v[116:119], a[212:215], v[128:131], v[116:119]\n" \
"s_lshl2_add_u32 s85, s85, %[wid]\n" \
"buffer_load_dwordx4 v248, s[32:35], s89 offen offset:0 sc1 lds\n" \
"v_mfma_f32_16x16x32_bf16 v[116:119], a[244:247], v[136:139], v[116:119]\n" \
"s_lshl_b32 s85, s85, 2\n" \
"buffer_load_dwordx4 v248, s[32:35], s89 offen offset:1024 sc1 lds\n" \
"v_mfma_f32_16x16x32_bf16 v[120:123], a[248:251], v[136:139], v[120:123]\n" \
"s_add_u32 s85, s85, 655360\n" \
"buffer_load_dwordx4 v248, s[32:35], s89 offen offset:2048 sc1 lds\n" \
"v_mfma_f32_16x16x32_bf16 v[120:123], a[216:219], v[128:131], v[120:123]\n" \
"s_waitcnt vmcnt(15)\n" \
"buffer_store_dword a56, off, s[32:35], s85 sc1\n" \
"ds_read_b128 a[156:159], v248 offset:56320\n" \
"ds_read_b128 a[152:155], v248 offset:55296\n" \
"ds_read_b128 a[148:151], v248 offset:54272\n" \
"ds_read_b128 a[144:147], v248 offset:53248\n" \
"v_cvt_pk_bf16_f32 v145, v146, v147\n" \
"v_mfma_f32_16x16x32_bf16 v[124:127], a[220:223], v[128:131], v[124:127]\n" \
"v_exp_f32 v148, v148\n" \
"v_mfma_f32_16x16x32_bf16 v[124:127], a[252:255], v[136:139], v[124:127]\n" \
"s_waitcnt lgkmcnt(4)\n" \
"buffer_load_dwordx4 v248, s[32:35], s89 offen offset:3072 sc1 lds\n" \
"v_mfma_f32_16x16x32_bf16 v[128:131], a[128:131], v[160:163], 0\n" \
"s_add_u32 s20, s20, 0x4000\n" \
"ds_read_b128 a[160:163], v248 offset:57344\n" \
"ds_read_b128 a[164:167], v248 offset:58368\n" \
"v_mfma_f32_16x16x32_bf16 v[128:131], a[132:135], v[164:167], v[128:131]\n" \
"s_and_b32 s20, s20, 0xffff\n" \
"ds_read_b128 a[168:171], v248 offset:59392\n" \
"ds_read_b128 a[172:175], v248 offset:60416\n" \
"v_mfma_f32_16x16x32_bf16 v[128:131], a[136:139], v[168:171], v[128:131]\n" \
"s_bitset1_b32 s20, 16\n" \
"v_exp_f32 v149, v149\n" \
"v_mfma_f32_16x16x32_bf16 v[128:131], a[140:143], v[172:175], v[128:131]\n" \
"s_waitcnt lgkmcnt(4)\n" \
"ds_read_b128 a[192:195], v242 offset:32768\n" \
"v_cvt_pk_bf16_f32 v146, v148, v149\n" \
"v_mfma_f32_16x16x32_bf16 v[132:135], a[156:159], v[172:175], 0\n" \
"s_mov_b32 m0, s19\n" \
"ds_read_b128 a[196:199], v242 offset:33792\n" \
"ds_read_b128 a[188:191], v248 offset:64512\n" \
"v_mfma_f32_16x16x32_bf16 v[132:135], a[152:155], v[168:171], v[132:135]\n" \
"buffer_load_dwordx4 v248, s[32:35], s88 offen offset:0 sc1 lds\n" \
"ds_read_b128 a[200:203], v242 offset:34816\n" \
"ds_read_b128 a[204:207], v242 offset:35840\n" \
"ds_read_b128 a[184:187], v248 offset:63488\n" \
"ds_read_b128 a[180:183], v248 offset:62464\n" \
"ds_read_b128 a[176:179], v248 offset:61440\n" \
"v_mfma_f32_16x16x32_bf16 v[132:135], a[148:151], v[164:167], v[132:135]\n" \
"ds_read_b128 a[208:211], v242 offset:36864\n" \
"ds_read_b128 a[212:215], v242 offset:37888\n" \
"v_mfma_f32_16x16x32_bf16 v[132:135], a[144:147], v[160:163], v[132:135]\n" \
"s_waitcnt lgkmcnt(10)\n" \
"v_exp_f32 v150, v150\n" \
"v_mfma_f32_16x16x32_bf16 v[136:139], a[160:163], v[160:163], v[224:227]\n" \
"buffer_load_dwordx4 v248, s[32:35], s88 offen offset:1024 sc1 lds\n" \
"v_mfma_f32_16x16x32_bf16 v[136:139], a[164:167], v[164:167], v[136:139]\n" \
"ds_read_b128 a[216:219], v242 offset:38912\n" \
"ds_read_b128 a[220:223], v242 offset:39936\n" \
"v_mfma_f32_16x16x32_bf16 v[136:139], a[168:171], v[168:171], v[136:139]\n" \
"v_mfma_f32_16x16x32_bf16 v[136:139], a[172:175], v[172:175], v[136:139]\n" \
"s_waitcnt lgkmcnt(4)\n" \
"v_exp_f32 v151, v151\n" \
"v_mfma_f32_16x16x32_bf16 v[140:143], a[188:191], v[172:175], v[228:231]\n" \
"v_exp_f32 v128, v128\n" \
"v_mfma_f32_16x16x32_bf16 v[140:143], a[184:187], v[168:171], v[140:143]\n" \
"v_exp_f32 v129, v129\n" \
"v_mfma_f32_16x16x32_bf16 v[140:143], a[180:183], v[164:167], v[140:143]\n" \
"v_exp_f32 v130, v130\n" \
"v_cvt_pk_bf16_f32 v147, v150, v151\n" \
"v_mfma_f32_16x16x32_bf16 v[140:143], a[176:179], v[160:163], v[140:143]\n" \
"v_exp_f32 v131, v131\n" \
"v_mfma_f32_16x16x32_bf16 a[44:47], a[48:51], v[144:147], a[44:47]\n" \
"v_exp_f32 v132, v132\n" \
"v_mfma_f32_16x16x32_bf16 v[96:99], a[192:195], v[144:147], v[96:99]\n" \
"v_exp_f32 v133, v133\n" \
"v_mfma_f32_16x16x32_bf16 v[100:103], a[196:199], v[144:147], v[100:103]\n" \
"v_exp_f32 v134, v134\n" \
"v_mfma_f32_16x16x32_bf16 v[104:107], a[200:203], v[144:147], v[104:107]\n" \
"buffer_load_dwordx4 v248, s[32:35], s88 offen offset:2048 sc1 lds\n" \
"v_mfma_f32_16x16x32_bf16 v[108:111], a[204:207], v[144:147], v[108:111]\n" \
"s_waitcnt lgkmcnt(0)\n" \
"v_exp_f32 v135, v135\n" \
"v_mfma_f32_16x16x32_bf16 v[112:115], a[208:211], v[144:147], v[112:115]\n" \
"buffer_load_dwordx4 v248, s[32:35], s88 offen offset:3072 sc1 lds\n" \
"v_mfma_f32_16x16x32_bf16 v[116:119], a[212:215], v[144:147], v[116:119]\n" \
"s_add_u32 s89, s88, 589824\n" \
"v_exp_f32 v136, v136\n" \
"v_mfma_f32_16x16x32_bf16 v[120:123], a[216:219], v[144:147], v[120:123]\n" \
"s_add_u32 s88, s88, s27\n" \
"v_cvt_pk_bf16_f32 v128, v128, v129\n" \
"v_cvt_pk_bf16_f32 v129, v130, v131\n" \
"v_mfma_f32_16x16x32_bf16 v[124:127], a[220:223], v[144:147], v[124:127]\n" \
"s_add_u32 s19, s19, 0x4000\n" \
"v_exp_f32 v137, v137\n" \
"v_mfma_f32_16x16x32_bf16 v[144:147], a[128:131], v[176:179], 0\n" \
"s_waitcnt vmcnt(13)\n" \
"s_barrier\n" \
"v_cvt_pk_bf16_f32 v130, v132, v133\n" \
"v_mfma_f32_16x16x32_bf16 v[144:147], a[132:135], v[180:183], v[144:147]\n" \
"s_bitset0_b32 s19, 16\n" \
"v_exp_f32 v138, v138\n" \
"v_mfma_f32_16x16x32_bf16 v[144:147], a[136:139], v[184:187], v[144:147]\n" \
"s_cmp_eq_u32 s88, s16\n" \
"v_cvt_pk_bf16_f32 v131, v134, v135\n" \
"v_cvt_pk_bf16_f32 v136, v136, v137\n" \
"v_mfma_f32_16x16x32_bf16 v[144:147], a[140:143], v[188:191], v[144:147]\n" \
"s_cmov_b32 s88, s21\n" \
"v_exp_f32 v139, v139\n" \
"v_mfma_f32_16x16x32_bf16 v[148:151], a[156:159], v[188:191], 0\n" \
"s_cmov_b32 s27, s91\n" \
"ds_read_b128 a[224:227], v242 offset:57344\n" \
"v_cvt_pk_bf16_f32 v137, v138, v139\n" \
"v_mfma_f32_16x16x32_bf16 v[148:151], a[152:155], v[184:187], v[148:151]\n" \
"s_cmov_b32 s16, s77\n" \
"v_exp_f32 v140, v140\n" \
"v_mfma_f32_16x16x32_bf16 v[148:151], a[148:151], v[180:183], v[148:151]\n" \
"s_cmov_b32 s21, s92\n" \
"ds_read_b128 a[192:195], v242 offset:49152\n" \
"ds_read_b128 a[196:199], v242 offset:50176\n" \
"v_mfma_f32_16x16x32_bf16 v[148:151], a[144:147], v[176:179], v[148:151]\n" \
"s_cmovk_i32 s91, 0x4000\n" \
"v_exp_f32 v141, v141\n" \
"v_mfma_f32_16x16x32_bf16 v[152:155], a[160:163], v[176:179], 0\n" \
"s_mov_b32 m0, s20\n" \
"ds_read_b128 a[228:231], v242 offset:58368\n" \
"v_cvt_pk_bf16_f32 v138, v140, v141\n" \
"v_mfma_f32_16x16x32_bf16 v[152:155], a[164:167], v[180:183], v[152:155]\n" \
"v_exp_f32 v142, v142\n" \
"v_mfma_f32_16x16x32_bf16 v[152:155], a[168:171], v[184:187], v[152:155]\n" \
"buffer_load_dwordx4 v248, s[32:35], s89 offen offset:0 sc1 lds\n" \
"v_mfma_f32_16x16x32_bf16 v[152:155], a[172:175], v[188:191], v[152:155]\n" \
"v_exp_f32 v143, v143\n" \
"v_mfma_f32_16x16x32_bf16 v[156:159], a[188:191], v[188:191], 0\n" \
"buffer_load_dwordx4 v248, s[32:35], s89 offen offset:1024 sc1 lds\n" \
"v_mfma_f32_16x16x32_bf16 v[156:159], a[184:187], v[184:187], v[156:159]\n" \
"ds_read_b128 a[232:235], v242 offset:59392\n" \
"v_cvt_pk_bf16_f32 v139, v142, v143\n" \
"v_mfma_f32_16x16x32_bf16 v[156:159], a[180:183], v[180:183], v[156:159]\n" \
"v_exp_f32 v144, v144\n" \
"v_mfma_f32_16x16x32_bf16 v[156:159], a[176:179], v[176:179], v[156:159]\n" \
"buffer_load_dwordx4 v248, s[32:35], s89 offen offset:2048 sc1 lds\n" \
"ds_read_b128 a[200:203], v242 offset:51200\n" \
"ds_read_b128 a[204:207], v242 offset:52224\n" \
"ds_read_b128 a[236:239], v242 offset:60416\n" \
"v_mfma_f32_16x16x32_bf16 a[40:43], a[48:51], v[128:131], a[40:43]\n" \
"v_exp_f32 v145, v145\n" \
"v_mfma_f32_16x16x32_bf16 a[40:43], a[48:51], v[136:139], a[40:43]\n" \
"s_waitcnt lgkmcnt(4)\n" \
"ds_read_b128 a[240:243], v242 offset:61440\n" \
"v_cvt_pk_bf16_f32 v144, v144, v145\n" \
"v_mfma_f32_16x16x32_bf16 v[64:67], a[224:227], v[136:139], v[64:67]\n" \
"v_exp_f32 v146, v146\n" \
"v_mfma_f32_16x16x32_bf16 v[64:67], a[192:195], v[128:131], v[64:67]\n" \
"ds_read_b128 a[208:211], v242 offset:53248\n" \
"ds_read_b128 a[128:131], v248 offset:0\n" \
"ds_read_b128 a[212:215], v242 offset:54272\n" \
"ds_read_b128 a[132:135], v248 offset:1024\n" \
"ds_read_b128 a[244:247], v242 offset:62464\n" \
"v_mfma_f32_16x16x32_bf16 v[68:71], a[196:199], v[128:131], v[68:71]\n" \
"v_exp_f32 v147, v147\n" \
"v_mfma_f32_16x16x32_bf16 v[68:71], a[228:231], v[136:139], v[68:71]\n" \
"s_waitcnt lgkmcnt(6)\n" \
"buffer_load_dwordx4 v248, s[32:35], s89 offen offset:3072 sc1 lds\n" \
"v_mfma_f32_16x16x32_bf16 v[72:75], a[232:235], v[136:139], v[72:75]\n" \
"s_add_u32 s20, s20, 0x4000\n" \
"v_exp_f32 v148, v148\n" \
"v_mfma_f32_16x16x32_bf16 v[72:75], a[200:203], v[128:131], v[72:75]\n" \
"s_and_b32 s20, s20, 0xffff\n" \
"ds_read_b128 a[136:139], v248 offset:2048\n" \
"ds_read_b128 a[140:143], v248 offset:3072\n" \
"ds_read_b128 a[248:251], v242 offset:63488\n" \
"ds_read_b128 a[216:219], v242 offset:55296\n" \
"ds_read_b128 a[156:159], v248 offset:7168\n" \
"ds_read_b128 a[220:223], v242 offset:56320\n" \
"ds_read_b128 a[152:155], v248 offset:6144\n" \
"ds_read_b128 a[252:255], v242 offset:64512\n" \
"v_cvt_pk_bf16_f32 v145, v146, v147\n" \
"v_mfma_f32_16x16x32_bf16 v[76:79], a[204:207], v[128:131], v[76:79]\n" \
"s_bitset1_b32 s20, 16\n" \
"v_exp_f32 v149, v149\n" \
"v_mfma_f32_16x16x32_bf16 v[76:79], a[236:239], v[136:139], v[76:79]\n" \
"s_waitcnt lgkmcnt(8)\n" \
"ds_read_b128 a[148:151], v248 offset:5120\n" \
"v_cvt_pk_bf16_f32 v146, v148, v149\n" \
"v_mfma_f32_16x16x32_bf16 v[80:83], a[240:243], v[136:139], v[80:83]\n" \
"v_exp_f32 v150, v150\n" \
"v_mfma_f32_16x16x32_bf16 v[80:83], a[208:211], v[128:131], v[80:83]\n" \
"ds_read_b128 a[144:147], v248 offset:4096\n" \
"ds_read_b128 a[160:163], v248 offset:8192\n" \
"v_mfma_f32_16x16x32_bf16 v[84:87], a[212:215], v[128:131], v[84:87]\n" \
"v_exp_f32 v151, v151\n" \
"v_mfma_f32_16x16x32_bf16 v[84:87], a[244:247], v[136:139], v[84:87]\n" \
"s_waitcnt lgkmcnt(3)\n" \
"ds_read_b128 a[164:167], v248 offset:9216\n" \
"v_cvt_pk_bf16_f32 v147, v150, v151\n" \
"v_mfma_f32_16x16x32_bf16 v[88:91], a[248:251], v[136:139], v[88:91]\n" \
"ds_read_b128 a[168:171], v248 offset:10240\n" \
"ds_read_b128 a[172:175], v248 offset:11264\n" \
"v_mfma_f32_16x16x32_bf16 v[88:91], a[216:219], v[128:131], v[88:91]\n" \
"v_exp_f32 v152, v152\n" \
"v_mfma_f32_16x16x32_bf16 v[92:95], a[220:223], v[128:131], v[92:95]\n" \
"v_exp_f32 v153, v153\n" \
"v_mfma_f32_16x16x32_bf16 v[92:95], a[252:255], v[136:139], v[92:95]\n" \
"v_and_b32 v230, 7, v251\n" \
"v_lshrrev_b32 v232, 3, v251\n" \
"v_lshlrev_b32 v231, 4, v230\n" \
"v_mul_u32_u24 v230, 7168, v230\n" \
"v_lshl_add_u32 v230, v232, 4, v230\n" \
"v_lshl_or_b32 v232, v232, 14, v231\n" \
"v_and_b32 v231, 3, v251\n" \
"v_lshlrev_b32 v231, 7, v231\n" \
"v_bfe_u32 v237, v251, 4, 1\n" \
"v_lshlrev_b32 v237, 1, v237\n" \
"v_and_b32 v238, 3, v251\n" \
"v_xor_b32 v237, v237, v238\n" \
"v_lshrrev_b32 v238, 5, v251\n" \
"v_lshl_or_b32 v237, v238, 2, v237\n" \
"v_bfe_u32 v238, v251, 2, 3\n" \
"v_lshl_or_b32 v237, v238, 6, v237\n" \
"v_lshlrev_b32 v237, 4, v237\n" \
"v_and_b32 v238, 7, v251\n" \
"v_lshlrev_b32 v238, 7, v238\n" \
"s_branch .Ldj_%=\n" \
".Lspq_%=:\n" \
"s_mov_b32 s82, 0x100000\n" \
".Lsqq_%=:\n" \
"s_sleep 6\n" \
"buffer_load_dwordx2 v[204:205], v253, s[32:35], s81 offen sc1\n" \
"s_waitcnt vmcnt(0)\n" \
"v_cmp_ne_u64 vcc, v[204:205], v[202:203]\n" \
"s_cbranch_vccz .Lsbq_%=\n" \
"s_sub_u32 s82, s82, 1\n" \
"s_cbranch_scc0 .Lsqq_%=\n" \
"s_branch .Lsbq_%=\n" \
".Lsph_%=:\n" \
"s_mov_b32 s82, 0x100000\n" \
".Lsqh_%=:\n" \
"s_sleep 6\n" \
"buffer_load_dword v247, v252, s[32:35], s81 offen sc1\n" \
"s_waitcnt vmcnt(0)\n" \
"v_cmp_eq_u32 vcc, 0, v247\n" \
"s_cbranch_vccz .Lsbh_%=\n" \
"s_sub_u32 s82, s82, 1\n" \
"s_cbranch_scc0 .Lsqh_%=\n" \
"s_branch .Lsbh_%=\n" \
".Lspv_%=:\n" \
"s_mov_b32 s82, 0x100000\n" \
".Lsqv_%=:\n" \
"s_sleep 6\n" \
"buffer_load_dword v253, v252, s[32:35], s81 offen sc1\n" \
"s_waitcnt vmcnt(0)\n" \
"v_cmp_eq_u32 vcc, 0, v253\n" \
"s_cbranch_vccz .Lsbv_%=\n" \
"s_sub_u32 s82, s82, 1\n" \
"s_cbranch_scc0 .Lsqv_%=\n" \
"s_branch .Lsbv_%=\n" \
".Lspw_%=:\n" \
"s_mov_b32 s82, 0x100000\n" \
".Lsqw_%=:\n" \
"s_sleep 6\n" \
"buffer_load_dword v253, v252, s[32:35], s81 offen sc1\n" \
"s_waitcnt vmcnt(0)\n" \
"v_cmp_eq_u32 vcc, 0, v253\n" \
"s_cbranch_vccz .Lsbw_%=\n" \
"s_sub_u32 s82, s82, 1\n" \
"s_cbranch_scc0 .Lsqw_%=\n" \
"s_branch .Lsbw_%=\n" \
".Lchd_%=:\n" \
".Lst1_9_%=:\n" \
"s_cbranch_execz .Lst1_13_%=\n" \
"v_cmp_eq_u32 vcc, 0, v247\n" \
"s_sub_u32 s23, s18, 262144\n" \
"s_cmp_eq_u32 s24, s23\n" \
"s_cbranch_scc1 .Ldhd_%=\n" \
"s_mov_b32 s24, s23\n" \
"buffer_load_dword v247, v244, s[32:35], s26 offen sc1\n" \
"s_branch .Lsbhd_%=\n" \
".Ldhd_%=:\n" \
"s_cbranch_vccz .Lsbhd_%=\n" \
".Lsphd_%=:\n" \
"s_mov_b32 s82, 0x100000\n" \
".Lsqhd_%=:\n" \
"s_sleep 6\n" \
"buffer_load_dword v247, v244, s[32:35], s26 offen sc1\n" \
"s_waitcnt vmcnt(0)\n" \
"v_cmp_eq_u32 vcc, 0, v247\n" \
"s_cbranch_vccz .Lsbhd_%=\n" \
"s_sub_u32 s82, s82, 1\n" \
"s_cbranch_scc0 .Lsqhd_%=\n" \
"s_branch .Lsbhd_%=\n" \
".Land_%=:\n" \
".Lst1_10_%=:\n" \
"s_cbranch_execz .Lst1_14_%=\n" \
"s_waitcnt vmcnt(4)\n" \
"buffer_store_dword a56, off, s[32:35], s25 sc1\n" \
"buffer_store_dword a56, off, s[32:35], s78 sc1\n" \
"s_mov_b32 s78, 1073741824\n" \
"s_mov_b32 s76, 0\n" \
"s_branch .Labd_%=\n" \
".Lcht_%=:\n" \
".Lst1_11_%=:\n" \
"s_cbranch_execz .Lst1_15_%=\n" \
"v_cmp_eq_u32 vcc, 0, v247\n" \
"s_sub_u32 s23, s18, 262144\n" \
"s_cmp_eq_u32 s24, s23\n" \
"s_cbranch_scc1 .Ldht_%=\n" \
"s_mov_b32 s24, s23\n" \
"buffer_load_dword v247, v244, s[32:35], s26 offen sc1\n" \
"s_branch .Lsbht_%=\n" \
".Ldht_%=:\n" \
"s_cbranch_vccz .Lsbht_%=\n" \
".Lspht_%=:\n" \
"s_mov_b32 s82, 0x100000\n" \
".Lsqht_%=:\n" \
"s_sleep 6\n" \
"buffer_load_dword v247, v244, s[32:35], s26 offen sc1\n" \
"s_waitcnt vmcnt(0)\n" \
"v_cmp_eq_u32 vcc, 0, v247\n" \
"s_cbranch_vccz .Lsbht_%=\n" \
"s_sub_u32 s82, s82, 1\n" \
"s_cbranch_scc0 .Lsqht_%=\n" \
"s_branch .Lsbht_%=\n" \
".Lant_%=:\n" \
".Lst1_12_%=:\n" \
"s_cbranch_execz .Lstb1_%=\n" \
"s_waitcnt vmcnt(9)\n" \
"buffer_store_dword a56, off, s[32:35], s25 sc1\n" \
"buffer_store_dword a56, off, s[32:35], s78 sc1\n" \
"s_mov_b32 s78, 1073741824\n" \
"s_mov_b32 s76, 0\n" \
"s_branch .Labt_%=\n" \
".Lchtx_%=:\n" \
".Lst1_13_%=:\n" \
"s_cbranch_execz .Lstb1_%=\n" \
"v_cmp_eq_u32 vcc, 0, v247\n" \
"s_sub_u32 s23, s18, 262144\n" \
"s_cmp_eq_u32 s24, s23\n" \
"s_cbranch_scc1 .Ldhtx_%=\n" \
"s_mov_b32 s24, s23\n" \
"buffer_load_dword v247, v244, s[32:35], s26 offen sc1\n" \
"s_branch .Lsbhtx_%=\n" \
".Ldhtx_%=:\n" \
"s_cbranch_vccz .Lsbhtx_%=\n" \
".Lsphtx_%=:\n" \
"s_mov_b32 s82, 0x100000\n" \
".Lsqhtx_%=:\n" \
"s_sleep 6\n" \
"buffer_load_dword v247, v244, s[32:35], s26 offen sc1\n" \
"s_waitcnt vmcnt(0)\n" \
"v_cmp_eq_u32 vcc, 0, v247\n" \
"s_cbranch_vccz .Lsbhtx_%=\n" \
"s_sub_u32 s82, s82, 1\n" \
"s_cbranch_scc0 .Lsqhtx_%=\n" \
"s_branch .Lsbhtx_%=\n" \
".Lantx_%=:\n" \
".Lst1_14_%=:\n" \
"s_cbranch_execz .Lstb1_%=\n" \
"s_waitcnt vmcnt(10)\n" \
"buffer_store_dword a56, off, s[32:35], s25 sc1\n" \
"buffer_store_dword a56, off, s[32:35], s78 sc1\n" \
"s_mov_b32 s78, 1073741824\n" \
"s_mov_b32 s76, 0\n" \
"s_branch .Labtx_%=\n" \
".Lspl_%=:\n" \
"s_mov_b32 s82, 0x100000\n" \
".Lsql_%=:\n" \
"s_sleep 6\n" \
"buffer_load_dword v247, v255, s[32:35], s31 offen sc1\n" \
"s_waitcnt vmcnt(0)\n" \
"v_cmp_eq_u32 vcc, 0, v247\n" \
"s_cbranch_vccz .Lsbl_%=\n" \
"s_sub_u32 s82, s82, 1\n" \
"s_cbranch_scc0 .Lsql_%=\n" \
"s_branch .Lsbl_%=\n" \
".Lrh_%=:\n" \
"s_call_b64 s[98:99], .Lredo2_%=\n" \
"v_accvgpr_read_b32 v224, a40\n" \
"v_rcp_f32 v224, v224\n" \
"s_nop 0\n" \
"v_mul_f32 v64, v64, v224\n" \
"v_mul_f32 v65, v65, v224\n" \
"v_mul_f32 v66, v66, v224\n" \
"v_mul_f32 v67, v67, v224\n" \
"v_mul_f32 v68, v68, v224\n" \
"v_mul_f32 v69, v69, v224\n" \
"v_mul_f32 v70, v70, v224\n" \
"v_mul_f32 v71, v71, v224\n" \
"v_cvt_pk_bf16_f32 v192, v64, v65\n" \
"v_cvt_pk_bf16_f32 v193, v66, v67\n" \
"v_cvt_pk_bf16_f32 v194, v68, v69\n" \
"v_cvt_pk_bf16_f32 v195, v70, v71\n" \
"v_mul_f32 v72, v72, v224\n" \
"v_mul_f32 v73, v73, v224\n" \
"v_mul_f32 v74, v74, v224\n" \
"v_mul_f32 v75, v75, v224\n" \
"v_mul_f32 v76, v76, v224\n" \
"v_mul_f32 v77, v77, v224\n" \
"v_mul_f32 v78, v78, v224\n" \
"v_mul_f32 v79, v79, v224\n" \
"v_cvt_pk_bf16_f32 v200, v72, v73\n" \
"v_cvt_pk_bf16_f32 v201, v74, v75\n" \
"v_cvt_pk_bf16_f32 v202, v76, v77\n" \
"v_cvt_pk_bf16_f32 v203, v78, v79\n" \
"v_mul_f32 v80, v80, v224\n" \
"buffer_store_dwordx4 v[192:195], v248, s[32:35], s39 offen offset:0 sc1\n" \
"s_add_u32 s38, s66, 3\n" \
"s_and_b32 s38, s38, 3\n" \
"s_lshl_b32 s38, s38, 13\n" \
"s_add_u32 s38, s38, s29\n" \
"buffer_load_dwordx4 a[120:123], v248, s[32:35], s38 offen offset:0 sc1\n" \
"buffer_load_dwordx4 a[124:127], v248, s[32:35], s38 offen offset:1024 sc1\n" \
"buffer_load_dwordx4 a[128:131], v248, s[32:35], s38 offen offset:2048 sc1\n" \
"buffer_load_dwordx4 a[132:135], v248, s[32:35], s38 offen offset:3072 sc1\n" \
"s_add_u32 s38, s38, 4096\n" \
"buffer_load_dwordx4 a[136:139], v248, s[32:35], s38 offen offset:0 sc1\n" \
"buffer_load_dwordx4 a[140:143], v248, s[32:35], s38 offen offset:1024 sc1\n" \
"buffer_load_dwordx4 a[144:147], v248, s[32:35], s38 offen offset:2048 sc1\n" \
"buffer_load_dwordx4 a[148:151], v248, s[32:35], s38 offen offset:3072 sc1\n" \
"s_waitcnt vmcnt(0)\n" \
"s_branch .Lrhb_%=\n" \
".LspcA_%=:\n" \
"s_mov_b32 s82, 0x100000\n" \
".LsqcA_%=:\n" \
"s_sleep 6\n" \
"buffer_load_dword v247, v254, s[32:35], s41 offen sc1\n" \
"s_waitcnt vmcnt(0)\n" \
"v_cmp_eq_u32 vcc, 0, v247\n" \
"s_cbranch_vccz .LsbcA_%=\n" \
"s_sub_u32 s82, s82, 1\n" \
"s_cbranch_scc0 .LsqcA_%=\n" \
"s_branch .LsbcA_%=\n" \
".Lspg_%=:\n" \
"s_mov_b32 s82, 0x100000\n" \
".Lsqg_%=:\n" \
"s_sleep 6\n" \
"buffer_load_dwordx2 v[230:231], v232, s[32:35], s70 offen sc1\n" \
"s_waitcnt vmcnt(0)\n" \
"v_cmp_ne_u64 vcc, v[230:231], v[234:235]\n" \
"s_cbranch_vccz .Lsbg_%=\n" \
"s_sub_u32 s82, s82, 1\n" \
"s_cbranch_scc0 .Lsqg_%=\n" \
"s_branch .Lsbg_%=\n" \
".Lch3_%=:\n" \
".Lst1_15_%=:\n" \
"s_cbranch_execz .Lstb1_%=\n" \
"s_mov_b64 exec, 0\n" \
"s_cmp_eq_u32 %[wid], 0\n" \
"s_cbranch_scc1 .Lst3_0_%=\n" \
"s_cmp_eq_u32 %[wid], 1\n" \
"s_cbranch_scc1 .Lst3_1_%=\n" \
"s_cmp_eq_u32 %[wid], 2\n" \
"s_cbranch_scc1 .Lst3_2_%=\n" \
"s_branch .Lst3_3_%=\n" \
".Lstb3_%=:\n" \
"s_mov_b64 exec, -1\n" \
"s_setpc_b64 s[98:99]\n" \
".Lga_%=:\n" \
".Lst3_1_%=:\n" \
"s_cbranch_execz .Lstb3_%=\n" \
"v_cmp_ne_u32 vcc, 0, v245\n" \
"s_nop 3\n" \
"s_lshr_b32 s76, vcc_lo, 1\n" \
"s_and_b32 s76, s76, vcc_lo\n" \
"s_lshr_b32 s77, s76, 2\n" \
"s_and_b32 s76, s76, s77\n" \
"s_sub_u32 s82, s82, 1\n" \
"s_cselect_b32 s76, -1, s76\n" \
"s_and_b32 s76, s76, 0x11111111\n" \
"s_andn2_b32 s76, s76, s74\n" \
"s_cbranch_scc0 .Lgx_%=\n" \
"s_or_b32 s74, s74, s76\n" \
"s_bitcmp1_b32 s76, 0\n" \
"s_cbranch_scc0 .Lgs0_%=\n" \
"s_add_u32 s24, s95, 0\n" \
"buffer_load_dwordx4 a[128:131], v214, s[32:35], s24 offen sc1\n" \
"buffer_load_dwordx4 a[160:163], v214, s[32:35], s24 offen offset:64 sc1\n" \
".Lgs0_%=:\n" \
"s_bitcmp1_b32 s76, 4\n" \
"s_cbranch_scc0 .Lgs1_%=\n" \
"s_add_u32 s24, s95, 2097152\n" \
"buffer_load_dwordx4 a[132:135], v214, s[32:35], s24 offen sc1\n" \
"buffer_load_dwordx4 a[164:167], v214, s[32:35], s24 offen offset:64 sc1\n" \
".Lgs1_%=:\n" \
"s_bitcmp1_b32 s76, 8\n" \
"s_cbranch_scc0 .Lgs2_%=\n" \
"s_add_u32 s24, s95, 4194304\n" \
"buffer_load_dwordx4 a[136:139], v214, s[32:35], s24 offen sc1\n" \
"buffer_load_dwordx4 a[168:171], v214, s[32:35], s24 offen offset:64 sc1\n" \
".Lgs2_%=:\n" \
"s_bitcmp1_b32 s76, 12\n" \
"s_cbranch_scc0 .Lgs3_%=\n" \
"s_add_u32 s24, s95, 6291456\n" \
"buffer_load_dwordx4 a[140:143], v214, s[32:35], s24 offen sc1\n" \
"buffer_load_dwordx4 a[172:175], v214, s[32:35], s24 offen offset:64 sc1\n" \
".Lgs3_%=:\n" \
"s_bitcmp1_b32 s76, 16\n" \
"s_cbranch_scc0 .Lgs4_%=\n" \
"s_add_u32 s24, s95, 8388608\n" \
"buffer_load_dwordx4 a[144:147], v214, s[32:35], s24 offen sc1\n" \
"buffer_load_dwordx4 a[176:179], v214, s[32:35], s24 offen offset:64 sc1\n" \
".Lgs4_%=:\n" \
"s_bitcmp1_b32 s76, 20\n" \
"s_cbranch_scc0 .Lgs5_%=\n" \
"s_add_u32 s24, s95, 10485760\n" \
"buffer_load_dwordx4 a[148:151], v214, s[32:35], s24 offen sc1\n" \
"buffer_load_dwordx4 a[180:183], v214, s[32:35], s24 offen offset:64 sc1\n" \
".Lgs5_%=:\n" \
"s_bitcmp1_b32 s76, 24\n" \
"s_cbranch_scc0 .Lgs6_%=\n" \
"s_add_u32 s24, s95, 12582912\n" \
"buffer_load_dwordx4 a[152:155], v214, s[32:35], s24 offen sc1\n" \
"buffer_load_dwordx4 a[184:187], v214, s[32:35], s24 offen offset:64 sc1\n" \
".Lgs6_%=:\n" \
"s_bitcmp1_b32 s76, 28\n" \
"s_cbranch_scc0 .Lgs7_%=\n" \
"s_add_u32 s24, s95, 14680064\n" \
"buffer_load_dwordx4 a[156:159], v214, s[32:35], s24 offen sc1\n" \
"buffer_load_dwordx4 a[188:191], v214, s[32:35], s24 offen offset:64 sc1\n" \
".Lgs7_%=:\n" \
"s_cmp_eq_u32 s74, 0x11111111\n" \
"s_cbranch_scc1 .Lg0_%=\n" \
".Lgx_%=:\n" \
"s_not_b32 vcc_lo, vcc_lo\n" \
"s_mov_b32 vcc_hi, 0\n" \
"s_bcnt1_i32_b64 s27, vcc\n" \
"s_cmp_lt_u32 s27, 8\n" \
"s_cbranch_scc1 .Lnap1_%=\n" \
"s_sleep 10\n" \
".Lnap1_%=:\n" \
"s_sleep 2\n" \
"buffer_load_dword v245, v252, s[32:35], s18 offen sc1\n" \
"s_branch .Lg0_%=\n" \
".Lkr_%=:\n" \
".Lst3_2_%=:\n" \
"s_cbranch_execz .Lstb3_%=\n" \
"s_sub_u32 s82, s82, 1\n" \
"s_cbranch_scc1 .Lkb_%=\n" \
"s_cmp_lt_u32 s65, 28\n" \
"s_cbranch_scc1 .Lkn_%=\n" \
"v_max_u32_dpp v88, v88, v88 quad_perm:[1,0,3,2] row_mask:0xf bank_mask:0xf\n" \
"s_nop 1\n" \
"v_max_u32_dpp v88, v88, v88 quad_perm:[2,3,0,1] row_mask:0xf bank_mask:0xf\n" \
"s_nop 1\n" \
"v_max_u32_dpp v88, v88, v88 row_half_mirror row_mask:0xf bank_mask:0xf\n" \
"s_nop 1\n" \
"v_max_u32_dpp v88, v88, v88 row_mirror row_mask:0xf bank_mask:0xf\n" \
"s_nop 1\n" \
"v_max_u32_dpp v88, v88, v88 row_bcast:15 row_mask:0xa bank_mask:0xf\n" \
"s_nop 1\n" \
"v_max_u32_dpp v88, v88, v88 row_bcast:31 row_mask:0xc bank_mask:0xf\n" \
"s_nop 0\n" \
"v_readlane_b32 s25, v88, 63\n" \
"v_lshrrev_b32 v89, 4, v251\n" \
"v_and_b32 v90, 15, v251\n" \
"s_cmp_eq_u32 s25, s93\n" \
"s_cbranch_scc1 .Lkn_%=\n" \
"s_mov_b32 s93, s25\n" \
"s_bfe_u32 s25, s25, 0xb0005\n" \
"s_sub_u32 s25, 2047, s25\n" \
"s_mov_b32 s22, 0x18100800\n" \
"s_mov_b32 s23, 0x01091119\n" \
"s_bfe_u32 s24, s25, 0x30004\n" \
"s_lshl_b32 s24, s24, 3\n" \
"s_lshr_b64 s[22:23], s[22:23], s24\n" \
"s_and_b32 s22, s22, 0xff\n" \
"s_lshr_b32 s26, s25, 7\n" \
"s_lshl_b32 s26, s26, 5\n" \
"s_add_u32 s26, s26, s22\n" \
"s_lshl_b32 s26, s26, 6\n" \
"s_and_b32 s24, s25, 15\n" \
"s_add_u32 s26, s26, s24\n" \
"s_lshl_b32 s26, s26, 6\n" \
"s_add_u32 s26, s26, 59768832\n" \
"v_lshlrev_b32 v89, 21, v89\n" \
"v_and_b32 v91, 3, v251\n" \
"v_lshl_or_b32 v89, v91, 10, v89\n" \
"v_lshrrev_b32 v91, 2, v90\n" \
"v_lshl_or_b32 v89, v91, 13, v89\n" \
"v_lshlrev_b32 v90, 6, v90\n" \
"s_lshl_b32 s25, s25, 10\n" \
"s_add_u32 s24, s26, 8388608\n" \
"buffer_load_dword v91, v89, s[32:35], s26 offen sc1\n" \
"buffer_load_dword v91, v89, s[32:35], s24 offen sc1\n" \
"buffer_load_dword v91, v90, s[44:47], s25 offen\n" \
".Lkn_%=:\n" \
"s_bcnt1_i32_b64 s27, vcc\n" \
"s_cmp_lt_u32 s27, 6\n" \
"s_cbranch_scc1 .Lnap2_%=\n" \
"s_sleep 20\n" \
".Lnap2_%=:\n" \
"s_sleep 2\n" \
"buffer_load_dwordx2 v[86:87], v235, s[32:35], s38 offen sc1\n" \
"s_waitcnt vmcnt(0)\n" \
"s_branch .Lkp_%=\n" \
".Lfr_%=:\n" \
".Lst3_3_%=:\n" \
"s_cbranch_execz .Lstb3_%=\n" \
"s_sub_u32 s82, s82, 1\n" \
"s_cbranch_scc1 .Lfb_%=\n" \
"s_sleep 2\n" \
"s_branch .Lfp_%=\n" \
  \
".Lredo0_%=:\n" \
"s_nop 7\n" \
"v_and_b32 v228, 15, v251\n" \
"v_lshrrev_b32 v229, 4, v251\n" \
"v_lshlrev_b32 v229, 2, v229\n" \
"v_sub_u32 v228, v228, v229\n" \
"v_mov_b32 v231, 0xff800000\n" \
"s_mov_b32 s14, s67\n" \
"s_xor_b32 s15, %[wid], 0\n" \
"s_lshl3_add_u32 s15, s14, s15\n" \
"s_lshl_b32 s15, s15, 4\n" \
"v_add_u32 v226, s15, v228\n" \
"v_mov_b32 v224, 0xff7fffff\n" \
"s_xor_b32 s15, %[wid], 7\n" \
"s_lshl3_add_u32 s15, s14, s15\n" \
"s_lshl_b32 s15, s15, 4\n" \
"v_add_u32 v227, s15, v228\n" \
"v_mov_b32 v225, 0xff7fffff\n" \
"v_mov_b32 v0, 0\n" \
"v_mov_b32 v1, 0\n" \
"v_mov_b32 v2, 0\n" \
"v_mov_b32 v3, 0\n" \
"v_mov_b32 v4, 0\n" \
"v_mov_b32 v5, 0\n" \
"v_mov_b32 v6, 0\n" \
"v_mov_b32 v7, 0\n" \
"v_mov_b32 v8, 0\n" \
"v_mov_b32 v9, 0\n" \
"v_mov_b32 v10, 0\n" \
"v_mov_b32 v11, 0\n" \
"v_mov_b32 v12, 0\n" \
"v_mov_b32 v13, 0\n" \
"v_mov_b32 v14, 0\n" \
"v_mov_b32 v15, 0\n" \
"v_mov_b32 v16, 0\n" \
"v_mov_b32 v17, 0\n" \
"v_mov_b32 v18, 0\n" \
"v_mov_b32 v19, 0\n" \
"v_mov_b32 v20, 0\n" \
"v_mov_b32 v21, 0\n" \
"v_mov_b32 v22, 0\n" \
"v_mov_b32 v23, 0\n" \
"v_mov_b32 v24, 0\n" \
"v_mov_b32 v25, 0\n" \
"v_mov_b32 v26, 0\n" \
"v_mov_b32 v27, 0\n" \
"v_mov_b32 v28, 0\n" \
"v_mov_b32 v29, 0\n" \
"v_mov_b32 v30, 0\n" \
"v_mov_b32 v31, 0\n" \
"v_mov_b32 v32, 0\n" \
"v_mov_b32 v33, 0\n" \
"v_mov_b32 v34, 0\n" \
"v_mov_b32 v35, 0\n" \
"v_mov_b32 v36, 0\n" \
"v_mov_b32 v37, 0\n" \
"v_mov_b32 v38, 0\n" \
"v_mov_b32 v39, 0\n" \
"v_mov_b32 v40, 0\n" \
"v_mov_b32 v41, 0\n" \
"v_mov_b32 v42, 0\n" \
"v_mov_b32 v43, 0\n" \
"v_mov_b32 v44, 0\n" \
"v_mov_b32 v45, 0\n" \
"v_mov_b32 v46, 0\n" \
"v_mov_b32 v47, 0\n" \
"v_mov_b32 v48, 0\n" \
"v_mov_b32 v49, 0\n" \
"v_mov_b32 v50, 0\n" \
"v_mov_b32 v51, 0\n" \
"v_mov_b32 v52, 0\n" \
"v_mov_b32 v53, 0\n" \
"v_mov_b32 v54, 0\n" \
"v_mov_b32 v55, 0\n" \
"v_mov_b32 v56, 0\n" \
"v_mov_b32 v57, 0\n" \
"v_mov_b32 v58, 0\n" \
"v_mov_b32 v59, 0\n" \
"v_mov_b32 v60, 0\n" \
"v_mov_b32 v61, 0\n" \
"v_mov_b32 v62, 0\n" \
"v_mov_b32 v63, 0\n" \
"v_accvgpr_write_b32 a32, 0\n" \
"v_accvgpr_write_b32 a33, 0\n" \
"v_accvgpr_write_b32 a34, 0\n" \
"v_accvgpr_write_b32 a35, 0\n" \
"v_accvgpr_write_b32 a36, 0\n" \
"v_accvgpr_write_b32 a37, 0\n" \
"v_accvgpr_write_b32 a38, 0\n" \
"v_accvgpr_write_b32 a39, 0\n" \
"s_lshl1_add_u32 s14, s14, 1\n" \
"s_mov_b32 s15, s73\n" \
".Lslr0_%=:\n" \
"s_add_u32 s82, s15, 0\n" \
"buffer_load_dwordx4 a[64:67], v248, s[32:35], s82 offen offset:0 sc1\n" \
"buffer_load_dwordx4 a[68:71], v248, s[32:35], s82 offen offset:1024 sc1\n" \
"buffer_load_dwordx4 a[72:75], v248, s[32:35], s82 offen offset:2048 sc1\n" \
"buffer_load_dwordx4 a[76:79], v248, s[32:35], s82 offen offset:3072 sc1\n" \
"s_add_u32 s82, s15, 4096\n" \
"buffer_load_dwordx4 a[80:83], v248, s[32:35], s82 offen offset:0 sc1\n" \
"buffer_load_dwordx4 a[84:87], v248, s[32:35], s82 offen offset:1024 sc1\n" \
"buffer_load_dwordx4 a[88:91], v248, s[32:35], s82 offen offset:2048 sc1\n" \
"buffer_load_dwordx4 a[92:95], v248, s[32:35], s82 offen offset:3072 sc1\n" \
"s_add_u32 s82, s15, 8192\n" \
"buffer_load_dwordx4 a[96:99], v248, s[32:35], s82 offen offset:0 sc1\n" \
"buffer_load_dwordx4 a[100:103], v248, s[32:35], s82 offen offset:1024 sc1\n" \
"buffer_load_dwordx4 a[104:107], v248, s[32:35], s82 offen offset:2048 sc1\n" \
"buffer_load_dwordx4 a[108:111], v248, s[32:35], s82 offen offset:3072 sc1\n" \
"s_add_u32 s82, s15, 12288\n" \
"buffer_load_dwordx4 a[112:115], v248, s[32:35], s82 offen offset:0 sc1\n" \
"buffer_load_dwordx4 a[116:119], v248, s[32:35], s82 offen offset:1024 sc1\n" \
"buffer_load_dwordx4 a[120:123], v248, s[32:35], s82 offen offset:2048 sc1\n" \
"buffer_load_dwordx4 a[124:127], v248, s[32:35], s82 offen offset:3072 sc1\n" \
"s_waitcnt vmcnt(0)\n" \
"v_mfma_f32_16x16x32_bf16 v[128:131], a[64:67], v[192:195], 0\n" \
"v_mfma_f32_16x16x32_bf16 v[128:131], a[68:71], v[196:199], v[128:131]\n" \
"v_mfma_f32_16x16x32_bf16 v[128:131], a[72:75], v[200:203], v[128:131]\n" \
"v_mfma_f32_16x16x32_bf16 v[128:131], a[76:79], v[204:207], v[128:131]\n" \
"v_mfma_f32_16x16x32_bf16 v[132:135], a[92:95], v[204:207], 0\n" \
"v_mfma_f32_16x16x32_bf16 v[132:135], a[88:91], v[200:203], v[132:135]\n" \
"v_mfma_f32_16x16x32_bf16 v[132:135], a[84:87], v[196:199], v[132:135]\n" \
"v_mfma_f32_16x16x32_bf16 v[132:135], a[80:83], v[192:195], v[132:135]\n" \
"v_mfma_f32_16x16x32_bf16 v[136:139], a[96:99], v[192:195], 0\n" \
"v_mfma_f32_16x16x32_bf16 v[136:139], a[100:103], v[196:199], v[136:139]\n" \
"v_mfma_f32_16x16x32_bf16 v[136:139], a[104:107], v[200:203], v[136:139]\n" \
"v_mfma_f32_16x16x32_bf16 v[136:139], a[108:111], v[204:207], v[136:139]\n" \
"v_mfma_f32_16x16x32_bf16 v[140:143], a[124:127], v[204:207], 0\n" \
"v_mfma_f32_16x16x32_bf16 v[140:143], a[120:123], v[200:203], v[140:143]\n" \
"v_mfma_f32_16x16x32_bf16 v[140:143], a[116:119], v[196:199], v[140:143]\n" \
"v_mfma_f32_16x16x32_bf16 v[140:143], a[112:115], v[192:195], v[140:143]\n" \
"v_cmp_gt_i32 vcc, 0, v226\n" \
"s_nop 1\n" \
"v_cndmask_b32 v128, v128, v231, vcc\n" \
"v_cmp_gt_i32 vcc, 1, v226\n" \
"s_nop 1\n" \
"v_cndmask_b32 v129, v129, v231, vcc\n" \
"v_cmp_gt_i32 vcc, 2, v226\n" \
"s_nop 1\n" \
"v_cndmask_b32 v130, v130, v231, vcc\n" \
"v_cmp_gt_i32 vcc, 3, v226\n" \
"s_nop 1\n" \
"v_cndmask_b32 v131, v131, v231, vcc\n" \
"v_cmp_gt_i32 vcc, 16, v226\n" \
"s_nop 1\n" \
"v_cndmask_b32 v132, v132, v231, vcc\n" \
"v_cmp_gt_i32 vcc, 17, v226\n" \
"s_nop 1\n" \
"v_cndmask_b32 v133, v133, v231, vcc\n" \
"v_cmp_gt_i32 vcc, 18, v226\n" \
"s_nop 1\n" \
"v_cndmask_b32 v134, v134, v231, vcc\n" \
"v_cmp_gt_i32 vcc, 19, v226\n" \
"s_nop 1\n" \
"v_cndmask_b32 v135, v135, v231, vcc\n" \
"v_cmp_gt_i32 vcc, 32, v226\n" \
"s_nop 1\n" \
"v_cndmask_b32 v136, v136, v231, vcc\n" \
"v_cmp_gt_i32 vcc, 33, v226\n" \
"s_nop 1\n" \
"v_cndmask_b32 v137, v137, v231, vcc\n" \
"v_cmp_gt_i32 vcc, 34, v226\n" \
"s_nop 1\n" \
"v_cndmask_b32 v138, v138, v231, vcc\n" \
"v_cmp_gt_i32 vcc, 35, v226\n" \
"s_nop 1\n" \
"v_cndmask_b32 v139, v139, v231, vcc\n" \
"v_cmp_gt_i32 vcc, 48, v226\n" \
"s_nop 1\n" \
"v_cndmask_b32 v140, v140, v231, vcc\n" \
"v_cmp_gt_i32 vcc, 49, v226\n" \
"s_nop 1\n" \
"v_cndmask_b32 v141, v141, v231, vcc\n" \
"v_cmp_gt_i32 vcc, 50, v226\n" \
"s_nop 1\n" \
"v_cndmask_b32 v142, v142, v231, vcc\n" \
"v_cmp_gt_i32 vcc, 51, v226\n" \
"s_nop 1\n" \
"v_cndmask_b32 v143, v143, v231, vcc\n" \
"v_max3_f32 v228, v224, v128, v129\n" \
"v_max3_f32 v228, v228, v130, v131\n" \
"v_max3_f32 v228, v228, v132, v133\n" \
"v_max3_f32 v228, v228, v134, v135\n" \
"v_max3_f32 v228, v228, v136, v137\n" \
"v_max3_f32 v228, v228, v138, v139\n" \
"v_max3_f32 v228, v228, v140, v141\n" \
"v_max3_f32 v228, v228, v142, v143\n" \
"v_mov_b32 v229, v228\n" \
"s_nop 1\n" \
"v_permlane32_swap_b32 v228, v229\n" \
"v_max_f32 v228, v228, v229\n" \
"v_mov_b32 v229, v228\n" \
"s_nop 1\n" \
"v_permlane16_swap_b32 v228, v229\n" \
"v_max_f32 v228, v228, v229\n" \
"v_sub_f32 v230, v224, v228\n" \
"v_mov_b32 v224, v228\n" \
"v_exp_f32 v230, v230\n" \
"v_subrev_u32 v226, 64, v226\n" \
"s_add_u32 s82, s15, 589824\n" \
"buffer_load_dwordx4 a[64:67], v248, s[32:35], s82 offen offset:0 sc1\n" \
"buffer_load_dwordx4 a[68:71], v248, s[32:35], s82 offen offset:1024 sc1\n" \
"buffer_load_dwordx4 a[72:75], v248, s[32:35], s82 offen offset:2048 sc1\n" \
"buffer_load_dwordx4 a[76:79], v248, s[32:35], s82 offen offset:3072 sc1\n" \
"s_add_u32 s82, s15, 593920\n" \
"buffer_load_dwordx4 a[80:83], v248, s[32:35], s82 offen offset:0 sc1\n" \
"buffer_load_dwordx4 a[84:87], v248, s[32:35], s82 offen offset:1024 sc1\n" \
"buffer_load_dwordx4 a[88:91], v248, s[32:35], s82 offen offset:2048 sc1\n" \
"buffer_load_dwordx4 a[92:95], v248, s[32:35], s82 offen offset:3072 sc1\n" \
"s_add_u32 s82, s15, 598016\n" \
"buffer_load_dwordx4 a[96:99], v248, s[32:35], s82 offen offset:0 sc1\n" \
"buffer_load_dwordx4 a[100:103], v248, s[32:35], s82 offen offset:1024 sc1\n" \
"buffer_load_dwordx4 a[104:107], v248, s[32:35], s82 offen offset:2048 sc1\n" \
"buffer_load_dwordx4 a[108:111], v248, s[32:35], s82 offen offset:3072 sc1\n" \
"s_add_u32 s82, s15, 602112\n" \
"buffer_load_dwordx4 a[112:115], v248, s[32:35], s82 offen offset:0 sc1\n" \
"buffer_load_dwordx4 a[116:119], v248, s[32:35], s82 offen offset:1024 sc1\n" \
"buffer_load_dwordx4 a[120:123], v248, s[32:35], s82 offen offset:2048 sc1\n" \
"buffer_load_dwordx4 a[124:127], v248, s[32:35], s82 offen offset:3072 sc1\n" \
"s_waitcnt vmcnt(0)\n" \
"v_sub_f32 v128, v128, v228\n" \
"v_sub_f32 v129, v129, v228\n" \
"v_sub_f32 v130, v130, v228\n" \
"v_sub_f32 v131, v131, v228\n" \
"v_sub_f32 v132, v132, v228\n" \
"v_sub_f32 v133, v133, v228\n" \
"v_sub_f32 v134, v134, v228\n" \
"v_sub_f32 v135, v135, v228\n" \
"v_sub_f32 v136, v136, v228\n" \
"v_sub_f32 v137, v137, v228\n" \
"v_sub_f32 v138, v138, v228\n" \
"v_sub_f32 v139, v139, v228\n" \
"v_sub_f32 v140, v140, v228\n" \
"v_sub_f32 v141, v141, v228\n" \
"v_sub_f32 v142, v142, v228\n" \
"v_sub_f32 v143, v143, v228\n" \
"v_exp_f32 v128, v128\n" \
"v_exp_f32 v129, v129\n" \
"v_exp_f32 v130, v130\n" \
"v_exp_f32 v131, v131\n" \
"v_exp_f32 v132, v132\n" \
"v_exp_f32 v133, v133\n" \
"v_exp_f32 v134, v134\n" \
"v_exp_f32 v135, v135\n" \
"v_exp_f32 v136, v136\n" \
"v_exp_f32 v137, v137\n" \
"v_exp_f32 v138, v138\n" \
"v_exp_f32 v139, v139\n" \
"v_exp_f32 v140, v140\n" \
"v_exp_f32 v141, v141\n" \
"v_exp_f32 v142, v142\n" \
"v_exp_f32 v143, v143\n" \
"v_cvt_pk_bf16_f32 v128, v128, v129\n" \
"v_cvt_pk_bf16_f32 v129, v130, v131\n" \
"v_cvt_pk_bf16_f32 v130, v132, v133\n" \
"v_cvt_pk_bf16_f32 v131, v134, v135\n" \
"v_cvt_pk_bf16_f32 v136, v136, v137\n" \
"v_cvt_pk_bf16_f32 v137, v138, v139\n" \
"v_cvt_pk_bf16_f32 v138, v140, v141\n" \
"v_cvt_pk_bf16_f32 v139, v142, v143\n" \
"v_mul_f32 v0, v230, v0\n" \
"v_mul_f32 v1, v230, v1\n" \
"v_mul_f32 v2, v230, v2\n" \
"v_mul_f32 v3, v230, v3\n" \
"v_mul_f32 v4, v230, v4\n" \
"v_mul_f32 v5, v230, v5\n" \
"v_mul_f32 v6, v230, v6\n" \
"v_mul_f32 v7, v230, v7\n" \
"v_mul_f32 v8, v230, v8\n" \
"v_mul_f32 v9, v230, v9\n" \
"v_mul_f32 v10, v230, v10\n" \
"v_mul_f32 v11, v230, v11\n" \
"v_mul_f32 v12, v230, v12\n" \
"v_mul_f32 v13, v230, v13\n" \
"v_mul_f32 v14, v230, v14\n" \
"v_mul_f32 v15, v230, v15\n" \
"v_mul_f32 v16, v230, v16\n" \
"v_mul_f32 v17, v230, v17\n" \
"v_mul_f32 v18, v230, v18\n" \
"v_mul_f32 v19, v230, v19\n" \
"v_mul_f32 v20, v230, v20\n" \
"v_mul_f32 v21, v230, v21\n" \
"v_mul_f32 v22, v230, v22\n" \
"v_mul_f32 v23, v230, v23\n" \
"v_mul_f32 v24, v230, v24\n" \
"v_mul_f32 v25, v230, v25\n" \
"v_mul_f32 v26, v230, v26\n" \
"v_mul_f32 v27, v230, v27\n" \
"v_mul_f32 v28, v230, v28\n" \
"v_mul_f32 v29, v230, v29\n" \
"v_mul_f32 v30, v230, v30\n" \
"v_mul_f32 v31, v230, v31\n" \
"v_accvgpr_read_b32 v229, a32\n" \
"v_mul_f32 v229, v230, v229\n" \
"v_accvgpr_write_b32 a32, v229\n" \
"v_accvgpr_read_b32 v229, a33\n" \
"v_mul_f32 v229, v230, v229\n" \
"v_accvgpr_write_b32 a33, v229\n" \
"v_accvgpr_read_b32 v229, a34\n" \
"v_mul_f32 v229, v230, v229\n" \
"v_accvgpr_write_b32 a34, v229\n" \
"v_accvgpr_read_b32 v229, a35\n" \
"v_mul_f32 v229, v230, v229\n" \
"v_accvgpr_write_b32 a35, v229\n" \
"s_nop 1\n" \
"v_mfma_f32_16x16x32_bf16 a[32:35], a[48:51], v[128:131], a[32:35]\n" \
"v_mfma_f32_16x16x32_bf16 a[32:35], a[48:51], v[136:139], a[32:35]\n" \
"v_mfma_f32_16x16x32_bf16 v[0:3], a[96:99], v[136:139], v[0:3]\n" \
"v_mfma_f32_16x16x32_bf16 v[0:3], a[64:67], v[128:131], v[0:3]\n" \
"v_mfma_f32_16x16x32_bf16 v[4:7], a[68:71], v[128:131], v[4:7]\n" \
"v_mfma_f32_16x16x32_bf16 v[4:7], a[100:103], v[136:139], v[4:7]\n" \
"v_mfma_f32_16x16x32_bf16 v[8:11], a[104:107], v[136:139], v[8:11]\n" \
"v_mfma_f32_16x16x32_bf16 v[8:11], a[72:75], v[128:131], v[8:11]\n" \
"v_mfma_f32_16x16x32_bf16 v[12:15], a[76:79], v[128:131], v[12:15]\n" \
"v_mfma_f32_16x16x32_bf16 v[12:15], a[108:111], v[136:139], v[12:15]\n" \
"v_mfma_f32_16x16x32_bf16 v[16:19], a[112:115], v[136:139], v[16:19]\n" \
"v_mfma_f32_16x16x32_bf16 v[16:19], a[80:83], v[128:131], v[16:19]\n" \
"v_mfma_f32_16x16x32_bf16 v[20:23], a[84:87], v[128:131], v[20:23]\n" \
"v_mfma_f32_16x16x32_bf16 v[20:23], a[116:119], v[136:139], v[20:23]\n" \
"v_mfma_f32_16x16x32_bf16 v[24:27], a[120:123], v[136:139], v[24:27]\n" \
"v_mfma_f32_16x16x32_bf16 v[24:27], a[88:91], v[128:131], v[24:27]\n" \
"v_mfma_f32_16x16x32_bf16 v[28:31], a[92:95], v[128:131], v[28:31]\n" \
"v_mfma_f32_16x16x32_bf16 v[28:31], a[124:127], v[136:139], v[28:31]\n" \
"s_nop 7\n" \
"s_add_u32 s82, s15, 0\n" \
"buffer_load_dwordx4 a[64:67], v248, s[32:35], s82 offen offset:0 sc1\n" \
"buffer_load_dwordx4 a[68:71], v248, s[32:35], s82 offen offset:1024 sc1\n" \
"buffer_load_dwordx4 a[72:75], v248, s[32:35], s82 offen offset:2048 sc1\n" \
"buffer_load_dwordx4 a[76:79], v248, s[32:35], s82 offen offset:3072 sc1\n" \
"s_add_u32 s82, s15, 4096\n" \
"buffer_load_dwordx4 a[80:83], v248, s[32:35], s82 offen offset:0 sc1\n" \
"buffer_load_dwordx4 a[84:87], v248, s[32:35], s82 offen offset:1024 sc1\n" \
"buffer_load_dwordx4 a[88:91], v248, s[32:35], s82 offen offset:2048 sc1\n" \
"buffer_load_dwordx4 a[92:95], v248, s[32:35], s82 offen offset:3072 sc1\n" \
"s_add_u32 s82, s15, 8192\n" \
"buffer_load_dwordx4 a[96:99], v248, s[32:35], s82 offen offset:0 sc1\n" \
"buffer_load_dwordx4 a[100:103], v248, s[32:35], s82 offen offset:1024 sc1\n" \
"buffer_load_dwordx4 a[104:107], v248, s[32:35], s82 offen offset:2048 sc1\n" \
"buffer_load_dwordx4 a[108:111], v248, s[32:35], s82 offen offset:3072 sc1\n" \
"s_add_u32 s82, s15, 12288\n" \
"buffer_load_dwordx4 a[112:115], v248, s[32:35], s82 offen offset:0 sc1\n" \
"buffer_load_dwordx4 a[116:119], v248, s[32:35], s82 offen offset:1024 sc1\n" \
"buffer_load_dwordx4 a[120:123], v248, s[32:35], s82 offen offset:2048 sc1\n" \
"buffer_load_dwordx4 a[124:127], v248, s[32:35], s82 offen offset:3072 sc1\n" \
"s_waitcnt vmcnt(0)\n" \
"v_mfma_f32_16x16x32_bf16 v[128:131], a[64:67], v[208:211], 0\n" \
"v_mfma_f32_16x16x32_bf16 v[128:131], a[68:71], v[212:215], v[128:131]\n" \
"v_mfma_f32_16x16x32_bf16 v[128:131], a[72:75], v[216:219], v[128:131]\n" \
"v_mfma_f32_16x16x32_bf16 v[128:131], a[76:79], v[220:223], v[128:131]\n" \
"v_mfma_f32_16x16x32_bf16 v[132:135], a[92:95], v[220:223], 0\n" \
"v_mfma_f32_16x16x32_bf16 v[132:135], a[88:91], v[216:219], v[132:135]\n" \
"v_mfma_f32_16x16x32_bf16 v[132:135], a[84:87], v[212:215], v[132:135]\n" \
"v_mfma_f32_16x16x32_bf16 v[132:135], a[80:83], v[208:211], v[132:135]\n" \
"v_mfma_f32_16x16x32_bf16 v[136:139], a[96:99], v[208:211], 0\n" \
"v_mfma_f32_16x16x32_bf16 v[136:139], a[100:103], v[212:215], v[136:139]\n" \
"v_mfma_f32_16x16x32_bf16 v[136:139], a[104:107], v[216:219], v[136:139]\n" \
"v_mfma_f32_16x16x32_bf16 v[136:139], a[108:111], v[220:223], v[136:139]\n" \
"v_mfma_f32_16x16x32_bf16 v[140:143], a[124:127], v[220:223], 0\n" \
"v_mfma_f32_16x16x32_bf16 v[140:143], a[120:123], v[216:219], v[140:143]\n" \
"v_mfma_f32_16x16x32_bf16 v[140:143], a[116:119], v[212:215], v[140:143]\n" \
"v_mfma_f32_16x16x32_bf16 v[140:143], a[112:115], v[208:211], v[140:143]\n" \
"v_cmp_gt_i32 vcc, 0, v227\n" \
"s_nop 1\n" \
"v_cndmask_b32 v128, v128, v231, vcc\n" \
"v_cmp_gt_i32 vcc, 1, v227\n" \
"s_nop 1\n" \
"v_cndmask_b32 v129, v129, v231, vcc\n" \
"v_cmp_gt_i32 vcc, 2, v227\n" \
"s_nop 1\n" \
"v_cndmask_b32 v130, v130, v231, vcc\n" \
"v_cmp_gt_i32 vcc, 3, v227\n" \
"s_nop 1\n" \
"v_cndmask_b32 v131, v131, v231, vcc\n" \
"v_cmp_gt_i32 vcc, 16, v227\n" \
"s_nop 1\n" \
"v_cndmask_b32 v132, v132, v231, vcc\n" \
"v_cmp_gt_i32 vcc, 17, v227\n" \
"s_nop 1\n" \
"v_cndmask_b32 v133, v133, v231, vcc\n" \
"v_cmp_gt_i32 vcc, 18, v227\n" \
"s_nop 1\n" \
"v_cndmask_b32 v134, v134, v231, vcc\n" \
"v_cmp_gt_i32 vcc, 19, v227\n" \
"s_nop 1\n" \
"v_cndmask_b32 v135, v135, v231, vcc\n" \
"v_cmp_gt_i32 vcc, 32, v227\n" \
"s_nop 1\n" \
"v_cndmask_b32 v136, v136, v231, vcc\n" \
"v_cmp_gt_i32 vcc, 33, v227\n" \
"s_nop 1\n" \
"v_cndmask_b32 v137, v137, v231, vcc\n" \
"v_cmp_gt_i32 vcc, 34, v227\n" \
"s_nop 1\n" \
"v_cndmask_b32 v138, v138, v231, vcc\n" \
"v_cmp_gt_i32 vcc, 35, v227\n" \
"s_nop 1\n" \
"v_cndmask_b32 v139, v139, v231, vcc\n" \
"v_cmp_gt_i32 vcc, 48, v227\n" \
"s_nop 1\n" \
"v_cndmask_b32 v140, v140, v231, vcc\n" \
"v_cmp_gt_i32 vcc, 49, v227\n" \
"s_nop 1\n" \
"v_cndmask_b32 v141, v141, v231, vcc\n" \
"v_cmp_gt_i32 vcc, 50, v227\n" \
"s_nop 1\n" \
"v_cndmask_b32 v142, v142, v231, vcc\n" \
"v_cmp_gt_i32 vcc, 51, v227\n" \
"s_nop 1\n" \
"v_cndmask_b32 v143, v143, v231, vcc\n" \
"v_max3_f32 v228, v225, v128, v129\n" \
"v_max3_f32 v228, v228, v130, v131\n" \
"v_max3_f32 v228, v228, v132, v133\n" \
"v_max3_f32 v228, v228, v134, v135\n" \
"v_max3_f32 v228, v228, v136, v137\n" \
"v_max3_f32 v228, v228, v138, v139\n" \
"v_max3_f32 v228, v228, v140, v141\n" \
"v_max3_f32 v228, v228, v142, v143\n" \
"v_mov_b32 v229, v228\n" \
"s_nop 1\n" \
"v_permlane32_swap_b32 v228, v229\n" \
"v_max_f32 v228, v228, v229\n" \
"v_mov_b32 v229, v228\n" \
"s_nop 1\n" \
"v_permlane16_swap_b32 v228, v229\n" \
"v_max_f32 v228, v228, v229\n" \
"v_sub_f32 v230, v225, v228\n" \
"v_mov_b32 v225, v228\n" \
"v_exp_f32 v230, v230\n" \
"v_subrev_u32 v227, 64, v227\n" \
"s_add_u32 s82, s15, 589824\n" \
"buffer_load_dwordx4 a[64:67], v248, s[32:35], s82 offen offset:0 sc1\n" \
"buffer_load_dwordx4 a[68:71], v248, s[32:35], s82 offen offset:1024 sc1\n" \
"buffer_load_dwordx4 a[72:75], v248, s[32:35], s82 offen offset:2048 sc1\n" \
"buffer_load_dwordx4 a[76:79], v248, s[32:35], s82 offen offset:3072 sc1\n" \
"s_add_u32 s82, s15, 593920\n" \
"buffer_load_dwordx4 a[80:83], v248, s[32:35], s82 offen offset:0 sc1\n" \
"buffer_load_dwordx4 a[84:87], v248, s[32:35], s82 offen offset:1024 sc1\n" \
"buffer_load_dwordx4 a[88:91], v248, s[32:35], s82 offen offset:2048 sc1\n" \
"buffer_load_dwordx4 a[92:95], v248, s[32:35], s82 offen offset:3072 sc1\n" \
"s_add_u32 s82, s15, 598016\n" \
"buffer_load_dwordx4 a[96:99], v248, s[32:35], s82 offen offset:0 sc1\n" \
"buffer_load_dwordx4 a[100:103], v248, s[32:35], s82 offen offset:1024 sc1\n" \
"buffer_load_dwordx4 a[104:107], v248, s[32:35], s82 offen offset:2048 sc1\n" \
"buffer_load_dwordx4 a[108:111], v248, s[32:35], s82 offen offset:3072 sc1\n" \
"s_add_u32 s82, s15, 602112\n" \
"buffer_load_dwordx4 a[112:115], v248, s[32:35], s82 offen offset:0 sc1\n" \
"buffer_load_dwordx4 a[116:119], v248, s[32:35], s82 offen offset:1024 sc1\n" \
"buffer_load_dwordx4 a[120:123], v248, s[32:35], s82 offen offset:2048 sc1\n" \
"buffer_load_dwordx4 a[124:127], v248, s[32:35], s82 offen offset:3072 sc1\n" \
"s_waitcnt vmcnt(0)\n" \
"v_sub_f32 v128, v128, v228\n" \
"v_sub_f32 v129, v129, v228\n" \
"v_sub_f32 v130, v130, v228\n" \
"v_sub_f32 v131, v131, v228\n" \
"v_sub_f32 v132, v132, v228\n" \
"v_sub_f32 v133, v133, v228\n" \
"v_sub_f32 v134, v134, v228\n" \
"v_sub_f32 v135, v135, v228\n" \
"v_sub_f32 v136, v136, v228\n" \
"v_sub_f32 v137, v137, v228\n" \
"v_sub_f32 v138, v138, v228\n" \
"v_sub_f32 v139, v139, v228\n" \
"v_sub_f32 v140, v140, v228\n" \
"v_sub_f32 v141, v141, v228\n" \
"v_sub_f32 v142, v142, v228\n" \
"v_sub_f32 v143, v143, v228\n" \
"v_exp_f32 v128, v128\n" \
"v_exp_f32 v129, v129\n" \
"v_exp_f32 v130, v130\n" \
"v_exp_f32 v131, v131\n" \
"v_exp_f32 v132, v132\n" \
"v_exp_f32 v133, v133\n" \
"v_exp_f32 v134, v134\n" \
"v_exp_f32 v135, v135\n" \
"v_exp_f32 v136, v136\n" \
"v_exp_f32 v137, v137\n" \
"v_exp_f32 v138, v138\n" \
"v_exp_f32 v139, v139\n" \
"v_exp_f32 v140, v140\n" \
"v_exp_f32 v141, v141\n" \
"v_exp_f32 v142, v142\n" \
"v_exp_f32 v143, v143\n" \
"v_cvt_pk_bf16_f32 v128, v128, v129\n" \
"v_cvt_pk_bf16_f32 v129, v130, v131\n" \
"v_cvt_pk_bf16_f32 v130, v132, v133\n" \
"v_cvt_pk_bf16_f32 v131, v134, v135\n" \
"v_cvt_pk_bf16_f32 v136, v136, v137\n" \
"v_cvt_pk_bf16_f32 v137, v138, v139\n" \
"v_cvt_pk_bf16_f32 v138, v140, v141\n" \
"v_cvt_pk_bf16_f32 v139, v142, v143\n" \
"v_mul_f32 v32, v230, v32\n" \
"v_mul_f32 v33, v230, v33\n" \
"v_mul_f32 v34, v230, v34\n" \
"v_mul_f32 v35, v230, v35\n" \
"v_mul_f32 v36, v230, v36\n" \
"v_mul_f32 v37, v230, v37\n" \
"v_mul_f32 v38, v230, v38\n" \
"v_mul_f32 v39, v230, v39\n" \
"v_mul_f32 v40, v230, v40\n" \
"v_mul_f32 v41, v230, v41\n" \
"v_mul_f32 v42, v230, v42\n" \
"v_mul_f32 v43, v230, v43\n" \
"v_mul_f32 v44, v230, v44\n" \
"v_mul_f32 v45, v230, v45\n" \
"v_mul_f32 v46, v230, v46\n" \
"v_mul_f32 v47, v230, v47\n" \
"v_mul_f32 v48, v230, v48\n" \
"v_mul_f32 v49, v230, v49\n" \
"v_mul_f32 v50, v230, v50\n" \
"v_mul_f32 v51, v230, v51\n" \
"v_mul_f32 v52, v230, v52\n" \
"v_mul_f32 v53, v230, v53\n" \
"v_mul_f32 v54, v230, v54\n" \
"v_mul_f32 v55, v230, v55\n" \
"v_mul_f32 v56, v230, v56\n" \
"v_mul_f32 v57, v230, v57\n" \
"v_mul_f32 v58, v230, v58\n" \
"v_mul_f32 v59, v230, v59\n" \
"v_mul_f32 v60, v230, v60\n" \
"v_mul_f32 v61, v230, v61\n" \
"v_mul_f32 v62, v230, v62\n" \
"v_mul_f32 v63, v230, v63\n" \
"v_accvgpr_read_b32 v229, a36\n" \
"v_mul_f32 v229, v230, v229\n" \
"v_accvgpr_write_b32 a36, v229\n" \
"v_accvgpr_read_b32 v229, a37\n" \
"v_mul_f32 v229, v230, v229\n" \
"v_accvgpr_write_b32 a37, v229\n" \
"v_accvgpr_read_b32 v229, a38\n" \
"v_mul_f32 v229, v230, v229\n" \
"v_accvgpr_write_b32 a38, v229\n" \
"v_accvgpr_read_b32 v229, a39\n" \
"v_mul_f32 v229, v230, v229\n" \
"v_accvgpr_write_b32 a39, v229\n" \
"s_nop 1\n" \
"v_mfma_f32_16x16x32_bf16 a[36:39], a[48:51], v[128:131], a[36:39]\n" \
"v_mfma_f32_16x16x32_bf16 a[36:39], a[48:51], v[136:139], a[36:39]\n" \
"v_mfma_f32_16x16x32_bf16 v[32:35], a[96:99], v[136:139], v[32:35]\n" \
"v_mfma_f32_16x16x32_bf16 v[32:35], a[64:67], v[128:131], v[32:35]\n" \
"v_mfma_f32_16x16x32_bf16 v[36:39], a[68:71], v[128:131], v[36:39]\n" \
"v_mfma_f32_16x16x32_bf16 v[36:39], a[100:103], v[136:139], v[36:39]\n" \
"v_mfma_f32_16x16x32_bf16 v[40:43], a[104:107], v[136:139], v[40:43]\n" \
"v_mfma_f32_16x16x32_bf16 v[40:43], a[72:75], v[128:131], v[40:43]\n" \
"v_mfma_f32_16x16x32_bf16 v[44:47], a[76:79], v[128:131], v[44:47]\n" \
"v_mfma_f32_16x16x32_bf16 v[44:47], a[108:111], v[136:139], v[44:47]\n" \
"v_mfma_f32_16x16x32_bf16 v[48:51], a[112:115], v[136:139], v[48:51]\n" \
"v_mfma_f32_16x16x32_bf16 v[48:51], a[80:83], v[128:131], v[48:51]\n" \
"v_mfma_f32_16x16x32_bf16 v[52:55], a[84:87], v[128:131], v[52:55]\n" \
"v_mfma_f32_16x16x32_bf16 v[52:55], a[116:119], v[136:139], v[52:55]\n" \
"v_mfma_f32_16x16x32_bf16 v[56:59], a[120:123], v[136:139], v[56:59]\n" \
"v_mfma_f32_16x16x32_bf16 v[56:59], a[88:91], v[128:131], v[56:59]\n" \
"v_mfma_f32_16x16x32_bf16 v[60:63], a[92:95], v[128:131], v[60:63]\n" \
"v_mfma_f32_16x16x32_bf16 v[60:63], a[124:127], v[136:139], v[60:63]\n" \
"s_nop 7\n" \
"s_add_u32 s15, s15, 0x4000\n" \
"s_sub_u32 s14, s14, 1\n" \
"s_cbranch_scc0 .Lslr0_%=\n" \
"s_nop 7\n" \
"s_setpc_b64 s[98:99]\n" \
  \
".Lredo2_%=:\n" \
"s_nop 7\n" \
"v_and_b32 v196, 31, v251\n" \
"v_lshlrev_b32 v196, 2, v196\n" \
"s_branch .Lspr2_%=\n" \
".Lsbr2_%=:\n" \
"v_and_b32 v196, 15, v251\n" \
"v_lshrrev_b32 v197, 4, v251\n" \
"v_lshlrev_b32 v197, 2, v197\n" \
"v_sub_u32 v196, v196, v197\n" \
"v_mov_b32 v199, 0xff800000\n" \
"s_sub_u32 s14, 15, s67\n" \
"s_xor_b32 s15, %[wid], 0\n" \
"s_lshl3_add_u32 s15, s14, s15\n" \
"s_lshl_b32 s15, s15, 4\n" \
"v_add_u32 v194, s15, v196\n" \
"v_mov_b32 v192, 0xff7fffff\n" \
"s_xor_b32 s15, %[wid], 7\n" \
"s_lshl3_add_u32 s15, s14, s15\n" \
"s_lshl_b32 s15, s15, 4\n" \
"v_add_u32 v195, s15, v196\n" \
"v_mov_b32 v193, 0xff7fffff\n" \
"v_mov_b32 v64, 0\n" \
"v_mov_b32 v65, 0\n" \
"v_mov_b32 v66, 0\n" \
"v_mov_b32 v67, 0\n" \
"v_mov_b32 v68, 0\n" \
"v_mov_b32 v69, 0\n" \
"v_mov_b32 v70, 0\n" \
"v_mov_b32 v71, 0\n" \
"v_mov_b32 v72, 0\n" \
"v_mov_b32 v73, 0\n" \
"v_mov_b32 v74, 0\n" \
"v_mov_b32 v75, 0\n" \
"v_mov_b32 v76, 0\n" \
"v_mov_b32 v77, 0\n" \
"v_mov_b32 v78, 0\n" \
"v_mov_b32 v79, 0\n" \
"v_mov_b32 v80, 0\n" \
"v_mov_b32 v81, 0\n" \
"v_mov_b32 v82, 0\n" \
"v_mov_b32 v83, 0\n" \
"v_mov_b32 v84, 0\n" \
"v_mov_b32 v85, 0\n" \
"v_mov_b32 v86, 0\n" \
"v_mov_b32 v87, 0\n" \
"v_mov_b32 v88, 0\n" \
"v_mov_b32 v89, 0\n" \
"v_mov_b32 v90, 0\n" \
"v_mov_b32 v91, 0\n" \
"v_mov_b32 v92, 0\n" \
"v_mov_b32 v93, 0\n" \
"v_mov_b32 v94, 0\n" \
"v_mov_b32 v95, 0\n" \
"v_mov_b32 v96, 0\n" \
"v_mov_b32 v97, 0\n" \
"v_mov_b32 v98, 0\n" \
"v_mov_b32 v99, 0\n" \
"v_mov_b32 v100, 0\n" \
"v_mov_b32 v101, 0\n" \
"v_mov_b32 v102, 0\n" \
"v_mov_b32 v103, 0\n" \
"v_mov_b32 v104, 0\n" \
"v_mov_b32 v105, 0\n" \
"v_mov_b32 v106, 0\n" \
"v_mov_b32 v107, 0\n" \
"v_mov_b32 v108, 0\n" \
"v_mov_b32 v109, 0\n" \
"v_mov_b32 v110, 0\n" \
"v_mov_b32 v111, 0\n" \
"v_mov_b32 v112, 0\n" \
"v_mov_b32 v113, 0\n" \
"v_mov_b32 v114, 0\n" \
"v_mov_b32 v115, 0\n" \
"v_mov_b32 v116, 0\n" \
"v_mov_b32 v117, 0\n" \
"v_mov_b32 v118, 0\n" \
"v_mov_b32 v119, 0\n" \
"v_mov_b32 v120, 0\n" \
"v_mov_b32 v121, 0\n" \
"v_mov_b32 v122, 0\n" \
"v_mov_b32 v123, 0\n" \
"v_mov_b32 v124, 0\n" \
"v_mov_b32 v125, 0\n" \
"v_mov_b32 v126, 0\n" \
"v_mov_b32 v127, 0\n" \
"v_accvgpr_write_b32 a40, 0\n" \
"v_accvgpr_write_b32 a41, 0\n" \
"v_accvgpr_write_b32 a42, 0\n" \
"v_accvgpr_write_b32 a43, 0\n" \
"v_accvgpr_write_b32 a44, 0\n" \
"v_accvgpr_write_b32 a45, 0\n" \
"v_accvgpr_write_b32 a46, 0\n" \
"v_accvgpr_write_b32 a47, 0\n" \
"s_lshl1_add_u32 s14, s14, 1\n" \
"s_mov_b32 s15, s73\n" \
".Lslr2_%=:\n" \
"s_add_u32 s82, s15, 0\n" \
"buffer_load_dwordx4 a[128:131], v248, s[32:35], s82 offen offset:0 sc1\n" \
"buffer_load_dwordx4 a[132:135], v248, s[32:35], s82 offen offset:1024 sc1\n" \
"buffer_load_dwordx4 a[136:139], v248, s[32:35], s82 offen offset:2048 sc1\n" \
"buffer_load_dwordx4 a[140:143], v248, s[32:35], s82 offen offset:3072 sc1\n" \
"s_add_u32 s82, s15, 4096\n" \
"buffer_load_dwordx4 a[144:147], v248, s[32:35], s82 offen offset:0 sc1\n" \
"buffer_load_dwordx4 a[148:151], v248, s[32:35], s82 offen offset:1024 sc1\n" \
"buffer_load_dwordx4 a[152:155], v248, s[32:35], s82 offen offset:2048 sc1\n" \
"buffer_load_dwordx4 a[156:159], v248, s[32:35], s82 offen offset:3072 sc1\n" \
"s_add_u32 s82, s15, 8192\n" \
"buffer_load_dwordx4 a[160:163], v248, s[32:35], s82 offen offset:0 sc1\n" \
"buffer_load_dwordx4 a[164:167], v248, s[32:35], s82 offen offset:1024 sc1\n" \
"buffer_load_dwordx4 a[168:171], v248, s[32:35], s82 offen offset:2048 sc1\n" \
"buffer_load_dwordx4 a[172:175], v248, s[32:35], s82 offen offset:3072 sc1\n" \
"s_add_u32 s82, s15, 12288\n" \
"buffer_load_dwordx4 a[176:179], v248, s[32:35], s82 offen offset:0 sc1\n" \
"buffer_load_dwordx4 a[180:183], v248, s[32:35], s82 offen offset:1024 sc1\n" \
"buffer_load_dwordx4 a[184:187], v248, s[32:35], s82 offen offset:2048 sc1\n" \
"buffer_load_dwordx4 a[188:191], v248, s[32:35], s82 offen offset:3072 sc1\n" \
"s_waitcnt vmcnt(0)\n" \
"v_mfma_f32_16x16x32_bf16 v[128:131], a[128:131], v[160:163], 0\n" \
"v_mfma_f32_16x16x32_bf16 v[128:131], a[132:135], v[164:167], v[128:131]\n" \
"v_mfma_f32_16x16x32_bf16 v[128:131], a[136:139], v[168:171], v[128:131]\n" \
"v_mfma_f32_16x16x32_bf16 v[128:131], a[140:143], v[172:175], v[128:131]\n" \
"v_mfma_f32_16x16x32_bf16 v[132:135], a[156:159], v[172:175], 0\n" \
"v_mfma_f32_16x16x32_bf16 v[132:135], a[152:155], v[168:171], v[132:135]\n" \
"v_mfma_f32_16x16x32_bf16 v[132:135], a[148:151], v[164:167], v[132:135]\n" \
"v_mfma_f32_16x16x32_bf16 v[132:135], a[144:147], v[160:163], v[132:135]\n" \
"v_mfma_f32_16x16x32_bf16 v[136:139], a[160:163], v[160:163], 0\n" \
"v_mfma_f32_16x16x32_bf16 v[136:139], a[164:167], v[164:167], v[136:139]\n" \
"v_mfma_f32_16x16x32_bf16 v[136:139], a[168:171], v[168:171], v[136:139]\n" \
"v_mfma_f32_16x16x32_bf16 v[136:139], a[172:175], v[172:175], v[136:139]\n" \
"v_mfma_f32_16x16x32_bf16 v[140:143], a[188:191], v[172:175], 0\n" \
"v_mfma_f32_16x16x32_bf16 v[140:143], a[184:187], v[168:171], v[140:143]\n" \
"v_mfma_f32_16x16x32_bf16 v[140:143], a[180:183], v[164:167], v[140:143]\n" \
"v_mfma_f32_16x16x32_bf16 v[140:143], a[176:179], v[160:163], v[140:143]\n" \
"v_cmp_gt_i32 vcc, 0, v194\n" \
"s_nop 1\n" \
"v_cndmask_b32 v128, v128, v199, vcc\n" \
"v_cmp_gt_i32 vcc, 1, v194\n" \
"s_nop 1\n" \
"v_cndmask_b32 v129, v129, v199, vcc\n" \
"v_cmp_gt_i32 vcc, 2, v194\n" \
"s_nop 1\n" \
"v_cndmask_b32 v130, v130, v199, vcc\n" \
"v_cmp_gt_i32 vcc, 3, v194\n" \
"s_nop 1\n" \
"v_cndmask_b32 v131, v131, v199, vcc\n" \
"v_cmp_gt_i32 vcc, 16, v194\n" \
"s_nop 1\n" \
"v_cndmask_b32 v132, v132, v199, vcc\n" \
"v_cmp_gt_i32 vcc, 17, v194\n" \
"s_nop 1\n" \
"v_cndmask_b32 v133, v133, v199, vcc\n" \
"v_cmp_gt_i32 vcc, 18, v194\n" \
"s_nop 1\n" \
"v_cndmask_b32 v134, v134, v199, vcc\n" \
"v_cmp_gt_i32 vcc, 19, v194\n" \
"s_nop 1\n" \
"v_cndmask_b32 v135, v135, v199, vcc\n" \
"v_cmp_gt_i32 vcc, 32, v194\n" \
"s_nop 1\n" \
"v_cndmask_b32 v136, v136, v199, vcc\n" \
"v_cmp_gt_i32 vcc, 33, v194\n" \
"s_nop 1\n" \
"v_cndmask_b32 v137, v137, v199, vcc\n" \
"v_cmp_gt_i32 vcc, 34, v194\n" \
"s_nop 1\n" \
"v_cndmask_b32 v138, v138, v199, vcc\n" \
"v_cmp_gt_i32 vcc, 35, v194\n" \
"s_nop 1\n" \
"v_cndmask_b32 v139, v139, v199, vcc\n" \
"v_cmp_gt_i32 vcc, 48, v194\n" \
"s_nop 1\n" \
"v_cndmask_b32 v140, v140, v199, vcc\n" \
"v_cmp_gt_i32 vcc, 49, v194\n" \
"s_nop 1\n" \
"v_cndmask_b32 v141, v141, v199, vcc\n" \
"v_cmp_gt_i32 vcc, 50, v194\n" \
"s_nop 1\n" \
"v_cndmask_b32 v142, v142, v199, vcc\n" \
"v_cmp_gt_i32 vcc, 51, v194\n" \
"s_nop 1\n" \
"v_cndmask_b32 v143, v143, v199, vcc\n" \
"v_max3_f32 v196, v192, v128, v129\n" \
"v_max3_f32 v196, v196, v130, v131\n" \
"v_max3_f32 v196, v196, v132, v133\n" \
"v_max3_f32 v196, v196, v134, v135\n" \
"v_max3_f32 v196, v196, v136, v137\n" \
"v_max3_f32 v196, v196, v138, v139\n" \
"v_max3_f32 v196, v196, v140, v141\n" \
"v_max3_f32 v196, v196, v142, v143\n" \
"v_mov_b32 v197, v196\n" \
"s_nop 1\n" \
"v_permlane32_swap_b32 v196, v197\n" \
"v_max_f32 v196, v196, v197\n" \
"v_mov_b32 v197, v196\n" \
"s_nop 1\n" \
"v_permlane16_swap_b32 v196, v197\n" \
"v_max_f32 v196, v196, v197\n" \
"v_sub_f32 v198, v192, v196\n" \
"v_mov_b32 v192, v196\n" \
"v_exp_f32 v198, v198\n" \
"v_subrev_u32 v194, 64, v194\n" \
"s_add_u32 s82, s15, 589824\n" \
"buffer_load_dwordx4 a[192:195], v248, s[32:35], s82 offen offset:0 sc1\n" \
"buffer_load_dwordx4 a[196:199], v248, s[32:35], s82 offen offset:1024 sc1\n" \
"buffer_load_dwordx4 a[200:203], v248, s[32:35], s82 offen offset:2048 sc1\n" \
"buffer_load_dwordx4 a[204:207], v248, s[32:35], s82 offen offset:3072 sc1\n" \
"s_add_u32 s82, s15, 593920\n" \
"buffer_load_dwordx4 a[208:211], v248, s[32:35], s82 offen offset:0 sc1\n" \
"buffer_load_dwordx4 a[212:215], v248, s[32:35], s82 offen offset:1024 sc1\n" \
"buffer_load_dwordx4 a[216:219], v248, s[32:35], s82 offen offset:2048 sc1\n" \
"buffer_load_dwordx4 a[220:223], v248, s[32:35], s82 offen offset:3072 sc1\n" \
"s_add_u32 s82, s15, 598016\n" \
"buffer_load_dwordx4 a[224:227], v248, s[32:35], s82 offen offset:0 sc1\n" \
"buffer_load_dwordx4 a[228:231], v248, s[32:35], s82 offen offset:1024 sc1\n" \
"buffer_load_dwordx4 a[232:235], v248, s[32:35], s82 offen offset:2048 sc1\n" \
"buffer_load_dwordx4 a[236:239], v248, s[32:35], s82 offen offset:3072 sc1\n" \
"s_add_u32 s82, s15, 602112\n" \
"buffer_load_dwordx4 a[240:243], v248, s[32:35], s82 offen offset:0 sc1\n" \
"buffer_load_dwordx4 a[244:247], v248, s[32:35], s82 offen offset:1024 sc1\n" \
"buffer_load_dwordx4 a[248:251], v248, s[32:35], s82 offen offset:2048 sc1\n" \
"buffer_load_dwordx4 a[252:255], v248, s[32:35], s82 offen offset:3072 sc1\n" \
"s_waitcnt vmcnt(0)\n" \
"v_sub_f32 v128, v128, v196\n" \
"v_sub_f32 v129, v129, v196\n" \
"v_sub_f32 v130, v130, v196\n" \
"v_sub_f32 v131, v131, v196\n" \
"v_sub_f32 v132, v132, v196\n" \
"v_sub_f32 v133, v133, v196\n" \
"v_sub_f32 v134, v134, v196\n" \
"v_sub_f32 v135, v135, v196\n" \
"v_sub_f32 v136, v136, v196\n" \
"v_sub_f32 v137, v137, v196\n" \
"v_sub_f32 v138, v138, v196\n" \
"v_sub_f32 v139, v139, v196\n" \
"v_sub_f32 v140, v140, v196\n" \
"v_sub_f32 v141, v141, v196\n" \
"v_sub_f32 v142, v142, v196\n" \
"v_sub_f32 v143, v143, v196\n" \
"v_exp_f32 v128, v128\n" \
"v_exp_f32 v129, v129\n" \
"v_exp_f32 v130, v130\n" \
"v_exp_f32 v131, v131\n" \
"v_exp_f32 v132, v132\n" \
"v_exp_f32 v133, v133\n" \
"v_exp_f32 v134, v134\n" \
"v_exp_f32 v135, v135\n" \
"v_exp_f32 v136, v136\n" \
"v_exp_f32 v137, v137\n" \
"v_exp_f32 v138, v138\n" \
"v_exp_f32 v139, v139\n" \
"v_exp_f32 v140, v140\n" \
"v_exp_f32 v141, v141\n" \
"v_exp_f32 v142, v142\n" \
"v_exp_f32 v143, v143\n" \
"v_cvt_pk_bf16_f32 v128, v128, v129\n" \
"v_cvt_pk_bf16_f32 v129, v130, v131\n" \
"v_cvt_pk_bf16_f32 v130, v132, v133\n" \
"v_cvt_pk_bf16_f32 v131, v134, v135\n" \
"v_cvt_pk_bf16_f32 v136, v136, v137\n" \
"v_cvt_pk_bf16_f32 v137, v138, v139\n" \
"v_cvt_pk_bf16_f32 v138, v140, v141\n" \
"v_cvt_pk_bf16_f32 v139, v142, v143\n" \
"v_mul_f32 v64, v198, v64\n" \
"v_mul_f32 v65, v198, v65\n" \
"v_mul_f32 v66, v198, v66\n" \
"v_mul_f32 v67, v198, v67\n" \
"v_mul_f32 v68, v198, v68\n" \
"v_mul_f32 v69, v198, v69\n" \
"v_mul_f32 v70, v198, v70\n" \
"v_mul_f32 v71, v198, v71\n" \
"v_mul_f32 v72, v198, v72\n" \
"v_mul_f32 v73, v198, v73\n" \
"v_mul_f32 v74, v198, v74\n" \
"v_mul_f32 v75, v198, v75\n" \
"v_mul_f32 v76, v198, v76\n" \
"v_mul_f32 v77, v198, v77\n" \
"v_mul_f32 v78, v198, v78\n" \
"v_mul_f32 v79, v198, v79\n" \
"v_mul_f32 v80, v198, v80\n" \
"v_mul_f32 v81, v198, v81\n" \
"v_mul_f32 v82, v198, v82\n" \
"v_mul_f32 v83, v198, v83\n" \
"v_mul_f32 v84, v198, v84\n" \
"v_mul_f32 v85, v198, v85\n" \
"v_mul_f32 v86, v198, v86\n" \
"v_mul_f32 v87, v198, v87\n" \
"v_mul_f32 v88, v198, v88\n" \
"v_mul_f32 v89, v198, v89\n" \
"v_mul_f32 v90, v198, v90\n" \
"v_mul_f32 v91, v198, v91\n" \
"v_mul_f32 v92, v198, v92\n" \
"v_mul_f32 v93, v198, v93\n" \
"v_mul_f32 v94, v198, v94\n" \
"v_mul_f32 v95, v198, v95\n" \
"v_accvgpr_read_b32 v197, a40\n" \
"v_mul_f32 v197, v198, v197\n" \
"v_accvgpr_write_b32 a40, v197\n" \
"v_accvgpr_read_b32 v197, a41\n" \
"v_mul_f32 v197, v198, v197\n" \
"v_accvgpr_write_b32 a41, v197\n" \
"v_accvgpr_read_b32 v197, a42\n" \
"v_mul_f32 v197, v198, v197\n" \
"v_accvgpr_write_b32 a42, v197\n" \
"v_accvgpr_read_b32 v197, a43\n" \
"v_mul_f32 v197, v198, v197\n" \
"v_accvgpr_write_b32 a43, v197\n" \
"s_nop 1\n" \
"v_mfma_f32_16x16x32_bf16 a[40:43], a[48:51], v[128:131], a[40:43]\n" \
"v_mfma_f32_16x16x32_bf16 a[40:43], a[48:51], v[136:139], a[40:43]\n" \
"v_mfma_f32_16x16x32_bf16 v[64:67], a[224:227], v[136:139], v[64:67]\n" \
"v_mfma_f32_16x16x32_bf16 v[64:67], a[192:195], v[128:131], v[64:67]\n" \
"v_mfma_f32_16x16x32_bf16 v[68:71], a[196:199], v[128:131], v[68:71]\n" \
"v_mfma_f32_16x16x32_bf16 v[68:71], a[228:231], v[136:139], v[68:71]\n" \
"v_mfma_f32_16x16x32_bf16 v[72:75], a[232:235], v[136:139], v[72:75]\n" \
"v_mfma_f32_16x16x32_bf16 v[72:75], a[200:203], v[128:131], v[72:75]\n" \
"v_mfma_f32_16x16x32_bf16 v[76:79], a[204:207], v[128:131], v[76:79]\n" \
"v_mfma_f32_16x16x32_bf16 v[76:79], a[236:239], v[136:139], v[76:79]\n" \
"v_mfma_f32_16x16x32_bf16 v[80:83], a[240:243], v[136:139], v[80:83]\n" \
"v_mfma_f32_16x16x32_bf16 v[80:83], a[208:211], v[128:131], v[80:83]\n" \
"v_mfma_f32_16x16x32_bf16 v[84:87], a[212:215], v[128:131], v[84:87]\n" \
"v_mfma_f32_16x16x32_bf16 v[84:87], a[244:247], v[136:139], v[84:87]\n" \
"v_mfma_f32_16x16x32_bf16 v[88:91], a[248:251], v[136:139], v[88:91]\n" \
"v_mfma_f32_16x16x32_bf16 v[88:91], a[216:219], v[128:131], v[88:91]\n" \
"v_mfma_f32_16x16x32_bf16 v[92:95], a[220:223], v[128:131], v[92:95]\n" \
"v_mfma_f32_16x16x32_bf16 v[92:95], a[252:255], v[136:139], v[92:95]\n" \
"s_nop 7\n" \
"v_mfma_f32_16x16x32_bf16 v[128:131], a[128:131], v[176:179], 0\n" \
"v_mfma_f32_16x16x32_bf16 v[128:131], a[132:135], v[180:183], v[128:131]\n" \
"v_mfma_f32_16x16x32_bf16 v[128:131], a[136:139], v[184:187], v[128:131]\n" \
"v_mfma_f32_16x16x32_bf16 v[128:131], a[140:143], v[188:191], v[128:131]\n" \
"v_mfma_f32_16x16x32_bf16 v[132:135], a[156:159], v[188:191], 0\n" \
"v_mfma_f32_16x16x32_bf16 v[132:135], a[152:155], v[184:187], v[132:135]\n" \
"v_mfma_f32_16x16x32_bf16 v[132:135], a[148:151], v[180:183], v[132:135]\n" \
"v_mfma_f32_16x16x32_bf16 v[132:135], a[144:147], v[176:179], v[132:135]\n" \
"v_mfma_f32_16x16x32_bf16 v[136:139], a[160:163], v[176:179], 0\n" \
"v_mfma_f32_16x16x32_bf16 v[136:139], a[164:167], v[180:183], v[136:139]\n" \
"v_mfma_f32_16x16x32_bf16 v[136:139], a[168:171], v[184:187], v[136:139]\n" \
"v_mfma_f32_16x16x32_bf16 v[136:139], a[172:175], v[188:191], v[136:139]\n" \
"v_mfma_f32_16x16x32_bf16 v[140:143], a[188:191], v[188:191], 0\n" \
"v_mfma_f32_16x16x32_bf16 v[140:143], a[184:187], v[184:187], v[140:143]\n" \
"v_mfma_f32_16x16x32_bf16 v[140:143], a[180:183], v[180:183], v[140:143]\n" \
"v_mfma_f32_16x16x32_bf16 v[140:143], a[176:179], v[176:179], v[140:143]\n" \
"v_cmp_gt_i32 vcc, 0, v195\n" \
"s_nop 1\n" \
"v_cndmask_b32 v128, v128, v199, vcc\n" \
"v_cmp_gt_i32 vcc, 1, v195\n" \
"s_nop 1\n" \
"v_cndmask_b32 v129, v129, v199, vcc\n" \
"v_cmp_gt_i32 vcc, 2, v195\n" \
"s_nop 1\n" \
"v_cndmask_b32 v130, v130, v199, vcc\n" \
"v_cmp_gt_i32 vcc, 3, v195\n" \
"s_nop 1\n" \
"v_cndmask_b32 v131, v131, v199, vcc\n" \
"v_cmp_gt_i32 vcc, 16, v195\n" \
"s_nop 1\n" \
"v_cndmask_b32 v132, v132, v199, vcc\n" \
"v_cmp_gt_i32 vcc, 17, v195\n" \
"s_nop 1\n" \
"v_cndmask_b32 v133, v133, v199, vcc\n" \
"v_cmp_gt_i32 vcc, 18, v195\n" \
"s_nop 1\n" \
"v_cndmask_b32 v134, v134, v199, vcc\n" \
"v_cmp_gt_i32 vcc, 19, v195\n" \
"s_nop 1\n" \
"v_cndmask_b32 v135, v135, v199, vcc\n" \
"v_cmp_gt_i32 vcc, 32, v195\n" \
"s_nop 1\n" \
"v_cndmask_b32 v136, v136, v199, vcc\n" \
"v_cmp_gt_i32 vcc, 33, v195\n" \
"s_nop 1\n" \
"v_cndmask_b32 v137, v137, v199, vcc\n" \
"v_cmp_gt_i32 vcc, 34, v195\n" \
"s_nop 1\n" \
"v_cndmask_b32 v138, v138, v199, vcc\n" \
"v_cmp_gt_i32 vcc, 35, v195\n" \
"s_nop 1\n" \
"v_cndmask_b32 v139, v139, v199, vcc\n" \
"v_cmp_gt_i32 vcc, 48, v195\n" \
"s_nop 1\n" \
"v_cndmask_b32 v140, v140, v199, vcc\n" \
"v_cmp_gt_i32 vcc, 49, v195\n" \
"s_nop 1\n" \
"v_cndmask_b32 v141, v141, v199, vcc\n" \
"v_cmp_gt_i32 vcc, 50, v195\n" \
"s_nop 1\n" \
"v_cndmask_b32 v142, v142, v199, vcc\n" \
"v_cmp_gt_i32 vcc, 51, v195\n" \
"s_nop 1\n" \
"v_cndmask_b32 v143, v143, v199, vcc\n" \
"v_max3_f32 v196, v193, v128, v129\n" \
"v_max3_f32 v196, v196, v130, v131\n" \
"v_max3_f32 v196, v196, v132, v133\n" \
"v_max3_f32 v196, v196, v134, v135\n" \
"v_max3_f32 v196, v196, v136, v137\n" \
"v_max3_f32 v196, v196, v138, v139\n" \
"v_max3_f32 v196, v196, v140, v141\n" \
"v_max3_f32 v196, v196, v142, v143\n" \
"v_mov_b32 v197, v196\n" \
"s_nop 1\n" \
"v_permlane32_swap_b32 v196, v197\n" \
"v_max_f32 v196, v196, v197\n" \
"v_mov_b32 v197, v196\n" \
"s_nop 1\n" \
"v_permlane16_swap_b32 v196, v197\n" \
"v_max_f32 v196, v196, v197\n" \
"v_sub_f32 v198, v193, v196\n" \
"v_mov_b32 v193, v196\n" \
"v_exp_f32 v198, v198\n" \
"v_subrev_u32 v195, 64, v195\n" \
"v_sub_f32 v128, v128, v196\n" \
"v_sub_f32 v129, v129, v196\n" \
"v_sub_f32 v130, v130, v196\n" \
"v_sub_f32 v131, v131, v196\n" \
"v_sub_f32 v132, v132, v196\n" \
"v_sub_f32 v133, v133, v196\n" \
"v_sub_f32 v134, v134, v196\n" \
"v_sub_f32 v135, v135, v196\n" \
"v_sub_f32 v136, v136, v196\n" \
"v_sub_f32 v137, v137, v196\n" \
"v_sub_f32 v138, v138, v196\n" \
"v_sub_f32 v139, v139, v196\n" \
"v_sub_f32 v140, v140, v196\n" \
"v_sub_f32 v141, v141, v196\n" \
"v_sub_f32 v142, v142, v196\n" \
"v_sub_f32 v143, v143, v196\n" \
"v_exp_f32 v128, v128\n" \
"v_exp_f32 v129, v129\n" \
"v_exp_f32 v130, v130\n" \
"v_exp_f32 v131, v131\n" \
"v_exp_f32 v132, v132\n" \
"v_exp_f32 v133, v133\n" \
"v_exp_f32 v134, v134\n" \
"v_exp_f32 v135, v135\n" \
"v_exp_f32 v136, v136\n" \
"v_exp_f32 v137, v137\n" \
"v_exp_f32 v138, v138\n" \
"v_exp_f32 v139, v139\n" \
"v_exp_f32 v140, v140\n" \
"v_exp_f32 v141, v141\n" \
"v_exp_f32 v142, v142\n" \
"v_exp_f32 v143, v143\n" \
"v_cvt_pk_bf16_f32 v128, v128, v129\n" \
"v_cvt_pk_bf16_f32 v129, v130, v131\n" \
"v_cvt_pk_bf16_f32 v130, v132, v133\n" \
"v_cvt_pk_bf16_f32 v131, v134, v135\n" \
"v_cvt_pk_bf16_f32 v136, v136, v137\n" \
"v_cvt_pk_bf16_f32 v137, v138, v139\n" \
"v_cvt_pk_bf16_f32 v138, v140, v141\n" \
"v_cvt_pk_bf16_f32 v139, v142, v143\n" \
"v_mul_f32 v96, v198, v96\n" \
"v_mul_f32 v97, v198, v97\n" \
"v_mul_f32 v98, v198, v98\n" \
"v_mul_f32 v99, v198, v99\n" \
"v_mul_f32 v100, v198, v100\n" \
"v_mul_f32 v101, v198, v101\n" \
"v_mul_f32 v102, v198, v102\n" \
"v_mul_f32 v103, v198, v103\n" \
"v_mul_f32 v104, v198, v104\n" \
"v_mul_f32 v105, v198, v105\n" \
"v_mul_f32 v106, v198, v106\n" \
"v_mul_f32 v107, v198, v107\n" \
"v_mul_f32 v108, v198, v108\n" \
"v_mul_f32 v109, v198, v109\n" \
"v_mul_f32 v110, v198, v110\n" \
"v_mul_f32 v111, v198, v111\n" \
"v_mul_f32 v112, v198, v112\n" \
"v_mul_f32 v113, v198, v113\n" \
"v_mul_f32 v114, v198, v114\n" \
"v_mul_f32 v115, v198, v115\n" \
"v_mul_f32 v116, v198, v116\n" \
"v_mul_f32 v117, v198, v117\n" \
"v_mul_f32 v118, v198, v118\n" \
"v_mul_f32 v119, v198, v119\n" \
"v_mul_f32 v120, v198, v120\n" \
"v_mul_f32 v121, v198, v121\n" \
"v_mul_f32 v122, v198, v122\n" \
"v_mul_f32 v123, v198, v123\n" \
"v_mul_f32 v124, v198, v124\n" \
"v_mul_f32 v125, v198, v125\n" \
"v_mul_f32 v126, v198, v126\n" \
"v_mul_f32 v127, v198, v127\n" \
"v_accvgpr_read_b32 v197, a44\n" \
"v_mul_f32 v197, v198, v197\n" \
"v_accvgpr_write_b32 a44, v197\n" \
"v_accvgpr_read_b32 v197, a45\n" \
"v_mul_f32 v197, v198, v197\n" \
"v_accvgpr_write_b32 a45, v197\n" \
"v_accvgpr_read_b32 v197, a46\n" \
"v_mul_f32 v197, v198, v197\n" \
"v_accvgpr_write_b32 a46, v197\n" \
"v_accvgpr_read_b32 v197, a47\n" \
"v_mul_f32 v197, v198, v197\n" \
"v_accvgpr_write_b32 a47, v197\n" \
"s_nop 1\n" \
"v_mfma_f32_16x16x32_bf16 a[44:47], a[48:51], v[128:131], a[44:47]\n" \
"v_mfma_f32_16x16x32_bf16 a[44:47], a[48:51], v[136:139], a[44:47]\n" \
"v_mfma_f32_16x16x32_bf16 v[96:99], a[224:227], v[136:139], v[96:99]\n" \
"v_mfma_f32_16x16x32_bf16 v[96:99], a[192:195], v[128:131], v[96:99]\n" \
"v_mfma_f32_16x16x32_bf16 v[100:103], a[196:199], v[128:131], v[100:103]\n" \
"v_mfma_f32_16x16x32_bf16 v[100:103], a[228:231], v[136:139], v[100:103]\n" \
"v_mfma_f32_16x16x32_bf16 v[104:107], a[232:235], v[136:139], v[104:107]\n" \
"v_mfma_f32_16x16x32_bf16 v[104:107], a[200:203], v[128:131], v[104:107]\n" \
"v_mfma_f32_16x16x32_bf16 v[108:111], a[204:207], v[128:131], v[108:111]\n" \
"v_mfma_f32_16x16x32_bf16 v[108:111], a[236:239], v[136:139], v[108:111]\n" \
"v_mfma_f32_16x16x32_bf16 v[112:115], a[240:243], v[136:139], v[112:115]\n" \
"v_mfma_f32_16x16x32_bf16 v[112:115], a[208:211], v[128:131], v[112:115]\n" \
"v_mfma_f32_16x16x32_bf16 v[116:119], a[212:215], v[128:131], v[116:119]\n" \
"v_mfma_f32_16x16x32_bf16 v[116:119], a[244:247], v[136:139], v[116:119]\n" \
"v_mfma_f32_16x16x32_bf16 v[120:123], a[248:251], v[136:139], v[120:123]\n" \
"v_mfma_f32_16x16x32_bf16 v[120:123], a[216:219], v[128:131], v[120:123]\n" \
"v_mfma_f32_16x16x32_bf16 v[124:127], a[220:223], v[128:131], v[124:127]\n" \
"v_mfma_f32_16x16x32_bf16 v[124:127], a[252:255], v[136:139], v[124:127]\n" \
"s_nop 7\n" \
"s_add_u32 s15, s15, 0x4000\n" \
"s_sub_u32 s14, s14, 1\n" \
"s_cbranch_scc0 .Lslr2_%=\n" \
"s_nop 7\n" \
"s_setpc_b64 s[98:99]\n" \
".Lspr2_%=:\n" \
"s_mov_b32 s82, 0x100000\n" \
".Lsqr2_%=:\n" \
"s_sleep 6\n" \
"buffer_load_dword v197, v196, s[32:35], s26 offen sc1\n" \
"s_waitcnt vmcnt(0)\n" \
"v_cmp_eq_u32 vcc, 0, v197\n" \
"s_cbranch_vccz .Lsbr2_%=\n" \
"s_sub_u32 s82, s82, 1\n" \
"s_cbranch_scc0 .Lsqr2_%=\n" \
"s_branch .Lsbr2_%=\n" \
".Lend_%=:\n" \
".Lst3_4_%=:\n" \
"s_cbranch_execz .Lstb3_%=\n"
struct Desc { const void* p; unsigned bytes, flags; };
struct Args { Desc ckv, w_uk, w_uv, w_q, ws, w_mod, cvec, x, w_o, wg, wu, wd, out; const void* w_r; };
constexpr size_t WS_DBG = (size_t)56 << 20; constexpr unsigned WS_BYTES = 80u << 20, LDS_BYTES = 163840;
typedef const __attribute__((address_space(4))) Args* kargs_t;
typedef unsigned u32x16 __attribute__((ext_vector_type(16)));
#define VCLOB_LOW "v0","v1","v2","v3","v4","v5","v6","v7","v8","v9","v10","v11","v12","v13","v14","v15","v16","v17","v18","v19","v20","v21","v22","v23","v24","v25","v26","v27","v28","v29","v30","v31","v32","v33","v34","v35","v36","v37","v38","v39","v40","v41","v42","v43","v44","v45","v46","v47","v48","v49","v50","v51","v52","v53","v54","v55","v56","v57","v58","v59","v60","v61","v62","v63","v64","v65","v66","v67","v68","v69","v70","v71","v72","v73","v74","v75","v76","v77","v78","v79","v80","v81","v82","v83","v84","v85","v86","v87","v88","v89","v90","v91","v92","v93","v94","v95","v96","v97","v98","v99","v100","v101","v102","v103","v104","v105","v106","v107","v108","v109","v110","v111","v112","v113","v114","v115","v116","v117","v118","v119","v120","v121","v122","v123","v124","v125","v126","v127","v128","v129","v130","v131","v132","v133","v134","v135","v136","v137","v138","v139","v140","v141","v142","v143","v144","v145","v146","v147","v148","v149","v150","v151","v152","v153","v154","v155","v156","v157","v158","v159","v160","v161","v162","v163","v164","v165","v166","v167","v168","v169","v170","v171","v172","v173","v174","v175","v176","v177","v178","v179","v180","v181","v182","v183","v184","v185","v186","v187","v188","v189","v190","v191","v192","v193","v194","v195","v196","v197","v198","v199","v200","v201","v202","v203","v204","v205","v206","v207","v208","v209","v210","v211","v212","v213","v214","v215","v216","v217","v218","v219","v220","v221","v222","v223","v224","v225","v226","v227","v228","v229","v230","v231","v232","v233","v234","v235","v236","v237","v238","v239"
#define SCLOB "s14","s15","s16","s17","s18","s19","s20","s21","s22","s23","s24","s25","s26","s27","s28","s29","s30","s31","s32","s33","s34","s35","s36","s37","s38","s39","s40","s41","s42","s43","s44","s45","s46","s47","s48","s49","s50","s51","s52","s53","s54","s55","s56","s57","s58","s59","s60","s61","s62","s63","s64","s65","s66","s67","s68","s69","s70","s71","s72","s73","s74","s75","s76","s77","s78","s79","s80","s81","s82","s83","s84","s85","s86","s87","s88","s89","s90","s91","s92","s93","s94","s95","s96","s97","s98","s99","s100","s101"
__global__ __launch_bounds__(256, 1) __attribute__((aligned(4096))) void k_all(const Args a) {
    unsigned wid = __builtin_amdgcn_readfirstlane(threadIdx.x >> 6);
    u32x16 RB;
    asm volatile(MAIN_ASM : "={v[240:255]}"(RB)
                 : [ka] "s"(__builtin_amdgcn_kernarg_segment_ptr()), [bid] "s"(blockIdx.x), [wid] "s"(wid) : "memory", "vcc", "scc", VCLOB_LOW, SCLOB, ACLOB_ALL);
    asm volatile(".Lat_code_e:");
}
extern "C" void kernel_launch(void* const* d_in, const int* in_sizes, int n_in, void* d_out, int out_size, void* d_ws, size_t ws_size, hipStream_t stream) {
    Args a;
    auto D = [](const void* p, unsigned bytes) { return Desc{p, bytes, 0x00020000u}; };
    a.ckv = D(d_in[1], SEQ * DM * 2); a.w_uk = D(d_in[3], DM * HD * 2); a.w_uv = D(d_in[4], DM * HD * 2); a.w_q = D(d_in[2], DM * HD * 2);
    a.ws = D(d_ws, WS_BYTES); a.w_mod = D(d_in[12], DM * 6 * DM * 2); a.cvec = D(d_in[11], DM * 2); a.x = D(d_in[0], SEQ * DM * 2);
    a.w_o = D(d_in[5], HD * DM * 2); a.wg = D(d_in[8], DM * FF * 2); a.wu = D(d_in[9], DM * FF * 2); a.wd = D(d_in[10], FF * DM * 2);
    a.out = D(d_out, SEQ * DM * 2); a.w_r = d_in[7];
    (void)hipFuncSetAttribute((const void*)k_all, hipFuncAttributeMaxDynamicSharedMemorySize, LDS_BYTES);
    k_all<<<256, 256, LDS_BYTES, stream>>>(a);
}
```

```cpp
#include <hip/hip_runtime.h>
#include <stdint.h>
constexpr int SEQ = 2048, DM = 512, HD = 4096, FF = 1792;
#define ACLOB_ALL "a0","a1","a2","a3","a4","a5","a6","a7","a8","a9","a10","a11","a12","a13","a14","a15","a16","a17","a18","a19","a20","a21","a22","a23","a24","a25","a26","a27","a28","a29","a30","a31","a32","a33","a34","a35","a36","a37","a38","a39","a40","a41","a42","a43","a44","a45","a46","a47","a48","a49","a50","a51","a52","a53","a54","a55","a56","a57","a58","a59","a60","a61","a62","a63","a64","a65","a66","a67","a68","a69","a70","a71","a72","a73","a74","a75","a76","a77","a78","a79","a80","a81","a82","a83","a84","a85","a86","a87","a88","a89","a90","a91","a92","a93","a94","a95","a96","a97","a98","a99","a100","a101","a102","a103","a104","a105","a106","a107","a108","a109","a110","a111","a112","a113","a114","a115","a116","a117","a118","a119","a120","a121","a122","a123","a124","a125","a126","a127","a128","a129","a130","a131","a132","a133","a134","a135","a136","a137","a138","a139","a140","a141","a142","a143","a144","a145","a146","a147","a148","a149","a150","a151","a152","a153","a154","a155","a156","a157","a158","a159","a160","a161","a162","a163","a164","a165","a166","a167","a168","a169","a170","a171","a172","a173","a174","a175","a176","a177","a178","a179","a180","a181","a182","a183","a184","a185","a186","a187","a188","a189","a190","a191","a192","a193","a194","a195","a196","a197","a198","a199","a200","a201","a202","a203","a204","a205","a206","a207","a208","a209","a210","a211","a212","a213","a214","a215","a216","a217","a218","a219","a220","a221","a222","a223","a224","a225","a226","a227","a228","a229","a230","a231","a232","a233","a234","a235","a236","a237","a238","a239","a240","a241","a242","a243","a244","a245","a246","a247","a248","a249","a250","a251","a252","a253","a254","a255"
#define MAIN_ASM \
  \
".p2align 4\n" \
"s_load_dwordx16 s[16:31], %[ka], 0\n" \
"s_load_dwordx16 s[32:47], %[ka], 64\n" \
"s_load_dwordx16 s[48:63], %[ka], 128\n" \
"v_mbcnt_lo_u32_b32 v251, -1, 0\n" \
"v_mbcnt_hi_u32_b32 v251, -1, v251\n" \
"s_and_b32 s64, %[bid], 7\n" \
"s_lshr_b32 s65, %[bid], 3\n" \
"s_and_b32 s66, s65, 3\n" \
"s_lshr_b32 s67, %[bid], 5\n" \
"s_lshl2_add_u32 s68, s64, s66\n" \
"v_lshlrev_b32 v248, 4, v251\n" \
"v_lshlrev_b32 v0, 7, v251\n" \
"s_getpc_b64 s[96:97]\n" \
".Lpf0_%=:\n" \
"s_and_b32 s97, s97, 0xffff\n" \
"s_mov_b32 s98, .Lat_code_e-.Lpf0_%=\n" \
"s_mov_b32 s99, 0x20000\n" \
"s_lshl2_add_u32 s74, s66, %[wid]\n" \
"s_lshl_b32 s74, s74, 13\n" \
"buffer_load_dword v238, v0, s[96:99], s74 offen\n" \
"v_bfe_u32 v1, v251, 2, 2\n" \
"v_bfe_u32 v2, v251, 4, 1\n" \
"v_lshl_add_u32 v1, v2, 3, v1\n" \
"v_lshlrev_b32 v1, 13, v1\n" \
"v_lshrrev_b32 v3, 5, v251\n" \
"v_lshl_or_b32 v1, v3, 6, v1\n" \
"v_and_b32 v4, 3, v251\n" \
"v_lshlrev_b32 v5, 1, v2\n" \
"v_xor_b32 v4, v4, v5\n" \
"v_lshl_or_b32 v246, v4, 4, v1\n" \
"v_lshrrev_b32 v6, 3, v251\n" \
"v_and_b32 v7, 7, v251\n" \
"v_lshrrev_b32 v8, 4, v251\n" \
"v_xor_b32 v9, v7, v8\n" \
"v_lshlrev_b32 v9, 4, v9\n" \
"v_lshl_or_b32 v249, v6, 10, v9\n" \
"v_xor_b32 v250, 64, v249\n" \
"s_sub_u32 s76, %[bid], 192\n" \
"s_min_u32 s76, s76, %[bid]\n" \
"s_mul_i32 s77, s76, 1366\n" \
"s_lshr_b32 s77, s77, 16\n" \
"s_mul_i32 s78, s77, 48\n" \
"s_sub_u32 s76, s76, s78\n" \
"s_lshl2_add_u32 s78, s77, %[wid]\n" \
"s_lshl_b32 s78, s78, 5\n" \
"v_lshl_add_u32 v10, v6, 2, s78\n" \
"v_lshlrev_b32 v11, 1, v10\n" \
"v_mul_u32_u24 v12, 0x1800, v10\n" \
"v_lshl_add_u32 v12, v7, 4, v12\n" \
"s_lshl_b32 s74, s76, 7\n" \
"s_add_u32 s74, s74, 3072\n" \
"v_add_u32 v12, s74, v12\n" \
"v_add_u32 v13, 0x3000, v12\n" \
"v_mov_b32 v14, 720896\n" \
"s_mul_i32 s81, s77, 12288\n" \
"s_lshl_b32 s74, s76, 8\n" \
"s_add_u32 s81, s81, s74\n" \
"s_add_u32 s81, s81, 917504\n" \
"s_lshl_b32 s74, %[wid], 8\n" \
"s_or_b32 s74, s74, %[bid]\n" \
"s_cmp_lt_u32 s74, 192\n" \
"s_cselect_b32 s81, s81, 1073741824\n" \
"s_lshr_b32 s74, %[wid], 1\n" \
"s_lshl_b32 s69, s74, 17\n" \
"s_and_b32 s74, %[wid], 1\n" \
"s_lshl_b32 s74, s74, 15\n" \
"s_add_u32 s69, s69, s74\n" \
"s_lshl_b32 s74, s68, 8\n" \
"s_add_u32 s69, s69, s74\n" \
"s_lshl_b32 s70, %[wid], 11\n" \
"s_add_u32 s70, s70, 65536\n" \
"s_lshl2_add_u32 s71, s67, %[wid]\n" \
"s_lshl_b32 s71, s71, 15\n" \
"s_lshl_b32 s72, %[wid], 14\n" \
"s_lshl_b32 s74, s67, 18\n" \
"s_sub_u32 s94, s71, s74\n" \
"s_add_u32 s94, s94, 1933312\n" \
"s_sub_u32 s74, 15, s67\n" \
"s_cmp_lt_u32 s68, 16\n" \
"s_cselect_b32 s74, s67, s74\n" \
"s_lshl_b32 s74, s74, 4\n" \
"s_bfe_u32 s76, s68, 0x20002\n" \
"s_sub_u32 s78, 7, s76\n" \
"s_bitcmp1_b32 s68, 1\n" \
"s_cselect_b32 s76, s78, s76\n" \
"s_lshl1_add_u32 s74, s76, s74\n" \
"s_and_b32 s76, s68, 1\n" \
"s_add_u32 s74, s74, s76\n" \
"s_lshl_b32 s78, s74, 3\n" \
"s_lshl1_add_u32 s74, %[wid], s78\n" \
"s_lshl_b32 s75, s74, 10\n" \
"s_waitcnt lgkmcnt(0)\n" \
"global_load_dwordx2 v[16:17], v11, s[40:41]\n" \
"global_load_dwordx4 v[20:23], v12, s[36:37] offset:-3072 nt\n" \
"global_load_dwordx4 v[24:27], v12, s[36:37] offset:3072 nt\n" \
"global_load_dwordx4 v[28:31], v13, s[36:37] offset:-3072 nt\n" \
"global_load_dwordx4 v[32:35], v13, s[36:37] offset:3072 nt\n" \
"s_add_u32 m0, s70, 0\n" \
"s_add_u32 s74, s69, 0\n" \
"buffer_load_dwordx4 v246, s[20:23], s74 offen lds\n" \
"s_add_u32 m0, s70, 1024\n" \
"s_add_u32 s74, s69, 128\n" \
"buffer_load_dwordx4 v246, s[20:23], s74 offen lds\n" \
"s_add_u32 m0, s72, 0\n" \
"s_add_u32 s74, s71, 0\n" \
"buffer_load_dwordx4 v249, s[16:19], s74 offen lds\n" \
"s_add_u32 m0, s72, 1024\n" \
"s_add_u32 s74, s71, 8192\n" \
"buffer_load_dwordx4 v250, s[16:19], s74 offen lds\n" \
"s_add_u32 m0, s72, 2048\n" \
"s_add_u32 s74, s71, 16384\n" \
"buffer_load_dwordx4 v249, s[16:19], s74 offen lds\n" \
"s_add_u32 m0, s72, 3072\n" \
"s_add_u32 s74, s71, 24576\n" \
"buffer_load_dwordx4 v250, s[16:19], s74 offen lds\n" \
"s_add_u32 m0, s72, 4096\n" \
"s_add_u32 s74, s94, 32768\n" \
"buffer_load_dwordx4 v249, s[16:19], s74 offen lds\n" \
"s_add_u32 m0, s72, 5120\n" \
"s_add_u32 s74, s94, 40960\n" \
"buffer_load_dwordx4 v250, s[16:19], s74 offen lds\n" \
"s_add_u32 m0, s72, 6144\n" \
"s_add_u32 s74, s94, 49152\n" \
"buffer_load_dwordx4 v249, s[16:19], s74 offen lds\n" \
"s_add_u32 m0, s72, 7168\n" \
"s_add_u32 s74, s94, 57344\n" \
"buffer_load_dwordx4 v250, s[16:19], s74 offen lds\n" \
"s_add_u32 m0, s70, 8192\n" \
"s_add_u32 s74, s69, 262144\n" \
"buffer_load_dwordx4 v246, s[20:23], s74 offen lds\n" \
"s_add_u32 m0, s70, 9216\n" \
"s_add_u32 s74, s69, 262272\n" \
"buffer_load_dwordx4 v246, s[20:23], s74 offen lds\n" \
"s_mov_b64 exec, 0\n" \
"s_cmp_eq_u32 %[wid], 0\n" \
"s_cbranch_scc1 .Lst0_0_%=\n" \
"s_cmp_eq_u32 %[wid], 1\n" \
"s_cbranch_scc1 .Lst0_1_%=\n" \
"s_cmp_eq_u32 %[wid], 2\n" \
"s_cbranch_scc1 .Lst0_2_%=\n" \
"s_branch .Lst0_3_%=\n" \
".Lstb0_%=:\n" \
"s_mov_b64 exec, -1\n" \
"v_mov_b32 v224, -1\n" \
"v_mov_b32 v225, -1\n" \
"s_min_u32 s74, %[bid], 223\n" \
"s_lshl_b32 s74, s74, 11\n" \
"s_lshl_b32 s76, %[wid], 9\n" \
"s_add_u32 s74, s74, s76\n" \
"s_add_u32 s74, s74, 98304\n" \
"v_lshl_add_u32 v228, v251, 3, s74\n" \
"s_lshl_b32 s74, %[wid], 8\n" \
"s_or_b32 s74, s74, %[bid]\n" \
"s_lshl_b32 s76, %[bid], 7\n" \
"s_add_u32 s76, s76, 65536\n" \
"s_cmp_lt_u32 s74, 256\n" \
"s_cselect_b32 s77, s76, 1073741824\n" \
"s_add_u32 s76, s76, 491392\n" \
"s_cmp_lt_u32 s74, 128\n" \
"s_cselect_b32 s76, s76, 1073741824\n" \
"v_mov_b32 v37, s77\n" \
"v_mov_b32 v38, s76\n" \
"v_cmp_gt_u32 vcc, 32, v251\n" \
"v_mov_b32 v230, 0\n" \
"s_lshl_b32 s77, %[wid], 16\n" \
"v_cndmask_b32 v37, v38, v37, vcc\n" \
"s_lshl_b32 s76, %[bid], 8\n" \
"s_add_u32 s76, s76, s77\n" \
"s_add_u32 s76, s76, 524288\n" \
"v_mov_b32 v38, s76\n" \
"s_sub_u32 s77, %[wid], 1\n" \
"s_cmp_lt_u32 s77, 2\n" \
"s_cselect_b64 vcc, -1, 0\n" \
"v_cndmask_b32 v37, v37, v38, vcc\n" \
"v_lshl_add_u32 v229, v251, 2, v37\n" \
"v_lshlrev_b32 v40, 12, v3\n" \
"v_lshl_or_b32 v40, v2, 8, v40\n" \
"v_bfe_u32 v41, v251, 2, 2\n" \
"v_lshl_or_b32 v40, v41, 6, v40\n" \
"v_lshl_or_b32 v40, v2, 5, v40\n" \
"v_and_b32 v41, 3, v251\n" \
"v_lshl_or_b32 v40, v41, 3, v40\n" \
"v_add_u32 v240, 65536, v40\n" \
"v_xor_b32 v241, 32, v240\n" \
"v_and_b32 v42, 15, v251\n" \
"v_bfe_u32 v43, v251, 1, 3\n" \
"v_xor_b32 v43, v43, v8\n" \
"v_lshlrev_b32 v43, 4, v43\n" \
"v_lshl_or_b32 v43, v42, 7, v43\n" \
"v_add_u32 v242, s72, v43\n" \
"v_xor_b32 v244, 64, v242\n" \
"s_mov_b32 s76, 0x05040100\n" \
"s_mov_b32 s77, 0x07060302\n" \
"s_add_u32 m0, s70, 16384\n" \
"s_add_u32 s74, s69, 0\n" \
"buffer_load_dwordx4 v246, s[24:27], s74 offen lds\n" \
"s_add_u32 m0, s70, 17408\n" \
"s_add_u32 s74, s69, 128\n" \
"buffer_load_dwordx4 v246, s[24:27], s74 offen lds\n" \
"s_waitcnt vmcnt(14)\n" \
"v_lshlrev_b32 v44, 16, v16\n" \
"v_and_b32 v45, 0xffff0000, v16\n" \
"v_lshlrev_b32 v46, 16, v17\n" \
"v_and_b32 v47, 0xffff0000, v17\n" \
"v_mul_f32 v48, 0xbfb8aa3b, v44\n" \
"v_mul_f32 v49, 0xbfb8aa3b, v45\n" \
"v_mul_f32 v50, 0xbfb8aa3b, v46\n" \
"v_mul_f32 v51, 0xbfb8aa3b, v47\n" \
"v_exp_f32 v48, v48\n" \
"v_exp_f32 v49, v49\n" \
"v_exp_f32 v50, v50\n" \
"v_exp_f32 v51, v51\n" \
"v_add_f32 v48, 1.0, v48\n" \
"v_add_f32 v49, 1.0, v49\n" \
"v_add_f32 v50, 1.0, v50\n" \
"v_add_f32 v51, 1.0, v51\n" \
"v_rcp_f32 v48, v48\n" \
"v_rcp_f32 v49, v49\n" \
"v_rcp_f32 v50, v50\n" \
"v_rcp_f32 v51, v51\n" \
"v_mul_f32 v44, v44, v48\n" \
"v_mul_f32 v45, v45, v49\n" \
"v_mul_f32 v46, v46, v50\n" \
"v_mul_f32 v47, v47, v51\n" \
"v_cvt_pk_bf16_f32 v44, v44, v45\n" \
"v_cvt_pk_bf16_f32 v45, v46, v47\n" \
"s_add_u32 m0, s70, 24576\n" \
"s_add_u32 s74, s69, 262144\n" \
"buffer_load_dwordx4 v246, s[24:27], s74 offen lds\n" \
"v_perm_b32 v64, v24, v20, s76\n" \
"v_perm_b32 v65, v24, v20, s77\n" \
"v_perm_b32 v66, v25, v21, s76\n" \
"v_perm_b32 v67, v25, v21, s77\n" \
"v_perm_b32 v68, v26, v22, s76\n" \
"v_perm_b32 v69, v26, v22, s77\n" \
"v_perm_b32 v70, v27, v23, s76\n" \
"v_perm_b32 v71, v27, v23, s77\n" \
"v_dot2_f32_bf16 v52, v64, v44, 0\n" \
"v_dot2_f32_bf16 v53, v65, v44, 0\n" \
"v_dot2_f32_bf16 v54, v66, v44, 0\n" \
"v_dot2_f32_bf16 v55, v67, v44, 0\n" \
"v_dot2_f32_bf16 v56, v68, v44, 0\n" \
"v_dot2_f32_bf16 v57, v69, v44, 0\n" \
"v_dot2_f32_bf16 v58, v70, v44, 0\n" \
"v_dot2_f32_bf16 v59, v71, v44, 0\n" \
"s_add_u32 m0, s70, 25600\n" \
"s_add_u32 s74, s69, 262272\n" \
"buffer_load_dwordx4 v246, s[24:27], s74 offen lds\n" \
"v_perm_b32 v64, v32, v28, s76\n" \
"v_perm_b32 v65, v32, v28, s77\n" \
"v_perm_b32 v66, v33, v29, s76\n" \
"v_perm_b32 v67, v33, v29, s77\n" \
"v_perm_b32 v68, v34, v30, s76\n" \
"v_perm_b32 v69, v34, v30, s77\n" \
"v_perm_b32 v70, v35, v31, s76\n" \
"v_perm_b32 v71, v35, v31, s77\n" \
"v_dot2_f32_bf16 v52, v64, v45, v52\n" \
"v_dot2_f32_bf16 v53, v65, v45, v53\n" \
"v_dot2_f32_bf16 v54, v66, v45, v54\n" \
"v_dot2_f32_bf16 v55, v67, v45, v55\n" \
"v_dot2_f32_bf16 v56, v68, v45, v56\n" \
"v_dot2_f32_bf16 v57, v69, v45, v57\n" \
"v_dot2_f32_bf16 v58, v70, v45, v58\n" \
"v_dot2_f32_bf16 v59, v71, v45, v59\n" \
"buffer_load_dwordx2 v[202:203], v14, s[32:35], 0 offen sc1\n" \
"s_nop 2\n" \
"v_permlane32_swap_b32 v52, v56\n" \
"v_permlane32_swap_b32 v53, v57\n" \
"v_permlane32_swap_b32 v54, v58\n" \
"v_permlane32_swap_b32 v55, v59\n" \
"v_add_f32 v52, v52, v56\n" \
"v_add_f32 v53, v53, v57\n" \
"v_add_f32 v54, v54, v58\n" \
"v_add_f32 v55, v55, v59\n" \
"s_nop 0\n" \
"v_permlane16_swap_b32 v52, v54\n" \
"v_permlane16_swap_b32 v53, v55\n" \
"v_add_f32 v52, v52, v54\n" \
"v_add_f32 v53, v53, v55\n" \
"s_lshl_b32 s74, %[wid], 8\n" \
"s_add_u32 s74, s74, 147456\n" \
"v_lshl_add_u32 v60, v7, 5, s74\n" \
"v_lshl_add_u32 v60, v8, 3, v60\n" \
"v_lshlrev_b32 v231, 2, v251\n" \
"v_add_u32 v62, 147456, v231\n" \
"v_add_f32_dpp v52, v52, v52 row_ror:8 row_mask:0xf bank_mask:0xf\n" \
"v_add_f32_dpp v53, v53, v53 row_ror:8 row_mask:0xf bank_mask:0xf\n" \
"ds_write_b64 v60, v[52:53]\n" \
"buffer_load_dwordx4 v[128:131], v248, s[44:47], s75 offen\n" \
"buffer_load_dwordx4 v[132:135], v248, s[44:47], s75 offen offset:1024\n" \
"s_waitcnt vmcnt(7) lgkmcnt(0)\n" \
"s_barrier\n" \
"ds_read2st64_b32 v[232:233], v62 offset1:1\n" \
"ds_read2st64_b32 v[234:235], v62 offset0:2 offset1:3\n" \
"ds_read_b64_tr_b16 a[192:193], v240 offset:0\n" \
"ds_read_b64_tr_b16 a[194:195], v240 offset:2048\n" \
"ds_read_b64_tr_b16 a[196:197], v240 offset:8192\n" \
"ds_read_b64_tr_b16 a[198:199], v240 offset:10240\n" \
"ds_read_b64_tr_b16 a[200:201], v241 offset:0\n" \
"ds_read_b64_tr_b16 a[202:203], v241 offset:2048\n" \
"ds_read_b64_tr_b16 a[204:205], v241 offset:8192\n" \
"ds_read_b64_tr_b16 a[206:207], v241 offset:10240\n" \
"ds_read_b64_tr_b16 a[208:209], v240 offset:512\n" \
"ds_read_b64_tr_b16 a[210:211], v240 offset:2560\n" \
"ds_read_b64_tr_b16 a[212:213], v240 offset:8704\n" \
"ds_read_b64_tr_b16 a[214:215], v240 offset:10752\n" \
"ds_read_b128 a[128:131], v242 offset:0\n" \
"ds_read_b128 a[132:135], v242 offset:2048\n" \
"ds_read_b128 a[136:139], v242 offset:4096\n" \
"ds_read_b128 a[140:143], v242 offset:6144\n" \
"ds_read_b128 a[144:147], v244 offset:0\n" \
"ds_read_b128 a[148:151], v244 offset:2048\n" \
"ds_read_b128 a[152:155], v244 offset:4096\n" \
"ds_read_b128 a[156:159], v244 offset:6144\n" \
"s_waitcnt lgkmcnt(0)\n" \
  \
"v_mfma_f32_16x16x32_bf16 v[0:3], a[192:195], a[128:131], 0\n" \
"s_add_u32 m0, s72, 8192\n" \
"ds_read_b64_tr_b16 a[216:217], v241 offset:512\n" \
"v_add_f32 v232, v232, v233\n" \
"v_mfma_f32_16x16x32_bf16 v[0:3], a[196:199], a[144:147], v[0:3]\n" \
"s_add_u32 s74, s71, 128\n" \
"buffer_load_dwordx4 v249, s[16:19], s74 offen lds\n" \
"v_mfma_f32_16x16x32_bf16 v[32:35], a[196:199], a[148:151], 0\n" \
"s_add_u32 m0, s72, 9216\n" \
"ds_read_b64_tr_b16 a[218:219], v241 offset:2560\n" \
"v_add_f32 v234, v234, v235\n" \
"v_mfma_f32_16x16x32_bf16 v[32:35], a[192:195], a[132:135], v[32:35]\n" \
"s_add_u32 s74, s71, 8320\n" \
"v_add_f32 v232, v232, v234\n" \
"v_mfma_f32_16x16x32_bf16 v[64:67], a[192:195], a[136:139], 0\n" \
"buffer_load_dwordx4 v250, s[16:19], s74 offen lds\n" \
"v_mfma_f32_16x16x32_bf16 v[64:67], a[196:199], a[152:155], v[64:67]\n" \
"s_add_u32 m0, s72, 10240\n" \
"buffer_store_dword v232, v231, s[32:35], s81 offen sc1\n" \
"v_mov_b32 v239, 1\n" \
"v_mfma_f32_16x16x32_bf16 v[96:99], a[196:199], a[156:159], 0\n" \
"s_add_u32 s74, s71, 16512\n" \
"buffer_store_dwordx2 v[224:225], v228, s[32:35], 0 offen sc1\n" \
"v_mfma_f32_16x16x32_bf16 v[96:99], a[192:195], a[140:143], v[96:99]\n" \
"ds_read_b64_tr_b16 a[220:221], v241 offset:8704\n" \
"ds_read_b64_tr_b16 a[222:223], v241 offset:10752\n" \
"v_mfma_f32_16x16x32_bf16 v[4:7], a[200:203], a[128:131], 0\n" \
"buffer_store_dword v230, v229, s[32:35], 0 offen sc1\n" \
"v_bfe_u32 v210, v251, 1, 3\n" \
"v_mfma_f32_16x16x32_bf16 v[4:7], a[204:207], a[144:147], v[4:7]\n" \
"ds_read_b64_tr_b16 a[192:193], v240 offset:1024\n" \
"v_lshlrev_b32 v210, 13, v210\n" \
"v_mfma_f32_16x16x32_bf16 v[36:39], a[204:207], a[148:151], 0\n" \
"ds_read_b64_tr_b16 a[194:195], v240 offset:3072\n" \
"v_and_b32 v208, 1, v251\n" \
"v_mfma_f32_16x16x32_bf16 v[36:39], a[200:203], a[132:135], v[36:39]\n" \
"buffer_load_dwordx4 v249, s[16:19], s74 offen lds\n" \
"v_mfma_f32_16x16x32_bf16 v[68:71], a[200:203], a[136:139], 0\n" \
"s_add_u32 m0, s72, 11264\n" \
"ds_read_b64_tr_b16 a[196:197], v240 offset:9216\n" \
"v_lshl_or_b32 v208, v208, 7, v210\n" \
"v_mfma_f32_16x16x32_bf16 v[68:71], a[204:207], a[152:155], v[68:71]\n" \
"s_add_u32 s74, s71, 24704\n" \
"v_and_b32 v209, 15, v251\n" \
"v_lshlrev_b32 v209, 7, v209\n" \
"v_mfma_f32_16x16x32_bf16 v[100:103], a[204:207], a[156:159], 0\n" \
"buffer_load_dwordx4 v250, s[16:19], s74 offen lds\n" \
"v_mfma_f32_16x16x32_bf16 v[100:103], a[200:203], a[140:143], v[100:103]\n" \
"s_add_u32 m0, s72, 12288\n" \
"ds_read_b64_tr_b16 a[198:199], v240 offset:11264\n" \
"v_and_b32 v211, 0x7fff, v208\n" \
"v_mfma_f32_16x16x32_bf16 v[8:11], a[208:211], a[128:131], 0\n" \
"s_add_u32 s74, s94, 32896\n" \
"ds_read_b64_tr_b16 a[200:201], v241 offset:1024\n" \
"v_mfma_f32_16x16x32_bf16 v[8:11], a[212:215], a[144:147], v[8:11]\n" \
"buffer_load_dwordx4 v249, s[16:19], s74 offen lds\n" \
"v_mfma_f32_16x16x32_bf16 v[40:43], a[212:215], a[148:151], 0\n" \
"s_add_u32 m0, s72, 13312\n" \
"s_add_u32 s74, s94, 41088\n" \
"ds_read_b64_tr_b16 a[202:203], v241 offset:3072\n" \
"v_mfma_f32_16x16x32_bf16 v[40:43], a[208:211], a[132:135], v[40:43]\n" \
"v_mfma_f32_16x16x32_bf16 v[72:75], a[208:211], a[136:139], 0\n" \
"buffer_load_dwordx4 v250, s[16:19], s74 offen lds\n" \
"v_mfma_f32_16x16x32_bf16 v[72:75], a[212:215], a[152:155], v[72:75]\n" \
"s_add_u32 m0, s72, 14336\n" \
"s_add_u32 s74, s94, 49280\n" \
"ds_read_b64_tr_b16 a[204:205], v241 offset:9216\n" \
"v_mfma_f32_16x16x32_bf16 v[104:107], a[212:215], a[156:159], 0\n" \
"ds_read_b64_tr_b16 a[206:207], v241 offset:11264\n" \
"v_mfma_f32_16x16x32_bf16 v[104:107], a[208:211], a[140:143], v[104:107]\n" \
"s_waitcnt lgkmcnt(8)\n" \
"buffer_load_dwordx4 v249, s[16:19], s74 offen lds\n" \
"v_mfma_f32_16x16x32_bf16 v[12:15], a[216:219], a[128:131], 0\n" \
"s_add_u32 m0, s72, 15360\n" \
"s_add_u32 s74, s94, 57472\n" \
"ds_read_b64_tr_b16 a[208:209], v240 offset:1536\n" \
"v_mfma_f32_16x16x32_bf16 v[12:15], a[220:223], a[144:147], v[12:15]\n" \
"v_mfma_f32_16x16x32_bf16 v[44:47], a[220:223], a[148:151], 0\n" \
"buffer_load_dwordx4 v250, s[16:19], s74 offen lds\n" \
"v_mfma_f32_16x16x32_bf16 v[44:47], a[216:219], a[132:135], v[44:47]\n" \
"s_add_u32 m0, s70, 32768\n" \
"s_add_u32 s74, s69, 524288\n" \
"ds_read_b64_tr_b16 a[210:211], v240 offset:3584\n" \
"v_mfma_f32_16x16x32_bf16 v[76:79], a[216:219], a[136:139], 0\n" \
"ds_read_b64_tr_b16 a[212:213], v240 offset:9728\n" \
"v_mfma_f32_16x16x32_bf16 v[76:79], a[220:223], a[152:155], v[76:79]\n" \
"buffer_load_dwordx4 v246, s[20:23], s74 offen lds\n" \
"v_mfma_f32_16x16x32_bf16 v[108:111], a[220:223], a[156:159], 0\n" \
"s_add_u32 m0, s70, 33792\n" \
"s_add_u32 s74, s69, 524416\n" \
"ds_read_b64_tr_b16 a[214:215], v240 offset:11776\n" \
"v_mfma_f32_16x16x32_bf16 v[108:111], a[216:219], a[140:143], v[108:111]\n" \
"s_waitcnt lgkmcnt(8)\n" \
"v_mfma_f32_16x16x32_bf16 v[16:19], a[192:195], a[128:131], 0\n" \
"buffer_load_dwordx4 v246, s[20:23], s74 offen lds\n" \
"v_mfma_f32_16x16x32_bf16 v[16:19], a[196:199], a[144:147], v[16:19]\n" \
"s_add_u32 m0, s70, 40960\n" \
"s_add_u32 s74, s69, 786432\n" \
"ds_read_b64_tr_b16 a[216:217], v241 offset:1536\n" \
"v_mfma_f32_16x16x32_bf16 v[48:51], a[196:199], a[148:151], 0\n" \
"ds_read_b64_tr_b16 a[218:219], v241 offset:3584\n" \
"v_mfma_f32_16x16x32_bf16 v[48:51], a[192:195], a[132:135], v[48:51]\n" \
"buffer_load_dwordx4 v246, s[20:23], s74 offen lds\n" \
"v_mfma_f32_16x16x32_bf16 v[80:83], a[192:195], a[136:139], 0\n" \
"s_add_u32 m0, s70, 41984\n" \
"ds_read_b64_tr_b16 a[220:221], v241 offset:9728\n" \
"ds_read_b64_tr_b16 a[222:223], v241 offset:11776\n" \
"v_mfma_f32_16x16x32_bf16 v[80:83], a[196:199], a[152:155], v[80:83]\n" \
"s_add_u32 s74, s69, 786560\n" \
"v_mfma_f32_16x16x32_bf16 v[112:115], a[196:199], a[156:159], 0\n" \
"buffer_load_dwordx4 v246, s[20:23], s74 offen lds\n" \
"v_mfma_f32_16x16x32_bf16 v[112:115], a[192:195], a[140:143], v[112:115]\n" \
"s_waitcnt lgkmcnt(8)\n" \
"s_add_u32 m0, s70, 49152\n" \
"s_add_u32 s74, s69, 524288\n" \
"v_mfma_f32_16x16x32_bf16 v[20:23], a[200:203], a[128:131], 0\n" \
"s_mov_b32 s77, 0x7f4a7c15\n" \
"v_mov_b32 v253, 0x9e3779b9\n" \
"v_mfma_f32_16x16x32_bf16 v[20:23], a[204:207], a[144:147], v[20:23]\n" \
"buffer_load_dwordx4 v246, s[24:27], s74 offen lds\n" \
"v_mfma_f32_16x16x32_bf16 v[52:55], a[204:207], a[148:151], 0\n" \
"s_waitcnt vmcnt(18)\n" \
"s_add_u32 m0, s70, 50176\n" \
"v_add_co_u32 v202, vcc, s77, v202\n" \
"v_mfma_f32_16x16x32_bf16 v[52:55], a[200:203], a[132:135], v[52:55]\n" \
"s_waitcnt vmcnt(16)\n" \
"s_add_u32 s74, s69, 524416\n" \
"v_lshlrev_b32 v136, 16, v128\n" \
"v_mfma_f32_16x16x32_bf16 v[84:87], a[200:203], a[136:139], 0\n" \
"s_waitcnt vmcnt(11) lgkmcnt(0)\n" \
"s_barrier\n" \
"v_addc_co_u32 v203, vcc, v253, v203, vcc\n" \
"v_mfma_f32_16x16x32_bf16 v[84:87], a[204:207], a[152:155], v[84:87]\n" \
"s_lshl_b32 s85, %[bid], 3\n" \
"buffer_load_dwordx4 v246, s[24:27], s74 offen lds\n" \
"v_mfma_f32_16x16x32_bf16 v[116:119], a[204:207], a[156:159], 0\n" \
"s_add_u32 m0, s70, 57344\n" \
"ds_read_b64_tr_b16 a[192:193], v240 offset:16384\n" \
"ds_read_b64_tr_b16 a[194:195], v240 offset:18432\n" \
"v_mfma_f32_16x16x32_bf16 v[116:119], a[200:203], a[140:143], v[116:119]\n" \
"s_add_u32 s74, s69, 786432\n" \
"ds_read_b64_tr_b16 a[196:197], v240 offset:24576\n" \
"ds_read_b64_tr_b16 a[198:199], v240 offset:26624\n" \
"v_mfma_f32_16x16x32_bf16 v[24:27], a[208:211], a[128:131], 0\n" \
"s_add_u32 s85, s85, 573440\n" \
"buffer_load_dwordx4 v246, s[24:27], s74 offen lds\n" \
"v_mfma_f32_16x16x32_bf16 v[24:27], a[212:215], a[144:147], v[24:27]\n" \
"s_add_u32 m0, s70, 58368\n" \
"s_cmp_eq_u32 %[wid], 0\n" \
"s_cselect_b32 s85, s85, 1073741824\n" \
"v_mfma_f32_16x16x32_bf16 v[56:59], a[212:215], a[148:151], 0\n" \
"s_add_u32 s74, s69, 786560\n" \
"ds_read_b64_tr_b16 a[200:201], v241 offset:16384\n" \
"ds_read_b64_tr_b16 a[202:203], v241 offset:18432\n" \
"v_mfma_f32_16x16x32_bf16 v[56:59], a[208:211], a[132:135], v[56:59]\n" \
"buffer_load_dwordx4 v246, s[24:27], s74 offen lds\n" \
"v_mfma_f32_16x16x32_bf16 v[88:91], a[208:211], a[136:139], 0\n" \
"s_add_u32 m0, s72, 0\n" \
"ds_read_b64_tr_b16 a[204:205], v241 offset:24576\n" \
"v_and_b32 v252, 7, v251\n" \
"v_mfma_f32_16x16x32_bf16 v[88:91], a[212:215], a[152:155], v[88:91]\n" \
"s_add_u32 s74, s71, 256\n" \
"v_lshlrev_b32 v252, 11, v252\n" \
"v_and_b32 v137, 0xffff0000, v128\n" \
"v_mfma_f32_16x16x32_bf16 v[120:123], a[212:215], a[156:159], 0\n" \
"buffer_load_dwordx4 v249, s[16:19], s74 offen lds\n" \
"v_mfma_f32_16x16x32_bf16 v[120:123], a[208:211], a[140:143], v[120:123]\n" \
"s_add_u32 m0, s72, 1024\n" \
"buffer_store_dwordx2 v[202:203], v252, s[32:35], s85 offen sc1\n" \
"v_lshrrev_b32 v253, 1, v248\n" \
"v_mfma_f32_16x16x32_bf16 v[28:31], a[216:219], a[128:131], 0\n" \
"s_add_u32 s74, s71, 8448\n" \
"ds_read_b64_tr_b16 a[206:207], v241 offset:26624\n" \
"ds_read_b64_tr_b16 a[208:209], v240 offset:16896\n" \
"v_mfma_f32_16x16x32_bf16 v[28:31], a[220:223], a[144:147], v[28:31]\n" \
"v_lshlrev_b32 v138, 16, v129\n" \
"v_and_b32 v139, 0xffff0000, v129\n" \
"v_mfma_f32_16x16x32_bf16 v[60:63], a[220:223], a[148:151], 0\n" \
"buffer_load_dwordx4 v250, s[16:19], s74 offen lds\n" \
"v_mfma_f32_16x16x32_bf16 v[60:63], a[216:219], a[132:135], v[60:63]\n" \
"s_add_u32 m0, s72, 2048\n" \
"ds_read_b64_tr_b16 a[210:211], v240 offset:18944\n" \
"v_lshlrev_b32 v140, 16, v130\n" \
"v_mfma_f32_16x16x32_bf16 v[92:95], a[216:219], a[136:139], 0\n" \
"s_add_u32 s74, s71, 16640\n" \
"ds_read_b64_tr_b16 a[212:213], v240 offset:25088\n" \
"v_and_b32 v141, 0xffff0000, v130\n" \
"v_mfma_f32_16x16x32_bf16 v[92:95], a[220:223], a[152:155], v[92:95]\n" \
"buffer_load_dwordx4 v249, s[16:19], s74 offen lds\n" \
"v_mfma_f32_16x16x32_bf16 v[124:127], a[220:223], a[156:159], 0\n" \
"s_add_u32 m0, s72, 3072\n" \
"ds_read_b64_tr_b16 a[214:215], v240 offset:27136\n" \
"v_lshlrev_b32 v142, 16, v131\n" \
"v_mfma_f32_16x16x32_bf16 v[124:127], a[216:219], a[140:143], v[124:127]\n" \
"s_waitcnt lgkmcnt(8)\n" \
"v_and_b32 v143, 0xffff0000, v131\n" \
"v_lshlrev_b32 v144, 16, v132\n" \
"v_mfma_f32_16x16x32_bf16 a[0:3], a[128:131], a[192:195], 0\n" \
"s_add_u32 s74, s71, 24832\n" \
"buffer_load_dwordx4 v250, s[16:19], s74 offen lds\n" \
"v_mfma_f32_16x16x32_bf16 a[0:3], a[144:147], a[196:199], a[0:3]\n" \
"s_add_u32 m0, s72, 4096\n" \
"ds_read_b64_tr_b16 a[216:217], v241 offset:16896\n" \
"v_and_b32 v145, 0xffff0000, v132\n" \
"v_mfma_f32_16x16x32_bf16 a[4:7], a[148:151], a[196:199], 0\n" \
"s_add_u32 s74, s94, 33024\n" \
"ds_read_b64_tr_b16 a[218:219], v241 offset:18944\n" \
"v_lshlrev_b32 v146, 16, v133\n" \
"v_mfma_f32_16x16x32_bf16 a[4:7], a[132:135], a[192:195], a[4:7]\n" \
"buffer_load_dwordx4 v249, s[16:19], s74 offen lds\n" \
"v_mfma_f32_16x16x32_bf16 a[64:67], a[136:139], a[192:195], 0\n" \
"s_add_u32 m0, s70, 0\n" \
"ds_read_b64_tr_b16 a[220:221], v241 offset:25088\n" \
"v_and_b32 v147, 0xffff0000, v133\n" \
"v_mfma_f32_16x16x32_bf16 a[64:67], a[152:155], a[196:199], a[64:67]\n" \
"s_add_u32 s74, s69, 1048576\n" \
"v_lshlrev_b32 v148, 16, v134\n" \
"v_and_b32 v149, 0xffff0000, v134\n" \
"v_mfma_f32_16x16x32_bf16 a[68:71], a[156:159], a[196:199], 0\n" \
"buffer_load_dwordx4 v246, s[20:23], s74 offen lds\n" \
"v_mfma_f32_16x16x32_bf16 a[68:71], a[140:143], a[192:195], a[68:71]\n" \
"s_waitcnt lgkmcnt(7)\n" \
"ds_read_b64_tr_b16 a[222:223], v241 offset:27136\n" \
"v_lshlrev_b32 v150, 16, v135\n" \
"v_mfma_f32_16x16x32_bf16 a[8:11], a[128:131], a[200:203], 0\n" \
"s_add_u32 m0, s72, 5120\n" \
"ds_read_b64_tr_b16 a[192:193], v240 offset:17408\n" \
"v_and_b32 v151, 0xffff0000, v135\n" \
"v_mfma_f32_16x16x32_bf16 a[8:11], a[144:147], a[204:207], a[8:11]\n" \
"s_add_u32 s74, s94, 41216\n" \
"buffer_load_dwordx4 v250, s[16:19], s74 offen lds\n" \
"v_mfma_f32_16x16x32_bf16 a[12:15], a[148:151], a[204:207], 0\n" \
"s_add_u32 m0, s72, 6144\n" \
"ds_read_b64_tr_b16 a[194:195], v240 offset:19456\n" \
"v_add_f32 v152, v136, v137\n" \
"v_mfma_f32_16x16x32_bf16 a[12:15], a[132:135], a[200:203], a[12:15]\n" \
"s_add_u32 s74, s94, 49408\n" \
"v_add_f32 v154, v138, v139\n" \
"v_add_f32 v153, v144, v145\n" \
"v_mfma_f32_16x16x32_bf16 a[72:75], a[136:139], a[200:203], 0\n" \
"buffer_load_dwordx4 v249, s[16:19], s74 offen lds\n" \
"v_mfma_f32_16x16x32_bf16 a[72:75], a[152:155], a[204:207], a[72:75]\n" \
"s_add_u32 m0, s70, 1024\n" \
"ds_read_b64_tr_b16 a[196:197], v240 offset:25600\n" \
"v_add_f32 v155, v146, v147\n" \
"v_mfma_f32_16x16x32_bf16 a[76:79], a[156:159], a[204:207], 0\n" \
"s_add_u32 s74, s69, 1048704\n" \
"ds_read_b64_tr_b16 a[198:199], v240 offset:27648\n" \
"v_add_f32 v152, v152, v140\n" \
"v_mfma_f32_16x16x32_bf16 a[76:79], a[140:143], a[200:203], a[76:79]\n" \
"s_waitcnt lgkmcnt(8)\n" \
"buffer_load_dwordx4 v246, s[20:23], s74 offen lds\n" \
"v_mfma_f32_16x16x32_bf16 a[16:19], a[128:131], a[208:211], 0\n" \
"s_add_u32 m0, s72, 7168\n" \
"ds_read_b64_tr_b16 a[200:201], v241 offset:17408\n" \
"v_add_f32 v154, v154, v141\n" \
"v_mfma_f32_16x16x32_bf16 a[16:19], a[144:147], a[212:215], a[16:19]\n" \
"s_add_u32 s74, s94, 57600\n" \
"v_add_f32 v153, v153, v148\n" \
"v_add_f32 v155, v155, v149\n" \
"v_mfma_f32_16x16x32_bf16 a[20:23], a[148:151], a[212:215], 0\n" \
"buffer_load_dwordx4 v250, s[16:19], s74 offen lds\n" \
"v_mfma_f32_16x16x32_bf16 a[20:23], a[132:135], a[208:211], a[20:23]\n" \
"s_add_u32 m0, s70, 8192\n" \
"ds_read_b64_tr_b16 a[202:203], v241 offset:19456\n" \
"v_add_f32 v152, v152, v142\n" \
"v_mfma_f32_16x16x32_bf16 a[80:83], a[136:139], a[208:211], 0\n" \
"s_add_u32 s74, s69, 1310720\n" \
"ds_read_b64_tr_b16 a[204:205], v241 offset:25600\n" \
"v_add_f32 v154, v154, v143\n" \
"v_mfma_f32_16x16x32_bf16 a[80:83], a[152:155], a[212:215], a[80:83]\n" \
"buffer_load_dwordx4 v246, s[20:23], s74 offen lds\n" \
"v_mfma_f32_16x16x32_bf16 a[84:87], a[156:159], a[212:215], 0\n" \
"s_add_u32 m0, s70, 9216\n" \
"ds_read_b64_tr_b16 a[206:207], v241 offset:27648\n" \
"v_add_f32 v153, v153, v150\n" \
"v_mfma_f32_16x16x32_bf16 a[84:87], a[140:143], a[208:211], a[84:87]\n" \
"s_waitcnt lgkmcnt(8)\n" \
"v_add_f32 v155, v155, v151\n" \
"v_add_f32 v152, v152, v154\n" \
"v_mfma_f32_16x16x32_bf16 a[24:27], a[128:131], a[216:219], 0\n" \
"s_add_u32 s74, s69, 1310848\n" \
"buffer_load_dwordx4 v246, s[20:23], s74 offen lds\n" \
"v_mfma_f32_16x16x32_bf16 a[24:27], a[144:147], a[220:223], a[24:27]\n" \
"ds_read_b64_tr_b16 a[208:209], v240 offset:17920\n" \
"v_add_f32 v153, v153, v155\n" \
"v_mfma_f32_16x16x32_bf16 a[28:31], a[148:151], a[220:223], 0\n" \
"ds_read_b64_tr_b16 a[210:211], v240 offset:19968\n" \
"v_add_f32_dpp v152, v152, v152 quad_perm:[1,0,3,2] row_mask:0xf bank_mask:0xf\n" \
"v_mfma_f32_16x16x32_bf16 a[28:31], a[132:135], a[216:219], a[28:31]\n" \
"v_add_f32_dpp v153, v153, v153 quad_perm:[1,0,3,2] row_mask:0xf bank_mask:0xf\n" \
"v_mfma_f32_16x16x32_bf16 a[88:91], a[136:139], a[216:219], 0\n" \
"ds_read_b64_tr_b16 a[212:213], v240 offset:26112\n" \
"v_add_f32_dpp v152, v152, v152 quad_perm:[2,3,0,1] row_mask:0xf bank_mask:0xf\n" \
"v_mfma_f32_16x16x32_bf16 a[88:91], a[152:155], a[220:223], a[88:91]\n" \
".Lst0_0_%=:\n" \
"s_cbranch_execz .Lst0_4_%=\n" \
"v_add_f32_dpp v153, v153, v153 quad_perm:[2,3,0,1] row_mask:0xf bank_mask:0xf\n" \
"v_mfma_f32_16x16x32_bf16 a[92:95], a[156:159], a[220:223], 0\n" \
"ds_read_b64_tr_b16 a[214:215], v240 offset:28160\n" \
"v_add_f32_dpp v152, v152, v152 row_half_mirror row_mask:0xf bank_mask:0xf\n" \
"v_mfma_f32_16x16x32_bf16 a[92:95], a[140:143], a[216:219], a[92:95]\n" \
"s_waitcnt lgkmcnt(8)\n" \
"v_add_f32_dpp v153, v153, v153 row_half_mirror row_mask:0xf bank_mask:0xf\n" \
"v_mfma_f32_16x16x32_bf16 a[32:35], a[128:131], a[192:195], 0\n" \
"s_waitcnt vmcnt(21)\n" \
"ds_read_b64_tr_b16 a[216:217], v241 offset:17920\n" \
"ds_read_b128 a[160:163], v242 offset:8192\n" \
"v_mfma_f32_16x16x32_bf16 a[32:35], a[144:147], a[196:199], a[32:35]\n" \
"v_add_f32_dpp v152, v152, v152 row_mirror row_mask:0xf bank_mask:0xf\n" \
"v_add_f32_dpp v153, v153, v153 row_mirror row_mask:0xf bank_mask:0xf\n" \
"v_mfma_f32_16x16x32_bf16 a[36:39], a[148:151], a[196:199], 0\n" \
"ds_read_b64_tr_b16 a[218:219], v241 offset:19968\n" \
"ds_read_b128 a[164:167], v242 offset:10240\n" \
"v_mfma_f32_16x16x32_bf16 a[36:39], a[132:135], a[192:195], a[36:39]\n" \
"ds_read_b64_tr_b16 a[220:221], v241 offset:26112\n" \
"v_mov_b32 v154, v152\n" \
"v_mfma_f32_16x16x32_bf16 a[96:99], a[136:139], a[192:195], 0\n" \
"ds_read_b64_tr_b16 a[222:223], v241 offset:28160\n" \
"ds_read_b128 a[168:171], v242 offset:12288\n" \
"v_mfma_f32_16x16x32_bf16 a[96:99], a[152:155], a[196:199], a[96:99]\n" \
"v_mov_b32 v155, v153\n" \
"v_mfma_f32_16x16x32_bf16 a[100:103], a[156:159], a[196:199], 0\n" \
"ds_read_b128 a[172:175], v242 offset:14336\n" \
"v_mfma_f32_16x16x32_bf16 a[100:103], a[140:143], a[192:195], a[100:103]\n" \
"s_waitcnt lgkmcnt(12)\n" \
"v_permlane16_swap_b32 v152, v154\n" \
"v_mfma_f32_16x16x32_bf16 a[40:43], a[128:131], a[200:203], 0\n" \
"v_permlane16_swap_b32 v153, v155\n" \
"v_mfma_f32_16x16x32_bf16 a[40:43], a[144:147], a[204:207], a[40:43]\n" \
"v_add_f32 v152, v152, v154\n" \
"v_add_f32 v153, v153, v155\n" \
"v_mfma_f32_16x16x32_bf16 a[44:47], a[148:151], a[204:207], 0\n" \
"v_mov_b32 v154, v152\n" \
"v_mov_b32 v155, v153\n" \
"v_mfma_f32_16x16x32_bf16 a[44:47], a[132:135], a[200:203], a[44:47]\n" \
"v_mfma_f32_16x16x32_bf16 a[104:107], a[136:139], a[200:203], 0\n" \
"s_waitcnt vmcnt(17) lgkmcnt(2)\n" \
"s_barrier\n" \
"ds_read_b128 a[176:179], v244 offset:8192\n" \
"v_mfma_f32_16x16x32_bf16 a[104:107], a[152:155], a[204:207], a[104:107]\n" \
"s_add_u32 m0, s70, 16384\n" \
"ds_read_b64_tr_b16 a[192:193], v240 offset:32768\n" \
"ds_read_b64_tr_b16 a[194:195], v240 offset:34816\n" \
"v_mfma_f32_16x16x32_bf16 a[108:111], a[156:159], a[204:207], 0\n" \
"s_add_u32 s74, s69, 1048576\n" \
"buffer_load_dwordx4 v246, s[24:27], s74 offen lds\n" \
"v_mfma_f32_16x16x32_bf16 a[108:111], a[140:143], a[200:203], a[108:111]\n" \
"ds_read_b64_tr_b16 a[196:197], v240 offset:40960\n" \
"ds_read_b64_tr_b16 a[198:199], v240 offset:43008\n" \
"v_mfma_f32_16x16x32_bf16 a[48:51], a[128:131], a[208:211], 0\n" \
"ds_read_b64_tr_b16 a[200:201], v241 offset:32768\n" \
"ds_read_b128 a[180:183], v244 offset:10240\n" \
"v_mfma_f32_16x16x32_bf16 a[48:51], a[144:147], a[212:215], a[48:51]\n" \
"ds_read_b128 a[184:187], v244 offset:12288\n" \
"v_mfma_f32_16x16x32_bf16 a[52:55], a[148:151], a[212:215], 0\n" \
"ds_read_b64_tr_b16 a[202:203], v241 offset:34816\n" \
"ds_read_b128 a[188:191], v244 offset:14336\n" \
"v_mfma_f32_16x16x32_bf16 a[52:55], a[132:135], a[208:211], a[52:55]\n" \
"v_permlane32_swap_b32 v152, v154\n" \
"v_mfma_f32_16x16x32_bf16 a[112:115], a[136:139], a[208:211], 0\n" \
"ds_read_b64_tr_b16 a[204:205], v241 offset:40960\n" \
"v_mfma_f32_16x16x32_bf16 a[112:115], a[152:155], a[212:215], a[112:115]\n" \
"v_permlane32_swap_b32 v153, v155\n" \
"v_mfma_f32_16x16x32_bf16 a[116:119], a[156:159], a[212:215], 0\n" \
"ds_read_b64_tr_b16 a[206:207], v241 offset:43008\n" \
"v_add_f32 v152, v152, v154\n" \
"v_mfma_f32_16x16x32_bf16 a[116:119], a[140:143], a[208:211], a[116:119]\n" \
"v_add_f32 v153, v153, v155\n" \
"v_mul_f32 v152, 0x3b000000, v152\n" \
"v_mfma_f32_16x16x32_bf16 a[56:59], a[128:131], a[216:219], 0\n" \
"ds_read_b64_tr_b16 a[208:209], v240 offset:33280\n" \
"v_mul_f32 v153, 0x3b000000, v153\n" \
"v_mfma_f32_16x16x32_bf16 a[56:59], a[144:147], a[220:223], a[56:59]\n" \
"v_sub_f32 v254, v136, v152\n" \
"v_sub_f32 v255, v144, v153\n" \
"v_mfma_f32_16x16x32_bf16 a[60:63], a[148:151], a[220:223], 0\n" \
"s_add_u32 m0, s70, 17408\n" \
"ds_read_b64_tr_b16 a[210:211], v240 offset:35328\n" \
"v_mul_f32 v156, v254, v254\n" \
"v_mfma_f32_16x16x32_bf16 a[60:63], a[132:135], a[216:219], a[60:63]\n" \
"s_add_u32 s74, s69, 1048704\n" \
"v_mul_f32 v157, v255, v255\n" \
"v_sub_f32 v254, v137, v152\n" \
"v_mfma_f32_16x16x32_bf16 a[120:123], a[136:139], a[216:219], 0\n" \
"buffer_load_dwordx4 v246, s[24:27], s74 offen lds\n" \
"v_mfma_f32_16x16x32_bf16 a[120:123], a[152:155], a[220:223], a[120:123]\n" \
"ds_read_b64_tr_b16 a[212:213], v240 offset:41472\n" \
"v_sub_f32 v255, v145, v153\n" \
"v_mfma_f32_16x16x32_bf16 a[124:127], a[156:159], a[220:223], 0\n" \
"s_waitcnt lgkmcnt(5)\n" \
"ds_read_b64_tr_b16 a[214:215], v240 offset:43520\n" \
"v_mfma_f32_16x16x32_bf16 a[124:127], a[140:143], a[216:219], a[124:127]\n" \
"s_add_u32 m0, s72, 8192\n" \
"v_mul_f32 v158, v254, v254\n" \
"v_mul_f32 v159, v255, v255\n" \
"v_mfma_f32_16x16x32_bf16 v[0:3], a[192:195], a[160:163], v[0:3]\n" \
"s_add_u32 s74, s71, 384\n" \
"buffer_load_dwordx4 v249, s[16:19], s74 offen lds\n" \
"v_mfma_f32_16x16x32_bf16 v[0:3], a[196:199], a[176:179], v[0:3]\n" \
"ds_read_b64_tr_b16 a[216:217], v241 offset:33280\n" \
"v_sub_f32 v254, v138, v152\n" \
"v_mfma_f32_16x16x32_bf16 v[32:35], a[196:199], a[180:183], v[32:35]\n" \
"ds_read_b64_tr_b16 a[218:219], v241 offset:35328\n" \
"v_sub_f32 v255, v146, v153\n" \
"v_mfma_f32_16x16x32_bf16 v[32:35], a[192:195], a[164:167], v[32:35]\n" \
"v_fmac_f32 v156, v254, v254\n" \
"v_fmac_f32 v157, v255, v255\n" \
"v_mfma_f32_16x16x32_bf16 v[64:67], a[192:195], a[168:171], v[64:67]\n" \
"s_add_u32 m0, s72, 9216\n" \
"ds_read_b64_tr_b16 a[220:221], v241 offset:41472\n" \
"v_sub_f32 v254, v139, v152\n" \
"v_mfma_f32_16x16x32_bf16 v[64:67], a[196:199], a[184:187], v[64:67]\n" \
"s_add_u32 s74, s71, 8576\n" \
"v_sub_f32 v255, v147, v153\n" \
"v_fmac_f32 v158, v254, v254\n" \
"v_mfma_f32_16x16x32_bf16 v[96:99], a[196:199], a[188:191], v[96:99]\n" \
"buffer_load_dwordx4 v250, s[16:19], s74 offen lds\n" \
"v_mfma_f32_16x16x32_bf16 v[96:99], a[192:195], a[172:175], v[96:99]\n" \
"s_waitcnt lgkmcnt(7)\n" \
"ds_read_b64_tr_b16 a[222:223], v241 offset:43520\n" \
"v_fmac_f32 v159, v255, v255\n" \
"v_mfma_f32_16x16x32_bf16 v[4:7], a[200:203], a[160:163], v[4:7]\n" \
"s_add_u32 m0, s70, 24576\n" \
"ds_read_b64_tr_b16 a[192:193], v240 offset:33792\n" \
"v_sub_f32 v254, v140, v152\n" \
"v_mfma_f32_16x16x32_bf16 v[4:7], a[204:207], a[176:179], v[4:7]\n" \
"s_add_u32 s74, s69, 1310720\n" \
"v_sub_f32 v255, v148, v153\n" \
"v_fmac_f32 v156, v254, v254\n" \
"v_mfma_f32_16x16x32_bf16 v[36:39], a[204:207], a[180:183], v[36:39]\n" \
"buffer_load_dwordx4 v246, s[24:27], s74 offen lds\n" \
"v_mfma_f32_16x16x32_bf16 v[36:39], a[200:203], a[164:167], v[36:39]\n" \
"s_add_u32 m0, s72, 10240\n" \
"ds_read_b64_tr_b16 a[194:195], v240 offset:35840\n" \
"v_fmac_f32 v157, v255, v255\n" \
"v_mfma_f32_16x16x32_bf16 v[68:71], a[200:203], a[168:171], v[68:71]\n" \
"s_add_u32 s74, s71, 16768\n" \
"ds_read_b64_tr_b16 a[196:197], v240 offset:41984\n" \
"v_sub_f32 v254, v141, v152\n" \
"v_mfma_f32_16x16x32_bf16 v[68:71], a[204:207], a[184:187], v[68:71]\n" \
"buffer_load_dwordx4 v249, s[16:19], s74 offen lds\n" \
"v_mfma_f32_16x16x32_bf16 v[100:103], a[204:207], a[188:191], v[100:103]\n" \
"ds_read_b64_tr_b16 a[198:199], v240 offset:44032\n" \
"v_sub_f32 v255, v149, v153\n" \
"v_mfma_f32_16x16x32_bf16 v[100:103], a[200:203], a[172:175], v[100:103]\n" \
"s_waitcnt lgkmcnt(8)\n" \
"v_fmac_f32 v158, v254, v254\n" \
"v_fmac_f32 v159, v255, v255\n" \
"v_mfma_f32_16x16x32_bf16 v[8:11], a[208:211], a[160:163], v[8:11]\n" \
"s_add_u32 m0, s72, 11264\n" \
"ds_read_b64_tr_b16 a[200:201], v241 offset:33792\n" \
"v_sub_f32 v254, v142, v152\n" \
"v_mfma_f32_16x16x32_bf16 v[8:11], a[212:215], a[176:179], v[8:11]\n" \
"s_add_u32 s74, s71, 24960\n" \
"v_sub_f32 v255, v150, v153\n" \
"v_fmac_f32 v156, v254, v254\n" \
"v_mfma_f32_16x16x32_bf16 v[40:43], a[212:215], a[180:183], v[40:43]\n" \
"s_lshl2_add_u32 s85, s67, %[wid]\n" \
"buffer_load_dwordx4 v250, s[16:19], s74 offen lds\n" \
"v_mfma_f32_16x16x32_bf16 v[40:43], a[208:211], a[164:167], v[40:43]\n" \
"s_lshl_b32 s85, s85, 16\n" \
"ds_read_b64_tr_b16 a[202:203], v241 offset:35840\n" \
"v_fmac_f32 v157, v255, v255\n" \
"v_mfma_f32_16x16x32_bf16 v[72:75], a[208:211], a[168:171], v[72:75]\n" \
"s_lshl_b32 s86, s68, 8\n" \
"ds_read_b64_tr_b16 a[204:205], v241 offset:41984\n" \
"v_sub_f32 v254, v143, v152\n" \
"v_mfma_f32_16x16x32_bf16 v[72:75], a[212:215], a[184:187], v[72:75]\n" \
"s_add_u32 s85, s85, s86\n" \
"v_sub_f32 v255, v151, v153\n" \
"v_fmac_f32 v158, v254, v254\n" \
"v_mfma_f32_16x16x32_bf16 v[104:107], a[212:215], a[188:191], v[104:107]\n" \
"s_add_u32 m0, s70, 25600\n" \
"ds_read_b64_tr_b16 a[206:207], v241 offset:44032\n" \
"v_fmac_f32 v159, v255, v255\n" \
"v_mfma_f32_16x16x32_bf16 v[104:107], a[208:211], a[172:175], v[104:107]\n" \
"s_waitcnt lgkmcnt(8)\n" \
"v_add_f32 v156, v156, v158\n" \
"v_add_f32 v157, v157, v159\n" \
"v_mfma_f32_16x16x32_bf16 v[12:15], a[216:219], a[160:163], v[12:15]\n" \
"s_add_u32 s74, s69, 1310848\n" \
"buffer_load_dwordx4 v246, s[24:27], s74 offen lds\n" \
"v_mfma_f32_16x16x32_bf16 v[12:15], a[220:223], a[176:179], v[12:15]\n" \
"s_add_u32 m0, s72, 12288\n" \
"ds_read_b64_tr_b16 a[208:209], v240 offset:34304\n" \
"v_add_f32_dpp v156, v156, v156 quad_perm:[1,0,3,2] row_mask:0xf bank_mask:0xf\n" \
"v_mfma_f32_16x16x32_bf16 v[44:47], a[220:223], a[180:183], v[44:47]\n" \
"s_add_u32 s74, s94, 33152\n" \
"ds_read_b64_tr_b16 a[210:211], v240 offset:36352\n" \
"v_add_f32_dpp v157, v157, v157 quad_perm:[1,0,3,2] row_mask:0xf bank_mask:0xf\n" \
"v_mfma_f32_16x16x32_bf16 v[44:47], a[216:219], a[164:167], v[44:47]\n" \
"s_add_u32 s85, s85, 2097152\n" \
"buffer_load_dwordx4 v249, s[16:19], s74 offen lds\n" \
"v_mfma_f32_16x16x32_bf16 v[76:79], a[216:219], a[168:171], v[76:79]\n" \
"s_add_u32 m0, s72, 13312\n" \
"buffer_load_dword v238, v208, s[20:23], s85 offen\n" \
"v_add_f32_dpp v156, v156, v156 quad_perm:[2,3,0,1] row_mask:0xf bank_mask:0xf\n" \
"v_mfma_f32_16x16x32_bf16 v[76:79], a[220:223], a[184:187], v[76:79]\n" \
"s_add_u32 s74, s94, 41344\n" \
"ds_read_b64_tr_b16 a[212:213], v240 offset:42496\n" \
"v_add_f32_dpp v157, v157, v157 quad_perm:[2,3,0,1] row_mask:0xf bank_mask:0xf\n" \
"v_mfma_f32_16x16x32_bf16 v[108:111], a[220:223], a[188:191], v[108:111]\n" \
"ds_read_b64_tr_b16 a[214:215], v240 offset:44544\n" \
"v_add_f32_dpp v156, v156, v156 row_half_mirror row_mask:0xf bank_mask:0xf\n" \
"v_mfma_f32_16x16x32_bf16 v[108:111], a[216:219], a[172:175], v[108:111]\n" \
"s_waitcnt lgkmcnt(8)\n" \
"buffer_load_dwordx4 v250, s[16:19], s74 offen lds\n" \
"v_mfma_f32_16x16x32_bf16 v[16:19], a[192:195], a[160:163], v[16:19]\n" \
"ds_read_b64_tr_b16 a[216:217], v241 offset:34304\n" \
"v_add_f32_dpp v157, v157, v157 row_half_mirror row_mask:0xf bank_mask:0xf\n" \
"v_mfma_f32_16x16x32_bf16 v[16:19], a[196:199], a[176:179], v[16:19]\n" \
"v_add_f32_dpp v156, v156, v156 row_mirror row_mask:0xf bank_mask:0xf\n" \
"v_mfma_f32_16x16x32_bf16 v[48:51], a[196:199], a[180:183], v[48:51]\n" \
"s_add_u32 m0, s72, 14336\n" \
"ds_read_b64_tr_b16 a[218:219], v241 offset:36352\n" \
"v_add_f32_dpp v157, v157, v157 row_mirror row_mask:0xf bank_mask:0xf\n" \
"v_mfma_f32_16x16x32_bf16 v[48:51], a[192:195], a[164:167], v[48:51]\n" \
"s_add_u32 s74, s94, 49536\n" \
"ds_read_b64_tr_b16 a[220:221], v241 offset:42496\n" \
"v_mov_b32 v158, v156\n" \
"v_mfma_f32_16x16x32_bf16 v[80:83], a[192:195], a[168:171], v[80:83]\n" \
"buffer_load_dwordx4 v249, s[16:19], s74 offen lds\n" \
"v_mfma_f32_16x16x32_bf16 v[80:83], a[196:199], a[184:187], v[80:83]\n" \
"ds_read_b64_tr_b16 a[222:223], v241 offset:44544\n" \
"v_mov_b32 v159, v157\n" \
"v_mfma_f32_16x16x32_bf16 v[112:115], a[196:199], a[188:191], v[112:115]\n" \
"v_permlane16_swap_b32 v156, v158\n" \
"v_mfma_f32_16x16x32_bf16 v[112:115], a[192:195], a[172:175], v[112:115]\n" \
"s_waitcnt lgkmcnt(8)\n" \
"v_permlane16_swap_b32 v157, v159\n" \
"v_mfma_f32_16x16x32_bf16 v[20:23], a[200:203], a[160:163], v[20:23]\n" \
"s_add_u32 m0, s72, 15360\n" \
"v_add_f32 v156, v156, v158\n" \
"v_add_f32 v157, v157, v159\n" \
"v_mfma_f32_16x16x32_bf16 v[20:23], a[204:207], a[176:179], v[20:23]\n" \
"s_add_u32 s74, s94, 57728\n" \
"v_mov_b32 v158, v156\n" \
"v_mov_b32 v159, v157\n" \
"v_mfma_f32_16x16x32_bf16 v[52:55], a[204:207], a[180:183], v[52:55]\n" \
"buffer_load_dwordx4 v250, s[16:19], s74 offen lds\n" \
"v_mfma_f32_16x16x32_bf16 v[52:55], a[200:203], a[164:167], v[52:55]\n" \
"v_permlane32_swap_b32 v156, v158\n" \
"v_mfma_f32_16x16x32_bf16 v[84:87], a[200:203], a[168:171], v[84:87]\n" \
"s_waitcnt vmcnt(26) lgkmcnt(0)\n" \
"s_barrier\n" \
"s_add_u32 m0, s70, 32768\n" \
"v_mfma_f32_16x16x32_bf16 v[84:87], a[204:207], a[184:187], v[84:87]\n" \
"s_add_u32 s74, s69, 1572864\n" \
"ds_read_b64_tr_b16 a[192:193], v240 offset:49152\n" \
"ds_read_b64_tr_b16 a[194:195], v240 offset:51200\n" \
"v_mfma_f32_16x16x32_bf16 v[116:119], a[204:207], a[188:191], v[116:119]\n" \
"buffer_load_dwordx4 v246, s[20:23], s74 offen lds\n" \
"v_mfma_f32_16x16x32_bf16 v[116:119], a[200:203], a[172:175], v[116:119]\n" \
"ds_read_b64_tr_b16 a[196:197], v240 offset:57344\n" \
"ds_read_b64_tr_b16 a[198:199], v240 offset:59392\n" \
"v_mfma_f32_16x16x32_bf16 v[24:27], a[208:211], a[160:163], v[24:27]\n" \
"ds_read_b64_tr_b16 a[200:201], v241 offset:49152\n" \
"v_mfma_f32_16x16x32_bf16 v[24:27], a[212:215], a[176:179], v[24:27]\n" \
"v_permlane32_swap_b32 v157, v159\n" \
"v_mfma_f32_16x16x32_bf16 v[56:59], a[212:215], a[180:183], v[56:59]\n" \
"ds_read_b64_tr_b16 a[202:203], v241 offset:51200\n" \
"v_add_f32 v156, v156, v158\n" \
"v_mfma_f32_16x16x32_bf16 v[56:59], a[208:211], a[164:167], v[56:59]\n" \
"v_add_f32 v157, v157, v159\n" \
"v_mul_f32 v156, 0x3b000000, v156\n" \
"v_mfma_f32_16x16x32_bf16 v[88:91], a[208:211], a[168:171], v[88:91]\n" \
"ds_read_b64_tr_b16 a[204:205], v241 offset:57344\n" \
"v_mul_f32 v157, 0x3b000000, v157\n" \
"v_mfma_f32_16x16x32_bf16 v[88:91], a[212:215], a[184:187], v[88:91]\n" \
"v_cvt_pk_bf16_f32 v254, v156, v157\n" \
"v_cvt_pk_bf16_f32 v255, v152, v153\n" \
"v_mfma_f32_16x16x32_bf16 v[120:123], a[212:215], a[188:191], v[120:123]\n" \
"ds_read_b64_tr_b16 a[206:207], v241 offset:59392\n" \
"v_lshlrev_b32 v156, 16, v254\n" \
"v_mfma_f32_16x16x32_bf16 v[120:123], a[208:211], a[172:175], v[120:123]\n" \
"v_and_b32 v157, 0xffff0000, v254\n" \
"v_lshlrev_b32 v152, 16, v255\n" \
"v_mfma_f32_16x16x32_bf16 v[28:31], a[216:219], a[160:163], v[28:31]\n" \
"ds_read_b64_tr_b16 a[208:209], v240 offset:49664\n" \
"v_and_b32 v153, 0xffff0000, v255\n" \
"v_mfma_f32_16x16x32_bf16 v[28:31], a[220:223], a[176:179], v[28:31]\n" \
"v_add_f32 v156, 0x3727c5ac, v156\n" \
"v_add_f32 v157, 0x3727c5ac, v157\n" \
"v_mfma_f32_16x16x32_bf16 v[60:63], a[220:223], a[180:183], v[60:63]\n" \
"s_add_u32 m0, s70, 33792\n" \
"ds_read_b64_tr_b16 a[210:211], v240 offset:51712\n" \
"v_cvt_pk_bf16_f32 v254, v156, v157\n" \
"v_mfma_f32_16x16x32_bf16 v[60:63], a[216:219], a[164:167], v[60:63]\n" \
"s_add_u32 s74, s69, 1572992\n" \
"v_lshlrev_b32 v156, 16, v254\n" \
"v_and_b32 v157, 0xffff0000, v254\n" \
"v_mfma_f32_16x16x32_bf16 v[92:95], a[216:219], a[168:171], v[92:95]\n" \
"buffer_load_dwordx4 v246, s[20:23], s74 offen lds\n" \
"v_mfma_f32_16x16x32_bf16 v[92:95], a[220:223], a[184:187], v[92:95]\n" \
"ds_read_b64_tr_b16 a[212:213], v240 offset:57856\n" \
"v_mfma_f32_16x16x32_bf16 v[124:127], a[220:223], a[188:191], v[124:127]\n" \
"s_waitcnt vmcnt(17)\n" \
"ds_read_b64_tr_b16 a[214:215], v240 offset:59904\n" \
"ds_read_b128 a[224:227], v242 offset:0\n" \
"v_mfma_f32_16x16x32_bf16 v[124:127], a[216:219], a[172:175], v[124:127]\n" \
"s_waitcnt lgkmcnt(9)\n" \
"v_sqrt_f32 v156, v156\n" \
"v_mfma_f32_16x16x32_bf16 a[0:3], a[160:163], a[192:195], a[0:3]\n" \
"ds_read_b64_tr_b16 a[216:217], v241 offset:49664\n" \
"ds_read_b128 a[228:231], v242 offset:2048\n" \
"v_mfma_f32_16x16x32_bf16 a[0:3], a[176:179], a[196:199], a[0:3]\n" \
"v_sqrt_f32 v157, v157\n" \
"v_mfma_f32_16x16x32_bf16 a[4:7], a[180:183], a[196:199], a[4:7]\n" \
"ds_read_b64_tr_b16 a[218:219], v241 offset:51712\n" \
"ds_read_b128 a[232:235], v242 offset:4096\n" \
"v_mfma_f32_16x16x32_bf16 a[4:7], a[164:167], a[192:195], a[4:7]\n" \
"v_cvt_pk_bf16_f32 v254, v156, v157\n" \
"v_lshlrev_b32 v156, 16, v254\n" \
"v_mfma_f32_16x16x32_bf16 a[64:67], a[168:171], a[192:195], a[64:67]\n" \
"ds_read_b64_tr_b16 a[220:221], v241 offset:57856\n" \
"ds_read_b128 a[236:239], v242 offset:6144\n" \
"v_mfma_f32_16x16x32_bf16 a[64:67], a[184:187], a[196:199], a[64:67]\n" \
"v_and_b32 v157, 0xffff0000, v254\n" \
"v_mfma_f32_16x16x32_bf16 a[68:71], a[188:191], a[196:199], a[68:71]\n" \
"ds_read_b64_tr_b16 a[222:223], v241 offset:59904\n" \
"v_mfma_f32_16x16x32_bf16 a[68:71], a[172:175], a[192:195], a[68:71]\n" \
"s_waitcnt lgkmcnt(12)\n" \
"v_rcp_f32 v156, v156\n" \
"v_mfma_f32_16x16x32_bf16 a[8:11], a[160:163], a[200:203], a[8:11]\n" \
"s_add_u32 m0, s70, 40960\n" \
"ds_read_b64_tr_b16 a[192:193], v240 offset:50176\n" \
"v_mfma_f32_16x16x32_bf16 a[8:11], a[176:179], a[204:207], a[8:11]\n" \
"s_add_u32 s74, s69, 1835008\n" \
"v_rcp_f32 v157, v157\n" \
"v_mfma_f32_16x16x32_bf16 a[12:15], a[180:183], a[204:207], a[12:15]\n" \
"buffer_load_dwordx4 v246, s[20:23], s74 offen lds\n" \
"v_mfma_f32_16x16x32_bf16 a[12:15], a[164:167], a[200:203], a[12:15]\n" \
"ds_read_b64_tr_b16 a[194:195], v240 offset:52224\n" \
"ds_read_b128 a[240:243], v244 offset:0\n" \
"v_mfma_f32_16x16x32_bf16 a[72:75], a[168:171], a[200:203], a[72:75]\n" \
"ds_read_b64_tr_b16 a[196:197], v240 offset:58368\n" \
"ds_read_b128 a[244:247], v244 offset:2048\n" \
"v_mfma_f32_16x16x32_bf16 a[72:75], a[184:187], a[204:207], a[72:75]\n" \
"v_sub_f32 v136, v136, v152\n" \
"v_sub_f32 v137, v137, v152\n" \
"v_mfma_f32_16x16x32_bf16 a[76:79], a[188:191], a[204:207], a[76:79]\n" \
"ds_read_b64_tr_b16 a[198:199], v240 offset:60416\n" \
"ds_read_b128 a[248:251], v244 offset:4096\n" \
"v_mfma_f32_16x16x32_bf16 a[76:79], a[172:175], a[200:203], a[76:79]\n" \
"s_waitcnt lgkmcnt(15)\n" \
"v_cvt_pk_bf16_f32 v128, v136, v137\n" \
"v_lshlrev_b32 v136, 16, v128\n" \
"v_mfma_f32_16x16x32_bf16 a[16:19], a[160:163], a[208:211], a[16:19]\n" \
"s_lshl2_add_u32 s81, s64, %[wid]\n" \
"ds_read_b64_tr_b16 a[200:201], v241 offset:50176\n" \
"ds_read_b128 a[252:255], v244 offset:6144\n" \
"v_mfma_f32_16x16x32_bf16 a[16:19], a[176:179], a[212:215], a[16:19]\n" \
"s_lshl_b32 s81, s81, 9\n" \
"v_and_b32 v137, 0xffff0000, v128\n" \
"v_mul_f32 v136, v136, v156\n" \
"v_mfma_f32_16x16x32_bf16 a[20:23], a[180:183], a[212:215], a[20:23]\n" \
"s_add_u32 s81, s81, 573440\n" \
"buffer_load_dwordx2 v[204:205], v253, s[32:35], s81 offen sc1\n" \
"v_mul_f32 v137, v137, v156\n" \
"v_mfma_f32_16x16x32_bf16 a[20:23], a[164:167], a[208:211], a[20:23]\n" \
"ds_read_b64_tr_b16 a[202:203], v241 offset:52224\n" \
"v_cvt_pk_bf16_f32 v128, v136, v137\n" \
"v_mfma_f32_16x16x32_bf16 a[80:83], a[168:171], a[208:211], a[80:83]\n" \
"ds_read_b64_tr_b16 a[204:205], v241 offset:58368\n" \
"v_sub_f32 v138, v138, v152\n" \
"v_mfma_f32_16x16x32_bf16 a[80:83], a[184:187], a[212:215], a[80:83]\n" \
"v_sub_f32 v139, v139, v152\n" \
"v_cvt_pk_bf16_f32 v129, v138, v139\n" \
"v_mfma_f32_16x16x32_bf16 a[84:87], a[188:191], a[212:215], a[84:87]\n" \
"s_add_u32 m0, s70, 41984\n" \
"ds_read_b64_tr_b16 a[206:207], v241 offset:60416\n" \
"v_lshlrev_b32 v138, 16, v129\n" \
"v_mfma_f32_16x16x32_bf16 a[84:87], a[172:175], a[208:211], a[84:87]\n" \
"s_waitcnt lgkmcnt(12)\n" \
"v_and_b32 v139, 0xffff0000, v129\n" \
"v_mul_f32 v138, v138, v156\n" \
"v_mfma_f32_16x16x32_bf16 a[24:27], a[160:163], a[216:219], a[24:27]\n" \
"s_add_u32 s74, s69, 1835136\n" \
"buffer_load_dwordx4 v246, s[20:23], s74 offen lds\n" \
"v_mfma_f32_16x16x32_bf16 a[24:27], a[176:179], a[220:223], a[24:27]\n" \
"ds_read_b64_tr_b16 a[208:209], v240 offset:50688\n" \
"v_mul_f32 v139, v139, v156\n" \
"v_mfma_f32_16x16x32_bf16 a[28:31], a[180:183], a[220:223], a[28:31]\n" \
"ds_read_b64_tr_b16 a[210:211], v240 offset:52736\n" \
"v_cvt_pk_bf16_f32 v129, v138, v139\n" \
"v_mfma_f32_16x16x32_bf16 a[28:31], a[164:167], a[216:219], a[28:31]\n" \
"v_sub_f32 v140, v140, v152\n" \
"v_sub_f32 v141, v141, v152\n" \
"v_mfma_f32_16x16x32_bf16 a[88:91], a[168:171], a[216:219], a[88:91]\n" \
"s_add_u32 m0, s72, 0\n" \
"ds_read_b64_tr_b16 a[212:213], v240 offset:58880\n" \
"v_cvt_pk_bf16_f32 v130, v140, v141\n" \
"v_mfma_f32_16x16x32_bf16 a[88:91], a[184:187], a[220:223], a[88:91]\n" \
"s_add_u32 s74, s71, 512\n" \
"v_lshlrev_b32 v140, 16, v130\n" \
"v_and_b32 v141, 0xffff0000, v130\n" \
"v_mfma_f32_16x16x32_bf16 a[92:95], a[188:191], a[220:223], a[92:95]\n" \
"s_waitcnt lgkmcnt(6)\n" \
"buffer_load_dwordx4 v249, s[16:19], s74 offen lds\n" \
"v_mfma_f32_16x16x32_bf16 a[92:95], a[172:175], a[216:219], a[92:95]\n" \
"ds_read_b64_tr_b16 a[214:215], v240 offset:60928\n" \
"v_mul_f32 v140, v140, v156\n" \
"v_mfma_f32_16x16x32_bf16 a[32:35], a[160:163], a[192:195], a[32:35]\n" \
"ds_read_b64_tr_b16 a[216:217], v241 offset:50688\n" \
"v_mul_f32 v141, v141, v156\n" \
"v_mfma_f32_16x16x32_bf16 a[32:35], a[176:179], a[196:199], a[32:35]\n" \
"s_lshl2_add_u32 s85, s67, %[wid]\n" \
"v_cvt_pk_bf16_f32 v130, v140, v141\n" \
"v_sub_f32 v142, v142, v152\n" \
"v_mfma_f32_16x16x32_bf16 a[36:39], a[180:183], a[196:199], a[36:39]\n" \
"s_add_u32 m0, s72, 1024\n" \
"ds_read_b64_tr_b16 a[218:219], v241 offset:52736\n" \
"v_sub_f32 v143, v143, v152\n" \
"v_mfma_f32_16x16x32_bf16 a[36:39], a[164:167], a[192:195], a[36:39]\n" \
"s_add_u32 s74, s71, 8704\n" \
"ds_read_b64_tr_b16 a[220:221], v241 offset:58880\n" \
"v_cvt_pk_bf16_f32 v131, v142, v143\n" \
"v_mfma_f32_16x16x32_bf16 a[96:99], a[168:171], a[192:195], a[96:99]\n" \
"s_lshl_b32 s85, s85, 16\n" \
"buffer_load_dwordx4 v250, s[16:19], s74 offen lds\n" \
"v_mfma_f32_16x16x32_bf16 a[96:99], a[184:187], a[196:199], a[96:99]\n" \
"s_lshl_b32 s86, s68, 8\n" \
"ds_read_b64_tr_b16 a[222:223], v241 offset:60928\n" \
"v_lshlrev_b32 v142, 16, v131\n" \
"v_mfma_f32_16x16x32_bf16 a[100:103], a[188:191], a[196:199], a[100:103]\n" \
"s_add_u32 s85, s85, s86\n" \
"v_and_b32 v143, 0xffff0000, v131\n" \
"v_mul_f32 v142, v142, v156\n" \
"v_mfma_f32_16x16x32_bf16 a[100:103], a[172:175], a[192:195], a[100:103]\n" \
"s_waitcnt lgkmcnt(8)\n" \
"v_mul_f32 v143, v143, v156\n" \
"v_cvt_pk_bf16_f32 v131, v142, v143\n" \
"v_mfma_f32_16x16x32_bf16 a[40:43], a[160:163], a[200:203], a[40:43]\n" \
"s_add_u32 m0, s72, 2048\n" \
"v_sub_f32 v144, v144, v153\n" \
"v_sub_f32 v145, v145, v153\n" \
"v_mfma_f32_16x16x32_bf16 a[40:43], a[176:179], a[204:207], a[40:43]\n" \
"s_add_u32 s74, s71, 16896\n" \
"v_cvt_pk_bf16_f32 v132, v144, v145\n" \
"v_lshlrev_b32 v144, 16, v132\n" \
"v_mfma_f32_16x16x32_bf16 a[44:47], a[180:183], a[204:207], a[44:47]\n" \
"s_add_u32 s85, s85, 2097152\n" \
"buffer_load_dwordx4 v249, s[16:19], s74 offen lds\n" \
"v_mfma_f32_16x16x32_bf16 a[44:47], a[164:167], a[200:203], a[44:47]\n" \
"buffer_load_dword v238, v208, s[24:27], s85 offen\n" \
"v_and_b32 v145, 0xffff0000, v132\n" \
"v_mfma_f32_16x16x32_bf16 a[104:107], a[168:171], a[200:203], a[104:107]\n" \
"s_waitcnt vmcnt(22) lgkmcnt(0)\n" \
"s_barrier\n" \
"v_mul_f32 v144, v144, v157\n" \
"v_mfma_f32_16x16x32_bf16 a[104:107], a[184:187], a[204:207], a[104:107]\n" \
"s_add_u32 m0, s70, 49152\n" \
"ds_read_b64_tr_b16 a[192:193], v240 offset:0\n" \
"ds_read_b64_tr_b16 a[194:195], v240 offset:2048\n" \
"v_mfma_f32_16x16x32_bf16 a[108:111], a[188:191], a[204:207], a[108:111]\n" \
"s_add_u32 s74, s69, 1572864\n" \
"buffer_load_dwordx4 v246, s[24:27], s74 offen lds\n" \
"v_mfma_f32_16x16x32_bf16 a[108:111], a[172:175], a[200:203], a[108:111]\n" \
"s_add_u32 m0, s72, 3072\n" \
"ds_read_b64_tr_b16 a[196:197], v240 offset:8192\n" \
"ds_read_b64_tr_b16 a[198:199], v240 offset:10240\n" \
"v_mfma_f32_16x16x32_bf16 a[48:51], a[160:163], a[208:211], a[48:51]\n" \
"s_add_u32 s74, s71, 25088\n" \
"ds_read_b64_tr_b16 a[200:201], v241 offset:0\n" \
"v_mul_f32 v145, v145, v157\n" \
"v_mfma_f32_16x16x32_bf16 a[48:51], a[176:179], a[212:215], a[48:51]\n" \
"buffer_load_dwordx4 v250, s[16:19], s74 offen lds\n" \
"v_mfma_f32_16x16x32_bf16 a[52:55], a[180:183], a[212:215], a[52:55]\n" \
"ds_read_b64_tr_b16 a[202:203], v241 offset:2048\n" \
"v_cvt_pk_bf16_f32 v132, v144, v145\n" \
"v_mfma_f32_16x16x32_bf16 a[52:55], a[164:167], a[208:211], a[52:55]\n" \
"v_sub_f32 v146, v146, v153\n" \
"v_sub_f32 v147, v147, v153\n" \
"v_mfma_f32_16x16x32_bf16 a[112:115], a[168:171], a[208:211], a[112:115]\n" \
"s_add_u32 m0, s72, 4096\n" \
"ds_read_b64_tr_b16 a[204:205], v241 offset:8192\n" \
"v_cvt_pk_bf16_f32 v133, v146, v147\n" \
"v_mfma_f32_16x16x32_bf16 a[112:115], a[184:187], a[212:215], a[112:115]\n" \
"s_add_u32 s74, s94, 33280\n" \
"v_lshlrev_b32 v146, 16, v133\n" \
"v_and_b32 v147, 0xffff0000, v133\n" \
"v_mfma_f32_16x16x32_bf16 a[116:119], a[188:191], a[212:215], a[116:119]\n" \
"buffer_load_dwordx4 v249, s[16:19], s74 offen lds\n" \
"v_mfma_f32_16x16x32_bf16 a[116:119], a[172:175], a[208:211], a[116:119]\n" \
"ds_read_b64_tr_b16 a[206:207], v241 offset:10240\n" \
"v_mul_f32 v146, v146, v157\n" \
"v_mfma_f32_16x16x32_bf16 a[56:59], a[160:163], a[216:219], a[56:59]\n" \
"ds_read_b64_tr_b16 a[208:209], v240 offset:512\n" \
"v_mul_f32 v147, v147, v157\n" \
"v_mfma_f32_16x16x32_bf16 a[56:59], a[176:179], a[220:223], a[56:59]\n" \
"v_cvt_pk_bf16_f32 v133, v146, v147\n" \
"v_sub_f32 v148, v148, v153\n" \
"v_mfma_f32_16x16x32_bf16 a[60:63], a[180:183], a[220:223], a[60:63]\n" \
"s_add_u32 m0, s70, 50176\n" \
"ds_read_b64_tr_b16 a[210:211], v240 offset:2560\n" \
"v_sub_f32 v149, v149, v153\n" \
"v_mfma_f32_16x16x32_bf16 a[60:63], a[164:167], a[216:219], a[60:63]\n" \
"s_add_u32 s74, s69, 1572992\n" \
"v_cvt_pk_bf16_f32 v134, v148, v149\n" \
"v_lshlrev_b32 v148, 16, v134\n" \
"v_mfma_f32_16x16x32_bf16 a[120:123], a[168:171], a[216:219], a[120:123]\n" \
"buffer_load_dwordx4 v246, s[24:27], s74 offen lds\n" \
"v_mfma_f32_16x16x32_bf16 a[120:123], a[184:187], a[220:223], a[120:123]\n" \
"s_add_u32 m0, s72, 5120\n" \
"ds_read_b64_tr_b16 a[212:213], v240 offset:8704\n" \
"v_and_b32 v149, 0xffff0000, v134\n" \
"v_mfma_f32_16x16x32_bf16 a[124:127], a[188:191], a[220:223], a[124:127]\n" \
"s_add_u32 s74, s94, 41472\n" \
"ds_read_b64_tr_b16 a[214:215], v240 offset:10752\n" \
"v_mul_f32 v148, v148, v157\n" \
"v_mfma_f32_16x16x32_bf16 a[124:127], a[172:175], a[216:219], a[124:127]\n" \
"s_waitcnt lgkmcnt(8)\n" \
"buffer_load_dwordx4 v250, s[16:19], s74 offen lds\n" \
"v_mfma_f32_16x16x32_bf16 v[0:3], a[192:195], a[224:227], v[0:3]\n" \
"s_add_u32 m0, s72, 6144\n" \
"ds_read_b64_tr_b16 a[216:217], v241 offset:512\n" \
"v_mul_f32 v149, v149, v157\n" \
"v_mfma_f32_16x16x32_bf16 v[0:3], a[196:199], a[240:243], v[0:3]\n" \
"s_add_u32 s74, s94, 49664\n" \
"v_cvt_pk_bf16_f32 v134, v148, v149\n" \
"v_sub_f32 v150, v150, v153\n" \
"v_mfma_f32_16x16x32_bf16 v[32:35], a[196:199], a[244:247], v[32:35]\n" \
"buffer_load_dwordx4 v249, s[16:19], s74 offen lds\n" \
"v_mfma_f32_16x16x32_bf16 v[32:35], a[192:195], a[228:231], v[32:35]\n" \
"ds_read_b64_tr_b16 a[218:219], v241 offset:2560\n" \
"v_sub_f32 v151, v151, v153\n" \
"v_mfma_f32_16x16x32_bf16 v[64:67], a[192:195], a[232:235], v[64:67]\n" \
"ds_read_b64_tr_b16 a[220:221], v241 offset:8704\n" \
"v_cvt_pk_bf16_f32 v135, v150, v151\n" \
"v_mfma_f32_16x16x32_bf16 v[64:67], a[196:199], a[248:251], v[64:67]\n" \
"v_lshlrev_b32 v150, 16, v135\n" \
"v_and_b32 v151, 0xffff0000, v135\n" \
"v_mfma_f32_16x16x32_bf16 v[96:99], a[196:199], a[252:255], v[96:99]\n" \
"s_add_u32 m0, s72, 7168\n" \
"ds_read_b64_tr_b16 a[222:223], v241 offset:10752\n" \
"v_mul_f32 v150, v150, v157\n" \
"v_mfma_f32_16x16x32_bf16 v[96:99], a[192:195], a[236:239], v[96:99]\n" \
"s_waitcnt lgkmcnt(8)\n" \
"v_mul_f32 v151, v151, v157\n" \
"v_cvt_pk_bf16_f32 v135, v150, v151\n" \
"v_mfma_f32_16x16x32_bf16 v[4:7], a[200:203], a[224:227], v[4:7]\n" \
"s_add_u32 s74, s94, 57856\n" \
"buffer_load_dwordx4 v250, s[16:19], s74 offen lds\n" \
"v_mfma_f32_16x16x32_bf16 v[4:7], a[204:207], a[240:243], v[4:7]\n" \
"s_add_u32 m0, s70, 57344\n" \
"s_add_u32 s74, s69, 1835008\n" \
"ds_read_b64_tr_b16 a[192:193], v240 offset:1024\n" \
"v_mfma_f32_16x16x32_bf16 v[36:39], a[204:207], a[244:247], v[36:39]\n" \
"s_mul_i32 s76, %[wid], 2080\n" \
"s_add_u32 s76, s76, 149504\n" \
"ds_read_b64_tr_b16 a[194:195], v240 offset:3072\n" \
"v_mfma_f32_16x16x32_bf16 v[36:39], a[200:203], a[228:231], v[36:39]\n" \
"buffer_load_dwordx4 v246, s[24:27], s74 offen lds\n" \
"v_mfma_f32_16x16x32_bf16 v[68:71], a[200:203], a[232:235], v[68:71]\n" \
"ds_read_b64_tr_b16 a[196:197], v240 offset:9216\n" \
"v_add_u32 v254, s76, v248\n" \
"v_mfma_f32_16x16x32_bf16 v[68:71], a[204:207], a[248:251], v[68:71]\n" \
"ds_write_b128 v254, v[128:131]\n" \
"v_mfma_f32_16x16x32_bf16 v[100:103], a[204:207], a[252:255], v[100:103]\n" \
"ds_read_b64_tr_b16 a[198:199], v240 offset:11264\n" \
"v_mfma_f32_16x16x32_bf16 v[100:103], a[200:203], a[236:239], v[100:103]\n" \
"s_waitcnt lgkmcnt(9)\n" \
"ds_write_b128 v254, v[132:135] offset:1040\n" \
"v_and_b32 v254, 7, v251\n" \
"v_mfma_f32_16x16x32_bf16 v[8:11], a[208:211], a[224:227], v[8:11]\n" \
"ds_read_b64_tr_b16 a[200:201], v241 offset:1024\n" \
"v_mul_u32_u24 v254, 1040, v254\n" \
"v_mfma_f32_16x16x32_bf16 v[8:11], a[212:215], a[240:243], v[8:11]\n" \
"v_lshrrev_b32 v255, 3, v251\n" \
"v_lshl_add_u32 v254, v255, 4, v254\n" \
"v_mfma_f32_16x16x32_bf16 v[40:43], a[212:215], a[244:247], v[40:43]\n" \
"s_lshl_b32 s76, %[wid], 7\n" \
"s_add_u32 s76, s76, 149504\n" \
"ds_read_b64_tr_b16 a[202:203], v241 offset:3072\n" \
"v_mfma_f32_16x16x32_bf16 v[40:43], a[208:211], a[228:231], v[40:43]\n" \
"v_add_u32 v254, s76, v254\n" \
"v_mfma_f32_16x16x32_bf16 v[72:75], a[208:211], a[232:235], v[72:75]\n" \
"ds_read_b64_tr_b16 a[204:205], v241 offset:9216\n" \
"v_mfma_f32_16x16x32_bf16 v[72:75], a[212:215], a[248:251], v[72:75]\n" \
"v_mfma_f32_16x16x32_bf16 v[104:107], a[212:215], a[252:255], v[104:107]\n" \
"s_add_u32 m0, s70, 58368\n" \
"ds_read_b64_tr_b16 a[206:207], v241 offset:11264\n" \
"v_mfma_f32_16x16x32_bf16 v[104:107], a[208:211], a[236:239], v[104:107]\n" \
".Lst0_1_%=:\n" \
"s_cbranch_execz .Lst0_5_%=\n" \
"s_waitcnt lgkmcnt(10)\n" \
"s_add_u32 s74, s69, 1835136\n" \
"v_mfma_f32_16x16x32_bf16 v[12:15], a[216:219], a[224:227], v[12:15]\n" \
"buffer_load_dwordx4 v246, s[24:27], s74 offen lds\n" \
"v_mfma_f32_16x16x32_bf16 v[12:15], a[220:223], a[240:243], v[12:15]\n" \
"ds_read_b64_tr_b16 a[208:209], v240 offset:1536\n" \
"v_mfma_f32_16x16x32_bf16 v[44:47], a[220:223], a[244:247], v[44:47]\n" \
"ds_read_b64_tr_b16 a[210:211], v240 offset:3584\n" \
"v_mfma_f32_16x16x32_bf16 v[44:47], a[216:219], a[228:231], v[44:47]\n" \
"v_mfma_f32_16x16x32_bf16 v[76:79], a[216:219], a[232:235], v[76:79]\n" \
"ds_read_b64_tr_b16 a[212:213], v240 offset:9728\n" \
"v_mfma_f32_16x16x32_bf16 v[76:79], a[220:223], a[248:251], v[76:79]\n" \
"v_mfma_f32_16x16x32_bf16 v[108:111], a[220:223], a[252:255], v[108:111]\n" \
"ds_read_b64_tr_b16 a[214:215], v240 offset:11776\n" \
"v_mfma_f32_16x16x32_bf16 v[108:111], a[216:219], a[236:239], v[108:111]\n" \
"s_waitcnt lgkmcnt(9)\n" \
"v_mfma_f32_16x16x32_bf16 v[16:19], a[192:195], a[224:227], v[16:19]\n" \
"s_waitcnt vmcnt(14)\n" \
"ds_read_b64_tr_b16 a[216:217], v241 offset:1536\n" \
"v_cmp_ne_u64 vcc, v[204:205], v[202:203]\n" \
"v_mfma_f32_16x16x32_bf16 v[16:19], a[196:199], a[240:243], v[16:19]\n" \
"v_mfma_f32_16x16x32_bf16 v[48:51], a[196:199], a[244:247], v[48:51]\n" \
"ds_read_b64_tr_b16 a[218:219], v241 offset:3584\n" \
"v_mfma_f32_16x16x32_bf16 v[48:51], a[192:195], a[228:231], v[48:51]\n" \
"s_cbranch_vccnz .Lspq_%=\n" \
".Lsbq_%=:\n" \
"ds_read_b64_tr_b16 a[220:221], v241 offset:9728\n" \
"v_mfma_f32_16x16x32_bf16 v[80:83], a[192:195], a[232:235], v[80:83]\n" \
"ds_read_b64_tr_b16 a[222:223], v241 offset:11776\n" \
"v_mfma_f32_16x16x32_bf16 v[80:83], a[196:199], a[248:251], v[80:83]\n" \
"v_mfma_f32_16x16x32_bf16 v[112:115], a[196:199], a[252:255], v[112:115]\n" \
"v_mfma_f32_16x16x32_bf16 v[112:115], a[192:195], a[236:239], v[112:115]\n" \
"s_waitcnt lgkmcnt(8)\n" \
"v_mfma_f32_16x16x32_bf16 v[20:23], a[200:203], a[224:227], v[20:23]\n" \
"v_mfma_f32_16x16x32_bf16 v[20:23], a[204:207], a[240:243], v[20:23]\n" \
"v_mfma_f32_16x16x32_bf16 v[52:55], a[204:207], a[244:247], v[52:55]\n" \
"v_mfma_f32_16x16x32_bf16 v[52:55], a[200:203], a[228:231], v[52:55]\n" \
"v_mfma_f32_16x16x32_bf16 v[84:87], a[200:203], a[232:235], v[84:87]\n" \
"s_waitcnt lgkmcnt(0)\n" \
"s_barrier\n" \
"ds_read_b128 a[128:131], v242 offset:8192\n" \
"v_mfma_f32_16x16x32_bf16 v[84:87], a[204:207], a[248:251], v[84:87]\n" \
"s_add_u32 m0, s70, 0\n" \
"ds_read_b64_tr_b16 a[192:193], v240 offset:16384\n" \
"ds_read_b64_tr_b16 a[194:195], v240 offset:18432\n" \
"v_mfma_f32_16x16x32_bf16 v[116:119], a[204:207], a[252:255], v[116:119]\n" \
"s_add_u32 s74, s69, 2097152\n" \
"buffer_load_dwordx4 v246, s[20:23], s74 offen lds\n" \
"v_mfma_f32_16x16x32_bf16 v[116:119], a[200:203], a[236:239], v[116:119]\n" \
"s_lshl_b32 s85, %[wid], 10\n" \
"ds_read_b64_tr_b16 a[196:197], v240 offset:24576\n" \
"ds_read_b64_tr_b16 a[198:199], v240 offset:26624\n" \
"v_mfma_f32_16x16x32_bf16 v[24:27], a[208:211], a[224:227], v[24:27]\n" \
"s_add_u32 s85, s85, 917504\n" \
"buffer_load_dwordx4 v[168:171], v248, s[32:35], s85 offen sc1\n" \
"v_and_b32 v252, 3, v251\n" \
"v_mfma_f32_16x16x32_bf16 v[24:27], a[212:215], a[240:243], v[24:27]\n" \
"s_lshl_b32 s85, %[wid], 10\n" \
"ds_read_b64_tr_b16 a[200:201], v241 offset:16384\n" \
"ds_read_b128 a[132:135], v242 offset:10240\n" \
"v_mfma_f32_16x16x32_bf16 v[56:59], a[212:215], a[244:247], v[56:59]\n" \
"s_add_u32 s85, s85, 929792\n" \
"buffer_load_dwordx4 v[172:175], v248, s[32:35], s85 offen sc1\n" \
"v_mul_u32_u24 v252, 0x3000, v252\n" \
"v_mfma_f32_16x16x32_bf16 v[56:59], a[208:211], a[228:231], v[56:59]\n" \
"s_lshl_b32 s85, %[wid], 10\n" \
"ds_read_b64_tr_b16 a[202:203], v241 offset:18432\n" \
"ds_read_b128 a[136:139], v242 offset:12288\n" \
"v_mfma_f32_16x16x32_bf16 v[88:91], a[208:211], a[232:235], v[88:91]\n" \
"s_add_u32 s85, s85, 942080\n" \
"buffer_load_dwordx4 v[176:179], v248, s[32:35], s85 offen sc1\n" \
"v_and_b32 v253, 28, v251\n" \
"v_mfma_f32_16x16x32_bf16 v[88:91], a[212:215], a[248:251], v[88:91]\n" \
"s_lshl_b32 s85, %[wid], 10\n" \
"ds_read_b64_tr_b16 a[204:205], v241 offset:24576\n" \
"ds_read_b128 a[140:143], v242 offset:14336\n" \
"v_mfma_f32_16x16x32_bf16 v[120:123], a[212:215], a[252:255], v[120:123]\n" \
"s_add_u32 s85, s85, 954368\n" \
"buffer_load_dwordx4 v[180:183], v248, s[32:35], s85 offen sc1\n" \
"v_add_u32 v252, v252, v253\n" \
"v_mfma_f32_16x16x32_bf16 v[120:123], a[208:211], a[236:239], v[120:123]\n" \
"s_lshl_b32 s85, %[bid], 5\n" \
"s_add_u32 s85, s85, 921600\n" \
"ds_read_b64_tr_b16 a[206:207], v241 offset:26624\n" \
"v_mfma_f32_16x16x32_bf16 v[28:31], a[216:219], a[224:227], v[28:31]\n" \
"s_cmp_lt_u32 %[wid], 2\n" \
"s_cselect_b32 s83, 0, 1.0\n" \
"buffer_load_dword v200, v252, s[32:35], s85 offen sc1\n" \
"v_mfma_f32_16x16x32_bf16 v[28:31], a[220:223], a[240:243], v[28:31]\n" \
"ds_read_b64_tr_b16 a[208:209], v240 offset:16896\n" \
"ds_read_b128 a[144:147], v244 offset:8192\n" \
"v_mfma_f32_16x16x32_bf16 v[60:63], a[220:223], a[244:247], v[60:63]\n" \
"s_add_u32 m0, s70, 1024\n" \
"ds_read_b64_tr_b16 a[210:211], v240 offset:18944\n" \
"ds_read_b128 a[148:151], v244 offset:10240\n" \
"v_mfma_f32_16x16x32_bf16 v[60:63], a[216:219], a[228:231], v[60:63]\n" \
"s_add_u32 s74, s69, 2097280\n" \
"v_mfma_f32_16x16x32_bf16 v[92:95], a[216:219], a[232:235], v[92:95]\n" \
"buffer_load_dwordx4 v246, s[20:23], s74 offen lds\n" \
"v_mfma_f32_16x16x32_bf16 v[92:95], a[220:223], a[248:251], v[92:95]\n" \
"ds_read_b64_tr_b16 a[212:213], v240 offset:25088\n" \
"ds_read_b128 a[152:155], v244 offset:12288\n" \
"v_mfma_f32_16x16x32_bf16 v[124:127], a[220:223], a[252:255], v[124:127]\n" \
"ds_read_b64_tr_b16 a[214:215], v240 offset:27136\n" \
"ds_read_b128 a[156:159], v244 offset:14336\n" \
"v_mfma_f32_16x16x32_bf16 v[124:127], a[216:219], a[236:239], v[124:127]\n" \
"s_waitcnt lgkmcnt(15)\n" \
"v_mfma_f32_16x16x32_bf16 a[0:3], a[224:227], a[192:195], a[0:3]\n" \
"ds_read_b64_tr_b16 a[216:217], v241 offset:16896\n" \
"v_mfma_f32_16x16x32_bf16 a[0:3], a[240:243], a[196:199], a[0:3]\n" \
"v_mfma_f32_16x16x32_bf16 a[4:7], a[244:247], a[196:199], a[4:7]\n" \
"ds_read_b64_tr_b16 a[218:219], v241 offset:18944\n" \
"v_mfma_f32_16x16x32_bf16 a[4:7], a[228:231], a[192:195], a[4:7]\n" \
"v_mfma_f32_16x16x32_bf16 a[64:67], a[232:235], a[192:195], a[64:67]\n" \
"ds_read_b64_tr_b16 a[220:221], v241 offset:25088\n" \
"v_mfma_f32_16x16x32_bf16 a[64:67], a[248:251], a[196:199], a[64:67]\n" \
"v_mfma_f32_16x16x32_bf16 a[68:71], a[252:255], a[196:199], a[68:71]\n" \
"ds_read_b64_tr_b16 a[222:223], v241 offset:27136\n" \
"v_mfma_f32_16x16x32_bf16 a[68:71], a[236:239], a[192:195], a[68:71]\n" \
"s_waitcnt lgkmcnt(12)\n" \
"v_mfma_f32_16x16x32_bf16 a[8:11], a[224:227], a[200:203], a[8:11]\n" \
"s_add_u32 m0, s70, 8192\n" \
"ds_read_b64_tr_b16 a[192:193], v240 offset:17408\n" \
"v_mfma_f32_16x16x32_bf16 a[8:11], a[240:243], a[204:207], a[8:11]\n" \
"s_add_u32 s74, s69, 2359296\n" \
"v_mfma_f32_16x16x32_bf16 a[12:15], a[244:247], a[204:207], a[12:15]\n" \
"s_lshl_b32 s89, %[wid], 10\n" \
"buffer_load_dwordx4 v246, s[20:23], s74 offen lds\n" \
"v_mfma_f32_16x16x32_bf16 a[12:15], a[228:231], a[200:203], a[12:15]\n" \
"s_add_u32 m0, s72, 8192\n" \
"s_add_u32 s74, s71, 640\n" \
"ds_read_b64_tr_b16 a[194:195], v240 offset:19456\n" \
"v_mfma_f32_16x16x32_bf16 a[72:75], a[232:235], a[200:203], a[72:75]\n" \
"s_add_u32 s89, s89, 159744\n" \
"ds_read_b64_tr_b16 a[196:197], v240 offset:25600\n" \
"v_add_u32 v236, s89, v248\n" \
"v_mfma_f32_16x16x32_bf16 a[72:75], a[248:251], a[204:207], a[72:75]\n" \
"s_waitcnt lgkmcnt(7)\n" \
"buffer_load_dwordx4 v249, s[16:19], s74 offen lds\n" \
"v_mfma_f32_16x16x32_bf16 a[76:79], a[252:255], a[204:207], a[76:79]\n" \
"s_mov_b32 s77, 0x80b583ec\n" \
"ds_read_b64_tr_b16 a[198:199], v240 offset:27648\n" \
"v_lshrrev_b32 v237, 3, v251\n" \
"v_mfma_f32_16x16x32_bf16 a[76:79], a[236:239], a[200:203], a[76:79]\n" \
"v_lshlrev_b32 v237, 5, v237\n" \
"v_mov_b32 v253, 0x61c88646\n" \
"v_mfma_f32_16x16x32_bf16 a[16:19], a[224:227], a[208:211], a[16:19]\n" \
"s_add_u32 m0, s72, 9216\n" \
"ds_read_b64_tr_b16 a[200:201], v241 offset:17408\n" \
"v_add_co_u32 v202, vcc, s77, v202\n" \
"v_mfma_f32_16x16x32_bf16 a[16:19], a[240:243], a[212:215], a[16:19]\n" \
"s_add_u32 s74, s71, 8832\n" \
"s_lshl_b32 s89, %[wid], 8\n" \
"s_add_u32 s89, s89, 159744\n" \
"v_mfma_f32_16x16x32_bf16 a[20:23], a[244:247], a[212:215], a[20:23]\n" \
"buffer_load_dwordx4 v250, s[16:19], s74 offen lds\n" \
"v_mfma_f32_16x16x32_bf16 a[20:23], a[228:231], a[208:211], a[20:23]\n" \
"s_or_b32 s85, %[bid], %[wid]\n" \
"ds_read_b64_tr_b16 a[202:203], v241 offset:19456\n" \
"v_add_u32 v237, s89, v237\n" \
"v_mfma_f32_16x16x32_bf16 a[80:83], a[232:235], a[208:211], a[80:83]\n" \
"s_cmp_eq_u32 s85, 0\n" \
"s_cselect_b32 s85, 720896, -1\n" \
"ds_read_b64_tr_b16 a[204:205], v241 offset:25600\n" \
"v_mfma_f32_16x16x32_bf16 a[80:83], a[248:251], a[212:215], a[80:83]\n" \
"s_lshr_b32 s93, %[bid], 6\n" \
"v_addc_co_u32 v203, vcc, v253, v203, vcc\n" \
"v_mov_b32 v253, 0\n" \
"v_mfma_f32_16x16x32_bf16 a[84:87], a[252:255], a[212:215], a[84:87]\n" \
"s_add_u32 m0, s70, 9216\n" \
"buffer_store_dwordx2 v[202:203], v253, s[32:35], s85 offen sc1\n" \
"v_mfma_f32_16x16x32_bf16 a[84:87], a[236:239], a[208:211], a[84:87]\n" \
"s_waitcnt lgkmcnt(7)\n" \
"s_add_u32 s74, s69, 2359424\n" \
"ds_read_b64_tr_b16 a[206:207], v241 offset:27648\n" \
"v_mfma_f32_16x16x32_bf16 a[24:27], a[224:227], a[216:219], a[24:27]\n" \
"s_cmp_eq_u32 s93, 2\n" \
"s_cselect_b32 s93, 1.0, 0\n" \
"ds_read_b64_tr_b16 a[208:209], v240 offset:17920\n" \
"v_mfma_f32_16x16x32_bf16 a[24:27], a[240:243], a[220:223], a[24:27]\n" \
"buffer_load_dwordx4 v246, s[20:23], s74 offen lds\n" \
"v_mfma_f32_16x16x32_bf16 a[28:31], a[244:247], a[220:223], a[28:31]\n" \
"s_add_u32 m0, s72, 10240\n" \
"s_add_u32 s74, s71, 17024\n" \
"ds_read_b64_tr_b16 a[210:211], v240 offset:19968\n" \
"v_mfma_f32_16x16x32_bf16 a[28:31], a[228:231], a[216:219], a[28:31]\n" \
"v_mfma_f32_16x16x32_bf16 a[88:91], a[232:235], a[216:219], a[88:91]\n" \
"buffer_load_dwordx4 v249, s[16:19], s74 offen lds\n" \
"v_mfma_f32_16x16x32_bf16 a[88:91], a[248:251], a[220:223], a[88:91]\n" \
"s_add_u32 m0, s72, 11264\n" \
"s_add_u32 s74, s71, 25216\n" \
"ds_read_b64_tr_b16 a[212:213], v240 offset:26112\n" \
"v_mfma_f32_16x16x32_bf16 a[92:95], a[252:255], a[220:223], a[92:95]\n" \
"ds_read_b64_tr_b16 a[214:215], v240 offset:28160\n" \
"v_mfma_f32_16x16x32_bf16 a[92:95], a[236:239], a[216:219], a[92:95]\n" \
"s_waitcnt lgkmcnt(8)\n" \
"buffer_load_dwordx4 v250, s[16:19], s74 offen lds\n" \
"v_mfma_f32_16x16x32_bf16 a[32:35], a[224:227], a[192:195], a[32:35]\n" \
"ds_read_b64_tr_b16 a[216:217], v241 offset:17920\n" \
"v_mfma_f32_16x16x32_bf16 a[32:35], a[240:243], a[196:199], a[32:35]\n" \
"v_mfma_f32_16x16x32_bf16 a[36:39], a[244:247], a[196:199], a[36:39]\n" \
"s_add_u32 m0, s72, 12288\n" \
"ds_read_b64_tr_b16 a[218:219], v241 offset:19968\n" \
"v_mfma_f32_16x16x32_bf16 a[36:39], a[228:231], a[192:195], a[36:39]\n" \
"s_add_u32 s74, s94, 33408\n" \
"ds_read_b64_tr_b16 a[220:221], v241 offset:26112\n" \
"v_mfma_f32_16x16x32_bf16 a[96:99], a[232:235], a[192:195], a[96:99]\n" \
"buffer_load_dwordx4 v249, s[16:19], s74 offen lds\n" \
"v_mfma_f32_16x16x32_bf16 a[96:99], a[248:251], a[196:199], a[96:99]\n" \
"ds_read_b64_tr_b16 a[222:223], v241 offset:28160\n" \
"v_mfma_f32_16x16x32_bf16 a[100:103], a[252:255], a[196:199], a[100:103]\n" \
"v_mfma_f32_16x16x32_bf16 a[100:103], a[236:239], a[192:195], a[100:103]\n" \
"s_waitcnt lgkmcnt(8)\n" \
"v_mfma_f32_16x16x32_bf16 a[40:43], a[224:227], a[200:203], a[40:43]\n" \
"s_add_u32 m0, s72, 13312\n" \
"v_mfma_f32_16x16x32_bf16 a[40:43], a[240:243], a[204:207], a[40:43]\n" \
"s_add_u32 s74, s94, 41600\n" \
"v_mfma_f32_16x16x32_bf16 a[44:47], a[244:247], a[204:207], a[44:47]\n" \
"buffer_load_dwordx4 v250, s[16:19], s74 offen lds\n" \
"v_mfma_f32_16x16x32_bf16 a[44:47], a[228:231], a[200:203], a[44:47]\n" \
"v_mfma_f32_16x16x32_bf16 a[104:107], a[232:235], a[200:203], a[104:107]\n" \
"s_waitcnt vmcnt(29) lgkmcnt(0)\n" \
"s_barrier\n" \
"s_add_u32 m0, s70, 16384\n" \
"v_mfma_f32_16x16x32_bf16 a[104:107], a[248:251], a[204:207], a[104:107]\n" \
"s_add_u32 s74, s69, 2621440\n" \
"ds_read_b64_tr_b16 a[192:193], v240 offset:32768\n" \
"ds_read_b64_tr_b16 a[194:195], v240 offset:34816\n" \
"v_mfma_f32_16x16x32_bf16 a[108:111], a[252:255], a[204:207], a[108:111]\n" \
"buffer_load_dwordx4 v246, s[20:23], s74 offen lds\n" \
"v_mfma_f32_16x16x32_bf16 a[108:111], a[236:239], a[200:203], a[108:111]\n" \
"s_add_u32 m0, s72, 14336\n" \
"ds_read_b64_tr_b16 a[196:197], v240 offset:40960\n" \
"ds_read_b64_tr_b16 a[198:199], v240 offset:43008\n" \
"v_mfma_f32_16x16x32_bf16 a[48:51], a[224:227], a[208:211], a[48:51]\n" \
"s_add_u32 s74, s94, 49792\n" \
"ds_read_b64_tr_b16 a[200:201], v241 offset:32768\n" \
"ds_read_b128 v[160:163], v254\n" \
"v_mfma_f32_16x16x32_bf16 a[48:51], a[240:243], a[212:215], a[48:51]\n" \
"s_lshl2_add_u32 s85, s65, %[wid]\n" \
"buffer_load_dwordx4 v249, s[16:19], s74 offen lds\n" \
"v_mfma_f32_16x16x32_bf16 a[52:55], a[244:247], a[212:215], a[52:55]\n" \
"s_lshl_b32 s85, s85, 15\n" \
"ds_read_b64_tr_b16 a[202:203], v241 offset:34816\n" \
"ds_read_b128 v[164:167], v254 offset:512\n" \
"v_mfma_f32_16x16x32_bf16 a[52:55], a[228:231], a[208:211], a[52:55]\n" \
"s_waitcnt vmcnt(13)\n" \
"s_lshl_b32 s86, s64, 10\n" \
"v_add_f32 v168, v168, v172\n" \
"v_mfma_f32_16x16x32_bf16 a[112:115], a[232:235], a[208:211], a[112:115]\n" \
"s_add_u32 m0, s72, 15360\n" \
"ds_read_b64_tr_b16 a[204:205], v241 offset:40960\n" \
"v_add_f32 v176, v176, v180\n" \
"v_mfma_f32_16x16x32_bf16 a[112:115], a[248:251], a[212:215], a[112:115]\n" \
"s_add_u32 s74, s94, 57984\n" \
"v_add_f32 v169, v169, v173\n" \
"v_add_f32 v177, v177, v181\n" \
"v_mfma_f32_16x16x32_bf16 a[116:119], a[252:255], a[212:215], a[116:119]\n" \
"s_add_u32 s85, s85, s86\n" \
"buffer_load_dwordx4 v250, s[16:19], s74 offen lds\n" \
"v_mfma_f32_16x16x32_bf16 a[116:119], a[236:239], a[208:211], a[116:119]\n" \
"buffer_load_dword v238, v211, s[28:31], s85 offen offset:256\n" \
"v_add_f32 v170, v170, v174\n" \
"v_mfma_f32_16x16x32_bf16 a[56:59], a[224:227], a[216:219], a[56:59]\n" \
"s_getpc_b64 s[96:97]\n" \
".Lpf1_%=:\n" \
"s_add_u32 s96, s96, .Lat_b_%=-.Lpf1_%=\n" \
"s_addc_u32 s97, s97, 0\n" \
"v_mfma_f32_16x16x32_bf16 a[56:59], a[240:243], a[220:223], a[56:59]\n" \
"s_and_b32 s97, s97, 0xffff\n" \
"ds_read_b64_tr_b16 a[206:207], v241 offset:43008\n" \
"ds_read_b64_tr_b16 a[208:209], v240 offset:33280\n" \
"v_mfma_f32_16x16x32_bf16 a[60:63], a[244:247], a[220:223], a[60:63]\n" \
"s_add_u32 m0, s70, 17408\n" \
"ds_read_b64_tr_b16 a[210:211], v240 offset:35328\n" \
"v_add_f32 v178, v178, v182\n" \
"v_mfma_f32_16x16x32_bf16 a[60:63], a[228:231], a[216:219], a[60:63]\n" \
"s_add_u32 s74, s69, 2621568\n" \
"v_add_f32 v171, v171, v175\n" \
"v_add_f32 v179, v179, v183\n" \
"v_mfma_f32_16x16x32_bf16 a[120:123], a[232:235], a[216:219], a[120:123]\n" \
"s_mov_b32 s98, .Lat_code_e-.Lat_b_%=\n" \
"buffer_load_dwordx4 v246, s[20:23], s74 offen lds\n" \
"v_mfma_f32_16x16x32_bf16 a[120:123], a[248:251], a[220:223], a[120:123]\n" \
"s_lshl2_add_u32 s85, s65, %[wid]\n" \
"ds_read_b64_tr_b16 a[212:213], v240 offset:41472\n" \
"v_add_f32 v168, v168, v176\n" \
"v_mfma_f32_16x16x32_bf16 a[124:127], a[252:255], a[220:223], a[124:127]\n" \
"s_and_b32 s85, s85, 31\n" \
"ds_read_b64_tr_b16 a[214:215], v240 offset:43520\n" \
"v_add_f32 v169, v169, v177\n" \
"v_mfma_f32_16x16x32_bf16 a[124:127], a[236:239], a[216:219], a[124:127]\n" \
"s_waitcnt lgkmcnt(10)\n" \
"v_add_f32 v170, v170, v178\n" \
"v_add_f32 v171, v171, v179\n" \
"v_mfma_f32_16x16x32_bf16 v[0:3], a[192:195], a[128:131], v[0:3]\n" \
"s_lshl_b32 s85, s85, 11\n" \
"buffer_load_dword v238, v209, s[96:99], s85 offen\n" \
"v_cvt_pk_bf16_f32 v172, v168, v169\n" \
"v_mfma_f32_16x16x32_bf16 v[0:3], a[196:199], a[144:147], v[0:3]\n" \
"ds_read_b64_tr_b16 a[216:217], v241 offset:33280\n" \
"v_cvt_pk_bf16_f32 v173, v170, v171\n" \
"v_mfma_f32_16x16x32_bf16 v[32:35], a[196:199], a[148:151], v[32:35]\n" \
"ds_read_b64_tr_b16 a[218:219], v241 offset:35328\n" \
"v_lshlrev_b32 v168, 16, v172\n" \
"v_mfma_f32_16x16x32_bf16 v[32:35], a[192:195], a[132:135], v[32:35]\n" \
"v_and_b32 v169, 0xffff0000, v172\n" \
"v_lshlrev_b32 v170, 16, v173\n" \
"v_mfma_f32_16x16x32_bf16 v[64:67], a[192:195], a[136:139], v[64:67]\n" \
"ds_read_b64_tr_b16 a[220:221], v241 offset:41472\n" \
"v_and_b32 v171, 0xffff0000, v173\n" \
"v_mfma_f32_16x16x32_bf16 v[64:67], a[196:199], a[152:155], v[64:67]\n" \
"v_add_f32 v168, s83, v168\n" \
"v_add_f32 v169, s83, v169\n" \
"v_mfma_f32_16x16x32_bf16 v[96:99], a[196:199], a[156:159], v[96:99]\n" \
"ds_read_b64_tr_b16 a[222:223], v241 offset:43520\n" \
"v_add_f32 v170, s83, v170\n" \
"v_mfma_f32_16x16x32_bf16 v[96:99], a[192:195], a[140:143], v[96:99]\n" \
"s_waitcnt lgkmcnt(8)\n" \
"v_add_f32 v171, s83, v171\n" \
"v_cvt_pk_bf16_f32 v172, v168, v169\n" \
"v_mfma_f32_16x16x32_bf16 v[4:7], a[200:203], a[128:131], v[4:7]\n" \
"s_add_u32 m0, s70, 24576\n" \
"ds_read_b64_tr_b16 a[192:193], v240 offset:33792\n" \
"v_cvt_pk_bf16_f32 v173, v170, v171\n" \
"v_mfma_f32_16x16x32_bf16 v[4:7], a[204:207], a[144:147], v[4:7]\n" \
"s_add_u32 s74, s69, 2883584\n" \
"v_lshlrev_b32 v168, 16, v172\n" \
"v_and_b32 v169, 0xffff0000, v172\n" \
"v_mfma_f32_16x16x32_bf16 v[36:39], a[204:207], a[148:151], v[36:39]\n" \
"buffer_load_dwordx4 v246, s[20:23], s74 offen lds\n" \
"v_mfma_f32_16x16x32_bf16 v[36:39], a[200:203], a[132:135], v[36:39]\n" \
"ds_read_b64_tr_b16 a[194:195], v240 offset:35840\n" \
"v_lshlrev_b32 v170, 16, v173\n" \
"v_mfma_f32_16x16x32_bf16 v[68:71], a[200:203], a[136:139], v[68:71]\n" \
"ds_read_b64_tr_b16 a[196:197], v240 offset:41984\n" \
"v_and_b32 v171, 0xffff0000, v173\n" \
"v_mfma_f32_16x16x32_bf16 v[68:71], a[204:207], a[152:155], v[68:71]\n" \
"s_waitcnt vmcnt(17)\n" \
"ds_write_b128 v236, v[168:171]\n" \
"v_add_f32_dpp v200, v200, v200 quad_perm:[1,0,3,2] row_mask:0xf bank_mask:0xf\n" \
"v_mfma_f32_16x16x32_bf16 v[100:103], a[204:207], a[156:159], v[100:103]\n" \
"ds_read_b64_tr_b16 a[198:199], v240 offset:44032\n" \
"v_lshrrev_b32 v152, 5, v251\n" \
"v_mfma_f32_16x16x32_bf16 v[100:103], a[200:203], a[140:143], v[100:103]\n" \
"s_waitcnt lgkmcnt(9)\n" \
"v_add_f32_dpp v200, v200, v200 quad_perm:[2,3,0,1] row_mask:0xf bank_mask:0xf\n" \
"v_cvt_pk_bf16_f32 v200, v200, v200\n" \
"v_mfma_f32_16x16x32_bf16 v[8:11], a[208:211], a[128:131], v[8:11]\n" \
"s_lshl3_add_u32 s79, s67, %[wid]\n" \
"ds_read_b64_tr_b16 a[200:201], v241 offset:33792\n" \
"ds_read_b128 a[160:163], v242 offset:0\n" \
"v_mfma_f32_16x16x32_bf16 v[8:11], a[212:215], a[144:147], v[8:11]\n" \
"s_lshl_b32 s79, s79, 14\n" \
"v_lshlrev_b32 v200, 16, v200\n" \
"v_and_b32 v252, 0x23, v251\n" \
"v_mfma_f32_16x16x32_bf16 v[40:43], a[212:215], a[148:151], v[40:43]\n" \
"s_add_u32 s79, s79, 1048576\n" \
"ds_read_b64_tr_b16 a[202:203], v241 offset:35840\n" \
"ds_read_b128 a[164:167], v242 offset:2048\n" \
"v_mfma_f32_16x16x32_bf16 v[40:43], a[208:211], a[132:135], v[40:43]\n" \
"s_lshl_b32 s90, %[wid], 15\n" \
"v_cmp_eq_u32 vcc, 0, v252\n" \
"v_add_f32 v200, s93, v200\n" \
"v_mfma_f32_16x16x32_bf16 v[72:75], a[208:211], a[136:139], v[72:75]\n" \
"s_sub_u32 s80, s79, s90\n" \
"ds_read_b64_tr_b16 a[204:205], v241 offset:41984\n" \
"ds_read_b128 a[168:171], v242 offset:4096\n" \
"v_mfma_f32_16x16x32_bf16 v[72:75], a[212:215], a[152:155], v[72:75]\n" \
"s_add_u32 s80, s80, 114688\n" \
"v_cvt_pk_bf16_f32 v200, v200, v200\n" \
"v_bfe_u32 v201, v251, 1, 3\n" \
"v_mfma_f32_16x16x32_bf16 v[104:107], a[212:215], a[156:159], v[104:107]\n" \
"s_add_u32 m0, s70, 25600\n" \
"ds_read_b64_tr_b16 a[206:207], v241 offset:44032\n" \
"ds_read_b128 a[172:175], v242 offset:6144\n" \
"v_mfma_f32_16x16x32_bf16 v[104:107], a[208:211], a[140:143], v[104:107]\n" \
"s_waitcnt lgkmcnt(13)\n" \
"v_and_or_b32 v201, v251, 16, v201\n" \
"v_mov_b32 v252, 1073741824\n" \
"v_mfma_f32_16x16x32_bf16 v[12:15], a[216:219], a[128:131], v[12:15]\n" \
"s_add_u32 s74, s69, 2883712\n" \
"buffer_load_dwordx4 v246, s[20:23], s74 offen lds\n" \
"v_mfma_f32_16x16x32_bf16 v[12:15], a[220:223], a[144:147], v[12:15]\n" \
"s_lshl_b32 s90, s67, 18\n" \
"ds_read_b64_tr_b16 a[208:209], v240 offset:34304\n" \
"v_cndmask_b32 v252, v252, v201, vcc\n" \
"v_mfma_f32_16x16x32_bf16 v[44:47], a[220:223], a[148:151], v[44:47]\n" \
"s_sub_u32 s90, 1966080, s90\n" \
"ds_read_b64_tr_b16 a[210:211], v240 offset:36352\n" \
"v_lshlrev_b32 v152, 10, v152\n" \
"v_mfma_f32_16x16x32_bf16 v[44:47], a[216:219], a[132:135], v[44:47]\n" \
"s_add_u32 s36, s79, s90\n" \
"v_bfe_u32 v255, v251, 3, 2\n" \
"v_lshl_or_b32 v152, v255, 8, v152\n" \
"v_mfma_f32_16x16x32_bf16 v[76:79], a[216:219], a[136:139], v[76:79]\n" \
"s_add_u32 s37, s80, s90\n" \
"ds_read_b64_tr_b16 a[212:213], v240 offset:42496\n" \
"ds_read_b128 a[176:179], v244 offset:0\n" \
"v_mfma_f32_16x16x32_bf16 v[76:79], a[220:223], a[152:155], v[76:79]\n" \
"s_mul_i32 s73, s68, 1179648\n" \
"v_and_b32 v255, 7, v251\n" \
"v_lshl_or_b32 v152, v255, 4, v152\n" \
"v_mfma_f32_16x16x32_bf16 v[108:111], a[220:223], a[156:159], v[108:111]\n" \
"s_add_u32 s73, s73, 3145728\n" \
"ds_read_b64_tr_b16 a[214:215], v240 offset:44544\n" \
"ds_read_b128 a[180:183], v244 offset:2048\n" \
"v_mfma_f32_16x16x32_bf16 v[108:111], a[216:219], a[140:143], v[108:111]\n" \
"s_waitcnt lgkmcnt(14)\n" \
"v_lshlrev_b32 v136, 16, v160\n" \
"v_and_b32 v137, 0xffff0000, v160\n" \
"v_mfma_f32_16x16x32_bf16 v[16:19], a[192:195], a[128:131], v[16:19]\n" \
"ds_read_b64_tr_b16 a[216:217], v241 offset:34304\n" \
"ds_read_b128 a[184:187], v244 offset:4096\n" \
"v_mfma_f32_16x16x32_bf16 v[16:19], a[196:199], a[144:147], v[16:19]\n" \
"v_lshlrev_b32 v138, 16, v161\n" \
"v_and_b32 v139, 0xffff0000, v161\n" \
"v_mfma_f32_16x16x32_bf16 v[48:51], a[196:199], a[148:151], v[48:51]\n" \
"ds_read_b64_tr_b16 a[218:219], v241 offset:36352\n" \
"ds_read_b128 a[188:191], v244 offset:6144\n" \
"v_mfma_f32_16x16x32_bf16 v[48:51], a[192:195], a[132:135], v[48:51]\n" \
"ds_read_b64_tr_b16 a[220:221], v241 offset:42496\n" \
"v_lshlrev_b32 v140, 16, v162\n" \
"v_mfma_f32_16x16x32_bf16 v[80:83], a[192:195], a[136:139], v[80:83]\n" \
"ds_read_b64_tr_b16 a[222:223], v241 offset:44544\n" \
"v_and_b32 v141, 0xffff0000, v162\n" \
"v_mfma_f32_16x16x32_bf16 v[80:83], a[196:199], a[152:155], v[80:83]\n" \
"v_lshlrev_b32 v142, 16, v163\n" \
"v_and_b32 v143, 0xffff0000, v163\n" \
"v_mfma_f32_16x16x32_bf16 v[112:115], a[196:199], a[156:159], v[112:115]\n" \
"v_lshlrev_b32 v144, 16, v164\n" \
"v_and_b32 v145, 0xffff0000, v164\n" \
"v_mfma_f32_16x16x32_bf16 v[112:115], a[192:195], a[140:143], v[112:115]\n" \
"s_waitcnt lgkmcnt(13)\n" \
"v_lshlrev_b32 v146, 16, v165\n" \
"v_and_b32 v147, 0xffff0000, v165\n" \
"v_mfma_f32_16x16x32_bf16 v[20:23], a[200:203], a[128:131], v[20:23]\n" \
"v_lshlrev_b32 v148, 16, v166\n" \
"v_and_b32 v149, 0xffff0000, v166\n" \
"v_mfma_f32_16x16x32_bf16 v[20:23], a[204:207], a[144:147], v[20:23]\n" \
"v_lshlrev_b32 v150, 16, v167\n" \
"v_and_b32 v151, 0xffff0000, v167\n" \
"v_mfma_f32_16x16x32_bf16 v[52:55], a[204:207], a[148:151], v[52:55]\n" \
"v_mfma_f32_16x16x32_bf16 v[52:55], a[200:203], a[132:135], v[52:55]\n" \
"v_mfma_f32_16x16x32_bf16 v[84:87], a[200:203], a[136:139], v[84:87]\n" \
"s_waitcnt lgkmcnt(0)\n" \
"s_barrier\n" \
"s_add_u32 m0, s70, 32768\n" \
"v_mfma_f32_16x16x32_bf16 v[84:87], a[204:207], a[152:155], v[84:87]\n" \
"s_add_u32 s74, s69, 3145728\n" \
"ds_read_b128 v[184:187], v237 offset:2048\n" \
"ds_read_b128 v[188:191], v237 offset:2064\n" \
"v_mfma_f32_16x16x32_bf16 v[116:119], a[204:207], a[156:159], v[116:119]\n" \
"s_lshr_b32 s85, %[bid], 2\n" \
"buffer_load_dwordx4 v246, s[20:23], s74 offen lds\n" \
"v_mfma_f32_16x16x32_bf16 v[116:119], a[200:203], a[140:143], v[116:119]\n" \
"s_add_u32 m0, s72, 0\n" \
"ds_read_b128 v[168:171], v237 offset:0\n" \
"ds_read_b128 v[172:175], v237 offset:16\n" \
"v_mfma_f32_16x16x32_bf16 v[24:27], a[208:211], a[128:131], v[24:27]\n" \
"s_add_u32 s74, s71, 768\n" \
"ds_read_b128 v[192:195], v237 offset:3072\n" \
"ds_read_b128 v[196:199], v237 offset:3088\n" \
"v_mfma_f32_16x16x32_bf16 v[24:27], a[212:215], a[144:147], v[24:27]\n" \
"s_lshl_b32 s85, s85, 6\n" \
"buffer_load_dwordx4 v249, s[16:19], s74 offen lds\n" \
"v_mfma_f32_16x16x32_bf16 v[56:59], a[212:215], a[148:151], v[56:59]\n" \
"s_and_b32 s86, %[bid], 2\n" \
"ds_read_b128 v[176:179], v237 offset:1024\n" \
"ds_read_b128 v[180:183], v237 offset:1040\n" \
"v_mfma_f32_16x16x32_bf16 v[56:59], a[208:211], a[132:135], v[56:59]\n" \
"s_lshl2_add_u32 s85, s86, s85\n" \
"ds_read_b64_tr_b16 a[192:193], v240 offset:49152\n" \
"ds_read_b64_tr_b16 a[194:195], v240 offset:51200\n" \
"v_mfma_f32_16x16x32_bf16 v[88:91], a[208:211], a[136:139], v[88:91]\n" \
"s_add_u32 m0, s72, 1024\n" \
"ds_read_b64_tr_b16 a[196:197], v240 offset:57344\n" \
"ds_read_b64_tr_b16 a[198:199], v240 offset:59392\n" \
"v_mfma_f32_16x16x32_bf16 v[88:91], a[212:215], a[152:155], v[88:91]\n" \
"s_add_u32 s74, s71, 8960\n" \
"ds_read_b64_tr_b16 a[200:201], v241 offset:49152\n" \
"ds_read_b64_tr_b16 a[202:203], v241 offset:51200\n" \
"v_mfma_f32_16x16x32_bf16 v[120:123], a[212:215], a[156:159], v[120:123]\n" \
"s_and_b32 s86, %[bid], 1\n" \
"buffer_load_dwordx4 v250, s[16:19], s74 offen lds\n" \
"v_mfma_f32_16x16x32_bf16 v[120:123], a[208:211], a[140:143], v[120:123]\n" \
"s_lshl_b32 s86, s86, 5\n" \
"ds_read_b64_tr_b16 a[204:205], v241 offset:57344\n" \
"ds_read_b64_tr_b16 a[206:207], v241 offset:59392\n" \
"v_mfma_f32_16x16x32_bf16 v[28:31], a[216:219], a[128:131], v[28:31]\n" \
"s_add_u32 s85, s85, s86\n" \
"s_cmp_eq_u32 %[wid], 0\n" \
"s_cselect_b32 s85, s85, 1073741824\n" \
"v_mfma_f32_16x16x32_bf16 v[28:31], a[220:223], a[144:147], v[28:31]\n" \
"s_mov_b32 s84, 1040356334\n" \
"buffer_store_short v200, v252, s[32:35], s85 offen sc1\n" \
"v_mfma_f32_16x16x32_bf16 v[60:63], a[220:223], a[148:151], v[60:63]\n" \
"s_add_u32 m0, s70, 33792\n" \
"ds_read_b64_tr_b16 a[208:209], v240 offset:49664\n" \
"ds_read_b64_tr_b16 a[210:211], v240 offset:51712\n" \
"v_mfma_f32_16x16x32_bf16 v[60:63], a[216:219], a[132:135], v[60:63]\n" \
"s_waitcnt lgkmcnt(14)\n" \
"s_add_u32 s74, s69, 3145856\n" \
"v_mul_f32 v136, v136, v184\n" \
"v_mfma_f32_16x16x32_bf16 v[92:95], a[216:219], a[136:139], v[92:95]\n" \
"buffer_load_dwordx4 v246, s[20:23], s74 offen lds\n" \
"v_mfma_f32_16x16x32_bf16 v[92:95], a[220:223], a[152:155], v[92:95]\n" \
"s_add_u32 m0, s72, 2048\n" \
"ds_read_b64_tr_b16 a[212:213], v240 offset:57856\n" \
"v_mul_f32 v138, v138, v186\n" \
"v_mfma_f32_16x16x32_bf16 v[124:127], a[220:223], a[156:159], v[124:127]\n" \
"s_add_u32 s74, s71, 17152\n" \
"ds_read_b64_tr_b16 a[214:215], v240 offset:59904\n" \
"v_mul_f32 v137, v137, v185\n" \
"v_mfma_f32_16x16x32_bf16 v[124:127], a[216:219], a[140:143], v[124:127]\n" \
"s_waitcnt lgkmcnt(8)\n" \
"buffer_load_dwordx4 v249, s[16:19], s74 offen lds\n" \
"v_mfma_f32_16x16x32_bf16 a[0:3], a[128:131], a[192:195], a[0:3]\n" \
"s_add_u32 m0, s72, 3072\n" \
"ds_read_b64_tr_b16 a[216:217], v241 offset:49664\n" \
"v_mul_f32 v139, v139, v187\n" \
"v_mfma_f32_16x16x32_bf16 a[0:3], a[144:147], a[196:199], a[0:3]\n" \
"s_add_u32 s74, s71, 25344\n" \
"v_cvt_pk_bf16_f32 v160, v136, v137\n" \
"v_cvt_pk_bf16_f32 v161, v138, v139\n" \
"v_mfma_f32_16x16x32_bf16 a[4:7], a[148:151], a[196:199], a[4:7]\n" \
"buffer_load_dwordx4 v250, s[16:19], s74 offen lds\n" \
"v_mfma_f32_16x16x32_bf16 a[4:7], a[132:135], a[192:195], a[4:7]\n" \
"ds_read_b64_tr_b16 a[218:219], v241 offset:51712\n" \
"v_lshlrev_b32 v136, 16, v160\n" \
"v_mfma_f32_16x16x32_bf16 a[64:67], a[136:139], a[192:195], a[64:67]\n" \
"ds_read_b64_tr_b16 a[220:221], v241 offset:57856\n" \
"v_lshlrev_b32 v138, 16, v161\n" \
"v_mfma_f32_16x16x32_bf16 a[64:67], a[152:155], a[196:199], a[64:67]\n" \
"v_and_b32 v137, 0xffff0000, v160\n" \
"v_and_b32 v139, 0xffff0000, v161\n" \
"v_mfma_f32_16x16x32_bf16 a[68:71], a[156:159], a[196:199], a[68:71]\n" \
"s_add_u32 m0, s72, 4096\n" \
"ds_read_b64_tr_b16 a[222:223], v241 offset:59904\n" \
"v_add_f32 v136, v136, v168\n" \
"v_mfma_f32_16x16x32_bf16 a[68:71], a[140:143], a[192:195], a[68:71]\n" \
"s_waitcnt lgkmcnt(8)\n" \
"v_add_f32 v138, v138, v170\n" \
"v_add_f32 v137, v137, v169\n" \
"v_mfma_f32_16x16x32_bf16 a[8:11], a[128:131], a[200:203], a[8:11]\n" \
"s_add_u32 s74, s94, 33536\n" \
"buffer_load_dwordx4 v249, s[16:19], s74 offen lds\n" \
"v_mfma_f32_16x16x32_bf16 a[8:11], a[144:147], a[204:207], a[8:11]\n" \
"s_add_u32 m0, s70, 40960\n" \
"ds_read_b64_tr_b16 a[192:193], v240 offset:50176\n" \
"v_add_f32 v139, v139, v171\n" \
"v_mfma_f32_16x16x32_bf16 a[12:15], a[148:151], a[204:207], a[12:15]\n" \
"s_add_u32 s74, s69, 3407872\n" \
"ds_read_b64_tr_b16 a[194:195], v240 offset:52224\n" \
"v_cvt_pk_bf16_f32 v160, v136, v137\n" \
"v_mfma_f32_16x16x32_bf16 a[12:15], a[132:135], a[200:203], a[12:15]\n" \
"buffer_load_dwordx4 v246, s[20:23], s74 offen lds\n" \
"v_mfma_f32_16x16x32_bf16 a[72:75], a[136:139], a[200:203], a[72:75]\n" \
"s_add_u32 m0, s72, 5120\n" \
"ds_read_b64_tr_b16 a[196:197], v240 offset:58368\n" \
"v_cvt_pk_bf16_f32 v161, v138, v139\n" \
"v_mfma_f32_16x16x32_bf16 a[72:75], a[152:155], a[204:207], a[72:75]\n" \
"s_add_u32 s74, s94, 41728\n" \
"v_mul_f32 v140, v140, v188\n" \
"v_mul_f32 v142, v142, v190\n" \
"v_mfma_f32_16x16x32_bf16 a[76:79], a[156:159], a[204:207], a[76:79]\n" \
"s_lshr_b32 s85, s78, 4\n" \
"buffer_load_dwordx4 v250, s[16:19], s74 offen lds\n" \
"v_mfma_f32_16x16x32_bf16 a[76:79], a[140:143], a[200:203], a[76:79]\n" \
"s_waitcnt lgkmcnt(7)\n" \
"ds_read_b64_tr_b16 a[198:199], v240 offset:60416\n" \
"v_mul_f32 v141, v141, v189\n" \
"v_mfma_f32_16x16x32_bf16 a[16:19], a[128:131], a[208:211], a[16:19]\n" \
"s_lshl_b32 s85, s85, 14\n" \
"ds_read_b64_tr_b16 a[200:201], v241 offset:50176\n" \
"v_mul_f32 v143, v143, v191\n" \
"v_mfma_f32_16x16x32_bf16 a[16:19], a[144:147], a[212:215], a[16:19]\n" \
"s_and_b32 s86, s78, 8\n" \
"v_cvt_pk_bf16_f32 v162, v140, v141\n" \
"v_cvt_pk_bf16_f32 v163, v142, v143\n" \
"v_mfma_f32_16x16x32_bf16 a[20:23], a[148:151], a[212:215], a[20:23]\n" \
".Lst0_2_%=:\n" \
"s_cbranch_execz .Lst0_6_%=\n" \
"s_add_u32 m0, s72, 6144\n" \
"ds_read_b64_tr_b16 a[202:203], v241 offset:52224\n" \
"v_lshlrev_b32 v140, 16, v162\n" \
"v_mfma_f32_16x16x32_bf16 a[20:23], a[132:135], a[208:211], a[20:23]\n" \
"s_add_u32 s74, s94, 49920\n" \
"v_lshlrev_b32 v142, 16, v163\n" \
"v_and_b32 v141, 0xffff0000, v162\n" \
"v_mfma_f32_16x16x32_bf16 a[80:83], a[136:139], a[208:211], a[80:83]\n" \
"s_lshl4_add_u32 s85, s86, s85\n" \
"buffer_load_dwordx4 v249, s[16:19], s74 offen lds\n" \
"v_mfma_f32_16x16x32_bf16 a[80:83], a[152:155], a[212:215], a[80:83]\n" \
"s_lshl_b32 s86, %[wid], 11\n" \
"ds_read_b64_tr_b16 a[204:205], v241 offset:58368\n" \
"v_and_b32 v143, 0xffff0000, v163\n" \
"v_mfma_f32_16x16x32_bf16 a[84:87], a[156:159], a[212:215], a[84:87]\n" \
"s_add_u32 m0, s70, 41984\n" \
"ds_read_b64_tr_b16 a[206:207], v241 offset:60416\n" \
"v_add_f32 v140, v140, v172\n" \
"v_mfma_f32_16x16x32_bf16 a[84:87], a[140:143], a[208:211], a[84:87]\n" \
"s_waitcnt lgkmcnt(8)\n" \
"v_add_f32 v142, v142, v174\n" \
"v_add_f32 v141, v141, v173\n" \
"v_mfma_f32_16x16x32_bf16 a[24:27], a[128:131], a[216:219], a[24:27]\n" \
"s_add_u32 s74, s69, 3408000\n" \
"buffer_load_dwordx4 v246, s[20:23], s74 offen lds\n" \
"v_mfma_f32_16x16x32_bf16 a[24:27], a[144:147], a[220:223], a[24:27]\n" \
"s_add_u32 m0, s72, 7168\n" \
"ds_read_b64_tr_b16 a[208:209], v240 offset:50688\n" \
"v_add_f32 v143, v143, v175\n" \
"v_mfma_f32_16x16x32_bf16 a[28:31], a[148:151], a[220:223], a[28:31]\n" \
"s_add_u32 s74, s94, 58112\n" \
"ds_read_b64_tr_b16 a[210:211], v240 offset:52736\n" \
"v_cvt_pk_bf16_f32 v162, v140, v141\n" \
"v_mfma_f32_16x16x32_bf16 a[28:31], a[132:135], a[216:219], a[28:31]\n" \
"s_add_u32 s85, s85, s86\n" \
"buffer_load_dwordx4 v250, s[16:19], s74 offen lds\n" \
"v_mfma_f32_16x16x32_bf16 a[88:91], a[136:139], a[216:219], a[88:91]\n" \
"s_add_u32 s85, s85, 1048576\n" \
"ds_read_b64_tr_b16 a[212:213], v240 offset:58880\n" \
"v_cvt_pk_bf16_f32 v163, v142, v143\n" \
"v_mfma_f32_16x16x32_bf16 a[88:91], a[152:155], a[220:223], a[88:91]\n" \
"v_mul_f32 v144, v144, v192\n" \
"v_mul_f32 v146, v146, v194\n" \
"v_mfma_f32_16x16x32_bf16 a[92:95], a[156:159], a[220:223], a[92:95]\n" \
"buffer_store_dwordx4 v[160:163], v152, s[32:35], s85 offen sc1\n" \
"v_mfma_f32_16x16x32_bf16 a[92:95], a[140:143], a[216:219], a[92:95]\n" \
"s_waitcnt lgkmcnt(7)\n" \
"ds_read_b64_tr_b16 a[214:215], v240 offset:60928\n" \
"v_mul_f32 v145, v145, v193\n" \
"v_mfma_f32_16x16x32_bf16 a[32:35], a[128:131], a[192:195], a[32:35]\n" \
"s_add_u32 s85, s85, 8192\n" \
"ds_read_b64_tr_b16 a[216:217], v241 offset:50688\n" \
"v_mul_f32 v147, v147, v195\n" \
"v_mfma_f32_16x16x32_bf16 a[32:35], a[144:147], a[196:199], a[32:35]\n" \
"s_waitcnt vmcnt(19)\n" \
"ds_read_b128 a[224:227], v242 offset:8192\n" \
"v_cvt_pk_bf16_f32 v164, v144, v145\n" \
"v_mfma_f32_16x16x32_bf16 a[36:39], a[148:151], a[196:199], a[36:39]\n" \
"ds_read_b64_tr_b16 a[218:219], v241 offset:52736\n" \
"ds_read_b128 a[228:231], v242 offset:10240\n" \
"v_mfma_f32_16x16x32_bf16 a[36:39], a[132:135], a[192:195], a[36:39]\n" \
"ds_read_b64_tr_b16 a[220:221], v241 offset:58880\n" \
"v_cvt_pk_bf16_f32 v165, v146, v147\n" \
"v_mfma_f32_16x16x32_bf16 a[96:99], a[136:139], a[192:195], a[96:99]\n" \
"ds_read_b64_tr_b16 a[222:223], v241 offset:60928\n" \
"ds_read_b128 a[232:235], v242 offset:12288\n" \
"v_mfma_f32_16x16x32_bf16 a[96:99], a[152:155], a[196:199], a[96:99]\n" \
"v_lshlrev_b32 v144, 16, v164\n" \
"v_lshlrev_b32 v146, 16, v165\n" \
"v_mfma_f32_16x16x32_bf16 a[100:103], a[156:159], a[196:199], a[100:103]\n" \
"ds_read_b128 a[236:239], v242 offset:14336\n" \
"v_and_b32 v145, 0xffff0000, v164\n" \
"v_mfma_f32_16x16x32_bf16 a[100:103], a[140:143], a[192:195], a[100:103]\n" \
"s_waitcnt lgkmcnt(12)\n" \
"v_and_b32 v147, 0xffff0000, v165\n" \
"v_add_f32 v144, v144, v176\n" \
"v_mfma_f32_16x16x32_bf16 a[40:43], a[128:131], a[200:203], a[40:43]\n" \
"v_add_f32 v146, v146, v178\n" \
"v_add_f32 v145, v145, v177\n" \
"v_mfma_f32_16x16x32_bf16 a[40:43], a[144:147], a[204:207], a[40:43]\n" \
"v_add_f32 v147, v147, v179\n" \
"v_cvt_pk_bf16_f32 v164, v144, v145\n" \
"v_mfma_f32_16x16x32_bf16 a[44:47], a[148:151], a[204:207], a[44:47]\n" \
"v_cvt_pk_bf16_f32 v165, v146, v147\n" \
"v_mul_f32 v148, v148, v196\n" \
"v_mfma_f32_16x16x32_bf16 a[44:47], a[132:135], a[200:203], a[44:47]\n" \
"v_mul_f32 v150, v150, v198\n" \
"v_mul_f32 v149, v149, v197\n" \
"v_mfma_f32_16x16x32_bf16 a[104:107], a[136:139], a[200:203], a[104:107]\n" \
"s_waitcnt lgkmcnt(2)\n" \
"s_barrier\n" \
"ds_read_b128 a[240:243], v244 offset:8192\n" \
"v_mfma_f32_16x16x32_bf16 a[104:107], a[152:155], a[204:207], a[104:107]\n" \
"s_add_u32 m0, s70, 49152\n" \
"ds_read_b64_tr_b16 a[192:193], v240 offset:0\n" \
"ds_read_b64_tr_b16 a[194:195], v240 offset:2048\n" \
"v_mfma_f32_16x16x32_bf16 a[108:111], a[156:159], a[204:207], a[108:111]\n" \
"s_add_u32 s74, s69, 3670016\n" \
"buffer_load_dwordx4 v246, s[20:23], s74 offen lds\n" \
"v_mfma_f32_16x16x32_bf16 a[108:111], a[140:143], a[200:203], a[108:111]\n" \
"ds_read_b64_tr_b16 a[196:197], v240 offset:8192\n" \
"ds_read_b64_tr_b16 a[198:199], v240 offset:10240\n" \
"v_mfma_f32_16x16x32_bf16 a[48:51], a[128:131], a[208:211], a[48:51]\n" \
"ds_read_b64_tr_b16 a[200:201], v241 offset:0\n" \
"ds_read_b128 a[244:247], v244 offset:10240\n" \
"v_mfma_f32_16x16x32_bf16 a[48:51], a[144:147], a[212:215], a[48:51]\n" \
"ds_read_b128 a[248:251], v244 offset:12288\n" \
"v_mul_f32 v151, v151, v199\n" \
"v_mfma_f32_16x16x32_bf16 a[52:55], a[148:151], a[212:215], a[52:55]\n" \
"ds_read_b64_tr_b16 a[202:203], v241 offset:2048\n" \
"ds_read_b128 a[252:255], v244 offset:14336\n" \
"v_mfma_f32_16x16x32_bf16 a[52:55], a[132:135], a[208:211], a[52:55]\n" \
"v_cvt_pk_bf16_f32 v166, v148, v149\n" \
"v_cvt_pk_bf16_f32 v167, v150, v151\n" \
"v_mfma_f32_16x16x32_bf16 a[112:115], a[136:139], a[208:211], a[112:115]\n" \
"ds_read_b64_tr_b16 a[204:205], v241 offset:8192\n" \
"v_lshlrev_b32 v148, 16, v166\n" \
"v_mfma_f32_16x16x32_bf16 a[112:115], a[152:155], a[212:215], a[112:115]\n" \
"v_lshlrev_b32 v150, 16, v167\n" \
"v_and_b32 v149, 0xffff0000, v166\n" \
"v_mfma_f32_16x16x32_bf16 a[116:119], a[156:159], a[212:215], a[116:119]\n" \
"ds_read_b64_tr_b16 a[206:207], v241 offset:10240\n" \
"v_and_b32 v151, 0xffff0000, v167\n" \
"v_mfma_f32_16x16x32_bf16 a[116:119], a[140:143], a[208:211], a[116:119]\n" \
"v_add_f32 v148, v148, v180\n" \
"v_add_f32 v150, v150, v182\n" \
"v_mfma_f32_16x16x32_bf16 a[56:59], a[128:131], a[216:219], a[56:59]\n" \
"ds_read_b64_tr_b16 a[208:209], v240 offset:512\n" \
"v_add_f32 v149, v149, v181\n" \
"v_mfma_f32_16x16x32_bf16 a[56:59], a[144:147], a[220:223], a[56:59]\n" \
"v_add_f32 v151, v151, v183\n" \
"v_cvt_pk_bf16_f32 v166, v148, v149\n" \
"v_mfma_f32_16x16x32_bf16 a[60:63], a[148:151], a[220:223], a[60:63]\n" \
"s_add_u32 m0, s70, 50176\n" \
"ds_read_b64_tr_b16 a[210:211], v240 offset:2560\n" \
"v_cvt_pk_bf16_f32 v167, v150, v151\n" \
"v_mfma_f32_16x16x32_bf16 a[60:63], a[132:135], a[216:219], a[60:63]\n" \
"s_add_u32 s74, s69, 3670144\n" \
"buffer_store_dwordx4 v[164:167], v152, s[32:35], s85 offen sc1\n" \
"v_mfma_f32_16x16x32_bf16 a[120:123], a[136:139], a[216:219], a[120:123]\n" \
"ds_read_b64_tr_b16 a[212:213], v240 offset:8704\n" \
"v_mfma_f32_16x16x32_bf16 a[120:123], a[152:155], a[220:223], a[120:123]\n" \
"v_mfma_f32_16x16x32_bf16 a[124:127], a[156:159], a[220:223], a[124:127]\n" \
"buffer_load_dwordx4 v246, s[20:23], s74 offen lds\n" \
"v_mfma_f32_16x16x32_bf16 a[124:127], a[140:143], a[216:219], a[124:127]\n" \
"s_waitcnt lgkmcnt(10)\n" \
"s_add_u32 m0, s72, 8192\n" \
"ds_read_b64_tr_b16 a[214:215], v240 offset:10752\n" \
  \
"v_mfma_f32_16x16x32_bf16 v[0:3], a[192:195], a[160:163], v[0:3]\n" \
"s_add_u32 s74, s71, 896\n" \
"ds_read_b64_tr_b16 a[216:217], v241 offset:512\n" \
"v_mfma_f32_16x16x32_bf16 v[0:3], a[196:199], a[176:179], v[0:3]\n" \
"s_waitcnt lgkmcnt(7)\n" \
"buffer_load_dwordx4 v249, s[16:19], s74 offen lds\n" \
"v_mfma_f32_16x16x32_bf16 v[32:35], a[196:199], a[180:183], v[32:35]\n" \
"ds_read_b64_tr_b16 a[218:219], v241 offset:2560\n" \
"v_mfma_f32_16x16x32_bf16 v[32:35], a[192:195], a[164:167], v[32:35]\n" \
"v_mfma_f32_16x16x32_bf16 v[64:67], a[192:195], a[168:171], v[64:67]\n" \
"s_add_u32 m0, s72, 9216\n" \
"ds_read_b64_tr_b16 a[220:221], v241 offset:8704\n" \
"v_mfma_f32_16x16x32_bf16 v[64:67], a[196:199], a[184:187], v[64:67]\n" \
"s_add_u32 s74, s71, 9088\n" \
"v_mfma_f32_16x16x32_bf16 v[96:99], a[196:199], a[188:191], v[96:99]\n" \
"buffer_load_dwordx4 v250, s[16:19], s74 offen lds\n" \
"v_mfma_f32_16x16x32_bf16 v[96:99], a[192:195], a[172:175], v[96:99]\n" \
"s_waitcnt lgkmcnt(7)\n" \
"ds_read_b64_tr_b16 a[222:223], v241 offset:10752\n" \
"v_mfma_f32_16x16x32_bf16 v[4:7], a[200:203], a[160:163], v[4:7]\n" \
"s_add_u32 m0, s70, 57344\n" \
"ds_read_b64_tr_b16 a[192:193], v240 offset:1024\n" \
"v_mfma_f32_16x16x32_bf16 v[4:7], a[204:207], a[176:179], v[4:7]\n" \
"s_add_u32 s74, s69, 3932160\n" \
"v_mfma_f32_16x16x32_bf16 v[36:39], a[204:207], a[180:183], v[36:39]\n" \
"buffer_load_dwordx4 v246, s[20:23], s74 offen lds\n" \
"v_mfma_f32_16x16x32_bf16 v[36:39], a[200:203], a[164:167], v[36:39]\n" \
"s_add_u32 m0, s72, 10240\n" \
"s_add_u32 s74, s71, 17280\n" \
"ds_read_b64_tr_b16 a[194:195], v240 offset:3072\n" \
"v_mfma_f32_16x16x32_bf16 v[68:71], a[200:203], a[168:171], v[68:71]\n" \
"ds_read_b64_tr_b16 a[196:197], v240 offset:9216\n" \
"v_mfma_f32_16x16x32_bf16 v[68:71], a[204:207], a[184:187], v[68:71]\n" \
"buffer_load_dwordx4 v249, s[16:19], s74 offen lds\n" \
"v_mfma_f32_16x16x32_bf16 v[100:103], a[204:207], a[188:191], v[100:103]\n" \
"ds_read_b64_tr_b16 a[198:199], v240 offset:11264\n" \
"v_mfma_f32_16x16x32_bf16 v[100:103], a[200:203], a[172:175], v[100:103]\n" \
"s_waitcnt lgkmcnt(8)\n" \
"v_mfma_f32_16x16x32_bf16 v[8:11], a[208:211], a[160:163], v[8:11]\n" \
"s_add_u32 m0, s72, 11264\n" \
"ds_read_b64_tr_b16 a[200:201], v241 offset:1024\n" \
"v_mfma_f32_16x16x32_bf16 v[8:11], a[212:215], a[176:179], v[8:11]\n" \
"s_add_u32 s74, s71, 25472\n" \
"v_mfma_f32_16x16x32_bf16 v[40:43], a[212:215], a[180:183], v[40:43]\n" \
"buffer_load_dwordx4 v250, s[16:19], s74 offen lds\n" \
"v_mfma_f32_16x16x32_bf16 v[40:43], a[208:211], a[164:167], v[40:43]\n" \
"ds_read_b64_tr_b16 a[202:203], v241 offset:3072\n" \
"v_mfma_f32_16x16x32_bf16 v[72:75], a[208:211], a[168:171], v[72:75]\n" \
"ds_read_b64_tr_b16 a[204:205], v241 offset:9216\n" \
"v_mfma_f32_16x16x32_bf16 v[72:75], a[212:215], a[184:187], v[72:75]\n" \
"v_mfma_f32_16x16x32_bf16 v[104:107], a[212:215], a[188:191], v[104:107]\n" \
"s_add_u32 m0, s70, 58368\n" \
"ds_read_b64_tr_b16 a[206:207], v241 offset:11264\n" \
"v_mfma_f32_16x16x32_bf16 v[104:107], a[208:211], a[172:175], v[104:107]\n" \
"s_waitcnt lgkmcnt(8)\n" \
"s_add_u32 s74, s69, 3932288\n" \
"v_mfma_f32_16x16x32_bf16 v[12:15], a[216:219], a[160:163], v[12:15]\n" \
"buffer_load_dwordx4 v246, s[20:23], s74 offen lds\n" \
"v_mfma_f32_16x16x32_bf16 v[12:15], a[220:223], a[176:179], v[12:15]\n" \
"s_add_u32 m0, s72, 12288\n" \
"s_add_u32 s74, s94, 33664\n" \
"ds_read_b64_tr_b16 a[208:209], v240 offset:1536\n" \
"v_mfma_f32_16x16x32_bf16 v[44:47], a[220:223], a[180:183], v[44:47]\n" \
"ds_read_b64_tr_b16 a[210:211], v240 offset:3584\n" \
"v_mfma_f32_16x16x32_bf16 v[44:47], a[216:219], a[164:167], v[44:47]\n" \
"buffer_load_dwordx4 v249, s[16:19], s74 offen lds\n" \
"v_mfma_f32_16x16x32_bf16 v[76:79], a[216:219], a[168:171], v[76:79]\n" \
"s_add_u32 m0, s72, 13312\n" \
"ds_read_b64_tr_b16 a[212:213], v240 offset:9728\n" \
"v_mfma_f32_16x16x32_bf16 v[76:79], a[220:223], a[184:187], v[76:79]\n" \
"s_add_u32 s74, s94, 41856\n" \
"v_mfma_f32_16x16x32_bf16 v[108:111], a[220:223], a[188:191], v[108:111]\n" \
"buffer_load_dwordx4 v250, s[16:19], s74 offen lds\n" \
"v_mfma_f32_16x16x32_bf16 v[108:111], a[216:219], a[172:175], v[108:111]\n" \
"s_waitcnt lgkmcnt(7)\n" \
"ds_read_b64_tr_b16 a[214:215], v240 offset:11776\n" \
"v_mfma_f32_16x16x32_bf16 v[16:19], a[192:195], a[160:163], v[16:19]\n" \
"ds_read_b64_tr_b16 a[216:217], v241 offset:1536\n" \
"v_mfma_f32_16x16x32_bf16 v[16:19], a[196:199], a[176:179], v[16:19]\n" \
"v_mfma_f32_16x16x32_bf16 v[48:51], a[196:199], a[180:183], v[48:51]\n" \
"s_add_u32 m0, s72, 14336\n" \
"ds_read_b64_tr_b16 a[218:219], v241 offset:3584\n" \
"v_mfma_f32_16x16x32_bf16 v[48:51], a[192:195], a[164:167], v[48:51]\n" \
"s_add_u32 s74, s94, 50048\n" \
"ds_read_b64_tr_b16 a[220:221], v241 offset:9728\n" \
"v_mfma_f32_16x16x32_bf16 v[80:83], a[192:195], a[168:171], v[80:83]\n" \
"buffer_load_dwordx4 v249, s[16:19], s74 offen lds\n" \
"v_mfma_f32_16x16x32_bf16 v[80:83], a[196:199], a[184:187], v[80:83]\n" \
"ds_read_b64_tr_b16 a[222:223], v241 offset:11776\n" \
"v_mfma_f32_16x16x32_bf16 v[112:115], a[196:199], a[188:191], v[112:115]\n" \
"v_mfma_f32_16x16x32_bf16 v[112:115], a[192:195], a[172:175], v[112:115]\n" \
"s_waitcnt lgkmcnt(8)\n" \
"v_mfma_f32_16x16x32_bf16 v[20:23], a[200:203], a[160:163], v[20:23]\n" \
"s_add_u32 m0, s72, 15360\n" \
"v_mfma_f32_16x16x32_bf16 v[20:23], a[204:207], a[176:179], v[20:23]\n" \
"s_add_u32 s74, s94, 58240\n" \
"v_mfma_f32_16x16x32_bf16 v[52:55], a[204:207], a[180:183], v[52:55]\n" \
"buffer_load_dwordx4 v250, s[16:19], s74 offen lds\n" \
"v_mfma_f32_16x16x32_bf16 v[52:55], a[200:203], a[164:167], v[52:55]\n" \
"v_mfma_f32_16x16x32_bf16 v[84:87], a[200:203], a[168:171], v[84:87]\n" \
"s_waitcnt vmcnt(27) lgkmcnt(0)\n" \
"s_barrier\n" \
"s_add_u32 m0, s70, 0\n" \
"v_mfma_f32_16x16x32_bf16 v[84:87], a[204:207], a[184:187], v[84:87]\n" \
"s_add_u32 s74, s69, 3670016\n" \
"ds_read_b64_tr_b16 a[192:193], v240 offset:16384\n" \
"ds_read_b64_tr_b16 a[194:195], v240 offset:18432\n" \
"v_mfma_f32_16x16x32_bf16 v[116:119], a[204:207], a[188:191], v[116:119]\n" \
"buffer_load_dwordx4 v246, s[24:27], s74 offen lds\n" \
"v_mfma_f32_16x16x32_bf16 v[116:119], a[200:203], a[172:175], v[116:119]\n" \
"ds_read_b64_tr_b16 a[196:197], v240 offset:24576\n" \
"ds_read_b64_tr_b16 a[198:199], v240 offset:26624\n" \
"v_mfma_f32_16x16x32_bf16 v[24:27], a[208:211], a[160:163], v[24:27]\n" \
"s_waitcnt vmcnt(15)\n" \
"ds_read_b64_tr_b16 a[200:201], v241 offset:16384\n" \
"ds_read_b128 a[128:131], v242 offset:0\n" \
"v_mfma_f32_16x16x32_bf16 v[24:27], a[212:215], a[176:179], v[24:27]\n" \
"v_mfma_f32_16x16x32_bf16 v[56:59], a[212:215], a[180:183], v[56:59]\n" \
"ds_read_b64_tr_b16 a[202:203], v241 offset:18432\n" \
"ds_read_b128 a[132:135], v242 offset:2048\n" \
"v_mfma_f32_16x16x32_bf16 v[56:59], a[208:211], a[164:167], v[56:59]\n" \
"v_mfma_f32_16x16x32_bf16 v[88:91], a[208:211], a[168:171], v[88:91]\n" \
"ds_read_b64_tr_b16 a[204:205], v241 offset:24576\n" \
"ds_read_b128 a[136:139], v242 offset:4096\n" \
"v_mfma_f32_16x16x32_bf16 v[88:91], a[212:215], a[184:187], v[88:91]\n" \
"v_mfma_f32_16x16x32_bf16 v[120:123], a[212:215], a[188:191], v[120:123]\n" \
"ds_read_b64_tr_b16 a[206:207], v241 offset:26624\n" \
"ds_read_b128 a[140:143], v242 offset:6144\n" \
"v_mfma_f32_16x16x32_bf16 v[120:123], a[208:211], a[172:175], v[120:123]\n" \
"v_mfma_f32_16x16x32_bf16 v[28:31], a[216:219], a[160:163], v[28:31]\n" \
"ds_read_b64_tr_b16 a[208:209], v240 offset:16896\n" \
"v_mfma_f32_16x16x32_bf16 v[28:31], a[220:223], a[176:179], v[28:31]\n" \
"v_mfma_f32_16x16x32_bf16 v[60:63], a[220:223], a[180:183], v[60:63]\n" \
"s_add_u32 m0, s70, 1024\n" \
"ds_read_b64_tr_b16 a[210:211], v240 offset:18944\n" \
"v_mfma_f32_16x16x32_bf16 v[60:63], a[216:219], a[164:167], v[60:63]\n" \
"s_add_u32 s74, s69, 3670144\n" \
"v_mfma_f32_16x16x32_bf16 v[92:95], a[216:219], a[168:171], v[92:95]\n" \
"buffer_load_dwordx4 v246, s[24:27], s74 offen lds\n" \
"v_mfma_f32_16x16x32_bf16 v[92:95], a[220:223], a[184:187], v[92:95]\n" \
"ds_read_b64_tr_b16 a[212:213], v240 offset:25088\n" \
"ds_read_b128 a[144:147], v244 offset:0\n" \
"v_mfma_f32_16x16x32_bf16 v[124:127], a[220:223], a[188:191], v[124:127]\n" \
"ds_read_b64_tr_b16 a[214:215], v240 offset:27136\n" \
"ds_read_b128 a[148:151], v244 offset:2048\n" \
"v_mfma_f32_16x16x32_bf16 v[124:127], a[216:219], a[172:175], v[124:127]\n" \
"s_waitcnt lgkmcnt(14)\n" \
"v_mfma_f32_16x16x32_bf16 v[0:3], a[192:195], a[224:227], v[0:3]\n" \
"ds_read_b64_tr_b16 a[216:217], v241 offset:16896\n" \
"ds_read_b128 a[152:155], v244 offset:4096\n" \
"v_mfma_f32_16x16x32_bf16 v[0:3], a[196:199], a[240:243], v[0:3]\n" \
"v_mfma_f32_16x16x32_bf16 v[32:35], a[196:199], a[244:247], v[32:35]\n" \
"ds_read_b64_tr_b16 a[218:219], v241 offset:18944\n" \
"ds_read_b128 a[156:159], v244 offset:6144\n" \
"v_mfma_f32_16x16x32_bf16 v[32:35], a[192:195], a[228:231], v[32:35]\n" \
"v_mfma_f32_16x16x32_bf16 v[64:67], a[192:195], a[232:235], v[64:67]\n" \
"ds_read_b64_tr_b16 a[220:221], v241 offset:25088\n" \
"v_mfma_f32_16x16x32_bf16 v[64:67], a[196:199], a[248:251], v[64:67]\n" \
"v_mfma_f32_16x16x32_bf16 v[96:99], a[196:199], a[252:255], v[96:99]\n" \
"ds_read_b64_tr_b16 a[222:223], v241 offset:27136\n" \
"v_mfma_f32_16x16x32_bf16 v[96:99], a[192:195], a[236:239], v[96:99]\n" \
"s_waitcnt lgkmcnt(13)\n" \
"s_lshl_b32 s85, s67, 5\n" \
"v_mfma_f32_16x16x32_bf16 v[4:7], a[200:203], a[224:227], v[4:7]\n" \
"s_add_u32 m0, s70, 8192\n" \
"s_add_u32 s85, s85, s68\n" \
"ds_read_b64_tr_b16 a[192:193], v240 offset:17408\n" \
"v_mfma_f32_16x16x32_bf16 v[4:7], a[204:207], a[240:243], v[4:7]\n" \
"s_add_u32 s74, s69, 3932160\n" \
"s_lshl2_add_u32 s85, s85, %[wid]\n" \
"v_mfma_f32_16x16x32_bf16 v[36:39], a[204:207], a[244:247], v[36:39]\n" \
"s_lshl_b32 s85, s85, 2\n" \
"buffer_load_dwordx4 v246, s[24:27], s74 offen lds\n" \
"v_mfma_f32_16x16x32_bf16 v[36:39], a[200:203], a[228:231], v[36:39]\n" \
"s_add_u32 s85, s85, 90112\n" \
"ds_read_b64_tr_b16 a[194:195], v240 offset:19456\n" \
"v_mfma_f32_16x16x32_bf16 v[68:71], a[200:203], a[232:235], v[68:71]\n" \
"s_waitcnt vmcnt(14)\n" \
"buffer_store_dword v239, off, s[32:35], s85 sc1\n" \
"v_mfma_f32_16x16x32_bf16 v[68:71], a[204:207], a[248:251], v[68:71]\n" \
"ds_read_b64_tr_b16 a[196:197], v240 offset:25600\n" \
"v_mfma_f32_16x16x32_bf16 v[100:103], a[204:207], a[252:255], v[100:103]\n" \
"ds_read_b64_tr_b16 a[198:199], v240 offset:27648\n" \
"v_mfma_f32_16x16x32_bf16 v[100:103], a[200:203], a[236:239], v[100:103]\n" \
"s_waitcnt lgkmcnt(11)\n" \
"v_mfma_f32_16x16x32_bf16 v[8:11], a[208:211], a[224:227], v[8:11]\n" \
"ds_read_b64_tr_b16 a[200:201], v241 offset:17408\n" \
"v_mfma_f32_16x16x32_bf16 v[8:11], a[212:215], a[240:243], v[8:11]\n" \
"v_mfma_f32_16x16x32_bf16 v[40:43], a[212:215], a[244:247], v[40:43]\n" \
"ds_read_b64_tr_b16 a[202:203], v241 offset:19456\n" \
"v_mfma_f32_16x16x32_bf16 v[40:43], a[208:211], a[228:231], v[40:43]\n" \
"v_mfma_f32_16x16x32_bf16 v[72:75], a[208:211], a[232:235], v[72:75]\n" \
"ds_read_b64_tr_b16 a[204:205], v241 offset:25600\n" \
"v_mfma_f32_16x16x32_bf16 v[72:75], a[212:215], a[248:251], v[72:75]\n" \
"v_mfma_f32_16x16x32_bf16 v[104:107], a[212:215], a[252:255], v[104:107]\n" \
"s_add_u32 m0, s70, 9216\n" \
"ds_read_b64_tr_b16 a[206:207], v241 offset:27648\n" \
"v_mfma_f32_16x16x32_bf16 v[104:107], a[208:211], a[236:239], v[104:107]\n" \
"s_waitcnt lgkmcnt(8)\n" \
"s_add_u32 s74, s69, 3932288\n" \
"v_mfma_f32_16x16x32_bf16 v[12:15], a[216:219], a[224:227], v[12:15]\n" \
"buffer_load_dwordx4 v246, s[24:27], s74 offen lds\n" \
"v_mfma_f32_16x16x32_bf16 v[12:15], a[220:223], a[240:243], v[12:15]\n" \
"ds_read_b64_tr_b16 a[208:209], v240 offset:17920\n" \
"v_mfma_f32_16x16x32_bf16 v[44:47], a[220:223], a[244:247], v[44:47]\n" \
"ds_read_b64_tr_b16 a[210:211], v240 offset:19968\n" \
"v_mfma_f32_16x16x32_bf16 v[44:47], a[216:219], a[228:231], v[44:47]\n" \
"v_mfma_f32_16x16x32_bf16 v[76:79], a[216:219], a[232:235], v[76:79]\n" \
"ds_read_b64_tr_b16 a[212:213], v240 offset:26112\n" \
"v_mfma_f32_16x16x32_bf16 v[76:79], a[220:223], a[248:251], v[76:79]\n" \
"v_mfma_f32_16x16x32_bf16 v[108:111], a[220:223], a[252:255], v[108:111]\n" \
"ds_read_b64_tr_b16 a[214:215], v240 offset:28160\n" \
"v_mfma_f32_16x16x32_bf16 v[108:111], a[216:219], a[236:239], v[108:111]\n" \
"s_waitcnt lgkmcnt(8)\n" \
"v_mfma_f32_16x16x32_bf16 v[16:19], a[192:195], a[224:227], v[16:19]\n" \
"ds_read_b64_tr_b16 a[216:217], v241 offset:17920\n" \
"v_mfma_f32_16x16x32_bf16 v[16:19], a[196:199], a[240:243], v[16:19]\n" \
"v_mfma_f32_16x16x32_bf16 v[48:51], a[196:199], a[244:247], v[48:51]\n" \
"ds_read_b64_tr_b16 a[218:219], v241 offset:19968\n" \
"v_mfma_f32_16x16x32_bf16 v[48:51], a[192:195], a[228:231], v[48:51]\n" \
"ds_read_b64_tr_b16 a[220:221], v241 offset:26112\n" \
"v_mfma_f32_16x16x32_bf16 v[80:83], a[192:195], a[232:235], v[80:83]\n" \
"ds_read_b64_tr_b16 a[222:223], v241 offset:28160\n" \
"v_mfma_f32_16x16x32_bf16 v[80:83], a[196:199], a[248:251], v[80:83]\n" \
"v_mfma_f32_16x16x32_bf16 v[112:115], a[196:199], a[252:255], v[112:115]\n" \
"v_mfma_f32_16x16x32_bf16 v[112:115], a[192:195], a[236:239], v[112:115]\n" \
"s_waitcnt lgkmcnt(8)\n" \
"v_mfma_f32_16x16x32_bf16 v[20:23], a[200:203], a[224:227], v[20:23]\n" \
"v_mfma_f32_16x16x32_bf16 v[20:23], a[204:207], a[240:243], v[20:23]\n" \
"v_mfma_f32_16x16x32_bf16 v[52:55], a[204:207], a[244:247], v[52:55]\n" \
"v_mfma_f32_16x16x32_bf16 v[52:55], a[200:203], a[228:231], v[52:55]\n" \
"v_mfma_f32_16x16x32_bf16 v[84:87], a[200:203], a[232:235], v[84:87]\n" \
"s_waitcnt lgkmcnt(0)\n" \
"s_barrier\n" \
"s_add_u32 m0, s70, 16384\n" \
"v_mfma_f32_16x16x32_bf16 v[84:87], a[204:207], a[248:251], v[84:87]\n" \
"s_add_u32 s74, s69, 2097152\n" \
"ds_read_b64_tr_b16 a[192:193], v240 offset:32768\n" \
"ds_read_b64_tr_b16 a[194:195], v240 offset:34816\n" \
"v_mfma_f32_16x16x32_bf16 v[116:119], a[204:207], a[252:255], v[116:119]\n" \
"buffer_load_dwordx4 v246, s[24:27], s74 offen lds\n" \
"v_mfma_f32_16x16x32_bf16 v[116:119], a[200:203], a[236:239], v[116:119]\n" \
"ds_read_b64_tr_b16 a[196:197], v240 offset:40960\n" \
"ds_read_b64_tr_b16 a[198:199], v240 offset:43008\n" \
"v_mfma_f32_16x16x32_bf16 v[24:27], a[208:211], a[224:227], v[24:27]\n" \
"ds_read_b64_tr_b16 a[200:201], v241 offset:32768\n" \
"v_mfma_f32_16x16x32_bf16 v[24:27], a[212:215], a[240:243], v[24:27]\n" \
"v_mfma_f32_16x16x32_bf16 v[56:59], a[212:215], a[244:247], v[56:59]\n" \
"ds_read_b64_tr_b16 a[202:203], v241 offset:34816\n" \
"v_mfma_f32_16x16x32_bf16 v[56:59], a[208:211], a[228:231], v[56:59]\n" \
"v_mfma_f32_16x16x32_bf16 v[88:91], a[208:211], a[232:235], v[88:91]\n" \
"ds_read_b64_tr_b16 a[204:205], v241 offset:40960\n" \
"v_mfma_f32_16x16x32_bf16 v[88:91], a[212:215], a[248:251], v[88:91]\n" \
"v_mfma_f32_16x16x32_bf16 v[120:123], a[212:215], a[252:255], v[120:123]\n" \
"ds_read_b64_tr_b16 a[206:207], v241 offset:43008\n" \
"v_mfma_f32_16x16x32_bf16 v[120:123], a[208:211], a[236:239], v[120:123]\n" \
"v_mfma_f32_16x16x32_bf16 v[28:31], a[216:219], a[224:227], v[28:31]\n" \
"ds_read_b64_tr_b16 a[208:209], v240 offset:33280\n" \
"v_mfma_f32_16x16x32_bf16 v[28:31], a[220:223], a[240:243], v[28:31]\n" \
"v_mfma_f32_16x16x32_bf16 v[60:63], a[220:223], a[244:247], v[60:63]\n" \
"s_add_u32 m0, s70, 17408\n" \
"ds_read_b64_tr_b16 a[210:211], v240 offset:35328\n" \
"v_mfma_f32_16x16x32_bf16 v[60:63], a[216:219], a[228:231], v[60:63]\n" \
"s_add_u32 s74, s69, 2097280\n" \
"s_lshl2_add_u32 s85, s67, %[wid]\n" \
"v_mfma_f32_16x16x32_bf16 v[92:95], a[216:219], a[232:235], v[92:95]\n" \
"s_lshl_b32 s85, s85, 16\n" \
"buffer_load_dwordx4 v246, s[24:27], s74 offen lds\n" \
"v_mfma_f32_16x16x32_bf16 v[92:95], a[220:223], a[248:251], v[92:95]\n" \
"s_lshl_b32 s86, s68, 8\n" \
"ds_read_b64_tr_b16 a[212:213], v240 offset:41472\n" \
"v_mfma_f32_16x16x32_bf16 v[124:127], a[220:223], a[252:255], v[124:127]\n" \
"s_add_u32 s85, s85, s86\n" \
"ds_read_b64_tr_b16 a[214:215], v240 offset:43520\n" \
"v_mfma_f32_16x16x32_bf16 v[124:127], a[216:219], a[236:239], v[124:127]\n" \
"s_waitcnt lgkmcnt(8)\n" \
"s_add_u32 s85, s85, 0\n" \
"v_mfma_f32_16x16x32_bf16 v[0:3], a[192:195], a[128:131], v[0:3]\n" \
"buffer_load_dword v238, v208, s[28:31], s85 offen\n" \
"v_mfma_f32_16x16x32_bf16 v[0:3], a[196:199], a[144:147], v[0:3]\n" \
"s_lshl2_add_u32 s85, s67, %[wid]\n" \
"s_lshl_b32 s85, s85, 16\n" \
"ds_read_b64_tr_b16 a[216:217], v241 offset:33280\n" \
"v_mfma_f32_16x16x32_bf16 v[32:35], a[196:199], a[148:151], v[32:35]\n" \
"s_lshl_b32 s86, s68, 8\n" \
"s_add_u32 s85, s85, s86\n" \
"ds_read_b64_tr_b16 a[218:219], v241 offset:35328\n" \
"v_mfma_f32_16x16x32_bf16 v[32:35], a[192:195], a[132:135], v[32:35]\n" \
"s_add_u32 s85, s85, 2097152\n" \
"v_mfma_f32_16x16x32_bf16 v[64:67], a[192:195], a[136:139], v[64:67]\n" \
"buffer_load_dword v238, v208, s[28:31], s85 offen\n" \
"v_mfma_f32_16x16x32_bf16 v[64:67], a[196:199], a[152:155], v[64:67]\n" \
"ds_read_b64_tr_b16 a[220:221], v241 offset:41472\n" \
"v_mfma_f32_16x16x32_bf16 v[96:99], a[196:199], a[156:159], v[96:99]\n" \
"ds_read_b64_tr_b16 a[222:223], v241 offset:43520\n" \
"v_mfma_f32_16x16x32_bf16 v[96:99], a[192:195], a[140:143], v[96:99]\n" \
"s_waitcnt lgkmcnt(8)\n" \
"v_mfma_f32_16x16x32_bf16 v[4:7], a[200:203], a[128:131], v[4:7]\n" \
"s_add_u32 m0, s70, 24576\n" \
"ds_read_b64_tr_b16 a[192:193], v240 offset:33792\n" \
"v_mfma_f32_16x16x32_bf16 v[4:7], a[204:207], a[144:147], v[4:7]\n" \
"s_add_u32 s74, s69, 2359296\n" \
"v_mfma_f32_16x16x32_bf16 v[36:39], a[204:207], a[148:151], v[36:39]\n" \
"buffer_load_dwordx4 v246, s[24:27], s74 offen lds\n" \
"v_mfma_f32_16x16x32_bf16 v[36:39], a[200:203], a[132:135], v[36:39]\n" \
"s_waitcnt vmcnt(10)\n" \
"ds_read_b64_tr_b16 a[194:195], v240 offset:35840\n" \
"ds_read_b128 v[192:195], v242 offset:8192\n" \
"v_mfma_f32_16x16x32_bf16 v[68:71], a[200:203], a[136:139], v[68:71]\n" \
"ds_read_b64_tr_b16 a[196:197], v240 offset:41984\n" \
"ds_read_b128 v[196:199], v242 offset:10240\n" \
"v_mfma_f32_16x16x32_bf16 v[68:71], a[204:207], a[152:155], v[68:71]\n" \
"v_mfma_f32_16x16x32_bf16 v[100:103], a[204:207], a[156:159], v[100:103]\n" \
"ds_read_b64_tr_b16 a[198:199], v240 offset:44032\n" \
"ds_read_b128 v[200:203], v242 offset:12288\n" \
"v_mfma_f32_16x16x32_bf16 v[100:103], a[200:203], a[140:143], v[100:103]\n" \
"s_waitcnt lgkmcnt(11)\n" \
"v_mfma_f32_16x16x32_bf16 v[8:11], a[208:211], a[128:131], v[8:11]\n" \
"ds_read_b64_tr_b16 a[200:201], v241 offset:33792\n" \
"ds_read_b128 v[204:207], v242 offset:14336\n" \
"v_mfma_f32_16x16x32_bf16 v[8:11], a[212:215], a[144:147], v[8:11]\n" \
"v_mfma_f32_16x16x32_bf16 v[40:43], a[212:215], a[148:151], v[40:43]\n" \
"ds_read_b64_tr_b16 a[202:203], v241 offset:35840\n" \
"v_mfma_f32_16x16x32_bf16 v[40:43], a[208:211], a[132:135], v[40:43]\n" \
"v_mfma_f32_16x16x32_bf16 v[72:75], a[208:211], a[136:139], v[72:75]\n" \
"ds_read_b64_tr_b16 a[204:205], v241 offset:41984\n" \
"ds_read_b128 v[208:211], v244 offset:8192\n" \
"v_mfma_f32_16x16x32_bf16 v[72:75], a[212:215], a[152:155], v[72:75]\n" \
"v_mfma_f32_16x16x32_bf16 v[104:107], a[212:215], a[156:159], v[104:107]\n" \
"s_add_u32 m0, s70, 25600\n" \
"ds_read_b64_tr_b16 a[206:207], v241 offset:44032\n" \
"ds_read_b128 v[212:215], v244 offset:10240\n" \
"v_mfma_f32_16x16x32_bf16 v[104:107], a[208:211], a[140:143], v[104:107]\n" \
"s_waitcnt lgkmcnt(14)\n" \
"s_add_u32 s74, s69, 2359424\n" \
"v_mfma_f32_16x16x32_bf16 v[12:15], a[216:219], a[128:131], v[12:15]\n" \
"buffer_load_dwordx4 v246, s[24:27], s74 offen lds\n" \
"v_mfma_f32_16x16x32_bf16 v[12:15], a[220:223], a[144:147], v[12:15]\n" \
"ds_read_b64_tr_b16 a[208:209], v240 offset:34304\n" \
"ds_read_b128 v[216:219], v244 offset:12288\n" \
"v_mfma_f32_16x16x32_bf16 v[44:47], a[220:223], a[148:151], v[44:47]\n" \
"ds_read_b64_tr_b16 a[210:211], v240 offset:36352\n" \
"ds_read_b128 v[220:223], v244 offset:14336\n" \
"v_mfma_f32_16x16x32_bf16 v[44:47], a[216:219], a[132:135], v[44:47]\n" \
"v_mfma_f32_16x16x32_bf16 v[76:79], a[216:219], a[136:139], v[76:79]\n" \
"ds_read_b64_tr_b16 a[212:213], v240 offset:42496\n" \
"v_mfma_f32_16x16x32_bf16 v[76:79], a[220:223], a[152:155], v[76:79]\n" \
"v_mfma_f32_16x16x32_bf16 v[108:111], a[220:223], a[156:159], v[108:111]\n" \
"ds_read_b64_tr_b16 a[214:215], v240 offset:44544\n" \
"v_mfma_f32_16x16x32_bf16 v[108:111], a[216:219], a[140:143], v[108:111]\n" \
"s_waitcnt lgkmcnt(14)\n" \
"v_mfma_f32_16x16x32_bf16 v[16:19], a[192:195], a[128:131], v[16:19]\n" \
"ds_read_b64_tr_b16 a[216:217], v241 offset:34304\n" \
"v_mfma_f32_16x16x32_bf16 v[16:19], a[196:199], a[144:147], v[16:19]\n" \
"v_mfma_f32_16x16x32_bf16 v[48:51], a[196:199], a[148:151], v[48:51]\n" \
"ds_read_b64_tr_b16 a[218:219], v241 offset:36352\n" \
"v_mfma_f32_16x16x32_bf16 v[48:51], a[192:195], a[132:135], v[48:51]\n" \
"ds_read_b64_tr_b16 a[220:221], v241 offset:42496\n" \
"v_mfma_f32_16x16x32_bf16 v[80:83], a[192:195], a[136:139], v[80:83]\n" \
"ds_read_b64_tr_b16 a[222:223], v241 offset:44544\n" \
"v_mfma_f32_16x16x32_bf16 v[80:83], a[196:199], a[152:155], v[80:83]\n" \
"v_mfma_f32_16x16x32_bf16 v[112:115], a[196:199], a[156:159], v[112:115]\n" \
"v_mfma_f32_16x16x32_bf16 v[112:115], a[192:195], a[140:143], v[112:115]\n" \
"s_waitcnt lgkmcnt(11)\n" \
"v_mfma_f32_16x16x32_bf16 v[20:23], a[200:203], a[128:131], v[20:23]\n" \
"v_mfma_f32_16x16x32_bf16 v[20:23], a[204:207], a[144:147], v[20:23]\n" \
"v_mfma_f32_16x16x32_bf16 v[52:55], a[204:207], a[148:151], v[52:55]\n" \
"v_mfma_f32_16x16x32_bf16 v[52:55], a[200:203], a[132:135], v[52:55]\n" \
"v_mfma_f32_16x16x32_bf16 v[84:87], a[200:203], a[136:139], v[84:87]\n" \
"s_waitcnt lgkmcnt(0)\n" \
"s_barrier\n" \
"s_add_u32 m0, s70, 32768\n" \
"v_mfma_f32_16x16x32_bf16 v[84:87], a[204:207], a[152:155], v[84:87]\n" \
"s_add_u32 s74, s69, 2621440\n" \
"ds_read_b64_tr_b16 a[192:193], v240 offset:49152\n" \
"ds_read_b64_tr_b16 a[194:195], v240 offset:51200\n" \
"v_mfma_f32_16x16x32_bf16 v[116:119], a[204:207], a[156:159], v[116:119]\n" \
"buffer_load_dwordx4 v246, s[24:27], s74 offen lds\n" \
"v_mfma_f32_16x16x32_bf16 v[116:119], a[200:203], a[140:143], v[116:119]\n" \
"ds_read_b64_tr_b16 a[196:197], v240 offset:57344\n" \
"ds_read_b64_tr_b16 a[198:199], v240 offset:59392\n" \
"v_mfma_f32_16x16x32_bf16 v[24:27], a[208:211], a[128:131], v[24:27]\n" \
"ds_read_b64_tr_b16 a[200:201], v241 offset:49152\n" \
"v_mfma_f32_16x16x32_bf16 v[24:27], a[212:215], a[144:147], v[24:27]\n" \
"v_mfma_f32_16x16x32_bf16 v[56:59], a[212:215], a[148:151], v[56:59]\n" \
"ds_read_b64_tr_b16 a[202:203], v241 offset:51200\n" \
"v_mfma_f32_16x16x32_bf16 v[56:59], a[208:211], a[132:135], v[56:59]\n" \
"v_mfma_f32_16x16x32_bf16 v[88:91], a[208:211], a[136:139], v[88:91]\n" \
"ds_read_b64_tr_b16 a[204:205], v241 offset:57344\n" \
"v_mfma_f32_16x16x32_bf16 v[88:91], a[212:215], a[152:155], v[88:91]\n" \
"v_mfma_f32_16x16x32_bf16 v[120:123], a[212:215], a[156:159], v[120:123]\n" \
"ds_read_b64_tr_b16 a[206:207], v241 offset:59392\n" \
"v_mfma_f32_16x16x32_bf16 v[120:123], a[208:211], a[140:143], v[120:123]\n" \
"v_mfma_f32_16x16x32_bf16 v[28:31], a[216:219], a[128:131], v[28:31]\n" \
"ds_read_b64_tr_b16 a[208:209], v240 offset:49664\n" \
"v_mfma_f32_16x16x32_bf16 v[28:31], a[220:223], a[144:147], v[28:31]\n" \
"v_mfma_f32_16x16x32_bf16 v[60:63], a[220:223], a[148:151], v[60:63]\n" \
"s_add_u32 m0, s70, 33792\n" \
"ds_read_b64_tr_b16 a[210:211], v240 offset:51712\n" \
"v_mfma_f32_16x16x32_bf16 v[60:63], a[216:219], a[132:135], v[60:63]\n" \
".Lst0_3_%=:\n" \
"s_cbranch_execz .Lst0_7_%=\n" \
"s_add_u32 s74, s69, 2621568\n" \
"v_mfma_f32_16x16x32_bf16 v[92:95], a[216:219], a[136:139], v[92:95]\n" \
"s_lshr_b32 s71, s71, 2\n" \
"buffer_load_dwordx4 v246, s[24:27], s74 offen lds\n" \
"v_mfma_f32_16x16x32_bf16 v[92:95], a[220:223], a[152:155], v[92:95]\n" \
"s_add_u32 s71, s71, s73\n" \
"s_add_u32 s94, s94, 32768\n" \
"ds_read_b64_tr_b16 a[212:213], v240 offset:57856\n" \
"v_mfma_f32_16x16x32_bf16 v[124:127], a[220:223], a[156:159], v[124:127]\n" \
"s_lshr_b32 s94, s94, 2\n" \
"s_add_u32 s94, s94, s73\n" \
"ds_read_b64_tr_b16 a[214:215], v240 offset:59904\n" \
"v_mfma_f32_16x16x32_bf16 v[124:127], a[216:219], a[140:143], v[124:127]\n" \
"s_waitcnt lgkmcnt(8)\n" \
"s_lshl_b32 s72, %[wid], 13\n" \
"v_mfma_f32_16x16x32_bf16 v[0:3], a[192:195], v[192:195], v[0:3]\n" \
"ds_read_b64_tr_b16 a[216:217], v241 offset:49664\n" \
"v_mfma_f32_16x16x32_bf16 v[0:3], a[196:199], v[208:211], v[0:3]\n" \
"v_mfma_f32_16x16x32_bf16 v[32:35], a[196:199], v[212:215], v[32:35]\n" \
"ds_read_b64_tr_b16 a[218:219], v241 offset:51712\n" \
"v_add_u32 v244, s72, v248\n" \
"v_mfma_f32_16x16x32_bf16 v[32:35], a[192:195], v[196:199], v[32:35]\n" \
"v_add_u32 v249, 0x10000, v244\n" \
"v_mov_b32 v250, 0\n" \
"v_mfma_f32_16x16x32_bf16 v[64:67], a[192:195], v[200:203], v[64:67]\n" \
"ds_read_b64_tr_b16 a[220:221], v241 offset:57856\n" \
"v_mfma_f32_16x16x32_bf16 v[64:67], a[196:199], v[216:219], v[64:67]\n" \
"v_mfma_f32_16x16x32_bf16 v[96:99], a[196:199], v[220:223], v[96:99]\n" \
"ds_read_b64_tr_b16 a[222:223], v241 offset:59904\n" \
"v_mfma_f32_16x16x32_bf16 v[96:99], a[192:195], v[204:207], v[96:99]\n" \
"s_waitcnt lgkmcnt(8)\n" \
"v_mfma_f32_16x16x32_bf16 v[4:7], a[200:203], v[192:195], v[4:7]\n" \
"s_add_u32 m0, s70, 40960\n" \
"ds_read_b64_tr_b16 a[192:193], v240 offset:50176\n" \
"v_mfma_f32_16x16x32_bf16 v[4:7], a[204:207], v[208:211], v[4:7]\n" \
"s_add_u32 s74, s69, 2883584\n" \
"v_mfma_f32_16x16x32_bf16 v[36:39], a[204:207], v[212:215], v[36:39]\n" \
"buffer_load_dwordx4 v246, s[24:27], s74 offen lds\n" \
"v_mfma_f32_16x16x32_bf16 v[36:39], a[200:203], v[196:199], v[36:39]\n" \
"ds_read_b64_tr_b16 a[194:195], v240 offset:52224\n" \
"v_mfma_f32_16x16x32_bf16 v[68:71], a[200:203], v[200:203], v[68:71]\n" \
"ds_read_b64_tr_b16 a[196:197], v240 offset:58368\n" \
"v_mfma_f32_16x16x32_bf16 v[68:71], a[204:207], v[216:219], v[68:71]\n" \
"v_mfma_f32_16x16x32_bf16 v[100:103], a[204:207], v[220:223], v[100:103]\n" \
"ds_read_b64_tr_b16 a[198:199], v240 offset:60416\n" \
"v_mfma_f32_16x16x32_bf16 v[100:103], a[200:203], v[204:207], v[100:103]\n" \
"s_waitcnt lgkmcnt(8)\n" \
"v_mfma_f32_16x16x32_bf16 v[8:11], a[208:211], v[192:195], v[8:11]\n" \
"ds_read_b64_tr_b16 a[200:201], v241 offset:50176\n" \
"v_mfma_f32_16x16x32_bf16 v[8:11], a[212:215], v[208:211], v[8:11]\n" \
"v_mfma_f32_16x16x32_bf16 v[40:43], a[212:215], v[212:215], v[40:43]\n" \
"ds_read_b64_tr_b16 a[202:203], v241 offset:52224\n" \
"v_mfma_f32_16x16x32_bf16 v[40:43], a[208:211], v[196:199], v[40:43]\n" \
"v_mfma_f32_16x16x32_bf16 v[72:75], a[208:211], v[200:203], v[72:75]\n" \
"ds_read_b64_tr_b16 a[204:205], v241 offset:58368\n" \
"v_mfma_f32_16x16x32_bf16 v[72:75], a[212:215], v[216:219], v[72:75]\n" \
"v_mfma_f32_16x16x32_bf16 v[104:107], a[212:215], v[220:223], v[104:107]\n" \
"s_add_u32 m0, s70, 41984\n" \
"ds_read_b64_tr_b16 a[206:207], v241 offset:60416\n" \
"v_mfma_f32_16x16x32_bf16 v[104:107], a[208:211], v[204:207], v[104:107]\n" \
"s_waitcnt lgkmcnt(8)\n" \
"s_add_u32 s74, s69, 2883712\n" \
"v_mfma_f32_16x16x32_bf16 v[12:15], a[216:219], v[192:195], v[12:15]\n" \
"buffer_load_dwordx4 v246, s[24:27], s74 offen lds\n" \
"v_mfma_f32_16x16x32_bf16 v[12:15], a[220:223], v[208:211], v[12:15]\n" \
"ds_read_b64_tr_b16 a[208:209], v240 offset:50688\n" \
"v_mfma_f32_16x16x32_bf16 v[44:47], a[220:223], v[212:215], v[44:47]\n" \
"ds_read_b64_tr_b16 a[210:211], v240 offset:52736\n" \
"v_mfma_f32_16x16x32_bf16 v[44:47], a[216:219], v[196:199], v[44:47]\n" \
"v_mfma_f32_16x16x32_bf16 v[76:79], a[216:219], v[200:203], v[76:79]\n" \
"ds_read_b64_tr_b16 a[212:213], v240 offset:58880\n" \
"v_mfma_f32_16x16x32_bf16 v[76:79], a[220:223], v[216:219], v[76:79]\n" \
"v_mfma_f32_16x16x32_bf16 v[108:111], a[220:223], v[220:223], v[108:111]\n" \
"ds_read_b64_tr_b16 a[214:215], v240 offset:60928\n" \
"v_mfma_f32_16x16x32_bf16 v[108:111], a[216:219], v[204:207], v[108:111]\n" \
"s_waitcnt lgkmcnt(8)\n" \
"v_mfma_f32_16x16x32_bf16 v[16:19], a[192:195], v[192:195], v[16:19]\n" \
"ds_read_b64_tr_b16 a[216:217], v241 offset:50688\n" \
"v_mfma_f32_16x16x32_bf16 v[16:19], a[196:199], v[208:211], v[16:19]\n" \
"v_mfma_f32_16x16x32_bf16 v[48:51], a[196:199], v[212:215], v[48:51]\n" \
"ds_read_b64_tr_b16 a[218:219], v241 offset:52736\n" \
"v_mfma_f32_16x16x32_bf16 v[48:51], a[192:195], v[196:199], v[48:51]\n" \
"ds_read_b64_tr_b16 a[220:221], v241 offset:58880\n" \
"v_mfma_f32_16x16x32_bf16 v[80:83], a[192:195], v[200:203], v[80:83]\n" \
"ds_read_b64_tr_b16 a[222:223], v241 offset:60928\n" \
"v_mul_f32 v0, s84, v0\n" \
"v_mfma_f32_16x16x32_bf16 v[80:83], a[196:199], v[216:219], v[80:83]\n" \
"v_mul_f32 v1, s84, v1\n" \
"v_mul_f32 v2, s84, v2\n" \
"v_mfma_f32_16x16x32_bf16 v[112:115], a[196:199], v[220:223], v[112:115]\n" \
"v_mul_f32 v3, s84, v3\n" \
"v_mul_f32 v4, s84, v4\n" \
"v_mfma_f32_16x16x32_bf16 v[112:115], a[192:195], v[204:207], v[112:115]\n" \
"s_waitcnt lgkmcnt(8)\n" \
"v_mul_f32 v5, s84, v5\n" \
"v_mul_f32 v6, s84, v6\n" \
"v_mfma_f32_16x16x32_bf16 v[20:23], a[200:203], v[192:195], v[20:23]\n" \
"v_mul_f32 v7, s84, v7\n" \
"v_cvt_pk_bf16_f32 v128, v0, v1\n" \
"v_mfma_f32_16x16x32_bf16 v[20:23], a[204:207], v[208:211], v[20:23]\n" \
"v_cvt_pk_bf16_f32 v129, v2, v3\n" \
"v_cvt_pk_bf16_f32 v130, v4, v5\n" \
"v_mfma_f32_16x16x32_bf16 v[52:55], a[204:207], v[212:215], v[52:55]\n" \
"v_cvt_pk_bf16_f32 v131, v6, v7\n" \
"v_mfma_f32_16x16x32_bf16 v[52:55], a[200:203], v[196:199], v[52:55]\n" \
"ds_write_b128 v244, v[128:131] offset:0\n" \
"v_mfma_f32_16x16x32_bf16 v[84:87], a[200:203], v[200:203], v[84:87]\n" \
"s_waitcnt vmcnt(10) lgkmcnt(1)\n" \
"s_barrier\n" \
"s_add_u32 m0, s70, 49152\n" \
"v_mfma_f32_16x16x32_bf16 v[84:87], a[204:207], v[216:219], v[84:87]\n" \
"s_add_u32 s74, s69, 3145728\n" \
"ds_read_b64_tr_b16 a[192:193], v240 offset:0\n" \
"ds_read_b64_tr_b16 a[194:195], v240 offset:2048\n" \
"v_mfma_f32_16x16x32_bf16 v[116:119], a[204:207], v[220:223], v[116:119]\n" \
"buffer_load_dwordx4 v246, s[24:27], s74 offen lds\n" \
"v_mfma_f32_16x16x32_bf16 v[116:119], a[200:203], v[204:207], v[116:119]\n" \
"ds_read_b64_tr_b16 a[196:197], v240 offset:8192\n" \
"ds_read_b64_tr_b16 a[198:199], v240 offset:10240\n" \
"v_mfma_f32_16x16x32_bf16 v[24:27], a[208:211], v[192:195], v[24:27]\n" \
"ds_read_b64_tr_b16 a[200:201], v241 offset:0\n" \
"v_mfma_f32_16x16x32_bf16 v[24:27], a[212:215], v[208:211], v[24:27]\n" \
"v_mfma_f32_16x16x32_bf16 v[56:59], a[212:215], v[212:215], v[56:59]\n" \
"ds_read_b64_tr_b16 a[202:203], v241 offset:2048\n" \
"v_mul_f32 v8, s84, v8\n" \
"v_mfma_f32_16x16x32_bf16 v[56:59], a[208:211], v[196:199], v[56:59]\n" \
"v_mul_f32 v9, s84, v9\n" \
"v_mul_f32 v10, s84, v10\n" \
"v_mfma_f32_16x16x32_bf16 v[88:91], a[208:211], v[200:203], v[88:91]\n" \
"ds_read_b64_tr_b16 a[204:205], v241 offset:8192\n" \
"v_and_b32 v252, 15, v251\n" \
"v_mfma_f32_16x16x32_bf16 v[88:91], a[212:215], v[216:219], v[88:91]\n" \
"v_lshlrev_b32 v252, 2, v252\n" \
"v_and_b32 v253, 16, v251\n" \
"v_mfma_f32_16x16x32_bf16 v[120:123], a[212:215], v[220:223], v[120:123]\n" \
"ds_read_b64_tr_b16 a[206:207], v241 offset:10240\n" \
"v_lshl_or_b32 v252, v253, 4, v252\n" \
"v_mfma_f32_16x16x32_bf16 v[120:123], a[208:211], v[204:207], v[120:123]\n" \
"v_mul_f32 v11, s84, v11\n" \
"v_mul_f32 v12, s84, v12\n" \
"v_mfma_f32_16x16x32_bf16 v[28:31], a[216:219], v[192:195], v[28:31]\n" \
"ds_read_b64_tr_b16 a[208:209], v240 offset:512\n" \
"v_mul_f32 v13, s84, v13\n" \
"v_mfma_f32_16x16x32_bf16 v[28:31], a[220:223], v[208:211], v[28:31]\n" \
"v_mul_f32 v14, s84, v14\n" \
"v_mul_f32 v15, s84, v15\n" \
"v_mfma_f32_16x16x32_bf16 v[60:63], a[220:223], v[212:215], v[60:63]\n" \
"s_add_u32 m0, s70, 50176\n" \
"ds_read_b64_tr_b16 a[210:211], v240 offset:2560\n" \
"v_cvt_pk_bf16_f32 v132, v8, v9\n" \
"v_mfma_f32_16x16x32_bf16 v[60:63], a[216:219], v[196:199], v[60:63]\n" \
"s_add_u32 s74, s69, 3145856\n" \
"v_cvt_pk_bf16_f32 v133, v10, v11\n" \
"v_cvt_pk_bf16_f32 v134, v12, v13\n" \
"v_mfma_f32_16x16x32_bf16 v[92:95], a[216:219], v[200:203], v[92:95]\n" \
"buffer_load_dwordx4 v246, s[24:27], s74 offen lds\n" \
"v_mfma_f32_16x16x32_bf16 v[92:95], a[220:223], v[216:219], v[92:95]\n" \
"ds_read_b64_tr_b16 a[212:213], v240 offset:8704\n" \
"v_cvt_pk_bf16_f32 v135, v14, v15\n" \
"v_mfma_f32_16x16x32_bf16 v[124:127], a[220:223], v[220:223], v[124:127]\n" \
"ds_read_b64_tr_b16 a[214:215], v240 offset:10752\n" \
"v_mfma_f32_16x16x32_bf16 v[124:127], a[216:219], v[204:207], v[124:127]\n" \
"s_waitcnt lgkmcnt(8)\n" \
"ds_write_b128 v244, v[132:135] offset:1024\n" \
  \
"v_mfma_f32_16x16x32_bf16 a[0:3], v[192:195], a[192:195], a[0:3]\n" \
"ds_read_b64_tr_b16 a[216:217], v241 offset:512\n" \
"v_mul_f32 v16, s84, v16\n" \
"v_mfma_f32_16x16x32_bf16 a[0:3], v[208:211], a[196:199], a[0:3]\n" \
"v_mul_f32 v17, s84, v17\n" \
"v_mul_f32 v18, s84, v18\n" \
"v_mfma_f32_16x16x32_bf16 a[4:7], v[212:215], a[196:199], a[4:7]\n" \
"ds_read_b64_tr_b16 a[218:219], v241 offset:2560\n" \
"v_mul_f32 v19, s84, v19\n" \
"v_mfma_f32_16x16x32_bf16 a[4:7], v[196:199], a[192:195], a[4:7]\n" \
"v_mul_f32 v20, s84, v20\n" \
"v_mul_f32 v21, s84, v21\n" \
"v_mfma_f32_16x16x32_bf16 a[64:67], v[200:203], a[192:195], a[64:67]\n" \
"ds_read_b64_tr_b16 a[220:221], v241 offset:8704\n" \
"v_mul_f32 v22, s84, v22\n" \
"v_mfma_f32_16x16x32_bf16 a[64:67], v[216:219], a[196:199], a[64:67]\n" \
"v_mul_f32 v23, s84, v23\n" \
"v_cvt_pk_bf16_f32 v136, v16, v17\n" \
"v_mfma_f32_16x16x32_bf16 a[68:71], v[220:223], a[196:199], a[68:71]\n" \
"ds_read_b64_tr_b16 a[222:223], v241 offset:10752\n" \
"v_cvt_pk_bf16_f32 v137, v18, v19\n" \
"v_mfma_f32_16x16x32_bf16 a[68:71], v[204:207], a[192:195], a[68:71]\n" \
"s_waitcnt lgkmcnt(9)\n" \
"v_cvt_pk_bf16_f32 v138, v20, v21\n" \
"v_cvt_pk_bf16_f32 v139, v22, v23\n" \
"v_mfma_f32_16x16x32_bf16 a[8:11], v[192:195], a[200:203], a[8:11]\n" \
"s_add_u32 m0, s70, 57344\n" \
"ds_read_b64_tr_b16 a[192:193], v240 offset:1024\n" \
"v_mfma_f32_16x16x32_bf16 a[8:11], v[208:211], a[204:207], a[8:11]\n" \
"s_add_u32 s74, s69, 3407872\n" \
"ds_write_b128 v244, v[136:139] offset:2048\n" \
"v_mfma_f32_16x16x32_bf16 a[12:15], v[212:215], a[204:207], a[12:15]\n" \
"buffer_load_dwordx4 v246, s[24:27], s74 offen lds\n" \
"v_mfma_f32_16x16x32_bf16 a[12:15], v[196:199], a[200:203], a[12:15]\n" \
"ds_read_b64_tr_b16 a[194:195], v240 offset:3072\n" \
"v_mfma_f32_16x16x32_bf16 a[72:75], v[200:203], a[200:203], a[72:75]\n" \
"ds_read_b64_tr_b16 a[196:197], v240 offset:9216\n" \
"v_mfma_f32_16x16x32_bf16 a[72:75], v[216:219], a[204:207], a[72:75]\n" \
"v_mfma_f32_16x16x32_bf16 a[76:79], v[220:223], a[204:207], a[76:79]\n" \
"ds_read_b64_tr_b16 a[198:199], v240 offset:11264\n" \
"v_mul_f32 v24, s84, v24\n" \
"v_mfma_f32_16x16x32_bf16 a[76:79], v[204:207], a[200:203], a[76:79]\n" \
"s_waitcnt lgkmcnt(10)\n" \
"v_mul_f32 v25, s84, v25\n" \
"v_mul_f32 v26, s84, v26\n" \
"v_mfma_f32_16x16x32_bf16 a[16:19], v[192:195], a[208:211], a[16:19]\n" \
"ds_read_b64_tr_b16 a[200:201], v241 offset:1024\n" \
"v_mul_f32 v27, s84, v27\n" \
"v_mfma_f32_16x16x32_bf16 a[16:19], v[208:211], a[212:215], a[16:19]\n" \
"v_mul_f32 v28, s84, v28\n" \
"v_mul_f32 v29, s84, v29\n" \
"v_mfma_f32_16x16x32_bf16 a[20:23], v[212:215], a[212:215], a[20:23]\n" \
"ds_read_b64_tr_b16 a[202:203], v241 offset:3072\n" \
"v_mul_f32 v30, s84, v30\n" \
"v_mfma_f32_16x16x32_bf16 a[20:23], v[196:199], a[208:211], a[20:23]\n" \
"v_mul_f32 v31, s84, v31\n" \
"v_cvt_pk_bf16_f32 v140, v24, v25\n" \
"v_mfma_f32_16x16x32_bf16 a[80:83], v[200:203], a[208:211], a[80:83]\n" \
"ds_read_b64_tr_b16 a[204:205], v241 offset:9216\n" \
"v_cvt_pk_bf16_f32 v141, v26, v27\n" \
"v_mfma_f32_16x16x32_bf16 a[80:83], v[216:219], a[212:215], a[80:83]\n" \
"s_add_u32 s85, s71, 0\n" \
"v_cvt_pk_bf16_f32 v142, v28, v29\n" \
"v_cvt_pk_bf16_f32 v143, v30, v31\n" \
"v_mfma_f32_16x16x32_bf16 a[84:87], v[220:223], a[212:215], a[84:87]\n" \
"s_add_u32 m0, s70, 58368\n" \
"buffer_store_dwordx4 v[128:131], v248, s[32:35], s85 offen offset:0 sc1\n" \
"v_mfma_f32_16x16x32_bf16 a[84:87], v[204:207], a[208:211], a[84:87]\n" \
"s_waitcnt lgkmcnt(8)\n" \
"s_add_u32 s74, s69, 3408000\n" \
"ds_read_b64_tr_b16 a[206:207], v241 offset:11264\n" \
"v_mfma_f32_16x16x32_bf16 a[24:27], v[192:195], a[216:219], a[24:27]\n" \
"s_lshl_b32 s81, s67, 3\n" \
"ds_read_b64_tr_b16 a[208:209], v240 offset:1536\n" \
"v_mfma_f32_16x16x32_bf16 a[24:27], v[208:211], a[220:223], a[24:27]\n" \
"s_add_u32 s81, s81, %[wid]\n" \
"buffer_load_dwordx4 v246, s[24:27], s74 offen lds\n" \
"v_mfma_f32_16x16x32_bf16 a[28:31], v[212:215], a[220:223], a[28:31]\n" \
"s_lshl_b32 s81, s81, 6\n" \
"ds_read_b64_tr_b16 a[210:211], v240 offset:3584\n" \
"v_mfma_f32_16x16x32_bf16 a[28:31], v[196:199], a[216:219], a[28:31]\n" \
"s_add_u32 s81, s81, 90112\n" \
"ds_write_b128 v244, v[140:143] offset:3072\n" \
"v_mfma_f32_16x16x32_bf16 a[88:91], v[200:203], a[216:219], a[88:91]\n" \
"buffer_load_dword v253, v252, s[32:35], s81 offen sc1\n" \
"v_mul_f32 v32, s84, v32\n" \
"v_mfma_f32_16x16x32_bf16 a[88:91], v[216:219], a[220:223], a[88:91]\n" \
"ds_read_b64_tr_b16 a[212:213], v240 offset:9728\n" \
"v_mul_f32 v33, s84, v33\n" \
"v_mfma_f32_16x16x32_bf16 a[92:95], v[220:223], a[220:223], a[92:95]\n" \
"ds_read_b64_tr_b16 a[214:215], v240 offset:11776\n" \
"v_mul_f32 v34, s84, v34\n" \
"v_mfma_f32_16x16x32_bf16 a[92:95], v[204:207], a[216:219], a[92:95]\n" \
"s_waitcnt lgkmcnt(9)\n" \
"v_mul_f32 v35, s84, v35\n" \
"v_mul_f32 v36, s84, v36\n" \
"v_mfma_f32_16x16x32_bf16 a[32:35], v[192:195], a[192:195], a[32:35]\n" \
"ds_read_b64_tr_b16 a[216:217], v241 offset:1536\n" \
"v_mul_f32 v37, s84, v37\n" \
"v_mfma_f32_16x16x32_bf16 a[32:35], v[208:211], a[196:199], a[32:35]\n" \
"v_mul_f32 v38, s84, v38\n" \
"v_mul_f32 v39, s84, v39\n" \
"v_mfma_f32_16x16x32_bf16 a[36:39], v[212:215], a[196:199], a[36:39]\n" \
"ds_read_b64_tr_b16 a[218:219], v241 offset:3584\n" \
"v_cvt_pk_bf16_f32 v144, v32, v33\n" \
"v_mfma_f32_16x16x32_bf16 a[36:39], v[196:199], a[192:195], a[36:39]\n" \
"ds_read_b64_tr_b16 a[220:221], v241 offset:9728\n" \
"v_cvt_pk_bf16_f32 v145, v34, v35\n" \
"v_mfma_f32_16x16x32_bf16 a[96:99], v[200:203], a[192:195], a[96:99]\n" \
"ds_read_b64_tr_b16 a[222:223], v241 offset:11776\n" \
"v_cvt_pk_bf16_f32 v146, v36, v37\n" \
"v_mfma_f32_16x16x32_bf16 a[96:99], v[216:219], a[196:199], a[96:99]\n" \
"v_cvt_pk_bf16_f32 v147, v38, v39\n" \
"v_mfma_f32_16x16x32_bf16 a[100:103], v[220:223], a[196:199], a[100:103]\n" \
"ds_write_b128 v244, v[144:147] offset:4096\n" \
"v_mfma_f32_16x16x32_bf16 a[100:103], v[204:207], a[192:195], a[100:103]\n" \
"s_waitcnt lgkmcnt(10)\n" \
"v_mfma_f32_16x16x32_bf16 a[40:43], v[192:195], a[200:203], a[40:43]\n" \
"v_mfma_f32_16x16x32_bf16 a[40:43], v[208:211], a[204:207], a[40:43]\n" \
"v_mfma_f32_16x16x32_bf16 a[44:47], v[212:215], a[204:207], a[44:47]\n" \
"v_mul_f32 v40, s84, v40\n" \
"v_mul_f32 v41, s84, v41\n" \
"v_mfma_f32_16x16x32_bf16 a[44:47], v[196:199], a[200:203], a[44:47]\n" \
"v_mul_f32 v42, s84, v42\n" \
"v_mul_f32 v43, s84, v43\n" \
"v_mfma_f32_16x16x32_bf16 a[104:107], v[200:203], a[200:203], a[104:107]\n" \
"s_waitcnt vmcnt(10) lgkmcnt(1)\n" \
"s_barrier\n" \
"v_mul_f32 v44, s84, v44\n" \
"v_mfma_f32_16x16x32_bf16 a[104:107], v[216:219], a[204:207], a[104:107]\n" \
"s_add_u32 m0, s70, 65536\n" \
"ds_read_b64_tr_b16 a[192:193], v240 offset:16384\n" \
"ds_read_b64_tr_b16 a[194:195], v240 offset:18432\n" \
"v_mfma_f32_16x16x32_bf16 a[108:111], v[220:223], a[204:207], a[108:111]\n" \
"s_add_u32 s74, s69, 0\n" \
"buffer_load_dwordx4 v246, s[28:31], s74 offen lds\n" \
"v_mfma_f32_16x16x32_bf16 a[108:111], v[204:207], a[200:203], a[108:111]\n" \
"ds_read_b64_tr_b16 a[196:197], v240 offset:24576\n" \
"ds_read_b64_tr_b16 a[198:199], v240 offset:26624\n" \
"v_mfma_f32_16x16x32_bf16 a[48:51], v[192:195], a[208:211], a[48:51]\n" \
"ds_read_b64_tr_b16 a[200:201], v241 offset:16384\n" \
"v_mul_f32 v45, s84, v45\n" \
"v_mfma_f32_16x16x32_bf16 a[48:51], v[208:211], a[212:215], a[48:51]\n" \
"v_mul_f32 v46, s84, v46\n" \
"v_mul_f32 v47, s84, v47\n" \
"v_mfma_f32_16x16x32_bf16 a[52:55], v[212:215], a[212:215], a[52:55]\n" \
"ds_read_b64_tr_b16 a[202:203], v241 offset:18432\n" \
"v_cvt_pk_bf16_f32 v148, v40, v41\n" \
"v_mfma_f32_16x16x32_bf16 a[52:55], v[196:199], a[208:211], a[52:55]\n" \
"s_add_u32 s85, s71, 0\n" \
"v_cvt_pk_bf16_f32 v149, v42, v43\n" \
"v_cvt_pk_bf16_f32 v150, v44, v45\n" \
"v_mfma_f32_16x16x32_bf16 a[112:115], v[200:203], a[208:211], a[112:115]\n" \
"buffer_store_dwordx4 v[132:135], v248, s[32:35], s85 offen offset:1024 sc1\n" \
"v_mfma_f32_16x16x32_bf16 a[112:115], v[216:219], a[212:215], a[112:115]\n" \
"ds_read_b64_tr_b16 a[204:205], v241 offset:24576\n" \
"v_cvt_pk_bf16_f32 v151, v46, v47\n" \
"v_mfma_f32_16x16x32_bf16 a[116:119], v[220:223], a[212:215], a[116:119]\n" \
"ds_read_b64_tr_b16 a[206:207], v241 offset:26624\n" \
"v_mfma_f32_16x16x32_bf16 a[116:119], v[204:207], a[208:211], a[116:119]\n" \
"ds_write_b128 v244, v[148:151] offset:5120\n" \
"v_mfma_f32_16x16x32_bf16 a[56:59], v[192:195], a[216:219], a[56:59]\n" \
"ds_read_b64_tr_b16 a[208:209], v240 offset:16896\n" \
"v_mul_f32 v48, s84, v48\n" \
"v_mfma_f32_16x16x32_bf16 a[56:59], v[208:211], a[220:223], a[56:59]\n" \
"v_mul_f32 v49, s84, v49\n" \
"v_mul_f32 v50, s84, v50\n" \
"v_mfma_f32_16x16x32_bf16 a[60:63], v[212:215], a[220:223], a[60:63]\n" \
"s_add_u32 m0, s70, 66560\n" \
"ds_read_b64_tr_b16 a[210:211], v240 offset:18944\n" \
"v_mul_f32 v51, s84, v51\n" \
"v_mfma_f32_16x16x32_bf16 a[60:63], v[196:199], a[216:219], a[60:63]\n" \
"s_add_u32 s74, s69, 128\n" \
"v_mul_f32 v52, s84, v52\n" \
"v_mul_f32 v53, s84, v53\n" \
"v_mfma_f32_16x16x32_bf16 a[120:123], v[200:203], a[216:219], a[120:123]\n" \
"buffer_load_dwordx4 v246, s[28:31], s74 offen lds\n" \
"v_mfma_f32_16x16x32_bf16 a[120:123], v[216:219], a[220:223], a[120:123]\n" \
"ds_read_b64_tr_b16 a[212:213], v240 offset:25088\n" \
"v_mul_f32 v54, s84, v54\n" \
"v_mfma_f32_16x16x32_bf16 a[124:127], v[220:223], a[220:223], a[124:127]\n" \
"ds_read_b64_tr_b16 a[214:215], v240 offset:27136\n" \
"v_mul_f32 v55, s84, v55\n" \
"v_mfma_f32_16x16x32_bf16 a[124:127], v[204:207], a[216:219], a[124:127]\n" \
"s_waitcnt lgkmcnt(9)\n" \
"v_cvt_pk_bf16_f32 v152, v48, v49\n" \
"v_cvt_pk_bf16_f32 v153, v50, v51\n" \
"v_mfma_f32_16x16x32_bf16 a[0:3], a[160:163], a[192:195], a[0:3]\n" \
"ds_read_b64_tr_b16 a[216:217], v241 offset:16896\n" \
"v_cvt_pk_bf16_f32 v154, v52, v53\n" \
"v_mfma_f32_16x16x32_bf16 a[0:3], a[176:179], a[196:199], a[0:3]\n" \
"v_cvt_pk_bf16_f32 v155, v54, v55\n" \
"v_mfma_f32_16x16x32_bf16 a[4:7], a[180:183], a[196:199], a[4:7]\n" \
"ds_read_b64_tr_b16 a[218:219], v241 offset:18944\n" \
"v_mfma_f32_16x16x32_bf16 a[4:7], a[164:167], a[192:195], a[4:7]\n" \
"ds_write_b128 v244, v[152:155] offset:6144\n" \
"v_mfma_f32_16x16x32_bf16 a[64:67], a[168:171], a[192:195], a[64:67]\n" \
"ds_read_b64_tr_b16 a[220:221], v241 offset:25088\n" \
"v_mfma_f32_16x16x32_bf16 a[64:67], a[184:187], a[196:199], a[64:67]\n" \
"v_mfma_f32_16x16x32_bf16 a[68:71], a[188:191], a[196:199], a[68:71]\n" \
"ds_read_b64_tr_b16 a[222:223], v241 offset:27136\n" \
"v_mul_f32 v56, s84, v56\n" \
"v_mfma_f32_16x16x32_bf16 a[68:71], a[172:175], a[192:195], a[68:71]\n" \
"s_waitcnt lgkmcnt(10)\n" \
"v_mul_f32 v57, s84, v57\n" \
"v_mul_f32 v58, s84, v58\n" \
"v_mfma_f32_16x16x32_bf16 a[8:11], a[160:163], a[200:203], a[8:11]\n" \
"s_add_u32 m0, s70, 73728\n" \
"ds_read_b64_tr_b16 a[192:193], v240 offset:17408\n" \
"v_mul_f32 v59, s84, v59\n" \
"v_mfma_f32_16x16x32_bf16 a[8:11], a[176:179], a[204:207], a[8:11]\n" \
"s_add_u32 s74, s69, 262144\n" \
"v_mul_f32 v60, s84, v60\n" \
"v_mul_f32 v61, s84, v61\n" \
"v_mfma_f32_16x16x32_bf16 a[12:15], a[180:183], a[204:207], a[12:15]\n" \
"buffer_load_dwordx4 v246, s[28:31], s74 offen lds\n" \
"v_mfma_f32_16x16x32_bf16 a[12:15], a[164:167], a[200:203], a[12:15]\n" \
"ds_read_b64_tr_b16 a[194:195], v240 offset:19456\n" \
"v_mul_f32 v62, s84, v62\n" \
"v_mfma_f32_16x16x32_bf16 a[72:75], a[168:171], a[200:203], a[72:75]\n" \
"ds_read_b64_tr_b16 a[196:197], v240 offset:25600\n" \
"v_mul_f32 v63, s84, v63\n" \
"v_mfma_f32_16x16x32_bf16 a[72:75], a[184:187], a[204:207], a[72:75]\n" \
"v_cvt_pk_bf16_f32 v156, v56, v57\n" \
"v_cvt_pk_bf16_f32 v157, v58, v59\n" \
"v_mfma_f32_16x16x32_bf16 a[76:79], a[188:191], a[204:207], a[76:79]\n" \
"ds_read_b64_tr_b16 a[198:199], v240 offset:27648\n" \
"v_cvt_pk_bf16_f32 v158, v60, v61\n" \
"v_mfma_f32_16x16x32_bf16 a[76:79], a[172:175], a[200:203], a[76:79]\n" \
"s_waitcnt lgkmcnt(9)\n" \
"v_cvt_pk_bf16_f32 v159, v62, v63\n" \
"v_mfma_f32_16x16x32_bf16 a[16:19], a[160:163], a[208:211], a[16:19]\n" \
"ds_read_b64_tr_b16 a[200:201], v241 offset:17408\n" \
"v_mfma_f32_16x16x32_bf16 a[16:19], a[176:179], a[212:215], a[16:19]\n" \
"s_add_u32 s85, s71, 0\n" \
"ds_write_b128 v244, v[156:159] offset:7168\n" \
"v_mfma_f32_16x16x32_bf16 a[20:23], a[180:183], a[212:215], a[20:23]\n" \
"buffer_store_dwordx4 v[136:139], v248, s[32:35], s85 offen offset:2048 sc1\n" \
"v_mfma_f32_16x16x32_bf16 a[20:23], a[164:167], a[208:211], a[20:23]\n" \
"ds_read_b64_tr_b16 a[202:203], v241 offset:19456\n" \
"v_mfma_f32_16x16x32_bf16 a[80:83], a[168:171], a[208:211], a[80:83]\n" \
"s_waitcnt vmcnt(5)\n" \
"ds_read_b64_tr_b16 a[204:205], v241 offset:25600\n" \
"v_cmp_eq_u32 vcc, 0, v253\n" \
"v_mfma_f32_16x16x32_bf16 a[80:83], a[184:187], a[212:215], a[80:83]\n" \
"v_mul_f32 v64, s84, v64\n" \
"v_mul_f32 v65, s84, v65\n" \
"v_mfma_f32_16x16x32_bf16 a[84:87], a[188:191], a[212:215], a[84:87]\n" \
"s_add_u32 m0, s70, 74752\n" \
"ds_read_b64_tr_b16 a[206:207], v241 offset:27648\n" \
"v_mul_f32 v66, s84, v66\n" \
"v_mfma_f32_16x16x32_bf16 a[84:87], a[172:175], a[208:211], a[84:87]\n" \
"s_waitcnt lgkmcnt(9)\n" \
"s_cbranch_vccnz .Lsph_%=\n" \
".Lsbh_%=:\n" \
"v_mfma_f32_16x16x32_bf16 a[24:27], a[160:163], a[216:219], a[24:27]\n" \
"s_add_u32 s74, s69, 262272\n" \
"buffer_load_dwordx4 v246, s[28:31], s74 offen lds\n" \
"v_mfma_f32_16x16x32_bf16 a[24:27], a[176:179], a[220:223], a[24:27]\n" \
"ds_read_b64_tr_b16 a[208:209], v240 offset:17920\n" \
"v_mul_f32 v67, s84, v67\n" \
"v_mfma_f32_16x16x32_bf16 a[28:31], a[180:183], a[220:223], a[28:31]\n" \
"buffer_load_dwordx4 v[192:195], v248, s[32:35], s79 offen offset:0 sc1\n" \
"v_mul_f32 v68, s84, v68\n" \
"v_mfma_f32_16x16x32_bf16 a[28:31], a[164:167], a[216:219], a[28:31]\n" \
"ds_read_b64_tr_b16 a[210:211], v240 offset:19968\n" \
"v_mul_f32 v69, s84, v69\n" \
"v_mfma_f32_16x16x32_bf16 a[88:91], a[168:171], a[216:219], a[88:91]\n" \
"ds_read_b64_tr_b16 a[212:213], v240 offset:26112\n" \
"v_mul_f32 v70, s84, v70\n" \
"v_mfma_f32_16x16x32_bf16 a[88:91], a[184:187], a[220:223], a[88:91]\n" \
"v_mul_f32 v71, s84, v71\n" \
"v_cvt_pk_bf16_f32 v160, v64, v65\n" \
"v_mfma_f32_16x16x32_bf16 a[92:95], a[188:191], a[220:223], a[92:95]\n" \
"ds_read_b64_tr_b16 a[214:215], v240 offset:28160\n" \
"v_cvt_pk_bf16_f32 v161, v66, v67\n" \
"v_mfma_f32_16x16x32_bf16 a[92:95], a[172:175], a[216:219], a[92:95]\n" \
"s_waitcnt lgkmcnt(9)\n" \
"v_cvt_pk_bf16_f32 v162, v68, v69\n" \
"v_cvt_pk_bf16_f32 v163, v70, v71\n" \
"v_mfma_f32_16x16x32_bf16 a[32:35], a[160:163], a[192:195], a[32:35]\n" \
"ds_read_b64_tr_b16 a[216:217], v241 offset:17920\n" \
"v_xor_b32 v244, 0x4000, v244\n" \
"v_mfma_f32_16x16x32_bf16 a[32:35], a[176:179], a[196:199], a[32:35]\n" \
"ds_write_b128 v244, v[160:163] offset:32768\n" \
"v_mfma_f32_16x16x32_bf16 a[36:39], a[180:183], a[196:199], a[36:39]\n" \
"buffer_load_dwordx4 v[196:199], v248, s[32:35], s80 offen offset:0 sc1\n" \
"v_mul_f32 v72, s84, v72\n" \
"v_mfma_f32_16x16x32_bf16 a[36:39], a[164:167], a[192:195], a[36:39]\n" \
"ds_read_b64_tr_b16 a[218:219], v241 offset:19968\n" \
"ds_read_b64_tr_b16 a[220:221], v241 offset:26112\n" \
"v_mfma_f32_16x16x32_bf16 a[96:99], a[168:171], a[192:195], a[96:99]\n" \
"ds_read_b64_tr_b16 a[222:223], v241 offset:28160\n" \
"v_mul_f32 v73, s84, v73\n" \
"v_mfma_f32_16x16x32_bf16 a[96:99], a[184:187], a[196:199], a[96:99]\n" \
"v_mul_f32 v74, s84, v74\n" \
"v_mul_f32 v75, s84, v75\n" \
"v_mfma_f32_16x16x32_bf16 a[100:103], a[188:191], a[196:199], a[100:103]\n" \
"v_mul_f32 v76, s84, v76\n" \
"v_mul_f32 v77, s84, v77\n" \
"v_mfma_f32_16x16x32_bf16 a[100:103], a[172:175], a[192:195], a[100:103]\n" \
"s_waitcnt lgkmcnt(9)\n" \
"v_mul_f32 v78, s84, v78\n" \
"v_mul_f32 v79, s84, v79\n" \
"v_mfma_f32_16x16x32_bf16 a[40:43], a[160:163], a[200:203], a[40:43]\n" \
"v_cvt_pk_bf16_f32 v164, v72, v73\n" \
"v_cvt_pk_bf16_f32 v165, v74, v75\n" \
"v_mfma_f32_16x16x32_bf16 a[40:43], a[176:179], a[204:207], a[40:43]\n" \
"v_cvt_pk_bf16_f32 v166, v76, v77\n" \
"v_cvt_pk_bf16_f32 v167, v78, v79\n" \
"v_mfma_f32_16x16x32_bf16 a[44:47], a[180:183], a[204:207], a[44:47]\n" \
"buffer_load_dwordx4 v[200:203], v248, s[32:35], s36 offen offset:0 sc1\n" \
"v_mfma_f32_16x16x32_bf16 a[44:47], a[164:167], a[200:203], a[44:47]\n" \
"ds_write_b128 v244, v[164:167] offset:33792\n" \
"v_mfma_f32_16x16x32_bf16 a[104:107], a[168:171], a[200:203], a[104:107]\n" \
"s_waitcnt lgkmcnt(1)\n" \
"s_barrier\n" \
"s_add_u32 m0, s70, 81920\n" \
"v_mfma_f32_16x16x32_bf16 a[104:107], a[184:187], a[204:207], a[104:107]\n" \
"s_add_u32 s74, s69, 524288\n" \
"ds_read_b64_tr_b16 a[192:193], v240 offset:32768\n" \
"ds_read_b64_tr_b16 a[194:195], v240 offset:34816\n" \
"v_mfma_f32_16x16x32_bf16 a[108:111], a[188:191], a[204:207], a[108:111]\n" \
"buffer_load_dwordx4 v246, s[28:31], s74 offen lds\n" \
"v_mfma_f32_16x16x32_bf16 a[108:111], a[172:175], a[200:203], a[108:111]\n" \
"s_add_u32 s85, s71, 0\n" \
"ds_read_b64_tr_b16 a[196:197], v240 offset:40960\n" \
"ds_read_b64_tr_b16 a[198:199], v240 offset:43008\n" \
"v_mfma_f32_16x16x32_bf16 a[48:51], a[160:163], a[208:211], a[48:51]\n" \
"ds_read_b64_tr_b16 a[200:201], v241 offset:32768\n" \
"v_mul_f32 v80, s84, v80\n" \
"v_mfma_f32_16x16x32_bf16 a[48:51], a[176:179], a[212:215], a[48:51]\n" \
"buffer_store_dwordx4 v[140:143], v248, s[32:35], s85 offen offset:3072 sc1\n" \
"v_mfma_f32_16x16x32_bf16 a[52:55], a[180:183], a[212:215], a[52:55]\n" \
"buffer_load_dwordx4 v[204:207], v248, s[32:35], s37 offen offset:0 sc1\n" \
"v_mul_f32 v81, s84, v81\n" \
"v_mfma_f32_16x16x32_bf16 a[52:55], a[164:167], a[208:211], a[52:55]\n" \
"ds_read_b64_tr_b16 a[202:203], v241 offset:34816\n" \
"v_mul_f32 v82, s84, v82\n" \
"v_mfma_f32_16x16x32_bf16 a[112:115], a[168:171], a[208:211], a[112:115]\n" \
"ds_read_b64_tr_b16 a[204:205], v241 offset:40960\n" \
"v_mul_f32 v83, s84, v83\n" \
"v_mfma_f32_16x16x32_bf16 a[112:115], a[184:187], a[212:215], a[112:115]\n" \
"v_mul_f32 v84, s84, v84\n" \
"v_mul_f32 v85, s84, v85\n" \
"v_mfma_f32_16x16x32_bf16 a[116:119], a[188:191], a[212:215], a[116:119]\n" \
"ds_read_b64_tr_b16 a[206:207], v241 offset:43008\n" \
"v_mul_f32 v86, s84, v86\n" \
"v_mfma_f32_16x16x32_bf16 a[116:119], a[172:175], a[208:211], a[116:119]\n" \
"v_mul_f32 v87, s84, v87\n" \
"v_cvt_pk_bf16_f32 v168, v80, v81\n" \
"v_mfma_f32_16x16x32_bf16 a[56:59], a[160:163], a[216:219], a[56:59]\n" \
"ds_read_b64_tr_b16 a[208:209], v240 offset:33280\n" \
"v_cvt_pk_bf16_f32 v169, v82, v83\n" \
"v_mfma_f32_16x16x32_bf16 a[56:59], a[176:179], a[220:223], a[56:59]\n" \
"v_cvt_pk_bf16_f32 v170, v84, v85\n" \
"v_cvt_pk_bf16_f32 v171, v86, v87\n" \
"v_mfma_f32_16x16x32_bf16 a[60:63], a[180:183], a[220:223], a[60:63]\n" \
"s_add_u32 m0, s70, 82944\n" \
"ds_read_b64_tr_b16 a[210:211], v240 offset:35328\n" \
"v_mfma_f32_16x16x32_bf16 a[60:63], a[164:167], a[216:219], a[60:63]\n" \
"s_add_u32 s74, s69, 524416\n" \
"ds_write_b128 v244, v[168:171] offset:34816\n" \
"v_mfma_f32_16x16x32_bf16 a[120:123], a[168:171], a[216:219], a[120:123]\n" \
"buffer_load_dwordx4 v246, s[28:31], s74 offen lds\n" \
"v_mfma_f32_16x16x32_bf16 a[120:123], a[184:187], a[220:223], a[120:123]\n" \
"ds_read_b64_tr_b16 a[212:213], v240 offset:41472\n" \
"v_mfma_f32_16x16x32_bf16 a[124:127], a[188:191], a[220:223], a[124:127]\n" \
"ds_read_b64_tr_b16 a[214:215], v240 offset:43520\n" \
"v_mul_f32 v88, s84, v88\n" \
"v_mfma_f32_16x16x32_bf16 a[124:127], a[172:175], a[216:219], a[124:127]\n" \
"s_waitcnt lgkmcnt(9)\n" \
"v_mul_f32 v89, s84, v89\n" \
"v_mul_f32 v90, s84, v90\n" \
"v_mfma_f32_16x16x32_bf16 a[0:3], a[224:227], a[192:195], a[0:3]\n" \
"ds_read_b64_tr_b16 a[216:217], v241 offset:33280\n" \
"v_mul_f32 v91, s84, v91\n" \
"v_mfma_f32_16x16x32_bf16 a[0:3], a[240:243], a[196:199], a[0:3]\n" \
"buffer_load_dwordx4 a[160:163], v248, s[32:35], s79 offen offset:1024 sc1\n" \
"v_mul_f32 v92, s84, v92\n" \
"v_mfma_f32_16x16x32_bf16 a[4:7], a[244:247], a[196:199], a[4:7]\n" \
"ds_read_b64_tr_b16 a[218:219], v241 offset:35328\n" \
"v_mul_f32 v93, s84, v93\n" \
"v_mfma_f32_16x16x32_bf16 a[4:7], a[228:231], a[192:195], a[4:7]\n" \
"v_mul_f32 v94, s84, v94\n" \
"v_mul_f32 v95, s84, v95\n" \
"v_mfma_f32_16x16x32_bf16 a[64:67], a[232:235], a[192:195], a[64:67]\n" \
"ds_read_b64_tr_b16 a[220:221], v241 offset:41472\n" \
"v_cvt_pk_bf16_f32 v172, v88, v89\n" \
"v_mfma_f32_16x16x32_bf16 a[64:67], a[248:251], a[196:199], a[64:67]\n" \
"v_cvt_pk_bf16_f32 v173, v90, v91\n" \
"v_cvt_pk_bf16_f32 v174, v92, v93\n" \
"v_mfma_f32_16x16x32_bf16 a[68:71], a[252:255], a[196:199], a[68:71]\n" \
"ds_read_b64_tr_b16 a[222:223], v241 offset:43520\n" \
"v_cvt_pk_bf16_f32 v175, v94, v95\n" \
"v_mfma_f32_16x16x32_bf16 a[68:71], a[236:239], a[192:195], a[68:71]\n" \
"s_waitcnt lgkmcnt(9)\n" \
"ds_write_b128 v244, v[172:175] offset:35840\n" \
"v_mfma_f32_16x16x32_bf16 a[8:11], a[224:227], a[200:203], a[8:11]\n" \
"ds_read_b64_tr_b16 a[192:193], v240 offset:33792\n" \
"v_mfma_f32_16x16x32_bf16 a[8:11], a[240:243], a[204:207], a[8:11]\n" \
"buffer_load_dwordx4 a[164:167], v248, s[32:35], s80 offen offset:1024 sc1\n" \
"v_mfma_f32_16x16x32_bf16 a[12:15], a[244:247], a[204:207], a[12:15]\n" \
"s_add_u32 m0, s70, 90112\n" \
"s_add_u32 s74, s69, 786432\n" \
"ds_read_b64_tr_b16 a[194:195], v240 offset:35840\n" \
"v_mfma_f32_16x16x32_bf16 a[12:15], a[228:231], a[200:203], a[12:15]\n" \
"v_mfma_f32_16x16x32_bf16 a[72:75], a[232:235], a[200:203], a[72:75]\n" \
"buffer_load_dwordx4 v246, s[28:31], s74 offen lds\n" \
"v_mfma_f32_16x16x32_bf16 a[72:75], a[248:251], a[204:207], a[72:75]\n" \
"s_add_u32 s85, s71, 4096\n" \
"ds_read_b64_tr_b16 a[196:197], v240 offset:41984\n" \
"v_mul_f32 v96, s84, v96\n" \
"v_mfma_f32_16x16x32_bf16 a[76:79], a[252:255], a[204:207], a[76:79]\n" \
"ds_read_b64_tr_b16 a[198:199], v240 offset:44032\n" \
"v_mul_f32 v97, s84, v97\n" \
"v_mfma_f32_16x16x32_bf16 a[76:79], a[236:239], a[200:203], a[76:79]\n" \
"s_waitcnt lgkmcnt(9)\n" \
"buffer_store_dwordx4 v[144:147], v248, s[32:35], s85 offen offset:0 sc1\n" \
"v_mfma_f32_16x16x32_bf16 a[16:19], a[224:227], a[208:211], a[16:19]\n" \
"ds_read_b64_tr_b16 a[200:201], v241 offset:33792\n" \
"v_mul_f32 v98, s84, v98\n" \
"v_mfma_f32_16x16x32_bf16 a[16:19], a[240:243], a[212:215], a[16:19]\n" \
".Lst0_4_%=:\n" \
"s_cbranch_execz .Lst0_8_%=\n" \
"buffer_load_dwordx4 a[168:171], v248, s[32:35], s36 offen offset:1024 sc1\n" \
"v_mul_f32 v99, s84, v99\n" \
"v_mfma_f32_16x16x32_bf16 a[20:23], a[244:247], a[212:215], a[20:23]\n" \
"ds_read_b64_tr_b16 a[202:203], v241 offset:35840\n" \
"v_mul_f32 v100, s84, v100\n" \
"v_mfma_f32_16x16x32_bf16 a[20:23], a[228:231], a[208:211], a[20:23]\n" \
"v_mul_f32 v101, s84, v101\n" \
"v_mul_f32 v102, s84, v102\n" \
"v_mfma_f32_16x16x32_bf16 a[80:83], a[232:235], a[208:211], a[80:83]\n" \
"ds_read_b64_tr_b16 a[204:205], v241 offset:41984\n" \
"v_mul_f32 v103, s84, v103\n" \
"v_mfma_f32_16x16x32_bf16 a[80:83], a[248:251], a[212:215], a[80:83]\n" \
"v_cvt_pk_bf16_f32 v176, v96, v97\n" \
"v_cvt_pk_bf16_f32 v177, v98, v99\n" \
"v_mfma_f32_16x16x32_bf16 a[84:87], a[252:255], a[212:215], a[84:87]\n" \
"s_add_u32 m0, s70, 91136\n" \
"ds_read_b64_tr_b16 a[206:207], v241 offset:44032\n" \
"v_cvt_pk_bf16_f32 v178, v100, v101\n" \
"v_mfma_f32_16x16x32_bf16 a[84:87], a[236:239], a[208:211], a[84:87]\n" \
"s_waitcnt lgkmcnt(9)\n" \
"s_add_u32 s74, s69, 786560\n" \
"v_cvt_pk_bf16_f32 v179, v102, v103\n" \
"v_mfma_f32_16x16x32_bf16 a[24:27], a[224:227], a[216:219], a[24:27]\n" \
"buffer_load_dwordx4 v246, s[28:31], s74 offen lds\n" \
"v_mfma_f32_16x16x32_bf16 a[24:27], a[240:243], a[220:223], a[24:27]\n" \
"buffer_load_dwordx4 a[172:175], v248, s[32:35], s37 offen offset:1024 sc1\n" \
"v_mfma_f32_16x16x32_bf16 a[28:31], a[244:247], a[220:223], a[28:31]\n" \
"buffer_load_dwordx4 a[176:179], v248, s[32:35], s79 offen offset:2048 sc1\n" \
"v_mfma_f32_16x16x32_bf16 a[28:31], a[228:231], a[216:219], a[28:31]\n" \
"ds_read_b64_tr_b16 a[208:209], v240 offset:34304\n" \
"ds_read_b64_tr_b16 a[210:211], v240 offset:36352\n" \
"v_mfma_f32_16x16x32_bf16 a[88:91], a[232:235], a[216:219], a[88:91]\n" \
"ds_read_b64_tr_b16 a[212:213], v240 offset:42496\n" \
"v_mfma_f32_16x16x32_bf16 a[88:91], a[248:251], a[220:223], a[88:91]\n" \
"ds_write_b128 v244, v[176:179] offset:36864\n" \
"v_mul_f32 v104, s84, v104\n" \
"v_mfma_f32_16x16x32_bf16 a[92:95], a[252:255], a[220:223], a[92:95]\n" \
"ds_read_b64_tr_b16 a[214:215], v240 offset:44544\n" \
"v_mul_f32 v105, s84, v105\n" \
"v_mfma_f32_16x16x32_bf16 a[92:95], a[236:239], a[216:219], a[92:95]\n" \
"s_waitcnt lgkmcnt(9)\n" \
"v_mul_f32 v106, s84, v106\n" \
"v_mul_f32 v107, s84, v107\n" \
"v_mfma_f32_16x16x32_bf16 a[32:35], a[224:227], a[192:195], a[32:35]\n" \
"ds_read_b64_tr_b16 a[216:217], v241 offset:34304\n" \
"v_mul_f32 v108, s84, v108\n" \
"v_mfma_f32_16x16x32_bf16 a[32:35], a[240:243], a[196:199], a[32:35]\n" \
"v_mul_f32 v109, s84, v109\n" \
"v_mul_f32 v110, s84, v110\n" \
"v_mfma_f32_16x16x32_bf16 a[36:39], a[244:247], a[196:199], a[36:39]\n" \
"buffer_load_dwordx4 a[180:183], v248, s[32:35], s80 offen offset:2048 sc1\n" \
"v_mul_f32 v111, s84, v111\n" \
"v_mfma_f32_16x16x32_bf16 a[36:39], a[228:231], a[192:195], a[36:39]\n" \
"ds_read_b64_tr_b16 a[218:219], v241 offset:36352\n" \
"ds_read_b64_tr_b16 a[220:221], v241 offset:42496\n" \
"v_mfma_f32_16x16x32_bf16 a[96:99], a[232:235], a[192:195], a[96:99]\n" \
"ds_read_b64_tr_b16 a[222:223], v241 offset:44544\n" \
"v_cvt_pk_bf16_f32 v180, v104, v105\n" \
"v_mfma_f32_16x16x32_bf16 a[96:99], a[248:251], a[196:199], a[96:99]\n" \
"v_cvt_pk_bf16_f32 v181, v106, v107\n" \
"v_cvt_pk_bf16_f32 v182, v108, v109\n" \
"v_mfma_f32_16x16x32_bf16 a[100:103], a[252:255], a[196:199], a[100:103]\n" \
"v_cvt_pk_bf16_f32 v183, v110, v111\n" \
"v_mfma_f32_16x16x32_bf16 a[100:103], a[236:239], a[192:195], a[100:103]\n" \
"s_waitcnt lgkmcnt(9)\n" \
"ds_write_b128 v244, v[180:183] offset:37888\n" \
"v_mfma_f32_16x16x32_bf16 a[40:43], a[224:227], a[200:203], a[40:43]\n" \
"v_mul_f32 v112, s84, v112\n" \
"v_mul_f32 v113, s84, v113\n" \
"v_mfma_f32_16x16x32_bf16 a[40:43], a[240:243], a[204:207], a[40:43]\n" \
"v_mul_f32 v114, s84, v114\n" \
"v_mul_f32 v115, s84, v115\n" \
"v_mfma_f32_16x16x32_bf16 a[44:47], a[244:247], a[204:207], a[44:47]\n" \
"buffer_load_dwordx4 a[184:187], v248, s[32:35], s36 offen offset:2048 sc1\n" \
"v_mul_f32 v116, s84, v116\n" \
"v_mfma_f32_16x16x32_bf16 a[44:47], a[228:231], a[200:203], a[44:47]\n" \
"s_add_u32 s85, s71, 4096\n" \
"v_mul_f32 v117, s84, v117\n" \
"v_mul_f32 v118, s84, v118\n" \
"v_mfma_f32_16x16x32_bf16 a[104:107], a[232:235], a[200:203], a[104:107]\n" \
"s_waitcnt lgkmcnt(1)\n" \
"s_barrier\n" \
"v_mul_f32 v119, s84, v119\n" \
"v_mfma_f32_16x16x32_bf16 a[104:107], a[248:251], a[204:207], a[104:107]\n" \
"s_add_u32 m0, s70, 32768\n" \
"buffer_store_dwordx4 v[148:151], v248, s[32:35], s85 offen offset:1024 sc1\n" \
"v_mfma_f32_16x16x32_bf16 a[108:111], a[252:255], a[204:207], a[108:111]\n" \
"s_add_u32 s74, s69, 1048576\n" \
"ds_read_b64_tr_b16 a[192:193], v240 offset:49152\n" \
"ds_read_b64_tr_b16 a[194:195], v240 offset:51200\n" \
"v_mfma_f32_16x16x32_bf16 a[108:111], a[236:239], a[200:203], a[108:111]\n" \
"ds_read_b64_tr_b16 a[196:197], v240 offset:57344\n" \
"ds_read_b64_tr_b16 a[198:199], v240 offset:59392\n" \
"v_mfma_f32_16x16x32_bf16 a[48:51], a[224:227], a[208:211], a[48:51]\n" \
"buffer_load_dwordx4 v246, s[28:31], s74 offen lds\n" \
"v_mfma_f32_16x16x32_bf16 a[48:51], a[240:243], a[212:215], a[48:51]\n" \
"ds_read_b64_tr_b16 a[200:201], v241 offset:49152\n" \
"v_cvt_pk_bf16_f32 v184, v112, v113\n" \
"v_mfma_f32_16x16x32_bf16 a[52:55], a[244:247], a[212:215], a[52:55]\n" \
"buffer_load_dwordx4 a[188:191], v248, s[32:35], s37 offen offset:2048 sc1\n" \
"v_cvt_pk_bf16_f32 v185, v114, v115\n" \
"v_mfma_f32_16x16x32_bf16 a[52:55], a[228:231], a[208:211], a[52:55]\n" \
"ds_read_b64_tr_b16 a[202:203], v241 offset:51200\n" \
"v_cvt_pk_bf16_f32 v186, v116, v117\n" \
"v_mfma_f32_16x16x32_bf16 a[112:115], a[232:235], a[208:211], a[112:115]\n" \
"ds_read_b64_tr_b16 a[204:205], v241 offset:57344\n" \
"v_cvt_pk_bf16_f32 v187, v118, v119\n" \
"v_mfma_f32_16x16x32_bf16 a[112:115], a[248:251], a[212:215], a[112:115]\n" \
"ds_write_b128 v244, v[184:187] offset:38912\n" \
"v_mfma_f32_16x16x32_bf16 a[116:119], a[252:255], a[212:215], a[116:119]\n" \
"ds_read_b64_tr_b16 a[206:207], v241 offset:59392\n" \
"v_mul_f32 v120, s84, v120\n" \
"v_mfma_f32_16x16x32_bf16 a[116:119], a[236:239], a[208:211], a[116:119]\n" \
"v_mul_f32 v121, s84, v121\n" \
"v_mul_f32 v122, s84, v122\n" \
"v_mfma_f32_16x16x32_bf16 a[56:59], a[224:227], a[216:219], a[56:59]\n" \
"ds_read_b64_tr_b16 a[208:209], v240 offset:49664\n" \
"v_mul_f32 v123, s84, v123\n" \
"v_mfma_f32_16x16x32_bf16 a[56:59], a[240:243], a[220:223], a[56:59]\n" \
"v_mul_f32 v124, s84, v124\n" \
"v_mul_f32 v125, s84, v125\n" \
"v_mfma_f32_16x16x32_bf16 a[60:63], a[244:247], a[220:223], a[60:63]\n" \
"s_add_u32 m0, s70, 33792\n" \
"ds_read_b64_tr_b16 a[210:211], v240 offset:51712\n" \
"v_mul_f32 v126, s84, v126\n" \
"v_mfma_f32_16x16x32_bf16 a[60:63], a[228:231], a[216:219], a[60:63]\n" \
"s_add_u32 s74, s69, 1048704\n" \
"v_mul_f32 v127, s84, v127\n" \
"v_cvt_pk_bf16_f32 v188, v120, v121\n" \
"v_mfma_f32_16x16x32_bf16 a[120:123], a[232:235], a[216:219], a[120:123]\n" \
"buffer_load_dwordx4 v246, s[28:31], s74 offen lds\n" \
"v_mfma_f32_16x16x32_bf16 a[120:123], a[248:251], a[220:223], a[120:123]\n" \
"ds_read_b64_tr_b16 a[212:213], v240 offset:57856\n" \
"v_cvt_pk_bf16_f32 v189, v122, v123\n" \
"v_mfma_f32_16x16x32_bf16 a[124:127], a[252:255], a[220:223], a[124:127]\n" \
"ds_read_b64_tr_b16 a[214:215], v240 offset:59904\n" \
"v_cvt_pk_bf16_f32 v190, v124, v125\n" \
"v_mfma_f32_16x16x32_bf16 a[124:127], a[236:239], a[216:219], a[124:127]\n" \
"s_waitcnt lgkmcnt(9)\n" \
"v_cvt_pk_bf16_f32 v191, v126, v127\n" \
"v_mfma_f32_16x16x32_bf16 a[0:3], a[128:131], a[192:195], a[0:3]\n" \
"ds_read_b64_tr_b16 a[216:217], v241 offset:49664\n" \
"v_mfma_f32_16x16x32_bf16 a[0:3], a[144:147], a[196:199], a[0:3]\n" \
"buffer_load_dwordx4 a[224:227], v248, s[32:35], s79 offen offset:3072 sc1\n" \
"v_mfma_f32_16x16x32_bf16 a[4:7], a[148:151], a[196:199], a[4:7]\n" \
"ds_read_b64_tr_b16 a[218:219], v241 offset:51712\n" \
"v_mfma_f32_16x16x32_bf16 a[4:7], a[132:135], a[192:195], a[4:7]\n" \
"ds_write_b128 v244, v[188:191] offset:39936\n" \
"v_mfma_f32_16x16x32_bf16 a[64:67], a[136:139], a[192:195], a[64:67]\n" \
"ds_read_b64_tr_b16 a[220:221], v241 offset:57856\n" \
"v_mfma_f32_16x16x32_bf16 a[64:67], a[152:155], a[196:199], a[64:67]\n" \
"v_mfma_f32_16x16x32_bf16 a[68:71], a[156:159], a[196:199], a[68:71]\n" \
"ds_read_b64_tr_b16 a[222:223], v241 offset:59904\n" \
"v_mfma_f32_16x16x32_bf16 a[68:71], a[140:143], a[192:195], a[68:71]\n" \
"s_waitcnt lgkmcnt(9)\n" \
"v_mfma_f32_16x16x32_bf16 a[8:11], a[128:131], a[200:203], a[8:11]\n" \
"ds_read_b64_tr_b16 a[192:193], v240 offset:50176\n" \
"v_mfma_f32_16x16x32_bf16 a[8:11], a[144:147], a[204:207], a[8:11]\n" \
"s_add_u32 s85, s71, 4096\n" \
"buffer_load_dwordx4 a[228:231], v248, s[32:35], s80 offen offset:3072 sc1\n" \
"v_mfma_f32_16x16x32_bf16 a[12:15], a[148:151], a[204:207], a[12:15]\n" \
"s_add_u32 m0, s70, 40960\n" \
"s_add_u32 s74, s69, 1310720\n" \
"ds_read_b64_tr_b16 a[194:195], v240 offset:52224\n" \
"v_mfma_f32_16x16x32_bf16 a[12:15], a[132:135], a[200:203], a[12:15]\n" \
"v_mfma_f32_16x16x32_bf16 a[72:75], a[136:139], a[200:203], a[72:75]\n" \
"buffer_load_dwordx4 v246, s[28:31], s74 offen lds\n" \
"v_mfma_f32_16x16x32_bf16 a[72:75], a[152:155], a[204:207], a[72:75]\n" \
"ds_read_b64_tr_b16 a[196:197], v240 offset:58368\n" \
"v_mfma_f32_16x16x32_bf16 a[76:79], a[156:159], a[204:207], a[76:79]\n" \
"ds_read_b64_tr_b16 a[198:199], v240 offset:60416\n" \
"v_mfma_f32_16x16x32_bf16 a[76:79], a[140:143], a[200:203], a[76:79]\n" \
"s_waitcnt lgkmcnt(9)\n" \
"buffer_store_dwordx4 v[152:155], v248, s[32:35], s85 offen offset:2048 sc1\n" \
"v_mfma_f32_16x16x32_bf16 a[16:19], a[128:131], a[208:211], a[16:19]\n" \
"ds_read_b64_tr_b16 a[200:201], v241 offset:50176\n" \
"v_mfma_f32_16x16x32_bf16 a[16:19], a[144:147], a[212:215], a[16:19]\n" \
"buffer_load_dwordx4 a[232:235], v248, s[32:35], s36 offen offset:3072 sc1\n" \
"v_mfma_f32_16x16x32_bf16 a[20:23], a[148:151], a[212:215], a[20:23]\n" \
"ds_read_b64_tr_b16 a[202:203], v241 offset:52224\n" \
"v_mfma_f32_16x16x32_bf16 a[20:23], a[132:135], a[208:211], a[20:23]\n" \
"v_mfma_f32_16x16x32_bf16 a[80:83], a[136:139], a[208:211], a[80:83]\n" \
"ds_read_b64_tr_b16 a[204:205], v241 offset:58368\n" \
"v_mfma_f32_16x16x32_bf16 a[80:83], a[152:155], a[212:215], a[80:83]\n" \
"v_mfma_f32_16x16x32_bf16 a[84:87], a[156:159], a[212:215], a[84:87]\n" \
"s_add_u32 m0, s70, 41984\n" \
"ds_read_b64_tr_b16 a[206:207], v241 offset:60416\n" \
"v_mfma_f32_16x16x32_bf16 a[84:87], a[140:143], a[208:211], a[84:87]\n" \
"s_waitcnt lgkmcnt(8)\n" \
"s_add_u32 s74, s69, 1310848\n" \
"v_mfma_f32_16x16x32_bf16 a[24:27], a[128:131], a[216:219], a[24:27]\n" \
"buffer_load_dwordx4 v246, s[28:31], s74 offen lds\n" \
"v_mfma_f32_16x16x32_bf16 a[24:27], a[144:147], a[220:223], a[24:27]\n" \
"buffer_load_dwordx4 a[236:239], v248, s[32:35], s37 offen offset:3072 sc1\n" \
"v_mfma_f32_16x16x32_bf16 a[28:31], a[148:151], a[220:223], a[28:31]\n" \
"s_add_u32 s79, s79, 4096\n" \
"buffer_load_dwordx4 a[240:243], v248, s[32:35], s79 offen offset:0 sc1\n" \
"v_mfma_f32_16x16x32_bf16 a[28:31], a[132:135], a[216:219], a[28:31]\n" \
"ds_read_b64_tr_b16 a[208:209], v240 offset:50688\n" \
"ds_read_b64_tr_b16 a[210:211], v240 offset:52736\n" \
"v_mfma_f32_16x16x32_bf16 a[88:91], a[136:139], a[216:219], a[88:91]\n" \
"ds_read_b64_tr_b16 a[212:213], v240 offset:58880\n" \
"v_mfma_f32_16x16x32_bf16 a[88:91], a[152:155], a[220:223], a[88:91]\n" \
"v_mfma_f32_16x16x32_bf16 a[92:95], a[156:159], a[220:223], a[92:95]\n" \
"ds_read_b64_tr_b16 a[214:215], v240 offset:60928\n" \
"v_mfma_f32_16x16x32_bf16 a[92:95], a[140:143], a[216:219], a[92:95]\n" \
"s_waitcnt lgkmcnt(8)\n" \
"v_mfma_f32_16x16x32_bf16 a[32:35], a[128:131], a[192:195], a[32:35]\n" \
"ds_read_b64_tr_b16 a[216:217], v241 offset:50688\n" \
"v_mfma_f32_16x16x32_bf16 a[32:35], a[144:147], a[196:199], a[32:35]\n" \
"s_add_u32 s80, s80, 4096\n" \
"v_mfma_f32_16x16x32_bf16 a[36:39], a[148:151], a[196:199], a[36:39]\n" \
"buffer_load_dwordx4 a[244:247], v248, s[32:35], s80 offen offset:0 sc1\n" \
"v_mfma_f32_16x16x32_bf16 a[36:39], a[132:135], a[192:195], a[36:39]\n" \
"ds_read_b64_tr_b16 a[218:219], v241 offset:52736\n" \
"ds_read_b64_tr_b16 a[220:221], v241 offset:58880\n" \
"v_mfma_f32_16x16x32_bf16 a[96:99], a[136:139], a[192:195], a[96:99]\n" \
"ds_read_b64_tr_b16 a[222:223], v241 offset:60928\n" \
"v_xor_b32 v240, 0x30000, v240\n" \
"v_mfma_f32_16x16x32_bf16 a[96:99], a[152:155], a[196:199], a[96:99]\n" \
"v_xor_b32 v241, 0x30000, v241\n" \
"v_mfma_f32_16x16x32_bf16 a[100:103], a[156:159], a[196:199], a[100:103]\n" \
"v_mfma_f32_16x16x32_bf16 a[100:103], a[140:143], a[192:195], a[100:103]\n" \
"s_waitcnt lgkmcnt(8)\n" \
"s_add_u32 s85, s71, 4096\n" \
"v_mfma_f32_16x16x32_bf16 a[40:43], a[128:131], a[200:203], a[40:43]\n" \
"buffer_store_dwordx4 v[156:159], v248, s[32:35], s85 offen offset:3072 sc1\n" \
"v_mfma_f32_16x16x32_bf16 a[40:43], a[144:147], a[204:207], a[40:43]\n" \
"s_add_u32 s36, s36, 4096\n" \
"v_mfma_f32_16x16x32_bf16 a[44:47], a[148:151], a[204:207], a[44:47]\n" \
"buffer_load_dwordx4 a[248:251], v248, s[32:35], s36 offen offset:0 sc1\n" \
"v_mfma_f32_16x16x32_bf16 a[44:47], a[132:135], a[200:203], a[44:47]\n" \
"v_mfma_f32_16x16x32_bf16 a[104:107], a[136:139], a[200:203], a[104:107]\n" \
"s_waitcnt vmcnt(32) lgkmcnt(0)\n" \
"s_barrier\n" \
"s_add_u32 m0, s70, 49152\n" \
"v_mfma_f32_16x16x32_bf16 a[104:107], a[152:155], a[204:207], a[104:107]\n" \
"s_add_u32 s74, s69, 1572864\n" \
"ds_read_b64_tr_b16 a[192:193], v240 offset:0\n" \
"ds_read_b64_tr_b16 a[194:195], v240 offset:2048\n" \
"v_mfma_f32_16x16x32_bf16 a[108:111], a[156:159], a[204:207], a[108:111]\n" \
"buffer_load_dwordx4 v246, s[28:31], s74 offen lds\n" \
"v_mfma_f32_16x16x32_bf16 a[108:111], a[140:143], a[200:203], a[108:111]\n" \
"ds_read_b64_tr_b16 a[196:197], v240 offset:8192\n" \
"ds_read_b64_tr_b16 a[198:199], v240 offset:10240\n" \
"v_mfma_f32_16x16x32_bf16 a[48:51], a[128:131], a[208:211], a[48:51]\n" \
"ds_read_b64_tr_b16 a[200:201], v241 offset:0\n" \
"v_mfma_f32_16x16x32_bf16 a[48:51], a[144:147], a[212:215], a[48:51]\n" \
"s_add_u32 s37, s37, 4096\n" \
"v_mfma_f32_16x16x32_bf16 a[52:55], a[148:151], a[212:215], a[52:55]\n" \
"buffer_load_dwordx4 a[252:255], v248, s[32:35], s37 offen offset:0 sc1\n" \
"v_mfma_f32_16x16x32_bf16 a[52:55], a[132:135], a[208:211], a[52:55]\n" \
"ds_read_b64_tr_b16 a[202:203], v241 offset:2048\n" \
"v_mfma_f32_16x16x32_bf16 a[112:115], a[136:139], a[208:211], a[112:115]\n" \
"ds_read_b64_tr_b16 a[204:205], v241 offset:8192\n" \
"v_mfma_f32_16x16x32_bf16 a[112:115], a[152:155], a[212:215], a[112:115]\n" \
"v_mfma_f32_16x16x32_bf16 a[116:119], a[156:159], a[212:215], a[116:119]\n" \
"ds_read_b64_tr_b16 a[206:207], v241 offset:10240\n" \
"v_mfma_f32_16x16x32_bf16 a[116:119], a[140:143], a[208:211], a[116:119]\n" \
"v_mfma_f32_16x16x32_bf16 a[56:59], a[128:131], a[216:219], a[56:59]\n" \
"ds_read_b64_tr_b16 a[208:209], v240 offset:512\n" \
"v_mfma_f32_16x16x32_bf16 a[56:59], a[144:147], a[220:223], a[56:59]\n" \
"v_mfma_f32_16x16x32_bf16 a[60:63], a[148:151], a[220:223], a[60:63]\n" \
"s_add_u32 m0, s70, 50176\n" \
"ds_read_b64_tr_b16 a[210:211], v240 offset:2560\n" \
"v_mfma_f32_16x16x32_bf16 a[60:63], a[132:135], a[216:219], a[60:63]\n" \
"s_add_u32 s74, s69, 1572992\n" \
"v_mfma_f32_16x16x32_bf16 a[120:123], a[136:139], a[216:219], a[120:123]\n" \
"buffer_load_dwordx4 v246, s[28:31], s74 offen lds\n" \
"v_mfma_f32_16x16x32_bf16 a[120:123], a[152:155], a[220:223], a[120:123]\n" \
"ds_read_b64_tr_b16 a[212:213], v240 offset:8704\n" \
"v_mfma_f32_16x16x32_bf16 a[124:127], a[156:159], a[220:223], a[124:127]\n" \
"s_waitcnt vmcnt(21)\n" \
"ds_read_b64_tr_b16 a[214:215], v240 offset:10752\n" \
"v_mfma_f32_16x16x32_bf16 a[124:127], a[140:143], a[216:219], a[124:127]\n" \
"s_waitcnt lgkmcnt(8)\n" \
  \
"v_mfma_f32_16x16x32_bf16 v[0:3], a[192:195], v[192:195], 0\n" \
"ds_read_b64_tr_b16 a[216:217], v241 offset:512\n" \
"v_mfma_f32_16x16x32_bf16 v[0:3], a[196:199], a[160:163], v[0:3]\n" \
"buffer_load_dwordx4 a[128:131], v248, s[32:35], s79 offen offset:1024 sc1\n" \
"v_accvgpr_read_b32 v144, a0\n" \
"v_mfma_f32_16x16x32_bf16 v[32:35], a[196:199], a[164:167], 0\n" \
"ds_read_b64_tr_b16 a[218:219], v241 offset:2560\n" \
"v_accvgpr_read_b32 v145, a1\n" \
"v_mfma_f32_16x16x32_bf16 v[32:35], a[192:195], v[196:199], v[32:35]\n" \
"v_accvgpr_read_b32 v146, a2\n" \
"v_accvgpr_read_b32 v147, a3\n" \
"v_mfma_f32_16x16x32_bf16 v[64:67], a[192:195], v[200:203], 0\n" \
"ds_read_b64_tr_b16 a[220:221], v241 offset:8704\n" \
"v_accvgpr_read_b32 v148, a4\n" \
"v_mfma_f32_16x16x32_bf16 v[64:67], a[196:199], a[168:171], v[64:67]\n" \
"v_accvgpr_read_b32 v149, a5\n" \
"v_accvgpr_read_b32 v150, a6\n" \
"v_mfma_f32_16x16x32_bf16 v[96:99], a[196:199], a[172:175], 0\n" \
"ds_read_b64_tr_b16 a[222:223], v241 offset:10752\n" \
"v_accvgpr_read_b32 v151, a7\n" \
"v_mfma_f32_16x16x32_bf16 v[96:99], a[192:195], v[204:207], v[96:99]\n" \
"s_waitcnt lgkmcnt(8)\n" \
"v_max3_f32 v250, |v144|, |v145|, v250\n" \
"v_max3_f32 v250, |v146|, |v147|, v250\n" \
"v_mfma_f32_16x16x32_bf16 v[4:7], a[200:203], v[192:195], 0\n" \
"ds_read_b64_tr_b16 a[192:193], v240 offset:1024\n" \
"v_max3_f32 v250, |v148|, |v149|, v250\n" \
"v_mfma_f32_16x16x32_bf16 v[4:7], a[204:207], a[160:163], v[4:7]\n" \
"buffer_load_dwordx4 a[132:135], v248, s[32:35], s80 offen offset:1024 sc1\n" \
"v_max3_f32 v250, |v150|, |v151|, v250\n" \
"v_mfma_f32_16x16x32_bf16 v[36:39], a[204:207], a[164:167], 0\n" \
"s_add_u32 m0, s70, 57344\n" \
"ds_read_b64_tr_b16 a[194:195], v240 offset:3072\n" \
"v_cvt_pk_bf16_f32 v144, v144, v145\n" \
"v_mfma_f32_16x16x32_bf16 v[36:39], a[200:203], v[196:199], v[36:39]\n" \
"s_add_u32 s74, s69, 1835008\n" \
"v_cvt_pk_bf16_f32 v145, v146, v147\n" \
"v_cvt_pk_bf16_f32 v146, v148, v149\n" \
"v_mfma_f32_16x16x32_bf16 v[68:71], a[200:203], v[200:203], 0\n" \
"buffer_load_dwordx4 v246, s[28:31], s74 offen lds\n" \
"v_mfma_f32_16x16x32_bf16 v[68:71], a[204:207], a[168:171], v[68:71]\n" \
"ds_read_b64_tr_b16 a[196:197], v240 offset:9216\n" \
"v_cvt_pk_bf16_f32 v147, v150, v151\n" \
"v_mfma_f32_16x16x32_bf16 v[100:103], a[204:207], a[172:175], 0\n" \
"ds_read_b64_tr_b16 a[198:199], v240 offset:11264\n" \
"v_mfma_f32_16x16x32_bf16 v[100:103], a[200:203], v[204:207], v[100:103]\n" \
"s_waitcnt lgkmcnt(8)\n" \
"ds_write_b128 v249, v[144:147] offset:0\n" \
"v_accvgpr_read_b32 v152, a64\n" \
"v_mfma_f32_16x16x32_bf16 v[8:11], a[208:211], v[192:195], 0\n" \
"ds_read_b64_tr_b16 a[200:201], v241 offset:1024\n" \
"v_accvgpr_read_b32 v153, a65\n" \
"v_mfma_f32_16x16x32_bf16 v[8:11], a[212:215], a[160:163], v[8:11]\n" \
"buffer_load_dwordx4 a[136:139], v248, s[32:35], s36 offen offset:1024 sc1\n" \
"v_accvgpr_read_b32 v154, a66\n" \
"v_mfma_f32_16x16x32_bf16 v[40:43], a[212:215], a[164:167], 0\n" \
"ds_read_b64_tr_b16 a[202:203], v241 offset:3072\n" \
"v_accvgpr_read_b32 v155, a67\n" \
"v_mfma_f32_16x16x32_bf16 v[40:43], a[208:211], v[196:199], v[40:43]\n" \
"v_accvgpr_read_b32 v156, a68\n" \
"v_accvgpr_read_b32 v157, a69\n" \
"v_mfma_f32_16x16x32_bf16 v[72:75], a[208:211], v[200:203], 0\n" \
"s_add_u32 s85, s71, 589824\n" \
"ds_read_b64_tr_b16 a[204:205], v241 offset:9216\n" \
"v_accvgpr_read_b32 v158, a70\n" \
"v_mfma_f32_16x16x32_bf16 v[72:75], a[212:215], a[168:171], v[72:75]\n" \
"buffer_store_dwordx4 v[144:147], v248, s[32:35], s85 offen offset:0 sc1\n" \
"v_mfma_f32_16x16x32_bf16 v[104:107], a[212:215], a[172:175], 0\n" \
"s_add_u32 m0, s70, 58368\n" \
"ds_read_b64_tr_b16 a[206:207], v241 offset:11264\n" \
"v_accvgpr_read_b32 v159, a71\n" \
"v_mfma_f32_16x16x32_bf16 v[104:107], a[208:211], v[204:207], v[104:107]\n" \
"s_waitcnt lgkmcnt(9)\n" \
"v_max3_f32 v250, |v152|, |v153|, v250\n" \
"v_max3_f32 v250, |v154|, |v155|, v250\n" \
"v_mfma_f32_16x16x32_bf16 v[12:15], a[216:219], v[192:195], 0\n" \
"s_add_u32 s74, s69, 1835136\n" \
"buffer_load_dwordx4 v246, s[28:31], s74 offen lds\n" \
"v_mfma_f32_16x16x32_bf16 v[12:15], a[220:223], a[160:163], v[12:15]\n" \
"buffer_load_dwordx4 a[140:143], v248, s[32:35], s37 offen offset:1024 sc1\n" \
"v_max3_f32 v250, |v156|, |v157|, v250\n" \
"v_mfma_f32_16x16x32_bf16 v[44:47], a[220:223], a[164:167], 0\n" \
"buffer_load_dwordx4 a[144:147], v248, s[32:35], s79 offen offset:2048 sc1\n" \
"v_max3_f32 v250, |v158|, |v159|, v250\n" \
"v_mfma_f32_16x16x32_bf16 v[44:47], a[216:219], v[196:199], v[44:47]\n" \
"ds_read_b64_tr_b16 a[208:209], v240 offset:1536\n" \
"ds_read_b64_tr_b16 a[210:211], v240 offset:3584\n" \
"v_mfma_f32_16x16x32_bf16 v[76:79], a[216:219], v[200:203], 0\n" \
"ds_read_b64_tr_b16 a[212:213], v240 offset:9728\n" \
"v_cvt_pk_bf16_f32 v152, v152, v153\n" \
"v_mfma_f32_16x16x32_bf16 v[76:79], a[220:223], a[168:171], v[76:79]\n" \
"v_cvt_pk_bf16_f32 v153, v154, v155\n" \
"v_cvt_pk_bf16_f32 v154, v156, v157\n" \
"v_mfma_f32_16x16x32_bf16 v[108:111], a[220:223], a[172:175], 0\n" \
"ds_read_b64_tr_b16 a[214:215], v240 offset:11776\n" \
"v_cvt_pk_bf16_f32 v155, v158, v159\n" \
"v_mfma_f32_16x16x32_bf16 v[108:111], a[216:219], v[204:207], v[108:111]\n" \
"s_waitcnt lgkmcnt(9)\n" \
"v_accvgpr_write_b32 a64, v152\n" \
"v_accvgpr_write_b32 a65, v153\n" \
"v_mfma_f32_16x16x32_bf16 v[16:19], a[192:195], v[192:195], 0\n" \
"ds_read_b64_tr_b16 a[216:217], v241 offset:1536\n" \
"v_accvgpr_write_b32 a66, v154\n" \
"v_mfma_f32_16x16x32_bf16 v[16:19], a[196:199], a[160:163], v[16:19]\n" \
"v_accvgpr_write_b32 a67, v155\n" \
"v_mfma_f32_16x16x32_bf16 v[48:51], a[196:199], a[164:167], 0\n" \
"buffer_load_dwordx4 a[148:151], v248, s[32:35], s80 offen offset:2048 sc1\n" \
"v_mfma_f32_16x16x32_bf16 v[48:51], a[192:195], v[196:199], v[48:51]\n" \
"ds_read_b64_tr_b16 a[218:219], v241 offset:3584\n" \
"ds_read_b64_tr_b16 a[220:221], v241 offset:9728\n" \
"v_mfma_f32_16x16x32_bf16 v[80:83], a[192:195], v[200:203], 0\n" \
"ds_read_b64_tr_b16 a[222:223], v241 offset:11776\n" \
"v_mfma_f32_16x16x32_bf16 v[80:83], a[196:199], a[168:171], v[80:83]\n" \
"v_mfma_f32_16x16x32_bf16 v[112:115], a[196:199], a[172:175], 0\n" \
"v_mfma_f32_16x16x32_bf16 v[112:115], a[192:195], v[204:207], v[112:115]\n" \
"s_waitcnt lgkmcnt(8)\n" \
"v_mfma_f32_16x16x32_bf16 v[20:23], a[200:203], v[192:195], 0\n" \
"v_mfma_f32_16x16x32_bf16 v[20:23], a[204:207], a[160:163], v[20:23]\n" \
"v_mfma_f32_16x16x32_bf16 v[52:55], a[204:207], a[164:167], 0\n" \
"buffer_load_dwordx4 a[152:155], v248, s[32:35], s36 offen offset:2048 sc1\n" \
"v_mfma_f32_16x16x32_bf16 v[52:55], a[200:203], v[196:199], v[52:55]\n" \
"v_mfma_f32_16x16x32_bf16 v[84:87], a[200:203], v[200:203], 0\n" \
"s_waitcnt lgkmcnt(0)\n" \
"s_barrier\n" \
"s_add_u32 m0, s70, 65536\n" \
"v_mfma_f32_16x16x32_bf16 v[84:87], a[204:207], a[168:171], v[84:87]\n" \
"s_add_u32 s74, s69, 2097152\n" \
"ds_read_b64_tr_b16 a[192:193], v240 offset:16384\n" \
"ds_read_b64_tr_b16 a[194:195], v240 offset:18432\n" \
"v_mfma_f32_16x16x32_bf16 v[116:119], a[204:207], a[172:175], 0\n" \
"buffer_load_dwordx4 v246, s[28:31], s74 offen lds\n" \
"v_mfma_f32_16x16x32_bf16 v[116:119], a[200:203], v[204:207], v[116:119]\n" \
"ds_read_b64_tr_b16 a[196:197], v240 offset:24576\n" \
"ds_read_b64_tr_b16 a[198:199], v240 offset:26624\n" \
"v_mfma_f32_16x16x32_bf16 v[24:27], a[208:211], v[192:195], 0\n" \
"ds_read_b64_tr_b16 a[200:201], v241 offset:16384\n" \
"v_mfma_f32_16x16x32_bf16 v[24:27], a[212:215], a[160:163], v[24:27]\n" \
"v_mfma_f32_16x16x32_bf16 v[56:59], a[212:215], a[164:167], 0\n" \
"s_add_u32 s85, s94, 0\n" \
"buffer_load_dwordx4 a[156:159], v248, s[32:35], s37 offen offset:2048 sc1\n" \
"v_mfma_f32_16x16x32_bf16 v[56:59], a[208:211], v[196:199], v[56:59]\n" \
"ds_read_b64_tr_b16 a[202:203], v241 offset:18432\n" \
"v_mfma_f32_16x16x32_bf16 v[88:91], a[208:211], v[200:203], 0\n" \
"ds_read_b64_tr_b16 a[204:205], v241 offset:24576\n" \
"v_mfma_f32_16x16x32_bf16 v[88:91], a[212:215], a[168:171], v[88:91]\n" \
"buffer_store_dwordx4 v[160:163], v248, s[32:35], s85 offen offset:0 sc1\n" \
"v_mfma_f32_16x16x32_bf16 v[120:123], a[212:215], a[172:175], 0\n" \
"ds_read_b64_tr_b16 a[206:207], v241 offset:26624\n" \
"v_mfma_f32_16x16x32_bf16 v[120:123], a[208:211], v[204:207], v[120:123]\n" \
"v_mfma_f32_16x16x32_bf16 v[28:31], a[216:219], v[192:195], 0\n" \
"ds_read_b64_tr_b16 a[208:209], v240 offset:16896\n" \
"v_mfma_f32_16x16x32_bf16 v[28:31], a[220:223], a[160:163], v[28:31]\n" \
"v_mfma_f32_16x16x32_bf16 v[60:63], a[220:223], a[164:167], 0\n" \
"s_add_u32 m0, s70, 66560\n" \
"ds_read_b64_tr_b16 a[210:211], v240 offset:18944\n" \
"v_mfma_f32_16x16x32_bf16 v[60:63], a[216:219], v[196:199], v[60:63]\n" \
"s_add_u32 s74, s69, 2097280\n" \
"v_mfma_f32_16x16x32_bf16 v[92:95], a[216:219], v[200:203], 0\n" \
"buffer_load_dwordx4 v246, s[28:31], s74 offen lds\n" \
"v_mfma_f32_16x16x32_bf16 v[92:95], a[220:223], a[168:171], v[92:95]\n" \
"s_lshl_b32 s85, s68, 4\n" \
"ds_read_b64_tr_b16 a[212:213], v240 offset:25088\n" \
"v_accvgpr_read_b32 v144, a8\n" \
"v_mfma_f32_16x16x32_bf16 v[124:127], a[220:223], a[172:175], 0\n" \
"s_waitcnt vmcnt(21)\n" \
"ds_read_b64_tr_b16 a[214:215], v240 offset:27136\n" \
"v_mfma_f32_16x16x32_bf16 v[124:127], a[216:219], v[204:207], v[124:127]\n" \
"s_waitcnt lgkmcnt(8)\n" \
"v_accvgpr_read_b32 v145, a9\n" \
"v_accvgpr_read_b32 v146, a10\n" \
"v_mfma_f32_16x16x32_bf16 v[0:3], a[192:195], a[176:179], v[0:3]\n" \
"s_add_u32 s85, s85, s67\n" \
"ds_read_b64_tr_b16 a[216:217], v241 offset:16896\n" \
"v_accvgpr_read_b32 v147, a11\n" \
"v_mfma_f32_16x16x32_bf16 v[0:3], a[196:199], a[224:227], v[0:3]\n" \
"s_lshl2_add_u32 s85, s85, %[wid]\n" \
"buffer_load_dwordx4 v[192:195], v248, s[32:35], s79 offen offset:3072 sc1\n" \
"v_accvgpr_read_b32 v148, a12\n" \
"v_mfma_f32_16x16x32_bf16 v[32:35], a[196:199], a[228:231], v[32:35]\n" \
"s_lshl_b32 s85, s85, 2\n" \
"ds_read_b64_tr_b16 a[218:219], v241 offset:18944\n" \
"v_accvgpr_read_b32 v149, a13\n" \
"v_mfma_f32_16x16x32_bf16 v[32:35], a[192:195], a[180:183], v[32:35]\n" \
"s_waitcnt vmcnt(19)\n" \
"s_add_u32 s85, s85, 81920\n" \
"buffer_store_dword v239, off, s[32:35], s85 sc1\n" \
"v_mfma_f32_16x16x32_bf16 v[64:67], a[192:195], a[184:187], v[64:67]\n" \
"ds_read_b64_tr_b16 a[220:221], v241 offset:25088\n" \
"v_accvgpr_read_b32 v150, a14\n" \
"v_mfma_f32_16x16x32_bf16 v[64:67], a[196:199], a[232:235], v[64:67]\n" \
"v_accvgpr_read_b32 v151, a15\n" \
"v_max3_f32 v250, |v144|, |v145|, v250\n" \
"v_mfma_f32_16x16x32_bf16 v[96:99], a[196:199], a[236:239], v[96:99]\n" \
"ds_read_b64_tr_b16 a[222:223], v241 offset:27136\n" \
"v_max3_f32 v250, |v146|, |v147|, v250\n" \
"v_mfma_f32_16x16x32_bf16 v[96:99], a[192:195], a[188:191], v[96:99]\n" \
"s_waitcnt lgkmcnt(8)\n" \
"v_max3_f32 v250, |v148|, |v149|, v250\n" \
"v_max3_f32 v250, |v150|, |v151|, v250\n" \
"v_mfma_f32_16x16x32_bf16 v[4:7], a[200:203], a[176:179], v[4:7]\n" \
"ds_read_b64_tr_b16 a[192:193], v240 offset:17408\n" \
"v_cvt_pk_bf16_f32 v144, v144, v145\n" \
"v_mfma_f32_16x16x32_bf16 v[4:7], a[204:207], a[224:227], v[4:7]\n" \
"buffer_load_dwordx4 v[196:199], v248, s[32:35], s80 offen offset:3072 sc1\n" \
"v_cvt_pk_bf16_f32 v145, v146, v147\n" \
"v_mfma_f32_16x16x32_bf16 v[36:39], a[204:207], a[228:231], v[36:39]\n" \
"s_add_u32 m0, s70, 73728\n" \
"ds_read_b64_tr_b16 a[194:195], v240 offset:19456\n" \
"v_cvt_pk_bf16_f32 v146, v148, v149\n" \
"v_mfma_f32_16x16x32_bf16 v[36:39], a[200:203], a[180:183], v[36:39]\n" \
"s_add_u32 s74, s69, 2359296\n" \
"v_cvt_pk_bf16_f32 v147, v150, v151\n" \
"v_mfma_f32_16x16x32_bf16 v[68:71], a[200:203], a[184:187], v[68:71]\n" \
"buffer_load_dwordx4 v246, s[28:31], s74 offen lds\n" \
"v_mfma_f32_16x16x32_bf16 v[68:71], a[204:207], a[232:235], v[68:71]\n" \
"ds_read_b64_tr_b16 a[196:197], v240 offset:25600\n" \
"v_mfma_f32_16x16x32_bf16 v[100:103], a[204:207], a[236:239], v[100:103]\n" \
"ds_read_b64_tr_b16 a[198:199], v240 offset:27648\n" \
"v_mfma_f32_16x16x32_bf16 v[100:103], a[200:203], a[188:191], v[100:103]\n" \
"s_waitcnt lgkmcnt(8)\n" \
"ds_write_b128 v249, v[144:147] offset:1024\n" \
"v_accvgpr_read_b32 v152, a72\n" \
"v_mfma_f32_16x16x32_bf16 v[8:11], a[208:211], a[176:179], v[8:11]\n" \
"s_add_u32 s85, s71, 589824\n" \
"ds_read_b64_tr_b16 a[200:201], v241 offset:17408\n" \
"v_accvgpr_read_b32 v153, a73\n" \
"v_mfma_f32_16x16x32_bf16 v[8:11], a[212:215], a[224:227], v[8:11]\n" \
"buffer_load_dwordx4 v[200:203], v248, s[32:35], s36 offen offset:3072 sc1\n" \
"v_accvgpr_read_b32 v154, a74\n" \
"v_mfma_f32_16x16x32_bf16 v[40:43], a[212:215], a[228:231], v[40:43]\n" \
"ds_read_b64_tr_b16 a[202:203], v241 offset:19456\n" \
"v_accvgpr_read_b32 v155, a75\n" \
"v_mfma_f32_16x16x32_bf16 v[40:43], a[208:211], a[180:183], v[40:43]\n" \
"v_accvgpr_read_b32 v156, a76\n" \
"v_accvgpr_read_b32 v157, a77\n" \
"v_mfma_f32_16x16x32_bf16 v[72:75], a[208:211], a[184:187], v[72:75]\n" \
"buffer_store_dwordx4 v[144:147], v248, s[32:35], s85 offen offset:1024 sc1\n" \
"v_mfma_f32_16x16x32_bf16 v[72:75], a[212:215], a[232:235], v[72:75]\n" \
"ds_read_b64_tr_b16 a[204:205], v241 offset:25600\n" \
"v_accvgpr_read_b32 v158, a78\n" \
"v_mfma_f32_16x16x32_bf16 v[104:107], a[212:215], a[236:239], v[104:107]\n" \
"s_add_u32 m0, s70, 74752\n" \
"ds_read_b64_tr_b16 a[206:207], v241 offset:27648\n" \
"v_accvgpr_read_b32 v159, a79\n" \
"v_mfma_f32_16x16x32_bf16 v[104:107], a[208:211], a[188:191], v[104:107]\n" \
"s_waitcnt lgkmcnt(9)\n" \
"v_max3_f32 v250, |v152|, |v153|, v250\n" \
"v_max3_f32 v250, |v154|, |v155|, v250\n" \
"v_mfma_f32_16x16x32_bf16 v[12:15], a[216:219], a[176:179], v[12:15]\n" \
"s_add_u32 s74, s69, 2359424\n" \
"buffer_load_dwordx4 v246, s[28:31], s74 offen lds\n" \
"v_mfma_f32_16x16x32_bf16 v[12:15], a[220:223], a[224:227], v[12:15]\n" \
"buffer_load_dwordx4 v[204:207], v248, s[32:35], s37 offen offset:3072 sc1\n" \
"v_max3_f32 v250, |v156|, |v157|, v250\n" \
"v_mfma_f32_16x16x32_bf16 v[44:47], a[220:223], a[228:231], v[44:47]\n" \
"s_add_u32 s79, s79, 4096\n" \
"buffer_load_dwordx4 a[160:163], v248, s[32:35], s79 offen offset:0 sc1\n" \
"v_max3_f32 v250, |v158|, |v159|, v250\n" \
"v_mfma_f32_16x16x32_bf16 v[44:47], a[216:219], a[180:183], v[44:47]\n" \
"ds_read_b64_tr_b16 a[208:209], v240 offset:17920\n" \
"ds_read_b64_tr_b16 a[210:211], v240 offset:19968\n" \
"v_mfma_f32_16x16x32_bf16 v[76:79], a[216:219], a[184:187], v[76:79]\n" \
"ds_read_b64_tr_b16 a[212:213], v240 offset:26112\n" \
"v_cvt_pk_bf16_f32 v152, v152, v153\n" \
"v_mfma_f32_16x16x32_bf16 v[76:79], a[220:223], a[232:235], v[76:79]\n" \
"v_cvt_pk_bf16_f32 v153, v154, v155\n" \
"v_cvt_pk_bf16_f32 v154, v156, v157\n" \
"v_mfma_f32_16x16x32_bf16 v[108:111], a[220:223], a[236:239], v[108:111]\n" \
".Lst0_5_%=:\n" \
"s_cbranch_execz .Lst0_9_%=\n" \
"ds_read_b64_tr_b16 a[214:215], v240 offset:28160\n" \
"v_cvt_pk_bf16_f32 v155, v158, v159\n" \
"v_mfma_f32_16x16x32_bf16 v[108:111], a[216:219], a[188:191], v[108:111]\n" \
"s_waitcnt lgkmcnt(9)\n" \
"v_accvgpr_write_b32 a72, v152\n" \
"v_accvgpr_write_b32 a73, v153\n" \
"v_mfma_f32_16x16x32_bf16 v[16:19], a[192:195], a[176:179], v[16:19]\n" \
"ds_read_b64_tr_b16 a[216:217], v241 offset:17920\n" \
"v_accvgpr_write_b32 a74, v154\n" \
"v_mfma_f32_16x16x32_bf16 v[16:19], a[196:199], a[224:227], v[16:19]\n" \
"s_add_u32 s80, s80, 4096\n" \
"v_accvgpr_write_b32 a75, v155\n" \
"v_mfma_f32_16x16x32_bf16 v[48:51], a[196:199], a[228:231], v[48:51]\n" \
"buffer_load_dwordx4 a[164:167], v248, s[32:35], s80 offen offset:0 sc1\n" \
"v_mfma_f32_16x16x32_bf16 v[48:51], a[192:195], a[180:183], v[48:51]\n" \
"ds_read_b64_tr_b16 a[218:219], v241 offset:19968\n" \
"ds_read_b64_tr_b16 a[220:221], v241 offset:26112\n" \
"v_mfma_f32_16x16x32_bf16 v[80:83], a[192:195], a[184:187], v[80:83]\n" \
"ds_read_b64_tr_b16 a[222:223], v241 offset:28160\n" \
"v_xor_b32 v240, 0x30000, v240\n" \
"v_mfma_f32_16x16x32_bf16 v[80:83], a[196:199], a[232:235], v[80:83]\n" \
"v_xor_b32 v241, 0x30000, v241\n" \
"v_mfma_f32_16x16x32_bf16 v[112:115], a[196:199], a[236:239], v[112:115]\n" \
"v_mfma_f32_16x16x32_bf16 v[112:115], a[192:195], a[188:191], v[112:115]\n" \
"s_waitcnt lgkmcnt(8)\n" \
"v_mfma_f32_16x16x32_bf16 v[20:23], a[200:203], a[176:179], v[20:23]\n" \
"v_mfma_f32_16x16x32_bf16 v[20:23], a[204:207], a[224:227], v[20:23]\n" \
"s_add_u32 s36, s36, 4096\n" \
"v_mfma_f32_16x16x32_bf16 v[52:55], a[204:207], a[228:231], v[52:55]\n" \
"buffer_load_dwordx4 a[168:171], v248, s[32:35], s36 offen offset:0 sc1\n" \
"v_mfma_f32_16x16x32_bf16 v[52:55], a[200:203], a[180:183], v[52:55]\n" \
"v_mfma_f32_16x16x32_bf16 v[84:87], a[200:203], a[184:187], v[84:87]\n" \
"s_waitcnt lgkmcnt(0)\n" \
"s_barrier\n" \
"s_add_u32 m0, s70, 81920\n" \
"v_mfma_f32_16x16x32_bf16 v[84:87], a[204:207], a[232:235], v[84:87]\n" \
"s_add_u32 s74, s69, 2621440\n" \
"ds_read_b64_tr_b16 a[192:193], v240 offset:32768\n" \
"ds_read_b64_tr_b16 a[194:195], v240 offset:34816\n" \
"v_mfma_f32_16x16x32_bf16 v[116:119], a[204:207], a[236:239], v[116:119]\n" \
"s_add_u32 s85, s94, 0\n" \
"buffer_load_dwordx4 v246, s[28:31], s74 offen lds\n" \
"v_mfma_f32_16x16x32_bf16 v[116:119], a[200:203], a[188:191], v[116:119]\n" \
"ds_read_b64_tr_b16 a[196:197], v240 offset:40960\n" \
"ds_read_b64_tr_b16 a[198:199], v240 offset:43008\n" \
"v_mfma_f32_16x16x32_bf16 v[24:27], a[208:211], a[176:179], v[24:27]\n" \
"ds_read_b64_tr_b16 a[200:201], v241 offset:32768\n" \
"v_mfma_f32_16x16x32_bf16 v[24:27], a[212:215], a[224:227], v[24:27]\n" \
"s_add_u32 s37, s37, 4096\n" \
"buffer_store_dwordx4 v[164:167], v248, s[32:35], s85 offen offset:1024 sc1\n" \
"v_mfma_f32_16x16x32_bf16 v[56:59], a[212:215], a[228:231], v[56:59]\n" \
"buffer_load_dwordx4 a[172:175], v248, s[32:35], s37 offen offset:0 sc1\n" \
"v_mfma_f32_16x16x32_bf16 v[56:59], a[208:211], a[180:183], v[56:59]\n" \
"ds_read_b64_tr_b16 a[202:203], v241 offset:34816\n" \
"v_mfma_f32_16x16x32_bf16 v[88:91], a[208:211], a[184:187], v[88:91]\n" \
"ds_read_b64_tr_b16 a[204:205], v241 offset:40960\n" \
"v_mfma_f32_16x16x32_bf16 v[88:91], a[212:215], a[232:235], v[88:91]\n" \
"v_mfma_f32_16x16x32_bf16 v[120:123], a[212:215], a[236:239], v[120:123]\n" \
"ds_read_b64_tr_b16 a[206:207], v241 offset:43008\n" \
"v_mfma_f32_16x16x32_bf16 v[120:123], a[208:211], a[188:191], v[120:123]\n" \
"v_mfma_f32_16x16x32_bf16 v[28:31], a[216:219], a[176:179], v[28:31]\n" \
"ds_read_b64_tr_b16 a[208:209], v240 offset:33280\n" \
"v_mfma_f32_16x16x32_bf16 v[28:31], a[220:223], a[224:227], v[28:31]\n" \
"v_accvgpr_read_b32 v144, a16\n" \
"v_accvgpr_read_b32 v145, a17\n" \
"v_mfma_f32_16x16x32_bf16 v[60:63], a[220:223], a[228:231], v[60:63]\n" \
"s_add_u32 m0, s70, 82944\n" \
"ds_read_b64_tr_b16 a[210:211], v240 offset:35328\n" \
"v_accvgpr_read_b32 v146, a18\n" \
"v_mfma_f32_16x16x32_bf16 v[60:63], a[216:219], a[180:183], v[60:63]\n" \
"s_add_u32 s74, s69, 2621568\n" \
"v_accvgpr_read_b32 v147, a19\n" \
"v_accvgpr_read_b32 v148, a20\n" \
"v_mfma_f32_16x16x32_bf16 v[92:95], a[216:219], a[184:187], v[92:95]\n" \
"buffer_load_dwordx4 v246, s[28:31], s74 offen lds\n" \
"v_mfma_f32_16x16x32_bf16 v[92:95], a[220:223], a[232:235], v[92:95]\n" \
"ds_read_b64_tr_b16 a[212:213], v240 offset:41472\n" \
"v_accvgpr_read_b32 v149, a21\n" \
"v_mfma_f32_16x16x32_bf16 v[124:127], a[220:223], a[236:239], v[124:127]\n" \
"s_waitcnt vmcnt(22)\n" \
"ds_read_b64_tr_b16 a[214:215], v240 offset:43520\n" \
"v_mfma_f32_16x16x32_bf16 v[124:127], a[216:219], a[188:191], v[124:127]\n" \
"s_waitcnt lgkmcnt(8)\n" \
"v_accvgpr_read_b32 v150, a22\n" \
"v_accvgpr_read_b32 v151, a23\n" \
"v_mfma_f32_16x16x32_bf16 v[0:3], a[192:195], a[240:243], v[0:3]\n" \
"ds_read_b64_tr_b16 a[216:217], v241 offset:33280\n" \
"v_max3_f32 v250, |v144|, |v145|, v250\n" \
"v_mfma_f32_16x16x32_bf16 v[0:3], a[196:199], a[128:131], v[0:3]\n" \
"buffer_load_dwordx4 a[176:179], v248, s[32:35], s79 offen offset:1024 sc1\n" \
"v_max3_f32 v250, |v146|, |v147|, v250\n" \
"v_mfma_f32_16x16x32_bf16 v[32:35], a[196:199], a[132:135], v[32:35]\n" \
"ds_read_b64_tr_b16 a[218:219], v241 offset:35328\n" \
"v_max3_f32 v250, |v148|, |v149|, v250\n" \
"v_mfma_f32_16x16x32_bf16 v[32:35], a[192:195], a[244:247], v[32:35]\n" \
"v_max3_f32 v250, |v150|, |v151|, v250\n" \
"v_cvt_pk_bf16_f32 v144, v144, v145\n" \
"v_mfma_f32_16x16x32_bf16 v[64:67], a[192:195], a[248:251], v[64:67]\n" \
"ds_read_b64_tr_b16 a[220:221], v241 offset:41472\n" \
"v_cvt_pk_bf16_f32 v145, v146, v147\n" \
"v_mfma_f32_16x16x32_bf16 v[64:67], a[196:199], a[136:139], v[64:67]\n" \
"v_cvt_pk_bf16_f32 v146, v148, v149\n" \
"v_cvt_pk_bf16_f32 v147, v150, v151\n" \
"v_mfma_f32_16x16x32_bf16 v[96:99], a[196:199], a[140:143], v[96:99]\n" \
"ds_read_b64_tr_b16 a[222:223], v241 offset:43520\n" \
"v_mfma_f32_16x16x32_bf16 v[96:99], a[192:195], a[252:255], v[96:99]\n" \
"s_waitcnt lgkmcnt(8)\n" \
"ds_write_b128 v249, v[144:147] offset:2048\n" \
"v_accvgpr_read_b32 v152, a80\n" \
"v_mfma_f32_16x16x32_bf16 v[4:7], a[200:203], a[240:243], v[4:7]\n" \
"ds_read_b64_tr_b16 a[192:193], v240 offset:33792\n" \
"v_accvgpr_read_b32 v153, a81\n" \
"v_mfma_f32_16x16x32_bf16 v[4:7], a[204:207], a[128:131], v[4:7]\n" \
"buffer_load_dwordx4 a[180:183], v248, s[32:35], s80 offen offset:1024 sc1\n" \
"v_accvgpr_read_b32 v154, a82\n" \
"v_mfma_f32_16x16x32_bf16 v[36:39], a[204:207], a[132:135], v[36:39]\n" \
"s_add_u32 m0, s70, 90112\n" \
"ds_read_b64_tr_b16 a[194:195], v240 offset:35840\n" \
"v_accvgpr_read_b32 v155, a83\n" \
"v_mfma_f32_16x16x32_bf16 v[36:39], a[200:203], a[244:247], v[36:39]\n" \
"s_add_u32 s74, s69, 2883584\n" \
"v_accvgpr_read_b32 v156, a84\n" \
"v_accvgpr_read_b32 v157, a85\n" \
"v_mfma_f32_16x16x32_bf16 v[68:71], a[200:203], a[248:251], v[68:71]\n" \
"s_add_u32 s85, s71, 589824\n" \
"buffer_load_dwordx4 v246, s[28:31], s74 offen lds\n" \
"v_mfma_f32_16x16x32_bf16 v[68:71], a[204:207], a[136:139], v[68:71]\n" \
"ds_read_b64_tr_b16 a[196:197], v240 offset:41984\n" \
"v_accvgpr_read_b32 v158, a86\n" \
"v_mfma_f32_16x16x32_bf16 v[100:103], a[204:207], a[140:143], v[100:103]\n" \
"ds_read_b64_tr_b16 a[198:199], v240 offset:44032\n" \
"v_accvgpr_read_b32 v159, a87\n" \
"v_mfma_f32_16x16x32_bf16 v[100:103], a[200:203], a[252:255], v[100:103]\n" \
"s_waitcnt lgkmcnt(9)\n" \
"buffer_store_dwordx4 v[144:147], v248, s[32:35], s85 offen offset:2048 sc1\n" \
"v_mfma_f32_16x16x32_bf16 v[8:11], a[208:211], a[240:243], v[8:11]\n" \
"ds_read_b64_tr_b16 a[200:201], v241 offset:33792\n" \
"v_max3_f32 v250, |v152|, |v153|, v250\n" \
"v_mfma_f32_16x16x32_bf16 v[8:11], a[212:215], a[128:131], v[8:11]\n" \
"buffer_load_dwordx4 a[184:187], v248, s[32:35], s36 offen offset:1024 sc1\n" \
"v_max3_f32 v250, |v154|, |v155|, v250\n" \
"v_mfma_f32_16x16x32_bf16 v[40:43], a[212:215], a[132:135], v[40:43]\n" \
"ds_read_b64_tr_b16 a[202:203], v241 offset:35840\n" \
"v_max3_f32 v250, |v156|, |v157|, v250\n" \
"v_mfma_f32_16x16x32_bf16 v[40:43], a[208:211], a[244:247], v[40:43]\n" \
"v_max3_f32 v250, |v158|, |v159|, v250\n" \
"v_cvt_pk_bf16_f32 v152, v152, v153\n" \
"v_mfma_f32_16x16x32_bf16 v[72:75], a[208:211], a[248:251], v[72:75]\n" \
"ds_read_b64_tr_b16 a[204:205], v241 offset:41984\n" \
"v_cvt_pk_bf16_f32 v153, v154, v155\n" \
"v_mfma_f32_16x16x32_bf16 v[72:75], a[212:215], a[136:139], v[72:75]\n" \
"v_cvt_pk_bf16_f32 v154, v156, v157\n" \
"v_cvt_pk_bf16_f32 v155, v158, v159\n" \
"v_mfma_f32_16x16x32_bf16 v[104:107], a[212:215], a[140:143], v[104:107]\n" \
"s_add_u32 m0, s70, 91136\n" \
"ds_read_b64_tr_b16 a[206:207], v241 offset:44032\n" \
"v_accvgpr_write_b32 a80, v152\n" \
"v_mfma_f32_16x16x32_bf16 v[104:107], a[208:211], a[252:255], v[104:107]\n" \
"s_waitcnt lgkmcnt(9)\n" \
"v_accvgpr_write_b32 a81, v153\n" \
"v_accvgpr_write_b32 a82, v154\n" \
"v_mfma_f32_16x16x32_bf16 v[12:15], a[216:219], a[240:243], v[12:15]\n" \
"s_add_u32 s74, s69, 2883712\n" \
"buffer_load_dwordx4 v246, s[28:31], s74 offen lds\n" \
"v_mfma_f32_16x16x32_bf16 v[12:15], a[220:223], a[128:131], v[12:15]\n" \
"buffer_load_dwordx4 a[188:191], v248, s[32:35], s37 offen offset:1024 sc1\n" \
"v_accvgpr_write_b32 a83, v155\n" \
"v_mfma_f32_16x16x32_bf16 v[44:47], a[220:223], a[132:135], v[44:47]\n" \
"buffer_load_dwordx4 a[0:3], v248, s[32:35], s79 offen offset:2048 sc1\n" \
"v_mfma_f32_16x16x32_bf16 v[44:47], a[216:219], a[244:247], v[44:47]\n" \
"ds_read_b64_tr_b16 a[208:209], v240 offset:34304\n" \
"ds_read_b64_tr_b16 a[210:211], v240 offset:36352\n" \
"v_mfma_f32_16x16x32_bf16 v[76:79], a[216:219], a[248:251], v[76:79]\n" \
"ds_read_b64_tr_b16 a[212:213], v240 offset:42496\n" \
"v_mfma_f32_16x16x32_bf16 v[76:79], a[220:223], a[136:139], v[76:79]\n" \
"v_mfma_f32_16x16x32_bf16 v[108:111], a[220:223], a[140:143], v[108:111]\n" \
"ds_read_b64_tr_b16 a[214:215], v240 offset:44544\n" \
"v_mfma_f32_16x16x32_bf16 v[108:111], a[216:219], a[252:255], v[108:111]\n" \
"s_waitcnt lgkmcnt(8)\n" \
"v_mfma_f32_16x16x32_bf16 v[16:19], a[192:195], a[240:243], v[16:19]\n" \
"ds_read_b64_tr_b16 a[216:217], v241 offset:34304\n" \
"v_mfma_f32_16x16x32_bf16 v[16:19], a[196:199], a[128:131], v[16:19]\n" \
"v_mfma_f32_16x16x32_bf16 v[48:51], a[196:199], a[132:135], v[48:51]\n" \
"buffer_load_dwordx4 a[4:7], v248, s[32:35], s80 offen offset:2048 sc1\n" \
"v_mfma_f32_16x16x32_bf16 v[48:51], a[192:195], a[244:247], v[48:51]\n" \
"ds_read_b64_tr_b16 a[218:219], v241 offset:36352\n" \
"ds_read_b64_tr_b16 a[220:221], v241 offset:42496\n" \
"v_mfma_f32_16x16x32_bf16 v[80:83], a[192:195], a[248:251], v[80:83]\n" \
"ds_read_b64_tr_b16 a[222:223], v241 offset:44544\n" \
"v_mfma_f32_16x16x32_bf16 v[80:83], a[196:199], a[136:139], v[80:83]\n" \
"v_mfma_f32_16x16x32_bf16 v[112:115], a[196:199], a[140:143], v[112:115]\n" \
"v_mfma_f32_16x16x32_bf16 v[112:115], a[192:195], a[252:255], v[112:115]\n" \
"s_waitcnt lgkmcnt(8)\n" \
"v_mfma_f32_16x16x32_bf16 v[20:23], a[200:203], a[240:243], v[20:23]\n" \
"v_mfma_f32_16x16x32_bf16 v[20:23], a[204:207], a[128:131], v[20:23]\n" \
"v_mfma_f32_16x16x32_bf16 v[52:55], a[204:207], a[132:135], v[52:55]\n" \
"s_add_u32 s85, s94, 0\n" \
"buffer_load_dwordx4 a[8:11], v248, s[32:35], s36 offen offset:2048 sc1\n" \
"v_mfma_f32_16x16x32_bf16 v[52:55], a[200:203], a[244:247], v[52:55]\n" \
"v_mfma_f32_16x16x32_bf16 v[84:87], a[200:203], a[248:251], v[84:87]\n" \
"s_waitcnt lgkmcnt(0)\n" \
"s_barrier\n" \
"s_add_u32 m0, s70, 32768\n" \
"v_mfma_f32_16x16x32_bf16 v[84:87], a[204:207], a[136:139], v[84:87]\n" \
"s_add_u32 s74, s69, 3145728\n" \
"buffer_store_dwordx4 v[168:171], v248, s[32:35], s85 offen offset:2048 sc1\n" \
"v_mfma_f32_16x16x32_bf16 v[116:119], a[204:207], a[140:143], v[116:119]\n" \
"ds_read_b64_tr_b16 a[192:193], v240 offset:49152\n" \
"ds_read_b64_tr_b16 a[194:195], v240 offset:51200\n" \
"v_mfma_f32_16x16x32_bf16 v[116:119], a[200:203], a[252:255], v[116:119]\n" \
"ds_read_b64_tr_b16 a[196:197], v240 offset:57344\n" \
"ds_read_b64_tr_b16 a[198:199], v240 offset:59392\n" \
"v_mfma_f32_16x16x32_bf16 v[24:27], a[208:211], a[240:243], v[24:27]\n" \
"buffer_load_dwordx4 v246, s[28:31], s74 offen lds\n" \
"v_mfma_f32_16x16x32_bf16 v[24:27], a[212:215], a[128:131], v[24:27]\n" \
"ds_read_b64_tr_b16 a[200:201], v241 offset:49152\n" \
"v_mfma_f32_16x16x32_bf16 v[56:59], a[212:215], a[132:135], v[56:59]\n" \
"buffer_load_dwordx4 a[12:15], v248, s[32:35], s37 offen offset:2048 sc1\n" \
"v_mfma_f32_16x16x32_bf16 v[56:59], a[208:211], a[244:247], v[56:59]\n" \
"ds_read_b64_tr_b16 a[202:203], v241 offset:51200\n" \
"v_mfma_f32_16x16x32_bf16 v[88:91], a[208:211], a[248:251], v[88:91]\n" \
"ds_read_b64_tr_b16 a[204:205], v241 offset:57344\n" \
"v_mfma_f32_16x16x32_bf16 v[88:91], a[212:215], a[136:139], v[88:91]\n" \
"v_accvgpr_read_b32 v144, a24\n" \
"v_accvgpr_read_b32 v145, a25\n" \
"v_mfma_f32_16x16x32_bf16 v[120:123], a[212:215], a[140:143], v[120:123]\n" \
"ds_read_b64_tr_b16 a[206:207], v241 offset:59392\n" \
"v_accvgpr_read_b32 v146, a26\n" \
"v_mfma_f32_16x16x32_bf16 v[120:123], a[208:211], a[252:255], v[120:123]\n" \
"v_accvgpr_read_b32 v147, a27\n" \
"v_accvgpr_read_b32 v148, a28\n" \
"v_mfma_f32_16x16x32_bf16 v[28:31], a[216:219], a[240:243], v[28:31]\n" \
"ds_read_b64_tr_b16 a[208:209], v240 offset:49664\n" \
"v_accvgpr_read_b32 v149, a29\n" \
"v_mfma_f32_16x16x32_bf16 v[28:31], a[220:223], a[128:131], v[28:31]\n" \
"v_accvgpr_read_b32 v150, a30\n" \
"v_accvgpr_read_b32 v151, a31\n" \
"v_mfma_f32_16x16x32_bf16 v[60:63], a[220:223], a[132:135], v[60:63]\n" \
"ds_read_b64_tr_b16 a[210:211], v240 offset:51712\n" \
"v_max3_f32 v250, |v144|, |v145|, v250\n" \
"v_mfma_f32_16x16x32_bf16 v[60:63], a[216:219], a[244:247], v[60:63]\n" \
"v_max3_f32 v250, |v146|, |v147|, v250\n" \
"v_max3_f32 v250, |v148|, |v149|, v250\n" \
"v_mfma_f32_16x16x32_bf16 v[92:95], a[216:219], a[248:251], v[92:95]\n" \
"ds_read_b64_tr_b16 a[212:213], v240 offset:57856\n" \
"v_max3_f32 v250, |v150|, |v151|, v250\n" \
"v_mfma_f32_16x16x32_bf16 v[92:95], a[220:223], a[136:139], v[92:95]\n" \
"v_cvt_pk_bf16_f32 v144, v144, v145\n" \
"v_cvt_pk_bf16_f32 v145, v146, v147\n" \
"v_mfma_f32_16x16x32_bf16 v[124:127], a[220:223], a[140:143], v[124:127]\n" \
"s_waitcnt vmcnt(20)\n" \
"ds_read_b64_tr_b16 a[214:215], v240 offset:59904\n" \
"v_mfma_f32_16x16x32_bf16 v[124:127], a[216:219], a[252:255], v[124:127]\n" \
"s_waitcnt lgkmcnt(8)\n" \
"v_cvt_pk_bf16_f32 v146, v148, v149\n" \
"v_cvt_pk_bf16_f32 v147, v150, v151\n" \
"v_mfma_f32_16x16x32_bf16 v[0:3], a[192:195], a[144:147], v[0:3]\n" \
"ds_read_b64_tr_b16 a[216:217], v241 offset:49664\n" \
"v_mfma_f32_16x16x32_bf16 v[0:3], a[196:199], v[192:195], v[0:3]\n" \
"ds_write_b128 v249, v[144:147] offset:3072\n" \
"v_mfma_f32_16x16x32_bf16 v[32:35], a[196:199], v[196:199], v[32:35]\n" \
"buffer_load_dwordx4 a[16:19], v248, s[32:35], s79 offen offset:3072 sc1\n" \
"v_mfma_f32_16x16x32_bf16 v[32:35], a[192:195], a[148:151], v[32:35]\n" \
"s_add_u32 m0, s70, 33792\n" \
"ds_read_b64_tr_b16 a[218:219], v241 offset:51712\n" \
"v_accvgpr_read_b32 v152, a88\n" \
"v_mfma_f32_16x16x32_bf16 v[64:67], a[192:195], a[152:155], v[64:67]\n" \
"s_add_u32 s74, s69, 3145856\n" \
"ds_read_b64_tr_b16 a[220:221], v241 offset:57856\n" \
"v_accvgpr_read_b32 v153, a89\n" \
"v_mfma_f32_16x16x32_bf16 v[64:67], a[196:199], v[200:203], v[64:67]\n" \
"buffer_load_dwordx4 v246, s[28:31], s74 offen lds\n" \
"v_mfma_f32_16x16x32_bf16 v[96:99], a[196:199], v[204:207], v[96:99]\n" \
"buffer_load_dwordx4 a[20:23], v248, s[32:35], s80 offen offset:3072 sc1\n" \
"v_accvgpr_read_b32 v154, a90\n" \
"v_mfma_f32_16x16x32_bf16 v[96:99], a[192:195], a[156:159], v[96:99]\n" \
"s_waitcnt lgkmcnt(8)\n" \
"ds_read_b64_tr_b16 a[222:223], v241 offset:59904\n" \
"v_accvgpr_read_b32 v155, a91\n" \
"v_mfma_f32_16x16x32_bf16 v[4:7], a[200:203], a[144:147], v[4:7]\n" \
"s_add_u32 m0, s70, 40960\n" \
"ds_read_b64_tr_b16 a[192:193], v240 offset:50176\n" \
"v_accvgpr_read_b32 v156, a92\n" \
"v_mfma_f32_16x16x32_bf16 v[4:7], a[204:207], v[192:195], v[4:7]\n" \
"s_add_u32 s74, s69, 3407872\n" \
"v_accvgpr_read_b32 v157, a93\n" \
"v_accvgpr_read_b32 v158, a94\n" \
"v_mfma_f32_16x16x32_bf16 v[36:39], a[204:207], v[196:199], v[36:39]\n" \
"s_add_u32 s85, s71, 589824\n" \
"buffer_load_dwordx4 v246, s[28:31], s74 offen lds\n" \
"v_mfma_f32_16x16x32_bf16 v[36:39], a[200:203], a[148:151], v[36:39]\n" \
"buffer_load_dwordx4 a[24:27], v248, s[32:35], s36 offen offset:3072 sc1\n" \
"v_accvgpr_read_b32 v159, a95\n" \
"v_mfma_f32_16x16x32_bf16 v[68:71], a[200:203], a[152:155], v[68:71]\n" \
"ds_read_b64_tr_b16 a[194:195], v240 offset:52224\n" \
"ds_read_b64_tr_b16 a[196:197], v240 offset:58368\n" \
"v_mfma_f32_16x16x32_bf16 v[68:71], a[204:207], v[200:203], v[68:71]\n" \
"v_max3_f32 v250, |v152|, |v153|, v250\n" \
"v_max3_f32 v250, |v154|, |v155|, v250\n" \
"v_mfma_f32_16x16x32_bf16 v[100:103], a[204:207], v[204:207], v[100:103]\n" \
"buffer_store_dwordx4 v[144:147], v248, s[32:35], s85 offen offset:3072 sc1\n" \
"v_mfma_f32_16x16x32_bf16 v[100:103], a[200:203], a[156:159], v[100:103]\n" \
"s_waitcnt lgkmcnt(8)\n" \
"ds_read_b64_tr_b16 a[198:199], v240 offset:60416\n" \
"v_max3_f32 v250, |v156|, |v157|, v250\n" \
"v_mfma_f32_16x16x32_bf16 v[8:11], a[208:211], a[144:147], v[8:11]\n" \
"ds_read_b64_tr_b16 a[200:201], v241 offset:50176\n" \
"v_max3_f32 v250, |v158|, |v159|, v250\n" \
"v_mfma_f32_16x16x32_bf16 v[8:11], a[212:215], v[192:195], v[8:11]\n" \
"v_cvt_pk_bf16_f32 v152, v152, v153\n" \
"v_cvt_pk_bf16_f32 v153, v154, v155\n" \
"v_mfma_f32_16x16x32_bf16 v[40:43], a[212:215], v[196:199], v[40:43]\n" \
"buffer_load_dwordx4 a[28:31], v248, s[32:35], s37 offen offset:3072 sc1\n" \
"v_cvt_pk_bf16_f32 v154, v156, v157\n" \
"v_mfma_f32_16x16x32_bf16 v[40:43], a[208:211], a[148:151], v[40:43]\n" \
"ds_read_b64_tr_b16 a[202:203], v241 offset:52224\n" \
"v_cvt_pk_bf16_f32 v155, v158, v159\n" \
"v_mfma_f32_16x16x32_bf16 v[72:75], a[208:211], a[152:155], v[72:75]\n" \
"ds_read_b64_tr_b16 a[204:205], v241 offset:58368\n" \
"v_accvgpr_write_b32 a88, v152\n" \
"v_mfma_f32_16x16x32_bf16 v[72:75], a[212:215], v[200:203], v[72:75]\n" \
"v_accvgpr_write_b32 a89, v153\n" \
"v_accvgpr_write_b32 a90, v154\n" \
"v_mfma_f32_16x16x32_bf16 v[104:107], a[212:215], v[204:207], v[104:107]\n" \
"s_add_u32 m0, s70, 41984\n" \
"ds_read_b64_tr_b16 a[206:207], v241 offset:60416\n" \
"v_accvgpr_write_b32 a91, v155\n" \
"v_mfma_f32_16x16x32_bf16 v[104:107], a[208:211], a[156:159], v[104:107]\n" \
"s_waitcnt lgkmcnt(8)\n" \
"s_add_u32 s74, s69, 3408000\n" \
"v_mfma_f32_16x16x32_bf16 v[12:15], a[216:219], a[144:147], v[12:15]\n" \
"buffer_load_dwordx4 v246, s[28:31], s74 offen lds\n" \
"v_mfma_f32_16x16x32_bf16 v[12:15], a[220:223], v[192:195], v[12:15]\n" \
"s_add_u32 s79, s79, 4096\n" \
"ds_read_b64_tr_b16 a[208:209], v240 offset:50688\n" \
"v_mfma_f32_16x16x32_bf16 v[44:47], a[220:223], v[196:199], v[44:47]\n" \
"buffer_load_dwordx4 a[128:131], v248, s[32:35], s79 offen offset:0 sc1\n" \
"v_mfma_f32_16x16x32_bf16 v[44:47], a[216:219], a[148:151], v[44:47]\n" \
"ds_read_b64_tr_b16 a[210:211], v240 offset:52736\n" \
"v_mfma_f32_16x16x32_bf16 v[76:79], a[216:219], a[152:155], v[76:79]\n" \
"ds_read_b64_tr_b16 a[212:213], v240 offset:58880\n" \
"v_mfma_f32_16x16x32_bf16 v[76:79], a[220:223], v[200:203], v[76:79]\n" \
"v_mfma_f32_16x16x32_bf16 v[108:111], a[220:223], v[204:207], v[108:111]\n" \
"ds_read_b64_tr_b16 a[214:215], v240 offset:60928\n" \
"v_mfma_f32_16x16x32_bf16 v[108:111], a[216:219], a[156:159], v[108:111]\n" \
"s_waitcnt lgkmcnt(8)\n" \
"v_mfma_f32_16x16x32_bf16 v[16:19], a[192:195], a[144:147], v[16:19]\n" \
"ds_read_b64_tr_b16 a[216:217], v241 offset:50688\n" \
"v_mfma_f32_16x16x32_bf16 v[16:19], a[196:199], v[192:195], v[16:19]\n" \
"s_add_u32 s80, s80, 4096\n" \
"v_mfma_f32_16x16x32_bf16 v[48:51], a[196:199], v[196:199], v[48:51]\n" \
"buffer_load_dwordx4 a[132:135], v248, s[32:35], s80 offen offset:0 sc1\n" \
"v_mfma_f32_16x16x32_bf16 v[48:51], a[192:195], a[148:151], v[48:51]\n" \
"ds_read_b64_tr_b16 a[218:219], v241 offset:52736\n" \
"ds_read_b64_tr_b16 a[220:221], v241 offset:58880\n" \
"v_mfma_f32_16x16x32_bf16 v[80:83], a[192:195], a[152:155], v[80:83]\n" \
"ds_read_b64_tr_b16 a[222:223], v241 offset:60928\n" \
"v_xor_b32 v240, 0x30000, v240\n" \
"v_mfma_f32_16x16x32_bf16 v[80:83], a[196:199], v[200:203], v[80:83]\n" \
"v_xor_b32 v241, 0x30000, v241\n" \
"v_mfma_f32_16x16x32_bf16 v[112:115], a[196:199], v[204:207], v[112:115]\n" \
"s_add_u32 s85, s94, 0\n" \
"v_mfma_f32_16x16x32_bf16 v[112:115], a[192:195], a[156:159], v[112:115]\n" \
"s_waitcnt lgkmcnt(8)\n" \
"buffer_store_dwordx4 v[172:175], v248, s[32:35], s85 offen offset:3072 sc1\n" \
"v_mfma_f32_16x16x32_bf16 v[20:23], a[200:203], a[144:147], v[20:23]\n" \
"v_mfma_f32_16x16x32_bf16 v[20:23], a[204:207], v[192:195], v[20:23]\n" \
"s_add_u32 s36, s36, 4096\n" \
"v_mfma_f32_16x16x32_bf16 v[52:55], a[204:207], v[196:199], v[52:55]\n" \
"buffer_load_dwordx4 a[136:139], v248, s[32:35], s36 offen offset:0 sc1\n" \
"v_mfma_f32_16x16x32_bf16 v[52:55], a[200:203], a[148:151], v[52:55]\n" \
"v_mfma_f32_16x16x32_bf16 v[84:87], a[200:203], a[152:155], v[84:87]\n" \
"s_waitcnt lgkmcnt(0)\n" \
"s_barrier\n" \
"s_add_u32 m0, s70, 49152\n" \
"v_mfma_f32_16x16x32_bf16 v[84:87], a[204:207], v[200:203], v[84:87]\n" \
"s_add_u32 s74, s69, 3670016\n" \
"ds_read_b64_tr_b16 a[192:193], v240 offset:0\n" \
"ds_read_b64_tr_b16 a[194:195], v240 offset:2048\n" \
"v_mfma_f32_16x16x32_bf16 v[116:119], a[204:207], v[204:207], v[116:119]\n" \
"buffer_load_dwordx4 v246, s[28:31], s74 offen lds\n" \
"v_mfma_f32_16x16x32_bf16 v[116:119], a[200:203], a[156:159], v[116:119]\n" \
"ds_read_b64_tr_b16 a[196:197], v240 offset:8192\n" \
"ds_read_b64_tr_b16 a[198:199], v240 offset:10240\n" \
"v_mfma_f32_16x16x32_bf16 v[24:27], a[208:211], a[144:147], v[24:27]\n" \
"ds_read_b64_tr_b16 a[200:201], v241 offset:0\n" \
"v_mfma_f32_16x16x32_bf16 v[24:27], a[212:215], v[192:195], v[24:27]\n" \
"s_add_u32 s37, s37, 4096\n" \
"v_accvgpr_read_b32 v144, a32\n" \
"v_accvgpr_read_b32 v145, a33\n" \
"v_mfma_f32_16x16x32_bf16 v[56:59], a[212:215], v[196:199], v[56:59]\n" \
"buffer_load_dwordx4 a[140:143], v248, s[32:35], s37 offen offset:0 sc1\n" \
"v_accvgpr_read_b32 v146, a34\n" \
"v_mfma_f32_16x16x32_bf16 v[56:59], a[208:211], a[148:151], v[56:59]\n" \
"ds_read_b64_tr_b16 a[202:203], v241 offset:2048\n" \
"v_accvgpr_read_b32 v147, a35\n" \
"v_mfma_f32_16x16x32_bf16 v[88:91], a[208:211], a[152:155], v[88:91]\n" \
"ds_read_b64_tr_b16 a[204:205], v241 offset:8192\n" \
"v_accvgpr_read_b32 v148, a36\n" \
"v_mfma_f32_16x16x32_bf16 v[88:91], a[212:215], v[200:203], v[88:91]\n" \
"v_accvgpr_read_b32 v149, a37\n" \
"v_accvgpr_read_b32 v150, a38\n" \
"v_mfma_f32_16x16x32_bf16 v[120:123], a[212:215], v[204:207], v[120:123]\n" \
"ds_read_b64_tr_b16 a[206:207], v241 offset:10240\n" \
"v_accvgpr_read_b32 v151, a39\n" \
"v_mfma_f32_16x16x32_bf16 v[120:123], a[208:211], a[156:159], v[120:123]\n" \
"v_max3_f32 v250, |v144|, |v145|, v250\n" \
"v_max3_f32 v250, |v146|, |v147|, v250\n" \
"v_mfma_f32_16x16x32_bf16 v[28:31], a[216:219], a[144:147], v[28:31]\n" \
"ds_read_b64_tr_b16 a[208:209], v240 offset:512\n" \
"v_max3_f32 v250, |v148|, |v149|, v250\n" \
"v_mfma_f32_16x16x32_bf16 v[28:31], a[220:223], v[192:195], v[28:31]\n" \
"v_max3_f32 v250, |v150|, |v151|, v250\n" \
"v_cvt_pk_bf16_f32 v144, v144, v145\n" \
"v_mfma_f32_16x16x32_bf16 v[60:63], a[220:223], v[196:199], v[60:63]\n" \
"s_add_u32 m0, s70, 50176\n" \
"ds_read_b64_tr_b16 a[210:211], v240 offset:2560\n" \
"v_cvt_pk_bf16_f32 v145, v146, v147\n" \
"v_mfma_f32_16x16x32_bf16 v[60:63], a[216:219], a[148:151], v[60:63]\n" \
"s_add_u32 s74, s69, 3670144\n" \
"v_cvt_pk_bf16_f32 v146, v148, v149\n" \
"v_cvt_pk_bf16_f32 v147, v150, v151\n" \
"v_mfma_f32_16x16x32_bf16 v[92:95], a[216:219], a[152:155], v[92:95]\n" \
"buffer_load_dwordx4 v246, s[28:31], s74 offen lds\n" \
"v_mfma_f32_16x16x32_bf16 v[92:95], a[220:223], v[200:203], v[92:95]\n" \
"ds_read_b64_tr_b16 a[212:213], v240 offset:8704\n" \
"v_mfma_f32_16x16x32_bf16 v[124:127], a[220:223], v[204:207], v[124:127]\n" \
"s_waitcnt vmcnt(21)\n" \
"ds_read_b64_tr_b16 a[214:215], v240 offset:10752\n" \
"v_mfma_f32_16x16x32_bf16 v[124:127], a[216:219], a[156:159], v[124:127]\n" \
"s_waitcnt lgkmcnt(8)\n" \
"ds_write_b128 v249, v[144:147] offset:4096\n" \
"v_accvgpr_read_b32 v152, a96\n" \
"v_mfma_f32_16x16x32_bf16 v[0:3], a[192:195], a[160:163], v[0:3]\n" \
"ds_read_b64_tr_b16 a[216:217], v241 offset:512\n" \
"v_accvgpr_read_b32 v153, a97\n" \
"v_mfma_f32_16x16x32_bf16 v[0:3], a[196:199], a[176:179], v[0:3]\n" \
"buffer_load_dwordx4 a[144:147], v248, s[32:35], s79 offen offset:1024 sc1\n" \
"v_accvgpr_read_b32 v154, a98\n" \
"v_mfma_f32_16x16x32_bf16 v[32:35], a[196:199], a[180:183], v[32:35]\n" \
"ds_read_b64_tr_b16 a[218:219], v241 offset:2560\n" \
"v_accvgpr_read_b32 v155, a99\n" \
"v_mfma_f32_16x16x32_bf16 v[32:35], a[192:195], a[164:167], v[32:35]\n" \
"v_accvgpr_read_b32 v156, a100\n" \
"v_accvgpr_read_b32 v157, a101\n" \
"v_mfma_f32_16x16x32_bf16 v[64:67], a[192:195], a[168:171], v[64:67]\n" \
"s_add_u32 s85, s71, 593920\n" \
"ds_read_b64_tr_b16 a[220:221], v241 offset:8704\n" \
"v_accvgpr_read_b32 v158, a102\n" \
"v_mfma_f32_16x16x32_bf16 v[64:67], a[196:199], a[184:187], v[64:67]\n" \
"buffer_store_dwordx4 v[144:147], v248, s[32:35], s85 offen offset:0 sc1\n" \
"v_mfma_f32_16x16x32_bf16 v[96:99], a[196:199], a[188:191], v[96:99]\n" \
"ds_read_b64_tr_b16 a[222:223], v241 offset:10752\n" \
"v_accvgpr_read_b32 v159, a103\n" \
"v_mfma_f32_16x16x32_bf16 v[96:99], a[192:195], a[172:175], v[96:99]\n" \
"s_waitcnt lgkmcnt(9)\n" \
"v_max3_f32 v250, |v152|, |v153|, v250\n" \
"v_max3_f32 v250, |v154|, |v155|, v250\n" \
"v_mfma_f32_16x16x32_bf16 v[4:7], a[200:203], a[160:163], v[4:7]\n" \
"ds_read_b64_tr_b16 a[192:193], v240 offset:1024\n" \
"v_max3_f32 v250, |v156|, |v157|, v250\n" \
"v_mfma_f32_16x16x32_bf16 v[4:7], a[204:207], a[176:179], v[4:7]\n" \
"buffer_load_dwordx4 a[148:151], v248, s[32:35], s80 offen offset:1024 sc1\n" \
"v_max3_f32 v250, |v158|, |v159|, v250\n" \
"v_mfma_f32_16x16x32_bf16 v[36:39], a[204:207], a[180:183], v[36:39]\n" \
"s_add_u32 m0, s70, 57344\n" \
"ds_read_b64_tr_b16 a[194:195], v240 offset:3072\n" \
"v_cvt_pk_bf16_f32 v152, v152, v153\n" \
"v_mfma_f32_16x16x32_bf16 v[36:39], a[200:203], a[164:167], v[36:39]\n" \
"s_add_u32 s74, s69, 3932160\n" \
"v_cvt_pk_bf16_f32 v153, v154, v155\n" \
"v_cvt_pk_bf16_f32 v154, v156, v157\n" \
"v_mfma_f32_16x16x32_bf16 v[68:71], a[200:203], a[168:171], v[68:71]\n" \
"buffer_load_dwordx4 v246, s[28:31], s74 offen lds\n" \
"v_mfma_f32_16x16x32_bf16 v[68:71], a[204:207], a[184:187], v[68:71]\n" \
"ds_read_b64_tr_b16 a[196:197], v240 offset:9216\n" \
"v_cvt_pk_bf16_f32 v155, v158, v159\n" \
"v_mfma_f32_16x16x32_bf16 v[100:103], a[204:207], a[188:191], v[100:103]\n" \
"ds_read_b64_tr_b16 a[198:199], v240 offset:11264\n" \
"v_accvgpr_write_b32 a96, v152\n" \
"v_mfma_f32_16x16x32_bf16 v[100:103], a[200:203], a[172:175], v[100:103]\n" \
"s_waitcnt lgkmcnt(9)\n" \
"v_accvgpr_write_b32 a97, v153\n" \
"v_accvgpr_write_b32 a98, v154\n" \
"v_mfma_f32_16x16x32_bf16 v[8:11], a[208:211], a[160:163], v[8:11]\n" \
"ds_read_b64_tr_b16 a[200:201], v241 offset:1024\n" \
"v_accvgpr_write_b32 a99, v155\n" \
"v_mfma_f32_16x16x32_bf16 v[8:11], a[212:215], a[176:179], v[8:11]\n" \
"buffer_load_dwordx4 a[152:155], v248, s[32:35], s36 offen offset:1024 sc1\n" \
"v_mfma_f32_16x16x32_bf16 v[40:43], a[212:215], a[180:183], v[40:43]\n" \
"ds_read_b64_tr_b16 a[202:203], v241 offset:3072\n" \
"v_mfma_f32_16x16x32_bf16 v[40:43], a[208:211], a[164:167], v[40:43]\n" \
"v_mfma_f32_16x16x32_bf16 v[72:75], a[208:211], a[168:171], v[72:75]\n" \
"s_and_b32 s91, %[wid], 1\n" \
"s_lshl_b32 s91, s91, 4\n" \
"ds_read_b64_tr_b16 a[204:205], v241 offset:9216\n" \
"v_mfma_f32_16x16x32_bf16 v[72:75], a[212:215], a[184:187], v[72:75]\n" \
"v_and_b32 v254, 15, v251\n" \
"v_lshrrev_b32 v255, 4, v251\n" \
"v_mfma_f32_16x16x32_bf16 v[104:107], a[212:215], a[188:191], v[104:107]\n" \
"s_add_u32 m0, s70, 58368\n" \
"ds_read_b64_tr_b16 a[206:207], v241 offset:11264\n" \
"v_lshlrev_b32 v255, 2, v255\n" \
"v_mfma_f32_16x16x32_bf16 v[104:107], a[208:211], a[172:175], v[104:107]\n" \
"s_waitcnt lgkmcnt(8)\n" \
"v_sub_u32 v254, v254, v255\n" \
"v_add_u32 v254, s91, v254\n" \
"v_mfma_f32_16x16x32_bf16 v[12:15], a[216:219], a[160:163], v[12:15]\n" \
"s_add_u32 s74, s69, 3932288\n" \
"buffer_load_dwordx4 v246, s[28:31], s74 offen lds\n" \
"v_mfma_f32_16x16x32_bf16 v[12:15], a[220:223], a[176:179], v[12:15]\n" \
"buffer_load_dwordx4 a[156:159], v248, s[32:35], s37 offen offset:1024 sc1\n" \
"v_mov_b32 v255, 0xff000000\n" \
"v_mfma_f32_16x16x32_bf16 v[44:47], a[220:223], a[180:183], v[44:47]\n" \
"buffer_load_dwordx4 a[224:227], v248, s[32:35], s79 offen offset:2048 sc1\n" \
"v_cmp_gt_i32 s[96:97], 0, v254\n" \
"v_mfma_f32_16x16x32_bf16 v[44:47], a[216:219], a[164:167], v[44:47]\n" \
"ds_read_b64_tr_b16 a[208:209], v240 offset:1536\n" \
"ds_read_b64_tr_b16 a[210:211], v240 offset:3584\n" \
"v_mfma_f32_16x16x32_bf16 v[76:79], a[216:219], a[168:171], v[76:79]\n" \
"ds_read_b64_tr_b16 a[212:213], v240 offset:9728\n" \
"v_cmp_gt_i32 s[98:99], 1, v254\n" \
"v_mfma_f32_16x16x32_bf16 v[76:79], a[220:223], a[184:187], v[76:79]\n" \
"v_cndmask_b32 v224, 0, v255, s[96:97]\n" \
"v_mfma_f32_16x16x32_bf16 v[108:111], a[220:223], a[188:191], v[108:111]\n" \
"ds_read_b64_tr_b16 a[214:215], v240 offset:11776\n" \
"v_cndmask_b32 v225, 0, v255, s[98:99]\n" \
"v_mfma_f32_16x16x32_bf16 v[108:111], a[216:219], a[172:175], v[108:111]\n" \
".Lst0_6_%=:\n" \
"s_cbranch_execz .Lst0_10_%=\n" \
"s_waitcnt lgkmcnt(8)\n" \
"v_cmp_gt_i32 s[96:97], 2, v254\n" \
"v_cmp_gt_i32 s[98:99], 3, v254\n" \
"v_mfma_f32_16x16x32_bf16 v[16:19], a[192:195], a[160:163], v[16:19]\n" \
"ds_read_b64_tr_b16 a[216:217], v241 offset:1536\n" \
"v_mfma_f32_16x16x32_bf16 v[16:19], a[196:199], a[176:179], v[16:19]\n" \
"v_cndmask_b32 v226, 0, v255, s[96:97]\n" \
"v_cndmask_b32 v227, 0, v255, s[98:99]\n" \
"v_mfma_f32_16x16x32_bf16 v[48:51], a[196:199], a[180:183], v[48:51]\n" \
"s_add_u32 s85, s94, 4096\n" \
"buffer_load_dwordx4 a[228:231], v248, s[32:35], s80 offen offset:2048 sc1\n" \
"v_cmp_gt_i32 s[96:97], 16, v254\n" \
"v_mfma_f32_16x16x32_bf16 v[48:51], a[192:195], a[164:167], v[48:51]\n" \
"ds_read_b64_tr_b16 a[218:219], v241 offset:3584\n" \
"ds_read_b64_tr_b16 a[220:221], v241 offset:9728\n" \
"v_mfma_f32_16x16x32_bf16 v[80:83], a[192:195], a[168:171], v[80:83]\n" \
"ds_read_b64_tr_b16 a[222:223], v241 offset:11776\n" \
"v_cmp_gt_i32 s[98:99], 17, v254\n" \
"v_mfma_f32_16x16x32_bf16 v[80:83], a[196:199], a[184:187], v[80:83]\n" \
"buffer_store_dwordx4 v[176:179], v248, s[32:35], s85 offen offset:0 sc1\n" \
"v_mfma_f32_16x16x32_bf16 v[112:115], a[196:199], a[188:191], v[112:115]\n" \
"v_cndmask_b32 v228, 0, v255, s[96:97]\n" \
"v_cndmask_b32 v229, 0, v255, s[98:99]\n" \
"v_mfma_f32_16x16x32_bf16 v[112:115], a[192:195], a[172:175], v[112:115]\n" \
"s_waitcnt lgkmcnt(8)\n" \
"v_cmp_gt_i32 s[96:97], 18, v254\n" \
"v_cmp_gt_i32 s[98:99], 19, v254\n" \
"v_mfma_f32_16x16x32_bf16 v[20:23], a[200:203], a[160:163], v[20:23]\n" \
"v_mfma_f32_16x16x32_bf16 v[20:23], a[204:207], a[176:179], v[20:23]\n" \
"v_cndmask_b32 v230, 0, v255, s[96:97]\n" \
"v_cndmask_b32 v231, 0, v255, s[98:99]\n" \
"v_mfma_f32_16x16x32_bf16 v[52:55], a[204:207], a[180:183], v[52:55]\n" \
"s_lshl_b32 s91, s91, 1\n" \
"s_sub_u32 s91, 16, s91\n" \
"buffer_load_dwordx4 a[232:235], v248, s[32:35], s36 offen offset:2048 sc1\n" \
"v_mfma_f32_16x16x32_bf16 v[52:55], a[200:203], a[164:167], v[52:55]\n" \
"v_add_u32 v254, s91, v254\n" \
"v_cmp_gt_i32 s[96:97], 0, v254\n" \
"v_mfma_f32_16x16x32_bf16 v[84:87], a[200:203], a[168:171], v[84:87]\n" \
"s_waitcnt lgkmcnt(0)\n" \
"s_barrier\n" \
"v_cmp_gt_i32 s[98:99], 1, v254\n" \
"v_mfma_f32_16x16x32_bf16 v[84:87], a[204:207], a[184:187], v[84:87]\n" \
"ds_read_b64_tr_b16 a[192:193], v240 offset:16384\n" \
"ds_read_b64_tr_b16 a[194:195], v240 offset:18432\n" \
"v_mfma_f32_16x16x32_bf16 v[116:119], a[204:207], a[188:191], v[116:119]\n" \
"ds_read_b64_tr_b16 a[196:197], v240 offset:24576\n" \
"ds_read_b64_tr_b16 a[198:199], v240 offset:26624\n" \
"v_mfma_f32_16x16x32_bf16 v[116:119], a[200:203], a[172:175], v[116:119]\n" \
"v_cndmask_b32 v232, 0, v255, s[96:97]\n" \
"v_cndmask_b32 v233, 0, v255, s[98:99]\n" \
"v_mfma_f32_16x16x32_bf16 v[24:27], a[208:211], a[160:163], v[24:27]\n" \
"ds_read_b64_tr_b16 a[200:201], v241 offset:16384\n" \
"v_cmp_gt_i32 s[96:97], 2, v254\n" \
"v_mfma_f32_16x16x32_bf16 v[24:27], a[212:215], a[176:179], v[24:27]\n" \
"v_cmp_gt_i32 s[98:99], 3, v254\n" \
"v_accvgpr_read_b32 v144, a40\n" \
"v_mfma_f32_16x16x32_bf16 v[56:59], a[212:215], a[180:183], v[56:59]\n" \
"buffer_load_dwordx4 a[236:239], v248, s[32:35], s37 offen offset:2048 sc1\n" \
"v_cndmask_b32 v234, 0, v255, s[96:97]\n" \
"v_mfma_f32_16x16x32_bf16 v[56:59], a[208:211], a[164:167], v[56:59]\n" \
"ds_read_b64_tr_b16 a[202:203], v241 offset:18432\n" \
"v_cndmask_b32 v235, 0, v255, s[98:99]\n" \
"v_mfma_f32_16x16x32_bf16 v[88:91], a[208:211], a[168:171], v[88:91]\n" \
"ds_read_b64_tr_b16 a[204:205], v241 offset:24576\n" \
"v_cmp_gt_i32 s[96:97], 16, v254\n" \
"v_mfma_f32_16x16x32_bf16 v[88:91], a[212:215], a[184:187], v[88:91]\n" \
"v_cmp_gt_i32 s[98:99], 17, v254\n" \
"v_accvgpr_read_b32 v145, a41\n" \
"v_mfma_f32_16x16x32_bf16 v[120:123], a[212:215], a[188:191], v[120:123]\n" \
"ds_read_b64_tr_b16 a[206:207], v241 offset:26624\n" \
"v_cndmask_b32 v236, 0, v255, s[96:97]\n" \
"v_mfma_f32_16x16x32_bf16 v[120:123], a[208:211], a[172:175], v[120:123]\n" \
"v_cndmask_b32 v237, 0, v255, s[98:99]\n" \
"v_cmp_gt_i32 s[96:97], 18, v254\n" \
"v_mfma_f32_16x16x32_bf16 v[28:31], a[216:219], a[160:163], v[28:31]\n" \
"ds_read_b64_tr_b16 a[208:209], v240 offset:16896\n" \
"v_cmp_gt_i32 s[98:99], 19, v254\n" \
"v_mfma_f32_16x16x32_bf16 v[28:31], a[220:223], a[176:179], v[28:31]\n" \
"v_cndmask_b32 v238, 0, v255, s[96:97]\n" \
"v_accvgpr_read_b32 v146, a42\n" \
"v_mfma_f32_16x16x32_bf16 v[60:63], a[220:223], a[180:183], v[60:63]\n" \
"buffer_load_dwordx4 a[240:243], v248, s[32:35], s79 offen offset:3072 sc1\n" \
"v_cndmask_b32 v239, 0, v255, s[98:99]\n" \
"v_mfma_f32_16x16x32_bf16 v[60:63], a[216:219], a[164:167], v[60:63]\n" \
"ds_read_b64_tr_b16 a[210:211], v240 offset:18944\n" \
"v_accvgpr_read_b32 v147, a43\n" \
"v_mfma_f32_16x16x32_bf16 v[92:95], a[216:219], a[168:171], v[92:95]\n" \
"ds_read_b64_tr_b16 a[212:213], v240 offset:25088\n" \
"v_accvgpr_read_b32 v148, a44\n" \
"v_mfma_f32_16x16x32_bf16 v[92:95], a[220:223], a[184:187], v[92:95]\n" \
"v_accvgpr_read_b32 v149, a45\n" \
"v_accvgpr_read_b32 v150, a46\n" \
"v_mfma_f32_16x16x32_bf16 v[124:127], a[220:223], a[188:191], v[124:127]\n" \
"s_waitcnt vmcnt(21)\n" \
"ds_read_b64_tr_b16 a[214:215], v240 offset:27136\n" \
"v_mfma_f32_16x16x32_bf16 v[124:127], a[216:219], a[172:175], v[124:127]\n" \
"s_waitcnt lgkmcnt(8)\n" \
"v_accvgpr_read_b32 v151, a47\n" \
"v_max3_f32 v250, |v144|, |v145|, v250\n" \
"v_mfma_f32_16x16x32_bf16 v[0:3], a[192:195], a[0:3], v[0:3]\n" \
"s_add_u32 s85, s71, 593920\n" \
"ds_read_b64_tr_b16 a[216:217], v241 offset:16896\n" \
"v_max3_f32 v250, |v146|, |v147|, v250\n" \
"v_mfma_f32_16x16x32_bf16 v[0:3], a[196:199], a[16:19], v[0:3]\n" \
"ds_read_b128 a[160:163], v248 offset:8192\n" \
"v_max3_f32 v250, |v148|, |v149|, v250\n" \
"v_mfma_f32_16x16x32_bf16 v[32:35], a[196:199], a[20:23], v[32:35]\n" \
"buffer_load_dwordx4 a[244:247], v248, s[32:35], s80 offen offset:3072 sc1\n" \
"v_max3_f32 v250, |v150|, |v151|, v250\n" \
"v_mfma_f32_16x16x32_bf16 v[32:35], a[192:195], a[4:7], v[32:35]\n" \
"ds_read_b64_tr_b16 a[218:219], v241 offset:18944\n" \
"v_cvt_pk_bf16_f32 v144, v144, v145\n" \
"v_mfma_f32_16x16x32_bf16 v[64:67], a[192:195], a[8:11], v[64:67]\n" \
"ds_read_b64_tr_b16 a[220:221], v241 offset:25088\n" \
"ds_read_b128 a[164:167], v248 offset:9216\n" \
"v_mfma_f32_16x16x32_bf16 v[64:67], a[196:199], a[24:27], v[64:67]\n" \
"v_cvt_pk_bf16_f32 v145, v146, v147\n" \
"v_cvt_pk_bf16_f32 v146, v148, v149\n" \
"v_mfma_f32_16x16x32_bf16 v[96:99], a[196:199], a[28:31], v[96:99]\n" \
"ds_read_b64_tr_b16 a[222:223], v241 offset:27136\n" \
"v_cvt_pk_bf16_f32 v147, v150, v151\n" \
"v_mfma_f32_16x16x32_bf16 v[96:99], a[192:195], a[12:15], v[96:99]\n" \
"s_waitcnt lgkmcnt(10)\n" \
"buffer_store_dwordx4 v[144:147], v248, s[32:35], s85 offen offset:1024 sc1\n" \
"v_mfma_f32_16x16x32_bf16 v[4:7], a[200:203], a[0:3], v[4:7]\n" \
"ds_read_b64_tr_b16 a[192:193], v240 offset:17408\n" \
"ds_read_b128 a[168:171], v248 offset:10240\n" \
"v_mfma_f32_16x16x32_bf16 v[4:7], a[204:207], a[16:19], v[4:7]\n" \
"ds_write_b128 v249, v[144:147] offset:5120\n" \
"v_accvgpr_read_b32 v152, a104\n" \
"v_mfma_f32_16x16x32_bf16 v[36:39], a[204:207], a[20:23], v[36:39]\n" \
"buffer_load_dwordx4 a[248:251], v248, s[32:35], s36 offen offset:3072 sc1\n" \
"v_accvgpr_read_b32 v153, a105\n" \
"v_mfma_f32_16x16x32_bf16 v[36:39], a[200:203], a[4:7], v[36:39]\n" \
"ds_read_b64_tr_b16 a[194:195], v240 offset:19456\n" \
"ds_read_b128 a[172:175], v248 offset:11264\n" \
"v_mfma_f32_16x16x32_bf16 v[68:71], a[200:203], a[8:11], v[68:71]\n" \
"ds_read_b64_tr_b16 a[196:197], v240 offset:25600\n" \
"v_accvgpr_read_b32 v154, a106\n" \
"v_mfma_f32_16x16x32_bf16 v[68:71], a[204:207], a[24:27], v[68:71]\n" \
"ds_read_b128 a[188:191], v248 offset:15360\n" \
"v_accvgpr_read_b32 v155, a107\n" \
"v_mfma_f32_16x16x32_bf16 v[100:103], a[204:207], a[28:31], v[100:103]\n" \
"ds_read_b64_tr_b16 a[198:199], v240 offset:27648\n" \
"v_accvgpr_read_b32 v156, a108\n" \
"v_mfma_f32_16x16x32_bf16 v[100:103], a[200:203], a[12:15], v[100:103]\n" \
"s_waitcnt lgkmcnt(14)\n" \
"v_accvgpr_read_b32 v157, a109\n" \
"v_accvgpr_read_b32 v158, a110\n" \
"v_mfma_f32_16x16x32_bf16 v[8:11], a[208:211], a[0:3], v[8:11]\n" \
"ds_read_b64_tr_b16 a[200:201], v241 offset:17408\n" \
"ds_read_b128 a[184:187], v248 offset:14336\n" \
"v_mfma_f32_16x16x32_bf16 v[8:11], a[212:215], a[16:19], v[8:11]\n" \
"v_accvgpr_read_b32 v159, a111\n" \
"v_max3_f32 v250, |v152|, |v153|, v250\n" \
"v_mfma_f32_16x16x32_bf16 v[40:43], a[212:215], a[20:23], v[40:43]\n" \
"buffer_load_dwordx4 a[252:255], v248, s[32:35], s37 offen offset:3072 sc1\n" \
"v_max3_f32 v250, |v154|, |v155|, v250\n" \
"v_mfma_f32_16x16x32_bf16 v[40:43], a[208:211], a[4:7], v[40:43]\n" \
"ds_read_b64_tr_b16 a[202:203], v241 offset:19456\n" \
"ds_read_b128 a[180:183], v248 offset:13312\n" \
"v_mfma_f32_16x16x32_bf16 v[72:75], a[208:211], a[8:11], v[72:75]\n" \
"ds_read_b64_tr_b16 a[204:205], v241 offset:25600\n" \
"v_max3_f32 v250, |v156|, |v157|, v250\n" \
"v_mfma_f32_16x16x32_bf16 v[72:75], a[212:215], a[24:27], v[72:75]\n" \
"v_max3_f32 v250, |v158|, |v159|, v250\n" \
"v_cvt_pk_bf16_f32 v152, v152, v153\n" \
"v_mfma_f32_16x16x32_bf16 v[104:107], a[212:215], a[28:31], v[104:107]\n" \
"ds_read_b64_tr_b16 a[206:207], v241 offset:27648\n" \
"ds_read_b128 a[176:179], v248 offset:12288\n" \
"v_mfma_f32_16x16x32_bf16 v[104:107], a[208:211], a[12:15], v[104:107]\n" \
"s_waitcnt lgkmcnt(15)\n" \
"v_cvt_pk_bf16_f32 v153, v154, v155\n" \
"v_cvt_pk_bf16_f32 v154, v156, v157\n" \
"v_mfma_f32_16x16x32_bf16 v[12:15], a[216:219], a[0:3], v[12:15]\n" \
"ds_read_b64_tr_b16 a[208:209], v240 offset:17920\n" \
"v_cvt_pk_bf16_f32 v155, v158, v159\n" \
"v_mfma_f32_16x16x32_bf16 v[12:15], a[220:223], a[16:19], v[12:15]\n" \
"v_accvgpr_write_b32 a104, v152\n" \
"v_accvgpr_write_b32 a105, v153\n" \
"v_mfma_f32_16x16x32_bf16 v[44:47], a[220:223], a[20:23], v[44:47]\n" \
"ds_read_b64_tr_b16 a[210:211], v240 offset:19968\n" \
"v_accvgpr_write_b32 a106, v154\n" \
"v_mfma_f32_16x16x32_bf16 v[44:47], a[216:219], a[4:7], v[44:47]\n" \
"v_accvgpr_write_b32 a107, v155\n" \
"v_mfma_f32_16x16x32_bf16 v[76:79], a[216:219], a[8:11], v[76:79]\n" \
"ds_read_b64_tr_b16 a[212:213], v240 offset:26112\n" \
"v_mfma_f32_16x16x32_bf16 v[76:79], a[220:223], a[24:27], v[76:79]\n" \
"v_mfma_f32_16x16x32_bf16 v[108:111], a[220:223], a[28:31], v[108:111]\n" \
"s_add_u32 s85, s94, 4096\n" \
"ds_read_b64_tr_b16 a[214:215], v240 offset:28160\n" \
"v_mfma_f32_16x16x32_bf16 v[108:111], a[216:219], a[12:15], v[108:111]\n" \
"s_waitcnt lgkmcnt(11)\n" \
"buffer_store_dwordx4 v[180:183], v248, s[32:35], s85 offen offset:1024 sc1\n" \
"v_mfma_f32_16x16x32_bf16 v[16:19], a[192:195], a[0:3], v[16:19]\n" \
"ds_read_b64_tr_b16 a[216:217], v241 offset:17920\n" \
"v_mfma_f32_16x16x32_bf16 v[16:19], a[196:199], a[16:19], v[16:19]\n" \
"v_mfma_f32_16x16x32_bf16 v[48:51], a[196:199], a[20:23], v[48:51]\n" \
"ds_read_b64_tr_b16 a[218:219], v241 offset:19968\n" \
"v_mfma_f32_16x16x32_bf16 v[48:51], a[192:195], a[4:7], v[48:51]\n" \
"ds_read_b64_tr_b16 a[220:221], v241 offset:26112\n" \
"v_mfma_f32_16x16x32_bf16 v[80:83], a[192:195], a[8:11], v[80:83]\n" \
"ds_read_b64_tr_b16 a[222:223], v241 offset:28160\n" \
"v_xor_b32 v240, 0x30000, v240\n" \
"v_mfma_f32_16x16x32_bf16 v[80:83], a[196:199], a[24:27], v[80:83]\n" \
"v_xor_b32 v241, 0x30000, v241\n" \
"v_mfma_f32_16x16x32_bf16 v[112:115], a[196:199], a[28:31], v[112:115]\n" \
"v_mfma_f32_16x16x32_bf16 v[112:115], a[192:195], a[12:15], v[112:115]\n" \
"s_waitcnt lgkmcnt(9)\n" \
"v_mfma_f32_16x16x32_bf16 v[20:23], a[200:203], a[0:3], v[20:23]\n" \
"v_mfma_f32_16x16x32_bf16 v[20:23], a[204:207], a[16:19], v[20:23]\n" \
"v_accvgpr_read_b32 v144, a48\n" \
"v_accvgpr_read_b32 v145, a49\n" \
"v_mfma_f32_16x16x32_bf16 v[52:55], a[204:207], a[20:23], v[52:55]\n" \
"v_accvgpr_read_b32 v146, a50\n" \
"v_accvgpr_read_b32 v147, a51\n" \
"v_mfma_f32_16x16x32_bf16 v[52:55], a[200:203], a[4:7], v[52:55]\n" \
"v_accvgpr_read_b32 v148, a52\n" \
"v_accvgpr_read_b32 v149, a53\n" \
"v_mfma_f32_16x16x32_bf16 v[84:87], a[200:203], a[8:11], v[84:87]\n" \
"s_waitcnt vmcnt(25) lgkmcnt(0)\n" \
"s_barrier\n" \
"v_accvgpr_read_b32 v150, a54\n" \
"v_mfma_f32_16x16x32_bf16 v[84:87], a[204:207], a[24:27], v[84:87]\n" \
"ds_read_b64_tr_b16 a[192:193], v240 offset:32768\n" \
"ds_read_b64_tr_b16 a[194:195], v240 offset:34816\n" \
"v_mfma_f32_16x16x32_bf16 v[116:119], a[204:207], a[28:31], v[116:119]\n" \
"ds_read_b64_tr_b16 a[196:197], v240 offset:40960\n" \
"ds_read_b64_tr_b16 a[198:199], v240 offset:43008\n" \
"v_mfma_f32_16x16x32_bf16 v[116:119], a[200:203], a[12:15], v[116:119]\n" \
"v_accvgpr_read_b32 v151, a55\n" \
"v_max3_f32 v250, |v144|, |v145|, v250\n" \
"v_mfma_f32_16x16x32_bf16 v[24:27], a[208:211], a[0:3], v[24:27]\n" \
"ds_read_b64_tr_b16 a[200:201], v241 offset:32768\n" \
"v_max3_f32 v250, |v146|, |v147|, v250\n" \
"v_mfma_f32_16x16x32_bf16 v[24:27], a[212:215], a[16:19], v[24:27]\n" \
"v_max3_f32 v250, |v148|, |v149|, v250\n" \
"v_max3_f32 v250, |v150|, |v151|, v250\n" \
"v_mfma_f32_16x16x32_bf16 v[56:59], a[212:215], a[20:23], v[56:59]\n" \
"ds_read_b64_tr_b16 a[202:203], v241 offset:34816\n" \
"v_cvt_pk_bf16_f32 v144, v144, v145\n" \
"v_mfma_f32_16x16x32_bf16 v[56:59], a[208:211], a[4:7], v[56:59]\n" \
"v_cvt_pk_bf16_f32 v145, v146, v147\n" \
"v_cvt_pk_bf16_f32 v146, v148, v149\n" \
"v_mfma_f32_16x16x32_bf16 v[88:91], a[208:211], a[8:11], v[88:91]\n" \
"ds_read_b64_tr_b16 a[204:205], v241 offset:40960\n" \
"v_cvt_pk_bf16_f32 v147, v150, v151\n" \
"v_mfma_f32_16x16x32_bf16 v[88:91], a[212:215], a[24:27], v[88:91]\n" \
"ds_write_b128 v249, v[144:147] offset:6144\n" \
"v_mfma_f32_16x16x32_bf16 v[120:123], a[212:215], a[28:31], v[120:123]\n" \
"ds_read_b64_tr_b16 a[206:207], v241 offset:43008\n" \
"v_mfma_f32_16x16x32_bf16 v[120:123], a[208:211], a[12:15], v[120:123]\n" \
"v_accvgpr_read_b32 v152, a112\n" \
"v_accvgpr_read_b32 v153, a113\n" \
"v_mfma_f32_16x16x32_bf16 v[28:31], a[216:219], a[0:3], v[28:31]\n" \
"s_lshl2_add_u32 s76, s67, %[wid]\n" \
"ds_read_b64_tr_b16 a[208:209], v240 offset:33280\n" \
"v_accvgpr_read_b32 v154, a114\n" \
"v_mfma_f32_16x16x32_bf16 v[28:31], a[220:223], a[16:19], v[28:31]\n" \
"s_sub_u32 s77, 15, s67\n" \
"v_accvgpr_read_b32 v155, a115\n" \
"v_accvgpr_read_b32 v156, a116\n" \
"v_mfma_f32_16x16x32_bf16 v[60:63], a[220:223], a[20:23], v[60:63]\n" \
"s_lshl2_add_u32 s77, s77, %[wid]\n" \
"ds_read_b64_tr_b16 a[210:211], v240 offset:35328\n" \
"v_accvgpr_read_b32 v157, a117\n" \
"v_mfma_f32_16x16x32_bf16 v[60:63], a[216:219], a[4:7], v[60:63]\n" \
"s_lshl_b32 s28, %[wid], 12\n" \
"v_accvgpr_read_b32 v158, a118\n" \
"v_accvgpr_read_b32 v159, a119\n" \
"v_mfma_f32_16x16x32_bf16 v[92:95], a[216:219], a[8:11], v[92:95]\n" \
"s_add_u32 s85, s71, 593920\n" \
"ds_read_b64_tr_b16 a[212:213], v240 offset:41472\n" \
"v_max3_f32 v250, |v152|, |v153|, v250\n" \
"v_mfma_f32_16x16x32_bf16 v[92:95], a[220:223], a[24:27], v[92:95]\n" \
"s_add_u32 s88, s73, s28\n" \
"buffer_store_dwordx4 v[144:147], v248, s[32:35], s85 offen offset:2048 sc1\n" \
"v_mfma_f32_16x16x32_bf16 v[124:127], a[220:223], a[28:31], v[124:127]\n" \
"s_waitcnt vmcnt(12)\n" \
"ds_read_b64_tr_b16 a[214:215], v240 offset:43520\n" \
"v_mfma_f32_16x16x32_bf16 v[124:127], a[216:219], a[12:15], v[124:127]\n" \
"s_waitcnt lgkmcnt(9)\n" \
"v_max3_f32 v250, |v154|, |v155|, v250\n" \
"v_max3_f32 v250, |v156|, |v157|, v250\n" \
"v_mfma_f32_16x16x32_bf16 v[0:3], a[192:195], a[128:131], v[0:3]\n" \
"s_add_u32 s18, s88, 524288\n" \
"ds_read_b64_tr_b16 a[216:217], v241 offset:33280\n" \
"v_max3_f32 v250, |v158|, |v159|, v250\n" \
"v_mfma_f32_16x16x32_bf16 v[0:3], a[196:199], a[144:147], v[0:3]\n" \
"s_add_u32 s24, s88, 229376\n" \
"v_cvt_pk_bf16_f32 v152, v152, v153\n" \
"v_cvt_pk_bf16_f32 v153, v154, v155\n" \
"v_mfma_f32_16x16x32_bf16 v[32:35], a[196:199], a[148:151], v[32:35]\n" \
"s_lshl_b32 s16, s67, 15\n" \
"ds_read_b64_tr_b16 a[218:219], v241 offset:35328\n" \
"v_cvt_pk_bf16_f32 v154, v156, v157\n" \
"v_mfma_f32_16x16x32_bf16 v[32:35], a[192:195], a[132:135], v[32:35]\n" \
"s_add_u32 s16, s16, s88\n" \
"v_cvt_pk_bf16_f32 v155, v158, v159\n" \
"v_accvgpr_write_b32 a112, v152\n" \
"v_mfma_f32_16x16x32_bf16 v[64:67], a[192:195], a[136:139], v[64:67]\n" \
"s_add_u32 s21, s16, 0x8000\n" \
"ds_read_b64_tr_b16 a[220:221], v241 offset:41472\n" \
"v_accvgpr_write_b32 a113, v153\n" \
"v_mfma_f32_16x16x32_bf16 v[64:67], a[196:199], a[152:155], v[64:67]\n" \
"s_cmp_eq_u32 s88, s16\n" \
"s_cmov_b32 s88, s21\n" \
"v_accvgpr_write_b32 a114, v154\n" \
"v_mfma_f32_16x16x32_bf16 v[96:99], a[196:199], a[156:159], v[96:99]\n" \
"s_mov_b32 s19, s28\n" \
"ds_read_b64_tr_b16 a[222:223], v241 offset:43520\n" \
"v_accvgpr_write_b32 a115, v155\n" \
"v_mfma_f32_16x16x32_bf16 v[96:99], a[192:195], a[140:143], v[96:99]\n" \
"s_waitcnt lgkmcnt(8)\n" \
"s_add_u32 s20, s28, 0x10000\n" \
"s_lshl_b32 s36, s64, 6\n" \
"v_mfma_f32_16x16x32_bf16 v[4:7], a[200:203], a[128:131], v[4:7]\n" \
"s_add_u32 s36, s36, s77\n" \
"s_lshl2_add_u32 s36, s36, s66\n" \
"ds_read_b64_tr_b16 a[192:193], v240 offset:33792\n" \
"v_mfma_f32_16x16x32_bf16 v[4:7], a[204:207], a[144:147], v[4:7]\n" \
"s_lshl_b32 s36, s36, 13\n" \
"s_add_u32 s36, s36, 59768832\n" \
"s_lshl_b32 s17, s64, 19\n" \
"v_mfma_f32_16x16x32_bf16 v[36:39], a[204:207], a[148:151], v[36:39]\n" \
"s_lshl_b32 s83, s66, 8\n" \
"s_add_u32 s17, s17, s83\n" \
"ds_read_b64_tr_b16 a[194:195], v240 offset:35840\n" \
"v_mfma_f32_16x16x32_bf16 v[36:39], a[200:203], a[132:135], v[36:39]\n" \
"s_lshr_b32 s83, %[wid], 1\n" \
"s_lshl1_add_u32 s93, s66, s83\n" \
"s_add_u32 s93, s93, 6\n" \
"v_mfma_f32_16x16x32_bf16 v[68:71], a[200:203], a[136:139], v[68:71]\n" \
"s_and_b32 s93, s93, 7\n" \
"s_and_b32 s84, %[wid], 1\n" \
"ds_read_b64_tr_b16 a[196:197], v240 offset:41984\n" \
"v_mfma_f32_16x16x32_bf16 v[68:71], a[204:207], a[152:155], v[68:71]\n" \
"s_lshl1_add_u32 s93, s93, s84\n" \
"s_lshl_b32 s93, s93, 15\n" \
"s_add_u32 s93, s93, s17\n" \
"v_mfma_f32_16x16x32_bf16 v[100:103], a[204:207], a[156:159], v[100:103]\n" \
"s_lshl_b32 s83, s83, 15\n" \
"s_add_u32 s17, s17, s83\n" \
"ds_read_b64_tr_b16 a[198:199], v240 offset:44032\n" \
"v_mfma_f32_16x16x32_bf16 v[100:103], a[200:203], a[140:143], v[100:103]\n" \
"s_waitcnt lgkmcnt(8)\n" \
"s_lshl_b32 s84, s84, 7\n" \
"s_add_u32 s17, s17, s84\n" \
"v_mfma_f32_16x16x32_bf16 v[8:11], a[208:211], a[128:131], v[8:11]\n" \
"s_lshl_b32 s87, %[wid], 13\n" \
"s_add_u32 s87, s87, 0x20000\n" \
"ds_read_b64_tr_b16 a[200:201], v241 offset:33792\n" \
"v_mfma_f32_16x16x32_bf16 v[8:11], a[212:215], a[144:147], v[8:11]\n" \
"s_mov_b32 s25, 1073741824\n" \
"s_lshl_b32 s26, s68, 7\n" \
"s_add_u32 s26, s26, 655360\n" \
"v_mfma_f32_16x16x32_bf16 v[40:43], a[212:215], a[148:151], v[40:43]\n" \
"s_lshl_b32 s81, s68, 8\n" \
"s_add_u32 s81, s81, 81920\n" \
"ds_read_b64_tr_b16 a[202:203], v241 offset:35840\n" \
"v_mfma_f32_16x16x32_bf16 v[40:43], a[208:211], a[132:135], v[40:43]\n" \
"s_lshl_b32 s84, s64, 15\n" \
"s_add_u32 s84, s84, 41943040\n" \
"s_lshl_b32 s29, s76, 18\n" \
"v_mfma_f32_16x16x32_bf16 v[72:75], a[208:211], a[136:139], v[72:75]\n" \
"s_add_u32 s29, s29, s84\n" \
"s_lshl_b32 s37, s77, 18\n" \
"ds_read_b64_tr_b16 a[204:205], v241 offset:41984\n" \
"v_mfma_f32_16x16x32_bf16 v[72:75], a[212:215], a[152:155], v[72:75]\n" \
"s_add_u32 s37, s37, s84\n" \
"s_lshl_b32 s83, s66, 13\n" \
"s_add_u32 s30, s29, s83\n" \
"v_mfma_f32_16x16x32_bf16 v[104:107], a[212:215], a[156:159], v[104:107]\n" \
"s_lshl3_add_u32 s83, s67, s64\n" \
"s_lshl3_add_u32 s83, s83, %[wid]\n" \
"ds_read_b64_tr_b16 a[206:207], v241 offset:44032\n" \
"v_mfma_f32_16x16x32_bf16 v[104:107], a[208:211], a[140:143], v[104:107]\n" \
"s_waitcnt lgkmcnt(8)\n" \
"s_lshl_b32 s83, s83, 4\n" \
"s_add_u32 s31, s83, 73728\n" \
"v_mfma_f32_16x16x32_bf16 v[12:15], a[216:219], a[128:131], v[12:15]\n" \
"s_mul_i32 s69, s64, 28\n" \
"ds_read_b64_tr_b16 a[208:209], v240 offset:34304\n" \
"v_mfma_f32_16x16x32_bf16 v[12:15], a[220:223], a[144:147], v[12:15]\n" \
"s_cmp_lt_u32 s65, 28\n" \
"s_cselect_b32 s83, s65, 0\n" \
"s_add_u32 s69, s69, s83\n" \
"v_mfma_f32_16x16x32_bf16 v[44:47], a[220:223], a[148:151], v[44:47]\n" \
"s_add_u32 s85, s94, 4096\n" \
"s_lshl_b32 s79, s69, 13\n" \
"ds_read_b64_tr_b16 a[210:211], v240 offset:36352\n" \
"v_mfma_f32_16x16x32_bf16 v[44:47], a[216:219], a[132:135], v[44:47]\n" \
"s_lshl_b32 s83, %[wid], 8\n" \
"buffer_store_dwordx4 v[184:187], v248, s[32:35], s85 offen offset:2048 sc1\n" \
"v_mfma_f32_16x16x32_bf16 v[76:79], a[216:219], a[136:139], v[76:79]\n" \
"s_add_u32 s79, s79, s83\n" \
"s_add_u32 s80, s79, 4096\n" \
"ds_read_b64_tr_b16 a[212:213], v240 offset:42496\n" \
"v_mfma_f32_16x16x32_bf16 v[76:79], a[220:223], a[152:155], v[76:79]\n" \
"s_mov_b64 s[42:43], s[54:55]\n" \
"v_mfma_f32_16x16x32_bf16 v[108:111], a[220:223], a[156:159], v[108:111]\n" \
"ds_read_b64_tr_b16 a[214:215], v240 offset:44544\n" \
"v_mfma_f32_16x16x32_bf16 v[108:111], a[216:219], a[140:143], v[108:111]\n" \
"s_waitcnt lgkmcnt(8)\n" \
"v_mfma_f32_16x16x32_bf16 v[16:19], a[192:195], a[128:131], v[16:19]\n" \
"ds_read_b64_tr_b16 a[216:217], v241 offset:34304\n" \
"v_accvgpr_read_b32 v144, a56\n" \
"v_mfma_f32_16x16x32_bf16 v[16:19], a[196:199], a[144:147], v[16:19]\n" \
"s_bitcmp1_b32 %[bid], 6\n" \
"s_cselect_b64 s[40:41], s[56:57], s[52:53]\n" \
"s_cselect_b32 s39, 2048, 0\n" \
"v_mfma_f32_16x16x32_bf16 v[48:51], a[196:199], a[148:151], v[48:51]\n" \
"s_bfe_u32 s83, %[bid], 0x40002\n" \
"ds_read_b64_tr_b16 a[218:219], v241 offset:36352\n" \
"v_accvgpr_read_b32 v145, a57\n" \
"v_mfma_f32_16x16x32_bf16 v[48:51], a[192:195], a[132:135], v[48:51]\n" \
"s_and_b32 s84, %[bid], 3\n" \
"ds_read_b64_tr_b16 a[220:221], v241 offset:42496\n" \
"v_accvgpr_read_b32 v146, a58\n" \
"v_mfma_f32_16x16x32_bf16 v[80:83], a[192:195], a[136:139], v[80:83]\n" \
"s_lshl_b32 s22, %[wid], 3\n" \
"ds_read_b64_tr_b16 a[222:223], v241 offset:44544\n" \
"v_accvgpr_read_b32 v147, a59\n" \
"v_mfma_f32_16x16x32_bf16 v[80:83], a[196:199], a[152:155], v[80:83]\n" \
"s_and_b32 s22, s22, 16\n" \
"v_accvgpr_read_b32 v148, a60\n" \
"v_accvgpr_read_b32 v149, a61\n" \
"v_mfma_f32_16x16x32_bf16 v[112:115], a[196:199], a[156:159], v[112:115]\n" \
"s_and_b32 s23, %[wid], 1\n" \
"v_accvgpr_read_b32 v150, a62\n" \
"v_accvgpr_read_b32 v151, a63\n" \
"v_mfma_f32_16x16x32_bf16 v[112:115], a[192:195], a[140:143], v[112:115]\n" \
"s_waitcnt lgkmcnt(8)\n" \
"v_max3_f32 v250, |v144|, |v145|, v250\n" \
"v_max3_f32 v250, |v146|, |v147|, v250\n" \
"v_mfma_f32_16x16x32_bf16 v[20:23], a[200:203], a[128:131], v[20:23]\n" \
"s_add_u32 s38, s22, s23\n" \
"v_max3_f32 v250, |v148|, |v149|, v250\n" \
"v_max3_f32 v250, |v150|, |v151|, v250\n" \
"v_mfma_f32_16x16x32_bf16 v[20:23], a[204:207], a[144:147], v[20:23]\n" \
"s_lshl_b32 s72, s83, 5\n" \
"v_cvt_pk_bf16_f32 v144, v144, v145\n" \
"v_cvt_pk_bf16_f32 v145, v146, v147\n" \
"v_mfma_f32_16x16x32_bf16 v[52:55], a[204:207], a[148:151], v[52:55]\n" \
"s_add_u32 s38, s38, s72\n" \
"v_cvt_pk_bf16_f32 v146, v148, v149\n" \
"v_cvt_pk_bf16_f32 v147, v150, v151\n" \
"v_mfma_f32_16x16x32_bf16 v[52:55], a[200:203], a[132:135], v[52:55]\n" \
"s_mul_i32 s38, s38, 3584\n" \
"ds_write_b128 v249, v[144:147] offset:7168\n" \
"v_lshlrev_b32 v252, 2, v251\n" \
"v_mfma_f32_16x16x32_bf16 v[84:87], a[200:203], a[136:139], v[84:87]\n" \
"s_waitcnt lgkmcnt(1)\n" \
"s_barrier\n" \
"v_mov_b32 v254, 0x3f803f80\n" \
"v_mfma_f32_16x16x32_bf16 v[84:87], a[204:207], a[152:155], v[84:87]\n" \
"s_lshl_b32 s72, s84, 7\n" \
"ds_read_b64_tr_b16 a[192:193], v240 offset:49152\n" \
"ds_read_b64_tr_b16 a[194:195], v240 offset:51200\n" \
"v_mfma_f32_16x16x32_bf16 v[116:119], a[204:207], a[156:159], v[116:119]\n" \
"s_add_u32 s38, s38, s72\n" \
"ds_read_b64_tr_b16 a[196:197], v240 offset:57344\n" \
"ds_read_b64_tr_b16 a[198:199], v240 offset:59392\n" \
"v_mfma_f32_16x16x32_bf16 v[116:119], a[200:203], a[140:143], v[116:119]\n" \
"s_lshl3_add_u32 s39, s22, s39\n" \
"v_accvgpr_write_b32 a48, v254\n" \
"v_accvgpr_write_b32 a49, v254\n" \
"v_mfma_f32_16x16x32_bf16 v[24:27], a[208:211], a[128:131], v[24:27]\n" \
"s_lshl_b32 s23, s23, 10\n" \
"ds_read_b64_tr_b16 a[200:201], v241 offset:49152\n" \
"v_accvgpr_write_b32 a50, v254\n" \
"v_mfma_f32_16x16x32_bf16 v[24:27], a[212:215], a[144:147], v[24:27]\n" \
"s_add_u32 s39, s39, s23\n" \
"v_accvgpr_write_b32 a51, v254\n" \
"v_accvgpr_write_b32 a56, 1\n" \
"v_mfma_f32_16x16x32_bf16 v[56:59], a[212:215], a[148:151], v[56:59]\n" \
"s_lshl_b32 s22, s84, 17\n" \
"ds_read_b64_tr_b16 a[202:203], v241 offset:51200\n" \
"v_accvgpr_write_b32 a32, 0\n" \
"v_mfma_f32_16x16x32_bf16 v[56:59], a[208:211], a[132:135], v[56:59]\n" \
"s_add_u32 s85, s71, 593920\n" \
"v_accvgpr_write_b32 a33, 0\n" \
"v_accvgpr_write_b32 a34, 0\n" \
"v_mfma_f32_16x16x32_bf16 v[88:91], a[208:211], a[136:139], v[88:91]\n" \
"s_add_u32 s39, s39, s22\n" \
"buffer_store_dwordx4 v[144:147], v248, s[32:35], s85 offen offset:3072 sc1\n" \
"v_mfma_f32_16x16x32_bf16 v[88:91], a[212:215], a[152:155], v[88:91]\n" \
"s_lshr_b32 s22, s83, 2\n" \
"ds_read_b64_tr_b16 a[204:205], v241 offset:57344\n" \
"v_accvgpr_write_b32 a35, 0\n" \
"v_mfma_f32_16x16x32_bf16 v[120:123], a[212:215], a[156:159], v[120:123]\n" \
"s_lshl_b32 s22, s22, 12\n" \
"ds_read_b64_tr_b16 a[206:207], v241 offset:59392\n" \
"v_accvgpr_write_b32 a36, 0\n" \
"v_mfma_f32_16x16x32_bf16 v[120:123], a[208:211], a[140:143], v[120:123]\n" \
"s_add_u32 s39, s39, s22\n" \
"v_accvgpr_write_b32 a37, 0\n" \
"v_accvgpr_write_b32 a38, 0\n" \
"v_mfma_f32_16x16x32_bf16 v[28:31], a[216:219], a[128:131], v[28:31]\n" \
"s_and_b32 s22, s83, 3\n" \
"ds_read_b64_tr_b16 a[208:209], v240 offset:49664\n" \
"v_accvgpr_write_b32 a39, 0\n" \
"v_mfma_f32_16x16x32_bf16 v[28:31], a[220:223], a[144:147], v[28:31]\n" \
"s_lshl_b32 s22, s22, 8\n" \
"v_accvgpr_write_b32 a40, 0\n" \
"v_accvgpr_write_b32 a41, 0\n" \
"v_mfma_f32_16x16x32_bf16 v[60:63], a[220:223], a[148:151], v[60:63]\n" \
"s_add_u32 s39, s39, s22\n" \
"ds_read_b64_tr_b16 a[210:211], v240 offset:51712\n" \
"v_accvgpr_write_b32 a42, 0\n" \
"v_mfma_f32_16x16x32_bf16 v[60:63], a[216:219], a[132:135], v[60:63]\n" \
"s_add_u32 s39, s39, 77070336\n" \
"v_accvgpr_write_b32 a43, 0\n" \
"v_accvgpr_write_b32 a44, 0\n" \
"v_mfma_f32_16x16x32_bf16 v[92:95], a[216:219], a[136:139], v[92:95]\n" \
"s_and_b32 s83, %[bid], 127\n" \
"ds_read_b64_tr_b16 a[212:213], v240 offset:57856\n" \
"v_accvgpr_write_b32 a45, 0\n" \
"v_mfma_f32_16x16x32_bf16 v[92:95], a[220:223], a[152:155], v[92:95]\n" \
"s_lshl_b32 s78, %[wid], 7\n" \
"v_accvgpr_write_b32 a46, 0\n" \
"v_accvgpr_write_b32 a47, 0\n" \
"v_mfma_f32_16x16x32_bf16 v[124:127], a[220:223], a[156:159], v[124:127]\n" \
"s_waitcnt vmcnt(4)\n" \
"ds_read_b64_tr_b16 a[214:215], v240 offset:59904\n" \
"v_mfma_f32_16x16x32_bf16 v[124:127], a[216:219], a[140:143], v[124:127]\n" \
"s_waitcnt lgkmcnt(8)\n" \
"v_and_b32 v244, 31, v251\n" \
"v_lshlrev_b32 v244, 2, v244\n" \
"v_mfma_f32_16x16x32_bf16 v[0:3], a[192:195], a[224:227], v[0:3]\n" \
"s_add_u32 s78, s78, s83\n" \
"ds_read_b64_tr_b16 a[216:217], v241 offset:49664\n" \
"v_accvgpr_read_b32 v152, a120\n" \
"v_mfma_f32_16x16x32_bf16 v[0:3], a[196:199], a[240:243], v[0:3]\n" \
"s_lshl_b32 s78, s78, 2\n" \
"ds_read_b64_tr_b16 a[218:219], v241 offset:51712\n" \
"ds_read_b64_tr_b16 a[220:221], v241 offset:57856\n" \
"v_mfma_f32_16x16x32_bf16 v[32:35], a[196:199], a[244:247], v[32:35]\n" \
"s_add_u32 s78, s78, 659456\n" \
"ds_read_b64_tr_b16 a[222:223], v241 offset:59904\n" \
"ds_read_b128 a[128:131], v248 offset:0\n" \
"v_mfma_f32_16x16x32_bf16 v[32:35], a[192:195], a[228:231], v[32:35]\n" \
"s_waitcnt lgkmcnt(9)\n" \
"s_cmp_lt_u32 s67, 4\n" \
"s_cselect_b32 s78, 1073741824, s78\n" \
"v_mfma_f32_16x16x32_bf16 v[4:7], a[200:203], a[224:227], v[4:7]\n" \
"s_lshl2_add_u32 s72, s83, %[wid]\n" \
"ds_read_b64_tr_b16 a[192:193], v240 offset:50176\n" \
"ds_read_b128 a[132:135], v248 offset:1024\n" \
"v_mfma_f32_16x16x32_bf16 v[4:7], a[204:207], a[240:243], v[4:7]\n" \
"s_mul_i32 s72, s72, 3584\n" \
"ds_read_b64_tr_b16 a[194:195], v240 offset:52224\n" \
"ds_read_b64_tr_b16 a[196:197], v240 offset:58368\n" \
"v_mfma_f32_16x16x32_bf16 v[36:39], a[204:207], a[244:247], v[36:39]\n" \
"s_mov_b32 s76, 0\n" \
"ds_read_b64_tr_b16 a[198:199], v240 offset:60416\n" \
"v_accvgpr_read_b32 v153, a121\n" \
"v_mfma_f32_16x16x32_bf16 v[36:39], a[200:203], a[228:231], v[36:39]\n" \
"s_waitcnt lgkmcnt(10)\n" \
"ds_read_b128 a[136:139], v248 offset:2048\n" \
"v_accvgpr_read_b32 v154, a122\n" \
"v_mfma_f32_16x16x32_bf16 v[8:11], a[208:211], a[224:227], v[8:11]\n" \
"s_add_u32 s85, s94, 4096\n" \
"buffer_store_dwordx4 v[188:191], v248, s[32:35], s85 offen offset:3072 sc1\n" \
"v_mfma_f32_16x16x32_bf16 v[8:11], a[212:215], a[240:243], v[8:11]\n" \
"s_lshl_b32 s84, s67, 1\n" \
"ds_read_b64_tr_b16 a[200:201], v241 offset:50176\n" \
"ds_read_b64_tr_b16 a[202:203], v241 offset:52224\n" \
"v_mfma_f32_16x16x32_bf16 v[40:43], a[212:215], a[244:247], v[40:43]\n" \
".Lst0_7_%=:\n" \
"s_cbranch_execz .Lst0_11_%=\n" \
"s_sub_u32 s84, s84, 11\n" \
"ds_read_b64_tr_b16 a[204:205], v241 offset:58368\n" \
"ds_read_b64_tr_b16 a[206:207], v241 offset:60416\n" \
"v_mfma_f32_16x16x32_bf16 v[40:43], a[208:211], a[228:231], v[40:43]\n" \
"s_waitcnt lgkmcnt(11)\n" \
"ds_read_b128 a[140:143], v248 offset:3072\n" \
"v_accvgpr_read_b32 v155, a123\n" \
"v_mfma_f32_16x16x32_bf16 v[12:15], a[216:219], a[224:227], v[12:15]\n" \
"s_max_i32 s84, s84, 0\n" \
"ds_read_b64_tr_b16 a[208:209], v240 offset:50688\n" \
"v_accvgpr_read_b32 v156, a124\n" \
"v_mfma_f32_16x16x32_bf16 v[12:15], a[220:223], a[240:243], v[12:15]\n" \
"s_add_u32 s83, s84, s67\n" \
"ds_read_b64_tr_b16 a[210:211], v240 offset:52736\n" \
"ds_read_b64_tr_b16 a[212:213], v240 offset:58880\n" \
"v_mfma_f32_16x16x32_bf16 v[44:47], a[220:223], a[244:247], v[44:47]\n" \
"s_and_b32 s83, s83, 1\n" \
"ds_read_b64_tr_b16 a[214:215], v240 offset:60928\n" \
"ds_read_b128 a[156:159], v248 offset:7168\n" \
"v_mfma_f32_16x16x32_bf16 v[44:47], a[216:219], a[228:231], v[44:47]\n" \
"s_waitcnt lgkmcnt(11)\n" \
"v_accvgpr_read_b32 v157, a125\n" \
"v_accvgpr_read_b32 v158, a126\n" \
"v_mfma_f32_16x16x32_bf16 v[16:19], a[192:195], a[224:227], v[16:19]\n" \
"s_lshl_b32 s83, s83, 15\n" \
"ds_read_b64_tr_b16 a[216:217], v241 offset:50688\n" \
"ds_read_b128 a[152:155], v248 offset:6144\n" \
"v_mfma_f32_16x16x32_bf16 v[16:19], a[196:199], a[240:243], v[16:19]\n" \
"s_movk_i32 s27, 0x4000\n" \
"ds_read_b64_tr_b16 a[218:219], v241 offset:52736\n" \
"ds_read_b64_tr_b16 a[220:221], v241 offset:58880\n" \
"v_mfma_f32_16x16x32_bf16 v[48:51], a[196:199], a[244:247], v[48:51]\n" \
"s_mov_b32 s92, s21\n" \
"ds_read_b64_tr_b16 a[222:223], v241 offset:60928\n" \
"v_accvgpr_read_b32 v159, a127\n" \
"v_mfma_f32_16x16x32_bf16 v[48:51], a[192:195], a[228:231], v[48:51]\n" \
"s_waitcnt lgkmcnt(11)\n" \
"ds_read_b128 a[148:151], v248 offset:5120\n" \
"v_max3_f32 v250, |v152|, |v153|, v250\n" \
"v_mfma_f32_16x16x32_bf16 v[20:23], a[200:203], a[224:227], v[20:23]\n" \
"s_sub_u32 s22, s18, 524288\n" \
"ds_read_b64_tr_b16 a[192:193], v240 offset:49152\n" \
"v_max3_f32 v250, |v154|, |v155|, v250\n" \
"v_mfma_f32_16x16x32_bf16 v[20:23], a[204:207], a[240:243], v[20:23]\n" \
"s_lshl_b32 s77, s84, 16\n" \
"ds_read_b64_tr_b16 a[194:195], v240 offset:51200\n" \
"ds_read_b64_tr_b16 a[196:197], v240 offset:57344\n" \
"v_mfma_f32_16x16x32_bf16 v[52:55], a[204:207], a[244:247], v[52:55]\n" \
"s_add_u32 s77, s77, s22\n" \
"ds_read_b64_tr_b16 a[198:199], v240 offset:59392\n" \
"ds_read_b128 a[144:147], v248 offset:4096\n" \
"v_mfma_f32_16x16x32_bf16 v[52:55], a[200:203], a[228:231], v[52:55]\n" \
"s_waitcnt lgkmcnt(12)\n" \
"v_max3_f32 v250, |v156|, |v157|, v250\n" \
"v_max3_f32 v250, |v158|, |v159|, v250\n" \
"v_mfma_f32_16x16x32_bf16 v[24:27], a[208:211], a[224:227], v[24:27]\n" \
"s_movk_i32 s91, 0x4000\n" \
"ds_read_b64_tr_b16 a[200:201], v241 offset:49152\n" \
"v_cvt_pk_bf16_f32 v152, v152, v153\n" \
"v_mfma_f32_16x16x32_bf16 v[24:27], a[212:215], a[240:243], v[24:27]\n" \
"ds_read_b64_tr_b16 a[202:203], v241 offset:51200\n" \
"ds_read_b64_tr_b16 a[204:205], v241 offset:57344\n" \
"v_mfma_f32_16x16x32_bf16 v[56:59], a[212:215], a[244:247], v[56:59]\n" \
"ds_read_b64_tr_b16 a[206:207], v241 offset:59392\n" \
"v_cvt_pk_bf16_f32 v153, v154, v155\n" \
"v_mfma_f32_16x16x32_bf16 v[56:59], a[208:211], a[228:231], v[56:59]\n" \
"s_waitcnt lgkmcnt(10)\n" \
"v_cvt_pk_bf16_f32 v154, v156, v157\n" \
"v_cvt_pk_bf16_f32 v155, v158, v159\n" \
"v_mfma_f32_16x16x32_bf16 v[28:31], a[216:219], a[224:227], v[28:31]\n" \
"ds_read_b64_tr_b16 a[208:209], v240 offset:49664\n" \
"v_accvgpr_write_b32 a120, v152\n" \
"v_mfma_f32_16x16x32_bf16 v[28:31], a[220:223], a[240:243], v[28:31]\n" \
"ds_read_b64_tr_b16 a[210:211], v240 offset:51712\n" \
"ds_read_b64_tr_b16 a[212:213], v240 offset:57856\n" \
"v_mfma_f32_16x16x32_bf16 v[60:63], a[220:223], a[244:247], v[60:63]\n" \
"ds_read_b64_tr_b16 a[214:215], v240 offset:59904\n" \
"v_accvgpr_write_b32 a121, v153\n" \
"v_mfma_f32_16x16x32_bf16 v[60:63], a[216:219], a[228:231], v[60:63]\n" \
"s_waitcnt lgkmcnt(9)\n" \
"v_accvgpr_write_b32 a122, v154\n" \
"v_accvgpr_write_b32 a123, v155\n" \
"v_mfma_f32_16x16x32_bf16 v[64:67], a[192:195], a[232:235], v[64:67]\n" \
"ds_read_b64_tr_b16 a[216:217], v241 offset:49664\n" \
"v_max_u32_dpp v250, v250, v250 quad_perm:[1,0,3,2] row_mask:0xf bank_mask:0xf\n" \
"v_mfma_f32_16x16x32_bf16 v[64:67], a[196:199], a[248:251], v[64:67]\n" \
"ds_read_b64_tr_b16 a[218:219], v241 offset:51712\n" \
"ds_read_b64_tr_b16 a[220:221], v241 offset:57856\n" \
"v_mfma_f32_16x16x32_bf16 v[96:99], a[196:199], a[252:255], v[96:99]\n" \
"ds_read_b64_tr_b16 a[222:223], v241 offset:59904\n" \
"v_max_u32_dpp v250, v250, v250 quad_perm:[2,3,0,1] row_mask:0xf bank_mask:0xf\n" \
"v_mfma_f32_16x16x32_bf16 v[96:99], a[192:195], a[236:239], v[96:99]\n" \
"s_waitcnt lgkmcnt(8)\n" \
"v_cvt_pk_bf16_f32 v192, v0, v1\n" \
"v_cvt_pk_bf16_f32 v193, v2, v3\n" \
"v_mfma_f32_16x16x32_bf16 v[68:71], a[200:203], a[232:235], v[68:71]\n" \
"ds_read_b64_tr_b16 a[192:193], v240 offset:50176\n" \
"v_max_u32_dpp v250, v250, v250 row_half_mirror row_mask:0xf bank_mask:0xf\n" \
"v_mfma_f32_16x16x32_bf16 v[68:71], a[204:207], a[248:251], v[68:71]\n" \
"ds_read_b64_tr_b16 a[194:195], v240 offset:52224\n" \
"ds_read_b64_tr_b16 a[196:197], v240 offset:58368\n" \
"v_mfma_f32_16x16x32_bf16 v[100:103], a[204:207], a[252:255], v[100:103]\n" \
"ds_read_b64_tr_b16 a[198:199], v240 offset:60416\n" \
"v_max_u32_dpp v250, v250, v250 row_mirror row_mask:0xf bank_mask:0xf\n" \
"v_mfma_f32_16x16x32_bf16 v[100:103], a[200:203], a[236:239], v[100:103]\n" \
"s_waitcnt lgkmcnt(8)\n" \
"v_mov_b32 v152, v250\n" \
"v_cvt_pk_bf16_f32 v194, v4, v5\n" \
"v_mfma_f32_16x16x32_bf16 v[72:75], a[208:211], a[232:235], v[72:75]\n" \
"ds_read_b64_tr_b16 a[200:201], v241 offset:50176\n" \
"v_cvt_pk_bf16_f32 v195, v6, v7\n" \
"v_mfma_f32_16x16x32_bf16 v[72:75], a[212:215], a[248:251], v[72:75]\n" \
"ds_read_b64_tr_b16 a[202:203], v241 offset:52224\n" \
"ds_read_b64_tr_b16 a[204:205], v241 offset:58368\n" \
"v_mfma_f32_16x16x32_bf16 v[104:107], a[212:215], a[252:255], v[104:107]\n" \
"ds_read_b64_tr_b16 a[206:207], v241 offset:60416\n" \
"v_cvt_pk_bf16_f32 v208, v32, v33\n" \
"v_mfma_f32_16x16x32_bf16 v[104:107], a[208:211], a[236:239], v[104:107]\n" \
"s_waitcnt lgkmcnt(8)\n" \
"v_permlane16_swap_b32 v250, v152\n" \
"v_mfma_f32_16x16x32_bf16 v[76:79], a[216:219], a[232:235], v[76:79]\n" \
"ds_read_b64_tr_b16 a[208:209], v240 offset:50688\n" \
"v_max_u32 v250, v250, v152\n" \
"v_mfma_f32_16x16x32_bf16 v[76:79], a[220:223], a[248:251], v[76:79]\n" \
"ds_read_b64_tr_b16 a[210:211], v240 offset:52736\n" \
"ds_read_b64_tr_b16 a[212:213], v240 offset:58880\n" \
"v_mfma_f32_16x16x32_bf16 v[108:111], a[220:223], a[252:255], v[108:111]\n" \
"ds_read_b64_tr_b16 a[214:215], v240 offset:60928\n" \
"v_mov_b32 v152, v250\n" \
"v_mfma_f32_16x16x32_bf16 v[108:111], a[216:219], a[236:239], v[108:111]\n" \
"s_waitcnt lgkmcnt(8)\n" \
"v_cvt_pk_bf16_f32 v209, v34, v35\n" \
"v_cvt_pk_bf16_f32 v210, v36, v37\n" \
"v_mfma_f32_16x16x32_bf16 v[80:83], a[192:195], a[232:235], v[80:83]\n" \
"ds_read_b64_tr_b16 a[216:217], v241 offset:50688\n" \
"v_cvt_pk_bf16_f32 v211, v38, v39\n" \
"v_mfma_f32_16x16x32_bf16 v[80:83], a[196:199], a[248:251], v[80:83]\n" \
"ds_read_b64_tr_b16 a[218:219], v241 offset:52736\n" \
"ds_read_b64_tr_b16 a[220:221], v241 offset:58880\n" \
"v_mfma_f32_16x16x32_bf16 v[112:115], a[196:199], a[252:255], v[112:115]\n" \
"ds_read_b64_tr_b16 a[222:223], v241 offset:60928\n" \
"v_cvt_pk_bf16_f32 v196, v8, v9\n" \
"v_mfma_f32_16x16x32_bf16 v[112:115], a[192:195], a[236:239], v[112:115]\n" \
"s_waitcnt lgkmcnt(8)\n" \
"v_permlane32_swap_b32 v250, v152\n" \
"v_mfma_f32_16x16x32_bf16 v[84:87], a[200:203], a[232:235], v[84:87]\n" \
"s_cmp_lg_u32 s84, 0\n" \
"s_cmov_b32 s21, s22\n" \
"s_cmov_b32 s91, 0x8000\n" \
"v_mfma_f32_16x16x32_bf16 v[84:87], a[204:207], a[248:251], v[84:87]\n" \
"v_max_u32 v250, v250, v152\n" \
"v_max_u32 v250, 1, v250\n" \
"v_mfma_f32_16x16x32_bf16 v[116:119], a[204:207], a[252:255], v[116:119]\n" \
"v_cvt_pk_bf16_f32 v197, v10, v11\n" \
"v_cvt_pk_bf16_f32 v198, v12, v13\n" \
"v_mfma_f32_16x16x32_bf16 v[116:119], a[200:203], a[236:239], v[116:119]\n" \
"s_waitcnt lgkmcnt(4)\n" \
"v_cvt_pk_bf16_f32 v199, v14, v15\n" \
"v_cvt_pk_bf16_f32 v212, v40, v41\n" \
"v_mfma_f32_16x16x32_bf16 v[88:91], a[208:211], a[232:235], v[88:91]\n" \
"v_cvt_pk_bf16_f32 v213, v42, v43\n" \
"v_cvt_pk_bf16_f32 v214, v44, v45\n" \
"v_mfma_f32_16x16x32_bf16 v[88:91], a[212:215], a[248:251], v[88:91]\n" \
"v_cvt_pk_bf16_f32 v215, v46, v47\n" \
"v_cvt_pk_bf16_f32 v200, v16, v17\n" \
"v_mfma_f32_16x16x32_bf16 v[120:123], a[212:215], a[252:255], v[120:123]\n" \
"v_cvt_pk_bf16_f32 v201, v18, v19\n" \
"v_cvt_pk_bf16_f32 v202, v20, v21\n" \
"v_mfma_f32_16x16x32_bf16 v[120:123], a[208:211], a[236:239], v[120:123]\n" \
"s_waitcnt lgkmcnt(0)\n" \
"v_cvt_pk_bf16_f32 v203, v22, v23\n" \
"v_cvt_pk_bf16_f32 v216, v48, v49\n" \
"v_mfma_f32_16x16x32_bf16 v[92:95], a[216:219], a[232:235], v[92:95]\n" \
"v_cvt_pk_bf16_f32 v217, v50, v51\n" \
"v_cvt_pk_bf16_f32 v218, v52, v53\n" \
"v_mfma_f32_16x16x32_bf16 v[92:95], a[220:223], a[248:251], v[92:95]\n" \
"v_cvt_pk_bf16_f32 v219, v54, v55\n" \
"v_cvt_pk_bf16_f32 v204, v24, v25\n" \
"v_mfma_f32_16x16x32_bf16 v[124:127], a[220:223], a[252:255], v[124:127]\n" \
"v_cvt_pk_bf16_f32 v205, v26, v27\n" \
"v_cvt_pk_bf16_f32 v206, v28, v29\n" \
"v_mfma_f32_16x16x32_bf16 v[124:127], a[216:219], a[236:239], v[124:127]\n" \
"s_lshl_b32 s85, s68, 4\n" \
"v_cvt_pk_bf16_f32 v207, v30, v31\n" \
"v_cvt_pk_bf16_f32 v220, v56, v57\n" \
  \
".Lat_b_%=:\n" \
"s_cmp_ge_u32 %[wid], 2\n" \
"s_cbranch_scc1 .Ldhi_%=\n" \
"v_mfma_f32_16x16x32_bf16 v[128:131], a[128:131], v[192:195], v[224:227]\n" \
"s_add_u32 s85, s85, s67\n" \
"v_cvt_pk_bf16_f32 v221, v58, v59\n" \
"v_cvt_pk_bf16_f32 v222, v60, v61\n" \
"v_mfma_f32_16x16x32_bf16 v[128:131], a[132:135], v[196:199], v[128:131]\n" \
"s_barrier\n" \
"v_cvt_pk_bf16_f32 v223, v62, v63\n" \
"v_add_u32 v242, 0x10000, v248\n" \
"v_mfma_f32_16x16x32_bf16 v[128:131], a[136:139], v[200:203], v[128:131]\n" \
"s_lshl2_add_u32 s85, s85, %[wid]\n" \
"v_bfe_i32 v241, v251, 0, 1\n" \
"v_and_b32 v241, 0xaaaaaaaa, v241\n" \
"v_mfma_f32_16x16x32_bf16 v[128:131], a[140:143], v[204:207], v[128:131]\n" \
"s_lshl_b32 s85, s85, 2\n" \
"v_or_b32 v241, 0x44444444, v241\n" \
"v_xor_b32 v249, 0x4000, v249\n" \
"v_mfma_f32_16x16x32_bf16 v[132:135], a[156:159], v[204:207], v[228:231]\n" \
"s_waitcnt vmcnt(1)\n" \
"s_add_u32 s85, s85, 82048\n" \
"buffer_store_dword v250, off, s[32:35], s85 sc1\n" \
"v_mfma_f32_16x16x32_bf16 v[132:135], a[152:155], v[200:203], v[132:135]\n" \
"ds_read_b128 a[192:195], v242 offset:0\n" \
"ds_read_b128 a[196:199], v242 offset:1024\n" \
"v_mfma_f32_16x16x32_bf16 v[132:135], a[148:151], v[196:199], v[132:135]\n" \
"v_exp_f32 v128, v128\n" \
"v_mfma_f32_16x16x32_bf16 v[132:135], a[144:147], v[192:195], v[132:135]\n" \
"s_add_u32 s85, s94, 589824\n" \
"ds_read_b128 a[200:203], v242 offset:2048\n" \
"ds_read_b128 a[204:207], v242 offset:3072\n" \
"v_mfma_f32_16x16x32_bf16 v[144:147], a[128:131], v[208:211], 0\n" \
"v_exp_f32 v129, v129\n" \
"v_mfma_f32_16x16x32_bf16 v[144:147], a[132:135], v[212:215], v[144:147]\n" \
"buffer_store_dwordx4 a[64:67], v248, s[32:35], s85 offen offset:0 sc1\n" \
"v_mfma_f32_16x16x32_bf16 v[144:147], a[136:139], v[216:219], v[144:147]\n" \
"v_exp_f32 v130, v130\n" \
"v_mfma_f32_16x16x32_bf16 v[144:147], a[140:143], v[220:223], v[144:147]\n" \
"ds_read_b128 a[208:211], v242 offset:4096\n" \
"v_cvt_pk_bf16_f32 v128, v128, v129\n" \
"v_mfma_f32_16x16x32_bf16 v[148:151], a[156:159], v[220:223], 0\n" \
"v_exp_f32 v131, v131\n" \
"v_mfma_f32_16x16x32_bf16 v[148:151], a[152:155], v[216:219], v[148:151]\n" \
"v_exp_f32 v132, v132\n" \
"v_mfma_f32_16x16x32_bf16 v[148:151], a[148:151], v[212:215], v[148:151]\n" \
"ds_read_b128 a[212:215], v242 offset:5120\n" \
"v_cvt_pk_bf16_f32 v129, v130, v131\n" \
"v_mfma_f32_16x16x32_bf16 v[148:151], a[144:147], v[208:211], v[148:151]\n" \
"v_exp_f32 v133, v133\n" \
"v_mfma_f32_16x16x32_bf16 v[152:155], a[160:163], v[208:211], 0\n" \
"ds_read_b128 a[216:219], v242 offset:6144\n" \
"v_cvt_pk_bf16_f32 v130, v132, v133\n" \
"v_mfma_f32_16x16x32_bf16 v[152:155], a[164:167], v[212:215], v[152:155]\n" \
"v_exp_f32 v134, v134\n" \
"v_mfma_f32_16x16x32_bf16 v[152:155], a[168:171], v[216:219], v[152:155]\n" \
"ds_read_b128 a[220:223], v242 offset:7168\n" \
"ds_read_b128 a[224:227], v242 offset:8192\n" \
"v_mfma_f32_16x16x32_bf16 v[152:155], a[172:175], v[220:223], v[152:155]\n" \
"v_exp_f32 v135, v135\n" \
"v_mfma_f32_16x16x32_bf16 v[156:159], a[188:191], v[220:223], 0\n" \
"v_exp_f32 v144, v144\n" \
"v_mfma_f32_16x16x32_bf16 v[156:159], a[184:187], v[216:219], v[156:159]\n" \
"v_exp_f32 v145, v145\n" \
"v_mfma_f32_16x16x32_bf16 v[156:159], a[180:183], v[212:215], v[156:159]\n" \
"v_exp_f32 v146, v146\n" \
"v_cvt_pk_bf16_f32 v131, v134, v135\n" \
"v_mov_b32 v132, 0\n" \
"v_mov_b32 v133, 0\n" \
"v_mov_b32 v134, 0\n" \
"v_mov_b32 v135, 0\n" \
"v_mfma_f32_16x16x32_bf16 v[156:159], a[176:179], v[208:211], v[156:159]\n" \
"v_exp_f32 v147, v147\n" \
"v_smfmac_f32_16x16x64_bf16 a[32:35], a[48:51], v[128:135], v241\n" \
"s_waitcnt lgkmcnt(5)\n" \
"ds_read_b128 a[228:231], v242 offset:9216\n" \
"v_cvt_pk_bf16_f32 v144, v144, v145\n" \
"v_mfma_f32_16x16x32_bf16 v[0:3], a[192:195], v[128:131], 0\n" \
"s_add_u32 s85, s94, 589824\n" \
"v_exp_f32 v148, v148\n" \
"v_mfma_f32_16x16x32_bf16 v[4:7], a[196:199], v[128:131], 0\n" \
"v_exp_f32 v149, v149\n" \
"v_mfma_f32_16x16x32_bf16 v[8:11], a[200:203], v[128:131], 0\n" \
"v_exp_f32 v150, v150\n" \
"v_mfma_f32_16x16x32_bf16 v[12:15], a[204:207], v[128:131], 0\n" \
"s_waitcnt lgkmcnt(2)\n" \
"v_exp_f32 v151, v151\n" \
"v_mfma_f32_16x16x32_bf16 v[16:19], a[208:211], v[128:131], 0\n" \
"buffer_store_dwordx4 a[72:75], v248, s[32:35], s85 offen offset:1024 sc1\n" \
"v_mfma_f32_16x16x32_bf16 v[20:23], a[212:215], v[128:131], 0\n" \
"v_exp_f32 v152, v152\n" \
"v_mfma_f32_16x16x32_bf16 v[24:27], a[216:219], v[128:131], 0\n" \
"v_cvt_pk_bf16_f32 v145, v146, v147\n" \
"v_cvt_pk_bf16_f32 v146, v148, v149\n" \
"v_cvt_pk_bf16_f32 v160, v64, v65\n" \
"v_cvt_pk_bf16_f32 v161, v66, v67\n" \
"v_cvt_pk_bf16_f32 v162, v68, v69\n" \
"v_cvt_pk_bf16_f32 v163, v70, v71\n" \
"v_mfma_f32_16x16x32_bf16 v[28:31], a[220:223], v[128:131], 0\n" \
"v_exp_f32 v153, v153\n" \
"v_cvt_pk_bf16_f32 v164, v72, v73\n" \
"v_cvt_pk_bf16_f32 v165, v74, v75\n" \
"v_cvt_pk_bf16_f32 v166, v76, v77\n" \
"v_cvt_pk_bf16_f32 v167, v78, v79\n" \
"v_mfma_f32_16x16x32_bf16 v[128:131], a[128:131], v[160:163], 0\n" \
"v_cvt_pk_bf16_f32 v147, v150, v151\n" \
"v_cvt_pk_bf16_f32 v148, v152, v153\n" \
"v_cvt_pk_bf16_f32 v168, v80, v81\n" \
"v_cvt_pk_bf16_f32 v169, v82, v83\n" \
"v_cvt_pk_bf16_f32 v170, v84, v85\n" \
"v_cvt_pk_bf16_f32 v171, v86, v87\n" \
"v_mfma_f32_16x16x32_bf16 v[128:131], a[132:135], v[164:167], v[128:131]\n" \
"v_exp_f32 v154, v154\n" \
"v_cvt_pk_bf16_f32 v172, v88, v89\n" \
"v_cvt_pk_bf16_f32 v173, v90, v91\n" \
"v_cvt_pk_bf16_f32 v174, v92, v93\n" \
"v_cvt_pk_bf16_f32 v175, v94, v95\n" \
"v_mfma_f32_16x16x32_bf16 v[128:131], a[136:139], v[168:171], v[128:131]\n" \
"ds_read_b128 a[232:235], v242 offset:10240\n" \
"ds_read_b128 a[236:239], v242 offset:11264\n" \
"v_mfma_f32_16x16x32_bf16 v[128:131], a[140:143], v[172:175], v[128:131]\n" \
"v_exp_f32 v155, v155\n" \
"v_mfma_f32_16x16x32_bf16 v[132:135], a[156:159], v[172:175], 0\n" \
"ds_read_b128 a[240:243], v242 offset:12288\n" \
"v_cvt_pk_bf16_f32 v149, v154, v155\n" \
"v_mfma_f32_16x16x32_bf16 v[132:135], a[152:155], v[168:171], v[132:135]\n" \
"v_exp_f32 v156, v156\n" \
"v_mfma_f32_16x16x32_bf16 v[132:135], a[148:151], v[164:167], v[132:135]\n" \
"ds_read_b128 a[244:247], v242 offset:13312\n" \
"ds_read_b128 a[248:251], v242 offset:14336\n" \
"v_mfma_f32_16x16x32_bf16 v[132:135], a[144:147], v[160:163], v[132:135]\n" \
"v_exp_f32 v157, v157\n" \
"v_mfma_f32_16x16x32_bf16 v[136:139], a[160:163], v[160:163], 0\n" \
"ds_read_b128 a[252:255], v242 offset:15360\n" \
"v_cvt_pk_bf16_f32 v150, v156, v157\n" \
"v_mfma_f32_16x16x32_bf16 v[136:139], a[164:167], v[164:167], v[136:139]\n" \
"v_exp_f32 v158, v158\n" \
"v_mfma_f32_16x16x32_bf16 v[136:139], a[168:171], v[168:171], v[136:139]\n" \
"s_add_u32 s85, s94, 589824\n" \
"v_cvt_pk_bf16_f32 v176, v96, v97\n" \
"v_cvt_pk_bf16_f32 v177, v98, v99\n" \
"v_mfma_f32_16x16x32_bf16 v[136:139], a[172:175], v[172:175], v[136:139]\n" \
"v_exp_f32 v159, v159\n" \
"v_mfma_f32_16x16x32_bf16 v[140:143], a[188:191], v[172:175], 0\n" \
"buffer_store_dwordx4 a[80:83], v248, s[32:35], s85 offen offset:2048 sc1\n" \
"v_mfma_f32_16x16x32_bf16 v[140:143], a[184:187], v[168:171], v[140:143]\n" \
"v_cvt_pk_bf16_f32 v151, v158, v159\n" \
"v_cvt_pk_bf16_f32 v178, v100, v101\n" \
"v_mfma_f32_16x16x32_bf16 v[140:143], a[180:183], v[164:167], v[140:143]\n" \
"v_exp_f32 v128, v128\n" \
"v_mfma_f32_16x16x32_bf16 v[140:143], a[176:179], v[160:163], v[140:143]\n" \
"v_cvt_pk_bf16_f32 v179, v102, v103\n" \
"v_cvt_pk_bf16_f32 v180, v104, v105\n" \
"v_smfmac_f32_16x16x64_bf16 a[36:39], a[48:51], v[144:151], v241\n" \
"s_waitcnt lgkmcnt(4)\n" \
"v_cvt_pk_bf16_f32 v181, v106, v107\n" \
"v_mfma_f32_16x16x32_bf16 v[32:35], a[224:227], v[148:151], 0\n" \
"v_exp_f32 v129, v129\n" \
"v_mfma_f32_16x16x32_bf16 v[32:35], a[192:195], v[144:147], v[32:35]\n" \
"v_exp_f32 v130, v130\n" \
"v_mfma_f32_16x16x32_bf16 v[36:39], a[196:199], v[144:147], 0\n" \
"ds_write_b128 v249, a[64:67] offset:32768\n" \
"v_cvt_pk_bf16_f32 v128, v128, v129\n" \
"v_mfma_f32_16x16x32_bf16 v[36:39], a[228:231], v[148:151], v[36:39]\n" \
"v_exp_f32 v131, v131\n" \
"v_mfma_f32_16x16x32_bf16 v[40:43], a[232:235], v[148:151], 0\n" \
"v_cvt_pk_bf16_f32 v129, v130, v131\n" \
"v_cvt_pk_bf16_f32 v182, v108, v109\n" \
"v_mfma_f32_16x16x32_bf16 v[40:43], a[200:203], v[144:147], v[40:43]\n" \
"v_exp_f32 v132, v132\n" \
"v_mfma_f32_16x16x32_bf16 v[44:47], a[204:207], v[144:147], 0\n" \
"v_cvt_pk_bf16_f32 v183, v110, v111\n" \
"v_cvt_pk_bf16_f32 v184, v112, v113\n" \
"v_mfma_f32_16x16x32_bf16 v[44:47], a[236:239], v[148:151], v[44:47]\n" \
"s_waitcnt lgkmcnt(1)\n" \
"v_exp_f32 v133, v133\n" \
"v_mfma_f32_16x16x32_bf16 v[48:51], a[240:243], v[148:151], 0\n" \
"v_cvt_pk_bf16_f32 v130, v132, v133\n" \
"v_cvt_pk_bf16_f32 v185, v114, v115\n" \
"v_mfma_f32_16x16x32_bf16 v[48:51], a[208:211], v[144:147], v[48:51]\n" \
"v_exp_f32 v134, v134\n" \
"v_mfma_f32_16x16x32_bf16 v[52:55], a[212:215], v[144:147], 0\n" \
"v_cvt_pk_bf16_f32 v186, v116, v117\n" \
"v_cvt_pk_bf16_f32 v187, v118, v119\n" \
"v_mfma_f32_16x16x32_bf16 v[52:55], a[244:247], v[148:151], v[52:55]\n" \
"s_add_u32 s85, s94, 589824\n" \
"v_exp_f32 v135, v135\n" \
"v_mfma_f32_16x16x32_bf16 v[56:59], a[248:251], v[148:151], 0\n" \
"buffer_store_dwordx4 a[88:91], v248, s[32:35], s85 offen offset:3072 sc1\n" \
"v_mfma_f32_16x16x32_bf16 v[56:59], a[216:219], v[144:147], v[56:59]\n" \
"v_exp_f32 v136, v136\n" \
"v_mfma_f32_16x16x32_bf16 v[60:63], a[220:223], v[144:147], 0\n" \
"ds_write_b128 v249, a[72:75] offset:33792\n" \
"v_cvt_pk_bf16_f32 v131, v134, v135\n" \
"v_mfma_f32_16x16x32_bf16 v[60:63], a[252:255], v[148:151], v[60:63]\n" \
"v_exp_f32 v137, v137\n" \
"v_mfma_f32_16x16x32_bf16 v[144:147], a[128:131], v[176:179], 0\n" \
"v_cvt_pk_bf16_f32 v132, v136, v137\n" \
"v_cvt_pk_bf16_f32 v188, v120, v121\n" \
"v_mfma_f32_16x16x32_bf16 v[144:147], a[132:135], v[180:183], v[144:147]\n" \
"v_exp_f32 v138, v138\n" \
"v_cvt_pk_bf16_f32 v189, v122, v123\n" \
"v_cvt_pk_bf16_f32 v190, v124, v125\n" \
"v_cvt_pk_bf16_f32 v191, v126, v127\n" \
"v_mfma_f32_16x16x32_bf16 v[144:147], a[136:139], v[184:187], v[144:147]\n" \
"ds_read_b128 a[128:131], v248 offset:16384\n" \
"ds_read_b128 a[132:135], v248 offset:17408\n" \
"v_mfma_f32_16x16x32_bf16 v[144:147], a[140:143], v[188:191], v[144:147]\n" \
"v_exp_f32 v139, v139\n" \
"v_mfma_f32_16x16x32_bf16 v[148:151], a[156:159], v[188:191], 0\n" \
"ds_read_b128 a[136:139], v248 offset:18432\n" \
"v_cvt_pk_bf16_f32 v133, v138, v139\n" \
"v_mfma_f32_16x16x32_bf16 v[148:151], a[152:155], v[184:187], v[148:151]\n" \
"v_exp_f32 v140, v140\n" \
"v_mfma_f32_16x16x32_bf16 v[148:151], a[148:151], v[180:183], v[148:151]\n" \
"ds_read_b128 a[140:143], v248 offset:19456\n" \
"v_mfma_f32_16x16x32_bf16 v[148:151], a[144:147], v[176:179], v[148:151]\n" \
"v_exp_f32 v141, v141\n" \
"v_mfma_f32_16x16x32_bf16 v[152:155], a[160:163], v[176:179], 0\n" \
"ds_read_b128 a[156:159], v248 offset:23552\n" \
"v_cvt_pk_bf16_f32 v134, v140, v141\n" \
"v_mfma_f32_16x16x32_bf16 v[152:155], a[164:167], v[180:183], v[152:155]\n" \
"v_exp_f32 v142, v142\n" \
"v_mfma_f32_16x16x32_bf16 v[152:155], a[168:171], v[184:187], v[152:155]\n" \
"ds_write_b128 v249, a[80:83] offset:34816\n" \
"v_mfma_f32_16x16x32_bf16 v[152:155], a[172:175], v[188:191], v[152:155]\n" \
"v_exp_f32 v143, v143\n" \
"v_mfma_f32_16x16x32_bf16 v[156:159], a[188:191], v[188:191], 0\n" \
"ds_read_b128 a[152:155], v248 offset:22528\n" \
"v_cvt_pk_bf16_f32 v135, v142, v143\n" \
"v_mfma_f32_16x16x32_bf16 v[156:159], a[184:187], v[184:187], v[156:159]\n" \
"s_add_u32 s85, s94, 593920\n" \
"ds_read_b128 a[148:151], v248 offset:21504\n" \
"ds_read_b128 a[144:147], v248 offset:20480\n" \
"v_mfma_f32_16x16x32_bf16 v[156:159], a[180:183], v[180:183], v[156:159]\n" \
"v_exp_f32 v144, v144\n" \
"v_mfma_f32_16x16x32_bf16 v[156:159], a[176:179], v[176:179], v[156:159]\n" \
"buffer_store_dwordx4 a[96:99], v248, s[32:35], s85 offen offset:0 sc1\n" \
"v_smfmac_f32_16x16x64_bf16 a[40:43], a[48:51], v[128:135], v241\n" \
"ds_read_b128 a[160:163], v248 offset:24576\n" \
"ds_read_b128 a[164:167], v248 offset:25600\n" \
"v_mfma_f32_16x16x32_bf16 v[64:67], a[224:227], v[132:135], 0\n" \
"v_exp_f32 v145, v145\n" \
"v_mfma_f32_16x16x32_bf16 v[64:67], a[192:195], v[128:131], v[64:67]\n" \
"v_exp_f32 v146, v146\n" \
"v_mfma_f32_16x16x32_bf16 v[68:71], a[196:199], v[128:131], 0\n" \
"ds_read_b128 a[168:171], v248 offset:26624\n" \
"v_cvt_pk_bf16_f32 v144, v144, v145\n" \
"v_mfma_f32_16x16x32_bf16 v[68:71], a[228:231], v[132:135], v[68:71]\n" \
"v_exp_f32 v147, v147\n" \
"v_mfma_f32_16x16x32_bf16 v[72:75], a[232:235], v[132:135], 0\n" \
"ds_read_b128 a[172:175], v248 offset:27648\n" \
"v_cvt_pk_bf16_f32 v145, v146, v147\n" \
"v_mfma_f32_16x16x32_bf16 v[72:75], a[200:203], v[128:131], v[72:75]\n" \
"v_exp_f32 v148, v148\n" \
"v_mfma_f32_16x16x32_bf16 v[76:79], a[204:207], v[128:131], 0\n" \
"ds_write_b128 v249, a[88:91] offset:35840\n" \
"v_mfma_f32_16x16x32_bf16 v[76:79], a[236:239], v[132:135], v[76:79]\n" \
"v_exp_f32 v149, v149\n" \
"v_mfma_f32_16x16x32_bf16 v[80:83], a[240:243], v[132:135], 0\n" \
"ds_read_b128 a[188:191], v248 offset:31744\n" \
"v_cvt_pk_bf16_f32 v146, v148, v149\n" \
"v_mfma_f32_16x16x32_bf16 v[80:83], a[208:211], v[128:131], v[80:83]\n" \
"v_exp_f32 v150, v150\n" \
"v_mfma_f32_16x16x32_bf16 v[84:87], a[212:215], v[128:131], 0\n" \
"ds_read_b128 a[184:187], v248 offset:30720\n" \
"ds_read_b128 a[180:183], v248 offset:29696\n" \
"v_mfma_f32_16x16x32_bf16 v[84:87], a[244:247], v[132:135], v[84:87]\n" \
"v_exp_f32 v151, v151\n" \
"v_mfma_f32_16x16x32_bf16 v[88:91], a[248:251], v[132:135], 0\n" \
"ds_read_b128 a[176:179], v248 offset:28672\n" \
"v_cvt_pk_bf16_f32 v147, v150, v151\n" \
"v_mfma_f32_16x16x32_bf16 v[88:91], a[216:219], v[128:131], v[88:91]\n" \
"v_exp_f32 v152, v152\n" \
"v_mfma_f32_16x16x32_bf16 v[92:95], a[220:223], v[128:131], 0\n" \
"s_add_u32 s85, s94, 593920\n" \
"v_mfma_f32_16x16x32_bf16 v[92:95], a[252:255], v[132:135], v[92:95]\n" \
"s_waitcnt lgkmcnt(14)\n" \
"v_exp_f32 v153, v153\n" \
"v_mfma_f32_16x16x32_bf16 v[128:131], a[128:131], v[208:211], 0\n" \
"buffer_store_dwordx4 a[104:107], v248, s[32:35], s85 offen offset:1024 sc1\n" \
"v_mfma_f32_16x16x32_bf16 v[128:131], a[132:135], v[212:215], v[128:131]\n" \
"v_exp_f32 v154, v154\n" \
"v_mfma_f32_16x16x32_bf16 v[128:131], a[136:139], v[216:219], v[128:131]\n" \
"v_mov_b32 v240, v248\n" \
"v_cvt_pk_bf16_f32 v148, v152, v153\n" \
"v_mfma_f32_16x16x32_bf16 v[128:131], a[140:143], v[220:223], v[128:131]\n" \
"s_waitcnt lgkmcnt(9)\n" \
"v_exp_f32 v155, v155\n" \
"v_mfma_f32_16x16x32_bf16 v[132:135], a[156:159], v[220:223], 0\n" \
"ds_write_b128 v249, a[96:99] offset:36864\n" \
"v_cvt_pk_bf16_f32 v149, v154, v155\n" \
"v_mfma_f32_16x16x32_bf16 v[132:135], a[152:155], v[216:219], v[132:135]\n" \
"v_exp_f32 v156, v156\n" \
"v_mfma_f32_16x16x32_bf16 v[132:135], a[148:151], v[212:215], v[132:135]\n" \
"buffer_load_dword v253, v252, s[32:35], s81 offen sc1\n" \
"v_mfma_f32_16x16x32_bf16 v[132:135], a[144:147], v[208:211], v[132:135]\n" \
"s_waitcnt lgkmcnt(6)\n" \
"v_exp_f32 v157, v157\n" \
"v_mfma_f32_16x16x32_bf16 v[136:139], a[160:163], v[208:211], v[232:235]\n" \
"v_cvt_pk_bf16_f32 v150, v156, v157\n" \
"v_mfma_f32_16x16x32_bf16 v[136:139], a[164:167], v[212:215], v[136:139]\n" \
"v_exp_f32 v158, v158\n" \
"v_mfma_f32_16x16x32_bf16 v[136:139], a[168:171], v[216:219], v[136:139]\n" \
"v_mfma_f32_16x16x32_bf16 v[136:139], a[172:175], v[220:223], v[136:139]\n" \
"s_waitcnt lgkmcnt(1)\n" \
"v_exp_f32 v159, v159\n" \
"v_mfma_f32_16x16x32_bf16 v[140:143], a[188:191], v[220:223], v[236:239]\n" \
"v_cvt_pk_bf16_f32 v151, v158, v159\n" \
"v_mfma_f32_16x16x32_bf16 v[140:143], a[184:187], v[216:219], v[140:143]\n" \
"v_mfma_f32_16x16x32_bf16 v[140:143], a[180:183], v[212:215], v[140:143]\n" \
"v_exp_f32 v128, v128\n" \
"v_mfma_f32_16x16x32_bf16 v[140:143], a[176:179], v[208:211], v[140:143]\n" \
"v_smfmac_f32_16x16x64_bf16 a[44:47], a[48:51], v[144:151], v241\n" \
"s_add_u32 s85, s94, 593920\n" \
"ds_write_b128 v249, a[104:107] offset:37888\n" \
"v_mfma_f32_16x16x32_bf16 v[96:99], a[224:227], v[148:151], 0\n" \
"v_exp_f32 v129, v129\n" \
"v_mfma_f32_16x16x32_bf16 v[96:99], a[192:195], v[144:147], v[96:99]\n" \
"v_exp_f32 v130, v130\n" \
"v_mfma_f32_16x16x32_bf16 v[100:103], a[196:199], v[144:147], 0\n" \
"buffer_store_dwordx4 a[112:115], v248, s[32:35], s85 offen offset:2048 sc1\n" \
"v_mfma_f32_16x16x32_bf16 v[100:103], a[228:231], v[148:151], v[100:103]\n" \
"v_exp_f32 v131, v131\n" \
"v_mfma_f32_16x16x32_bf16 v[104:107], a[232:235], v[148:151], 0\n" \
"v_cvt_pk_bf16_f32 v128, v128, v129\n" \
"v_cvt_pk_bf16_f32 v129, v130, v131\n" \
"v_mfma_f32_16x16x32_bf16 v[104:107], a[200:203], v[144:147], v[104:107]\n" \
"v_exp_f32 v132, v132\n" \
"v_mfma_f32_16x16x32_bf16 v[108:111], a[204:207], v[144:147], 0\n" \
"ds_read_b128 a[224:227], v242 offset:24576\n" \
"ds_read_b128 a[192:195], v242 offset:16384\n" \
"v_mfma_f32_16x16x32_bf16 v[108:111], a[236:239], v[148:151], v[108:111]\n" \
"v_exp_f32 v133, v133\n" \
"v_mfma_f32_16x16x32_bf16 v[112:115], a[240:243], v[148:151], 0\n" \
"ds_read_b128 a[196:199], v242 offset:17408\n" \
"v_cvt_pk_bf16_f32 v130, v132, v133\n" \
"v_mfma_f32_16x16x32_bf16 v[112:115], a[208:211], v[144:147], v[112:115]\n" \
"v_exp_f32 v134, v134\n" \
"v_mfma_f32_16x16x32_bf16 v[116:119], a[212:215], v[144:147], 0\n" \
"ds_read_b128 a[228:231], v242 offset:25600\n" \
"ds_read_b128 a[232:235], v242 offset:26624\n" \
"v_mfma_f32_16x16x32_bf16 v[116:119], a[244:247], v[148:151], v[116:119]\n" \
"v_exp_f32 v135, v135\n" \
"v_mfma_f32_16x16x32_bf16 v[120:123], a[248:251], v[148:151], 0\n" \
"ds_write_b128 v249, a[112:115] offset:38912\n" \
"v_cvt_pk_bf16_f32 v131, v134, v135\n" \
"v_mfma_f32_16x16x32_bf16 v[120:123], a[216:219], v[144:147], v[120:123]\n" \
"v_exp_f32 v136, v136\n" \
"v_mfma_f32_16x16x32_bf16 v[124:127], a[220:223], v[144:147], 0\n" \
"ds_read_b128 a[200:203], v242 offset:18432\n" \
"ds_read_b128 a[204:207], v242 offset:19456\n" \
"v_mfma_f32_16x16x32_bf16 v[124:127], a[252:255], v[148:151], v[124:127]\n" \
"v_exp_f32 v137, v137\n" \
"v_mfma_f32_16x16x32_bf16 v[144:147], a[128:131], v[160:163], 0\n" \
"ds_read_b128 a[236:239], v242 offset:27648\n" \
"v_cvt_pk_bf16_f32 v132, v136, v137\n" \
"v_mfma_f32_16x16x32_bf16 v[144:147], a[132:135], v[164:167], v[144:147]\n" \
"s_add_u32 s85, s94, 593920\n" \
"v_exp_f32 v138, v138\n" \
"v_mfma_f32_16x16x32_bf16 v[144:147], a[136:139], v[168:171], v[144:147]\n" \
"buffer_store_dwordx4 a[120:123], v248, s[32:35], s85 offen offset:3072 sc1\n" \
"v_mfma_f32_16x16x32_bf16 v[144:147], a[140:143], v[172:175], v[144:147]\n" \
"v_exp_f32 v139, v139\n" \
"v_mfma_f32_16x16x32_bf16 v[148:151], a[156:159], v[172:175], 0\n" \
"ds_read_b128 a[240:243], v242 offset:28672\n" \
"v_cvt_pk_bf16_f32 v133, v138, v139\n" \
"v_mfma_f32_16x16x32_bf16 v[148:151], a[152:155], v[168:171], v[148:151]\n" \
"v_exp_f32 v140, v140\n" \
"v_mfma_f32_16x16x32_bf16 v[148:151], a[148:151], v[164:167], v[148:151]\n" \
"ds_read_b128 a[208:211], v242 offset:20480\n" \
"ds_read_b128 a[212:215], v242 offset:21504\n" \
"v_mfma_f32_16x16x32_bf16 v[148:151], a[144:147], v[160:163], v[148:151]\n" \
"v_exp_f32 v141, v141\n" \
"v_mfma_f32_16x16x32_bf16 v[152:155], a[160:163], v[160:163], 0\n" \
"ds_read_b128 a[244:247], v242 offset:29696\n" \
"v_cvt_pk_bf16_f32 v134, v140, v141\n" \
"v_mfma_f32_16x16x32_bf16 v[152:155], a[164:167], v[164:167], v[152:155]\n" \
"v_exp_f32 v142, v142\n" \
"v_mfma_f32_16x16x32_bf16 v[152:155], a[168:171], v[168:171], v[152:155]\n" \
"ds_write_b128 v249, a[120:123] offset:39936\n" \
"v_mfma_f32_16x16x32_bf16 v[152:155], a[172:175], v[172:175], v[152:155]\n" \
"v_exp_f32 v143, v143\n" \
"v_mfma_f32_16x16x32_bf16 v[156:159], a[188:191], v[172:175], 0\n" \
"ds_read_b128 a[248:251], v242 offset:30720\n" \
"v_cvt_pk_bf16_f32 v135, v142, v143\n" \
"v_mfma_f32_16x16x32_bf16 v[156:159], a[184:187], v[168:171], v[156:159]\n" \
"ds_read_b128 a[216:219], v242 offset:22528\n" \
"ds_read_b128 a[220:223], v242 offset:23552\n" \
"v_mfma_f32_16x16x32_bf16 v[156:159], a[180:183], v[164:167], v[156:159]\n" \
".Lst0_8_%=:\n" \
"s_cbranch_execz .Lst0_12_%=\n" \
"v_exp_f32 v144, v144\n" \
"v_mfma_f32_16x16x32_bf16 v[156:159], a[176:179], v[160:163], v[156:159]\n" \
"ds_read_b128 a[252:255], v242 offset:31744\n" \
"v_smfmac_f32_16x16x64_bf16 a[36:39], a[48:51], v[128:135], v241\n" \
"s_waitcnt lgkmcnt(14)\n" \
"v_mfma_f32_16x16x32_bf16 v[32:35], a[224:227], v[132:135], v[32:35]\n" \
"v_exp_f32 v145, v145\n" \
"v_mfma_f32_16x16x32_bf16 v[32:35], a[192:195], v[128:131], v[32:35]\n" \
"v_exp_f32 v146, v146\n" \
"v_mfma_f32_16x16x32_bf16 v[36:39], a[196:199], v[128:131], v[36:39]\n" \
"v_cvt_pk_bf16_f32 v144, v144, v145\n" \
"v_mfma_f32_16x16x32_bf16 v[36:39], a[228:231], v[132:135], v[36:39]\n" \
"s_waitcnt lgkmcnt(9)\n" \
"v_exp_f32 v147, v147\n" \
"v_mfma_f32_16x16x32_bf16 v[40:43], a[232:235], v[132:135], v[40:43]\n" \
"v_cvt_pk_bf16_f32 v145, v146, v147\n" \
"v_mfma_f32_16x16x32_bf16 v[40:43], a[200:203], v[128:131], v[40:43]\n" \
"v_exp_f32 v148, v148\n" \
"v_mfma_f32_16x16x32_bf16 v[44:47], a[204:207], v[128:131], v[44:47]\n" \
"v_mfma_f32_16x16x32_bf16 v[44:47], a[236:239], v[132:135], v[44:47]\n" \
"s_waitcnt lgkmcnt(5)\n" \
"v_exp_f32 v149, v149\n" \
"v_mfma_f32_16x16x32_bf16 v[48:51], a[240:243], v[132:135], v[48:51]\n" \
"v_cvt_pk_bf16_f32 v146, v148, v149\n" \
"v_mfma_f32_16x16x32_bf16 v[48:51], a[208:211], v[128:131], v[48:51]\n" \
"v_exp_f32 v150, v150\n" \
"v_mfma_f32_16x16x32_bf16 v[52:55], a[212:215], v[128:131], v[52:55]\n" \
"v_mfma_f32_16x16x32_bf16 v[52:55], a[244:247], v[132:135], v[52:55]\n" \
"s_waitcnt lgkmcnt(0)\n" \
"v_exp_f32 v151, v151\n" \
"v_mfma_f32_16x16x32_bf16 v[56:59], a[248:251], v[132:135], v[56:59]\n" \
"s_waitcnt vmcnt(2)\n" \
"v_cmp_eq_u32 vcc, 0, v253\n" \
"v_cvt_pk_bf16_f32 v147, v150, v151\n" \
"v_mfma_f32_16x16x32_bf16 v[56:59], a[216:219], v[128:131], v[56:59]\n" \
"v_exp_f32 v152, v152\n" \
"v_mfma_f32_16x16x32_bf16 v[60:63], a[220:223], v[128:131], v[60:63]\n" \
"v_mfma_f32_16x16x32_bf16 v[60:63], a[252:255], v[132:135], v[60:63]\n" \
"s_cbranch_vccnz .Lspv_%=\n" \
".Lsbv_%=:\n" \
"v_mfma_f32_16x16x32_bf16 v[128:131], a[128:131], v[176:179], 0\n" \
"s_mov_b32 m0, s19\n" \
"v_exp_f32 v153, v153\n" \
"v_mfma_f32_16x16x32_bf16 v[128:131], a[132:135], v[180:183], v[128:131]\n" \
"s_barrier\n" \
"v_exp_f32 v154, v154\n" \
"v_mfma_f32_16x16x32_bf16 v[128:131], a[136:139], v[184:187], v[128:131]\n" \
"buffer_load_dwordx4 v248, s[32:35], s88 offen offset:0 sc1 lds\n" \
"v_mfma_f32_16x16x32_bf16 v[128:131], a[140:143], v[188:191], v[128:131]\n" \
"v_exp_f32 v155, v155\n" \
"v_mfma_f32_16x16x32_bf16 v[132:135], a[156:159], v[188:191], 0\n" \
"v_cmp_gt_u32 s[98:99], 32, v251\n" \
"v_cvt_pk_bf16_f32 v148, v152, v153\n" \
"v_mfma_f32_16x16x32_bf16 v[132:135], a[152:155], v[184:187], v[132:135]\n" \
"v_exp_f32 v156, v156\n" \
"v_mfma_f32_16x16x32_bf16 v[132:135], a[148:151], v[180:183], v[132:135]\n" \
"buffer_load_dwordx4 v248, s[32:35], s88 offen offset:1024 sc1 lds\n" \
"v_mfma_f32_16x16x32_bf16 v[132:135], a[144:147], v[176:179], v[132:135]\n" \
"v_exp_f32 v157, v157\n" \
"v_mfma_f32_16x16x32_bf16 v[136:139], a[160:163], v[176:179], 0\n" \
"v_cndmask_b32 v253, v253, 0, s[98:99]\n" \
"v_cvt_pk_bf16_f32 v149, v154, v155\n" \
"v_mfma_f32_16x16x32_bf16 v[136:139], a[164:167], v[180:183], v[136:139]\n" \
"v_exp_f32 v158, v158\n" \
"v_mfma_f32_16x16x32_bf16 v[136:139], a[168:171], v[184:187], v[136:139]\n" \
"buffer_load_dwordx4 v248, s[32:35], s88 offen offset:2048 sc1 lds\n" \
"v_mfma_f32_16x16x32_bf16 v[136:139], a[172:175], v[188:191], v[136:139]\n" \
"v_exp_f32 v159, v159\n" \
"v_mfma_f32_16x16x32_bf16 v[140:143], a[188:191], v[188:191], 0\n" \
"v_max_u32_dpp v253, v253, v253 quad_perm:[1,0,3,2] row_mask:0xf bank_mask:0xf\n" \
"v_cvt_pk_bf16_f32 v150, v156, v157\n" \
"v_mfma_f32_16x16x32_bf16 v[140:143], a[184:187], v[184:187], v[140:143]\n" \
"ds_read_b128 a[128:131], v248 offset:32768\n" \
"v_cvt_pk_bf16_f32 v151, v158, v159\n" \
"v_mfma_f32_16x16x32_bf16 v[140:143], a[180:183], v[180:183], v[140:143]\n" \
"v_exp_f32 v128, v128\n" \
"v_mfma_f32_16x16x32_bf16 v[140:143], a[176:179], v[176:179], v[140:143]\n" \
"buffer_load_dwordx4 v248, s[32:35], s88 offen offset:3072 sc1 lds\n" \
"v_smfmac_f32_16x16x64_bf16 a[40:43], a[48:51], v[144:151], v241\n" \
"s_add_u32 s89, s88, 589824\n" \
"ds_read_b128 a[132:135], v248 offset:33792\n" \
"v_max_u32_dpp v253, v253, v253 quad_perm:[2,3,0,1] row_mask:0xf bank_mask:0xf\n" \
"v_mfma_f32_16x16x32_bf16 v[64:67], a[224:227], v[148:151], v[64:67]\n" \
"s_add_u32 s88, s88, s27\n" \
"v_exp_f32 v129, v129\n" \
"v_mfma_f32_16x16x32_bf16 v[64:67], a[192:195], v[144:147], v[64:67]\n" \
"s_add_u32 s19, s19, 0x4000\n" \
"v_exp_f32 v130, v130\n" \
"v_mfma_f32_16x16x32_bf16 v[68:71], a[196:199], v[144:147], v[68:71]\n" \
"s_bitset0_b32 s19, 16\n" \
"v_max_u32_dpp v253, v253, v253 row_half_mirror row_mask:0xf bank_mask:0xf\n" \
"v_cvt_pk_bf16_f32 v128, v128, v129\n" \
"v_mfma_f32_16x16x32_bf16 v[68:71], a[228:231], v[148:151], v[68:71]\n" \
"s_cmp_eq_u32 s88, s16\n" \
"v_exp_f32 v131, v131\n" \
"v_mfma_f32_16x16x32_bf16 v[72:75], a[232:235], v[148:151], v[72:75]\n" \
"s_cmov_b32 s88, s21\n" \
"v_max_u32_dpp v253, v253, v253 row_mirror row_mask:0xf bank_mask:0xf\n" \
"v_mov_b32 v254, v253\n" \
"v_mfma_f32_16x16x32_bf16 v[72:75], a[200:203], v[144:147], v[72:75]\n" \
"s_cmov_b32 s27, s91\n" \
"v_exp_f32 v132, v132\n" \
"v_mfma_f32_16x16x32_bf16 v[76:79], a[204:207], v[144:147], v[76:79]\n" \
"s_cmov_b32 s16, s77\n" \
"v_permlane16_swap_b32 v253, v254\n" \
"v_mfma_f32_16x16x32_bf16 v[76:79], a[236:239], v[148:151], v[76:79]\n" \
"s_cmov_b32 s21, s92\n" \
"v_exp_f32 v133, v133\n" \
"v_mfma_f32_16x16x32_bf16 v[80:83], a[240:243], v[148:151], v[80:83]\n" \
"s_cmovk_i32 s91, 0x4000\n" \
"v_max_u32 v253, v253, v254\n" \
"v_mov_b32 v254, v253\n" \
"v_mfma_f32_16x16x32_bf16 v[80:83], a[208:211], v[144:147], v[80:83]\n" \
"s_mov_b32 m0, s20\n" \
"ds_read_b128 a[136:139], v248 offset:34816\n" \
"ds_read_b128 a[140:143], v248 offset:35840\n" \
"v_exp_f32 v134, v134\n" \
"v_mfma_f32_16x16x32_bf16 v[84:87], a[212:215], v[144:147], v[84:87]\n" \
"buffer_load_dwordx4 v248, s[32:35], s89 offen offset:0 sc1 lds\n" \
"v_mfma_f32_16x16x32_bf16 v[84:87], a[244:247], v[148:151], v[84:87]\n" \
"v_exp_f32 v135, v135\n" \
"v_mfma_f32_16x16x32_bf16 v[88:91], a[248:251], v[148:151], v[88:91]\n" \
"buffer_load_dwordx4 v248, s[32:35], s89 offen offset:1024 sc1 lds\n" \
"v_mfma_f32_16x16x32_bf16 v[88:91], a[216:219], v[144:147], v[88:91]\n" \
"ds_read_b128 a[156:159], v248 offset:39936\n" \
"ds_read_b128 a[152:155], v248 offset:38912\n" \
"ds_read_b128 a[148:151], v248 offset:37888\n" \
"ds_read_b128 a[144:147], v248 offset:36864\n" \
"v_exp_f32 v136, v136\n" \
"v_mfma_f32_16x16x32_bf16 v[92:95], a[220:223], v[144:147], v[92:95]\n" \
"buffer_load_dwordx4 v248, s[32:35], s89 offen offset:2048 sc1 lds\n" \
"v_mfma_f32_16x16x32_bf16 v[92:95], a[252:255], v[148:151], v[92:95]\n" \
"s_waitcnt lgkmcnt(4)\n" \
"v_exp_f32 v137, v137\n" \
"v_mfma_f32_16x16x32_bf16 v[144:147], a[128:131], v[176:179], 0\n" \
"buffer_load_dwordx4 v248, s[32:35], s89 offen offset:3072 sc1 lds\n" \
"v_mfma_f32_16x16x32_bf16 v[144:147], a[132:135], v[180:183], v[144:147]\n" \
"s_add_u32 s20, s20, 0x4000\n" \
"ds_read_b128 a[160:163], v248 offset:40960\n" \
"ds_read_b128 a[164:167], v248 offset:41984\n" \
"ds_read_b128 a[168:171], v248 offset:43008\n" \
"ds_read_b128 a[172:175], v248 offset:44032\n" \
"v_exp_f32 v138, v138\n" \
"v_mfma_f32_16x16x32_bf16 v[144:147], a[136:139], v[184:187], v[144:147]\n" \
"s_and_b32 s20, s20, 0xffff\n" \
"v_permlane32_swap_b32 v253, v254\n" \
"v_mfma_f32_16x16x32_bf16 v[144:147], a[140:143], v[188:191], v[144:147]\n" \
"s_waitcnt lgkmcnt(4)\n" \
"v_exp_f32 v139, v139\n" \
"v_mfma_f32_16x16x32_bf16 v[148:151], a[156:159], v[188:191], 0\n" \
"s_bitset1_b32 s20, 16\n" \
"v_max_u32 v253, v253, v254\n" \
"v_max_f32 v254, 1.0, v253\n" \
"v_mfma_f32_16x16x32_bf16 v[148:151], a[152:155], v[184:187], v[148:151]\n" \
"s_mov_b32 m0, s19\n" \
"ds_read_b128 a[188:191], v248 offset:48128\n" \
"ds_read_b128 a[184:187], v248 offset:47104\n" \
"ds_read_b128 a[180:183], v248 offset:46080\n" \
"ds_read_b128 a[176:179], v248 offset:45056\n" \
"v_exp_f32 v140, v140\n" \
"v_mfma_f32_16x16x32_bf16 v[148:151], a[148:151], v[180:183], v[148:151]\n" \
"buffer_load_dwordx4 v248, s[32:35], s88 offen offset:0 sc1 lds\n" \
"v_mfma_f32_16x16x32_bf16 v[148:151], a[144:147], v[176:179], v[148:151]\n" \
"s_waitcnt lgkmcnt(4)\n" \
"v_exp_f32 v141, v141\n" \
"v_mfma_f32_16x16x32_bf16 v[152:155], a[160:163], v[176:179], v[232:235]\n" \
"buffer_load_dwordx4 v248, s[32:35], s88 offen offset:1024 sc1 lds\n" \
"v_mfma_f32_16x16x32_bf16 v[152:155], a[164:167], v[180:183], v[152:155]\n" \
"v_exp_f32 v142, v142\n" \
"v_mfma_f32_16x16x32_bf16 v[152:155], a[168:171], v[184:187], v[152:155]\n" \
"buffer_load_dwordx4 v248, s[32:35], s88 offen offset:2048 sc1 lds\n" \
"v_mfma_f32_16x16x32_bf16 v[152:155], a[172:175], v[188:191], v[152:155]\n" \
"s_waitcnt lgkmcnt(0)\n" \
"v_exp_f32 v143, v143\n" \
"v_mfma_f32_16x16x32_bf16 v[156:159], a[188:191], v[188:191], v[236:239]\n" \
"s_lshl_b32 s85, s68, 3\n" \
"v_exp_f32 v144, v144\n" \
"v_mfma_f32_16x16x32_bf16 v[156:159], a[184:187], v[184:187], v[156:159]\n" \
"s_add_u32 s85, s85, s67\n" \
"v_exp_f32 v145, v145\n" \
"v_mfma_f32_16x16x32_bf16 v[156:159], a[180:183], v[180:183], v[156:159]\n" \
"s_lshl2_add_u32 s85, s85, %[wid]\n" \
"v_exp_f32 v146, v146\n" \
"v_cvt_pk_bf16_f32 v129, v130, v131\n" \
"v_cvt_pk_bf16_f32 v130, v132, v133\n" \
"v_cvt_pk_bf16_f32 v131, v134, v135\n" \
"v_cvt_pk_bf16_f32 v132, v136, v137\n" \
"v_cvt_pk_bf16_f32 v133, v138, v139\n" \
"v_cvt_pk_bf16_f32 v134, v140, v141\n" \
"v_cvt_pk_bf16_f32 v135, v142, v143\n" \
"v_mfma_f32_16x16x32_bf16 v[156:159], a[176:179], v[176:179], v[156:159]\n" \
"s_lshl_b32 s85, s85, 2\n" \
"v_exp_f32 v147, v147\n" \
"v_smfmac_f32_16x16x64_bf16 a[44:47], a[48:51], v[128:135], v241\n" \
"s_barrier\n" \
"buffer_load_dwordx4 v248, s[32:35], s88 offen offset:3072 sc1 lds\n" \
"v_mfma_f32_16x16x32_bf16 v[96:99], a[224:227], v[132:135], v[96:99]\n" \
"s_add_u32 s89, s88, 589824\n" \
"v_exp_f32 v148, v148\n" \
"v_mfma_f32_16x16x32_bf16 v[96:99], a[192:195], v[128:131], v[96:99]\n" \
"s_add_u32 s88, s88, s27\n" \
"v_exp_f32 v149, v149\n" \
"v_mfma_f32_16x16x32_bf16 v[100:103], a[196:199], v[128:131], v[100:103]\n" \
"s_add_u32 s19, s19, 0x4000\n" \
"v_exp_f32 v150, v150\n" \
"v_mfma_f32_16x16x32_bf16 v[100:103], a[228:231], v[132:135], v[100:103]\n" \
"s_bitset0_b32 s19, 16\n" \
"v_exp_f32 v151, v151\n" \
"v_mfma_f32_16x16x32_bf16 v[104:107], a[232:235], v[132:135], v[104:107]\n" \
"s_cmp_eq_u32 s88, s16\n" \
"v_min_f32 v253, 1.0, v253\n" \
"v_cvt_pk_bf16_f32 v144, v144, v145\n" \
"v_mfma_f32_16x16x32_bf16 v[104:107], a[200:203], v[128:131], v[104:107]\n" \
"s_cmov_b32 s88, s21\n" \
"v_exp_f32 v152, v152\n" \
"v_mfma_f32_16x16x32_bf16 v[108:111], a[204:207], v[128:131], v[108:111]\n" \
"s_cmov_b32 s27, s91\n" \
"v_rcp_f32 v254, v254\n" \
"v_mfma_f32_16x16x32_bf16 v[108:111], a[236:239], v[132:135], v[108:111]\n" \
"s_cmov_b32 s16, s77\n" \
"v_exp_f32 v153, v153\n" \
"v_mfma_f32_16x16x32_bf16 v[112:115], a[240:243], v[132:135], v[112:115]\n" \
"s_cmov_b32 s21, s92\n" \
"v_rcp_f32 v253, v253\n" \
"v_mfma_f32_16x16x32_bf16 v[112:115], a[208:211], v[128:131], v[112:115]\n" \
"s_cmovk_i32 s91, 0x4000\n" \
"ds_read_b128 a[128:131], v248 offset:49152\n" \
"ds_read_b128 a[132:135], v248 offset:50176\n" \
"ds_read_b128 a[136:139], v248 offset:51200\n" \
"ds_read_b128 a[140:143], v248 offset:52224\n" \
"v_exp_f32 v154, v154\n" \
"v_mfma_f32_16x16x32_bf16 v[116:119], a[212:215], v[128:131], v[116:119]\n" \
"s_mov_b32 m0, s20\n" \
"v_mul_f32 v250, 0x7e000000, v254\n" \
"v_mul_f32 v246, 0x0d800000, v253\n" \
"v_mfma_f32_16x16x32_bf16 v[116:119], a[244:247], v[132:135], v[116:119]\n" \
"s_add_u32 s85, s85, 655360\n" \
"v_exp_f32 v155, v155\n" \
"v_mfma_f32_16x16x32_bf16 v[120:123], a[248:251], v[132:135], v[120:123]\n" \
"buffer_load_dwordx4 v248, s[32:35], s89 offen offset:0 sc1 lds\n" \
"v_mfma_f32_16x16x32_bf16 v[120:123], a[216:219], v[128:131], v[120:123]\n" \
"ds_read_b128 a[156:159], v248 offset:56320\n" \
"ds_read_b128 a[152:155], v248 offset:55296\n" \
"ds_read_b128 a[148:151], v248 offset:54272\n" \
"ds_read_b128 a[144:147], v248 offset:53248\n" \
"v_exp_f32 v156, v156\n" \
"v_mfma_f32_16x16x32_bf16 v[124:127], a[220:223], v[128:131], v[124:127]\n" \
"buffer_load_dwordx4 v248, s[32:35], s89 offen offset:1024 sc1 lds\n" \
"v_mfma_f32_16x16x32_bf16 v[124:127], a[252:255], v[132:135], v[124:127]\n" \
"s_waitcnt lgkmcnt(4)\n" \
"v_exp_f32 v157, v157\n" \
"v_mfma_f32_16x16x32_bf16 v[128:131], a[128:131], v[160:163], v[224:227]\n" \
"buffer_load_dwordx4 v248, s[32:35], s89 offen offset:2048 sc1 lds\n" \
"v_mfma_f32_16x16x32_bf16 v[128:131], a[132:135], v[164:167], v[128:131]\n" \
"v_exp_f32 v158, v158\n" \
"v_mfma_f32_16x16x32_bf16 v[128:131], a[136:139], v[168:171], v[128:131]\n" \
"buffer_load_dwordx4 v248, s[32:35], s89 offen offset:3072 sc1 lds\n" \
"ds_read_b128 a[224:227], v242 offset:40960\n" \
"ds_read_b128 a[192:195], v242 offset:32768\n" \
"ds_read_b128 a[196:199], v242 offset:33792\n" \
"ds_read_b128 a[228:231], v242 offset:41984\n" \
"v_mfma_f32_16x16x32_bf16 v[128:131], a[140:143], v[172:175], v[128:131]\n" \
"s_waitcnt lgkmcnt(4)\n" \
"v_exp_f32 v159, v159\n" \
"v_mfma_f32_16x16x32_bf16 v[132:135], a[156:159], v[172:175], v[228:231]\n" \
"s_waitcnt vmcnt(16)\n" \
"s_add_u32 s20, s20, 0x4000\n" \
"buffer_store_dword a56, off, s[32:35], s85 sc1\n" \
"v_mfma_f32_16x16x32_bf16 v[132:135], a[152:155], v[168:171], v[132:135]\n" \
"s_and_b32 s20, s20, 0xffff\n" \
"v_cvt_pk_bf16_f32 v145, v146, v147\n" \
"v_cvt_pk_bf16_f32 v146, v148, v149\n" \
"v_mfma_f32_16x16x32_bf16 v[132:135], a[148:151], v[164:167], v[132:135]\n" \
"s_bitset1_b32 s20, 16\n" \
"ds_read_b128 a[232:235], v242 offset:43008\n" \
"ds_read_b128 a[200:203], v242 offset:34816\n" \
"ds_read_b128 a[204:207], v242 offset:35840\n" \
"ds_read_b128 a[236:239], v242 offset:44032\n" \
"v_exp_f32 v128, v128\n" \
"v_cvt_pk_bf16_f32 v147, v150, v151\n" \
"v_cvt_pk_bf16_f32 v148, v152, v153\n" \
"v_cvt_pk_bf16_f32 v149, v154, v155\n" \
"v_cvt_pk_bf16_f32 v150, v156, v157\n" \
"v_cvt_pk_bf16_f32 v151, v158, v159\n" \
"v_mfma_f32_16x16x32_bf16 v[132:135], a[144:147], v[160:163], v[132:135]\n" \
"s_mov_b32 m0, s19\n" \
"ds_read_b128 a[160:163], v248 offset:57344\n" \
"ds_read_b128 a[240:243], v242 offset:45056\n" \
"v_smfmac_f32_16x16x64_bf16 a[44:47], a[48:51], v[144:151], v241\n" \
"s_waitcnt lgkmcnt(6)\n" \
"buffer_load_dwordx4 v248, s[32:35], s88 offen offset:0 sc1 lds\n" \
"v_mfma_f32_16x16x32_bf16 v[96:99], a[224:227], v[148:151], v[96:99]\n" \
"buffer_load_dwordx4 v248, s[32:35], s88 offen offset:1024 sc1 lds\n" \
"v_mfma_f32_16x16x32_bf16 v[96:99], a[192:195], v[144:147], v[96:99]\n" \
"ds_read_b128 a[164:167], v248 offset:58368\n" \
"ds_read_b128 a[208:211], v242 offset:36864\n" \
"ds_read_b128 a[168:171], v248 offset:59392\n" \
"ds_read_b128 a[212:215], v242 offset:37888\n" \
"ds_read_b128 a[172:175], v248 offset:60416\n" \
"ds_read_b128 a[244:247], v242 offset:46080\n" \
"v_exp_f32 v129, v129\n" \
"v_mfma_f32_16x16x32_bf16 v[100:103], a[196:199], v[144:147], v[100:103]\n" \
"ds_read_b128 a[188:191], v248 offset:64512\n" \
"v_cvt_pk_bf16_f32 v128, v128, v129\n" \
"v_mfma_f32_16x16x32_bf16 v[100:103], a[228:231], v[148:151], v[100:103]\n" \
"s_waitcnt lgkmcnt(9)\n" \
"ds_read_b128 a[248:251], v242 offset:47104\n" \
"ds_read_b128 a[216:219], v242 offset:38912\n" \
"v_mfma_f32_16x16x32_bf16 v[104:107], a[232:235], v[148:151], v[104:107]\n" \
"buffer_load_dwordx4 v248, s[32:35], s88 offen offset:2048 sc1 lds\n" \
"v_mfma_f32_16x16x32_bf16 v[104:107], a[200:203], v[144:147], v[104:107]\n" \
"ds_read_b128 a[184:187], v248 offset:63488\n" \
"ds_read_b128 a[180:183], v248 offset:62464\n" \
"ds_read_b128 a[220:223], v242 offset:39936\n" \
"ds_read_b128 a[176:179], v248 offset:61440\n" \
"ds_read_b128 a[252:255], v242 offset:48128\n" \
"v_mfma_f32_16x16x32_bf16 v[108:111], a[204:207], v[144:147], v[108:111]\n" \
"v_exp_f32 v130, v130\n" \
"v_mfma_f32_16x16x32_bf16 v[108:111], a[236:239], v[148:151], v[108:111]\n" \
"s_waitcnt lgkmcnt(8)\n" \
"ds_read_b128 a[192:195], v242 offset:49152\n" \
"ds_read_b128 a[196:199], v242 offset:50176\n" \
"v_mfma_f32_16x16x32_bf16 v[112:115], a[240:243], v[148:151], v[112:115]\n" \
"ds_read_b128 a[200:203], v242 offset:51200\n" \
"ds_read_b128 a[204:207], v242 offset:52224\n" \
"v_mfma_f32_16x16x32_bf16 v[112:115], a[208:211], v[144:147], v[112:115]\n" \
"buffer_load_dwordx4 v248, s[32:35], s88 offen offset:3072 sc1 lds\n" \
"v_mfma_f32_16x16x32_bf16 v[116:119], a[212:215], v[144:147], v[116:119]\n" \
"s_add_u32 s89, s88, 589824\n" \
"v_exp_f32 v131, v131\n" \
"v_mfma_f32_16x16x32_bf16 v[116:119], a[244:247], v[148:151], v[116:119]\n" \
"s_waitcnt lgkmcnt(4)\n" \
"ds_read_b128 a[208:211], v242 offset:53248\n" \
"v_cvt_pk_bf16_f32 v129, v130, v131\n" \
"v_mfma_f32_16x16x32_bf16 v[120:123], a[248:251], v[148:151], v[120:123]\n" \
"s_add_u32 s88, s88, s27\n" \
"s_add_u32 s19, s19, 0x4000\n" \
"ds_read_b128 a[212:215], v242 offset:54272\n" \
"v_mfma_f32_16x16x32_bf16 v[120:123], a[216:219], v[144:147], v[120:123]\n" \
"s_bitset0_b32 s19, 16\n" \
"s_cmp_eq_u32 s88, s16\n" \
"s_cmov_b32 s88, s21\n" \
"v_mfma_f32_16x16x32_bf16 v[124:127], a[220:223], v[144:147], v[124:127]\n" \
"s_cmov_b32 s27, s91\n" \
"v_exp_f32 v132, v132\n" \
"v_mfma_f32_16x16x32_bf16 v[124:127], a[252:255], v[148:151], v[124:127]\n" \
"s_cmov_b32 s16, s77\n" \
"ds_read_b128 a[216:219], v242 offset:55296\n" \
"ds_read_b128 a[220:223], v242 offset:56320\n" \
"v_mfma_f32_16x16x32_bf16 v[144:147], a[128:131], v[176:179], 0\n" \
"s_waitcnt vmcnt(13)\n" \
"s_barrier\n" \
"v_and_b32 v230, 7, v251\n" \
"v_mfma_f32_16x16x32_bf16 v[144:147], a[132:135], v[180:183], v[144:147]\n" \
"s_cmov_b32 s21, s92\n" \
"ds_read_b128 a[128:131], v248 offset:0\n" \
"v_lshrrev_b32 v232, 3, v251\n" \
"v_mfma_f32_16x16x32_bf16 v[144:147], a[136:139], v[184:187], v[144:147]\n" \
"s_cmovk_i32 s91, 0x4000\n" \
"v_exp_f32 v133, v133\n" \
"v_mfma_f32_16x16x32_bf16 v[144:147], a[140:143], v[188:191], v[144:147]\n" \
"s_mov_b32 m0, s20\n" \
"ds_read_b128 a[132:135], v248 offset:1024\n" \
"v_cvt_pk_bf16_f32 v130, v132, v133\n" \
"v_mfma_f32_16x16x32_bf16 v[148:151], a[156:159], v[188:191], 0\n" \
"buffer_load_dwordx4 v248, s[32:35], s89 offen offset:0 sc1 lds\n" \
"v_mfma_f32_16x16x32_bf16 v[148:151], a[152:155], v[184:187], v[148:151]\n" \
"ds_read_b128 a[224:227], v242 offset:57344\n" \
"ds_read_b128 a[136:139], v248 offset:2048\n" \
"v_mfma_f32_16x16x32_bf16 v[148:151], a[148:151], v[180:183], v[148:151]\n" \
"ds_read_b128 a[228:231], v242 offset:58368\n" \
"ds_read_b128 a[140:143], v248 offset:3072\n" \
"v_mfma_f32_16x16x32_bf16 v[148:151], a[144:147], v[176:179], v[148:151]\n" \
"v_exp_f32 v134, v134\n" \
"v_mfma_f32_16x16x32_bf16 v[152:155], a[160:163], v[176:179], 0\n" \
"buffer_load_dwordx4 v248, s[32:35], s89 offen offset:1024 sc1 lds\n" \
"v_mfma_f32_16x16x32_bf16 v[152:155], a[164:167], v[180:183], v[152:155]\n" \
"ds_read_b128 a[232:235], v242 offset:59392\n" \
"ds_read_b128 a[236:239], v242 offset:60416\n" \
"v_mfma_f32_16x16x32_bf16 v[152:155], a[168:171], v[184:187], v[152:155]\n" \
"ds_read_b128 a[240:243], v242 offset:61440\n" \
"ds_read_b128 a[244:247], v242 offset:62464\n" \
"v_mfma_f32_16x16x32_bf16 v[152:155], a[172:175], v[188:191], v[152:155]\n" \
"v_exp_f32 v135, v135\n" \
"v_mfma_f32_16x16x32_bf16 v[156:159], a[188:191], v[188:191], 0\n" \
"v_exp_f32 v144, v144\n" \
"v_mfma_f32_16x16x32_bf16 v[156:159], a[184:187], v[184:187], v[156:159]\n" \
"v_exp_f32 v145, v145\n" \
"v_mfma_f32_16x16x32_bf16 v[156:159], a[180:183], v[180:183], v[156:159]\n" \
"v_exp_f32 v146, v146\n" \
"v_cvt_pk_bf16_f32 v131, v134, v135\n" \
"v_mov_b32 v132, 0\n" \
"v_mov_b32 v133, 0\n" \
"v_mov_b32 v134, 0\n" \
"v_mov_b32 v135, 0\n" \
"v_mfma_f32_16x16x32_bf16 v[156:159], a[176:179], v[176:179], v[156:159]\n" \
"v_exp_f32 v147, v147\n" \
"v_smfmac_f32_16x16x64_bf16 a[40:43], a[48:51], v[128:135], v241\n" \
"s_waitcnt lgkmcnt(14)\n" \
"buffer_load_dwordx4 v248, s[32:35], s89 offen offset:2048 sc1 lds\n" \
"v_mfma_f32_16x16x32_bf16 v[64:67], a[192:195], v[128:131], v[64:67]\n" \
"v_exp_f32 v148, v148\n" \
"v_mfma_f32_16x16x32_bf16 v[68:71], a[196:199], v[128:131], v[68:71]\n" \
"v_exp_f32 v149, v149\n" \
"v_mfma_f32_16x16x32_bf16 v[72:75], a[200:203], v[128:131], v[72:75]\n" \
"v_exp_f32 v150, v150\n" \
"v_mfma_f32_16x16x32_bf16 v[76:79], a[204:207], v[128:131], v[76:79]\n" \
"s_waitcnt lgkmcnt(10)\n" \
"v_exp_f32 v151, v151\n" \
"v_mfma_f32_16x16x32_bf16 v[80:83], a[208:211], v[128:131], v[80:83]\n" \
"buffer_load_dwordx4 v248, s[32:35], s89 offen offset:3072 sc1 lds\n" \
"v_mfma_f32_16x16x32_bf16 v[84:87], a[212:215], v[128:131], v[84:87]\n" \
"s_add_u32 s20, s20, 0x4000\n" \
"ds_read_b128 a[156:159], v248 offset:7168\n" \
"ds_read_b128 a[248:251], v242 offset:63488\n" \
"ds_read_b128 a[152:155], v248 offset:6144\n" \
"ds_read_b128 a[252:255], v242 offset:64512\n" \
"ds_read_b128 a[148:151], v248 offset:5120\n" \
"ds_read_b128 a[144:147], v248 offset:4096\n" \
"v_exp_f32 v152, v152\n" \
"v_mfma_f32_16x16x32_bf16 v[88:91], a[216:219], v[128:131], v[88:91]\n" \
"s_and_b32 s20, s20, 0xffff\n" \
"v_exp_f32 v153, v153\n" \
"v_mfma_f32_16x16x32_bf16 v[92:95], a[220:223], v[128:131], v[92:95]\n" \
"s_waitcnt lgkmcnt(10)\n" \
"ds_read_b128 a[160:163], v248 offset:8192\n" \
"ds_read_b128 a[164:167], v248 offset:9216\n" \
"ds_read_b128 a[168:171], v248 offset:10240\n" \
"ds_read_b128 a[172:175], v248 offset:11264\n" \
"v_cvt_pk_bf16_f32 v144, v144, v145\n" \
"v_cvt_pk_bf16_f32 v145, v146, v147\n" \
"v_cvt_pk_bf16_f32 v146, v148, v149\n" \
"v_cvt_pk_bf16_f32 v147, v150, v151\n" \
"s_bitset1_b32 s20, 16\n" \
"v_lshlrev_b32 v231, 4, v230\n" \
"v_mul_u32_u24 v230, 7168, v230\n" \
"v_lshl_add_u32 v230, v232, 4, v230\n" \
"v_lshl_or_b32 v232, v232, 14, v231\n" \
"v_and_b32 v231, 3, v251\n" \
"v_lshlrev_b32 v231, 7, v231\n" \
"v_bfe_u32 v237, v251, 4, 1\n" \
"v_lshlrev_b32 v237, 1, v237\n" \
"v_and_b32 v238, 3, v251\n" \
"v_xor_b32 v237, v237, v238\n" \
"v_lshrrev_b32 v238, 5, v251\n" \
"v_lshl_or_b32 v237, v238, 2, v237\n" \
"v_bfe_u32 v238, v251, 2, 3\n" \
"v_lshl_or_b32 v237, v238, 6, v237\n" \
"v_lshlrev_b32 v237, 4, v237\n" \
"v_and_b32 v238, 7, v251\n" \
"v_lshlrev_b32 v238, 7, v238\n" \
".Ldj_%=:\n" \
"s_mov_b32 s86, 0\n" \
"s_and_b32 s85, s65, 15\n" \
"s_lshl2_add_u32 s85, s85, %[wid]\n" \
"s_lshl_b32 s22, s64, 6\n" \
"s_add_u32 s85, s85, s22\n" \
"s_lshl_b32 s85, s85, 13\n" \
"s_lshl_b32 s95, s67, 1\n" \
"s_cmp_lt_u32 s67, 4\n" \
"s_cbranch_scc1 .Lld_%=\n" \
"s_lshl_b32 s95, s84, 1\n" \
"s_sub_u32 s95, s95, 1\n" \
"s_cbranch_scc1 .Ll0_%=\n" \
".p2align 3\n" \
".Lloop0_%=:\n" \
  \
"v_mfma_f32_16x16x32_bf16 v[128:131], a[128:131], v[192:195], 0\n" \
"s_add_u32 s86, s86, 0x4000\n" \
"ds_read_b128 a[188:191], v240 offset:15360\n" \
"v_cvt_pk_bf16_f32 v148, v152, v153\n" \
"v_mfma_f32_16x16x32_bf16 v[128:131], a[132:135], v[196:199], v[128:131]\n" \
"s_bitset0_b32 s86, 16\n" \
"v_exp_f32 v154, v154\n" \
"v_mfma_f32_16x16x32_bf16 v[128:131], a[136:139], v[200:203], v[128:131]\n" \
"ds_read_b128 a[184:187], v240 offset:14336\n" \
"v_add_u32 v242, 0x10000, v240\n" \
"v_mfma_f32_16x16x32_bf16 v[128:131], a[140:143], v[204:207], v[128:131]\n" \
"s_waitcnt lgkmcnt(6)\n" \
"v_exp_f32 v155, v155\n" \
"v_mfma_f32_16x16x32_bf16 v[132:135], a[156:159], v[204:207], 0\n" \
"s_mov_b32 m0, s19\n" \
"ds_read_b128 a[180:183], v240 offset:13312\n" \
"v_cvt_pk_bf16_f32 v149, v154, v155\n" \
"v_mfma_f32_16x16x32_bf16 v[132:135], a[152:155], v[200:203], v[132:135]\n" \
"ds_read_b128 a[176:179], v240 offset:12288\n" \
"v_exp_f32 v156, v156\n" \
"v_mfma_f32_16x16x32_bf16 v[132:135], a[148:151], v[196:199], v[132:135]\n" \
"buffer_load_dwordx4 v248, s[32:35], s88 offen offset:0 sc1 lds\n" \
"v_mfma_f32_16x16x32_bf16 v[132:135], a[144:147], v[192:195], v[132:135]\n" \
"s_waitcnt lgkmcnt(4)\n" \
"v_exp_f32 v157, v157\n" \
"v_mfma_f32_16x16x32_bf16 v[136:139], a[160:163], v[192:195], 0\n" \
"v_cvt_pk_bf16_f32 v150, v156, v157\n" \
"v_mfma_f32_16x16x32_bf16 v[136:139], a[164:167], v[196:199], v[136:139]\n" \
"v_exp_f32 v158, v158\n" \
"v_mfma_f32_16x16x32_bf16 v[136:139], a[168:171], v[200:203], v[136:139]\n" \
"buffer_load_dwordx4 v248, s[32:35], s88 offen offset:1024 sc1 lds\n" \
"v_mfma_f32_16x16x32_bf16 v[136:139], a[172:175], v[204:207], v[136:139]\n" \
"s_waitcnt lgkmcnt(0)\n" \
"v_exp_f32 v159, v159\n" \
"v_mfma_f32_16x16x32_bf16 v[140:143], a[188:191], v[204:207], 0\n" \
"v_add_u32 v240, s86, v248\n" \
"v_cvt_pk_bf16_f32 v151, v158, v159\n" \
"v_mfma_f32_16x16x32_bf16 v[140:143], a[184:187], v[200:203], v[140:143]\n" \
"buffer_load_dwordx4 v248, s[32:35], s88 offen offset:2048 sc1 lds\n" \
"v_mfma_f32_16x16x32_bf16 v[140:143], a[180:183], v[196:199], v[140:143]\n" \
"v_exp_f32 v128, v128\n" \
"v_mfma_f32_16x16x32_bf16 v[140:143], a[176:179], v[192:195], v[140:143]\n" \
"buffer_load_dwordx4 v248, s[32:35], s88 offen offset:3072 sc1 lds\n" \
"v_smfmac_f32_16x16x64_bf16 a[44:47], a[48:51], v[144:151], v241\n" \
"s_add_u32 s89, s88, 589824\n" \
"s_add_u32 s88, s88, s27\n" \
"s_add_u32 s19, s19, 0x4000\n" \
"v_mfma_f32_16x16x32_bf16 v[96:99], a[224:227], v[148:151], v[96:99]\n" \
"s_bitset0_b32 s19, 16\n" \
"v_exp_f32 v129, v129\n" \
"v_mfma_f32_16x16x32_bf16 v[96:99], a[192:195], v[144:147], v[96:99]\n" \
"s_cmp_eq_u32 s88, s16\n" \
"v_exp_f32 v130, v130\n" \
"v_mfma_f32_16x16x32_bf16 v[100:103], a[196:199], v[144:147], v[100:103]\n" \
"s_cmov_b32 s88, s21\n" \
"ds_read_b128 a[224:227], v242 offset:8192\n" \
"v_cvt_pk_bf16_f32 v128, v128, v129\n" \
"v_mfma_f32_16x16x32_bf16 v[100:103], a[228:231], v[148:151], v[100:103]\n" \
"s_cmov_b32 s27, s91\n" \
"v_exp_f32 v131, v131\n" \
"v_mfma_f32_16x16x32_bf16 v[104:107], a[232:235], v[148:151], v[104:107]\n" \
"s_cmov_b32 s16, s77\n" \
"ds_read_b128 a[192:195], v242 offset:0\n" \
"v_cvt_pk_bf16_f32 v129, v130, v131\n" \
"v_mfma_f32_16x16x32_bf16 v[104:107], a[200:203], v[144:147], v[104:107]\n" \
"s_cmov_b32 s21, s92\n" \
"v_exp_f32 v132, v132\n" \
"v_mfma_f32_16x16x32_bf16 v[108:111], a[204:207], v[144:147], v[108:111]\n" \
"s_cmovk_i32 s91, 0x4000\n" \
"ds_read_b128 a[196:199], v242 offset:1024\n" \
"ds_read_b128 a[228:231], v242 offset:9216\n" \
"v_mfma_f32_16x16x32_bf16 v[108:111], a[236:239], v[148:151], v[108:111]\n" \
"v_exp_f32 v133, v133\n" \
"v_mfma_f32_16x16x32_bf16 v[112:115], a[240:243], v[148:151], v[112:115]\n" \
"ds_read_b128 a[232:235], v242 offset:10240\n" \
"v_cvt_pk_bf16_f32 v130, v132, v133\n" \
"v_mfma_f32_16x16x32_bf16 v[112:115], a[208:211], v[144:147], v[112:115]\n" \
"v_exp_f32 v134, v134\n" \
"v_mfma_f32_16x16x32_bf16 v[116:119], a[212:215], v[144:147], v[116:119]\n" \
"ds_read_b128 a[200:203], v242 offset:2048\n" \
"ds_read_b128 a[204:207], v242 offset:3072\n" \
"v_mfma_f32_16x16x32_bf16 v[116:119], a[244:247], v[148:151], v[116:119]\n" \
"v_exp_f32 v135, v135\n" \
"v_mfma_f32_16x16x32_bf16 v[120:123], a[248:251], v[148:151], v[120:123]\n" \
"ds_read_b128 a[236:239], v242 offset:11264\n" \
"v_cvt_pk_bf16_f32 v131, v134, v135\n" \
"v_mfma_f32_16x16x32_bf16 v[120:123], a[216:219], v[144:147], v[120:123]\n" \
"v_exp_f32 v136, v136\n" \
"v_mfma_f32_16x16x32_bf16 v[124:127], a[220:223], v[144:147], v[124:127]\n" \
"ds_read_b128 a[240:243], v242 offset:12288\n" \
"v_mfma_f32_16x16x32_bf16 v[124:127], a[252:255], v[148:151], v[124:127]\n" \
"s_waitcnt vmcnt(12)\n" \
"v_exp_f32 v137, v137\n" \
"v_mfma_f32_16x16x32_bf16 v[144:147], a[128:131], v[208:211], 0\n" \
"s_barrier\n" \
"ds_read_b128 a[208:211], v242 offset:4096\n" \
"v_cvt_pk_bf16_f32 v132, v136, v137\n" \
"v_mfma_f32_16x16x32_bf16 v[144:147], a[132:135], v[212:215], v[144:147]\n" \
"s_mov_b32 m0, s20\n" \
"v_exp_f32 v138, v138\n" \
"v_mfma_f32_16x16x32_bf16 v[144:147], a[136:139], v[216:219], v[144:147]\n" \
"buffer_load_dwordx4 v248, s[32:35], s89 offen offset:0 sc1 lds\n" \
"v_mfma_f32_16x16x32_bf16 v[144:147], a[140:143], v[220:223], v[144:147]\n" \
"v_exp_f32 v139, v139\n" \
"v_mfma_f32_16x16x32_bf16 v[148:151], a[156:159], v[220:223], 0\n" \
"ds_read_b128 a[212:215], v242 offset:5120\n" \
"v_cvt_pk_bf16_f32 v133, v138, v139\n" \
"v_mfma_f32_16x16x32_bf16 v[148:151], a[152:155], v[216:219], v[148:151]\n" \
"v_exp_f32 v140, v140\n" \
"v_mfma_f32_16x16x32_bf16 v[148:151], a[148:151], v[212:215], v[148:151]\n" \
"ds_read_b128 a[244:247], v242 offset:13312\n" \
"ds_read_b128 a[248:251], v242 offset:14336\n" \
"v_mfma_f32_16x16x32_bf16 v[148:151], a[144:147], v[208:211], v[148:151]\n" \
"v_exp_f32 v141, v141\n" \
"v_mfma_f32_16x16x32_bf16 v[152:155], a[160:163], v[208:211], 0\n" \
"buffer_load_dwordx4 v248, s[32:35], s89 offen offset:1024 sc1 lds\n" \
"v_mfma_f32_16x16x32_bf16 v[152:155], a[164:167], v[212:215], v[152:155]\n" \
"v_exp_f32 v142, v142\n" \
"v_mfma_f32_16x16x32_bf16 v[152:155], a[168:171], v[216:219], v[152:155]\n" \
"ds_read_b128 a[216:219], v242 offset:6144\n" \
"v_cvt_pk_bf16_f32 v134, v140, v141\n" \
"v_mfma_f32_16x16x32_bf16 v[152:155], a[172:175], v[220:223], v[152:155]\n" \
"v_exp_f32 v143, v143\n" \
"v_mfma_f32_16x16x32_bf16 v[156:159], a[188:191], v[220:223], 0\n" \
"ds_read_b128 a[128:131], v240 offset:0\n" \
"v_cvt_pk_bf16_f32 v135, v142, v143\n" \
"v_mfma_f32_16x16x32_bf16 v[156:159], a[184:187], v[216:219], v[156:159]\n" \
"ds_read_b128 a[132:135], v240 offset:1024\n" \
"ds_read_b128 a[220:223], v242 offset:7168\n" \
"v_mfma_f32_16x16x32_bf16 v[156:159], a[180:183], v[212:215], v[156:159]\n" \
"v_exp_f32 v144, v144\n" \
"v_mfma_f32_16x16x32_bf16 v[156:159], a[176:179], v[208:211], v[156:159]\n" \
"buffer_load_dwordx4 v248, s[32:35], s89 offen offset:2048 sc1 lds\n" \
"v_smfmac_f32_16x16x64_bf16 a[32:35], a[48:51], v[128:135], v241\n" \
"s_waitcnt lgkmcnt(13)\n" \
"ds_read_b128 a[136:139], v240 offset:2048\n" \
"ds_read_b128 a[252:255], v242 offset:15360\n" \
"v_mfma_f32_16x16x32_bf16 v[0:3], a[224:227], v[132:135], v[0:3]\n" \
"v_exp_f32 v145, v145\n" \
"v_mfma_f32_16x16x32_bf16 v[0:3], a[192:195], v[128:131], v[0:3]\n" \
"v_exp_f32 v146, v146\n" \
"v_mfma_f32_16x16x32_bf16 v[4:7], a[196:199], v[128:131], v[4:7]\n" \
"ds_read_b128 a[140:143], v240 offset:3072\n" \
"v_cvt_pk_bf16_f32 v144, v144, v145\n" \
"v_mfma_f32_16x16x32_bf16 v[4:7], a[228:231], v[132:135], v[4:7]\n" \
"s_waitcnt lgkmcnt(12)\n" \
"v_exp_f32 v147, v147\n" \
"v_mfma_f32_16x16x32_bf16 v[8:11], a[232:235], v[132:135], v[8:11]\n" \
"buffer_load_dwordx4 v248, s[32:35], s89 offen offset:3072 sc1 lds\n" \
"v_mfma_f32_16x16x32_bf16 v[8:11], a[200:203], v[128:131], v[8:11]\n" \
"s_add_u32 s20, s20, 0x4000\n" \
"v_exp_f32 v148, v148\n" \
"v_mfma_f32_16x16x32_bf16 v[12:15], a[204:207], v[128:131], v[12:15]\n" \
".Lst0_9_%=:\n" \
"s_cbranch_execz .Lst0_13_%=\n" \
"s_and_b32 s20, s20, 0xffff\n" \
"ds_read_b128 a[156:159], v240 offset:7168\n" \
"v_cvt_pk_bf16_f32 v145, v146, v147\n" \
"v_mfma_f32_16x16x32_bf16 v[12:15], a[236:239], v[132:135], v[12:15]\n" \
"s_waitcnt lgkmcnt(9)\n" \
"v_exp_f32 v149, v149\n" \
"v_mfma_f32_16x16x32_bf16 v[16:19], a[240:243], v[132:135], v[16:19]\n" \
"s_bitset1_b32 s20, 16\n" \
"ds_read_b128 a[152:155], v240 offset:6144\n" \
"v_cvt_pk_bf16_f32 v146, v148, v149\n" \
"v_mfma_f32_16x16x32_bf16 v[16:19], a[208:211], v[128:131], v[16:19]\n" \
"v_exp_f32 v150, v150\n" \
"v_mfma_f32_16x16x32_bf16 v[20:23], a[212:215], v[128:131], v[20:23]\n" \
"ds_read_b128 a[148:151], v240 offset:5120\n" \
"ds_read_b128 a[144:147], v240 offset:4096\n" \
"v_mfma_f32_16x16x32_bf16 v[20:23], a[244:247], v[132:135], v[20:23]\n" \
"s_waitcnt lgkmcnt(5)\n" \
"v_exp_f32 v151, v151\n" \
"v_mfma_f32_16x16x32_bf16 v[24:27], a[248:251], v[132:135], v[24:27]\n" \
"ds_read_b128 a[160:163], v240 offset:8192\n" \
"v_cvt_pk_bf16_f32 v147, v150, v151\n" \
"v_mfma_f32_16x16x32_bf16 v[24:27], a[216:219], v[128:131], v[24:27]\n" \
"v_exp_f32 v152, v152\n" \
"v_mfma_f32_16x16x32_bf16 v[28:31], a[220:223], v[128:131], v[28:31]\n" \
"ds_read_b128 a[164:167], v240 offset:9216\n" \
"ds_read_b128 a[168:171], v240 offset:10240\n" \
"v_mfma_f32_16x16x32_bf16 v[28:31], a[252:255], v[132:135], v[28:31]\n" \
"s_waitcnt lgkmcnt(7)\n" \
"v_exp_f32 v153, v153\n" \
"v_mfma_f32_16x16x32_bf16 v[128:131], a[128:131], v[192:195], 0\n" \
"s_add_u32 s86, s86, 0x4000\n" \
"ds_read_b128 a[172:175], v240 offset:11264\n" \
"v_cvt_pk_bf16_f32 v148, v152, v153\n" \
"v_mfma_f32_16x16x32_bf16 v[128:131], a[132:135], v[196:199], v[128:131]\n" \
"s_bitset0_b32 s86, 16\n" \
"v_exp_f32 v154, v154\n" \
"v_mfma_f32_16x16x32_bf16 v[128:131], a[136:139], v[200:203], v[128:131]\n" \
"ds_read_b128 a[188:191], v240 offset:15360\n" \
"v_add_u32 v242, 0x10000, v240\n" \
"v_mfma_f32_16x16x32_bf16 v[128:131], a[140:143], v[204:207], v[128:131]\n" \
"s_waitcnt lgkmcnt(5)\n" \
"v_exp_f32 v155, v155\n" \
"v_mfma_f32_16x16x32_bf16 v[132:135], a[156:159], v[204:207], 0\n" \
"s_mov_b32 m0, s19\n" \
"ds_read_b128 a[184:187], v240 offset:14336\n" \
"v_cvt_pk_bf16_f32 v149, v154, v155\n" \
"v_mfma_f32_16x16x32_bf16 v[132:135], a[152:155], v[200:203], v[132:135]\n" \
"ds_read_b128 a[180:183], v240 offset:13312\n" \
"ds_read_b128 a[176:179], v240 offset:12288\n" \
"v_exp_f32 v156, v156\n" \
"v_mfma_f32_16x16x32_bf16 v[132:135], a[148:151], v[196:199], v[132:135]\n" \
"buffer_load_dwordx4 v248, s[32:35], s88 offen offset:0 sc1 lds\n" \
"v_mfma_f32_16x16x32_bf16 v[132:135], a[144:147], v[192:195], v[132:135]\n" \
"s_waitcnt lgkmcnt(4)\n" \
"v_exp_f32 v157, v157\n" \
"v_mfma_f32_16x16x32_bf16 v[136:139], a[160:163], v[192:195], 0\n" \
"v_cvt_pk_bf16_f32 v150, v156, v157\n" \
"v_mfma_f32_16x16x32_bf16 v[136:139], a[164:167], v[196:199], v[136:139]\n" \
"v_exp_f32 v158, v158\n" \
"v_mfma_f32_16x16x32_bf16 v[136:139], a[168:171], v[200:203], v[136:139]\n" \
"v_add_u32 v240, s86, v248\n" \
"v_mfma_f32_16x16x32_bf16 v[136:139], a[172:175], v[204:207], v[136:139]\n" \
"s_waitcnt lgkmcnt(0)\n" \
"v_exp_f32 v159, v159\n" \
"v_mfma_f32_16x16x32_bf16 v[140:143], a[188:191], v[204:207], 0\n" \
"v_cvt_pk_bf16_f32 v151, v158, v159\n" \
"v_mfma_f32_16x16x32_bf16 v[140:143], a[184:187], v[200:203], v[140:143]\n" \
"buffer_load_dwordx4 v248, s[32:35], s88 offen offset:1024 sc1 lds\n" \
"v_mfma_f32_16x16x32_bf16 v[140:143], a[180:183], v[196:199], v[140:143]\n" \
"v_exp_f32 v128, v128\n" \
"v_mfma_f32_16x16x32_bf16 v[140:143], a[176:179], v[192:195], v[140:143]\n" \
"v_smfmac_f32_16x16x64_bf16 a[36:39], a[48:51], v[144:151], v241\n" \
"v_mfma_f32_16x16x32_bf16 v[32:35], a[224:227], v[148:151], v[32:35]\n" \
"v_exp_f32 v129, v129\n" \
"v_mfma_f32_16x16x32_bf16 v[32:35], a[192:195], v[144:147], v[32:35]\n" \
"v_exp_f32 v130, v130\n" \
"v_mfma_f32_16x16x32_bf16 v[36:39], a[196:199], v[144:147], v[36:39]\n" \
"buffer_load_dwordx4 v248, s[32:35], s88 offen offset:2048 sc1 lds\n" \
"v_mfma_f32_16x16x32_bf16 v[36:39], a[228:231], v[148:151], v[36:39]\n" \
"v_exp_f32 v131, v131\n" \
"v_mfma_f32_16x16x32_bf16 v[40:43], a[232:235], v[148:151], v[40:43]\n" \
"v_cvt_pk_bf16_f32 v128, v128, v129\n" \
"v_cvt_pk_bf16_f32 v129, v130, v131\n" \
"v_mfma_f32_16x16x32_bf16 v[40:43], a[200:203], v[144:147], v[40:43]\n" \
"v_exp_f32 v132, v132\n" \
"v_mfma_f32_16x16x32_bf16 v[44:47], a[204:207], v[144:147], v[44:47]\n" \
"ds_read_b128 a[224:227], v242 offset:8192\n" \
"ds_read_b128 a[192:195], v242 offset:0\n" \
"v_mfma_f32_16x16x32_bf16 v[44:47], a[236:239], v[148:151], v[44:47]\n" \
"v_exp_f32 v133, v133\n" \
"v_mfma_f32_16x16x32_bf16 v[48:51], a[240:243], v[148:151], v[48:51]\n" \
"buffer_load_dwordx4 v248, s[32:35], s88 offen offset:3072 sc1 lds\n" \
"v_mfma_f32_16x16x32_bf16 v[48:51], a[208:211], v[144:147], v[48:51]\n" \
"s_mov_b32 m0, s87\n" \
"v_exp_f32 v134, v134\n" \
"v_mfma_f32_16x16x32_bf16 v[52:55], a[212:215], v[144:147], v[52:55]\n" \
"s_add_u32 s89, s88, 589824\n" \
"ds_read_b128 a[196:199], v242 offset:1024\n" \
"v_cvt_pk_bf16_f32 v130, v132, v133\n" \
"v_mfma_f32_16x16x32_bf16 v[52:55], a[244:247], v[148:151], v[52:55]\n" \
"s_add_u32 s88, s88, s27\n" \
"buffer_load_dwordx4 v237, s[48:51], s93 offen lds\n" \
"v_mfma_f32_16x16x32_bf16 v[56:59], a[248:251], v[148:151], v[56:59]\n" \
"s_add_u32 s93, s93, 8192\n" \
"v_exp_f32 v135, v135\n" \
"v_mfma_f32_16x16x32_bf16 v[56:59], a[216:219], v[144:147], v[56:59]\n" \
"s_add_u32 s87, s87, 1024\n" \
"v_exp_f32 v136, v136\n" \
"v_mfma_f32_16x16x32_bf16 v[60:63], a[220:223], v[144:147], v[60:63]\n" \
"s_and_b32 s22, s87, 0xfff\n" \
"ds_read_b128 a[228:231], v242 offset:9216\n" \
"v_cvt_pk_bf16_f32 v131, v134, v135\n" \
"v_mfma_f32_16x16x32_bf16 v[60:63], a[252:255], v[148:151], v[60:63]\n" \
"s_cselect_b32 s22, 0, 0xffff8080\n" \
"v_exp_f32 v137, v137\n" \
"v_mfma_f32_16x16x32_bf16 v[144:147], a[128:131], v[208:211], 0\n" \
"s_add_u32 s93, s93, s22\n" \
"ds_read_b128 a[232:235], v242 offset:10240\n" \
"v_cvt_pk_bf16_f32 v132, v136, v137\n" \
"v_mfma_f32_16x16x32_bf16 v[144:147], a[132:135], v[212:215], v[144:147]\n" \
"s_add_u32 s19, s19, 0x4000\n" \
"v_exp_f32 v138, v138\n" \
"v_mfma_f32_16x16x32_bf16 v[144:147], a[136:139], v[216:219], v[144:147]\n" \
"s_bitset0_b32 s19, 16\n" \
"s_cmp_eq_u32 s88, s16\n" \
"buffer_load_dword v245, v230, s[40:43], s38 offen offset:512\n" \
"v_mfma_f32_16x16x32_bf16 v[144:147], a[140:143], v[220:223], v[144:147]\n" \
"s_cmov_b32 s88, s21\n" \
"v_exp_f32 v139, v139\n" \
"v_mfma_f32_16x16x32_bf16 v[148:151], a[156:159], v[220:223], 0\n" \
"s_cmov_b32 s27, s91\n" \
"ds_read_b128 a[200:203], v242 offset:2048\n" \
"v_cvt_pk_bf16_f32 v133, v138, v139\n" \
"v_mfma_f32_16x16x32_bf16 v[148:151], a[152:155], v[216:219], v[148:151]\n" \
"s_cmov_b32 s16, s77\n" \
"v_exp_f32 v140, v140\n" \
"v_mfma_f32_16x16x32_bf16 v[148:151], a[148:151], v[212:215], v[148:151]\n" \
"s_cmov_b32 s21, s92\n" \
"ds_read_b128 a[204:207], v242 offset:3072\n" \
"ds_read_b128 a[236:239], v242 offset:11264\n" \
"v_mfma_f32_16x16x32_bf16 v[148:151], a[144:147], v[208:211], v[148:151]\n" \
"s_cmovk_i32 s91, 0x4000\n" \
"v_exp_f32 v141, v141\n" \
"v_mfma_f32_16x16x32_bf16 v[152:155], a[160:163], v[208:211], 0\n" \
"buffer_load_dword v245, v238, s[48:51], s85 offen\n" \
"v_cvt_pk_bf16_f32 v134, v140, v141\n" \
"v_mfma_f32_16x16x32_bf16 v[152:155], a[164:167], v[212:215], v[152:155]\n" \
"v_exp_f32 v142, v142\n" \
"v_mfma_f32_16x16x32_bf16 v[152:155], a[168:171], v[216:219], v[152:155]\n" \
"ds_read_b128 a[240:243], v242 offset:12288\n" \
"ds_read_b128 a[208:211], v242 offset:4096\n" \
"v_mfma_f32_16x16x32_bf16 v[152:155], a[172:175], v[220:223], v[152:155]\n" \
"v_exp_f32 v143, v143\n" \
"v_mfma_f32_16x16x32_bf16 v[156:159], a[188:191], v[220:223], 0\n" \
"ds_read_b128 a[212:215], v242 offset:5120\n" \
"v_cvt_pk_bf16_f32 v135, v142, v143\n" \
"v_mfma_f32_16x16x32_bf16 v[156:159], a[184:187], v[216:219], v[156:159]\n" \
"buffer_load_dword v245, v231, s[60:63], s72 offen\n" \
"v_mfma_f32_16x16x32_bf16 v[156:159], a[180:183], v[212:215], v[156:159]\n" \
"s_add_u32 s85, s85, 1024\n" \
"v_exp_f32 v144, v144\n" \
"v_mfma_f32_16x16x32_bf16 v[156:159], a[176:179], v[208:211], v[156:159]\n" \
"s_add_u32 s72, s72, 512\n" \
"ds_read_b128 a[244:247], v242 offset:13312\n" \
"ds_read_b128 a[248:251], v242 offset:14336\n" \
"v_smfmac_f32_16x16x64_bf16 a[32:35], a[48:51], v[128:135], v241\n" \
"s_waitcnt lgkmcnt(9)\n" \
"ds_read_b128 a[216:219], v242 offset:6144\n" \
"ds_read_b128 a[220:223], v242 offset:7168\n" \
"v_mfma_f32_16x16x32_bf16 v[0:3], a[224:227], v[132:135], v[0:3]\n" \
"v_exp_f32 v145, v145\n" \
"v_mfma_f32_16x16x32_bf16 v[0:3], a[192:195], v[128:131], v[0:3]\n" \
"v_exp_f32 v146, v146\n" \
"v_mfma_f32_16x16x32_bf16 v[4:7], a[196:199], v[128:131], v[4:7]\n" \
"ds_read_b128 a[252:255], v242 offset:15360\n" \
"v_cvt_pk_bf16_f32 v144, v144, v145\n" \
"v_mfma_f32_16x16x32_bf16 v[4:7], a[228:231], v[132:135], v[4:7]\n" \
"s_waitcnt lgkmcnt(8)\n" \
"v_exp_f32 v147, v147\n" \
"v_mfma_f32_16x16x32_bf16 v[8:11], a[232:235], v[132:135], v[8:11]\n" \
"v_cvt_pk_bf16_f32 v145, v146, v147\n" \
"v_mfma_f32_16x16x32_bf16 v[8:11], a[200:203], v[128:131], v[8:11]\n" \
"v_exp_f32 v148, v148\n" \
"v_mfma_f32_16x16x32_bf16 v[12:15], a[204:207], v[128:131], v[12:15]\n" \
"v_mfma_f32_16x16x32_bf16 v[12:15], a[236:239], v[132:135], v[12:15]\n" \
"s_waitcnt lgkmcnt(4)\n" \
"v_exp_f32 v149, v149\n" \
"v_mfma_f32_16x16x32_bf16 v[16:19], a[240:243], v[132:135], v[16:19]\n" \
"v_cvt_pk_bf16_f32 v146, v148, v149\n" \
"v_mfma_f32_16x16x32_bf16 v[16:19], a[208:211], v[128:131], v[16:19]\n" \
"v_exp_f32 v150, v150\n" \
"v_mfma_f32_16x16x32_bf16 v[20:23], a[212:215], v[128:131], v[20:23]\n" \
"v_mfma_f32_16x16x32_bf16 v[20:23], a[244:247], v[132:135], v[20:23]\n" \
"s_waitcnt lgkmcnt(0)\n" \
"v_exp_f32 v151, v151\n" \
"v_mfma_f32_16x16x32_bf16 v[24:27], a[248:251], v[132:135], v[24:27]\n" \
"v_cvt_pk_bf16_f32 v147, v150, v151\n" \
"v_mfma_f32_16x16x32_bf16 v[24:27], a[216:219], v[128:131], v[24:27]\n" \
"v_exp_f32 v152, v152\n" \
"v_mfma_f32_16x16x32_bf16 v[28:31], a[220:223], v[128:131], v[28:31]\n" \
"v_mfma_f32_16x16x32_bf16 v[28:31], a[252:255], v[132:135], v[28:31]\n" \
"v_exp_f32 v153, v153\n" \
"v_mfma_f32_16x16x32_bf16 v[128:131], a[128:131], v[160:163], 0\n" \
"v_cvt_pk_bf16_f32 v148, v152, v153\n" \
"v_mfma_f32_16x16x32_bf16 v[128:131], a[132:135], v[164:167], v[128:131]\n" \
"v_exp_f32 v154, v154\n" \
"v_mfma_f32_16x16x32_bf16 v[128:131], a[136:139], v[168:171], v[128:131]\n" \
"v_mfma_f32_16x16x32_bf16 v[128:131], a[140:143], v[172:175], v[128:131]\n" \
"v_exp_f32 v155, v155\n" \
"v_mfma_f32_16x16x32_bf16 v[132:135], a[156:159], v[172:175], 0\n" \
"v_cvt_pk_bf16_f32 v149, v154, v155\n" \
"v_mfma_f32_16x16x32_bf16 v[132:135], a[152:155], v[168:171], v[132:135]\n" \
"v_exp_f32 v156, v156\n" \
"v_mfma_f32_16x16x32_bf16 v[132:135], a[148:151], v[164:167], v[132:135]\n" \
"v_mfma_f32_16x16x32_bf16 v[132:135], a[144:147], v[160:163], v[132:135]\n" \
"v_exp_f32 v157, v157\n" \
"v_mfma_f32_16x16x32_bf16 v[136:139], a[160:163], v[160:163], 0\n" \
"v_cvt_pk_bf16_f32 v150, v156, v157\n" \
"v_mfma_f32_16x16x32_bf16 v[136:139], a[164:167], v[164:167], v[136:139]\n" \
"v_exp_f32 v158, v158\n" \
"v_mfma_f32_16x16x32_bf16 v[136:139], a[168:171], v[168:171], v[136:139]\n" \
"v_mfma_f32_16x16x32_bf16 v[136:139], a[172:175], v[172:175], v[136:139]\n" \
"v_exp_f32 v159, v159\n" \
"v_mfma_f32_16x16x32_bf16 v[140:143], a[188:191], v[172:175], 0\n" \
"v_cvt_pk_bf16_f32 v151, v158, v159\n" \
"v_mfma_f32_16x16x32_bf16 v[140:143], a[184:187], v[168:171], v[140:143]\n" \
"v_mfma_f32_16x16x32_bf16 v[140:143], a[180:183], v[164:167], v[140:143]\n" \
"v_exp_f32 v128, v128\n" \
"v_mfma_f32_16x16x32_bf16 v[140:143], a[176:179], v[160:163], v[140:143]\n" \
"v_smfmac_f32_16x16x64_bf16 a[36:39], a[48:51], v[144:151], v241\n" \
"v_mfma_f32_16x16x32_bf16 v[32:35], a[224:227], v[148:151], v[32:35]\n" \
"v_exp_f32 v129, v129\n" \
"v_mfma_f32_16x16x32_bf16 v[32:35], a[192:195], v[144:147], v[32:35]\n" \
"s_waitcnt vmcnt(16)\n" \
"v_exp_f32 v130, v130\n" \
"v_mfma_f32_16x16x32_bf16 v[36:39], a[196:199], v[144:147], v[36:39]\n" \
"buffer_store_dwordx4 a[60:63], v232, s[32:35], s39 offen sc1\n" \
"v_mfma_f32_16x16x32_bf16 v[36:39], a[228:231], v[148:151], v[36:39]\n" \
"v_exp_f32 v131, v131\n" \
"v_mfma_f32_16x16x32_bf16 v[40:43], a[232:235], v[148:151], v[40:43]\n" \
"v_cvt_pk_bf16_f32 v128, v128, v129\n" \
"v_cvt_pk_bf16_f32 v129, v130, v131\n" \
"v_mfma_f32_16x16x32_bf16 v[40:43], a[200:203], v[144:147], v[40:43]\n" \
"v_exp_f32 v132, v132\n" \
"v_mfma_f32_16x16x32_bf16 v[44:47], a[204:207], v[144:147], v[44:47]\n" \
"buffer_load_dwordx4 a[60:63], v230, s[40:43], s38 offen\n" \
"v_mfma_f32_16x16x32_bf16 v[44:47], a[236:239], v[148:151], v[44:47]\n" \
"s_add_u32 s38, s38, 512\n" \
"v_exp_f32 v133, v133\n" \
"v_mfma_f32_16x16x32_bf16 v[48:51], a[240:243], v[148:151], v[48:51]\n" \
"s_add_u32 s39, s39, 0x80000\n" \
"v_cvt_pk_bf16_f32 v130, v132, v133\n" \
"v_mfma_f32_16x16x32_bf16 v[48:51], a[208:211], v[144:147], v[48:51]\n" \
"v_exp_f32 v134, v134\n" \
"v_mfma_f32_16x16x32_bf16 v[52:55], a[212:215], v[144:147], v[52:55]\n" \
"v_mfma_f32_16x16x32_bf16 v[52:55], a[244:247], v[148:151], v[52:55]\n" \
"v_exp_f32 v135, v135\n" \
"v_mfma_f32_16x16x32_bf16 v[56:59], a[248:251], v[148:151], v[56:59]\n" \
"v_cvt_pk_bf16_f32 v131, v134, v135\n" \
"v_mfma_f32_16x16x32_bf16 v[56:59], a[216:219], v[144:147], v[56:59]\n" \
"v_exp_f32 v136, v136\n" \
"v_mfma_f32_16x16x32_bf16 v[60:63], a[220:223], v[144:147], v[60:63]\n" \
"v_mfma_f32_16x16x32_bf16 v[60:63], a[252:255], v[148:151], v[60:63]\n" \
"s_waitcnt vmcnt(18)\n" \
"v_exp_f32 v137, v137\n" \
"v_mfma_f32_16x16x32_bf16 v[144:147], a[128:131], v[176:179], 0\n" \
"s_barrier\n" \
"s_mov_b32 m0, s20\n" \
"v_cvt_pk_bf16_f32 v132, v136, v137\n" \
"v_mfma_f32_16x16x32_bf16 v[144:147], a[132:135], v[180:183], v[144:147]\n" \
"v_exp_f32 v138, v138\n" \
"v_mfma_f32_16x16x32_bf16 v[144:147], a[136:139], v[184:187], v[144:147]\n" \
"buffer_load_dwordx4 v248, s[32:35], s89 offen offset:0 sc1 lds\n" \
"v_mfma_f32_16x16x32_bf16 v[144:147], a[140:143], v[188:191], v[144:147]\n" \
"v_exp_f32 v139, v139\n" \
"v_mfma_f32_16x16x32_bf16 v[148:151], a[156:159], v[188:191], 0\n" \
"ds_read_b128 a[128:131], v240 offset:0\n" \
"v_cvt_pk_bf16_f32 v133, v138, v139\n" \
"v_mfma_f32_16x16x32_bf16 v[148:151], a[152:155], v[184:187], v[148:151]\n" \
"v_exp_f32 v140, v140\n" \
"v_mfma_f32_16x16x32_bf16 v[148:151], a[148:151], v[180:183], v[148:151]\n" \
"ds_read_b128 a[132:135], v240 offset:1024\n" \
"ds_read_b128 a[136:139], v240 offset:2048\n" \
"v_mfma_f32_16x16x32_bf16 v[148:151], a[144:147], v[176:179], v[148:151]\n" \
"v_exp_f32 v141, v141\n" \
"v_mfma_f32_16x16x32_bf16 v[152:155], a[160:163], v[176:179], 0\n" \
"buffer_load_dwordx4 v248, s[32:35], s89 offen offset:1024 sc1 lds\n" \
"v_mfma_f32_16x16x32_bf16 v[152:155], a[164:167], v[180:183], v[152:155]\n" \
"v_exp_f32 v142, v142\n" \
"v_mfma_f32_16x16x32_bf16 v[152:155], a[168:171], v[184:187], v[152:155]\n" \
"ds_read_b128 a[140:143], v240 offset:3072\n" \
"v_cvt_pk_bf16_f32 v134, v140, v141\n" \
"v_mfma_f32_16x16x32_bf16 v[152:155], a[172:175], v[188:191], v[152:155]\n" \
"v_exp_f32 v143, v143\n" \
"v_mfma_f32_16x16x32_bf16 v[156:159], a[188:191], v[188:191], 0\n" \
"ds_read_b128 a[156:159], v240 offset:7168\n" \
"v_cvt_pk_bf16_f32 v135, v142, v143\n" \
"v_mfma_f32_16x16x32_bf16 v[156:159], a[184:187], v[184:187], v[156:159]\n" \
"ds_read_b128 a[152:155], v240 offset:6144\n" \
"ds_read_b128 a[148:151], v240 offset:5120\n" \
"v_mfma_f32_16x16x32_bf16 v[156:159], a[180:183], v[180:183], v[156:159]\n" \
"v_exp_f32 v144, v144\n" \
"v_mfma_f32_16x16x32_bf16 v[156:159], a[176:179], v[176:179], v[156:159]\n" \
"buffer_load_dwordx4 v248, s[32:35], s89 offen offset:2048 sc1 lds\n" \
"v_smfmac_f32_16x16x64_bf16 a[40:43], a[48:51], v[128:135], v241\n" \
"ds_read_b128 a[144:147], v240 offset:4096\n" \
"ds_read_b128 a[160:163], v240 offset:8192\n" \
"v_mfma_f32_16x16x32_bf16 v[64:67], a[224:227], v[132:135], v[64:67]\n" \
"v_exp_f32 v145, v145\n" \
"v_mfma_f32_16x16x32_bf16 v[64:67], a[192:195], v[128:131], v[64:67]\n" \
"v_exp_f32 v146, v146\n" \
"v_mfma_f32_16x16x32_bf16 v[68:71], a[196:199], v[128:131], v[68:71]\n" \
"ds_read_b128 a[164:167], v240 offset:9216\n" \
"v_cvt_pk_bf16_f32 v144, v144, v145\n" \
"v_mfma_f32_16x16x32_bf16 v[68:71], a[228:231], v[132:135], v[68:71]\n" \
"v_exp_f32 v147, v147\n" \
"v_mfma_f32_16x16x32_bf16 v[72:75], a[232:235], v[132:135], v[72:75]\n" \
"buffer_load_dwordx4 v248, s[32:35], s89 offen offset:3072 sc1 lds\n" \
"v_mfma_f32_16x16x32_bf16 v[72:75], a[200:203], v[128:131], v[72:75]\n" \
"s_add_u32 s20, s20, 0x4000\n" \
"v_exp_f32 v148, v148\n" \
"v_mfma_f32_16x16x32_bf16 v[76:79], a[204:207], v[128:131], v[76:79]\n" \
"s_and_b32 s20, s20, 0xffff\n" \
"ds_read_b128 a[168:171], v240 offset:10240\n" \
"v_cvt_pk_bf16_f32 v145, v146, v147\n" \
"v_mfma_f32_16x16x32_bf16 v[76:79], a[236:239], v[132:135], v[76:79]\n" \
"s_bitset1_b32 s20, 16\n" \
"v_exp_f32 v149, v149\n" \
"v_mfma_f32_16x16x32_bf16 v[80:83], a[240:243], v[132:135], v[80:83]\n" \
"ds_read_b128 a[172:175], v240 offset:11264\n" \
"v_cvt_pk_bf16_f32 v146, v148, v149\n" \
"v_mfma_f32_16x16x32_bf16 v[80:83], a[208:211], v[128:131], v[80:83]\n" \
"v_exp_f32 v150, v150\n" \
"v_mfma_f32_16x16x32_bf16 v[84:87], a[212:215], v[128:131], v[84:87]\n" \
"s_sub_u32 s95, s95, 1\n" \
"v_mfma_f32_16x16x32_bf16 v[84:87], a[244:247], v[132:135], v[84:87]\n" \
"v_exp_f32 v151, v151\n" \
"v_mfma_f32_16x16x32_bf16 v[88:91], a[248:251], v[132:135], v[88:91]\n" \
"v_cvt_pk_bf16_f32 v147, v150, v151\n" \
"v_mfma_f32_16x16x32_bf16 v[88:91], a[216:219], v[128:131], v[88:91]\n" \
"v_exp_f32 v152, v152\n" \
"v_mfma_f32_16x16x32_bf16 v[92:95], a[220:223], v[128:131], v[92:95]\n" \
"s_waitcnt lgkmcnt(8)\n" \
"v_exp_f32 v153, v153\n" \
"v_mfma_f32_16x16x32_bf16 v[92:95], a[252:255], v[132:135], v[92:95]\n" \
"s_cbranch_scc0 .Lloop0_%=\n" \
".Ll0_%=:\n" \
".Lst0_10_%=:\n" \
"s_cbranch_execz .Lst0_14_%=\n" \
"s_lshl_b32 s95, s84, 1\n" \
"s_sub_u32 s95, 7, s95\n" \
".p2align 3\n" \
".Lloop_%=:\n" \
  \
"v_mfma_f32_16x16x32_bf16 v[128:131], a[128:131], v[192:195], 0\n" \
"s_add_u32 s86, s86, 0x4000\n" \
"ds_read_b128 a[188:191], v240 offset:15360\n" \
"v_cvt_pk_bf16_f32 v148, v152, v153\n" \
"v_mfma_f32_16x16x32_bf16 v[128:131], a[132:135], v[196:199], v[128:131]\n" \
"s_bitset0_b32 s86, 16\n" \
"v_exp_f32 v154, v154\n" \
"v_mfma_f32_16x16x32_bf16 v[128:131], a[136:139], v[200:203], v[128:131]\n" \
"ds_read_b128 a[184:187], v240 offset:14336\n" \
"v_add_u32 v242, 0x10000, v240\n" \
"v_mfma_f32_16x16x32_bf16 v[128:131], a[140:143], v[204:207], v[128:131]\n" \
"s_waitcnt lgkmcnt(6)\n" \
"v_exp_f32 v155, v155\n" \
"v_mfma_f32_16x16x32_bf16 v[132:135], a[156:159], v[204:207], 0\n" \
"s_mov_b32 m0, s19\n" \
"ds_read_b128 a[180:183], v240 offset:13312\n" \
"v_cvt_pk_bf16_f32 v149, v154, v155\n" \
"v_mfma_f32_16x16x32_bf16 v[132:135], a[152:155], v[200:203], v[132:135]\n" \
"ds_read_b128 a[176:179], v240 offset:12288\n" \
"v_exp_f32 v156, v156\n" \
"v_mfma_f32_16x16x32_bf16 v[132:135], a[148:151], v[196:199], v[132:135]\n" \
"buffer_load_dwordx4 v248, s[32:35], s88 offen offset:0 sc1 lds\n" \
"v_mfma_f32_16x16x32_bf16 v[132:135], a[144:147], v[192:195], v[132:135]\n" \
"s_waitcnt lgkmcnt(4)\n" \
"v_exp_f32 v157, v157\n" \
"v_mfma_f32_16x16x32_bf16 v[136:139], a[160:163], v[192:195], 0\n" \
"v_cvt_pk_bf16_f32 v150, v156, v157\n" \
"v_mfma_f32_16x16x32_bf16 v[136:139], a[164:167], v[196:199], v[136:139]\n" \
"v_exp_f32 v158, v158\n" \
"v_mfma_f32_16x16x32_bf16 v[136:139], a[168:171], v[200:203], v[136:139]\n" \
"v_add_u32 v240, s86, v248\n" \
"v_mfma_f32_16x16x32_bf16 v[136:139], a[172:175], v[204:207], v[136:139]\n" \
"s_waitcnt lgkmcnt(0)\n" \
"v_exp_f32 v159, v159\n" \
"v_mfma_f32_16x16x32_bf16 v[140:143], a[188:191], v[204:207], 0\n" \
"v_cvt_pk_bf16_f32 v151, v158, v159\n" \
"v_mfma_f32_16x16x32_bf16 v[140:143], a[184:187], v[200:203], v[140:143]\n" \
"buffer_load_dwordx4 v248, s[32:35], s88 offen offset:1024 sc1 lds\n" \
"v_mfma_f32_16x16x32_bf16 v[140:143], a[180:183], v[196:199], v[140:143]\n" \
"v_exp_f32 v128, v128\n" \
"v_mfma_f32_16x16x32_bf16 v[140:143], a[176:179], v[192:195], v[140:143]\n" \
"v_smfmac_f32_16x16x64_bf16 a[44:47], a[48:51], v[144:151], v241\n" \
"v_mfma_f32_16x16x32_bf16 v[96:99], a[224:227], v[148:151], v[96:99]\n" \
"v_exp_f32 v129, v129\n" \
"v_mfma_f32_16x16x32_bf16 v[96:99], a[192:195], v[144:147], v[96:99]\n" \
"v_exp_f32 v130, v130\n" \
"v_mfma_f32_16x16x32_bf16 v[100:103], a[196:199], v[144:147], v[100:103]\n" \
"buffer_load_dwordx4 v248, s[32:35], s88 offen offset:2048 sc1 lds\n" \
"v_mfma_f32_16x16x32_bf16 v[100:103], a[228:231], v[148:151], v[100:103]\n" \
"v_exp_f32 v131, v131\n" \
"v_mfma_f32_16x16x32_bf16 v[104:107], a[232:235], v[148:151], v[104:107]\n" \
"v_cvt_pk_bf16_f32 v128, v128, v129\n" \
"v_cvt_pk_bf16_f32 v129, v130, v131\n" \
"v_mfma_f32_16x16x32_bf16 v[104:107], a[200:203], v[144:147], v[104:107]\n" \
"v_exp_f32 v132, v132\n" \
"v_mfma_f32_16x16x32_bf16 v[108:111], a[204:207], v[144:147], v[108:111]\n" \
"ds_read_b128 a[224:227], v242 offset:8192\n" \
"ds_read_b128 a[192:195], v242 offset:0\n" \
"v_mfma_f32_16x16x32_bf16 v[108:111], a[236:239], v[148:151], v[108:111]\n" \
"v_exp_f32 v133, v133\n" \
"v_mfma_f32_16x16x32_bf16 v[112:115], a[240:243], v[148:151], v[112:115]\n" \
"buffer_load_dwordx4 v248, s[32:35], s88 offen offset:3072 sc1 lds\n" \
"v_mfma_f32_16x16x32_bf16 v[112:115], a[208:211], v[144:147], v[112:115]\n" \
"s_mov_b32 m0, s87\n" \
"v_exp_f32 v134, v134\n" \
"v_mfma_f32_16x16x32_bf16 v[116:119], a[212:215], v[144:147], v[116:119]\n" \
"s_add_u32 s89, s88, 589824\n" \
"ds_read_b128 a[196:199], v242 offset:1024\n" \
"v_cvt_pk_bf16_f32 v130, v132, v133\n" \
"v_mfma_f32_16x16x32_bf16 v[116:119], a[244:247], v[148:151], v[116:119]\n" \
"s_add_u32 s88, s88, s27\n" \
"buffer_load_dwordx4 v237, s[48:51], s93 offen lds\n" \
"v_mfma_f32_16x16x32_bf16 v[120:123], a[248:251], v[148:151], v[120:123]\n" \
"s_add_u32 s93, s93, 8192\n" \
"v_exp_f32 v135, v135\n" \
"v_mfma_f32_16x16x32_bf16 v[120:123], a[216:219], v[144:147], v[120:123]\n" \
"s_add_u32 s87, s87, 1024\n" \
"v_exp_f32 v136, v136\n" \
"v_mfma_f32_16x16x32_bf16 v[124:127], a[220:223], v[144:147], v[124:127]\n" \
"s_and_b32 s22, s87, 0xfff\n" \
"ds_read_b128 a[228:231], v242 offset:9216\n" \
"v_cvt_pk_bf16_f32 v131, v134, v135\n" \
"v_mfma_f32_16x16x32_bf16 v[124:127], a[252:255], v[148:151], v[124:127]\n" \
"s_cselect_b32 s22, 0, 0xffff8080\n" \
"v_exp_f32 v137, v137\n" \
"v_mfma_f32_16x16x32_bf16 v[144:147], a[128:131], v[208:211], 0\n" \
"s_add_u32 s93, s93, s22\n" \
"ds_read_b128 a[232:235], v242 offset:10240\n" \
"v_cvt_pk_bf16_f32 v132, v136, v137\n" \
"v_mfma_f32_16x16x32_bf16 v[144:147], a[132:135], v[212:215], v[144:147]\n" \
"s_add_u32 s19, s19, 0x4000\n" \
"v_exp_f32 v138, v138\n" \
"v_mfma_f32_16x16x32_bf16 v[144:147], a[136:139], v[216:219], v[144:147]\n" \
"s_bitset0_b32 s19, 16\n" \
"s_cmp_eq_u32 s88, s16\n" \
"buffer_load_dword v245, v230, s[40:43], s38 offen offset:512\n" \
"v_mfma_f32_16x16x32_bf16 v[144:147], a[140:143], v[220:223], v[144:147]\n" \
"s_cmov_b32 s88, s21\n" \
"v_exp_f32 v139, v139\n" \
"v_mfma_f32_16x16x32_bf16 v[148:151], a[156:159], v[220:223], 0\n" \
"s_cmov_b32 s27, s91\n" \
"ds_read_b128 a[200:203], v242 offset:2048\n" \
"v_cvt_pk_bf16_f32 v133, v138, v139\n" \
"v_mfma_f32_16x16x32_bf16 v[148:151], a[152:155], v[216:219], v[148:151]\n" \
"s_cmov_b32 s16, s77\n" \
"v_exp_f32 v140, v140\n" \
"v_mfma_f32_16x16x32_bf16 v[148:151], a[148:151], v[212:215], v[148:151]\n" \
"s_cmov_b32 s21, s92\n" \
"ds_read_b128 a[204:207], v242 offset:3072\n" \
"ds_read_b128 a[236:239], v242 offset:11264\n" \
"v_mfma_f32_16x16x32_bf16 v[148:151], a[144:147], v[208:211], v[148:151]\n" \
"s_cmovk_i32 s91, 0x4000\n" \
"v_exp_f32 v141, v141\n" \
"v_mfma_f32_16x16x32_bf16 v[152:155], a[160:163], v[208:211], 0\n" \
"buffer_load_dword v245, v238, s[48:51], s85 offen\n" \
"v_cvt_pk_bf16_f32 v134, v140, v141\n" \
"v_mfma_f32_16x16x32_bf16 v[152:155], a[164:167], v[212:215], v[152:155]\n" \
"v_exp_f32 v142, v142\n" \
"v_mfma_f32_16x16x32_bf16 v[152:155], a[168:171], v[216:219], v[152:155]\n" \
"ds_read_b128 a[240:243], v242 offset:12288\n" \
"ds_read_b128 a[208:211], v242 offset:4096\n" \
"v_mfma_f32_16x16x32_bf16 v[152:155], a[172:175], v[220:223], v[152:155]\n" \
"v_exp_f32 v143, v143\n" \
"v_mfma_f32_16x16x32_bf16 v[156:159], a[188:191], v[220:223], 0\n" \
"ds_read_b128 a[212:215], v242 offset:5120\n" \
"v_cvt_pk_bf16_f32 v135, v142, v143\n" \
"v_mfma_f32_16x16x32_bf16 v[156:159], a[184:187], v[216:219], v[156:159]\n" \
"buffer_load_dword v245, v231, s[60:63], s72 offen\n" \
"v_mfma_f32_16x16x32_bf16 v[156:159], a[180:183], v[212:215], v[156:159]\n" \
"s_add_u32 s85, s85, 1024\n" \
"v_exp_f32 v144, v144\n" \
"v_mfma_f32_16x16x32_bf16 v[156:159], a[176:179], v[208:211], v[156:159]\n" \
"s_add_u32 s72, s72, 512\n" \
"ds_read_b128 a[244:247], v242 offset:13312\n" \
"ds_read_b128 a[248:251], v242 offset:14336\n" \
"v_smfmac_f32_16x16x64_bf16 a[32:35], a[48:51], v[128:135], v241\n" \
"s_waitcnt lgkmcnt(9)\n" \
"ds_read_b128 a[216:219], v242 offset:6144\n" \
"ds_read_b128 a[220:223], v242 offset:7168\n" \
"v_mfma_f32_16x16x32_bf16 v[0:3], a[224:227], v[132:135], v[0:3]\n" \
"v_exp_f32 v145, v145\n" \
"v_mfma_f32_16x16x32_bf16 v[0:3], a[192:195], v[128:131], v[0:3]\n" \
"v_exp_f32 v146, v146\n" \
"v_mfma_f32_16x16x32_bf16 v[4:7], a[196:199], v[128:131], v[4:7]\n" \
"ds_read_b128 a[252:255], v242 offset:15360\n" \
"v_cvt_pk_bf16_f32 v144, v144, v145\n" \
"v_mfma_f32_16x16x32_bf16 v[4:7], a[228:231], v[132:135], v[4:7]\n" \
"s_waitcnt lgkmcnt(8)\n" \
"v_exp_f32 v147, v147\n" \
"v_mfma_f32_16x16x32_bf16 v[8:11], a[232:235], v[132:135], v[8:11]\n" \
"v_cvt_pk_bf16_f32 v145, v146, v147\n" \
"v_mfma_f32_16x16x32_bf16 v[8:11], a[200:203], v[128:131], v[8:11]\n" \
"v_exp_f32 v148, v148\n" \
"v_mfma_f32_16x16x32_bf16 v[12:15], a[204:207], v[128:131], v[12:15]\n" \
"v_mfma_f32_16x16x32_bf16 v[12:15], a[236:239], v[132:135], v[12:15]\n" \
"s_waitcnt lgkmcnt(4)\n" \
"v_exp_f32 v149, v149\n" \
"v_mfma_f32_16x16x32_bf16 v[16:19], a[240:243], v[132:135], v[16:19]\n" \
"v_cvt_pk_bf16_f32 v146, v148, v149\n" \
"v_mfma_f32_16x16x32_bf16 v[16:19], a[208:211], v[128:131], v[16:19]\n" \
"v_exp_f32 v150, v150\n" \
"v_mfma_f32_16x16x32_bf16 v[20:23], a[212:215], v[128:131], v[20:23]\n" \
"v_mfma_f32_16x16x32_bf16 v[20:23], a[244:247], v[132:135], v[20:23]\n" \
"s_waitcnt lgkmcnt(0)\n" \
"v_exp_f32 v151, v151\n" \
"v_mfma_f32_16x16x32_bf16 v[24:27], a[248:251], v[132:135], v[24:27]\n" \
"v_cvt_pk_bf16_f32 v147, v150, v151\n" \
"v_mfma_f32_16x16x32_bf16 v[24:27], a[216:219], v[128:131], v[24:27]\n" \
"v_exp_f32 v152, v152\n" \
"v_mfma_f32_16x16x32_bf16 v[28:31], a[220:223], v[128:131], v[28:31]\n" \
"v_mfma_f32_16x16x32_bf16 v[28:31], a[252:255], v[132:135], v[28:31]\n" \
"v_exp_f32 v153, v153\n" \
"v_mfma_f32_16x16x32_bf16 v[128:131], a[128:131], v[160:163], 0\n" \
"v_cvt_pk_bf16_f32 v148, v152, v153\n" \
"v_mfma_f32_16x16x32_bf16 v[128:131], a[132:135], v[164:167], v[128:131]\n" \
"v_exp_f32 v154, v154\n" \
"v_mfma_f32_16x16x32_bf16 v[128:131], a[136:139], v[168:171], v[128:131]\n" \
"v_mfma_f32_16x16x32_bf16 v[128:131], a[140:143], v[172:175], v[128:131]\n" \
"v_exp_f32 v155, v155\n" \
"v_mfma_f32_16x16x32_bf16 v[132:135], a[156:159], v[172:175], 0\n" \
"v_cvt_pk_bf16_f32 v149, v154, v155\n" \
"v_mfma_f32_16x16x32_bf16 v[132:135], a[152:155], v[168:171], v[132:135]\n" \
"v_exp_f32 v156, v156\n" \
"v_mfma_f32_16x16x32_bf16 v[132:135], a[148:151], v[164:167], v[132:135]\n" \
"v_mfma_f32_16x16x32_bf16 v[132:135], a[144:147], v[160:163], v[132:135]\n" \
"v_exp_f32 v157, v157\n" \
"v_mfma_f32_16x16x32_bf16 v[136:139], a[160:163], v[160:163], 0\n" \
"v_cvt_pk_bf16_f32 v150, v156, v157\n" \
"v_mfma_f32_16x16x32_bf16 v[136:139], a[164:167], v[164:167], v[136:139]\n" \
"v_exp_f32 v158, v158\n" \
"v_mfma_f32_16x16x32_bf16 v[136:139], a[168:171], v[168:171], v[136:139]\n" \
"v_mfma_f32_16x16x32_bf16 v[136:139], a[172:175], v[172:175], v[136:139]\n" \
"v_exp_f32 v159, v159\n" \
"v_mfma_f32_16x16x32_bf16 v[140:143], a[188:191], v[172:175], 0\n" \
"v_cvt_pk_bf16_f32 v151, v158, v159\n" \
"v_mfma_f32_16x16x32_bf16 v[140:143], a[184:187], v[168:171], v[140:143]\n" \
"v_mfma_f32_16x16x32_bf16 v[140:143], a[180:183], v[164:167], v[140:143]\n" \
"v_exp_f32 v128, v128\n" \
"v_mfma_f32_16x16x32_bf16 v[140:143], a[176:179], v[160:163], v[140:143]\n" \
"v_smfmac_f32_16x16x64_bf16 a[36:39], a[48:51], v[144:151], v241\n" \
"v_mfma_f32_16x16x32_bf16 v[32:35], a[224:227], v[148:151], v[32:35]\n" \
"v_exp_f32 v129, v129\n" \
"v_mfma_f32_16x16x32_bf16 v[32:35], a[192:195], v[144:147], v[32:35]\n" \
"s_waitcnt vmcnt(8)\n" \
"v_exp_f32 v130, v130\n" \
"v_mfma_f32_16x16x32_bf16 v[36:39], a[196:199], v[144:147], v[36:39]\n" \
"buffer_store_dwordx4 a[60:63], v232, s[32:35], s39 offen sc1\n" \
"v_mfma_f32_16x16x32_bf16 v[36:39], a[228:231], v[148:151], v[36:39]\n" \
"v_exp_f32 v131, v131\n" \
"v_mfma_f32_16x16x32_bf16 v[40:43], a[232:235], v[148:151], v[40:43]\n" \
"v_cvt_pk_bf16_f32 v128, v128, v129\n" \
"v_cvt_pk_bf16_f32 v129, v130, v131\n" \
"v_mfma_f32_16x16x32_bf16 v[40:43], a[200:203], v[144:147], v[40:43]\n" \
"v_exp_f32 v132, v132\n" \
"v_mfma_f32_16x16x32_bf16 v[44:47], a[204:207], v[144:147], v[44:47]\n" \
"buffer_load_dwordx4 a[60:63], v230, s[40:43], s38 offen\n" \
"v_mfma_f32_16x16x32_bf16 v[44:47], a[236:239], v[148:151], v[44:47]\n" \
"s_add_u32 s38, s38, 512\n" \
"v_exp_f32 v133, v133\n" \
"v_mfma_f32_16x16x32_bf16 v[48:51], a[240:243], v[148:151], v[48:51]\n" \
"s_add_u32 s39, s39, 0x80000\n" \
"v_cvt_pk_bf16_f32 v130, v132, v133\n" \
"v_mfma_f32_16x16x32_bf16 v[48:51], a[208:211], v[144:147], v[48:51]\n" \
"v_exp_f32 v134, v134\n" \
"v_mfma_f32_16x16x32_bf16 v[52:55], a[212:215], v[144:147], v[52:55]\n" \
"v_mfma_f32_16x16x32_bf16 v[52:55], a[244:247], v[148:151], v[52:55]\n" \
"v_exp_f32 v135, v135\n" \
"v_mfma_f32_16x16x32_bf16 v[56:59], a[248:251], v[148:151], v[56:59]\n" \
"v_cvt_pk_bf16_f32 v131, v134, v135\n" \
"v_mfma_f32_16x16x32_bf16 v[56:59], a[216:219], v[144:147], v[56:59]\n" \
"v_exp_f32 v136, v136\n" \
"v_mfma_f32_16x16x32_bf16 v[60:63], a[220:223], v[144:147], v[60:63]\n" \
"v_mfma_f32_16x16x32_bf16 v[60:63], a[252:255], v[148:151], v[60:63]\n" \
"s_waitcnt vmcnt(18)\n" \
"v_exp_f32 v137, v137\n" \
"v_mfma_f32_16x16x32_bf16 v[144:147], a[128:131], v[176:179], 0\n" \
"s_barrier\n" \
"s_mov_b32 m0, s20\n" \
"v_cvt_pk_bf16_f32 v132, v136, v137\n" \
"v_mfma_f32_16x16x32_bf16 v[144:147], a[132:135], v[180:183], v[144:147]\n" \
"v_exp_f32 v138, v138\n" \
"v_mfma_f32_16x16x32_bf16 v[144:147], a[136:139], v[184:187], v[144:147]\n" \
"buffer_load_dwordx4 v248, s[32:35], s89 offen offset:0 sc1 lds\n" \
"v_mfma_f32_16x16x32_bf16 v[144:147], a[140:143], v[188:191], v[144:147]\n" \
"v_exp_f32 v139, v139\n" \
"v_mfma_f32_16x16x32_bf16 v[148:151], a[156:159], v[188:191], 0\n" \
"ds_read_b128 a[128:131], v240 offset:0\n" \
"v_cvt_pk_bf16_f32 v133, v138, v139\n" \
"v_mfma_f32_16x16x32_bf16 v[148:151], a[152:155], v[184:187], v[148:151]\n" \
"v_exp_f32 v140, v140\n" \
"v_mfma_f32_16x16x32_bf16 v[148:151], a[148:151], v[180:183], v[148:151]\n" \
"ds_read_b128 a[132:135], v240 offset:1024\n" \
"ds_read_b128 a[136:139], v240 offset:2048\n" \
"v_mfma_f32_16x16x32_bf16 v[148:151], a[144:147], v[176:179], v[148:151]\n" \
".Lst0_11_%=:\n" \
"s_cbranch_execz .Lst0_15_%=\n" \
"v_exp_f32 v141, v141\n" \
"v_mfma_f32_16x16x32_bf16 v[152:155], a[160:163], v[176:179], 0\n" \
"buffer_load_dwordx4 v248, s[32:35], s89 offen offset:1024 sc1 lds\n" \
"v_mfma_f32_16x16x32_bf16 v[152:155], a[164:167], v[180:183], v[152:155]\n" \
"v_exp_f32 v142, v142\n" \
"v_mfma_f32_16x16x32_bf16 v[152:155], a[168:171], v[184:187], v[152:155]\n" \
"ds_read_b128 a[140:143], v240 offset:3072\n" \
"v_cvt_pk_bf16_f32 v134, v140, v141\n" \
"v_mfma_f32_16x16x32_bf16 v[152:155], a[172:175], v[188:191], v[152:155]\n" \
"v_exp_f32 v143, v143\n" \
"v_mfma_f32_16x16x32_bf16 v[156:159], a[188:191], v[188:191], 0\n" \
"ds_read_b128 a[156:159], v240 offset:7168\n" \
"v_cvt_pk_bf16_f32 v135, v142, v143\n" \
"v_mfma_f32_16x16x32_bf16 v[156:159], a[184:187], v[184:187], v[156:159]\n" \
"ds_read_b128 a[152:155], v240 offset:6144\n" \
"ds_read_b128 a[148:151], v240 offset:5120\n" \
"v_mfma_f32_16x16x32_bf16 v[156:159], a[180:183], v[180:183], v[156:159]\n" \
"v_exp_f32 v144, v144\n" \
"v_mfma_f32_16x16x32_bf16 v[156:159], a[176:179], v[176:179], v[156:159]\n" \
"buffer_load_dwordx4 v248, s[32:35], s89 offen offset:2048 sc1 lds\n" \
"v_smfmac_f32_16x16x64_bf16 a[40:43], a[48:51], v[128:135], v241\n" \
"ds_read_b128 a[144:147], v240 offset:4096\n" \
"ds_read_b128 a[160:163], v240 offset:8192\n" \
"v_mfma_f32_16x16x32_bf16 v[64:67], a[224:227], v[132:135], v[64:67]\n" \
"v_exp_f32 v145, v145\n" \
"v_mfma_f32_16x16x32_bf16 v[64:67], a[192:195], v[128:131], v[64:67]\n" \
"v_exp_f32 v146, v146\n" \
"v_mfma_f32_16x16x32_bf16 v[68:71], a[196:199], v[128:131], v[68:71]\n" \
"ds_read_b128 a[164:167], v240 offset:9216\n" \
"v_cvt_pk_bf16_f32 v144, v144, v145\n" \
"v_mfma_f32_16x16x32_bf16 v[68:71], a[228:231], v[132:135], v[68:71]\n" \
"v_exp_f32 v147, v147\n" \
"v_mfma_f32_16x16x32_bf16 v[72:75], a[232:235], v[132:135], v[72:75]\n" \
"buffer_load_dwordx4 v248, s[32:35], s89 offen offset:3072 sc1 lds\n" \
"v_mfma_f32_16x16x32_bf16 v[72:75], a[200:203], v[128:131], v[72:75]\n" \
"s_add_u32 s20, s20, 0x4000\n" \
"v_exp_f32 v148, v148\n" \
"v_mfma_f32_16x16x32_bf16 v[76:79], a[204:207], v[128:131], v[76:79]\n" \
"s_and_b32 s20, s20, 0xffff\n" \
"ds_read_b128 a[168:171], v240 offset:10240\n" \
"v_cvt_pk_bf16_f32 v145, v146, v147\n" \
"v_mfma_f32_16x16x32_bf16 v[76:79], a[236:239], v[132:135], v[76:79]\n" \
"s_bitset1_b32 s20, 16\n" \
"v_exp_f32 v149, v149\n" \
"v_mfma_f32_16x16x32_bf16 v[80:83], a[240:243], v[132:135], v[80:83]\n" \
"ds_read_b128 a[172:175], v240 offset:11264\n" \
"v_cvt_pk_bf16_f32 v146, v148, v149\n" \
"v_mfma_f32_16x16x32_bf16 v[80:83], a[208:211], v[128:131], v[80:83]\n" \
"v_exp_f32 v150, v150\n" \
"v_mfma_f32_16x16x32_bf16 v[84:87], a[212:215], v[128:131], v[84:87]\n" \
"s_sub_u32 s95, s95, 1\n" \
"v_mfma_f32_16x16x32_bf16 v[84:87], a[244:247], v[132:135], v[84:87]\n" \
"v_exp_f32 v151, v151\n" \
"v_mfma_f32_16x16x32_bf16 v[88:91], a[248:251], v[132:135], v[88:91]\n" \
"v_cvt_pk_bf16_f32 v147, v150, v151\n" \
"v_mfma_f32_16x16x32_bf16 v[88:91], a[216:219], v[128:131], v[88:91]\n" \
"v_exp_f32 v152, v152\n" \
"v_mfma_f32_16x16x32_bf16 v[92:95], a[220:223], v[128:131], v[92:95]\n" \
"s_waitcnt lgkmcnt(8)\n" \
"v_exp_f32 v153, v153\n" \
"v_mfma_f32_16x16x32_bf16 v[92:95], a[252:255], v[132:135], v[92:95]\n" \
"s_cbranch_scc0 .Lloop_%=\n" \
"s_sub_u32 s95, s67, s84\n" \
"s_lshl_b32 s95, s95, 1\n" \
"s_sub_u32 s95, s95, 8\n" \
"s_mov_b32 s76, 1\n" \
".Lld_%=:\n" \
".Lst0_12_%=:\n" \
"s_cbranch_execz .Lstb0_%=\n" \
"s_sub_u32 s95, s95, 1\n" \
"s_cbranch_scc1 .Lhook_%=\n" \
".p2align 3\n" \
".Lloop1_%=:\n" \
  \
"v_mfma_f32_16x16x32_bf16 v[128:131], a[128:131], v[192:195], 0\n" \
"s_add_u32 s86, s86, 0x4000\n" \
"ds_read_b128 a[188:191], v240 offset:15360\n" \
"v_cvt_pk_bf16_f32 v148, v152, v153\n" \
"v_mfma_f32_16x16x32_bf16 v[128:131], a[132:135], v[196:199], v[128:131]\n" \
"s_bitset0_b32 s86, 16\n" \
"v_exp_f32 v154, v154\n" \
"v_mfma_f32_16x16x32_bf16 v[128:131], a[136:139], v[200:203], v[128:131]\n" \
"s_cmp_eq_u32 s88, s24\n" \
"s_cbranch_scc1 .Lchd_%=\n" \
".Lsbhd_%=:\n" \
"v_mfma_f32_16x16x32_bf16 v[128:131], a[140:143], v[204:207], v[128:131]\n" \
"s_waitcnt lgkmcnt(5)\n" \
"v_exp_f32 v155, v155\n" \
"v_mfma_f32_16x16x32_bf16 v[132:135], a[156:159], v[204:207], 0\n" \
"s_mov_b32 m0, s19\n" \
"v_add_u32 v242, 0x10000, v240\n" \
"v_cvt_pk_bf16_f32 v149, v154, v155\n" \
"v_mfma_f32_16x16x32_bf16 v[132:135], a[152:155], v[200:203], v[132:135]\n" \
"ds_read_b128 a[184:187], v240 offset:14336\n" \
"ds_read_b128 a[180:183], v240 offset:13312\n" \
"ds_read_b128 a[176:179], v240 offset:12288\n" \
"v_exp_f32 v156, v156\n" \
"v_mfma_f32_16x16x32_bf16 v[132:135], a[148:151], v[196:199], v[132:135]\n" \
"buffer_load_dwordx4 v248, s[32:35], s88 offen offset:0 sc1 lds\n" \
"v_mfma_f32_16x16x32_bf16 v[132:135], a[144:147], v[192:195], v[132:135]\n" \
"s_waitcnt lgkmcnt(4)\n" \
"v_exp_f32 v157, v157\n" \
"v_mfma_f32_16x16x32_bf16 v[136:139], a[160:163], v[192:195], 0\n" \
"v_cvt_pk_bf16_f32 v150, v156, v157\n" \
"v_mfma_f32_16x16x32_bf16 v[136:139], a[164:167], v[196:199], v[136:139]\n" \
"v_exp_f32 v158, v158\n" \
"v_mfma_f32_16x16x32_bf16 v[136:139], a[168:171], v[200:203], v[136:139]\n" \
"v_add_u32 v240, s86, v248\n" \
"v_mfma_f32_16x16x32_bf16 v[136:139], a[172:175], v[204:207], v[136:139]\n" \
"s_waitcnt lgkmcnt(0)\n" \
"v_exp_f32 v159, v159\n" \
"v_mfma_f32_16x16x32_bf16 v[140:143], a[188:191], v[204:207], 0\n" \
"v_cvt_pk_bf16_f32 v151, v158, v159\n" \
"v_mfma_f32_16x16x32_bf16 v[140:143], a[184:187], v[200:203], v[140:143]\n" \
"buffer_load_dwordx4 v248, s[32:35], s88 offen offset:1024 sc1 lds\n" \
"v_mfma_f32_16x16x32_bf16 v[140:143], a[180:183], v[196:199], v[140:143]\n" \
"v_exp_f32 v128, v128\n" \
"v_mfma_f32_16x16x32_bf16 v[140:143], a[176:179], v[192:195], v[140:143]\n" \
"v_smfmac_f32_16x16x64_bf16 a[44:47], a[48:51], v[144:151], v241\n" \
"v_mfma_f32_16x16x32_bf16 v[96:99], a[224:227], v[148:151], v[96:99]\n" \
"v_exp_f32 v129, v129\n" \
"v_mfma_f32_16x16x32_bf16 v[96:99], a[192:195], v[144:147], v[96:99]\n" \
"v_exp_f32 v130, v130\n" \
"v_mfma_f32_16x16x32_bf16 v[100:103], a[196:199], v[144:147], v[100:103]\n" \
"ds_read_b128 a[224:227], v242 offset:8192\n" \
"v_cvt_pk_bf16_f32 v128, v128, v129\n" \
"v_mfma_f32_16x16x32_bf16 v[100:103], a[228:231], v[148:151], v[100:103]\n" \
"v_exp_f32 v131, v131\n" \
"v_mfma_f32_16x16x32_bf16 v[104:107], a[232:235], v[148:151], v[104:107]\n" \
"ds_read_b128 a[192:195], v242 offset:0\n" \
"v_cvt_pk_bf16_f32 v129, v130, v131\n" \
"v_mfma_f32_16x16x32_bf16 v[104:107], a[200:203], v[144:147], v[104:107]\n" \
"v_exp_f32 v132, v132\n" \
"v_mfma_f32_16x16x32_bf16 v[108:111], a[204:207], v[144:147], v[108:111]\n" \
"ds_read_b128 a[196:199], v242 offset:1024\n" \
"ds_read_b128 a[228:231], v242 offset:9216\n" \
"v_mfma_f32_16x16x32_bf16 v[108:111], a[236:239], v[148:151], v[108:111]\n" \
"v_exp_f32 v133, v133\n" \
"v_mfma_f32_16x16x32_bf16 v[112:115], a[240:243], v[148:151], v[112:115]\n" \
"ds_read_b128 a[232:235], v242 offset:10240\n" \
"v_cvt_pk_bf16_f32 v130, v132, v133\n" \
"v_mfma_f32_16x16x32_bf16 v[112:115], a[208:211], v[144:147], v[112:115]\n" \
"v_exp_f32 v134, v134\n" \
"v_mfma_f32_16x16x32_bf16 v[116:119], a[212:215], v[144:147], v[116:119]\n" \
"ds_read_b128 a[200:203], v242 offset:2048\n" \
"ds_read_b128 a[204:207], v242 offset:3072\n" \
"v_mfma_f32_16x16x32_bf16 v[116:119], a[244:247], v[148:151], v[116:119]\n" \
"v_exp_f32 v135, v135\n" \
"v_mfma_f32_16x16x32_bf16 v[120:123], a[248:251], v[148:151], v[120:123]\n" \
"ds_read_b128 a[236:239], v242 offset:11264\n" \
"v_cvt_pk_bf16_f32 v131, v134, v135\n" \
"v_mfma_f32_16x16x32_bf16 v[120:123], a[216:219], v[144:147], v[120:123]\n" \
"v_exp_f32 v136, v136\n" \
"v_mfma_f32_16x16x32_bf16 v[124:127], a[220:223], v[144:147], v[124:127]\n" \
"ds_read_b128 a[240:243], v242 offset:12288\n" \
"v_mfma_f32_16x16x32_bf16 v[124:127], a[252:255], v[148:151], v[124:127]\n" \
"v_exp_f32 v137, v137\n" \
"v_mfma_f32_16x16x32_bf16 v[144:147], a[128:131], v[208:211], 0\n" \
"ds_read_b128 a[208:211], v242 offset:4096\n" \
"v_cvt_pk_bf16_f32 v132, v136, v137\n" \
"v_mfma_f32_16x16x32_bf16 v[144:147], a[132:135], v[212:215], v[144:147]\n" \
"v_exp_f32 v138, v138\n" \
"v_mfma_f32_16x16x32_bf16 v[144:147], a[136:139], v[216:219], v[144:147]\n" \
"ds_read_b128 a[212:215], v242 offset:5120\n" \
"ds_read_b128 a[244:247], v242 offset:13312\n" \
"v_mfma_f32_16x16x32_bf16 v[144:147], a[140:143], v[220:223], v[144:147]\n" \
"v_exp_f32 v139, v139\n" \
"v_mfma_f32_16x16x32_bf16 v[148:151], a[156:159], v[220:223], 0\n" \
"ds_read_b128 a[248:251], v242 offset:14336\n" \
"v_cvt_pk_bf16_f32 v133, v138, v139\n" \
"v_mfma_f32_16x16x32_bf16 v[148:151], a[152:155], v[216:219], v[148:151]\n" \
"v_exp_f32 v140, v140\n" \
"v_mfma_f32_16x16x32_bf16 v[148:151], a[148:151], v[212:215], v[148:151]\n" \
"ds_read_b128 a[216:219], v242 offset:6144\n" \
"v_mfma_f32_16x16x32_bf16 v[148:151], a[144:147], v[208:211], v[148:151]\n" \
"v_exp_f32 v141, v141\n" \
"v_mfma_f32_16x16x32_bf16 v[152:155], a[160:163], v[208:211], 0\n" \
"ds_read_b128 a[220:223], v242 offset:7168\n" \
"v_cvt_pk_bf16_f32 v134, v140, v141\n" \
"v_mfma_f32_16x16x32_bf16 v[152:155], a[164:167], v[212:215], v[152:155]\n" \
"v_exp_f32 v142, v142\n" \
"v_mfma_f32_16x16x32_bf16 v[152:155], a[168:171], v[216:219], v[152:155]\n" \
"ds_read_b128 a[252:255], v242 offset:15360\n" \
"v_mfma_f32_16x16x32_bf16 v[152:155], a[172:175], v[220:223], v[152:155]\n" \
"v_exp_f32 v143, v143\n" \
"v_mfma_f32_16x16x32_bf16 v[156:159], a[188:191], v[220:223], 0\n" \
"v_cvt_pk_bf16_f32 v135, v142, v143\n" \
"v_mfma_f32_16x16x32_bf16 v[156:159], a[184:187], v[216:219], v[156:159]\n" \
"v_mfma_f32_16x16x32_bf16 v[156:159], a[180:183], v[212:215], v[156:159]\n" \
"v_exp_f32 v144, v144\n" \
"v_mfma_f32_16x16x32_bf16 v[156:159], a[176:179], v[208:211], v[156:159]\n" \
"buffer_load_dwordx4 v248, s[32:35], s88 offen offset:2048 sc1 lds\n" \
"v_smfmac_f32_16x16x64_bf16 a[32:35], a[48:51], v[128:135], v241\n" \
"s_waitcnt lgkmcnt(12)\n" \
"v_mfma_f32_16x16x32_bf16 v[0:3], a[224:227], v[132:135], v[0:3]\n" \
"v_exp_f32 v145, v145\n" \
"v_mfma_f32_16x16x32_bf16 v[0:3], a[192:195], v[128:131], v[0:3]\n" \
"v_exp_f32 v146, v146\n" \
"v_mfma_f32_16x16x32_bf16 v[4:7], a[196:199], v[128:131], v[4:7]\n" \
"v_cvt_pk_bf16_f32 v144, v144, v145\n" \
"v_mfma_f32_16x16x32_bf16 v[4:7], a[228:231], v[132:135], v[4:7]\n" \
"s_waitcnt lgkmcnt(8)\n" \
"v_exp_f32 v147, v147\n" \
"v_mfma_f32_16x16x32_bf16 v[8:11], a[232:235], v[132:135], v[8:11]\n" \
"v_cvt_pk_bf16_f32 v145, v146, v147\n" \
"v_mfma_f32_16x16x32_bf16 v[8:11], a[200:203], v[128:131], v[8:11]\n" \
"v_exp_f32 v148, v148\n" \
"v_mfma_f32_16x16x32_bf16 v[12:15], a[204:207], v[128:131], v[12:15]\n" \
"v_mfma_f32_16x16x32_bf16 v[12:15], a[236:239], v[132:135], v[12:15]\n" \
"s_waitcnt lgkmcnt(4)\n" \
"v_exp_f32 v149, v149\n" \
"v_mfma_f32_16x16x32_bf16 v[16:19], a[240:243], v[132:135], v[16:19]\n" \
"v_cvt_pk_bf16_f32 v146, v148, v149\n" \
"v_mfma_f32_16x16x32_bf16 v[16:19], a[208:211], v[128:131], v[16:19]\n" \
"v_exp_f32 v150, v150\n" \
"v_mfma_f32_16x16x32_bf16 v[20:23], a[212:215], v[128:131], v[20:23]\n" \
"v_mfma_f32_16x16x32_bf16 v[20:23], a[244:247], v[132:135], v[20:23]\n" \
"s_waitcnt lgkmcnt(0)\n" \
"v_exp_f32 v151, v151\n" \
"v_mfma_f32_16x16x32_bf16 v[24:27], a[248:251], v[132:135], v[24:27]\n" \
"v_cvt_pk_bf16_f32 v147, v150, v151\n" \
"v_mfma_f32_16x16x32_bf16 v[24:27], a[216:219], v[128:131], v[24:27]\n" \
"v_exp_f32 v152, v152\n" \
"v_mfma_f32_16x16x32_bf16 v[28:31], a[220:223], v[128:131], v[28:31]\n" \
"buffer_load_dwordx4 v248, s[32:35], s88 offen offset:3072 sc1 lds\n" \
"v_mfma_f32_16x16x32_bf16 v[28:31], a[252:255], v[132:135], v[28:31]\n" \
"s_add_u32 s89, s88, 589824\n" \
"v_exp_f32 v153, v153\n" \
"v_mfma_f32_16x16x32_bf16 v[128:131], a[128:131], v[160:163], 0\n" \
"s_add_u32 s88, s88, s27\n" \
"s_add_u32 s19, s19, 0x4000\n" \
"v_cvt_pk_bf16_f32 v148, v152, v153\n" \
"v_mfma_f32_16x16x32_bf16 v[128:131], a[132:135], v[164:167], v[128:131]\n" \
"s_bitset0_b32 s19, 16\n" \
"v_exp_f32 v154, v154\n" \
"v_mfma_f32_16x16x32_bf16 v[128:131], a[136:139], v[168:171], v[128:131]\n" \
"s_cmp_eq_u32 s88, s16\n" \
"s_cmov_b32 s88, s21\n" \
"s_cmov_b32 s27, s91\n" \
"v_mfma_f32_16x16x32_bf16 v[128:131], a[140:143], v[172:175], v[128:131]\n" \
"s_cmov_b32 s16, s77\n" \
"v_exp_f32 v155, v155\n" \
"v_mfma_f32_16x16x32_bf16 v[132:135], a[156:159], v[172:175], 0\n" \
"s_cmov_b32 s21, s92\n" \
"s_cmovk_i32 s91, 0x4000\n" \
"v_cvt_pk_bf16_f32 v149, v154, v155\n" \
"v_mfma_f32_16x16x32_bf16 v[132:135], a[152:155], v[168:171], v[132:135]\n" \
"s_cmp_lg_u32 s76, 0\n" \
"v_exp_f32 v156, v156\n" \
"v_mfma_f32_16x16x32_bf16 v[132:135], a[148:151], v[164:167], v[132:135]\n" \
"s_cbranch_scc1 .Land_%=\n" \
".Labd_%=:\n" \
"v_mfma_f32_16x16x32_bf16 v[132:135], a[144:147], v[160:163], v[132:135]\n" \
"v_exp_f32 v157, v157\n" \
"v_mfma_f32_16x16x32_bf16 v[136:139], a[160:163], v[160:163], 0\n" \
"v_cvt_pk_bf16_f32 v150, v156, v157\n" \
"v_mfma_f32_16x16x32_bf16 v[136:139], a[164:167], v[164:167], v[136:139]\n" \
"v_exp_f32 v158, v158\n" \
"v_mfma_f32_16x16x32_bf16 v[136:139], a[168:171], v[168:171], v[136:139]\n" \
"v_mfma_f32_16x16x32_bf16 v[136:139], a[172:175], v[172:175], v[136:139]\n" \
"v_exp_f32 v159, v159\n" \
"v_mfma_f32_16x16x32_bf16 v[140:143], a[188:191], v[172:175], 0\n" \
"v_cvt_pk_bf16_f32 v151, v158, v159\n" \
"v_mfma_f32_16x16x32_bf16 v[140:143], a[184:187], v[168:171], v[140:143]\n" \
"v_mfma_f32_16x16x32_bf16 v[140:143], a[180:183], v[164:167], v[140:143]\n" \
"v_exp_f32 v128, v128\n" \
"v_mfma_f32_16x16x32_bf16 v[140:143], a[176:179], v[160:163], v[140:143]\n" \
"v_smfmac_f32_16x16x64_bf16 a[36:39], a[48:51], v[144:151], v241\n" \
"v_mfma_f32_16x16x32_bf16 v[32:35], a[224:227], v[148:151], v[32:35]\n" \
"v_exp_f32 v129, v129\n" \
"v_mfma_f32_16x16x32_bf16 v[32:35], a[192:195], v[144:147], v[32:35]\n" \
"v_exp_f32 v130, v130\n" \
"v_mfma_f32_16x16x32_bf16 v[36:39], a[196:199], v[144:147], v[36:39]\n" \
"v_cvt_pk_bf16_f32 v128, v128, v129\n" \
"v_mfma_f32_16x16x32_bf16 v[36:39], a[228:231], v[148:151], v[36:39]\n" \
"v_exp_f32 v131, v131\n" \
"v_mfma_f32_16x16x32_bf16 v[40:43], a[232:235], v[148:151], v[40:43]\n" \
"v_cvt_pk_bf16_f32 v129, v130, v131\n" \
"v_mfma_f32_16x16x32_bf16 v[40:43], a[200:203], v[144:147], v[40:43]\n" \
"v_exp_f32 v132, v132\n" \
"v_mfma_f32_16x16x32_bf16 v[44:47], a[204:207], v[144:147], v[44:47]\n" \
"v_mfma_f32_16x16x32_bf16 v[44:47], a[236:239], v[148:151], v[44:47]\n" \
"v_exp_f32 v133, v133\n" \
"v_mfma_f32_16x16x32_bf16 v[48:51], a[240:243], v[148:151], v[48:51]\n" \
"v_cvt_pk_bf16_f32 v130, v132, v133\n" \
"v_mfma_f32_16x16x32_bf16 v[48:51], a[208:211], v[144:147], v[48:51]\n" \
"v_exp_f32 v134, v134\n" \
"v_mfma_f32_16x16x32_bf16 v[52:55], a[212:215], v[144:147], v[52:55]\n" \
"v_mfma_f32_16x16x32_bf16 v[52:55], a[244:247], v[148:151], v[52:55]\n" \
"v_exp_f32 v135, v135\n" \
"v_mfma_f32_16x16x32_bf16 v[56:59], a[248:251], v[148:151], v[56:59]\n" \
"v_cvt_pk_bf16_f32 v131, v134, v135\n" \
"v_mfma_f32_16x16x32_bf16 v[56:59], a[216:219], v[144:147], v[56:59]\n" \
"v_exp_f32 v136, v136\n" \
"v_mfma_f32_16x16x32_bf16 v[60:63], a[220:223], v[144:147], v[60:63]\n" \
"v_mfma_f32_16x16x32_bf16 v[60:63], a[252:255], v[148:151], v[60:63]\n" \
"s_waitcnt vmcnt(12)\n" \
"v_exp_f32 v137, v137\n" \
"v_mfma_f32_16x16x32_bf16 v[144:147], a[128:131], v[176:179], 0\n" \
"s_barrier\n" \
"s_mov_b32 m0, s20\n" \
"v_cvt_pk_bf16_f32 v132, v136, v137\n" \
"v_mfma_f32_16x16x32_bf16 v[144:147], a[132:135], v[180:183], v[144:147]\n" \
"v_exp_f32 v138, v138\n" \
"v_mfma_f32_16x16x32_bf16 v[144:147], a[136:139], v[184:187], v[144:147]\n" \
"buffer_load_dwordx4 v248, s[32:35], s89 offen offset:0 sc1 lds\n" \
"v_mfma_f32_16x16x32_bf16 v[144:147], a[140:143], v[188:191], v[144:147]\n" \
"v_exp_f32 v139, v139\n" \
"v_mfma_f32_16x16x32_bf16 v[148:151], a[156:159], v[188:191], 0\n" \
"ds_read_b128 a[128:131], v240 offset:0\n" \
"v_cvt_pk_bf16_f32 v133, v138, v139\n" \
"v_mfma_f32_16x16x32_bf16 v[148:151], a[152:155], v[184:187], v[148:151]\n" \
"v_exp_f32 v140, v140\n" \
"v_mfma_f32_16x16x32_bf16 v[148:151], a[148:151], v[180:183], v[148:151]\n" \
"ds_read_b128 a[132:135], v240 offset:1024\n" \
"ds_read_b128 a[136:139], v240 offset:2048\n" \
"v_mfma_f32_16x16x32_bf16 v[148:151], a[144:147], v[176:179], v[148:151]\n" \
"v_exp_f32 v141, v141\n" \
"v_mfma_f32_16x16x32_bf16 v[152:155], a[160:163], v[176:179], 0\n" \
"buffer_load_dwordx4 v248, s[32:35], s89 offen offset:1024 sc1 lds\n" \
"v_mfma_f32_16x16x32_bf16 v[152:155], a[164:167], v[180:183], v[152:155]\n" \
"v_exp_f32 v142, v142\n" \
"v_mfma_f32_16x16x32_bf16 v[152:155], a[168:171], v[184:187], v[152:155]\n" \
"ds_read_b128 a[140:143], v240 offset:3072\n" \
"v_cvt_pk_bf16_f32 v134, v140, v141\n" \
"v_mfma_f32_16x16x32_bf16 v[152:155], a[172:175], v[188:191], v[152:155]\n" \
"v_exp_f32 v143, v143\n" \
"v_mfma_f32_16x16x32_bf16 v[156:159], a[188:191], v[188:191], 0\n" \
"ds_read_b128 a[156:159], v240 offset:7168\n" \
"v_cvt_pk_bf16_f32 v135, v142, v143\n" \
"v_mfma_f32_16x16x32_bf16 v[156:159], a[184:187], v[184:187], v[156:159]\n" \
"ds_read_b128 a[152:155], v240 offset:6144\n" \
"ds_read_b128 a[148:151], v240 offset:5120\n" \
"v_mfma_f32_16x16x32_bf16 v[156:159], a[180:183], v[180:183], v[156:159]\n" \
"v_exp_f32 v144, v144\n" \
"v_mfma_f32_16x16x32_bf16 v[156:159], a[176:179], v[176:179], v[156:159]\n" \
"buffer_load_dwordx4 v248, s[32:35], s89 offen offset:2048 sc1 lds\n" \
"v_smfmac_f32_16x16x64_bf16 a[40:43], a[48:51], v[128:135], v241\n" \
"ds_read_b128 a[144:147], v240 offset:4096\n" \
"ds_read_b128 a[160:163], v240 offset:8192\n" \
"v_mfma_f32_16x16x32_bf16 v[64:67], a[224:227], v[132:135], v[64:67]\n" \
"v_exp_f32 v145, v145\n" \
"v_mfma_f32_16x16x32_bf16 v[64:67], a[192:195], v[128:131], v[64:67]\n" \
"v_exp_f32 v146, v146\n" \
"v_mfma_f32_16x16x32_bf16 v[68:71], a[196:199], v[128:131], v[68:71]\n" \
"ds_read_b128 a[164:167], v240 offset:9216\n" \
"v_cvt_pk_bf16_f32 v144, v144, v145\n" \
"v_mfma_f32_16x16x32_bf16 v[68:71], a[228:231], v[132:135], v[68:71]\n" \
"v_exp_f32 v147, v147\n" \
"v_mfma_f32_16x16x32_bf16 v[72:75], a[232:235], v[132:135], v[72:75]\n" \
"buffer_load_dwordx4 v248, s[32:35], s89 offen offset:3072 sc1 lds\n" \
"v_mfma_f32_16x16x32_bf16 v[72:75], a[200:203], v[128:131], v[72:75]\n" \
"s_add_u32 s20, s20, 0x4000\n" \
"v_exp_f32 v148, v148\n" \
"v_mfma_f32_16x16x32_bf16 v[76:79], a[204:207], v[128:131], v[76:79]\n" \
"s_and_b32 s20, s20, 0xffff\n" \
"ds_read_b128 a[168:171], v240 offset:10240\n" \
"v_cvt_pk_bf16_f32 v145, v146, v147\n" \
"v_mfma_f32_16x16x32_bf16 v[76:79], a[236:239], v[132:135], v[76:79]\n" \
"s_bitset1_b32 s20, 16\n" \
"v_exp_f32 v149, v149\n" \
"v_mfma_f32_16x16x32_bf16 v[80:83], a[240:243], v[132:135], v[80:83]\n" \
"ds_read_b128 a[172:175], v240 offset:11264\n" \
"v_cvt_pk_bf16_f32 v146, v148, v149\n" \
"v_mfma_f32_16x16x32_bf16 v[80:83], a[208:211], v[128:131], v[80:83]\n" \
"v_exp_f32 v150, v150\n" \
"v_mfma_f32_16x16x32_bf16 v[84:87], a[212:215], v[128:131], v[84:87]\n" \
"s_sub_u32 s95, s95, 1\n" \
"v_mfma_f32_16x16x32_bf16 v[84:87], a[244:247], v[132:135], v[84:87]\n" \
"v_exp_f32 v151, v151\n" \
"v_mfma_f32_16x16x32_bf16 v[88:91], a[248:251], v[132:135], v[88:91]\n" \
"v_cvt_pk_bf16_f32 v147, v150, v151\n" \
"v_mfma_f32_16x16x32_bf16 v[88:91], a[216:219], v[128:131], v[88:91]\n" \
"v_exp_f32 v152, v152\n" \
"v_mfma_f32_16x16x32_bf16 v[92:95], a[220:223], v[128:131], v[92:95]\n" \
"s_waitcnt lgkmcnt(8)\n" \
"v_exp_f32 v153, v153\n" \
"v_mfma_f32_16x16x32_bf16 v[92:95], a[252:255], v[132:135], v[92:95]\n" \
"s_cbranch_scc0 .Lloop1_%=\n" \
".Lhook_%=:\n" \
".Lst0_13_%=:\n" \
"s_cbranch_execz .Lstb0_%=\n" \
  \
"s_load_dwordx4 s[52:55], %[ka], 192\n" \
"s_load_dwordx2 s[96:97], %[ka], 208\n" \
"v_accvgpr_read_b32 v252, a32\n" \
"v_accvgpr_read_b32 v253, a33\n" \
"v_add_f32 v252, v252, v253\n" \
"v_accvgpr_read_b32 v254, a36\n" \
"v_accvgpr_read_b32 v255, a37\n" \
"v_add_f32 v254, v254, v255\n" \
"v_min_f32 v253, v252, v254\n" \
"v_max_f32 v255, v252, v254\n" \
"v_cmp_nle_f32 s[98:99], v246, v253\n" \
"v_cmp_nle_f32 vcc, v255, v250\n" \
"v_cmp_u_f32 s[14:15], v252, v254\n" \
"s_nop 1\n" \
"s_or_b64 vcc, vcc, s[98:99]\n" \
"s_or_b64 vcc, vcc, s[14:15]\n" \
"s_cbranch_vccz .Loka_%=\n" \
"s_call_b64 s[98:99], .Lredo0_%=\n" \
"v_accvgpr_read_b32 v252, a32\n" \
"v_accvgpr_read_b32 v253, a33\n" \
"v_add_f32 v252, v252, v253\n" \
"v_accvgpr_read_b32 v254, a36\n" \
"v_accvgpr_read_b32 v255, a37\n" \
"v_add_f32 v254, v254, v255\n" \
".Loka_%=:\n" \
"s_add_u32 s23, s30, 4096\n" \
"v_rcp_f32 v252, v252\n" \
"v_rcp_f32 v254, v254\n" \
"s_nop 0\n" \
"v_mov_b32 v253, v252\n" \
"v_mov_b32 v255, v254\n" \
"v_pk_mul_f32 v[0:1], v[0:1], v[252:253]\n" \
"v_pk_mul_f32 v[2:3], v[2:3], v[252:253]\n" \
"v_pk_mul_f32 v[4:5], v[4:5], v[252:253]\n" \
"v_pk_mul_f32 v[6:7], v[6:7], v[252:253]\n" \
"v_cvt_pk_bf16_f32 v0, v0, v1\n" \
"v_cvt_pk_bf16_f32 v1, v2, v3\n" \
"v_cvt_pk_bf16_f32 v2, v4, v5\n" \
"v_cvt_pk_bf16_f32 v3, v6, v7\n" \
"buffer_store_dwordx4 v[0:3], v248, s[32:35], s30 offen offset:0 sc1\n" \
"v_pk_mul_f32 v[32:33], v[32:33], v[254:255]\n" \
"v_pk_mul_f32 v[34:35], v[34:35], v[254:255]\n" \
"v_pk_mul_f32 v[36:37], v[36:37], v[254:255]\n" \
"v_pk_mul_f32 v[38:39], v[38:39], v[254:255]\n" \
"v_cvt_pk_bf16_f32 v4, v32, v33\n" \
"v_cvt_pk_bf16_f32 v5, v34, v35\n" \
"v_cvt_pk_bf16_f32 v6, v36, v37\n" \
"v_cvt_pk_bf16_f32 v7, v38, v39\n" \
"buffer_store_dwordx4 v[4:7], v248, s[32:35], s30 offen offset:1024 sc1\n" \
"v_pk_mul_f32 v[8:9], v[8:9], v[252:253]\n" \
"v_pk_mul_f32 v[10:11], v[10:11], v[252:253]\n" \
"v_pk_mul_f32 v[12:13], v[12:13], v[252:253]\n" \
"v_pk_mul_f32 v[14:15], v[14:15], v[252:253]\n" \
"v_cvt_pk_bf16_f32 v8, v8, v9\n" \
"v_cvt_pk_bf16_f32 v9, v10, v11\n" \
"v_cvt_pk_bf16_f32 v10, v12, v13\n" \
"v_cvt_pk_bf16_f32 v11, v14, v15\n" \
"buffer_store_dwordx4 v[8:11], v248, s[32:35], s30 offen offset:2048 sc1\n" \
"v_pk_mul_f32 v[40:41], v[40:41], v[254:255]\n" \
"v_pk_mul_f32 v[42:43], v[42:43], v[254:255]\n" \
"v_pk_mul_f32 v[44:45], v[44:45], v[254:255]\n" \
"v_pk_mul_f32 v[46:47], v[46:47], v[254:255]\n" \
"v_cvt_pk_bf16_f32 v12, v40, v41\n" \
"v_cvt_pk_bf16_f32 v13, v42, v43\n" \
"v_cvt_pk_bf16_f32 v14, v44, v45\n" \
"v_cvt_pk_bf16_f32 v15, v46, v47\n" \
"buffer_store_dwordx4 v[12:15], v248, s[32:35], s30 offen offset:3072 sc1\n" \
"s_mov_b64 exec, 0\n" \
"s_cmp_eq_u32 %[wid], 0\n" \
"s_cbranch_scc1 .Lst1_0_%=\n" \
"s_cmp_eq_u32 %[wid], 1\n" \
"s_cbranch_scc1 .Lst1_1_%=\n" \
"s_cmp_eq_u32 %[wid], 2\n" \
"s_cbranch_scc1 .Lst1_2_%=\n" \
"s_branch .Lst1_3_%=\n" \
".Lstb1_%=:\n" \
"s_mov_b64 exec, -1\n" \
"v_pk_mul_f32 v[16:17], v[16:17], v[252:253]\n" \
"v_pk_mul_f32 v[18:19], v[18:19], v[252:253]\n" \
"v_pk_mul_f32 v[20:21], v[20:21], v[252:253]\n" \
"v_pk_mul_f32 v[22:23], v[22:23], v[252:253]\n" \
"v_cvt_pk_bf16_f32 v16, v16, v17\n" \
"v_cvt_pk_bf16_f32 v17, v18, v19\n" \
"v_cvt_pk_bf16_f32 v18, v20, v21\n" \
"v_cvt_pk_bf16_f32 v19, v22, v23\n" \
"buffer_store_dwordx4 v[16:19], v248, s[32:35], s23 offen offset:0 sc1\n" \
"v_pk_mul_f32 v[48:49], v[48:49], v[254:255]\n" \
"v_pk_mul_f32 v[50:51], v[50:51], v[254:255]\n" \
"v_pk_mul_f32 v[52:53], v[52:53], v[254:255]\n" \
"v_pk_mul_f32 v[54:55], v[54:55], v[254:255]\n" \
"v_cvt_pk_bf16_f32 v20, v48, v49\n" \
"v_cvt_pk_bf16_f32 v21, v50, v51\n" \
"v_cvt_pk_bf16_f32 v22, v52, v53\n" \
"v_cvt_pk_bf16_f32 v23, v54, v55\n" \
"buffer_store_dwordx4 v[20:23], v248, s[32:35], s23 offen offset:1024 sc1\n" \
"v_pk_mul_f32 v[24:25], v[24:25], v[252:253]\n" \
"v_pk_mul_f32 v[26:27], v[26:27], v[252:253]\n" \
"v_pk_mul_f32 v[28:29], v[28:29], v[252:253]\n" \
"v_pk_mul_f32 v[30:31], v[30:31], v[252:253]\n" \
"v_cvt_pk_bf16_f32 v24, v24, v25\n" \
"v_cvt_pk_bf16_f32 v25, v26, v27\n" \
"v_cvt_pk_bf16_f32 v26, v28, v29\n" \
"v_cvt_pk_bf16_f32 v27, v30, v31\n" \
"buffer_store_dwordx4 v[24:27], v248, s[32:35], s23 offen offset:2048 sc1\n" \
"v_pk_mul_f32 v[56:57], v[56:57], v[254:255]\n" \
"v_pk_mul_f32 v[58:59], v[58:59], v[254:255]\n" \
"v_pk_mul_f32 v[60:61], v[60:61], v[254:255]\n" \
"v_pk_mul_f32 v[62:63], v[62:63], v[254:255]\n" \
"v_cvt_pk_bf16_f32 v28, v56, v57\n" \
"v_cvt_pk_bf16_f32 v29, v58, v59\n" \
"v_cvt_pk_bf16_f32 v30, v60, v61\n" \
"v_cvt_pk_bf16_f32 v31, v62, v63\n" \
"buffer_store_dwordx4 v[28:31], v248, s[32:35], s23 offen offset:3072 sc1\n" \
"s_lshl2_add_u32 s25, s66, s31\n" \
"s_mov_b32 s76, 1\n" \
"s_lshl_b32 s95, s67, 1\n" \
"s_cmp_lt_u32 s67, 4\n" \
"s_cselect_b32 s22, 7, 11\n" \
"s_sub_u32 s95, s22, s95\n" \
"s_max_i32 s95, s95, 0\n" \
"s_waitcnt lgkmcnt(0)\n" \
".p2align 3\n" \
".Lloop2_%=:\n" \
  \
"v_mfma_f32_16x16x32_bf16 v[128:131], a[128:131], v[160:163], 0\n" \
"s_add_u32 s86, s86, 0x4000\n" \
"ds_read_b128 a[188:191], v240 offset:15360\n" \
"v_cvt_pk_bf16_f32 v148, v152, v153\n" \
"v_mfma_f32_16x16x32_bf16 v[128:131], a[132:135], v[164:167], v[128:131]\n" \
"s_bitset0_b32 s86, 16\n" \
"v_exp_f32 v154, v154\n" \
"v_mfma_f32_16x16x32_bf16 v[128:131], a[136:139], v[168:171], v[128:131]\n" \
"ds_read_b128 a[184:187], v240 offset:14336\n" \
"v_add_u32 v242, 0x10000, v240\n" \
"v_mfma_f32_16x16x32_bf16 v[128:131], a[140:143], v[172:175], v[128:131]\n" \
"s_waitcnt lgkmcnt(6)\n" \
"v_exp_f32 v155, v155\n" \
"v_mfma_f32_16x16x32_bf16 v[132:135], a[156:159], v[172:175], 0\n" \
"s_mov_b32 m0, s19\n" \
"ds_read_b128 a[180:183], v240 offset:13312\n" \
"v_cvt_pk_bf16_f32 v149, v154, v155\n" \
"v_mfma_f32_16x16x32_bf16 v[132:135], a[152:155], v[168:171], v[132:135]\n" \
"ds_read_b128 a[176:179], v240 offset:12288\n" \
"v_exp_f32 v156, v156\n" \
"v_mfma_f32_16x16x32_bf16 v[132:135], a[148:151], v[164:167], v[132:135]\n" \
"buffer_load_dwordx4 v248, s[32:35], s88 offen offset:0 sc1 lds\n" \
"v_mfma_f32_16x16x32_bf16 v[132:135], a[144:147], v[160:163], v[132:135]\n" \
"s_waitcnt lgkmcnt(4)\n" \
"v_exp_f32 v157, v157\n" \
"v_mfma_f32_16x16x32_bf16 v[136:139], a[160:163], v[160:163], 0\n" \
"v_cvt_pk_bf16_f32 v150, v156, v157\n" \
"v_mfma_f32_16x16x32_bf16 v[136:139], a[164:167], v[164:167], v[136:139]\n" \
"v_exp_f32 v158, v158\n" \
"v_mfma_f32_16x16x32_bf16 v[136:139], a[168:171], v[168:171], v[136:139]\n" \
"buffer_load_dwordx4 v248, s[32:35], s88 offen offset:1024 sc1 lds\n" \
"v_mfma_f32_16x16x32_bf16 v[136:139], a[172:175], v[172:175], v[136:139]\n" \
"s_waitcnt lgkmcnt(0)\n" \
"v_exp_f32 v159, v159\n" \
"v_mfma_f32_16x16x32_bf16 v[140:143], a[188:191], v[172:175], 0\n" \
"v_add_u32 v240, s86, v248\n" \
"v_cvt_pk_bf16_f32 v151, v158, v159\n" \
"v_mfma_f32_16x16x32_bf16 v[140:143], a[184:187], v[168:171], v[140:143]\n" \
"buffer_load_dwordx4 v248, s[32:35], s88 offen offset:2048 sc1 lds\n" \
"v_mfma_f32_16x16x32_bf16 v[140:143], a[180:183], v[164:167], v[140:143]\n" \
"v_exp_f32 v128, v128\n" \
"v_mfma_f32_16x16x32_bf16 v[140:143], a[176:179], v[160:163], v[140:143]\n" \
"buffer_load_dwordx4 v248, s[32:35], s88 offen offset:3072 sc1 lds\n" \
"v_smfmac_f32_16x16x64_bf16 a[44:47], a[48:51], v[144:151], v241\n" \
"s_add_u32 s89, s88, 589824\n" \
"s_add_u32 s88, s88, s27\n" \
"s_add_u32 s19, s19, 0x4000\n" \
"v_mfma_f32_16x16x32_bf16 v[96:99], a[224:227], v[148:151], v[96:99]\n" \
"s_bitset0_b32 s19, 16\n" \
"v_exp_f32 v129, v129\n" \
"v_mfma_f32_16x16x32_bf16 v[96:99], a[192:195], v[144:147], v[96:99]\n" \
"s_cmp_eq_u32 s88, s16\n" \
"v_exp_f32 v130, v130\n" \
"v_mfma_f32_16x16x32_bf16 v[100:103], a[196:199], v[144:147], v[100:103]\n" \
"s_cmov_b32 s88, s21\n" \
"ds_read_b128 a[224:227], v242 offset:8192\n" \
"v_cvt_pk_bf16_f32 v128, v128, v129\n" \
"v_mfma_f32_16x16x32_bf16 v[100:103], a[228:231], v[148:151], v[100:103]\n" \
"s_cmov_b32 s27, s91\n" \
"v_exp_f32 v131, v131\n" \
"v_mfma_f32_16x16x32_bf16 v[104:107], a[232:235], v[148:151], v[104:107]\n" \
"s_cmov_b32 s16, s77\n" \
"ds_read_b128 a[192:195], v242 offset:0\n" \
"v_cvt_pk_bf16_f32 v129, v130, v131\n" \
"v_mfma_f32_16x16x32_bf16 v[104:107], a[200:203], v[144:147], v[104:107]\n" \
"s_cmov_b32 s21, s92\n" \
"v_exp_f32 v132, v132\n" \
"v_mfma_f32_16x16x32_bf16 v[108:111], a[204:207], v[144:147], v[108:111]\n" \
"s_cmovk_i32 s91, 0x4000\n" \
"ds_read_b128 a[196:199], v242 offset:1024\n" \
"ds_read_b128 a[228:231], v242 offset:9216\n" \
"v_mfma_f32_16x16x32_bf16 v[108:111], a[236:239], v[148:151], v[108:111]\n" \
"v_exp_f32 v133, v133\n" \
"v_mfma_f32_16x16x32_bf16 v[112:115], a[240:243], v[148:151], v[112:115]\n" \
"ds_read_b128 a[232:235], v242 offset:10240\n" \
"v_cvt_pk_bf16_f32 v130, v132, v133\n" \
"v_mfma_f32_16x16x32_bf16 v[112:115], a[208:211], v[144:147], v[112:115]\n" \
"v_exp_f32 v134, v134\n" \
"v_mfma_f32_16x16x32_bf16 v[116:119], a[212:215], v[144:147], v[116:119]\n" \
"ds_read_b128 a[200:203], v242 offset:2048\n" \
"ds_read_b128 a[204:207], v242 offset:3072\n" \
"v_mfma_f32_16x16x32_bf16 v[116:119], a[244:247], v[148:151], v[116:119]\n" \
"v_exp_f32 v135, v135\n" \
"v_mfma_f32_16x16x32_bf16 v[120:123], a[248:251], v[148:151], v[120:123]\n" \
"ds_read_b128 a[236:239], v242 offset:11264\n" \
"v_cvt_pk_bf16_f32 v131, v134, v135\n" \
"v_mfma_f32_16x16x32_bf16 v[120:123], a[216:219], v[144:147], v[120:123]\n" \
"v_exp_f32 v136, v136\n" \
"v_mfma_f32_16x16x32_bf16 v[124:127], a[220:223], v[144:147], v[124:127]\n" \
"ds_read_b128 a[240:243], v242 offset:12288\n" \
"v_mfma_f32_16x16x32_bf16 v[124:127], a[252:255], v[148:151], v[124:127]\n" \
"s_waitcnt vmcnt(12)\n" \
"v_exp_f32 v137, v137\n" \
"v_mfma_f32_16x16x32_bf16 v[144:147], a[128:131], v[176:179], 0\n" \
"s_barrier\n" \
"ds_read_b128 a[208:211], v242 offset:4096\n" \
"v_cvt_pk_bf16_f32 v132, v136, v137\n" \
"v_mfma_f32_16x16x32_bf16 v[144:147], a[132:135], v[180:183], v[144:147]\n" \
"s_mov_b32 m0, s20\n" \
"v_exp_f32 v138, v138\n" \
"v_mfma_f32_16x16x32_bf16 v[144:147], a[136:139], v[184:187], v[144:147]\n" \
"buffer_load_dwordx4 v248, s[32:35], s89 offen offset:0 sc1 lds\n" \
"v_mfma_f32_16x16x32_bf16 v[144:147], a[140:143], v[188:191], v[144:147]\n" \
"v_exp_f32 v139, v139\n" \
"v_mfma_f32_16x16x32_bf16 v[148:151], a[156:159], v[188:191], 0\n" \
"ds_read_b128 a[212:215], v242 offset:5120\n" \
"v_cvt_pk_bf16_f32 v133, v138, v139\n" \
"v_mfma_f32_16x16x32_bf16 v[148:151], a[152:155], v[184:187], v[148:151]\n" \
"v_exp_f32 v140, v140\n" \
"v_mfma_f32_16x16x32_bf16 v[148:151], a[148:151], v[180:183], v[148:151]\n" \
"ds_read_b128 a[244:247], v242 offset:13312\n" \
"ds_read_b128 a[248:251], v242 offset:14336\n" \
"v_mfma_f32_16x16x32_bf16 v[148:151], a[144:147], v[176:179], v[148:151]\n" \
".Lst1_0_%=:\n" \
"s_cbranch_execz .Lst1_4_%=\n" \
"v_exp_f32 v141, v141\n" \
"v_mfma_f32_16x16x32_bf16 v[152:155], a[160:163], v[176:179], 0\n" \
"buffer_load_dwordx4 v248, s[32:35], s89 offen offset:1024 sc1 lds\n" \
"v_mfma_f32_16x16x32_bf16 v[152:155], a[164:167], v[180:183], v[152:155]\n" \
"v_exp_f32 v142, v142\n" \
"v_mfma_f32_16x16x32_bf16 v[152:155], a[168:171], v[184:187], v[152:155]\n" \
"ds_read_b128 a[216:219], v242 offset:6144\n" \
"v_cvt_pk_bf16_f32 v134, v140, v141\n" \
"v_mfma_f32_16x16x32_bf16 v[152:155], a[172:175], v[188:191], v[152:155]\n" \
"v_exp_f32 v143, v143\n" \
"v_mfma_f32_16x16x32_bf16 v[156:159], a[188:191], v[188:191], 0\n" \
"ds_read_b128 a[128:131], v240 offset:0\n" \
"v_cvt_pk_bf16_f32 v135, v142, v143\n" \
"v_mfma_f32_16x16x32_bf16 v[156:159], a[184:187], v[184:187], v[156:159]\n" \
"ds_read_b128 a[132:135], v240 offset:1024\n" \
"ds_read_b128 a[220:223], v242 offset:7168\n" \
"v_mfma_f32_16x16x32_bf16 v[156:159], a[180:183], v[180:183], v[156:159]\n" \
"v_exp_f32 v144, v144\n" \
"v_mfma_f32_16x16x32_bf16 v[156:159], a[176:179], v[176:179], v[156:159]\n" \
"buffer_load_dwordx4 v248, s[32:35], s89 offen offset:2048 sc1 lds\n" \
"v_smfmac_f32_16x16x64_bf16 a[40:43], a[48:51], v[128:135], v241\n" \
"s_waitcnt lgkmcnt(13)\n" \
"ds_read_b128 a[136:139], v240 offset:2048\n" \
"ds_read_b128 a[252:255], v242 offset:15360\n" \
"v_mfma_f32_16x16x32_bf16 v[64:67], a[224:227], v[132:135], v[64:67]\n" \
"v_exp_f32 v145, v145\n" \
"v_mfma_f32_16x16x32_bf16 v[64:67], a[192:195], v[128:131], v[64:67]\n" \
"v_exp_f32 v146, v146\n" \
"v_mfma_f32_16x16x32_bf16 v[68:71], a[196:199], v[128:131], v[68:71]\n" \
"ds_read_b128 a[140:143], v240 offset:3072\n" \
"v_cvt_pk_bf16_f32 v144, v144, v145\n" \
"v_mfma_f32_16x16x32_bf16 v[68:71], a[228:231], v[132:135], v[68:71]\n" \
"s_waitcnt lgkmcnt(12)\n" \
"v_exp_f32 v147, v147\n" \
"v_mfma_f32_16x16x32_bf16 v[72:75], a[232:235], v[132:135], v[72:75]\n" \
"buffer_load_dwordx4 v248, s[32:35], s89 offen offset:3072 sc1 lds\n" \
"v_mfma_f32_16x16x32_bf16 v[72:75], a[200:203], v[128:131], v[72:75]\n" \
"s_add_u32 s20, s20, 0x4000\n" \
"v_exp_f32 v148, v148\n" \
"v_mfma_f32_16x16x32_bf16 v[76:79], a[204:207], v[128:131], v[76:79]\n" \
"s_and_b32 s20, s20, 0xffff\n" \
"ds_read_b128 a[156:159], v240 offset:7168\n" \
"v_cvt_pk_bf16_f32 v145, v146, v147\n" \
"v_mfma_f32_16x16x32_bf16 v[76:79], a[236:239], v[132:135], v[76:79]\n" \
"s_waitcnt lgkmcnt(9)\n" \
"v_exp_f32 v149, v149\n" \
"v_mfma_f32_16x16x32_bf16 v[80:83], a[240:243], v[132:135], v[80:83]\n" \
"s_bitset1_b32 s20, 16\n" \
"ds_read_b128 a[152:155], v240 offset:6144\n" \
"v_cvt_pk_bf16_f32 v146, v148, v149\n" \
"v_mfma_f32_16x16x32_bf16 v[80:83], a[208:211], v[128:131], v[80:83]\n" \
"v_exp_f32 v150, v150\n" \
"v_mfma_f32_16x16x32_bf16 v[84:87], a[212:215], v[128:131], v[84:87]\n" \
"ds_read_b128 a[148:151], v240 offset:5120\n" \
"ds_read_b128 a[144:147], v240 offset:4096\n" \
"v_mfma_f32_16x16x32_bf16 v[84:87], a[244:247], v[132:135], v[84:87]\n" \
"s_waitcnt lgkmcnt(5)\n" \
"v_exp_f32 v151, v151\n" \
"v_mfma_f32_16x16x32_bf16 v[88:91], a[248:251], v[132:135], v[88:91]\n" \
"ds_read_b128 a[160:163], v240 offset:8192\n" \
"v_cvt_pk_bf16_f32 v147, v150, v151\n" \
"v_mfma_f32_16x16x32_bf16 v[88:91], a[216:219], v[128:131], v[88:91]\n" \
"v_exp_f32 v152, v152\n" \
"v_mfma_f32_16x16x32_bf16 v[92:95], a[220:223], v[128:131], v[92:95]\n" \
"ds_read_b128 a[164:167], v240 offset:9216\n" \
"ds_read_b128 a[168:171], v240 offset:10240\n" \
"v_mfma_f32_16x16x32_bf16 v[92:95], a[252:255], v[132:135], v[92:95]\n" \
"s_waitcnt lgkmcnt(7)\n" \
"v_exp_f32 v153, v153\n" \
"v_mfma_f32_16x16x32_bf16 v[128:131], a[128:131], v[160:163], 0\n" \
"s_add_u32 s86, s86, 0x4000\n" \
"ds_read_b128 a[172:175], v240 offset:11264\n" \
"v_cvt_pk_bf16_f32 v148, v152, v153\n" \
"v_mfma_f32_16x16x32_bf16 v[128:131], a[132:135], v[164:167], v[128:131]\n" \
"s_bitset0_b32 s86, 16\n" \
"v_exp_f32 v154, v154\n" \
"v_mfma_f32_16x16x32_bf16 v[128:131], a[136:139], v[168:171], v[128:131]\n" \
"s_cmp_eq_u32 s88, s24\n" \
"s_cbranch_scc1 .Lcht_%=\n" \
".Lsbht_%=:\n" \
"v_mfma_f32_16x16x32_bf16 v[128:131], a[140:143], v[172:175], v[128:131]\n" \
"s_waitcnt lgkmcnt(4)\n" \
"v_exp_f32 v155, v155\n" \
"v_mfma_f32_16x16x32_bf16 v[132:135], a[156:159], v[172:175], 0\n" \
"s_mov_b32 m0, s19\n" \
"v_add_u32 v242, 0x10000, v240\n" \
"v_cvt_pk_bf16_f32 v149, v154, v155\n" \
"v_mfma_f32_16x16x32_bf16 v[132:135], a[152:155], v[168:171], v[132:135]\n" \
"s_cmp_lg_u32 s76, 0\n" \
"ds_read_b128 a[188:191], v240 offset:15360\n" \
"ds_read_b128 a[184:187], v240 offset:14336\n" \
"ds_read_b128 a[180:183], v240 offset:13312\n" \
"ds_read_b128 a[176:179], v240 offset:12288\n" \
"v_exp_f32 v156, v156\n" \
"v_mfma_f32_16x16x32_bf16 v[132:135], a[148:151], v[164:167], v[132:135]\n" \
"buffer_load_dwordx4 v248, s[32:35], s88 offen offset:0 sc1 lds\n" \
"v_mfma_f32_16x16x32_bf16 v[132:135], a[144:147], v[160:163], v[132:135]\n" \
"s_waitcnt lgkmcnt(4)\n" \
"v_exp_f32 v157, v157\n" \
"v_mfma_f32_16x16x32_bf16 v[136:139], a[160:163], v[160:163], 0\n" \
"s_cbranch_scc1 .Lant_%=\n" \
".Labt_%=:\n" \
"v_cvt_pk_bf16_f32 v150, v156, v157\n" \
"v_mfma_f32_16x16x32_bf16 v[136:139], a[164:167], v[164:167], v[136:139]\n" \
"v_exp_f32 v158, v158\n" \
"v_mfma_f32_16x16x32_bf16 v[136:139], a[168:171], v[168:171], v[136:139]\n" \
"buffer_load_dwordx4 v248, s[32:35], s88 offen offset:1024 sc1 lds\n" \
"v_mfma_f32_16x16x32_bf16 v[136:139], a[172:175], v[172:175], v[136:139]\n" \
"s_waitcnt lgkmcnt(0)\n" \
"v_exp_f32 v159, v159\n" \
"v_mfma_f32_16x16x32_bf16 v[140:143], a[188:191], v[172:175], 0\n" \
"v_add_u32 v240, s86, v248\n" \
"v_cvt_pk_bf16_f32 v151, v158, v159\n" \
"v_mfma_f32_16x16x32_bf16 v[140:143], a[184:187], v[168:171], v[140:143]\n" \
"buffer_load_dwordx4 v248, s[32:35], s88 offen offset:2048 sc1 lds\n" \
"v_mfma_f32_16x16x32_bf16 v[140:143], a[180:183], v[164:167], v[140:143]\n" \
"v_exp_f32 v128, v128\n" \
"v_mfma_f32_16x16x32_bf16 v[140:143], a[176:179], v[160:163], v[140:143]\n" \
"buffer_load_dwordx4 v248, s[32:35], s88 offen offset:3072 sc1 lds\n" \
"v_smfmac_f32_16x16x64_bf16 a[44:47], a[48:51], v[144:151], v241\n" \
"s_add_u32 s89, s88, 589824\n" \
"s_add_u32 s88, s88, s27\n" \
"s_add_u32 s19, s19, 0x4000\n" \
"v_mfma_f32_16x16x32_bf16 v[96:99], a[224:227], v[148:151], v[96:99]\n" \
"s_bitset0_b32 s19, 16\n" \
"v_exp_f32 v129, v129\n" \
"v_mfma_f32_16x16x32_bf16 v[96:99], a[192:195], v[144:147], v[96:99]\n" \
"s_cmp_eq_u32 s88, s16\n" \
"v_exp_f32 v130, v130\n" \
"v_mfma_f32_16x16x32_bf16 v[100:103], a[196:199], v[144:147], v[100:103]\n" \
"s_cmov_b32 s88, s21\n" \
"ds_read_b128 a[224:227], v242 offset:8192\n" \
"v_cvt_pk_bf16_f32 v128, v128, v129\n" \
"v_mfma_f32_16x16x32_bf16 v[100:103], a[228:231], v[148:151], v[100:103]\n" \
"s_cmov_b32 s27, s91\n" \
"v_exp_f32 v131, v131\n" \
"v_mfma_f32_16x16x32_bf16 v[104:107], a[232:235], v[148:151], v[104:107]\n" \
"s_cmov_b32 s16, s77\n" \
"ds_read_b128 a[192:195], v242 offset:0\n" \
"v_cvt_pk_bf16_f32 v129, v130, v131\n" \
"v_mfma_f32_16x16x32_bf16 v[104:107], a[200:203], v[144:147], v[104:107]\n" \
"s_cmov_b32 s21, s92\n" \
"v_exp_f32 v132, v132\n" \
"v_mfma_f32_16x16x32_bf16 v[108:111], a[204:207], v[144:147], v[108:111]\n" \
"s_cmovk_i32 s91, 0x4000\n" \
"ds_read_b128 a[196:199], v242 offset:1024\n" \
"ds_read_b128 a[228:231], v242 offset:9216\n" \
"v_mfma_f32_16x16x32_bf16 v[108:111], a[236:239], v[148:151], v[108:111]\n" \
"v_exp_f32 v133, v133\n" \
"v_mfma_f32_16x16x32_bf16 v[112:115], a[240:243], v[148:151], v[112:115]\n" \
"ds_read_b128 a[232:235], v242 offset:10240\n" \
"v_cvt_pk_bf16_f32 v130, v132, v133\n" \
"v_mfma_f32_16x16x32_bf16 v[112:115], a[208:211], v[144:147], v[112:115]\n" \
"v_exp_f32 v134, v134\n" \
"v_mfma_f32_16x16x32_bf16 v[116:119], a[212:215], v[144:147], v[116:119]\n" \
"ds_read_b128 a[200:203], v242 offset:2048\n" \
"ds_read_b128 a[204:207], v242 offset:3072\n" \
"v_mfma_f32_16x16x32_bf16 v[116:119], a[244:247], v[148:151], v[116:119]\n" \
"v_exp_f32 v135, v135\n" \
"v_mfma_f32_16x16x32_bf16 v[120:123], a[248:251], v[148:151], v[120:123]\n" \
"ds_read_b128 a[236:239], v242 offset:11264\n" \
"v_cvt_pk_bf16_f32 v131, v134, v135\n" \
"v_mfma_f32_16x16x32_bf16 v[120:123], a[216:219], v[144:147], v[120:123]\n" \
"v_exp_f32 v136, v136\n" \
"v_mfma_f32_16x16x32_bf16 v[124:127], a[220:223], v[144:147], v[124:127]\n" \
"ds_read_b128 a[240:243], v242 offset:12288\n" \
"v_mfma_f32_16x16x32_bf16 v[124:127], a[252:255], v[148:151], v[124:127]\n" \
"s_waitcnt vmcnt(12)\n" \
"v_exp_f32 v137, v137\n" \
"v_mfma_f32_16x16x32_bf16 v[144:147], a[128:131], v[176:179], 0\n" \
"s_barrier\n" \
"ds_read_b128 a[208:211], v242 offset:4096\n" \
"v_cvt_pk_bf16_f32 v132, v136, v137\n" \
"v_mfma_f32_16x16x32_bf16 v[144:147], a[132:135], v[180:183], v[144:147]\n" \
"s_mov_b32 m0, s20\n" \
"v_exp_f32 v138, v138\n" \
"v_mfma_f32_16x16x32_bf16 v[144:147], a[136:139], v[184:187], v[144:147]\n" \
"buffer_load_dwordx4 v248, s[32:35], s89 offen offset:0 sc1 lds\n" \
"v_mfma_f32_16x16x32_bf16 v[144:147], a[140:143], v[188:191], v[144:147]\n" \
"v_exp_f32 v139, v139\n" \
"v_mfma_f32_16x16x32_bf16 v[148:151], a[156:159], v[188:191], 0\n" \
"ds_read_b128 a[212:215], v242 offset:5120\n" \
"v_cvt_pk_bf16_f32 v133, v138, v139\n" \
"v_mfma_f32_16x16x32_bf16 v[148:151], a[152:155], v[184:187], v[148:151]\n" \
"v_exp_f32 v140, v140\n" \
"v_mfma_f32_16x16x32_bf16 v[148:151], a[148:151], v[180:183], v[148:151]\n" \
"ds_read_b128 a[244:247], v242 offset:13312\n" \
"ds_read_b128 a[248:251], v242 offset:14336\n" \
"v_mfma_f32_16x16x32_bf16 v[148:151], a[144:147], v[176:179], v[148:151]\n" \
"v_exp_f32 v141, v141\n" \
"v_mfma_f32_16x16x32_bf16 v[152:155], a[160:163], v[176:179], 0\n" \
"buffer_load_dwordx4 v248, s[32:35], s89 offen offset:1024 sc1 lds\n" \
"v_mfma_f32_16x16x32_bf16 v[152:155], a[164:167], v[180:183], v[152:155]\n" \
"v_exp_f32 v142, v142\n" \
"v_mfma_f32_16x16x32_bf16 v[152:155], a[168:171], v[184:187], v[152:155]\n" \
"ds_read_b128 a[216:219], v242 offset:6144\n" \
"v_cvt_pk_bf16_f32 v134, v140, v141\n" \
"v_mfma_f32_16x16x32_bf16 v[152:155], a[172:175], v[188:191], v[152:155]\n" \
"v_exp_f32 v143, v143\n" \
"v_mfma_f32_16x16x32_bf16 v[156:159], a[188:191], v[188:191], 0\n" \
"ds_read_b128 a[128:131], v240 offset:0\n" \
"v_cvt_pk_bf16_f32 v135, v142, v143\n" \
"v_mfma_f32_16x16x32_bf16 v[156:159], a[184:187], v[184:187], v[156:159]\n" \
"ds_read_b128 a[132:135], v240 offset:1024\n" \
"ds_read_b128 a[220:223], v242 offset:7168\n" \
"v_mfma_f32_16x16x32_bf16 v[156:159], a[180:183], v[180:183], v[156:159]\n" \
"v_exp_f32 v144, v144\n" \
"v_mfma_f32_16x16x32_bf16 v[156:159], a[176:179], v[176:179], v[156:159]\n" \
"buffer_load_dwordx4 v248, s[32:35], s89 offen offset:2048 sc1 lds\n" \
"v_smfmac_f32_16x16x64_bf16 a[40:43], a[48:51], v[128:135], v241\n" \
"s_waitcnt lgkmcnt(13)\n" \
"ds_read_b128 a[136:139], v240 offset:2048\n" \
"ds_read_b128 a[252:255], v242 offset:15360\n" \
"v_mfma_f32_16x16x32_bf16 v[64:67], a[224:227], v[132:135], v[64:67]\n" \
"v_exp_f32 v145, v145\n" \
"v_mfma_f32_16x16x32_bf16 v[64:67], a[192:195], v[128:131], v[64:67]\n" \
"v_exp_f32 v146, v146\n" \
"v_mfma_f32_16x16x32_bf16 v[68:71], a[196:199], v[128:131], v[68:71]\n" \
"ds_read_b128 a[140:143], v240 offset:3072\n" \
"v_cvt_pk_bf16_f32 v144, v144, v145\n" \
"v_mfma_f32_16x16x32_bf16 v[68:71], a[228:231], v[132:135], v[68:71]\n" \
"s_waitcnt lgkmcnt(12)\n" \
"v_exp_f32 v147, v147\n" \
"v_mfma_f32_16x16x32_bf16 v[72:75], a[232:235], v[132:135], v[72:75]\n" \
"buffer_load_dwordx4 v248, s[32:35], s89 offen offset:3072 sc1 lds\n" \
"v_mfma_f32_16x16x32_bf16 v[72:75], a[200:203], v[128:131], v[72:75]\n" \
"s_add_u32 s20, s20, 0x4000\n" \
"v_exp_f32 v148, v148\n" \
"v_mfma_f32_16x16x32_bf16 v[76:79], a[204:207], v[128:131], v[76:79]\n" \
"s_and_b32 s20, s20, 0xffff\n" \
"ds_read_b128 a[156:159], v240 offset:7168\n" \
"v_cvt_pk_bf16_f32 v145, v146, v147\n" \
"v_mfma_f32_16x16x32_bf16 v[76:79], a[236:239], v[132:135], v[76:79]\n" \
"s_waitcnt lgkmcnt(9)\n" \
"v_exp_f32 v149, v149\n" \
"v_mfma_f32_16x16x32_bf16 v[80:83], a[240:243], v[132:135], v[80:83]\n" \
"s_bitset1_b32 s20, 16\n" \
"ds_read_b128 a[152:155], v240 offset:6144\n" \
"v_cvt_pk_bf16_f32 v146, v148, v149\n" \
"v_mfma_f32_16x16x32_bf16 v[80:83], a[208:211], v[128:131], v[80:83]\n" \
"v_exp_f32 v150, v150\n" \
"v_mfma_f32_16x16x32_bf16 v[84:87], a[212:215], v[128:131], v[84:87]\n" \
"s_sub_u32 s95, s95, 1\n" \
"ds_read_b128 a[148:151], v240 offset:5120\n" \
"ds_read_b128 a[144:147], v240 offset:4096\n" \
"v_mfma_f32_16x16x32_bf16 v[84:87], a[244:247], v[132:135], v[84:87]\n" \
"s_waitcnt lgkmcnt(5)\n" \
"v_exp_f32 v151, v151\n" \
"v_mfma_f32_16x16x32_bf16 v[88:91], a[248:251], v[132:135], v[88:91]\n" \
"ds_read_b128 a[160:163], v240 offset:8192\n" \
"v_cvt_pk_bf16_f32 v147, v150, v151\n" \
"v_mfma_f32_16x16x32_bf16 v[88:91], a[216:219], v[128:131], v[88:91]\n" \
"v_exp_f32 v152, v152\n" \
"v_mfma_f32_16x16x32_bf16 v[92:95], a[220:223], v[128:131], v[92:95]\n" \
"s_waitcnt lgkmcnt(5)\n" \
"ds_read_b128 a[164:167], v240 offset:9216\n" \
"ds_read_b128 a[168:171], v240 offset:10240\n" \
"ds_read_b128 a[172:175], v240 offset:11264\n" \
"v_exp_f32 v153, v153\n" \
"v_mfma_f32_16x16x32_bf16 v[92:95], a[252:255], v[132:135], v[92:95]\n" \
"s_cbranch_scc0 .Lloop2_%=\n" \
"s_cmp_lt_u32 s67, 4\n" \
"s_cbranch_scc0 .Le1_%=\n" \
"s_mov_b32 s95, 3\n" \
".p2align 3\n" \
".Lloop3_%=:\n" \
".Lst1_1_%=:\n" \
"s_cbranch_execz .Lst1_5_%=\n" \
  \
"v_mfma_f32_16x16x32_bf16 v[128:131], a[128:131], v[160:163], 0\n" \
"s_add_u32 s86, s86, 0x4000\n" \
"ds_read_b128 a[188:191], v240 offset:15360\n" \
"v_cvt_pk_bf16_f32 v148, v152, v153\n" \
"v_mfma_f32_16x16x32_bf16 v[128:131], a[132:135], v[164:167], v[128:131]\n" \
"s_bitset0_b32 s86, 16\n" \
"v_exp_f32 v154, v154\n" \
"v_mfma_f32_16x16x32_bf16 v[128:131], a[136:139], v[168:171], v[128:131]\n" \
"ds_read_b128 a[184:187], v240 offset:14336\n" \
"v_add_u32 v242, 0x10000, v240\n" \
"v_mfma_f32_16x16x32_bf16 v[128:131], a[140:143], v[172:175], v[128:131]\n" \
"s_waitcnt lgkmcnt(6)\n" \
"v_exp_f32 v155, v155\n" \
"v_mfma_f32_16x16x32_bf16 v[132:135], a[156:159], v[172:175], 0\n" \
"s_mov_b32 m0, s19\n" \
"ds_read_b128 a[180:183], v240 offset:13312\n" \
"v_cvt_pk_bf16_f32 v149, v154, v155\n" \
"v_mfma_f32_16x16x32_bf16 v[132:135], a[152:155], v[168:171], v[132:135]\n" \
"ds_read_b128 a[176:179], v240 offset:12288\n" \
"v_exp_f32 v156, v156\n" \
"v_mfma_f32_16x16x32_bf16 v[132:135], a[148:151], v[164:167], v[132:135]\n" \
"buffer_load_dwordx4 v248, s[32:35], s88 offen offset:0 sc1 lds\n" \
"v_mfma_f32_16x16x32_bf16 v[132:135], a[144:147], v[160:163], v[132:135]\n" \
"s_waitcnt lgkmcnt(4)\n" \
"v_exp_f32 v157, v157\n" \
"v_mfma_f32_16x16x32_bf16 v[136:139], a[160:163], v[160:163], 0\n" \
"v_cvt_pk_bf16_f32 v150, v156, v157\n" \
"v_mfma_f32_16x16x32_bf16 v[136:139], a[164:167], v[164:167], v[136:139]\n" \
"v_exp_f32 v158, v158\n" \
"v_mfma_f32_16x16x32_bf16 v[136:139], a[168:171], v[168:171], v[136:139]\n" \
"buffer_load_dwordx4 v248, s[32:35], s88 offen offset:1024 sc1 lds\n" \
"v_mfma_f32_16x16x32_bf16 v[136:139], a[172:175], v[172:175], v[136:139]\n" \
"s_waitcnt lgkmcnt(0)\n" \
"v_exp_f32 v159, v159\n" \
"v_mfma_f32_16x16x32_bf16 v[140:143], a[188:191], v[172:175], 0\n" \
"v_add_u32 v240, s86, v248\n" \
"v_cvt_pk_bf16_f32 v151, v158, v159\n" \
"v_mfma_f32_16x16x32_bf16 v[140:143], a[184:187], v[168:171], v[140:143]\n" \
"buffer_load_dwordx4 v248, s[32:35], s88 offen offset:2048 sc1 lds\n" \
"v_mfma_f32_16x16x32_bf16 v[140:143], a[180:183], v[164:167], v[140:143]\n" \
"v_exp_f32 v128, v128\n" \
"v_mfma_f32_16x16x32_bf16 v[140:143], a[176:179], v[160:163], v[140:143]\n" \
"buffer_load_dwordx4 v248, s[32:35], s88 offen offset:3072 sc1 lds\n" \
"v_smfmac_f32_16x16x64_bf16 a[44:47], a[48:51], v[144:151], v241\n" \
"s_add_u32 s89, s88, 589824\n" \
"s_add_u32 s88, s88, s27\n" \
"s_add_u32 s19, s19, 0x4000\n" \
"v_mfma_f32_16x16x32_bf16 v[96:99], a[224:227], v[148:151], v[96:99]\n" \
"s_bitset0_b32 s19, 16\n" \
"v_exp_f32 v129, v129\n" \
"v_mfma_f32_16x16x32_bf16 v[96:99], a[192:195], v[144:147], v[96:99]\n" \
"s_cmp_eq_u32 s88, s16\n" \
"v_exp_f32 v130, v130\n" \
"v_mfma_f32_16x16x32_bf16 v[100:103], a[196:199], v[144:147], v[100:103]\n" \
"s_cmov_b32 s88, s21\n" \
"ds_read_b128 a[224:227], v242 offset:8192\n" \
"v_cvt_pk_bf16_f32 v128, v128, v129\n" \
"v_mfma_f32_16x16x32_bf16 v[100:103], a[228:231], v[148:151], v[100:103]\n" \
"s_cmov_b32 s27, s91\n" \
"v_exp_f32 v131, v131\n" \
"v_mfma_f32_16x16x32_bf16 v[104:107], a[232:235], v[148:151], v[104:107]\n" \
"s_cmov_b32 s16, s77\n" \
"ds_read_b128 a[192:195], v242 offset:0\n" \
"v_cvt_pk_bf16_f32 v129, v130, v131\n" \
"v_mfma_f32_16x16x32_bf16 v[104:107], a[200:203], v[144:147], v[104:107]\n" \
"s_cmov_b32 s21, s92\n" \
"v_exp_f32 v132, v132\n" \
"v_mfma_f32_16x16x32_bf16 v[108:111], a[204:207], v[144:147], v[108:111]\n" \
"s_cmovk_i32 s91, 0x4000\n" \
"ds_read_b128 a[196:199], v242 offset:1024\n" \
"ds_read_b128 a[228:231], v242 offset:9216\n" \
"v_mfma_f32_16x16x32_bf16 v[108:111], a[236:239], v[148:151], v[108:111]\n" \
"s_mov_b32 m0, s87\n" \
"v_exp_f32 v133, v133\n" \
"v_mfma_f32_16x16x32_bf16 v[112:115], a[240:243], v[148:151], v[112:115]\n" \
"ds_read_b128 a[232:235], v242 offset:10240\n" \
"v_cvt_pk_bf16_f32 v130, v132, v133\n" \
"v_mfma_f32_16x16x32_bf16 v[112:115], a[208:211], v[144:147], v[112:115]\n" \
"buffer_load_dwordx4 v237, s[48:51], s93 offen lds\n" \
"v_mfma_f32_16x16x32_bf16 v[116:119], a[212:215], v[144:147], v[116:119]\n" \
"s_add_u32 s93, s93, 8192\n" \
"v_exp_f32 v134, v134\n" \
"v_mfma_f32_16x16x32_bf16 v[116:119], a[244:247], v[148:151], v[116:119]\n" \
"s_add_u32 s87, s87, 1024\n" \
"v_exp_f32 v135, v135\n" \
"v_mfma_f32_16x16x32_bf16 v[120:123], a[248:251], v[148:151], v[120:123]\n" \
"s_and_b32 s22, s87, 0xfff\n" \
"ds_read_b128 a[200:203], v242 offset:2048\n" \
"v_cvt_pk_bf16_f32 v131, v134, v135\n" \
"v_mfma_f32_16x16x32_bf16 v[120:123], a[216:219], v[144:147], v[120:123]\n" \
"s_cselect_b32 s22, 0, 0xffff8080\n" \
"v_exp_f32 v136, v136\n" \
"v_mfma_f32_16x16x32_bf16 v[124:127], a[220:223], v[144:147], v[124:127]\n" \
"s_add_u32 s93, s93, s22\n" \
"ds_read_b128 a[204:207], v242 offset:3072\n" \
"ds_read_b128 a[236:239], v242 offset:11264\n" \
"v_mfma_f32_16x16x32_bf16 v[124:127], a[252:255], v[148:151], v[124:127]\n" \
"s_waitcnt vmcnt(13)\n" \
"v_exp_f32 v137, v137\n" \
"v_mfma_f32_16x16x32_bf16 v[144:147], a[128:131], v[176:179], 0\n" \
"s_barrier\n" \
"ds_read_b128 a[240:243], v242 offset:12288\n" \
"v_cvt_pk_bf16_f32 v132, v136, v137\n" \
"v_mfma_f32_16x16x32_bf16 v[144:147], a[132:135], v[180:183], v[144:147]\n" \
"s_mov_b32 m0, s20\n" \
"v_exp_f32 v138, v138\n" \
"v_mfma_f32_16x16x32_bf16 v[144:147], a[136:139], v[184:187], v[144:147]\n" \
"buffer_load_dwordx4 v248, s[32:35], s89 offen offset:0 sc1 lds\n" \
"v_mfma_f32_16x16x32_bf16 v[144:147], a[140:143], v[188:191], v[144:147]\n" \
"v_exp_f32 v139, v139\n" \
"v_mfma_f32_16x16x32_bf16 v[148:151], a[156:159], v[188:191], 0\n" \
"ds_read_b128 a[208:211], v242 offset:4096\n" \
"v_cvt_pk_bf16_f32 v133, v138, v139\n" \
"v_mfma_f32_16x16x32_bf16 v[148:151], a[152:155], v[184:187], v[148:151]\n" \
"v_exp_f32 v140, v140\n" \
"v_mfma_f32_16x16x32_bf16 v[148:151], a[148:151], v[180:183], v[148:151]\n" \
"ds_read_b128 a[212:215], v242 offset:5120\n" \
"ds_read_b128 a[244:247], v242 offset:13312\n" \
"v_mfma_f32_16x16x32_bf16 v[148:151], a[144:147], v[176:179], v[148:151]\n" \
"v_exp_f32 v141, v141\n" \
"v_mfma_f32_16x16x32_bf16 v[152:155], a[160:163], v[176:179], 0\n" \
"buffer_load_dwordx4 v248, s[32:35], s89 offen offset:1024 sc1 lds\n" \
"v_mfma_f32_16x16x32_bf16 v[152:155], a[164:167], v[180:183], v[152:155]\n" \
"v_exp_f32 v142, v142\n" \
"v_mfma_f32_16x16x32_bf16 v[152:155], a[168:171], v[184:187], v[152:155]\n" \
"ds_read_b128 a[248:251], v242 offset:14336\n" \
"v_cvt_pk_bf16_f32 v134, v140, v141\n" \
"v_mfma_f32_16x16x32_bf16 v[152:155], a[172:175], v[188:191], v[152:155]\n" \
"v_exp_f32 v143, v143\n" \
"v_mfma_f32_16x16x32_bf16 v[156:159], a[188:191], v[188:191], 0\n" \
"ds_read_b128 a[216:219], v242 offset:6144\n" \
"v_cvt_pk_bf16_f32 v135, v142, v143\n" \
"v_mfma_f32_16x16x32_bf16 v[156:159], a[184:187], v[184:187], v[156:159]\n" \
"ds_read_b128 a[128:131], v240 offset:0\n" \
"ds_read_b128 a[132:135], v240 offset:1024\n" \
"v_mfma_f32_16x16x32_bf16 v[156:159], a[180:183], v[180:183], v[156:159]\n" \
"v_exp_f32 v144, v144\n" \
"v_mfma_f32_16x16x32_bf16 v[156:159], a[176:179], v[176:179], v[156:159]\n" \
"buffer_load_dwordx4 v248, s[32:35], s89 offen offset:2048 sc1 lds\n" \
"v_smfmac_f32_16x16x64_bf16 a[40:43], a[48:51], v[128:135], v241\n" \
"s_waitcnt lgkmcnt(12)\n" \
"ds_read_b128 a[220:223], v242 offset:7168\n" \
"ds_read_b128 a[136:139], v240 offset:2048\n" \
"v_mfma_f32_16x16x32_bf16 v[64:67], a[224:227], v[132:135], v[64:67]\n" \
"v_exp_f32 v145, v145\n" \
"v_mfma_f32_16x16x32_bf16 v[64:67], a[192:195], v[128:131], v[64:67]\n" \
"v_exp_f32 v146, v146\n" \
"v_mfma_f32_16x16x32_bf16 v[68:71], a[196:199], v[128:131], v[68:71]\n" \
"ds_read_b128 a[252:255], v242 offset:15360\n" \
"v_cvt_pk_bf16_f32 v144, v144, v145\n" \
"v_mfma_f32_16x16x32_bf16 v[68:71], a[228:231], v[132:135], v[68:71]\n" \
"s_waitcnt lgkmcnt(11)\n" \
"v_exp_f32 v147, v147\n" \
"v_mfma_f32_16x16x32_bf16 v[72:75], a[232:235], v[132:135], v[72:75]\n" \
"buffer_load_dwordx4 v248, s[32:35], s89 offen offset:3072 sc1 lds\n" \
"v_mfma_f32_16x16x32_bf16 v[72:75], a[200:203], v[128:131], v[72:75]\n" \
"s_add_u32 s20, s20, 0x4000\n" \
"v_exp_f32 v148, v148\n" \
"v_mfma_f32_16x16x32_bf16 v[76:79], a[204:207], v[128:131], v[76:79]\n" \
"s_and_b32 s20, s20, 0xffff\n" \
"ds_read_b128 a[140:143], v240 offset:3072\n" \
"v_cvt_pk_bf16_f32 v145, v146, v147\n" \
"v_mfma_f32_16x16x32_bf16 v[76:79], a[236:239], v[132:135], v[76:79]\n" \
"s_waitcnt lgkmcnt(8)\n" \
"v_exp_f32 v149, v149\n" \
"v_mfma_f32_16x16x32_bf16 v[80:83], a[240:243], v[132:135], v[80:83]\n" \
"s_bitset1_b32 s20, 16\n" \
"ds_read_b128 a[156:159], v240 offset:7168\n" \
"v_cvt_pk_bf16_f32 v146, v148, v149\n" \
"v_mfma_f32_16x16x32_bf16 v[80:83], a[208:211], v[128:131], v[80:83]\n" \
"v_exp_f32 v150, v150\n" \
"v_mfma_f32_16x16x32_bf16 v[84:87], a[212:215], v[128:131], v[84:87]\n" \
"ds_read_b128 a[152:155], v240 offset:6144\n" \
"ds_read_b128 a[148:151], v240 offset:5120\n" \
"v_mfma_f32_16x16x32_bf16 v[84:87], a[244:247], v[132:135], v[84:87]\n" \
"s_waitcnt lgkmcnt(4)\n" \
"v_exp_f32 v151, v151\n" \
"v_mfma_f32_16x16x32_bf16 v[88:91], a[248:251], v[132:135], v[88:91]\n" \
"ds_read_b128 a[144:147], v240 offset:4096\n" \
"v_cvt_pk_bf16_f32 v147, v150, v151\n" \
"v_mfma_f32_16x16x32_bf16 v[88:91], a[216:219], v[128:131], v[88:91]\n" \
"v_exp_f32 v152, v152\n" \
"v_mfma_f32_16x16x32_bf16 v[92:95], a[220:223], v[128:131], v[92:95]\n" \
"ds_read_b128 a[160:163], v240 offset:8192\n" \
"ds_read_b128 a[164:167], v240 offset:9216\n" \
"v_mfma_f32_16x16x32_bf16 v[92:95], a[252:255], v[132:135], v[92:95]\n" \
"s_waitcnt lgkmcnt(6)\n" \
"v_exp_f32 v153, v153\n" \
"v_mfma_f32_16x16x32_bf16 v[128:131], a[128:131], v[160:163], 0\n" \
"s_add_u32 s86, s86, 0x4000\n" \
"ds_read_b128 a[168:171], v240 offset:10240\n" \
"v_cvt_pk_bf16_f32 v148, v152, v153\n" \
"v_mfma_f32_16x16x32_bf16 v[128:131], a[132:135], v[164:167], v[128:131]\n" \
"s_bitset0_b32 s86, 16\n" \
"ds_read_b128 a[172:175], v240 offset:11264\n" \
"v_exp_f32 v154, v154\n" \
"v_mfma_f32_16x16x32_bf16 v[128:131], a[136:139], v[168:171], v[128:131]\n" \
"s_cmp_eq_u32 s88, s24\n" \
"s_cbranch_scc1 .Lchtx_%=\n" \
".Lsbhtx_%=:\n" \
"v_mfma_f32_16x16x32_bf16 v[128:131], a[140:143], v[172:175], v[128:131]\n" \
"s_waitcnt lgkmcnt(4)\n" \
"v_exp_f32 v155, v155\n" \
"v_mfma_f32_16x16x32_bf16 v[132:135], a[156:159], v[172:175], 0\n" \
"s_mov_b32 m0, s19\n" \
"v_add_u32 v242, 0x10000, v240\n" \
"v_cvt_pk_bf16_f32 v149, v154, v155\n" \
"v_mfma_f32_16x16x32_bf16 v[132:135], a[152:155], v[168:171], v[132:135]\n" \
"s_cmp_lg_u32 s76, 0\n" \
"ds_read_b128 a[188:191], v240 offset:15360\n" \
"ds_read_b128 a[184:187], v240 offset:14336\n" \
"ds_read_b128 a[180:183], v240 offset:13312\n" \
"ds_read_b128 a[176:179], v240 offset:12288\n" \
"v_exp_f32 v156, v156\n" \
"v_mfma_f32_16x16x32_bf16 v[132:135], a[148:151], v[164:167], v[132:135]\n" \
"buffer_load_dwordx4 v248, s[32:35], s88 offen offset:0 sc1 lds\n" \
"v_mfma_f32_16x16x32_bf16 v[132:135], a[144:147], v[160:163], v[132:135]\n" \
"s_waitcnt lgkmcnt(4)\n" \
"v_exp_f32 v157, v157\n" \
"v_mfma_f32_16x16x32_bf16 v[136:139], a[160:163], v[160:163], 0\n" \
"s_cbranch_scc1 .Lantx_%=\n" \
".Labtx_%=:\n" \
"v_cvt_pk_bf16_f32 v150, v156, v157\n" \
"v_mfma_f32_16x16x32_bf16 v[136:139], a[164:167], v[164:167], v[136:139]\n" \
"v_exp_f32 v158, v158\n" \
"v_mfma_f32_16x16x32_bf16 v[136:139], a[168:171], v[168:171], v[136:139]\n" \
"buffer_load_dwordx4 v248, s[32:35], s88 offen offset:1024 sc1 lds\n" \
"v_mfma_f32_16x16x32_bf16 v[136:139], a[172:175], v[172:175], v[136:139]\n" \
"s_waitcnt lgkmcnt(0)\n" \
"v_exp_f32 v159, v159\n" \
"v_mfma_f32_16x16x32_bf16 v[140:143], a[188:191], v[172:175], 0\n" \
"v_add_u32 v240, s86, v248\n" \
"v_cvt_pk_bf16_f32 v151, v158, v159\n" \
"v_mfma_f32_16x16x32_bf16 v[140:143], a[184:187], v[168:171], v[140:143]\n" \
"buffer_load_dwordx4 v248, s[32:35], s88 offen offset:2048 sc1 lds\n" \
"v_mfma_f32_16x16x32_bf16 v[140:143], a[180:183], v[164:167], v[140:143]\n" \
"v_exp_f32 v128, v128\n" \
"v_mfma_f32_16x16x32_bf16 v[140:143], a[176:179], v[160:163], v[140:143]\n" \
"buffer_load_dwordx4 v248, s[32:35], s88 offen offset:3072 sc1 lds\n" \
"v_smfmac_f32_16x16x64_bf16 a[44:47], a[48:51], v[144:151], v241\n" \
"s_add_u32 s89, s88, 589824\n" \
"s_add_u32 s88, s88, s27\n" \
"s_add_u32 s19, s19, 0x4000\n" \
"v_mfma_f32_16x16x32_bf16 v[96:99], a[224:227], v[148:151], v[96:99]\n" \
"s_bitset0_b32 s19, 16\n" \
"v_exp_f32 v129, v129\n" \
"v_mfma_f32_16x16x32_bf16 v[96:99], a[192:195], v[144:147], v[96:99]\n" \
"s_cmp_eq_u32 s88, s16\n" \
"v_exp_f32 v130, v130\n" \
"v_mfma_f32_16x16x32_bf16 v[100:103], a[196:199], v[144:147], v[100:103]\n" \
"s_cmov_b32 s88, s21\n" \
"ds_read_b128 a[224:227], v242 offset:8192\n" \
"v_cvt_pk_bf16_f32 v128, v128, v129\n" \
"v_mfma_f32_16x16x32_bf16 v[100:103], a[228:231], v[148:151], v[100:103]\n" \
"s_cmov_b32 s27, s91\n" \
"v_exp_f32 v131, v131\n" \
"v_mfma_f32_16x16x32_bf16 v[104:107], a[232:235], v[148:151], v[104:107]\n" \
"s_cmov_b32 s16, s77\n" \
"ds_read_b128 a[192:195], v242 offset:0\n" \
"v_cvt_pk_bf16_f32 v129, v130, v131\n" \
"v_mfma_f32_16x16x32_bf16 v[104:107], a[200:203], v[144:147], v[104:107]\n" \
"s_cmov_b32 s21, s92\n" \
"v_exp_f32 v132, v132\n" \
"v_mfma_f32_16x16x32_bf16 v[108:111], a[204:207], v[144:147], v[108:111]\n" \
"s_cmovk_i32 s91, 0x4000\n" \
"ds_read_b128 a[196:199], v242 offset:1024\n" \
"ds_read_b128 a[228:231], v242 offset:9216\n" \
"v_mfma_f32_16x16x32_bf16 v[108:111], a[236:239], v[148:151], v[108:111]\n" \
"s_mov_b32 m0, s87\n" \
"v_exp_f32 v133, v133\n" \
"v_mfma_f32_16x16x32_bf16 v[112:115], a[240:243], v[148:151], v[112:115]\n" \
"ds_read_b128 a[232:235], v242 offset:10240\n" \
"v_cvt_pk_bf16_f32 v130, v132, v133\n" \
"v_mfma_f32_16x16x32_bf16 v[112:115], a[208:211], v[144:147], v[112:115]\n" \
"buffer_load_dwordx4 v237, s[48:51], s93 offen lds\n" \
"v_mfma_f32_16x16x32_bf16 v[116:119], a[212:215], v[144:147], v[116:119]\n" \
"s_add_u32 s93, s93, 8192\n" \
"v_exp_f32 v134, v134\n" \
"v_mfma_f32_16x16x32_bf16 v[116:119], a[244:247], v[148:151], v[116:119]\n" \
"s_add_u32 s87, s87, 1024\n" \
"v_exp_f32 v135, v135\n" \
"v_mfma_f32_16x16x32_bf16 v[120:123], a[248:251], v[148:151], v[120:123]\n" \
"s_and_b32 s22, s87, 0xfff\n" \
"ds_read_b128 a[200:203], v242 offset:2048\n" \
"v_cvt_pk_bf16_f32 v131, v134, v135\n" \
"v_mfma_f32_16x16x32_bf16 v[120:123], a[216:219], v[144:147], v[120:123]\n" \
"s_cselect_b32 s22, 0, 0xffff8080\n" \
"v_exp_f32 v136, v136\n" \
"v_mfma_f32_16x16x32_bf16 v[124:127], a[220:223], v[144:147], v[124:127]\n" \
"s_add_u32 s93, s93, s22\n" \
"ds_read_b128 a[204:207], v242 offset:3072\n" \
"ds_read_b128 a[236:239], v242 offset:11264\n" \
"v_mfma_f32_16x16x32_bf16 v[124:127], a[252:255], v[148:151], v[124:127]\n" \
"s_waitcnt vmcnt(14)\n" \
"v_exp_f32 v137, v137\n" \
"v_mfma_f32_16x16x32_bf16 v[144:147], a[128:131], v[176:179], 0\n" \
"s_barrier\n" \
"ds_read_b128 a[240:243], v242 offset:12288\n" \
"v_cvt_pk_bf16_f32 v132, v136, v137\n" \
"v_mfma_f32_16x16x32_bf16 v[144:147], a[132:135], v[180:183], v[144:147]\n" \
"s_mov_b32 m0, s20\n" \
"v_exp_f32 v138, v138\n" \
"v_mfma_f32_16x16x32_bf16 v[144:147], a[136:139], v[184:187], v[144:147]\n" \
"buffer_load_dwordx4 v248, s[32:35], s89 offen offset:0 sc1 lds\n" \
"v_mfma_f32_16x16x32_bf16 v[144:147], a[140:143], v[188:191], v[144:147]\n" \
"v_exp_f32 v139, v139\n" \
"v_mfma_f32_16x16x32_bf16 v[148:151], a[156:159], v[188:191], 0\n" \
"ds_read_b128 a[208:211], v242 offset:4096\n" \
"v_cvt_pk_bf16_f32 v133, v138, v139\n" \
"v_mfma_f32_16x16x32_bf16 v[148:151], a[152:155], v[184:187], v[148:151]\n" \
"v_exp_f32 v140, v140\n" \
"v_mfma_f32_16x16x32_bf16 v[148:151], a[148:151], v[180:183], v[148:151]\n" \
"ds_read_b128 a[212:215], v242 offset:5120\n" \
"ds_read_b128 a[244:247], v242 offset:13312\n" \
"v_mfma_f32_16x16x32_bf16 v[148:151], a[144:147], v[176:179], v[148:151]\n" \
"v_exp_f32 v141, v141\n" \
"v_mfma_f32_16x16x32_bf16 v[152:155], a[160:163], v[176:179], 0\n" \
"buffer_load_dwordx4 v248, s[32:35], s89 offen offset:1024 sc1 lds\n" \
"v_mfma_f32_16x16x32_bf16 v[152:155], a[164:167], v[180:183], v[152:155]\n" \
"v_exp_f32 v142, v142\n" \
"v_mfma_f32_16x16x32_bf16 v[152:155], a[168:171], v[184:187], v[152:155]\n" \
"ds_read_b128 a[248:251], v242 offset:14336\n" \
"v_cvt_pk_bf16_f32 v134, v140, v141\n" \
"v_mfma_f32_16x16x32_bf16 v[152:155], a[172:175], v[188:191], v[152:155]\n" \
"v_exp_f32 v143, v143\n" \
"v_mfma_f32_16x16x32_bf16 v[156:159], a[188:191], v[188:191], 0\n" \
"ds_read_b128 a[216:219], v242 offset:6144\n" \
"v_cvt_pk_bf16_f32 v135, v142, v143\n" \
"v_mfma_f32_16x16x32_bf16 v[156:159], a[184:187], v[184:187], v[156:159]\n" \
"ds_read_b128 a[128:131], v240 offset:0\n" \
"ds_read_b128 a[132:135], v240 offset:1024\n" \
"v_mfma_f32_16x16x32_bf16 v[156:159], a[180:183], v[180:183], v[156:159]\n" \
"v_exp_f32 v144, v144\n" \
"v_mfma_f32_16x16x32_bf16 v[156:159], a[176:179], v[176:179], v[156:159]\n" \
"buffer_load_dwordx4 v248, s[32:35], s89 offen offset:2048 sc1 lds\n" \
"v_smfmac_f32_16x16x64_bf16 a[40:43], a[48:51], v[128:135], v241\n" \
"s_waitcnt lgkmcnt(12)\n" \
"ds_read_b128 a[220:223], v242 offset:7168\n" \
"ds_read_b128 a[136:139], v240 offset:2048\n" \
"v_mfma_f32_16x16x32_bf16 v[64:67], a[224:227], v[132:135], v[64:67]\n" \
"v_exp_f32 v145, v145\n" \
"v_mfma_f32_16x16x32_bf16 v[64:67], a[192:195], v[128:131], v[64:67]\n" \
"v_exp_f32 v146, v146\n" \
"v_mfma_f32_16x16x32_bf16 v[68:71], a[196:199], v[128:131], v[68:71]\n" \
".Lst1_2_%=:\n" \
"s_cbranch_execz .Lst1_6_%=\n" \
"ds_read_b128 a[252:255], v242 offset:15360\n" \
"v_cvt_pk_bf16_f32 v144, v144, v145\n" \
"v_mfma_f32_16x16x32_bf16 v[68:71], a[228:231], v[132:135], v[68:71]\n" \
"s_waitcnt lgkmcnt(11)\n" \
"v_exp_f32 v147, v147\n" \
"v_mfma_f32_16x16x32_bf16 v[72:75], a[232:235], v[132:135], v[72:75]\n" \
"buffer_load_dwordx4 v248, s[32:35], s89 offen offset:3072 sc1 lds\n" \
"v_mfma_f32_16x16x32_bf16 v[72:75], a[200:203], v[128:131], v[72:75]\n" \
"s_add_u32 s20, s20, 0x4000\n" \
"v_exp_f32 v148, v148\n" \
"v_mfma_f32_16x16x32_bf16 v[76:79], a[204:207], v[128:131], v[76:79]\n" \
"s_and_b32 s20, s20, 0xffff\n" \
"ds_read_b128 a[140:143], v240 offset:3072\n" \
"v_cvt_pk_bf16_f32 v145, v146, v147\n" \
"v_mfma_f32_16x16x32_bf16 v[76:79], a[236:239], v[132:135], v[76:79]\n" \
"s_waitcnt lgkmcnt(8)\n" \
"v_exp_f32 v149, v149\n" \
"v_mfma_f32_16x16x32_bf16 v[80:83], a[240:243], v[132:135], v[80:83]\n" \
"s_bitset1_b32 s20, 16\n" \
"ds_read_b128 a[156:159], v240 offset:7168\n" \
"v_cvt_pk_bf16_f32 v146, v148, v149\n" \
"v_mfma_f32_16x16x32_bf16 v[80:83], a[208:211], v[128:131], v[80:83]\n" \
"v_exp_f32 v150, v150\n" \
"v_mfma_f32_16x16x32_bf16 v[84:87], a[212:215], v[128:131], v[84:87]\n" \
"s_sub_u32 s95, s95, 1\n" \
"ds_read_b128 a[152:155], v240 offset:6144\n" \
"ds_read_b128 a[148:151], v240 offset:5120\n" \
"v_mfma_f32_16x16x32_bf16 v[84:87], a[244:247], v[132:135], v[84:87]\n" \
"s_waitcnt lgkmcnt(4)\n" \
"v_exp_f32 v151, v151\n" \
"v_mfma_f32_16x16x32_bf16 v[88:91], a[248:251], v[132:135], v[88:91]\n" \
"ds_read_b128 a[144:147], v240 offset:4096\n" \
"v_cvt_pk_bf16_f32 v147, v150, v151\n" \
"v_mfma_f32_16x16x32_bf16 v[88:91], a[216:219], v[128:131], v[88:91]\n" \
"v_exp_f32 v152, v152\n" \
"v_mfma_f32_16x16x32_bf16 v[92:95], a[220:223], v[128:131], v[92:95]\n" \
"s_waitcnt lgkmcnt(4)\n" \
"ds_read_b128 a[160:163], v240 offset:8192\n" \
"ds_read_b128 a[164:167], v240 offset:9216\n" \
"ds_read_b128 a[168:171], v240 offset:10240\n" \
"ds_read_b128 a[172:175], v240 offset:11264\n" \
"v_exp_f32 v153, v153\n" \
"v_mfma_f32_16x16x32_bf16 v[92:95], a[252:255], v[132:135], v[92:95]\n" \
"s_cbranch_scc0 .Lloop3_%=\n" \
".Le1_%=:\n" \
".Lst1_3_%=:\n" \
"s_cbranch_execz .Lst1_7_%=\n" \
  \
"v_mfma_f32_16x16x32_bf16 v[128:131], a[128:131], v[160:163], 0\n" \
"s_add_u32 s86, s86, 0x4000\n" \
"v_and_b32 v255, 3, v251\n" \
"v_lshlrev_b32 v255, 2, v255\n" \
"v_mfma_f32_16x16x32_bf16 v[128:131], a[132:135], v[164:167], v[128:131]\n" \
"s_bitset0_b32 s86, 16\n" \
"v_exp_f32 v154, v154\n" \
"v_mfma_f32_16x16x32_bf16 v[128:131], a[136:139], v[168:171], v[128:131]\n" \
"buffer_load_dword v247, v255, s[32:35], s31 offen sc1\n" \
"v_add_u32 v242, 0x10000, v240\n" \
"v_mfma_f32_16x16x32_bf16 v[128:131], a[140:143], v[172:175], v[128:131]\n" \
"s_waitcnt lgkmcnt(4)\n" \
"v_exp_f32 v155, v155\n" \
"v_mfma_f32_16x16x32_bf16 v[132:135], a[156:159], v[172:175], 0\n" \
"s_mov_b32 m0, s19\n" \
"v_cvt_pk_bf16_f32 v148, v152, v153\n" \
"v_cvt_pk_bf16_f32 v149, v154, v155\n" \
"v_mfma_f32_16x16x32_bf16 v[132:135], a[152:155], v[168:171], v[132:135]\n" \
"ds_read_b128 a[188:191], v240 offset:15360\n" \
"ds_read_b128 a[184:187], v240 offset:14336\n" \
"ds_read_b128 a[180:183], v240 offset:13312\n" \
"ds_read_b128 a[176:179], v240 offset:12288\n" \
"v_exp_f32 v156, v156\n" \
"v_mfma_f32_16x16x32_bf16 v[132:135], a[148:151], v[164:167], v[132:135]\n" \
"buffer_load_dwordx4 v248, s[32:35], s88 offen offset:0 sc1 lds\n" \
"v_mfma_f32_16x16x32_bf16 v[132:135], a[144:147], v[160:163], v[132:135]\n" \
"s_waitcnt lgkmcnt(4)\n" \
"v_exp_f32 v157, v157\n" \
"v_mfma_f32_16x16x32_bf16 v[136:139], a[160:163], v[160:163], 0\n" \
"v_cvt_pk_bf16_f32 v150, v156, v157\n" \
"v_mfma_f32_16x16x32_bf16 v[136:139], a[164:167], v[164:167], v[136:139]\n" \
"v_exp_f32 v158, v158\n" \
"v_mfma_f32_16x16x32_bf16 v[136:139], a[168:171], v[168:171], v[136:139]\n" \
"buffer_load_dwordx4 v248, s[32:35], s88 offen offset:1024 sc1 lds\n" \
"v_mfma_f32_16x16x32_bf16 v[136:139], a[172:175], v[172:175], v[136:139]\n" \
"s_waitcnt lgkmcnt(0)\n" \
"v_exp_f32 v159, v159\n" \
"v_mfma_f32_16x16x32_bf16 v[140:143], a[188:191], v[172:175], 0\n" \
"v_add_u32 v240, s86, v248\n" \
"v_cvt_pk_bf16_f32 v151, v158, v159\n" \
"v_mfma_f32_16x16x32_bf16 v[140:143], a[184:187], v[168:171], v[140:143]\n" \
"buffer_load_dwordx4 v248, s[32:35], s88 offen offset:2048 sc1 lds\n" \
"v_mfma_f32_16x16x32_bf16 v[140:143], a[180:183], v[164:167], v[140:143]\n" \
"v_exp_f32 v128, v128\n" \
"v_mfma_f32_16x16x32_bf16 v[140:143], a[176:179], v[160:163], v[140:143]\n" \
"buffer_load_dwordx4 v248, s[32:35], s88 offen offset:3072 sc1 lds\n" \
"v_smfmac_f32_16x16x64_bf16 a[44:47], a[48:51], v[144:151], v241\n" \
"s_add_u32 s89, s88, 589824\n" \
"s_add_u32 s88, s88, s27\n" \
"s_add_u32 s19, s19, 0x4000\n" \
"v_mfma_f32_16x16x32_bf16 v[96:99], a[224:227], v[148:151], v[96:99]\n" \
"s_bitset0_b32 s19, 16\n" \
"v_exp_f32 v129, v129\n" \
"v_mfma_f32_16x16x32_bf16 v[96:99], a[192:195], v[144:147], v[96:99]\n" \
"s_cmp_eq_u32 s88, s16\n" \
"v_exp_f32 v130, v130\n" \
"v_mfma_f32_16x16x32_bf16 v[100:103], a[196:199], v[144:147], v[100:103]\n" \
"s_cmov_b32 s88, s21\n" \
"ds_read_b128 a[224:227], v242 offset:8192\n" \
"v_cvt_pk_bf16_f32 v128, v128, v129\n" \
"v_mfma_f32_16x16x32_bf16 v[100:103], a[228:231], v[148:151], v[100:103]\n" \
"s_cmov_b32 s27, s91\n" \
"v_exp_f32 v131, v131\n" \
"v_mfma_f32_16x16x32_bf16 v[104:107], a[232:235], v[148:151], v[104:107]\n" \
"s_cmov_b32 s16, s77\n" \
"ds_read_b128 a[192:195], v242 offset:0\n" \
"v_cvt_pk_bf16_f32 v129, v130, v131\n" \
"v_mfma_f32_16x16x32_bf16 v[104:107], a[200:203], v[144:147], v[104:107]\n" \
"s_cmov_b32 s21, s92\n" \
"v_exp_f32 v132, v132\n" \
"v_mfma_f32_16x16x32_bf16 v[108:111], a[204:207], v[144:147], v[108:111]\n" \
"s_cmovk_i32 s91, 0x4000\n" \
"ds_read_b128 a[196:199], v242 offset:1024\n" \
"ds_read_b128 a[228:231], v242 offset:9216\n" \
"v_mfma_f32_16x16x32_bf16 v[108:111], a[236:239], v[148:151], v[108:111]\n" \
"v_exp_f32 v133, v133\n" \
"v_mfma_f32_16x16x32_bf16 v[112:115], a[240:243], v[148:151], v[112:115]\n" \
"ds_read_b128 a[232:235], v242 offset:10240\n" \
"v_cvt_pk_bf16_f32 v130, v132, v133\n" \
"v_mfma_f32_16x16x32_bf16 v[112:115], a[208:211], v[144:147], v[112:115]\n" \
"v_exp_f32 v134, v134\n" \
"v_mfma_f32_16x16x32_bf16 v[116:119], a[212:215], v[144:147], v[116:119]\n" \
"ds_read_b128 a[200:203], v242 offset:2048\n" \
"ds_read_b128 a[204:207], v242 offset:3072\n" \
"v_mfma_f32_16x16x32_bf16 v[116:119], a[244:247], v[148:151], v[116:119]\n" \
"v_exp_f32 v135, v135\n" \
"v_mfma_f32_16x16x32_bf16 v[120:123], a[248:251], v[148:151], v[120:123]\n" \
"ds_read_b128 a[236:239], v242 offset:11264\n" \
"v_cvt_pk_bf16_f32 v131, v134, v135\n" \
"v_mfma_f32_16x16x32_bf16 v[120:123], a[216:219], v[144:147], v[120:123]\n" \
"v_exp_f32 v136, v136\n" \
"v_mfma_f32_16x16x32_bf16 v[124:127], a[220:223], v[144:147], v[124:127]\n" \
"s_waitcnt vmcnt(4)\n" \
"ds_read_b128 a[240:243], v242 offset:12288\n" \
"v_cmp_eq_u32 vcc, 0, v247\n" \
"v_mfma_f32_16x16x32_bf16 v[124:127], a[252:255], v[148:151], v[124:127]\n" \
"s_waitcnt vmcnt(13)\n" \
"v_exp_f32 v137, v137\n" \
"v_mfma_f32_16x16x32_bf16 v[144:147], a[128:131], v[176:179], 0\n" \
"s_barrier\n" \
"ds_read_b128 a[208:211], v242 offset:4096\n" \
"v_cvt_pk_bf16_f32 v132, v136, v137\n" \
"v_mfma_f32_16x16x32_bf16 v[144:147], a[132:135], v[180:183], v[144:147]\n" \
"s_mov_b32 m0, s20\n" \
"v_exp_f32 v138, v138\n" \
"v_mfma_f32_16x16x32_bf16 v[144:147], a[136:139], v[184:187], v[144:147]\n" \
"buffer_load_dwordx4 v248, s[32:35], s89 offen offset:0 sc1 lds\n" \
"v_mfma_f32_16x16x32_bf16 v[144:147], a[140:143], v[188:191], v[144:147]\n" \
"v_exp_f32 v139, v139\n" \
"v_mfma_f32_16x16x32_bf16 v[148:151], a[156:159], v[188:191], 0\n" \
"s_cbranch_vccnz .Lspl_%=\n" \
".Lsbl_%=:\n" \
"v_cvt_pk_bf16_f32 v133, v138, v139\n" \
"v_mfma_f32_16x16x32_bf16 v[148:151], a[152:155], v[184:187], v[148:151]\n" \
"s_add_u32 s38, s66, 1\n" \
"v_exp_f32 v140, v140\n" \
"v_mfma_f32_16x16x32_bf16 v[148:151], a[148:151], v[180:183], v[148:151]\n" \
"s_and_b32 s38, s38, 3\n" \
"ds_read_b128 a[212:215], v242 offset:5120\n" \
"ds_read_b128 a[244:247], v242 offset:13312\n" \
"v_mfma_f32_16x16x32_bf16 v[148:151], a[144:147], v[176:179], v[148:151]\n" \
"s_lshl_b32 s38, s38, 13\n" \
"s_add_u32 s38, s38, s29\n" \
"v_exp_f32 v141, v141\n" \
"v_mfma_f32_16x16x32_bf16 v[152:155], a[160:163], v[176:179], 0\n" \
"buffer_load_dwordx4 v248, s[32:35], s89 offen offset:1024 sc1 lds\n" \
"buffer_load_dwordx4 v[32:35], v248, s[32:35], s38 offen offset:0 sc1\n" \
"v_mfma_f32_16x16x32_bf16 v[152:155], a[164:167], v[180:183], v[152:155]\n" \
"v_exp_f32 v142, v142\n" \
"v_mfma_f32_16x16x32_bf16 v[152:155], a[168:171], v[184:187], v[152:155]\n" \
"ds_read_b128 a[248:251], v242 offset:14336\n" \
"v_cvt_pk_bf16_f32 v134, v140, v141\n" \
"v_mfma_f32_16x16x32_bf16 v[152:155], a[172:175], v[188:191], v[152:155]\n" \
"v_exp_f32 v143, v143\n" \
"v_mfma_f32_16x16x32_bf16 v[156:159], a[188:191], v[188:191], 0\n" \
"ds_read_b128 a[216:219], v242 offset:6144\n" \
"v_cvt_pk_bf16_f32 v135, v142, v143\n" \
"v_mfma_f32_16x16x32_bf16 v[156:159], a[184:187], v[184:187], v[156:159]\n" \
"buffer_load_dwordx4 v[36:39], v248, s[32:35], s38 offen offset:1024 sc1\n" \
"v_mfma_f32_16x16x32_bf16 v[156:159], a[180:183], v[180:183], v[156:159]\n" \
"v_exp_f32 v144, v144\n" \
"v_mfma_f32_16x16x32_bf16 v[156:159], a[176:179], v[176:179], v[156:159]\n" \
"buffer_load_dwordx4 v248, s[32:35], s89 offen offset:2048 sc1 lds\n" \
"v_smfmac_f32_16x16x64_bf16 a[40:43], a[48:51], v[128:135], v241\n" \
"s_waitcnt lgkmcnt(10)\n" \
"ds_read_b128 a[128:131], v240 offset:0\n" \
"ds_read_b128 a[132:135], v240 offset:1024\n" \
"v_mfma_f32_16x16x32_bf16 v[64:67], a[224:227], v[132:135], v[64:67]\n" \
"v_exp_f32 v145, v145\n" \
"v_mfma_f32_16x16x32_bf16 v[64:67], a[192:195], v[128:131], v[64:67]\n" \
"v_exp_f32 v146, v146\n" \
"v_mfma_f32_16x16x32_bf16 v[68:71], a[196:199], v[128:131], v[68:71]\n" \
"ds_read_b128 a[220:223], v242 offset:7168\n" \
"v_cvt_pk_bf16_f32 v144, v144, v145\n" \
"v_mfma_f32_16x16x32_bf16 v[68:71], a[228:231], v[132:135], v[68:71]\n" \
"s_waitcnt lgkmcnt(9)\n" \
"v_exp_f32 v147, v147\n" \
"v_mfma_f32_16x16x32_bf16 v[72:75], a[232:235], v[132:135], v[72:75]\n" \
"buffer_load_dwordx4 v248, s[32:35], s89 offen offset:3072 sc1 lds\n" \
"v_mfma_f32_16x16x32_bf16 v[72:75], a[200:203], v[128:131], v[72:75]\n" \
"s_add_u32 s20, s20, 0x4000\n" \
"ds_read_b128 a[136:139], v240 offset:2048\n" \
"ds_read_b128 a[252:255], v242 offset:15360\n" \
"v_exp_f32 v148, v148\n" \
"v_mfma_f32_16x16x32_bf16 v[76:79], a[204:207], v[128:131], v[76:79]\n" \
"s_and_b32 s20, s20, 0xffff\n" \
"buffer_load_dwordx4 v[40:43], v248, s[32:35], s38 offen offset:2048 sc1\n" \
"v_cvt_pk_bf16_f32 v145, v146, v147\n" \
"v_mfma_f32_16x16x32_bf16 v[76:79], a[236:239], v[132:135], v[76:79]\n" \
"s_waitcnt lgkmcnt(7)\n" \
"v_exp_f32 v149, v149\n" \
"v_mfma_f32_16x16x32_bf16 v[80:83], a[240:243], v[132:135], v[80:83]\n" \
"s_bitset1_b32 s20, 16\n" \
"ds_read_b128 a[140:143], v240 offset:3072\n" \
"v_cvt_pk_bf16_f32 v146, v148, v149\n" \
"v_mfma_f32_16x16x32_bf16 v[80:83], a[208:211], v[128:131], v[80:83]\n" \
"v_exp_f32 v150, v150\n" \
"v_mfma_f32_16x16x32_bf16 v[84:87], a[212:215], v[128:131], v[84:87]\n" \
"ds_read_b128 a[156:159], v240 offset:7168\n" \
"ds_read_b128 a[152:155], v240 offset:6144\n" \
"v_mfma_f32_16x16x32_bf16 v[84:87], a[244:247], v[132:135], v[84:87]\n" \
"s_waitcnt lgkmcnt(3)\n" \
"v_exp_f32 v151, v151\n" \
"v_mfma_f32_16x16x32_bf16 v[88:91], a[248:251], v[132:135], v[88:91]\n" \
"buffer_load_dwordx4 v[44:47], v248, s[32:35], s38 offen offset:3072 sc1\n" \
"v_cvt_pk_bf16_f32 v147, v150, v151\n" \
"v_mfma_f32_16x16x32_bf16 v[88:91], a[216:219], v[128:131], v[88:91]\n" \
"ds_read_b128 a[148:151], v240 offset:5120\n" \
"ds_read_b128 a[144:147], v240 offset:4096\n" \
"v_exp_f32 v152, v152\n" \
"v_mfma_f32_16x16x32_bf16 v[92:95], a[220:223], v[128:131], v[92:95]\n" \
"ds_read_b128 a[160:163], v240 offset:8192\n" \
"ds_read_b128 a[164:167], v240 offset:9216\n" \
"v_mfma_f32_16x16x32_bf16 v[92:95], a[252:255], v[132:135], v[92:95]\n" \
"s_waitcnt lgkmcnt(6)\n" \
"v_exp_f32 v153, v153\n" \
"v_mfma_f32_16x16x32_bf16 v[128:131], a[128:131], v[160:163], 0\n" \
"s_add_u32 s86, s86, 0x4000\n" \
"ds_read_b128 a[168:171], v240 offset:10240\n" \
"v_cvt_pk_bf16_f32 v148, v152, v153\n" \
"v_mfma_f32_16x16x32_bf16 v[128:131], a[132:135], v[164:167], v[128:131]\n" \
"s_bitset0_b32 s86, 16\n" \
"ds_read_b128 a[172:175], v240 offset:11264\n" \
"v_exp_f32 v154, v154\n" \
"v_mfma_f32_16x16x32_bf16 v[128:131], a[136:139], v[168:171], v[128:131]\n" \
"s_lshl1_add_u32 s22, s66, 0\n" \
"ds_read_b128 a[188:191], v240 offset:15360\n" \
"v_add_u32 v242, 0x10000, v240\n" \
"v_mfma_f32_16x16x32_bf16 v[128:131], a[140:143], v[172:175], v[128:131]\n" \
"s_waitcnt lgkmcnt(5)\n" \
"v_exp_f32 v155, v155\n" \
"v_mfma_f32_16x16x32_bf16 v[132:135], a[156:159], v[172:175], 0\n" \
"s_and_b32 s22, s22, 7\n" \
"ds_read_b128 a[184:187], v240 offset:14336\n" \
"v_cvt_pk_bf16_f32 v149, v154, v155\n" \
"v_mfma_f32_16x16x32_bf16 v[132:135], a[152:155], v[168:171], v[132:135]\n" \
"s_lshl_b32 s22, s22, 16\n" \
"ds_read_b128 a[180:183], v240 offset:13312\n" \
"ds_read_b128 a[176:179], v240 offset:12288\n" \
"v_exp_f32 v156, v156\n" \
"v_mfma_f32_16x16x32_bf16 v[132:135], a[148:151], v[164:167], v[132:135]\n" \
"s_add_u32 s22, s22, s17\n" \
"s_add_u32 s38, s38, 4096\n" \
"buffer_load_dwordx4 v[48:51], v248, s[32:35], s38 offen offset:0 sc1\n" \
"v_mfma_f32_16x16x32_bf16 v[132:135], a[144:147], v[160:163], v[132:135]\n" \
"s_waitcnt lgkmcnt(4)\n" \
"v_exp_f32 v157, v157\n" \
"v_mfma_f32_16x16x32_bf16 v[136:139], a[160:163], v[160:163], 0\n" \
"s_mov_b32 m0, s19\n" \
"v_cvt_pk_bf16_f32 v150, v156, v157\n" \
"v_mfma_f32_16x16x32_bf16 v[136:139], a[164:167], v[164:167], v[136:139]\n" \
"v_exp_f32 v158, v158\n" \
"v_mfma_f32_16x16x32_bf16 v[136:139], a[168:171], v[168:171], v[136:139]\n" \
"buffer_load_dwordx4 v237, s[48:51], s22 offen offset:0 lds\n" \
"v_mfma_f32_16x16x32_bf16 v[136:139], a[172:175], v[172:175], v[136:139]\n" \
"s_waitcnt lgkmcnt(0)\n" \
"v_exp_f32 v159, v159\n" \
"v_mfma_f32_16x16x32_bf16 v[140:143], a[188:191], v[172:175], 0\n" \
"s_add_u32 s22, s22, 7168\n" \
"buffer_load_dwordx4 v237, s[48:51], s22 offen offset:1024 lds\n" \
"v_mfma_f32_16x16x32_bf16 v[140:143], a[184:187], v[168:171], v[140:143]\n" \
"s_add_u32 s22, s22, 7168\n" \
"buffer_load_dwordx4 v237, s[48:51], s22 offen offset:2048 lds\n" \
"v_mfma_f32_16x16x32_bf16 v[140:143], a[180:183], v[164:167], v[140:143]\n" \
"s_add_u32 s22, s22, 7168\n" \
"v_exp_f32 v128, v128\n" \
"v_cvt_pk_bf16_f32 v151, v158, v159\n" \
"v_mfma_f32_16x16x32_bf16 v[140:143], a[176:179], v[160:163], v[140:143]\n" \
"buffer_load_dwordx4 v237, s[48:51], s22 offen offset:3072 lds\n" \
"buffer_load_dwordx4 v[52:55], v248, s[32:35], s38 offen offset:1024 sc1\n" \
"v_smfmac_f32_16x16x64_bf16 a[44:47], a[48:51], v[144:151], v241\n" \
"s_add_u32 s19, s19, 0x4000\n" \
"s_bitset0_b32 s19, 16\n" \
"v_add_u32 v240, s86, v248\n" \
"v_mfma_f32_16x16x32_bf16 v[96:99], a[224:227], v[148:151], v[96:99]\n" \
"v_exp_f32 v129, v129\n" \
"v_mfma_f32_16x16x32_bf16 v[96:99], a[192:195], v[144:147], v[96:99]\n" \
"v_exp_f32 v130, v130\n" \
"v_mfma_f32_16x16x32_bf16 v[100:103], a[196:199], v[144:147], v[100:103]\n" \
"ds_read_b128 a[224:227], v242 offset:8192\n" \
"v_cvt_pk_bf16_f32 v128, v128, v129\n" \
"v_mfma_f32_16x16x32_bf16 v[100:103], a[228:231], v[148:151], v[100:103]\n" \
"v_exp_f32 v131, v131\n" \
"v_mfma_f32_16x16x32_bf16 v[104:107], a[232:235], v[148:151], v[104:107]\n" \
"buffer_load_dwordx4 v[56:59], v248, s[32:35], s38 offen offset:2048 sc1\n" \
"v_cvt_pk_bf16_f32 v129, v130, v131\n" \
"v_mfma_f32_16x16x32_bf16 v[104:107], a[200:203], v[144:147], v[104:107]\n" \
"v_exp_f32 v132, v132\n" \
"v_mfma_f32_16x16x32_bf16 v[108:111], a[204:207], v[144:147], v[108:111]\n" \
"ds_read_b128 a[192:195], v242 offset:0\n" \
"ds_read_b128 a[196:199], v242 offset:1024\n" \
"v_mfma_f32_16x16x32_bf16 v[108:111], a[236:239], v[148:151], v[108:111]\n" \
"v_exp_f32 v133, v133\n" \
"v_mfma_f32_16x16x32_bf16 v[112:115], a[240:243], v[148:151], v[112:115]\n" \
"ds_read_b128 a[228:231], v242 offset:9216\n" \
"v_cvt_pk_bf16_f32 v130, v132, v133\n" \
"v_mfma_f32_16x16x32_bf16 v[112:115], a[208:211], v[144:147], v[112:115]\n" \
"v_exp_f32 v134, v134\n" \
"v_mfma_f32_16x16x32_bf16 v[116:119], a[212:215], v[144:147], v[116:119]\n" \
"ds_read_b128 a[232:235], v242 offset:10240\n" \
"ds_read_b128 a[200:203], v242 offset:2048\n" \
"v_mfma_f32_16x16x32_bf16 v[116:119], a[244:247], v[148:151], v[116:119]\n" \
"v_exp_f32 v135, v135\n" \
"v_mfma_f32_16x16x32_bf16 v[120:123], a[248:251], v[148:151], v[120:123]\n" \
"s_lshl1_add_u32 s22, s66, 2\n" \
"buffer_load_dwordx4 v[60:63], v248, s[32:35], s38 offen offset:3072 sc1\n" \
"v_cvt_pk_bf16_f32 v131, v134, v135\n" \
"v_mfma_f32_16x16x32_bf16 v[120:123], a[216:219], v[144:147], v[120:123]\n" \
"s_and_b32 s22, s22, 7\n" \
"v_exp_f32 v136, v136\n" \
"v_mfma_f32_16x16x32_bf16 v[124:127], a[220:223], v[144:147], v[124:127]\n" \
"s_lshl_b32 s22, s22, 16\n" \
"ds_read_b128 a[204:207], v242 offset:3072\n" \
"ds_read_b128 a[236:239], v242 offset:11264\n" \
"v_mfma_f32_16x16x32_bf16 v[124:127], a[252:255], v[148:151], v[124:127]\n" \
"s_waitcnt vmcnt(21)\n" \
"v_exp_f32 v137, v137\n" \
"v_mfma_f32_16x16x32_bf16 v[144:147], a[128:131], v[176:179], 0\n" \
"s_barrier\n" \
"ds_read_b128 a[240:243], v242 offset:12288\n" \
"v_cvt_pk_bf16_f32 v132, v136, v137\n" \
"v_mfma_f32_16x16x32_bf16 v[144:147], a[132:135], v[180:183], v[144:147]\n" \
"s_add_u32 s22, s22, s17\n" \
"v_exp_f32 v138, v138\n" \
"v_mfma_f32_16x16x32_bf16 v[144:147], a[136:139], v[184:187], v[144:147]\n" \
"s_mov_b32 m0, s20\n" \
"ds_read_b128 a[208:211], v242 offset:4096\n" \
"ds_read_b128 a[212:215], v242 offset:5120\n" \
"v_mfma_f32_16x16x32_bf16 v[144:147], a[140:143], v[188:191], v[144:147]\n" \
"s_add_u32 s38, s66, 2\n" \
"v_exp_f32 v139, v139\n" \
"v_mfma_f32_16x16x32_bf16 v[148:151], a[156:159], v[188:191], 0\n" \
"s_and_b32 s38, s38, 3\n" \
"buffer_load_dwordx4 v237, s[48:51], s22 offen offset:0 lds\n" \
"v_mfma_f32_16x16x32_bf16 v[148:151], a[152:155], v[184:187], v[148:151]\n" \
"s_add_u32 s22, s22, 7168\n" \
"s_lshl_b32 s38, s38, 13\n" \
"s_add_u32 s38, s38, s29\n" \
"v_exp_f32 v140, v140\n" \
"v_mfma_f32_16x16x32_bf16 v[148:151], a[148:151], v[180:183], v[148:151]\n" \
"buffer_load_dwordx4 a[64:67], v248, s[32:35], s38 offen offset:0 sc1\n" \
"v_cvt_pk_bf16_f32 v133, v138, v139\n" \
"v_mfma_f32_16x16x32_bf16 v[148:151], a[144:147], v[176:179], v[148:151]\n" \
"v_exp_f32 v141, v141\n" \
"v_mfma_f32_16x16x32_bf16 v[152:155], a[160:163], v[176:179], 0\n" \
"buffer_load_dwordx4 v237, s[48:51], s22 offen offset:1024 lds\n" \
"v_mfma_f32_16x16x32_bf16 v[152:155], a[164:167], v[180:183], v[152:155]\n" \
"s_add_u32 s22, s22, 7168\n" \
"v_exp_f32 v142, v142\n" \
"v_mfma_f32_16x16x32_bf16 v[152:155], a[168:171], v[184:187], v[152:155]\n" \
"ds_read_b128 a[244:247], v242 offset:13312\n" \
"v_cvt_pk_bf16_f32 v134, v140, v141\n" \
"v_mfma_f32_16x16x32_bf16 v[152:155], a[172:175], v[188:191], v[152:155]\n" \
"v_exp_f32 v143, v143\n" \
"v_mfma_f32_16x16x32_bf16 v[156:159], a[188:191], v[188:191], 0\n" \
"buffer_load_dwordx4 a[68:71], v248, s[32:35], s38 offen offset:1024 sc1\n" \
"v_cvt_pk_bf16_f32 v135, v142, v143\n" \
"v_mfma_f32_16x16x32_bf16 v[156:159], a[184:187], v[184:187], v[156:159]\n" \
"ds_read_b128 a[248:251], v242 offset:14336\n" \
"ds_read_b128 a[216:219], v242 offset:6144\n" \
"v_mfma_f32_16x16x32_bf16 v[156:159], a[180:183], v[180:183], v[156:159]\n" \
"v_exp_f32 v144, v144\n" \
"v_mfma_f32_16x16x32_bf16 v[156:159], a[176:179], v[176:179], v[156:159]\n" \
"buffer_load_dwordx4 v237, s[48:51], s22 offen offset:2048 lds\n" \
"v_smfmac_f32_16x16x64_bf16 a[40:43], a[48:51], v[128:135], v241\n" \
"s_waitcnt lgkmcnt(10)\n" \
"ds_read_b128 a[128:131], v240 offset:0\n" \
"ds_read_b128 a[132:135], v240 offset:1024\n" \
"v_mfma_f32_16x16x32_bf16 v[64:67], a[224:227], v[132:135], v[64:67]\n" \
"s_add_u32 s22, s22, 7168\n" \
"v_exp_f32 v145, v145\n" \
"v_mfma_f32_16x16x32_bf16 v[64:67], a[192:195], v[128:131], v[64:67]\n" \
"v_exp_f32 v146, v146\n" \
"v_mfma_f32_16x16x32_bf16 v[68:71], a[196:199], v[128:131], v[68:71]\n" \
"buffer_load_dwordx4 a[72:75], v248, s[32:35], s38 offen offset:2048 sc1\n" \
"v_cvt_pk_bf16_f32 v144, v144, v145\n" \
"v_mfma_f32_16x16x32_bf16 v[68:71], a[228:231], v[132:135], v[68:71]\n" \
"s_waitcnt lgkmcnt(8)\n" \
"v_exp_f32 v147, v147\n" \
"v_mfma_f32_16x16x32_bf16 v[72:75], a[232:235], v[132:135], v[72:75]\n" \
"buffer_load_dwordx4 v237, s[48:51], s22 offen offset:3072 lds\n" \
"v_mfma_f32_16x16x32_bf16 v[72:75], a[200:203], v[128:131], v[72:75]\n" \
"s_add_u32 s20, s20, 0x4000\n" \
"ds_read_b128 a[220:223], v242 offset:7168\n" \
"ds_read_b128 a[136:139], v240 offset:2048\n" \
"ds_read_b128 a[252:255], v242 offset:15360\n" \
"v_exp_f32 v148, v148\n" \
"v_mfma_f32_16x16x32_bf16 v[76:79], a[204:207], v[128:131], v[76:79]\n" \
"s_and_b32 s20, s20, 0xffff\n" \
"ds_read_b128 a[140:143], v240 offset:3072\n" \
"v_cvt_pk_bf16_f32 v145, v146, v147\n" \
"v_mfma_f32_16x16x32_bf16 v[76:79], a[236:239], v[132:135], v[76:79]\n" \
"s_waitcnt lgkmcnt(8)\n" \
"v_exp_f32 v149, v149\n" \
"v_mfma_f32_16x16x32_bf16 v[80:83], a[240:243], v[132:135], v[80:83]\n" \
"s_bitset1_b32 s20, 16\n" \
"ds_read_b128 a[156:159], v240 offset:7168\n" \
"v_cvt_pk_bf16_f32 v146, v148, v149\n" \
"v_mfma_f32_16x16x32_bf16 v[80:83], a[208:211], v[128:131], v[80:83]\n" \
"v_exp_f32 v150, v150\n" \
"v_mfma_f32_16x16x32_bf16 v[84:87], a[212:215], v[128:131], v[84:87]\n" \
"buffer_load_dwordx4 a[76:79], v248, s[32:35], s38 offen offset:3072 sc1\n" \
"v_mfma_f32_16x16x32_bf16 v[84:87], a[244:247], v[132:135], v[84:87]\n" \
"s_waitcnt lgkmcnt(2)\n" \
"v_exp_f32 v151, v151\n" \
"v_mfma_f32_16x16x32_bf16 v[88:91], a[248:251], v[132:135], v[88:91]\n" \
"ds_read_b128 a[152:155], v240 offset:6144\n" \
"v_cvt_pk_bf16_f32 v147, v150, v151\n" \
"v_mfma_f32_16x16x32_bf16 v[88:91], a[216:219], v[128:131], v[88:91]\n" \
"ds_read_b128 a[148:151], v240 offset:5120\n" \
"ds_read_b128 a[144:147], v240 offset:4096\n" \
"v_exp_f32 v152, v152\n" \
"v_mfma_f32_16x16x32_bf16 v[92:95], a[220:223], v[128:131], v[92:95]\n" \
"v_exp_f32 v153, v153\n" \
"v_mfma_f32_16x16x32_bf16 v[92:95], a[252:255], v[132:135], v[92:95]\n" \
"s_waitcnt lgkmcnt(4)\n" \
"ds_read_b128 a[160:163], v240 offset:8192\n" \
"ds_read_b128 a[164:167], v240 offset:9216\n" \
"ds_read_b128 a[168:171], v240 offset:10240\n" \
"ds_read_b128 a[172:175], v240 offset:11264\n" \
".Llast_%=:\n" \
".Lst1_4_%=:\n" \
"s_cbranch_execz .Lst1_8_%=\n" \
  \
"v_mfma_f32_16x16x32_bf16 v[128:131], a[128:131], v[160:163], 0\n" \
"s_add_u32 s86, s86, 0x4000\n" \
"ds_read_b128 a[188:191], v240 offset:15360\n" \
"v_cvt_pk_bf16_f32 v148, v152, v153\n" \
"v_mfma_f32_16x16x32_bf16 v[128:131], a[132:135], v[164:167], v[128:131]\n" \
"s_bitset0_b32 s86, 16\n" \
"v_exp_f32 v154, v154\n" \
"v_mfma_f32_16x16x32_bf16 v[128:131], a[136:139], v[168:171], v[128:131]\n" \
"s_lshl1_add_u32 s22, s66, 1\n" \
"ds_read_b128 a[184:187], v240 offset:14336\n" \
"v_add_u32 v242, 0x10000, v240\n" \
"v_mfma_f32_16x16x32_bf16 v[128:131], a[140:143], v[172:175], v[128:131]\n" \
"s_waitcnt lgkmcnt(6)\n" \
"v_exp_f32 v155, v155\n" \
"v_mfma_f32_16x16x32_bf16 v[132:135], a[156:159], v[172:175], 0\n" \
"s_and_b32 s22, s22, 7\n" \
"ds_read_b128 a[180:183], v240 offset:13312\n" \
"v_cvt_pk_bf16_f32 v149, v154, v155\n" \
"v_mfma_f32_16x16x32_bf16 v[132:135], a[152:155], v[168:171], v[132:135]\n" \
"s_lshl_b32 s22, s22, 16\n" \
"ds_read_b128 a[176:179], v240 offset:12288\n" \
"v_exp_f32 v156, v156\n" \
"v_mfma_f32_16x16x32_bf16 v[132:135], a[148:151], v[164:167], v[132:135]\n" \
"s_add_u32 s22, s22, s17\n" \
"s_mov_b32 m0, s19\n" \
"v_mfma_f32_16x16x32_bf16 v[132:135], a[144:147], v[160:163], v[132:135]\n" \
"s_waitcnt lgkmcnt(4)\n" \
"v_exp_f32 v157, v157\n" \
"v_mfma_f32_16x16x32_bf16 v[136:139], a[160:163], v[160:163], 0\n" \
"buffer_load_dwordx4 v237, s[48:51], s22 offen offset:0 lds\n" \
"v_mfma_f32_16x16x32_bf16 v[136:139], a[164:167], v[164:167], v[136:139]\n" \
"s_add_u32 s22, s22, 7168\n" \
"v_exp_f32 v158, v158\n" \
"v_mfma_f32_16x16x32_bf16 v[136:139], a[168:171], v[168:171], v[136:139]\n" \
"buffer_load_dwordx4 v237, s[48:51], s22 offen offset:1024 lds\n" \
"v_mfma_f32_16x16x32_bf16 v[136:139], a[172:175], v[172:175], v[136:139]\n" \
"s_waitcnt lgkmcnt(0)\n" \
"v_exp_f32 v159, v159\n" \
"v_mfma_f32_16x16x32_bf16 v[140:143], a[188:191], v[172:175], 0\n" \
"s_add_u32 s22, s22, 7168\n" \
"v_add_u32 v240, s86, v248\n" \
"v_cvt_pk_bf16_f32 v150, v156, v157\n" \
"v_mfma_f32_16x16x32_bf16 v[140:143], a[184:187], v[168:171], v[140:143]\n" \
"buffer_load_dwordx4 v237, s[48:51], s22 offen offset:2048 lds\n" \
"v_mfma_f32_16x16x32_bf16 v[140:143], a[180:183], v[164:167], v[140:143]\n" \
"s_add_u32 s22, s22, 7168\n" \
"v_exp_f32 v128, v128\n" \
"v_cvt_pk_bf16_f32 v151, v158, v159\n" \
"v_mfma_f32_16x16x32_bf16 v[140:143], a[176:179], v[160:163], v[140:143]\n" \
"buffer_load_dwordx4 v237, s[48:51], s22 offen offset:3072 lds\n" \
"v_smfmac_f32_16x16x64_bf16 a[44:47], a[48:51], v[144:151], v241\n" \
"s_add_u32 s19, s19, 0x4000\n" \
"s_bitset0_b32 s19, 16\n" \
"v_mfma_f32_16x16x32_bf16 v[96:99], a[224:227], v[148:151], v[96:99]\n" \
"s_add_u32 s38, s38, 4096\n" \
"v_exp_f32 v129, v129\n" \
"v_mfma_f32_16x16x32_bf16 v[96:99], a[192:195], v[144:147], v[96:99]\n" \
"v_exp_f32 v130, v130\n" \
"v_mfma_f32_16x16x32_bf16 v[100:103], a[196:199], v[144:147], v[100:103]\n" \
"ds_read_b128 a[224:227], v242 offset:8192\n" \
"v_cvt_pk_bf16_f32 v128, v128, v129\n" \
"v_mfma_f32_16x16x32_bf16 v[100:103], a[228:231], v[148:151], v[100:103]\n" \
"v_exp_f32 v131, v131\n" \
"v_mfma_f32_16x16x32_bf16 v[104:107], a[232:235], v[148:151], v[104:107]\n" \
"ds_read_b128 a[192:195], v242 offset:0\n" \
"buffer_load_dwordx4 a[80:83], v248, s[32:35], s38 offen offset:0 sc1\n" \
"v_cvt_pk_bf16_f32 v129, v130, v131\n" \
"v_mfma_f32_16x16x32_bf16 v[104:107], a[200:203], v[144:147], v[104:107]\n" \
"v_exp_f32 v132, v132\n" \
"v_mfma_f32_16x16x32_bf16 v[108:111], a[204:207], v[144:147], v[108:111]\n" \
"ds_read_b128 a[196:199], v242 offset:1024\n" \
"ds_read_b128 a[228:231], v242 offset:9216\n" \
"v_mfma_f32_16x16x32_bf16 v[108:111], a[236:239], v[148:151], v[108:111]\n" \
"v_exp_f32 v133, v133\n" \
"v_mfma_f32_16x16x32_bf16 v[112:115], a[240:243], v[148:151], v[112:115]\n" \
"ds_read_b128 a[232:235], v242 offset:10240\n" \
"v_cvt_pk_bf16_f32 v130, v132, v133\n" \
"v_mfma_f32_16x16x32_bf16 v[112:115], a[208:211], v[144:147], v[112:115]\n" \
"v_exp_f32 v134, v134\n" \
"v_mfma_f32_16x16x32_bf16 v[116:119], a[212:215], v[144:147], v[116:119]\n" \
"ds_read_b128 a[200:203], v242 offset:2048\n" \
"ds_read_b128 a[204:207], v242 offset:3072\n" \
"v_mfma_f32_16x16x32_bf16 v[116:119], a[244:247], v[148:151], v[116:119]\n" \
"v_exp_f32 v135, v135\n" \
"v_mfma_f32_16x16x32_bf16 v[120:123], a[248:251], v[148:151], v[120:123]\n" \
"s_lshl1_add_u32 s22, s66, 3\n" \
"ds_read_b128 a[236:239], v242 offset:11264\n" \
"buffer_load_dwordx4 a[84:87], v248, s[32:35], s38 offen offset:1024 sc1\n" \
"v_cvt_pk_bf16_f32 v131, v134, v135\n" \
"v_mfma_f32_16x16x32_bf16 v[120:123], a[216:219], v[144:147], v[120:123]\n" \
"s_and_b32 s22, s22, 7\n" \
"v_exp_f32 v136, v136\n" \
"v_mfma_f32_16x16x32_bf16 v[124:127], a[220:223], v[144:147], v[124:127]\n" \
"s_lshl_b32 s22, s22, 16\n" \
"s_add_u32 s22, s22, s17\n" \
"ds_read_b128 a[240:243], v242 offset:12288\n" \
"v_mfma_f32_16x16x32_bf16 v[124:127], a[252:255], v[148:151], v[124:127]\n" \
"s_waitcnt vmcnt(14)\n" \
"v_exp_f32 v137, v137\n" \
"v_mfma_f32_16x16x32_bf16 v[144:147], a[128:131], v[176:179], 0\n" \
"s_barrier\n" \
"ds_read_b128 a[208:211], v242 offset:4096\n" \
"v_cvt_pk_bf16_f32 v132, v136, v137\n" \
"v_mfma_f32_16x16x32_bf16 v[144:147], a[132:135], v[180:183], v[144:147]\n" \
"s_mov_b32 m0, s20\n" \
"v_exp_f32 v138, v138\n" \
"v_mfma_f32_16x16x32_bf16 v[144:147], a[136:139], v[184:187], v[144:147]\n" \
"buffer_load_dwordx4 v237, s[48:51], s22 offen offset:0 lds\n" \
"v_mfma_f32_16x16x32_bf16 v[144:147], a[140:143], v[188:191], v[144:147]\n" \
"s_add_u32 s22, s22, 7168\n" \
"v_exp_f32 v139, v139\n" \
"v_mfma_f32_16x16x32_bf16 v[148:151], a[156:159], v[188:191], 0\n" \
"ds_read_b128 a[212:215], v242 offset:5120\n" \
"buffer_load_dwordx4 a[88:91], v248, s[32:35], s38 offen offset:2048 sc1\n" \
"v_cvt_pk_bf16_f32 v133, v138, v139\n" \
"v_mfma_f32_16x16x32_bf16 v[148:151], a[152:155], v[184:187], v[148:151]\n" \
"v_exp_f32 v140, v140\n" \
"v_mfma_f32_16x16x32_bf16 v[148:151], a[148:151], v[180:183], v[148:151]\n" \
"ds_read_b128 a[244:247], v242 offset:13312\n" \
"ds_read_b128 a[248:251], v242 offset:14336\n" \
"v_mfma_f32_16x16x32_bf16 v[148:151], a[144:147], v[176:179], v[148:151]\n" \
"v_exp_f32 v141, v141\n" \
"v_mfma_f32_16x16x32_bf16 v[152:155], a[160:163], v[176:179], 0\n" \
"buffer_load_dwordx4 v237, s[48:51], s22 offen offset:1024 lds\n" \
"v_mfma_f32_16x16x32_bf16 v[152:155], a[164:167], v[180:183], v[152:155]\n" \
"s_add_u32 s22, s22, 7168\n" \
"v_exp_f32 v142, v142\n" \
"v_mfma_f32_16x16x32_bf16 v[152:155], a[168:171], v[184:187], v[152:155]\n" \
"ds_read_b128 a[216:219], v242 offset:6144\n" \
"v_cvt_pk_bf16_f32 v134, v140, v141\n" \
"v_mfma_f32_16x16x32_bf16 v[152:155], a[172:175], v[188:191], v[152:155]\n" \
"v_exp_f32 v143, v143\n" \
"v_mfma_f32_16x16x32_bf16 v[156:159], a[188:191], v[188:191], 0\n" \
"ds_read_b128 a[128:131], v240 offset:0\n" \
"buffer_load_dwordx4 a[92:95], v248, s[32:35], s38 offen offset:3072 sc1\n" \
"v_cvt_pk_bf16_f32 v135, v142, v143\n" \
"v_mfma_f32_16x16x32_bf16 v[156:159], a[184:187], v[184:187], v[156:159]\n" \
"s_add_u32 s38, s66, 3\n" \
"ds_read_b128 a[132:135], v240 offset:1024\n" \
"ds_read_b128 a[220:223], v242 offset:7168\n" \
"v_mfma_f32_16x16x32_bf16 v[156:159], a[180:183], v[180:183], v[156:159]\n" \
"s_and_b32 s38, s38, 3\n" \
"v_exp_f32 v144, v144\n" \
"v_mfma_f32_16x16x32_bf16 v[156:159], a[176:179], v[176:179], v[156:159]\n" \
"s_lshl_b32 s38, s38, 13\n" \
"buffer_load_dwordx4 v237, s[48:51], s22 offen offset:2048 lds\n" \
"v_smfmac_f32_16x16x64_bf16 a[40:43], a[48:51], v[128:135], v241\n" \
"s_waitcnt lgkmcnt(13)\n" \
"ds_read_b128 a[136:139], v240 offset:2048\n" \
"ds_read_b128 a[252:255], v242 offset:15360\n" \
"v_mfma_f32_16x16x32_bf16 v[64:67], a[224:227], v[132:135], v[64:67]\n" \
"s_add_u32 s22, s22, 7168\n" \
"v_exp_f32 v145, v145\n" \
"v_mfma_f32_16x16x32_bf16 v[64:67], a[192:195], v[128:131], v[64:67]\n" \
"s_add_u32 s38, s38, s29\n" \
"v_exp_f32 v146, v146\n" \
"v_mfma_f32_16x16x32_bf16 v[68:71], a[196:199], v[128:131], v[68:71]\n" \
"ds_read_b128 a[140:143], v240 offset:3072\n" \
"v_cvt_pk_bf16_f32 v144, v144, v145\n" \
"v_mfma_f32_16x16x32_bf16 v[68:71], a[228:231], v[132:135], v[68:71]\n" \
".Lst1_5_%=:\n" \
"s_cbranch_execz .Lst1_9_%=\n" \
"s_waitcnt lgkmcnt(12)\n" \
"buffer_load_dwordx4 a[96:99], v248, s[32:35], s38 offen offset:0 sc1\n" \
"v_exp_f32 v147, v147\n" \
"v_mfma_f32_16x16x32_bf16 v[72:75], a[232:235], v[132:135], v[72:75]\n" \
"buffer_load_dwordx4 v237, s[48:51], s22 offen offset:3072 lds\n" \
"v_mfma_f32_16x16x32_bf16 v[72:75], a[200:203], v[128:131], v[72:75]\n" \
"s_add_u32 s20, s20, 0x4000\n" \
"v_exp_f32 v148, v148\n" \
"v_mfma_f32_16x16x32_bf16 v[76:79], a[204:207], v[128:131], v[76:79]\n" \
"s_and_b32 s20, s20, 0xffff\n" \
"ds_read_b128 a[156:159], v240 offset:7168\n" \
"v_cvt_pk_bf16_f32 v145, v146, v147\n" \
"v_mfma_f32_16x16x32_bf16 v[76:79], a[236:239], v[132:135], v[76:79]\n" \
"s_waitcnt lgkmcnt(9)\n" \
"v_exp_f32 v149, v149\n" \
"v_mfma_f32_16x16x32_bf16 v[80:83], a[240:243], v[132:135], v[80:83]\n" \
"s_bitset1_b32 s20, 16\n" \
"ds_read_b128 a[152:155], v240 offset:6144\n" \
"v_cvt_pk_bf16_f32 v146, v148, v149\n" \
"v_mfma_f32_16x16x32_bf16 v[80:83], a[208:211], v[128:131], v[80:83]\n" \
"v_exp_f32 v150, v150\n" \
"v_mfma_f32_16x16x32_bf16 v[84:87], a[212:215], v[128:131], v[84:87]\n" \
"ds_read_b128 a[148:151], v240 offset:5120\n" \
"ds_read_b128 a[144:147], v240 offset:4096\n" \
"v_mfma_f32_16x16x32_bf16 v[84:87], a[244:247], v[132:135], v[84:87]\n" \
"s_waitcnt lgkmcnt(5)\n" \
"buffer_load_dwordx4 a[100:103], v248, s[32:35], s38 offen offset:1024 sc1\n" \
"v_exp_f32 v151, v151\n" \
"v_mfma_f32_16x16x32_bf16 v[88:91], a[248:251], v[132:135], v[88:91]\n" \
"ds_read_b128 a[160:163], v240 offset:8192\n" \
"v_cvt_pk_bf16_f32 v147, v150, v151\n" \
"v_mfma_f32_16x16x32_bf16 v[88:91], a[216:219], v[128:131], v[88:91]\n" \
"v_exp_f32 v152, v152\n" \
"v_mfma_f32_16x16x32_bf16 v[92:95], a[220:223], v[128:131], v[92:95]\n" \
"ds_read_b128 a[164:167], v240 offset:9216\n" \
"ds_read_b128 a[168:171], v240 offset:10240\n" \
"v_mfma_f32_16x16x32_bf16 v[92:95], a[252:255], v[132:135], v[92:95]\n" \
"s_waitcnt lgkmcnt(7)\n" \
"v_exp_f32 v153, v153\n" \
"v_mfma_f32_16x16x32_bf16 v[128:131], a[128:131], v[160:163], 0\n" \
"s_lshl1_add_u32 s22, s66, 4\n" \
"ds_read_b128 a[172:175], v240 offset:11264\n" \
"v_cvt_pk_bf16_f32 v148, v152, v153\n" \
"v_mfma_f32_16x16x32_bf16 v[128:131], a[132:135], v[164:167], v[128:131]\n" \
"s_and_b32 s22, s22, 7\n" \
"v_exp_f32 v154, v154\n" \
"v_mfma_f32_16x16x32_bf16 v[128:131], a[136:139], v[168:171], v[128:131]\n" \
"s_lshl_b32 s22, s22, 16\n" \
"ds_read_b128 a[188:191], v240 offset:15360\n" \
"v_add_u32 v242, 0x10000, v240\n" \
"v_mfma_f32_16x16x32_bf16 v[128:131], a[140:143], v[172:175], v[128:131]\n" \
"s_waitcnt lgkmcnt(5)\n" \
"buffer_load_dwordx4 a[104:107], v248, s[32:35], s38 offen offset:2048 sc1\n" \
"v_exp_f32 v155, v155\n" \
"v_mfma_f32_16x16x32_bf16 v[132:135], a[156:159], v[172:175], 0\n" \
"s_add_u32 s22, s22, s17\n" \
"ds_read_b128 a[184:187], v240 offset:14336\n" \
"v_cvt_pk_bf16_f32 v149, v154, v155\n" \
"v_mfma_f32_16x16x32_bf16 v[132:135], a[152:155], v[168:171], v[132:135]\n" \
"s_mov_b32 m0, s19\n" \
"ds_read_b128 a[180:183], v240 offset:13312\n" \
"ds_read_b128 a[176:179], v240 offset:12288\n" \
"v_exp_f32 v156, v156\n" \
"v_mfma_f32_16x16x32_bf16 v[132:135], a[148:151], v[164:167], v[132:135]\n" \
"buffer_load_dwordx4 v237, s[48:51], s22 offen offset:0 lds\n" \
"v_mfma_f32_16x16x32_bf16 v[132:135], a[144:147], v[160:163], v[132:135]\n" \
"s_waitcnt lgkmcnt(4)\n" \
"v_exp_f32 v157, v157\n" \
"v_mfma_f32_16x16x32_bf16 v[136:139], a[160:163], v[160:163], 0\n" \
"s_add_u32 s22, s22, 7168\n" \
"buffer_load_dwordx4 a[108:111], v248, s[32:35], s38 offen offset:3072 sc1\n" \
"v_cvt_pk_bf16_f32 v150, v156, v157\n" \
"v_mfma_f32_16x16x32_bf16 v[136:139], a[164:167], v[164:167], v[136:139]\n" \
"v_exp_f32 v158, v158\n" \
"v_mfma_f32_16x16x32_bf16 v[136:139], a[168:171], v[168:171], v[136:139]\n" \
"buffer_load_dwordx4 v237, s[48:51], s22 offen offset:1024 lds\n" \
"v_mfma_f32_16x16x32_bf16 v[136:139], a[172:175], v[172:175], v[136:139]\n" \
"s_waitcnt lgkmcnt(0)\n" \
"v_exp_f32 v159, v159\n" \
"v_mfma_f32_16x16x32_bf16 v[140:143], a[188:191], v[172:175], 0\n" \
"s_add_u32 s22, s22, 7168\n" \
"v_cvt_pk_bf16_f32 v151, v158, v159\n" \
"v_mfma_f32_16x16x32_bf16 v[140:143], a[184:187], v[168:171], v[140:143]\n" \
"buffer_load_dwordx4 v237, s[48:51], s22 offen offset:2048 lds\n" \
"v_mfma_f32_16x16x32_bf16 v[140:143], a[180:183], v[164:167], v[140:143]\n" \
"s_add_u32 s22, s22, 7168\n" \
"v_exp_f32 v128, v128\n" \
"v_mfma_f32_16x16x32_bf16 v[140:143], a[176:179], v[160:163], v[140:143]\n" \
"s_add_u32 s38, s38, 4096\n" \
"buffer_load_dwordx4 v237, s[48:51], s22 offen offset:3072 lds\n" \
"v_smfmac_f32_16x16x64_bf16 a[44:47], a[48:51], v[144:151], v241\n" \
"buffer_load_dwordx4 a[112:115], v248, s[32:35], s38 offen offset:0 sc1\n" \
"v_mfma_f32_16x16x32_bf16 v[96:99], a[224:227], v[148:151], v[96:99]\n" \
"v_exp_f32 v129, v129\n" \
"v_mfma_f32_16x16x32_bf16 v[96:99], a[192:195], v[144:147], v[96:99]\n" \
"v_exp_f32 v130, v130\n" \
"v_mfma_f32_16x16x32_bf16 v[100:103], a[196:199], v[144:147], v[100:103]\n" \
"ds_read_b128 a[224:227], v242 offset:8192\n" \
"v_cvt_pk_bf16_f32 v128, v128, v129\n" \
"v_mfma_f32_16x16x32_bf16 v[100:103], a[228:231], v[148:151], v[100:103]\n" \
"v_exp_f32 v131, v131\n" \
"v_mfma_f32_16x16x32_bf16 v[104:107], a[232:235], v[148:151], v[104:107]\n" \
"ds_read_b128 a[192:195], v242 offset:0\n" \
"v_cvt_pk_bf16_f32 v129, v130, v131\n" \
"v_mfma_f32_16x16x32_bf16 v[104:107], a[200:203], v[144:147], v[104:107]\n" \
"v_exp_f32 v132, v132\n" \
"v_mfma_f32_16x16x32_bf16 v[108:111], a[204:207], v[144:147], v[108:111]\n" \
"ds_read_b128 a[196:199], v242 offset:1024\n" \
"ds_read_b128 a[228:231], v242 offset:9216\n" \
"v_mfma_f32_16x16x32_bf16 v[108:111], a[236:239], v[148:151], v[108:111]\n" \
"v_exp_f32 v133, v133\n" \
"v_mfma_f32_16x16x32_bf16 v[112:115], a[240:243], v[148:151], v[112:115]\n" \
"ds_read_b128 a[232:235], v242 offset:10240\n" \
"v_cvt_pk_bf16_f32 v130, v132, v133\n" \
"v_mfma_f32_16x16x32_bf16 v[112:115], a[208:211], v[144:147], v[112:115]\n" \
"v_exp_f32 v134, v134\n" \
"v_mfma_f32_16x16x32_bf16 v[116:119], a[212:215], v[144:147], v[116:119]\n" \
"ds_read_b128 a[200:203], v242 offset:2048\n" \
"ds_read_b128 a[204:207], v242 offset:3072\n" \
"buffer_load_dwordx4 a[116:119], v248, s[32:35], s38 offen offset:1024 sc1\n" \
"v_mfma_f32_16x16x32_bf16 v[116:119], a[244:247], v[148:151], v[116:119]\n" \
"v_exp_f32 v135, v135\n" \
"v_mfma_f32_16x16x32_bf16 v[120:123], a[248:251], v[148:151], v[120:123]\n" \
"s_lshl1_add_u32 s22, s66, 5\n" \
"ds_read_b128 a[236:239], v242 offset:11264\n" \
"v_cvt_pk_bf16_f32 v131, v134, v135\n" \
"v_mfma_f32_16x16x32_bf16 v[120:123], a[216:219], v[144:147], v[120:123]\n" \
"s_and_b32 s22, s22, 7\n" \
"v_exp_f32 v136, v136\n" \
"v_mfma_f32_16x16x32_bf16 v[124:127], a[220:223], v[144:147], v[124:127]\n" \
"s_lshl_b32 s22, s22, 16\n" \
"ds_read_b128 a[240:243], v242 offset:12288\n" \
"v_lshlrev_b32 v232, 3, v251\n" \
"v_mfma_f32_16x16x32_bf16 v[124:127], a[252:255], v[148:151], v[124:127]\n" \
"s_waitcnt vmcnt(9)\n" \
"v_exp_f32 v137, v137\n" \
"v_mfma_f32_16x16x32_bf16 v[144:147], a[128:131], v[176:179], 0\n" \
"s_barrier\n" \
"ds_read_b128 a[208:211], v242 offset:4096\n" \
"v_cvt_pk_bf16_f32 v132, v136, v137\n" \
"v_mfma_f32_16x16x32_bf16 v[144:147], a[132:135], v[180:183], v[144:147]\n" \
"s_add_u32 s22, s22, s17\n" \
"v_exp_f32 v138, v138\n" \
"v_mfma_f32_16x16x32_bf16 v[144:147], a[136:139], v[184:187], v[144:147]\n" \
"s_mov_b32 m0, s20\n" \
"ds_read_b128 a[212:215], v242 offset:5120\n" \
"ds_read_b128 a[244:247], v242 offset:13312\n" \
"buffer_load_dwordx4 a[120:123], v248, s[32:35], s38 offen offset:2048 sc1\n" \
"v_mfma_f32_16x16x32_bf16 v[144:147], a[140:143], v[188:191], v[144:147]\n" \
"s_lshl_b32 s70, %[wid], 9\n" \
"v_exp_f32 v139, v139\n" \
"v_mfma_f32_16x16x32_bf16 v[148:151], a[156:159], v[188:191], 0\n" \
"s_add_u32 s70, s70, 659456\n" \
"buffer_load_dwordx4 v237, s[48:51], s22 offen offset:0 lds\n" \
"v_mfma_f32_16x16x32_bf16 v[148:151], a[152:155], v[184:187], v[148:151]\n" \
"s_add_u32 s22, s22, 7168\n" \
"v_exp_f32 v140, v140\n" \
"v_mfma_f32_16x16x32_bf16 v[148:151], a[148:151], v[180:183], v[148:151]\n" \
"ds_read_b128 a[248:251], v242 offset:14336\n" \
"v_cvt_pk_bf16_f32 v133, v138, v139\n" \
"v_mfma_f32_16x16x32_bf16 v[148:151], a[144:147], v[176:179], v[148:151]\n" \
"v_exp_f32 v141, v141\n" \
"v_mfma_f32_16x16x32_bf16 v[152:155], a[160:163], v[176:179], 0\n" \
"buffer_load_dwordx4 v237, s[48:51], s22 offen offset:1024 lds\n" \
"v_mfma_f32_16x16x32_bf16 v[152:155], a[164:167], v[180:183], v[152:155]\n" \
"s_add_u32 s22, s22, 7168\n" \
"v_exp_f32 v142, v142\n" \
"v_mfma_f32_16x16x32_bf16 v[152:155], a[168:171], v[184:187], v[152:155]\n" \
"ds_read_b128 a[216:219], v242 offset:6144\n" \
"buffer_load_dwordx4 a[124:127], v248, s[32:35], s38 offen offset:3072 sc1\n" \
"v_cvt_pk_bf16_f32 v134, v140, v141\n" \
"v_mfma_f32_16x16x32_bf16 v[152:155], a[172:175], v[188:191], v[152:155]\n" \
"v_exp_f32 v143, v143\n" \
"v_mfma_f32_16x16x32_bf16 v[156:159], a[188:191], v[188:191], 0\n" \
"v_exp_f32 v144, v144\n" \
"v_mfma_f32_16x16x32_bf16 v[156:159], a[184:187], v[184:187], v[156:159]\n" \
"v_exp_f32 v145, v145\n" \
"v_mfma_f32_16x16x32_bf16 v[156:159], a[180:183], v[180:183], v[156:159]\n" \
"v_exp_f32 v146, v146\n" \
"v_cvt_pk_bf16_f32 v135, v142, v143\n" \
"v_mfma_f32_16x16x32_bf16 v[156:159], a[176:179], v[176:179], v[156:159]\n" \
"v_exp_f32 v147, v147\n" \
"v_smfmac_f32_16x16x64_bf16 a[40:43], a[48:51], v[128:135], v241\n" \
"s_waitcnt lgkmcnt(10)\n" \
"buffer_load_dwordx4 v237, s[48:51], s22 offen offset:2048 lds\n" \
"v_cvt_pk_bf16_f32 v144, v144, v145\n" \
"v_mfma_f32_16x16x32_bf16 v[64:67], a[224:227], v[132:135], v[64:67]\n" \
"s_add_u32 s22, s22, 7168\n" \
"v_exp_f32 v148, v148\n" \
"v_exp_f32 v149, v149\n" \
"v_mfma_f32_16x16x32_bf16 v[64:67], a[192:195], v[128:131], v[64:67]\n" \
"v_exp_f32 v150, v150\n" \
"v_exp_f32 v151, v151\n" \
"v_mfma_f32_16x16x32_bf16 v[68:71], a[196:199], v[128:131], v[68:71]\n" \
"v_exp_f32 v152, v152\n" \
"v_exp_f32 v153, v153\n" \
"v_mfma_f32_16x16x32_bf16 v[68:71], a[228:231], v[132:135], v[68:71]\n" \
"s_waitcnt lgkmcnt(6)\n" \
"buffer_load_dwordx4 v237, s[48:51], s22 offen offset:3072 lds\n" \
"v_cvt_pk_bf16_f32 v145, v146, v147\n" \
"v_cvt_pk_bf16_f32 v146, v148, v149\n" \
"v_mfma_f32_16x16x32_bf16 v[72:75], a[232:235], v[132:135], v[72:75]\n" \
"v_exp_f32 v154, v154\n" \
"v_cvt_pk_bf16_f32 v147, v150, v151\n" \
"v_cvt_pk_bf16_f32 v148, v152, v153\n" \
"v_mfma_f32_16x16x32_bf16 v[72:75], a[200:203], v[128:131], v[72:75]\n" \
"ds_read_b128 a[220:223], v242 offset:7168\n" \
"ds_read_b128 a[252:255], v242 offset:15360\n" \
"v_exp_f32 v155, v155\n" \
"v_mfma_f32_16x16x32_bf16 v[76:79], a[204:207], v[128:131], v[76:79]\n" \
"v_cvt_pk_bf16_f32 v149, v154, v155\n" \
"v_mov_b32 v234, 1\n" \
"v_mov_b32 v235, 1\n" \
"v_accvgpr_read_b32 v224, a40\n" \
"v_mfma_f32_16x16x32_bf16 v[76:79], a[236:239], v[132:135], v[76:79]\n" \
"s_waitcnt lgkmcnt(4)\n" \
"buffer_load_dwordx2 v[230:231], v232, s[32:35], s70 offen sc1\n" \
"v_exp_f32 v156, v156\n" \
"v_bfe_u32 v239, v251, 4, 1\n" \
"v_mfma_f32_16x16x32_bf16 v[80:83], a[240:243], v[132:135], v[80:83]\n" \
"v_exp_f32 v157, v157\n" \
"v_mul_u32_u24 v239, 0x120, v239\n" \
"v_lshrrev_b32 v249, 5, v251\n" \
"v_mfma_f32_16x16x32_bf16 v[80:83], a[208:211], v[128:131], v[80:83]\n" \
"v_cvt_pk_bf16_f32 v150, v156, v157\n" \
"v_lshl_or_b32 v239, v249, 10, v239\n" \
"v_bfe_u32 v249, v251, 2, 2\n" \
"v_lshl_or_b32 v239, v249, 6, v239\n" \
"v_mfma_f32_16x16x32_bf16 v[84:87], a[212:215], v[128:131], v[84:87]\n" \
"v_exp_f32 v158, v158\n" \
"v_and_b32 v249, 3, v251\n" \
"v_lshl_or_b32 v239, v249, 3, v239\n" \
"v_mfma_f32_16x16x32_bf16 v[84:87], a[244:247], v[132:135], v[84:87]\n" \
"s_waitcnt lgkmcnt(0)\n" \
"v_exp_f32 v159, v159\n" \
"v_add_u32 v240, s83, v239\n" \
"v_xor_b32 v236, 32, v240\n" \
"v_mfma_f32_16x16x32_bf16 v[88:91], a[248:251], v[132:135], v[88:91]\n" \
"v_cvt_pk_bf16_f32 v151, v158, v159\n" \
"v_xor_b32 v244, 0x8000, v240\n" \
"v_add_u32 v249, 0x10000, v244\n" \
"v_add_u32 v239, 0x20000, v239\n" \
"v_mfma_f32_16x16x32_bf16 v[88:91], a[216:219], v[128:131], v[88:91]\n" \
"s_lshl_b32 s39, s66, 13\n" \
"v_accvgpr_read_b32 v226, a41\n" \
"v_add_f32 v224, v224, v226\n" \
"v_rcp_f32 v224, v224\n" \
"v_mfma_f32_16x16x32_bf16 v[92:95], a[220:223], v[128:131], v[92:95]\n" \
"s_add_u32 s39, s39, s37\n" \
"v_mul_f32 v64, v64, v224\n" \
"v_mul_f32 v65, v65, v224\n" \
"v_mul_f32 v66, v66, v224\n" \
"v_mul_f32 v67, v67, v224\n" \
"v_mfma_f32_16x16x32_bf16 v[92:95], a[252:255], v[132:135], v[92:95]\n" \
"s_add_u32 s19, s39, 4096\n" \
"v_mul_f32 v68, v68, v224\n" \
"v_mul_f32 v69, v69, v224\n" \
"v_mul_f32 v70, v70, v224\n" \
"v_mul_f32 v71, v71, v224\n" \
"v_smfmac_f32_16x16x64_bf16 a[44:47], a[48:51], v[144:151], v241\n" \
"v_cvt_pk_bf16_f32 v192, v64, v65\n" \
"v_mfma_f32_16x16x32_bf16 v[96:99], a[224:227], v[148:151], v[96:99]\n" \
"v_cvt_pk_bf16_f32 v193, v66, v67\n" \
"v_cvt_pk_bf16_f32 v194, v68, v69\n" \
"v_cvt_pk_bf16_f32 v195, v70, v71\n" \
"v_mul_f32 v72, v72, v224\n" \
"v_mfma_f32_16x16x32_bf16 v[96:99], a[192:195], v[144:147], v[96:99]\n" \
"buffer_store_dwordx4 v[192:195], v248, s[32:35], s39 offen offset:0 sc1\n" \
"v_mul_f32 v73, v73, v224\n" \
"v_mul_f32 v74, v74, v224\n" \
"v_mfma_f32_16x16x32_bf16 v[100:103], a[196:199], v[144:147], v[100:103]\n" \
"ds_read_b64_tr_b16 a[0:1], v240 offset:0\n" \
"v_mul_f32 v75, v75, v224\n" \
"v_mul_f32 v76, v76, v224\n" \
"v_mul_f32 v77, v77, v224\n" \
"v_mfma_f32_16x16x32_bf16 v[100:103], a[228:231], v[148:151], v[100:103]\n" \
"ds_read_b64_tr_b16 a[2:3], v240 offset:2048\n" \
"v_mul_f32 v78, v78, v224\n" \
"v_mul_f32 v79, v79, v224\n" \
"v_cvt_pk_bf16_f32 v200, v72, v73\n" \
"v_mfma_f32_16x16x32_bf16 v[104:107], a[232:235], v[148:151], v[104:107]\n" \
"v_accvgpr_read_b32 v228, a40\n" \
"v_accvgpr_read_b32 v252, a41\n" \
"v_add_f32 v228, v228, v252\n" \
"v_accvgpr_read_b32 v229, a44\n" \
"v_mfma_f32_16x16x32_bf16 v[104:107], a[200:203], v[144:147], v[104:107]\n" \
"v_accvgpr_read_b32 v253, a45\n" \
"v_add_f32 v229, v229, v253\n" \
"v_min_f32 v253, v228, v229\n" \
"v_max_f32 v252, v228, v229\n" \
"v_mfma_f32_16x16x32_bf16 v[108:111], a[204:207], v[144:147], v[108:111]\n" \
"ds_read_b64_tr_b16 a[4:5], v240 offset:8192\n" \
"v_cmp_nle_f32 s[98:99], v246, v253\n" \
"v_cmp_nle_f32 vcc, v252, v250\n" \
"v_cmp_u_f32 s[14:15], v228, v229\n" \
"v_mfma_f32_16x16x32_bf16 v[108:111], a[236:239], v[148:151], v[108:111]\n" \
"ds_read_b64_tr_b16 a[6:7], v240 offset:10240\n" \
"ds_read_b64_tr_b16 a[8:9], v236 offset:0\n" \
"v_cvt_pk_bf16_f32 v201, v74, v75\n" \
"v_cvt_pk_bf16_f32 v202, v76, v77\n" \
"v_mfma_f32_16x16x32_bf16 v[112:115], a[240:243], v[148:151], v[112:115]\n" \
"s_or_b64 vcc, vcc, s[98:99]\n" \
"ds_read_b64_tr_b16 a[10:11], v236 offset:2048\n" \
"ds_read_b64_tr_b16 a[12:13], v236 offset:8192\n" \
"v_cvt_pk_bf16_f32 v203, v78, v79\n" \
"v_mul_f32 v80, v80, v224\n" \
"v_mfma_f32_16x16x32_bf16 v[112:115], a[208:211], v[144:147], v[112:115]\n" \
"s_or_b64 vcc, vcc, s[14:15]\n" \
"ds_read_b64_tr_b16 a[14:15], v236 offset:10240\n" \
"v_mul_f32 v81, v81, v224\n" \
"v_mul_f32 v82, v82, v224\n" \
"v_mul_f32 v83, v83, v224\n" \
"v_mfma_f32_16x16x32_bf16 v[116:119], a[212:215], v[144:147], v[116:119]\n" \
"ds_read_b64_tr_b16 a[16:17], v240 offset:512\n" \
"v_mul_f32 v84, v84, v224\n" \
"v_mul_f32 v85, v85, v224\n" \
"v_mul_f32 v86, v86, v224\n" \
"v_mfma_f32_16x16x32_bf16 v[116:119], a[244:247], v[148:151], v[116:119]\n" \
"ds_read_b64_tr_b16 a[18:19], v240 offset:2560\n" \
"v_mul_f32 v87, v87, v224\n" \
"v_cvt_pk_bf16_f32 v208, v80, v81\n" \
"v_cvt_pk_bf16_f32 v209, v82, v83\n" \
"v_mfma_f32_16x16x32_bf16 v[120:123], a[248:251], v[148:151], v[120:123]\n" \
"ds_read_b64_tr_b16 a[20:21], v240 offset:8704\n" \
"v_cvt_pk_bf16_f32 v210, v84, v85\n" \
"v_cvt_pk_bf16_f32 v211, v86, v87\n" \
"v_mul_f32 v88, v88, v224\n" \
"v_mfma_f32_16x16x32_bf16 v[120:123], a[216:219], v[144:147], v[120:123]\n" \
"ds_read_b64_tr_b16 a[22:23], v240 offset:10752\n" \
"v_mul_f32 v89, v89, v224\n" \
"v_mul_f32 v90, v90, v224\n" \
"v_mul_f32 v91, v91, v224\n" \
"v_mfma_f32_16x16x32_bf16 v[124:127], a[220:223], v[144:147], v[124:127]\n" \
"buffer_store_dwordx4 v[200:203], v248, s[32:35], s39 offen offset:2048 sc1\n" \
"v_mul_f32 v92, v92, v224\n" \
"v_mul_f32 v93, v93, v224\n" \
"v_mfma_f32_16x16x32_bf16 v[124:127], a[252:255], v[148:151], v[124:127]\n" \
"buffer_store_dwordx4 v[208:211], v248, s[32:35], s19 offen offset:0 sc1\n" \
"v_mul_f32 v94, v94, v224\n" \
"v_mul_f32 v95, v95, v224\n" \
"s_cbranch_vccnz .Lrh_%=\n" \
".Lrhb_%=:\n" \
  \
"s_waitcnt lgkmcnt(10)\n" \
"v_mfma_f32_16x16x32_bf16 v[128:131], a[0:3], v[0:3], 0\n" \
"ds_read_b64_tr_b16 a[24:25], v236 offset:512\n" \
"ds_read_b64_tr_b16 a[26:27], v236 offset:2560\n" \
"v_cvt_pk_bf16_f32 v216, v88, v89\n" \
"v_cvt_pk_bf16_f32 v217, v90, v91\n" \
"v_cvt_pk_bf16_f32 v218, v92, v93\n" \
"s_waitcnt lgkmcnt(10)\n" \
"v_mfma_f32_16x16x32_bf16 v[128:131], a[4:7], v[8:11], v[128:131]\n" \
"v_cvt_pk_bf16_f32 v219, v94, v95\n" \
"v_accvgpr_read_b32 v225, a44\n" \
"v_accvgpr_read_b32 v226, a45\n" \
"v_add_f32 v225, v225, v226\n" \
"v_mfma_f32_16x16x32_bf16 v[136:139], a[4:7], v[12:15], 0\n" \
"ds_read_b64_tr_b16 a[28:29], v236 offset:8704\n" \
"ds_read_b64_tr_b16 a[30:31], v236 offset:10752\n" \
"v_rcp_f32 v225, v225\n" \
"s_nop 0\n" \
"v_mul_f32 v96, v96, v225\n" \
"v_mfma_f32_16x16x32_bf16 v[136:139], a[0:3], v[4:7], v[136:139]\n" \
"s_waitcnt lgkmcnt(8)\n" \
"buffer_store_dwordx4 v[216:219], v248, s[32:35], s19 offen offset:2048 sc1\n" \
"v_mul_f32 v97, v97, v225\n" \
"v_mul_f32 v98, v98, v225\n" \
"v_mul_f32 v99, v99, v225\n" \
"v_mfma_f32_16x16x32_bf16 v[132:135], a[8:11], v[0:3], 0\n" \
"ds_read_b64_tr_b16 a[0:1], v240 offset:4096\n" \
"ds_read_b64_tr_b16 a[2:3], v240 offset:6144\n" \
"v_mul_f32 v100, v100, v225\n" \
"v_mul_f32 v101, v101, v225\n" \
"v_mul_f32 v102, v102, v225\n" \
"v_mfma_f32_16x16x32_bf16 v[132:135], a[12:15], v[8:11], v[132:135]\n" \
"v_mul_f32 v103, v103, v225\n" \
"v_cvt_pk_bf16_f32 v196, v96, v97\n" \
"v_cvt_pk_bf16_f32 v197, v98, v99\n" \
"v_cvt_pk_bf16_f32 v198, v100, v101\n" \
"v_cvt_pk_bf16_f32 v199, v102, v103\n" \
"v_mfma_f32_16x16x32_bf16 v[140:143], a[12:15], v[12:15], 0\n" \
"ds_read_b64_tr_b16 a[4:5], v240 offset:12288\n" \
"ds_read_b64_tr_b16 a[6:7], v240 offset:14336\n" \
"v_mul_f32 v104, v104, v225\n" \
"v_mul_f32 v105, v105, v225\n" \
"v_mul_f32 v106, v106, v225\n" \
"v_mfma_f32_16x16x32_bf16 v[140:143], a[8:11], v[4:7], v[140:143]\n" \
"s_waitcnt lgkmcnt(8)\n" \
"buffer_store_dwordx4 v[196:199], v248, s[32:35], s39 offen offset:1024 sc1\n" \
"v_mul_f32 v107, v107, v225\n" \
"v_mul_f32 v108, v108, v225\n" \
"v_mul_f32 v109, v109, v225\n" \
"v_mfma_f32_16x16x32_bf16 v[144:147], a[16:19], v[0:3], 0\n" \
"ds_read_b64_tr_b16 a[8:9], v236 offset:4096\n" \
"ds_read_b64_tr_b16 a[10:11], v236 offset:6144\n" \
"v_mul_f32 v110, v110, v225\n" \
"v_mul_f32 v111, v111, v225\n" \
"v_cvt_pk_bf16_f32 v204, v104, v105\n" \
"v_mfma_f32_16x16x32_bf16 v[144:147], a[20:23], v[8:11], v[144:147]\n" \
"v_cvt_pk_bf16_f32 v205, v106, v107\n" \
"v_cvt_pk_bf16_f32 v206, v108, v109\n" \
"v_cvt_pk_bf16_f32 v207, v110, v111\n" \
"v_mul_f32 v112, v112, v225\n" \
"v_mul_f32 v113, v113, v225\n" \
"v_mfma_f32_16x16x32_bf16 v[152:155], a[20:23], v[12:15], 0\n" \
"ds_read_b64_tr_b16 a[12:13], v236 offset:12288\n" \
"ds_read_b64_tr_b16 a[14:15], v236 offset:14336\n" \
"v_mul_f32 v114, v114, v225\n" \
"v_mul_f32 v115, v115, v225\n" \
"v_mul_f32 v116, v116, v225\n" \
"v_mfma_f32_16x16x32_bf16 v[152:155], a[16:19], v[4:7], v[152:155]\n" \
"s_waitcnt lgkmcnt(8)\n" \
"buffer_store_dwordx4 v[204:207], v248, s[32:35], s39 offen offset:3072 sc1\n" \
"v_mul_f32 v117, v117, v225\n" \
"v_mul_f32 v118, v118, v225\n" \
"v_mul_f32 v119, v119, v225\n" \
"v_mfma_f32_16x16x32_bf16 v[148:151], a[24:27], v[0:3], 0\n" \
"ds_read_b64_tr_b16 a[16:17], v240 offset:4608\n" \
"ds_read_b64_tr_b16 a[18:19], v240 offset:6656\n" \
"v_cvt_pk_bf16_f32 v212, v112, v113\n" \
"v_cvt_pk_bf16_f32 v213, v114, v115\n" \
"v_cvt_pk_bf16_f32 v214, v116, v117\n" \
"v_mfma_f32_16x16x32_bf16 v[148:151], a[28:31], v[8:11], v[148:151]\n" \
"v_cvt_pk_bf16_f32 v215, v118, v119\n" \
"v_mul_f32 v120, v120, v225\n" \
"v_mul_f32 v121, v121, v225\n" \
"v_mul_f32 v122, v122, v225\n" \
"v_mul_f32 v123, v123, v225\n" \
"v_mfma_f32_16x16x32_bf16 v[156:159], a[28:31], v[12:15], 0\n" \
"ds_read_b64_tr_b16 a[20:21], v240 offset:12800\n" \
"ds_read_b64_tr_b16 a[22:23], v240 offset:14848\n" \
"v_mul_f32 v124, v124, v225\n" \
"v_mul_f32 v125, v125, v225\n" \
"v_mul_f32 v126, v126, v225\n" \
"v_mfma_f32_16x16x32_bf16 v[156:159], a[24:27], v[4:7], v[156:159]\n" \
"s_waitcnt lgkmcnt(8)\n" \
"buffer_store_dwordx4 v[212:215], v248, s[32:35], s19 offen offset:1024 sc1\n" \
"v_mul_f32 v127, v127, v225\n" \
"v_cvt_pk_bf16_f32 v220, v120, v121\n" \
"v_cvt_pk_bf16_f32 v221, v122, v123\n" \
"v_mfma_f32_16x16x32_bf16 v[160:163], a[0:3], v[0:3], 0\n" \
"ds_read_b64_tr_b16 a[24:25], v236 offset:4608\n" \
"ds_read_b64_tr_b16 a[26:27], v236 offset:6656\n" \
"v_cvt_pk_bf16_f32 v222, v124, v125\n" \
"v_cvt_pk_bf16_f32 v223, v126, v127\n" \
"v_mov_b32 v252, s66\n" \
"v_mfma_f32_16x16x32_bf16 v[160:163], a[4:7], v[8:11], v[160:163]\n" \
"buffer_store_dwordx4 v[220:223], v248, s[32:35], s19 offen offset:3072 sc1\n" \
"v_mfma_f32_16x16x32_bf16 v[168:171], a[4:7], v[12:15], 0\n" \
"ds_read_b64_tr_b16 a[28:29], v236 offset:12800\n" \
"ds_read_b64_tr_b16 a[30:31], v236 offset:14848\n" \
"v_mfma_f32_16x16x32_bf16 v[168:171], a[0:3], v[4:7], v[168:171]\n" \
"s_waitcnt lgkmcnt(8)\n" \
"v_lshlrev_b32 v252, 2, v252\n" \
"v_mov_b32 v253, 1\n" \
"v_mfma_f32_16x16x32_bf16 v[164:167], a[8:11], v[0:3], 0\n" \
"ds_read_b64_tr_b16 a[0:1], v240 offset:16384\n" \
"ds_read_b64_tr_b16 a[2:3], v240 offset:18432\n" \
"v_mfma_f32_16x16x32_bf16 v[164:167], a[12:15], v[8:11], v[164:167]\n" \
"v_and_b32 v254, 15, v251\n" \
"v_lshlrev_b32 v254, 6, v254\n" \
"v_mfma_f32_16x16x32_bf16 v[172:175], a[12:15], v[12:15], 0\n" \
"s_waitcnt vmcnt(8)\n" \
"ds_read_b64_tr_b16 a[4:5], v240 offset:24576\n" \
"v_cmp_ne_u64 vcc, v[230:231], v[234:235]\n" \
"v_mfma_f32_16x16x32_bf16 v[172:175], a[8:11], v[4:7], v[172:175]\n" \
"s_waitcnt lgkmcnt(7)\n" \
"ds_read_b64_tr_b16 a[6:7], v240 offset:26624\n" \
"v_and_or_b32 v250, v251, 48, v254\n" \
"v_mfma_f32_16x16x32_bf16 v[176:179], a[16:19], v[0:3], 0\n" \
"ds_read_b64_tr_b16 a[8:9], v236 offset:16384\n" \
"ds_read_b64_tr_b16 a[10:11], v236 offset:18432\n" \
"v_mfma_f32_16x16x32_bf16 v[176:179], a[20:23], v[8:11], v[176:179]\n" \
"s_cbranch_vccnz .Lspg_%=\n" \
".Lsbg_%=:\n" \
"v_min_u32 v254, 8, v255\n" \
"v_mfma_f32_16x16x32_bf16 v[184:187], a[20:23], v[12:15], 0\n" \
"ds_read_b64_tr_b16 a[12:13], v236 offset:24576\n" \
"ds_read_b64_tr_b16 a[14:15], v236 offset:26624\n" \
"v_mfma_f32_16x16x32_bf16 v[184:187], a[16:19], v[4:7], v[184:187]\n" \
"s_waitcnt lgkmcnt(8)\n" \
"v_add3_u32 v254, v254, v252, 4\n" \
"v_and_b32 v254, 12, v254\n" \
"v_mfma_f32_16x16x32_bf16 v[180:183], a[24:27], v[0:3], 0\n" \
"ds_read_b64_tr_b16 a[16:17], v240 offset:16896\n" \
"ds_read_b64_tr_b16 a[18:19], v240 offset:18944\n" \
"v_mfma_f32_16x16x32_bf16 v[180:183], a[28:31], v[8:11], v[180:183]\n" \
"v_mfma_f32_16x16x32_bf16 v[188:191], a[28:31], v[12:15], 0\n" \
"ds_read_b64_tr_b16 a[20:21], v240 offset:25088\n" \
"v_add_u32 v241, 0x10000, v240\n" \
"v_mfma_f32_16x16x32_bf16 v[188:191], a[24:27], v[4:7], v[188:191]\n" \
"s_waitcnt lgkmcnt(7)\n" \
"ds_read_b64_tr_b16 a[22:23], v240 offset:27136\n" \
"v_mfma_f32_16x16x32_bf16 v[128:131], a[0:3], v[16:19], v[128:131]\n" \
"ds_read_b64_tr_b16 a[24:25], v236 offset:16896\n" \
"ds_read_b64_tr_b16 a[26:27], v236 offset:18944\n" \
"v_mfma_f32_16x16x32_bf16 v[128:131], a[4:7], v[24:27], v[128:131]\n" \
"v_mfma_f32_16x16x32_bf16 v[136:139], a[4:7], v[28:31], v[136:139]\n" \
"v_xor_b32 v235, 32, v241\n" \
"v_xor_b32 v234, 32, v244\n" \
"v_mfma_f32_16x16x32_bf16 v[136:139], a[0:3], v[20:23], v[136:139]\n" \
"s_waitcnt lgkmcnt(6)\n" \
"v_xor_b32 v233, 32, v249\n" \
"v_xor_b32 v232, 32, v239\n" \
"v_mfma_f32_16x16x32_bf16 v[132:135], a[8:11], v[16:19], v[132:135]\n" \
"ds_read_b64_tr_b16 a[28:29], v236 offset:25088\n" \
"ds_read_b64_tr_b16 a[30:31], v236 offset:27136\n" \
"v_mfma_f32_16x16x32_bf16 v[132:135], a[12:15], v[24:27], v[132:135]\n" \
"ds_read_b64_tr_b16 a[0:1], v240 offset:20480\n" \
"ds_read_b64_tr_b16 a[2:3], v240 offset:22528\n" \
"v_mfma_f32_16x16x32_bf16 v[140:143], a[12:15], v[28:31], v[140:143]\n" \
"ds_read_b64_tr_b16 a[4:5], v240 offset:28672\n" \
"ds_read_b64_tr_b16 a[6:7], v240 offset:30720\n" \
"v_mfma_f32_16x16x32_bf16 v[140:143], a[8:11], v[20:23], v[140:143]\n" \
"s_waitcnt lgkmcnt(8)\n" \
"v_mfma_f32_16x16x32_bf16 v[144:147], a[16:19], v[16:19], v[144:147]\n" \
"ds_read_b64_tr_b16 a[8:9], v236 offset:20480\n" \
"ds_read_b64_tr_b16 a[10:11], v236 offset:22528\n" \
"v_mfma_f32_16x16x32_bf16 v[144:147], a[20:23], v[24:27], v[144:147]\n" \
"v_mfma_f32_16x16x32_bf16 v[152:155], a[20:23], v[28:31], v[152:155]\n" \
"ds_read_b64_tr_b16 a[12:13], v236 offset:28672\n" \
"ds_read_b64_tr_b16 a[14:15], v236 offset:30720\n" \
"v_mfma_f32_16x16x32_bf16 v[152:155], a[16:19], v[20:23], v[152:155]\n" \
"s_waitcnt lgkmcnt(8)\n" \
"v_mfma_f32_16x16x32_bf16 v[148:151], a[24:27], v[16:19], v[148:151]\n" \
"ds_read_b64_tr_b16 a[16:17], v240 offset:20992\n" \
"ds_read_b64_tr_b16 a[18:19], v240 offset:23040\n" \
"v_mfma_f32_16x16x32_bf16 v[148:151], a[28:31], v[24:27], v[148:151]\n" \
"v_mfma_f32_16x16x32_bf16 v[156:159], a[28:31], v[28:31], v[156:159]\n" \
"ds_read_b64_tr_b16 a[20:21], v240 offset:29184\n" \
"ds_read_b64_tr_b16 a[22:23], v240 offset:31232\n" \
"v_mfma_f32_16x16x32_bf16 v[156:159], a[24:27], v[20:23], v[156:159]\n" \
"s_waitcnt lgkmcnt(8)\n" \
"v_mfma_f32_16x16x32_bf16 v[160:163], a[0:3], v[16:19], v[160:163]\n" \
"s_barrier\n" \
"ds_read_b64_tr_b16 a[24:25], v236 offset:20992\n" \
"ds_read_b64_tr_b16 a[26:27], v236 offset:23040\n" \
"v_mfma_f32_16x16x32_bf16 v[160:163], a[4:7], v[24:27], v[160:163]\n" \
"s_lshl2_add_u32 s70, s69, %[wid]\n" \
"s_lshl_b32 s70, s70, 12\n" \
"s_add_u32 s70, s70, 77594624\n" \
"v_mfma_f32_16x16x32_bf16 v[168:171], a[4:7], v[28:31], v[168:171]\n" \
"ds_read_b64_tr_b16 a[28:29], v236 offset:29184\n" \
"ds_read_b64_tr_b16 a[30:31], v236 offset:31232\n" \
"v_mfma_f32_16x16x32_bf16 v[168:171], a[0:3], v[20:23], v[168:171]\n" \
"s_waitcnt lgkmcnt(8)\n" \
"buffer_load_dwordx4 a[160:163], v248, s[32:35], s70 offen offset:0 sc1\n" \
"v_mfma_f32_16x16x32_bf16 v[164:167], a[8:11], v[16:19], v[164:167]\n" \
"ds_read_b64_tr_b16 a[0:1], v241 offset:0\n" \
"ds_read_b64_tr_b16 a[2:3], v241 offset:2048\n" \
"v_mfma_f32_16x16x32_bf16 v[164:167], a[12:15], v[24:27], v[164:167]\n" \
"v_mfma_f32_16x16x32_bf16 v[172:175], a[12:15], v[28:31], v[172:175]\n" \
"ds_read_b64_tr_b16 a[4:5], v241 offset:8192\n" \
"ds_read_b64_tr_b16 a[6:7], v241 offset:10240\n" \
"v_mfma_f32_16x16x32_bf16 v[172:175], a[8:11], v[20:23], v[172:175]\n" \
"s_waitcnt lgkmcnt(8)\n" \
"buffer_load_dwordx4 a[164:167], v248, s[32:35], s70 offen offset:1024 sc1\n" \
"v_mfma_f32_16x16x32_bf16 v[176:179], a[16:19], v[16:19], v[176:179]\n" \
"ds_read_b64_tr_b16 a[8:9], v235 offset:0\n" \
"ds_read_b64_tr_b16 a[10:11], v235 offset:2048\n" \
"v_mfma_f32_16x16x32_bf16 v[176:179], a[20:23], v[24:27], v[176:179]\n" \
"v_mfma_f32_16x16x32_bf16 v[184:187], a[20:23], v[28:31], v[184:187]\n" \
"ds_read_b64_tr_b16 a[12:13], v235 offset:8192\n" \
"ds_read_b64_tr_b16 a[14:15], v235 offset:10240\n" \
"v_mfma_f32_16x16x32_bf16 v[184:187], a[16:19], v[20:23], v[184:187]\n" \
"s_waitcnt lgkmcnt(8)\n" \
"buffer_load_dwordx4 a[168:171], v248, s[32:35], s70 offen offset:2048 sc1\n" \
"v_mfma_f32_16x16x32_bf16 v[180:183], a[24:27], v[16:19], v[180:183]\n" \
"ds_read_b64_tr_b16 a[16:17], v241 offset:512\n" \
"ds_read_b64_tr_b16 a[18:19], v241 offset:2560\n" \
"v_mfma_f32_16x16x32_bf16 v[180:183], a[28:31], v[24:27], v[180:183]\n" \
"v_mfma_f32_16x16x32_bf16 v[188:191], a[28:31], v[28:31], v[188:191]\n" \
"ds_read_b64_tr_b16 a[20:21], v241 offset:8704\n" \
"ds_read_b64_tr_b16 a[22:23], v241 offset:10752\n" \
"v_mfma_f32_16x16x32_bf16 v[188:191], a[24:27], v[20:23], v[188:191]\n" \
"s_waitcnt lgkmcnt(8)\n" \
"buffer_load_dwordx4 a[172:175], v248, s[32:35], s70 offen offset:3072 sc1\n" \
"v_lshlrev_b32 v231, 2, v251\n" \
"v_mfma_f32_16x16x32_bf16 v[128:131], a[0:3], v[32:35], v[128:131]\n" \
"ds_read_b64_tr_b16 a[24:25], v235 offset:512\n" \
"ds_read_b64_tr_b16 a[26:27], v235 offset:2560\n" \
"v_mfma_f32_16x16x32_bf16 v[128:131], a[4:7], v[40:43], v[128:131]\n" \
"v_mfma_f32_16x16x32_bf16 v[136:139], a[4:7], v[44:47], v[136:139]\n" \
"ds_read_b64_tr_b16 a[28:29], v235 offset:8704\n" \
"ds_read_b64_tr_b16 a[30:31], v235 offset:10752\n" \
"v_mfma_f32_16x16x32_bf16 v[136:139], a[0:3], v[36:39], v[136:139]\n" \
"s_waitcnt lgkmcnt(8)\n" \
"buffer_load_dword a176, v231, s[60:63], s79 offen offset:0\n" \
"v_mfma_f32_16x16x32_bf16 v[132:135], a[8:11], v[32:35], v[132:135]\n" \
"ds_read_b64_tr_b16 a[0:1], v241 offset:16384\n" \
"ds_read_b64_tr_b16 a[2:3], v241 offset:18432\n" \
"v_mfma_f32_16x16x32_bf16 v[132:135], a[12:15], v[40:43], v[132:135]\n" \
"v_mfma_f32_16x16x32_bf16 v[140:143], a[12:15], v[44:47], v[140:143]\n" \
"ds_read_b64_tr_b16 a[4:5], v241 offset:24576\n" \
"ds_read_b64_tr_b16 a[6:7], v241 offset:26624\n" \
"v_mfma_f32_16x16x32_bf16 v[140:143], a[8:11], v[36:39], v[140:143]\n" \
"s_waitcnt lgkmcnt(8)\n" \
"buffer_load_dword a177, v231, s[60:63], s79 offen offset:1024\n" \
"v_mfma_f32_16x16x32_bf16 v[144:147], a[16:19], v[32:35], v[144:147]\n" \
"ds_read_b64_tr_b16 a[8:9], v235 offset:16384\n" \
"ds_read_b64_tr_b16 a[10:11], v235 offset:18432\n" \
"v_mfma_f32_16x16x32_bf16 v[144:147], a[20:23], v[40:43], v[144:147]\n" \
"v_mfma_f32_16x16x32_bf16 v[152:155], a[20:23], v[44:47], v[152:155]\n" \
"ds_read_b64_tr_b16 a[12:13], v235 offset:24576\n" \
"ds_read_b64_tr_b16 a[14:15], v235 offset:26624\n" \
"v_mfma_f32_16x16x32_bf16 v[152:155], a[16:19], v[36:39], v[152:155]\n" \
"s_waitcnt lgkmcnt(8)\n" \
"buffer_load_dword a178, v231, s[60:63], s79 offen offset:2048\n" \
"v_mfma_f32_16x16x32_bf16 v[148:151], a[24:27], v[32:35], v[148:151]\n" \
"ds_read_b64_tr_b16 a[16:17], v241 offset:16896\n" \
"ds_read_b64_tr_b16 a[18:19], v241 offset:18944\n" \
"v_mfma_f32_16x16x32_bf16 v[148:151], a[28:31], v[40:43], v[148:151]\n" \
".Lst1_6_%=:\n" \
"s_cbranch_execz .Lst1_10_%=\n" \
"v_mfma_f32_16x16x32_bf16 v[156:159], a[28:31], v[44:47], v[156:159]\n" \
"ds_read_b64_tr_b16 a[20:21], v241 offset:25088\n" \
"ds_read_b64_tr_b16 a[22:23], v241 offset:27136\n" \
"v_mfma_f32_16x16x32_bf16 v[156:159], a[24:27], v[36:39], v[156:159]\n" \
"s_waitcnt lgkmcnt(8)\n" \
"s_sub_u32 s41, s31, 8192\n" \
"buffer_load_dword a179, v231, s[60:63], s79 offen offset:3072\n" \
"v_mfma_f32_16x16x32_bf16 v[128:131], a[0:3], v[48:51], v[128:131]\n" \
"ds_read_b64_tr_b16 a[24:25], v235 offset:16896\n" \
"ds_read_b64_tr_b16 a[26:27], v235 offset:18944\n" \
"v_mfma_f32_16x16x32_bf16 v[128:131], a[4:7], v[56:59], v[128:131]\n" \
"s_waitcnt vmcnt(8)\n" \
"buffer_store_dword v253, v252, s[32:35], s41 offen sc1\n" \
"v_mfma_f32_16x16x32_bf16 v[136:139], a[4:7], v[60:63], v[136:139]\n" \
"ds_read_b64_tr_b16 a[28:29], v235 offset:25088\n" \
"ds_read_b64_tr_b16 a[30:31], v235 offset:27136\n" \
"v_mfma_f32_16x16x32_bf16 v[136:139], a[0:3], v[52:55], v[136:139]\n" \
"s_waitcnt lgkmcnt(8)\n" \
"v_mfma_f32_16x16x32_bf16 v[132:135], a[8:11], v[48:51], v[132:135]\n" \
"ds_read_b64_tr_b16 a[0:1], v244 offset:0\n" \
"ds_read_b64_tr_b16 a[2:3], v244 offset:2048\n" \
"v_mfma_f32_16x16x32_bf16 v[132:135], a[12:15], v[56:59], v[132:135]\n" \
"buffer_load_dword a180, v231, s[60:63], s80 offen offset:0\n" \
"v_mfma_f32_16x16x32_bf16 v[140:143], a[12:15], v[60:63], v[140:143]\n" \
"ds_read_b64_tr_b16 a[4:5], v244 offset:8192\n" \
"ds_read_b64_tr_b16 a[6:7], v244 offset:10240\n" \
"v_mfma_f32_16x16x32_bf16 v[140:143], a[8:11], v[52:55], v[140:143]\n" \
"s_waitcnt lgkmcnt(8)\n" \
"v_mfma_f32_16x16x32_bf16 v[144:147], a[16:19], v[48:51], v[144:147]\n" \
"ds_read_b64_tr_b16 a[8:9], v234 offset:0\n" \
"ds_read_b64_tr_b16 a[10:11], v234 offset:2048\n" \
"v_mfma_f32_16x16x32_bf16 v[144:147], a[20:23], v[56:59], v[144:147]\n" \
"buffer_load_dword a181, v231, s[60:63], s80 offen offset:1024\n" \
"v_mfma_f32_16x16x32_bf16 v[152:155], a[20:23], v[60:63], v[152:155]\n" \
"s_lshl_b32 s17, s67, 18\n" \
"ds_read_b64_tr_b16 a[12:13], v234 offset:8192\n" \
"ds_read_b64_tr_b16 a[14:15], v234 offset:10240\n" \
"v_mfma_f32_16x16x32_bf16 v[152:155], a[16:19], v[52:55], v[152:155]\n" \
"s_waitcnt lgkmcnt(8)\n" \
"s_sub_u32 s17, 1966080, s17\n" \
"s_sub_u32 s17, s36, s17\n" \
"v_mfma_f32_16x16x32_bf16 v[148:151], a[24:27], v[48:51], v[148:151]\n" \
"s_add_u32 s16, s17, 4096\n" \
"ds_read_b64_tr_b16 a[16:17], v244 offset:512\n" \
"ds_read_b64_tr_b16 a[18:19], v244 offset:2560\n" \
"v_mfma_f32_16x16x32_bf16 v[148:151], a[28:31], v[56:59], v[148:151]\n" \
"s_add_u32 s21, s36, 4096\n" \
"buffer_load_dword a182, v231, s[60:63], s80 offen offset:2048\n" \
"v_and_b32 v246, 7, v251\n" \
"v_mfma_f32_16x16x32_bf16 v[156:159], a[28:31], v[60:63], v[156:159]\n" \
"ds_read_b64_tr_b16 a[20:21], v244 offset:8704\n" \
"ds_read_b64_tr_b16 a[22:23], v244 offset:10752\n" \
"v_mfma_f32_16x16x32_bf16 v[156:159], a[24:27], v[52:55], v[156:159]\n" \
"s_waitcnt lgkmcnt(8)\n" \
"v_lshlrev_b32 v246, 8, v246\n" \
"v_lshlrev_b32 v255, 2, v251\n" \
"v_mfma_f32_16x16x32_bf16 v[128:131], a[0:3], a[64:67], v[128:131]\n" \
"ds_read_b64_tr_b16 a[24:25], v234 offset:512\n" \
"ds_read_b64_tr_b16 a[26:27], v234 offset:2560\n" \
"v_mfma_f32_16x16x32_bf16 v[128:131], a[4:7], a[72:75], v[128:131]\n" \
"buffer_load_dword a183, v231, s[60:63], s80 offen offset:3072\n" \
"v_and_b32 v252, 0x7c, v255\n" \
"v_mfma_f32_16x16x32_bf16 v[136:139], a[4:7], a[76:79], v[136:139]\n" \
"ds_read_b64_tr_b16 a[28:29], v234 offset:8704\n" \
"ds_read_b64_tr_b16 a[30:31], v234 offset:10752\n" \
"v_mfma_f32_16x16x32_bf16 v[136:139], a[0:3], a[68:71], v[136:139]\n" \
"s_waitcnt lgkmcnt(8)\n" \
"v_mfma_f32_16x16x32_bf16 v[132:135], a[8:11], a[64:67], v[132:135]\n" \
"ds_read_b64_tr_b16 a[0:1], v249 offset:0\n" \
"ds_read_b64_tr_b16 a[2:3], v249 offset:2048\n" \
"v_mfma_f32_16x16x32_bf16 v[132:135], a[12:15], a[72:75], v[132:135]\n" \
"v_mfma_f32_16x16x32_bf16 v[140:143], a[12:15], a[76:79], v[140:143]\n" \
"ds_read_b64_tr_b16 a[4:5], v249 offset:8192\n" \
"ds_read_b64_tr_b16 a[6:7], v249 offset:10240\n" \
"v_mfma_f32_16x16x32_bf16 v[140:143], a[8:11], a[68:71], v[140:143]\n" \
"s_waitcnt lgkmcnt(8)\n" \
"v_mfma_f32_16x16x32_bf16 v[144:147], a[16:19], a[64:67], v[144:147]\n" \
"ds_read_b64_tr_b16 a[8:9], v233 offset:0\n" \
"ds_read_b64_tr_b16 a[10:11], v233 offset:2048\n" \
"v_mfma_f32_16x16x32_bf16 v[144:147], a[20:23], a[72:75], v[144:147]\n" \
"v_mfma_f32_16x16x32_bf16 v[152:155], a[20:23], a[76:79], v[152:155]\n" \
"ds_read_b64_tr_b16 a[12:13], v233 offset:8192\n" \
"ds_read_b64_tr_b16 a[14:15], v233 offset:10240\n" \
"v_mfma_f32_16x16x32_bf16 v[152:155], a[16:19], a[68:71], v[152:155]\n" \
"s_waitcnt lgkmcnt(8)\n" \
"v_mfma_f32_16x16x32_bf16 v[148:151], a[24:27], a[64:67], v[148:151]\n" \
"ds_read_b64_tr_b16 a[16:17], v249 offset:512\n" \
"ds_read_b64_tr_b16 a[18:19], v249 offset:2560\n" \
"v_mfma_f32_16x16x32_bf16 v[148:151], a[28:31], a[72:75], v[148:151]\n" \
"v_mfma_f32_16x16x32_bf16 v[156:159], a[28:31], a[76:79], v[156:159]\n" \
"ds_read_b64_tr_b16 a[20:21], v249 offset:8704\n" \
"ds_read_b64_tr_b16 a[22:23], v249 offset:10752\n" \
"v_mfma_f32_16x16x32_bf16 v[156:159], a[24:27], a[68:71], v[156:159]\n" \
"s_waitcnt lgkmcnt(8)\n" \
"v_mfma_f32_16x16x32_bf16 v[128:131], a[0:3], a[80:83], v[128:131]\n" \
"ds_read_b64_tr_b16 a[24:25], v233 offset:512\n" \
"ds_read_b64_tr_b16 a[26:27], v233 offset:2560\n" \
"v_mfma_f32_16x16x32_bf16 v[128:131], a[4:7], a[88:91], v[128:131]\n" \
"v_mfma_f32_16x16x32_bf16 v[136:139], a[4:7], a[92:95], v[136:139]\n" \
"ds_read_b64_tr_b16 a[28:29], v233 offset:8704\n" \
"ds_read_b64_tr_b16 a[30:31], v233 offset:10752\n" \
"v_mfma_f32_16x16x32_bf16 v[136:139], a[0:3], a[84:87], v[136:139]\n" \
"s_waitcnt lgkmcnt(8)\n" \
"v_mfma_f32_16x16x32_bf16 v[132:135], a[8:11], a[80:83], v[132:135]\n" \
"ds_read_b64_tr_b16 a[0:1], v239 offset:0\n" \
"ds_read_b64_tr_b16 a[2:3], v239 offset:2048\n" \
"v_mfma_f32_16x16x32_bf16 v[132:135], a[12:15], a[88:91], v[132:135]\n" \
"v_mfma_f32_16x16x32_bf16 v[140:143], a[12:15], a[92:95], v[140:143]\n" \
"ds_read_b64_tr_b16 a[4:5], v239 offset:8192\n" \
"ds_read_b64_tr_b16 a[6:7], v239 offset:10240\n" \
"v_mfma_f32_16x16x32_bf16 v[140:143], a[8:11], a[84:87], v[140:143]\n" \
"s_waitcnt lgkmcnt(8)\n" \
"v_mfma_f32_16x16x32_bf16 v[144:147], a[16:19], a[80:83], v[144:147]\n" \
"ds_read_b64_tr_b16 a[8:9], v232 offset:0\n" \
"ds_read_b64_tr_b16 a[10:11], v232 offset:2048\n" \
"v_mfma_f32_16x16x32_bf16 v[144:147], a[20:23], a[88:91], v[144:147]\n" \
"v_mfma_f32_16x16x32_bf16 v[152:155], a[20:23], a[92:95], v[152:155]\n" \
"ds_read_b64_tr_b16 a[12:13], v232 offset:8192\n" \
"ds_read_b64_tr_b16 a[14:15], v232 offset:10240\n" \
"v_mfma_f32_16x16x32_bf16 v[152:155], a[16:19], a[84:87], v[152:155]\n" \
"s_waitcnt lgkmcnt(8)\n" \
"v_mfma_f32_16x16x32_bf16 v[148:151], a[24:27], a[80:83], v[148:151]\n" \
"ds_read_b64_tr_b16 a[16:17], v239 offset:512\n" \
"ds_read_b64_tr_b16 a[18:19], v239 offset:2560\n" \
"v_mfma_f32_16x16x32_bf16 v[148:151], a[28:31], a[88:91], v[148:151]\n" \
"v_mfma_f32_16x16x32_bf16 v[156:159], a[28:31], a[92:95], v[156:159]\n" \
"ds_read_b64_tr_b16 a[20:21], v239 offset:8704\n" \
"ds_read_b64_tr_b16 a[22:23], v239 offset:10752\n" \
"v_mfma_f32_16x16x32_bf16 v[156:159], a[24:27], a[84:87], v[156:159]\n" \
"s_waitcnt lgkmcnt(8)\n" \
"v_mfma_f32_16x16x32_bf16 v[128:131], a[0:3], a[96:99], v[128:131]\n" \
"ds_read_b64_tr_b16 a[24:25], v232 offset:512\n" \
"ds_read_b64_tr_b16 a[26:27], v232 offset:2560\n" \
"v_mfma_f32_16x16x32_bf16 v[128:131], a[4:7], a[104:107], v[128:131]\n" \
"v_mfma_f32_16x16x32_bf16 v[136:139], a[4:7], a[108:111], v[136:139]\n" \
"ds_read_b64_tr_b16 a[28:29], v232 offset:8704\n" \
"ds_read_b64_tr_b16 a[30:31], v232 offset:10752\n" \
"v_mfma_f32_16x16x32_bf16 v[136:139], a[0:3], a[100:103], v[136:139]\n" \
"s_waitcnt lgkmcnt(8)\n" \
"v_mfma_f32_16x16x32_bf16 v[132:135], a[8:11], a[96:99], v[132:135]\n" \
"ds_read_b64_tr_b16 a[0:1], v239 offset:16384\n" \
"ds_read_b64_tr_b16 a[2:3], v239 offset:18432\n" \
"v_mfma_f32_16x16x32_bf16 v[132:135], a[12:15], a[104:107], v[132:135]\n" \
"v_mfma_f32_16x16x32_bf16 v[140:143], a[12:15], a[108:111], v[140:143]\n" \
"ds_read_b64_tr_b16 a[4:5], v239 offset:24576\n" \
"ds_read_b64_tr_b16 a[6:7], v239 offset:26624\n" \
"v_mfma_f32_16x16x32_bf16 v[140:143], a[8:11], a[100:103], v[140:143]\n" \
"s_waitcnt lgkmcnt(8)\n" \
"v_mfma_f32_16x16x32_bf16 v[144:147], a[16:19], a[96:99], v[144:147]\n" \
"ds_read_b64_tr_b16 a[8:9], v232 offset:16384\n" \
"ds_read_b64_tr_b16 a[10:11], v232 offset:18432\n" \
"v_mfma_f32_16x16x32_bf16 v[144:147], a[20:23], a[104:107], v[144:147]\n" \
"v_mfma_f32_16x16x32_bf16 v[152:155], a[20:23], a[108:111], v[152:155]\n" \
"ds_read_b64_tr_b16 a[12:13], v232 offset:24576\n" \
"ds_read_b64_tr_b16 a[14:15], v232 offset:26624\n" \
"v_mfma_f32_16x16x32_bf16 v[152:155], a[16:19], a[100:103], v[152:155]\n" \
"s_waitcnt lgkmcnt(8)\n" \
"v_mfma_f32_16x16x32_bf16 v[148:151], a[24:27], a[96:99], v[148:151]\n" \
"ds_read_b64_tr_b16 a[16:17], v239 offset:16896\n" \
"ds_read_b64_tr_b16 a[18:19], v239 offset:18944\n" \
"v_mfma_f32_16x16x32_bf16 v[148:151], a[28:31], a[104:107], v[148:151]\n" \
"v_mfma_f32_16x16x32_bf16 v[156:159], a[28:31], a[108:111], v[156:159]\n" \
"ds_read_b64_tr_b16 a[20:21], v239 offset:25088\n" \
"ds_read_b64_tr_b16 a[22:23], v239 offset:27136\n" \
"v_mfma_f32_16x16x32_bf16 v[156:159], a[24:27], a[100:103], v[156:159]\n" \
"s_waitcnt lgkmcnt(8)\n" \
"v_mfma_f32_16x16x32_bf16 v[128:131], a[0:3], a[112:115], v[128:131]\n" \
"ds_read_b64_tr_b16 a[24:25], v232 offset:16896\n" \
"ds_read_b64_tr_b16 a[26:27], v232 offset:18944\n" \
"v_mfma_f32_16x16x32_bf16 v[128:131], a[4:7], a[120:123], v[128:131]\n" \
"v_mfma_f32_16x16x32_bf16 v[136:139], a[4:7], a[124:127], v[136:139]\n" \
"ds_read_b64_tr_b16 a[28:29], v232 offset:25088\n" \
"ds_read_b64_tr_b16 a[30:31], v232 offset:27136\n" \
"v_mfma_f32_16x16x32_bf16 v[136:139], a[0:3], a[116:119], v[136:139]\n" \
"s_waitcnt lgkmcnt(8)\n" \
"v_mfma_f32_16x16x32_bf16 v[132:135], a[8:11], a[112:115], v[132:135]\n" \
"s_lshl2_add_u32 s18, s67, %[wid]\n" \
"ds_read_b64_tr_b16 a[0:1], v241 offset:4096\n" \
"ds_read_b64_tr_b16 a[2:3], v241 offset:6144\n" \
"v_mfma_f32_16x16x32_bf16 v[132:135], a[12:15], a[120:123], v[132:135]\n" \
"s_lshl_b32 s18, s18, 11\n" \
"s_add_u32 s18, s18, 589824\n" \
"s_lshl2_add_u32 s39, s68, s18\n" \
"v_mfma_f32_16x16x32_bf16 v[140:143], a[12:15], a[124:127], v[140:143]\n" \
"s_lshl_b32 s20, s64, 8\n" \
"ds_read_b64_tr_b16 a[4:5], v241 offset:12288\n" \
"ds_read_b64_tr_b16 a[6:7], v241 offset:14336\n" \
"v_mfma_f32_16x16x32_bf16 v[140:143], a[8:11], a[116:119], v[140:143]\n" \
"s_waitcnt lgkmcnt(8)\n" \
"s_add_u32 s18, s18, s20\n" \
"v_mfma_f32_16x16x32_bf16 v[144:147], a[16:19], a[112:115], v[144:147]\n" \
"ds_read_b64_tr_b16 a[8:9], v235 offset:4096\n" \
"ds_read_b64_tr_b16 a[10:11], v235 offset:6144\n" \
"v_mfma_f32_16x16x32_bf16 v[144:147], a[20:23], a[120:123], v[144:147]\n" \
"v_mfma_f32_16x16x32_bf16 v[152:155], a[20:23], a[124:127], v[152:155]\n" \
"ds_read_b64_tr_b16 a[12:13], v235 offset:12288\n" \
"ds_read_b64_tr_b16 a[14:15], v235 offset:14336\n" \
"v_mfma_f32_16x16x32_bf16 v[152:155], a[16:19], a[116:119], v[152:155]\n" \
"s_waitcnt lgkmcnt(8)\n" \
"v_mfma_f32_16x16x32_bf16 v[148:151], a[24:27], a[112:115], v[148:151]\n" \
"ds_read_b64_tr_b16 a[16:17], v241 offset:20480\n" \
"ds_read_b64_tr_b16 a[18:19], v241 offset:22528\n" \
"v_mfma_f32_16x16x32_bf16 v[148:151], a[28:31], a[120:123], v[148:151]\n" \
"v_cvt_pk_bf16_f32 v128, v128, v129\n" \
"v_cvt_pk_bf16_f32 v129, v130, v131\n" \
"v_mfma_f32_16x16x32_bf16 v[156:159], a[28:31], a[124:127], v[156:159]\n" \
"ds_read_b64_tr_b16 a[20:21], v241 offset:28672\n" \
"ds_read_b64_tr_b16 a[22:23], v241 offset:30720\n" \
"v_mfma_f32_16x16x32_bf16 v[156:159], a[24:27], a[116:119], v[156:159]\n" \
"s_waitcnt lgkmcnt(8)\n" \
"v_cvt_pk_bf16_f32 v130, v132, v133\n" \
"v_cvt_pk_bf16_f32 v131, v134, v135\n" \
"v_mfma_f32_16x16x32_bf16 v[160:163], a[0:3], v[32:35], v[160:163]\n" \
"ds_read_b64_tr_b16 a[24:25], v235 offset:20480\n" \
"ds_read_b64_tr_b16 a[26:27], v235 offset:22528\n" \
"v_mfma_f32_16x16x32_bf16 v[160:163], a[4:7], v[40:43], v[160:163]\n" \
"v_mfma_f32_16x16x32_bf16 v[168:171], a[4:7], v[44:47], v[168:171]\n" \
"ds_read_b64_tr_b16 a[28:29], v235 offset:28672\n" \
"ds_read_b64_tr_b16 a[30:31], v235 offset:30720\n" \
"v_mfma_f32_16x16x32_bf16 v[168:171], a[0:3], v[36:39], v[168:171]\n" \
"s_waitcnt lgkmcnt(8)\n" \
"buffer_store_dwordx4 v[128:131], v250, s[32:35], s17 offen offset:0 sc1\n" \
"v_mfma_f32_16x16x32_bf16 v[164:167], a[8:11], v[32:35], v[164:167]\n" \
"ds_read_b64_tr_b16 a[0:1], v244 offset:4096\n" \
"ds_read_b64_tr_b16 a[2:3], v244 offset:6144\n" \
"v_mfma_f32_16x16x32_bf16 v[164:167], a[12:15], v[40:43], v[164:167]\n" \
"buffer_load_dword v247, v254, s[32:35], s41 offen sc1\n" \
"v_mfma_f32_16x16x32_bf16 v[172:175], a[12:15], v[44:47], v[172:175]\n" \
"ds_read_b64_tr_b16 a[4:5], v244 offset:12288\n" \
"ds_read_b64_tr_b16 a[6:7], v244 offset:14336\n" \
"v_mfma_f32_16x16x32_bf16 v[172:175], a[8:11], v[36:39], v[172:175]\n" \
"s_waitcnt lgkmcnt(8)\n" \
"v_cvt_pk_bf16_f32 v136, v136, v137\n" \
"v_cvt_pk_bf16_f32 v137, v138, v139\n" \
"v_mfma_f32_16x16x32_bf16 v[160:163], a[16:19], v[48:51], v[160:163]\n" \
"ds_read_b64_tr_b16 a[8:9], v234 offset:4096\n" \
"ds_read_b64_tr_b16 a[10:11], v234 offset:6144\n" \
"v_mfma_f32_16x16x32_bf16 v[160:163], a[20:23], v[56:59], v[160:163]\n" \
"v_cvt_pk_bf16_f32 v138, v140, v141\n" \
"v_cvt_pk_bf16_f32 v139, v142, v143\n" \
"v_mfma_f32_16x16x32_bf16 v[168:171], a[20:23], v[60:63], v[168:171]\n" \
"ds_read_b64_tr_b16 a[12:13], v234 offset:12288\n" \
"ds_read_b64_tr_b16 a[14:15], v234 offset:14336\n" \
"v_mfma_f32_16x16x32_bf16 v[168:171], a[16:19], v[52:55], v[168:171]\n" \
"s_waitcnt lgkmcnt(8)\n" \
"v_mfma_f32_16x16x32_bf16 v[164:167], a[24:27], v[48:51], v[164:167]\n" \
"ds_read_b64_tr_b16 a[16:17], v249 offset:4096\n" \
"ds_read_b64_tr_b16 a[18:19], v249 offset:6144\n" \
"v_mfma_f32_16x16x32_bf16 v[164:167], a[28:31], v[56:59], v[164:167]\n" \
"v_mfma_f32_16x16x32_bf16 v[172:175], a[28:31], v[60:63], v[172:175]\n" \
"ds_read_b64_tr_b16 a[20:21], v249 offset:12288\n" \
"ds_read_b64_tr_b16 a[22:23], v249 offset:14336\n" \
"v_mfma_f32_16x16x32_bf16 v[172:175], a[24:27], v[52:55], v[172:175]\n" \
"s_waitcnt lgkmcnt(8)\n" \
"buffer_store_dwordx4 v[136:139], v250, s[32:35], s16 offen offset:0 sc1\n" \
"v_mfma_f32_16x16x32_bf16 v[160:163], a[0:3], a[64:67], v[160:163]\n" \
"ds_read_b64_tr_b16 a[24:25], v233 offset:4096\n" \
"ds_read_b64_tr_b16 a[26:27], v233 offset:6144\n" \
"v_mfma_f32_16x16x32_bf16 v[160:163], a[4:7], a[72:75], v[160:163]\n" \
"v_mfma_f32_16x16x32_bf16 v[168:171], a[4:7], a[76:79], v[168:171]\n" \
"ds_read_b64_tr_b16 a[28:29], v233 offset:12288\n" \
"ds_read_b64_tr_b16 a[30:31], v233 offset:14336\n" \
"v_mfma_f32_16x16x32_bf16 v[168:171], a[0:3], a[68:71], v[168:171]\n" \
"s_waitcnt lgkmcnt(8)\n" \
"v_cvt_pk_bf16_f32 v144, v144, v145\n" \
"v_cvt_pk_bf16_f32 v145, v146, v147\n" \
"v_mfma_f32_16x16x32_bf16 v[164:167], a[8:11], a[64:67], v[164:167]\n" \
"ds_read_b64_tr_b16 a[0:1], v239 offset:4096\n" \
"ds_read_b64_tr_b16 a[2:3], v239 offset:6144\n" \
"v_mfma_f32_16x16x32_bf16 v[164:167], a[12:15], a[72:75], v[164:167]\n" \
"v_cvt_pk_bf16_f32 v146, v148, v149\n" \
"v_cvt_pk_bf16_f32 v147, v150, v151\n" \
"v_mfma_f32_16x16x32_bf16 v[172:175], a[12:15], a[76:79], v[172:175]\n" \
"ds_read_b64_tr_b16 a[4:5], v239 offset:12288\n" \
"ds_read_b64_tr_b16 a[6:7], v239 offset:14336\n" \
"v_mfma_f32_16x16x32_bf16 v[172:175], a[8:11], a[68:71], v[172:175]\n" \
"s_waitcnt lgkmcnt(8)\n" \
"v_mfma_f32_16x16x32_bf16 v[160:163], a[16:19], a[80:83], v[160:163]\n" \
"ds_read_b64_tr_b16 a[8:9], v232 offset:4096\n" \
"ds_read_b64_tr_b16 a[10:11], v232 offset:6144\n" \
"v_mfma_f32_16x16x32_bf16 v[160:163], a[20:23], a[88:91], v[160:163]\n" \
"v_mfma_f32_16x16x32_bf16 v[168:171], a[20:23], a[92:95], v[168:171]\n" \
"ds_read_b64_tr_b16 a[12:13], v232 offset:12288\n" \
"ds_read_b64_tr_b16 a[14:15], v232 offset:14336\n" \
"v_mfma_f32_16x16x32_bf16 v[168:171], a[16:19], a[84:87], v[168:171]\n" \
"s_waitcnt lgkmcnt(8)\n" \
"buffer_store_dwordx4 v[144:147], v250, s[32:35], s17 offen offset:1024 sc1\n" \
"v_mfma_f32_16x16x32_bf16 v[164:167], a[24:27], a[80:83], v[164:167]\n" \
"ds_read_b64_tr_b16 a[16:17], v239 offset:20480\n" \
"ds_read_b64_tr_b16 a[18:19], v239 offset:22528\n" \
"v_mfma_f32_16x16x32_bf16 v[164:167], a[28:31], a[88:91], v[164:167]\n" \
"v_mfma_f32_16x16x32_bf16 v[172:175], a[28:31], a[92:95], v[172:175]\n" \
"ds_read_b64_tr_b16 a[20:21], v239 offset:28672\n" \
"ds_read_b64_tr_b16 a[22:23], v239 offset:30720\n" \
"v_mfma_f32_16x16x32_bf16 v[172:175], a[24:27], a[84:87], v[172:175]\n" \
"s_waitcnt lgkmcnt(8)\n" \
"v_cvt_pk_bf16_f32 v152, v152, v153\n" \
"v_cvt_pk_bf16_f32 v153, v154, v155\n" \
"v_mfma_f32_16x16x32_bf16 v[160:163], a[0:3], a[96:99], v[160:163]\n" \
"ds_read_b64_tr_b16 a[24:25], v232 offset:20480\n" \
"ds_read_b64_tr_b16 a[26:27], v232 offset:22528\n" \
"v_mfma_f32_16x16x32_bf16 v[160:163], a[4:7], a[104:107], v[160:163]\n" \
"v_cvt_pk_bf16_f32 v154, v156, v157\n" \
"v_cvt_pk_bf16_f32 v155, v158, v159\n" \
"v_mfma_f32_16x16x32_bf16 v[168:171], a[4:7], a[108:111], v[168:171]\n" \
"ds_read_b64_tr_b16 a[28:29], v232 offset:28672\n" \
"ds_read_b64_tr_b16 a[30:31], v232 offset:30720\n" \
"v_mfma_f32_16x16x32_bf16 v[168:171], a[0:3], a[100:103], v[168:171]\n" \
"s_waitcnt lgkmcnt(8)\n" \
"v_mfma_f32_16x16x32_bf16 v[164:167], a[8:11], a[96:99], v[164:167]\n" \
"ds_read_b64_tr_b16 a[0:1], v241 offset:4608\n" \
"ds_read_b64_tr_b16 a[2:3], v241 offset:6656\n" \
"v_mfma_f32_16x16x32_bf16 v[164:167], a[12:15], a[104:107], v[164:167]\n" \
"v_mfma_f32_16x16x32_bf16 v[172:175], a[12:15], a[108:111], v[172:175]\n" \
"ds_read_b64_tr_b16 a[4:5], v241 offset:12800\n" \
"ds_read_b64_tr_b16 a[6:7], v241 offset:14848\n" \
"v_mfma_f32_16x16x32_bf16 v[172:175], a[8:11], a[100:103], v[172:175]\n" \
"s_waitcnt lgkmcnt(8)\n" \
"buffer_store_dwordx4 v[152:155], v250, s[32:35], s16 offen offset:1024 sc1\n" \
"v_mfma_f32_16x16x32_bf16 v[160:163], a[16:19], a[112:115], v[160:163]\n" \
"ds_read_b64_tr_b16 a[8:9], v235 offset:4608\n" \
"ds_read_b64_tr_b16 a[10:11], v235 offset:6656\n" \
"v_mfma_f32_16x16x32_bf16 v[160:163], a[20:23], a[120:123], v[160:163]\n" \
"v_mfma_f32_16x16x32_bf16 v[168:171], a[20:23], a[124:127], v[168:171]\n" \
"ds_read_b64_tr_b16 a[12:13], v235 offset:12800\n" \
"ds_read_b64_tr_b16 a[14:15], v235 offset:14848\n" \
"v_mfma_f32_16x16x32_bf16 v[168:171], a[16:19], a[116:119], v[168:171]\n" \
"s_waitcnt lgkmcnt(8)\n" \
"v_mfma_f32_16x16x32_bf16 v[164:167], a[24:27], a[112:115], v[164:167]\n" \
"ds_read_b64_tr_b16 a[16:17], v241 offset:20992\n" \
"ds_read_b64_tr_b16 a[18:19], v241 offset:23040\n" \
"v_mfma_f32_16x16x32_bf16 v[164:167], a[28:31], a[120:123], v[164:167]\n" \
"v_mfma_f32_16x16x32_bf16 v[172:175], a[28:31], a[124:127], v[172:175]\n" \
"ds_read_b64_tr_b16 a[20:21], v241 offset:29184\n" \
"ds_read_b64_tr_b16 a[22:23], v241 offset:31232\n" \
"v_mfma_f32_16x16x32_bf16 v[172:175], a[24:27], a[116:119], v[172:175]\n" \
"s_waitcnt lgkmcnt(8)\n" \
"v_mfma_f32_16x16x32_bf16 v[176:179], a[0:3], v[32:35], v[176:179]\n" \
"ds_read_b64_tr_b16 a[24:25], v235 offset:20992\n" \
"ds_read_b64_tr_b16 a[26:27], v235 offset:23040\n" \
"v_mfma_f32_16x16x32_bf16 v[176:179], a[4:7], v[40:43], v[176:179]\n" \
"v_mfma_f32_16x16x32_bf16 v[184:187], a[4:7], v[44:47], v[184:187]\n" \
"ds_read_b64_tr_b16 a[28:29], v235 offset:29184\n" \
"ds_read_b64_tr_b16 a[30:31], v235 offset:31232\n" \
"v_mfma_f32_16x16x32_bf16 v[184:187], a[0:3], v[36:39], v[184:187]\n" \
"s_waitcnt lgkmcnt(8)\n" \
"v_mfma_f32_16x16x32_bf16 v[180:183], a[8:11], v[32:35], v[180:183]\n" \
"ds_read_b64_tr_b16 a[0:1], v244 offset:4608\n" \
"ds_read_b64_tr_b16 a[2:3], v244 offset:6656\n" \
"v_mfma_f32_16x16x32_bf16 v[180:183], a[12:15], v[40:43], v[180:183]\n" \
"v_cvt_pk_bf16_f32 v160, v160, v161\n" \
"v_cvt_pk_bf16_f32 v161, v162, v163\n" \
"v_mfma_f32_16x16x32_bf16 v[188:191], a[12:15], v[44:47], v[188:191]\n" \
"ds_read_b64_tr_b16 a[4:5], v244 offset:12800\n" \
"ds_read_b64_tr_b16 a[6:7], v244 offset:14848\n" \
"v_mfma_f32_16x16x32_bf16 v[188:191], a[8:11], v[36:39], v[188:191]\n" \
"s_waitcnt lgkmcnt(8)\n" \
"v_cvt_pk_bf16_f32 v162, v164, v165\n" \
"v_cvt_pk_bf16_f32 v163, v166, v167\n" \
"v_mfma_f32_16x16x32_bf16 v[176:179], a[16:19], v[48:51], v[176:179]\n" \
"s_waitcnt vmcnt(3)\n" \
"ds_read_b64_tr_b16 a[8:9], v234 offset:4608\n" \
"v_cmp_eq_u32 vcc, 0, v247\n" \
"v_mfma_f32_16x16x32_bf16 v[176:179], a[20:23], v[56:59], v[176:179]\n" \
"s_add_u32 s38, s66, 1\n" \
"s_and_b32 s38, s38, 3\n" \
"ds_read_b64_tr_b16 a[10:11], v234 offset:6656\n" \
"v_mfma_f32_16x16x32_bf16 v[184:187], a[20:23], v[60:63], v[184:187]\n" \
"s_lshl_b32 s38, s38, 13\n" \
"ds_read_b64_tr_b16 a[12:13], v234 offset:12800\n" \
"ds_read_b64_tr_b16 a[14:15], v234 offset:14848\n" \
"v_mfma_f32_16x16x32_bf16 v[184:187], a[16:19], v[52:55], v[184:187]\n" \
"s_waitcnt lgkmcnt(8)\n" \
"s_cbranch_vccnz .LspcA_%=\n" \
".LsbcA_%=:\n" \
"v_mfma_f32_16x16x32_bf16 v[180:183], a[24:27], v[48:51], v[180:183]\n" \
"s_add_u32 s38, s38, s37\n" \
"ds_read_b64_tr_b16 a[16:17], v249 offset:4608\n" \
"ds_read_b64_tr_b16 a[18:19], v249 offset:6656\n" \
"v_mfma_f32_16x16x32_bf16 v[180:183], a[28:31], v[56:59], v[180:183]\n" \
"buffer_store_dwordx4 v[160:163], v250, s[32:35], s17 offen offset:2048 sc1\n" \
"v_mfma_f32_16x16x32_bf16 v[188:191], a[28:31], v[60:63], v[188:191]\n" \
"ds_read_b64_tr_b16 a[20:21], v249 offset:12800\n" \
"ds_read_b64_tr_b16 a[22:23], v249 offset:14848\n" \
"v_mfma_f32_16x16x32_bf16 v[188:191], a[24:27], v[52:55], v[188:191]\n" \
"s_waitcnt lgkmcnt(8)\n" \
"v_cvt_pk_bf16_f32 v168, v168, v169\n" \
"v_cvt_pk_bf16_f32 v169, v170, v171\n" \
"v_mfma_f32_16x16x32_bf16 v[176:179], a[0:3], a[64:67], v[176:179]\n" \
"ds_read_b64_tr_b16 a[24:25], v233 offset:4608\n" \
"ds_read_b64_tr_b16 a[26:27], v233 offset:6656\n" \
"v_mfma_f32_16x16x32_bf16 v[176:179], a[4:7], a[72:75], v[176:179]\n" \
"buffer_load_dwordx4 a[192:195], v248, s[32:35], s38 offen offset:0 sc1\n" \
"v_cvt_pk_bf16_f32 v170, v172, v173\n" \
"v_mfma_f32_16x16x32_bf16 v[184:187], a[4:7], a[76:79], v[184:187]\n" \
"ds_read_b64_tr_b16 a[28:29], v233 offset:12800\n" \
"ds_read_b64_tr_b16 a[30:31], v233 offset:14848\n" \
"v_mfma_f32_16x16x32_bf16 v[184:187], a[0:3], a[68:71], v[184:187]\n" \
"s_waitcnt lgkmcnt(8)\n" \
"v_cvt_pk_bf16_f32 v171, v174, v175\n" \
"v_mfma_f32_16x16x32_bf16 v[180:183], a[8:11], a[64:67], v[180:183]\n" \
"ds_read_b64_tr_b16 a[0:1], v239 offset:4608\n" \
"ds_read_b64_tr_b16 a[2:3], v239 offset:6656\n" \
"v_mfma_f32_16x16x32_bf16 v[180:183], a[12:15], a[72:75], v[180:183]\n" \
"buffer_load_dwordx4 a[196:199], v248, s[32:35], s38 offen offset:1024 sc1\n" \
"v_mfma_f32_16x16x32_bf16 v[188:191], a[12:15], a[76:79], v[188:191]\n" \
"ds_read_b64_tr_b16 a[4:5], v239 offset:12800\n" \
"ds_read_b64_tr_b16 a[6:7], v239 offset:14848\n" \
"v_mfma_f32_16x16x32_bf16 v[188:191], a[8:11], a[68:71], v[188:191]\n" \
"s_waitcnt lgkmcnt(8)\n" \
"buffer_store_dwordx4 v[168:171], v250, s[32:35], s16 offen offset:2048 sc1\n" \
"v_mfma_f32_16x16x32_bf16 v[176:179], a[16:19], a[80:83], v[176:179]\n" \
"ds_read_b64_tr_b16 a[8:9], v232 offset:4608\n" \
"ds_read_b64_tr_b16 a[10:11], v232 offset:6656\n" \
"v_mfma_f32_16x16x32_bf16 v[176:179], a[20:23], a[88:91], v[176:179]\n" \
"v_mfma_f32_16x16x32_bf16 v[184:187], a[20:23], a[92:95], v[184:187]\n" \
"ds_read_b64_tr_b16 a[12:13], v232 offset:12800\n" \
"ds_read_b64_tr_b16 a[14:15], v232 offset:14848\n" \
"v_mfma_f32_16x16x32_bf16 v[184:187], a[16:19], a[84:87], v[184:187]\n" \
"s_waitcnt lgkmcnt(8)\n" \
"buffer_load_dwordx4 a[200:203], v248, s[32:35], s38 offen offset:2048 sc1\n" \
"v_mfma_f32_16x16x32_bf16 v[180:183], a[24:27], a[80:83], v[180:183]\n" \
"ds_read_b64_tr_b16 a[16:17], v239 offset:20992\n" \
"ds_read_b64_tr_b16 a[18:19], v239 offset:23040\n" \
"v_mfma_f32_16x16x32_bf16 v[180:183], a[28:31], a[88:91], v[180:183]\n" \
"v_mfma_f32_16x16x32_bf16 v[188:191], a[28:31], a[92:95], v[188:191]\n" \
"ds_read_b64_tr_b16 a[20:21], v239 offset:29184\n" \
"ds_read_b64_tr_b16 a[22:23], v239 offset:31232\n" \
"v_mfma_f32_16x16x32_bf16 v[188:191], a[24:27], a[84:87], v[188:191]\n" \
"s_waitcnt lgkmcnt(8)\n" \
"buffer_load_dwordx4 a[204:207], v248, s[32:35], s38 offen offset:3072 sc1\n" \
"v_mfma_f32_16x16x32_bf16 v[176:179], a[0:3], a[96:99], v[176:179]\n" \
"s_add_u32 s38, s38, 4096\n" \
"ds_read_b64_tr_b16 a[24:25], v232 offset:20992\n" \
"ds_read_b64_tr_b16 a[26:27], v232 offset:23040\n" \
"v_mfma_f32_16x16x32_bf16 v[176:179], a[4:7], a[104:107], v[176:179]\n" \
"v_mfma_f32_16x16x32_bf16 v[184:187], a[4:7], a[108:111], v[184:187]\n" \
"ds_read_b64_tr_b16 a[28:29], v232 offset:29184\n" \
"ds_read_b64_tr_b16 a[30:31], v232 offset:31232\n" \
"v_mfma_f32_16x16x32_bf16 v[184:187], a[0:3], a[100:103], v[184:187]\n" \
"s_waitcnt lgkmcnt(8)\n" \
"buffer_load_dwordx4 a[208:211], v248, s[32:35], s38 offen offset:0 sc1\n" \
"v_mfma_f32_16x16x32_bf16 v[180:183], a[8:11], a[96:99], v[180:183]\n" \
"ds_read_b64_tr_b16 a[0:1], v240 offset:0\n" \
"ds_read_b64_tr_b16 a[2:3], v240 offset:2048\n" \
"v_mfma_f32_16x16x32_bf16 v[180:183], a[12:15], a[104:107], v[180:183]\n" \
"v_mfma_f32_16x16x32_bf16 v[188:191], a[12:15], a[108:111], v[188:191]\n" \
"ds_read_b64_tr_b16 a[4:5], v240 offset:8192\n" \
"ds_read_b64_tr_b16 a[6:7], v240 offset:10240\n" \
"v_mfma_f32_16x16x32_bf16 v[188:191], a[8:11], a[100:103], v[188:191]\n" \
"s_waitcnt lgkmcnt(8)\n" \
"buffer_load_dwordx4 a[212:215], v248, s[32:35], s38 offen offset:1024 sc1\n" \
"v_mfma_f32_16x16x32_bf16 v[176:179], a[16:19], a[112:115], v[176:179]\n" \
"ds_read_b64_tr_b16 a[8:9], v236 offset:0\n" \
"ds_read_b64_tr_b16 a[10:11], v236 offset:2048\n" \
"v_mfma_f32_16x16x32_bf16 v[176:179], a[20:23], a[120:123], v[176:179]\n" \
"v_mfma_f32_16x16x32_bf16 v[184:187], a[20:23], a[124:127], v[184:187]\n" \
"ds_read_b64_tr_b16 a[12:13], v236 offset:8192\n" \
"ds_read_b64_tr_b16 a[14:15], v236 offset:10240\n" \
"v_mfma_f32_16x16x32_bf16 v[184:187], a[16:19], a[116:119], v[184:187]\n" \
"s_waitcnt lgkmcnt(8)\n" \
"buffer_load_dwordx4 a[216:219], v248, s[32:35], s38 offen offset:2048 sc1\n" \
"v_mfma_f32_16x16x32_bf16 v[180:183], a[24:27], a[112:115], v[180:183]\n" \
"ds_read_b64_tr_b16 a[16:17], v240 offset:512\n" \
"ds_read_b64_tr_b16 a[18:19], v240 offset:2560\n" \
"v_mfma_f32_16x16x32_bf16 v[180:183], a[28:31], a[120:123], v[180:183]\n" \
"v_mfma_f32_16x16x32_bf16 v[188:191], a[28:31], a[124:127], v[188:191]\n" \
"ds_read_b64_tr_b16 a[20:21], v240 offset:8704\n" \
"ds_read_b64_tr_b16 a[22:23], v240 offset:10752\n" \
"v_mfma_f32_16x16x32_bf16 v[188:191], a[24:27], a[116:119], v[188:191]\n" \
"s_waitcnt lgkmcnt(8)\n" \
"buffer_load_dwordx4 a[220:223], v248, s[32:35], s38 offen offset:3072 sc1\n" \
"v_mfma_f32_16x16x32_bf16 v[64:67], a[0:3], v[192:195], 0\n" \
"s_add_u32 s38, s66, 2\n" \
"ds_read_b64_tr_b16 a[24:25], v236 offset:512\n" \
"ds_read_b64_tr_b16 a[26:27], v236 offset:2560\n" \
"v_mfma_f32_16x16x32_bf16 v[64:67], a[4:7], v[200:203], v[64:67]\n" \
"s_and_b32 s38, s38, 3\n" \
"s_lshl_b32 s38, s38, 13\n" \
"s_add_u32 s38, s38, s37\n" \
"v_mfma_f32_16x16x32_bf16 v[72:75], a[4:7], v[204:207], 0\n" \
"ds_read_b64_tr_b16 a[28:29], v236 offset:8704\n" \
"ds_read_b64_tr_b16 a[30:31], v236 offset:10752\n" \
"v_mfma_f32_16x16x32_bf16 v[72:75], a[0:3], v[196:199], v[72:75]\n" \
"s_waitcnt lgkmcnt(8)\n" \
"buffer_load_dwordx4 a[224:227], v248, s[32:35], s38 offen offset:0 sc1\n" \
"v_mfma_f32_16x16x32_bf16 v[68:71], a[8:11], v[192:195], 0\n" \
"ds_read_b64_tr_b16 a[0:1], v240 offset:4096\n" \
"ds_read_b64_tr_b16 a[2:3], v240 offset:6144\n" \
"v_mfma_f32_16x16x32_bf16 v[68:71], a[12:15], v[200:203], v[68:71]\n" \
"v_cvt_pk_bf16_f32 v176, v176, v177\n" \
"v_cvt_pk_bf16_f32 v177, v178, v179\n" \
"v_mfma_f32_16x16x32_bf16 v[76:79], a[12:15], v[204:207], 0\n" \
"ds_read_b64_tr_b16 a[4:5], v240 offset:12288\n" \
"ds_read_b64_tr_b16 a[6:7], v240 offset:14336\n" \
"v_mfma_f32_16x16x32_bf16 v[76:79], a[8:11], v[196:199], v[76:79]\n" \
"s_waitcnt lgkmcnt(8)\n" \
"buffer_load_dwordx4 a[228:231], v248, s[32:35], s38 offen offset:1024 sc1\n" \
"v_cvt_pk_bf16_f32 v178, v180, v181\n" \
"v_mfma_f32_16x16x32_bf16 v[80:83], a[16:19], v[192:195], 0\n" \
"ds_read_b64_tr_b16 a[8:9], v236 offset:4096\n" \
"ds_read_b64_tr_b16 a[10:11], v236 offset:6144\n" \
"v_mfma_f32_16x16x32_bf16 v[80:83], a[20:23], v[200:203], v[80:83]\n" \
"v_cvt_pk_bf16_f32 v179, v182, v183\n" \
"v_mfma_f32_16x16x32_bf16 v[88:91], a[20:23], v[204:207], 0\n" \
"ds_read_b64_tr_b16 a[12:13], v236 offset:12288\n" \
"ds_read_b64_tr_b16 a[14:15], v236 offset:14336\n" \
"v_mfma_f32_16x16x32_bf16 v[88:91], a[16:19], v[196:199], v[88:91]\n" \
"s_waitcnt lgkmcnt(8)\n" \
"buffer_store_dwordx4 v[176:179], v250, s[32:35], s17 offen offset:3072 sc1\n" \
"v_mfma_f32_16x16x32_bf16 v[84:87], a[24:27], v[192:195], 0\n" \
"ds_read_b64_tr_b16 a[16:17], v240 offset:4608\n" \
"ds_read_b64_tr_b16 a[18:19], v240 offset:6656\n" \
"v_mfma_f32_16x16x32_bf16 v[84:87], a[28:31], v[200:203], v[84:87]\n" \
"v_mfma_f32_16x16x32_bf16 v[92:95], a[28:31], v[204:207], 0\n" \
"ds_read_b64_tr_b16 a[20:21], v240 offset:12800\n" \
"ds_read_b64_tr_b16 a[22:23], v240 offset:14848\n" \
"v_mfma_f32_16x16x32_bf16 v[92:95], a[24:27], v[196:199], v[92:95]\n" \
"s_waitcnt lgkmcnt(8)\n" \
"buffer_load_dwordx4 a[232:235], v248, s[32:35], s38 offen offset:2048 sc1\n" \
"v_cvt_pk_bf16_f32 v184, v184, v185\n" \
"v_mfma_f32_16x16x32_bf16 v[96:99], a[0:3], v[192:195], 0\n" \
"ds_read_b64_tr_b16 a[24:25], v236 offset:4608\n" \
"ds_read_b64_tr_b16 a[26:27], v236 offset:6656\n" \
"v_mfma_f32_16x16x32_bf16 v[96:99], a[4:7], v[200:203], v[96:99]\n" \
"v_cvt_pk_bf16_f32 v185, v186, v187\n" \
"v_cvt_pk_bf16_f32 v186, v188, v189\n" \
"v_mfma_f32_16x16x32_bf16 v[104:107], a[4:7], v[204:207], 0\n" \
"ds_read_b64_tr_b16 a[28:29], v236 offset:12800\n" \
"ds_read_b64_tr_b16 a[30:31], v236 offset:14848\n" \
"v_mfma_f32_16x16x32_bf16 v[104:107], a[0:3], v[196:199], v[104:107]\n" \
"s_waitcnt lgkmcnt(8)\n" \
"buffer_load_dwordx4 a[236:239], v248, s[32:35], s38 offen offset:3072 sc1\n" \
"v_cvt_pk_bf16_f32 v187, v190, v191\n" \
"v_mfma_f32_16x16x32_bf16 v[100:103], a[8:11], v[192:195], 0\n" \
"s_add_u32 s38, s38, 4096\n" \
"ds_read_b64_tr_b16 a[0:1], v240 offset:16384\n" \
"ds_read_b64_tr_b16 a[2:3], v240 offset:18432\n" \
"v_mfma_f32_16x16x32_bf16 v[100:103], a[12:15], v[200:203], v[100:103]\n" \
"v_mfma_f32_16x16x32_bf16 v[108:111], a[12:15], v[204:207], 0\n" \
"ds_read_b64_tr_b16 a[4:5], v240 offset:24576\n" \
"ds_read_b64_tr_b16 a[6:7], v240 offset:26624\n" \
"v_mfma_f32_16x16x32_bf16 v[108:111], a[8:11], v[196:199], v[108:111]\n" \
"s_waitcnt lgkmcnt(8)\n" \
"buffer_store_dwordx4 v[184:187], v250, s[32:35], s16 offen offset:3072 sc1\n" \
"v_mfma_f32_16x16x32_bf16 v[112:115], a[16:19], v[192:195], 0\n" \
"ds_read_b64_tr_b16 a[8:9], v236 offset:16384\n" \
"ds_read_b64_tr_b16 a[10:11], v236 offset:18432\n" \
"v_mfma_f32_16x16x32_bf16 v[112:115], a[20:23], v[200:203], v[112:115]\n" \
".Lst1_7_%=:\n" \
"s_cbranch_execz .Lst1_11_%=\n" \
"v_lshrrev_b32 v188, 2, v251\n" \
"v_lshlrev_b32 v188, 6, v188\n" \
"v_mfma_f32_16x16x32_bf16 v[120:123], a[20:23], v[204:207], 0\n" \
"ds_read_b64_tr_b16 a[12:13], v236 offset:24576\n" \
"ds_read_b64_tr_b16 a[14:15], v236 offset:26624\n" \
"v_mfma_f32_16x16x32_bf16 v[120:123], a[16:19], v[196:199], v[120:123]\n" \
"s_waitcnt lgkmcnt(8)\n" \
"buffer_load_dwordx4 a[240:243], v248, s[32:35], s38 offen offset:0 sc1\n" \
"v_and_b32 v189, 3, v251\n" \
"v_mfma_f32_16x16x32_bf16 v[116:119], a[24:27], v[192:195], 0\n" \
"ds_read_b64_tr_b16 a[16:17], v240 offset:16896\n" \
"ds_read_b64_tr_b16 a[18:19], v240 offset:18944\n" \
"v_mfma_f32_16x16x32_bf16 v[116:119], a[28:31], v[200:203], v[116:119]\n" \
"v_lshl_or_b32 v188, v189, 3, v188\n" \
"v_mfma_f32_16x16x32_bf16 v[124:127], a[28:31], v[204:207], 0\n" \
"s_lshl_b32 s19, %[wid], 6\n" \
"ds_read_b64_tr_b16 a[20:21], v240 offset:25088\n" \
"ds_read_b64_tr_b16 a[22:23], v240 offset:27136\n" \
"v_mfma_f32_16x16x32_bf16 v[124:127], a[24:27], v[196:199], v[124:127]\n" \
"s_waitcnt lgkmcnt(8)\n" \
"buffer_load_dwordx4 a[244:247], v248, s[32:35], s38 offen offset:1024 sc1\n" \
"v_add_u32 v191, s19, v251\n" \
"v_mfma_f32_16x16x32_bf16 v[64:67], a[0:3], v[208:211], v[64:67]\n" \
"ds_read_b64_tr_b16 a[24:25], v236 offset:16896\n" \
"ds_read_b64_tr_b16 a[26:27], v236 offset:18944\n" \
"v_mfma_f32_16x16x32_bf16 v[64:67], a[4:7], v[216:219], v[64:67]\n" \
"v_lshlrev_b32 v190, 3, v191\n" \
"v_mfma_f32_16x16x32_bf16 v[72:75], a[4:7], v[220:223], v[72:75]\n" \
"ds_read_b64_tr_b16 a[28:29], v236 offset:25088\n" \
"ds_read_b64_tr_b16 a[30:31], v236 offset:27136\n" \
"v_mfma_f32_16x16x32_bf16 v[72:75], a[0:3], v[212:215], v[72:75]\n" \
"s_waitcnt lgkmcnt(8)\n" \
"buffer_load_dwordx4 a[248:251], v248, s[32:35], s38 offen offset:2048 sc1\n" \
"v_mfma_f32_16x16x32_bf16 v[68:71], a[8:11], v[208:211], v[68:71]\n" \
"ds_read_b64_tr_b16 a[0:1], v240 offset:20480\n" \
"ds_read_b64_tr_b16 a[2:3], v240 offset:22528\n" \
"v_mfma_f32_16x16x32_bf16 v[68:71], a[12:15], v[216:219], v[68:71]\n" \
"v_mfma_f32_16x16x32_bf16 v[76:79], a[12:15], v[220:223], v[76:79]\n" \
"ds_read_b64_tr_b16 a[4:5], v240 offset:28672\n" \
"ds_read_b64_tr_b16 a[6:7], v240 offset:30720\n" \
"v_mfma_f32_16x16x32_bf16 v[76:79], a[8:11], v[212:215], v[76:79]\n" \
"s_waitcnt lgkmcnt(8)\n" \
"buffer_load_dwordx4 a[252:255], v248, s[32:35], s38 offen offset:3072 sc1\n" \
"v_mfma_f32_16x16x32_bf16 v[80:83], a[16:19], v[208:211], v[80:83]\n" \
"s_add_u32 s38, s66, 3\n" \
"ds_read_b64_tr_b16 a[8:9], v236 offset:20480\n" \
"ds_read_b64_tr_b16 a[10:11], v236 offset:22528\n" \
"v_mfma_f32_16x16x32_bf16 v[80:83], a[20:23], v[216:219], v[80:83]\n" \
"s_and_b32 s38, s38, 3\n" \
"s_lshl_b32 s38, s38, 13\n" \
"s_add_u32 s38, s38, s37\n" \
"v_mfma_f32_16x16x32_bf16 v[88:91], a[20:23], v[220:223], v[88:91]\n" \
"ds_read_b64_tr_b16 a[12:13], v236 offset:28672\n" \
"ds_read_b64_tr_b16 a[14:15], v236 offset:30720\n" \
"v_mfma_f32_16x16x32_bf16 v[88:91], a[16:19], v[212:215], v[88:91]\n" \
"s_waitcnt lgkmcnt(8)\n" \
"buffer_load_dwordx4 a[64:67], v248, s[32:35], s38 offen offset:0 sc1\n" \
"v_mfma_f32_16x16x32_bf16 v[84:87], a[24:27], v[208:211], v[84:87]\n" \
"ds_read_b64_tr_b16 a[16:17], v240 offset:20992\n" \
"ds_read_b64_tr_b16 a[18:19], v240 offset:23040\n" \
"v_mfma_f32_16x16x32_bf16 v[84:87], a[28:31], v[216:219], v[84:87]\n" \
"v_mfma_f32_16x16x32_bf16 v[92:95], a[28:31], v[220:223], v[92:95]\n" \
"ds_read_b64_tr_b16 a[20:21], v240 offset:29184\n" \
"ds_read_b64_tr_b16 a[22:23], v240 offset:31232\n" \
"v_mfma_f32_16x16x32_bf16 v[92:95], a[24:27], v[212:215], v[92:95]\n" \
"s_waitcnt lgkmcnt(8)\n" \
"buffer_load_dwordx4 a[68:71], v248, s[32:35], s38 offen offset:1024 sc1\n" \
"v_mfma_f32_16x16x32_bf16 v[96:99], a[0:3], v[208:211], v[96:99]\n" \
"ds_read_b64_tr_b16 a[24:25], v236 offset:20992\n" \
"ds_read_b64_tr_b16 a[26:27], v236 offset:23040\n" \
"v_mfma_f32_16x16x32_bf16 v[96:99], a[4:7], v[216:219], v[96:99]\n" \
"v_mfma_f32_16x16x32_bf16 v[104:107], a[4:7], v[220:223], v[104:107]\n" \
"ds_read_b64_tr_b16 a[28:29], v236 offset:29184\n" \
"ds_read_b64_tr_b16 a[30:31], v236 offset:31232\n" \
"v_mfma_f32_16x16x32_bf16 v[104:107], a[0:3], v[212:215], v[104:107]\n" \
"s_waitcnt lgkmcnt(8)\n" \
"buffer_load_dwordx4 a[72:75], v248, s[32:35], s38 offen offset:2048 sc1\n" \
"v_mfma_f32_16x16x32_bf16 v[100:103], a[8:11], v[208:211], v[100:103]\n" \
"ds_read_b64_tr_b16 a[0:1], v241 offset:0\n" \
"ds_read_b64_tr_b16 a[2:3], v241 offset:2048\n" \
"v_mfma_f32_16x16x32_bf16 v[100:103], a[12:15], v[216:219], v[100:103]\n" \
"v_mfma_f32_16x16x32_bf16 v[108:111], a[12:15], v[220:223], v[108:111]\n" \
"ds_read_b64_tr_b16 a[4:5], v241 offset:8192\n" \
"ds_read_b64_tr_b16 a[6:7], v241 offset:10240\n" \
"v_mfma_f32_16x16x32_bf16 v[108:111], a[8:11], v[212:215], v[108:111]\n" \
"s_waitcnt lgkmcnt(8)\n" \
"buffer_load_dwordx4 a[76:79], v248, s[32:35], s38 offen offset:3072 sc1\n" \
"v_mfma_f32_16x16x32_bf16 v[112:115], a[16:19], v[208:211], v[112:115]\n" \
"s_add_u32 s38, s38, 4096\n" \
"ds_read_b64_tr_b16 a[8:9], v235 offset:0\n" \
"ds_read_b64_tr_b16 a[10:11], v235 offset:2048\n" \
"v_mfma_f32_16x16x32_bf16 v[112:115], a[20:23], v[216:219], v[112:115]\n" \
"v_mfma_f32_16x16x32_bf16 v[120:123], a[20:23], v[220:223], v[120:123]\n" \
"ds_read_b64_tr_b16 a[12:13], v235 offset:8192\n" \
"ds_read_b64_tr_b16 a[14:15], v235 offset:10240\n" \
"v_mfma_f32_16x16x32_bf16 v[120:123], a[16:19], v[212:215], v[120:123]\n" \
"s_waitcnt lgkmcnt(8)\n" \
"buffer_load_dwordx4 a[80:83], v248, s[32:35], s38 offen offset:0 sc1\n" \
"v_mfma_f32_16x16x32_bf16 v[116:119], a[24:27], v[208:211], v[116:119]\n" \
"ds_read_b64_tr_b16 a[16:17], v241 offset:512\n" \
"ds_read_b64_tr_b16 a[18:19], v241 offset:2560\n" \
"v_mfma_f32_16x16x32_bf16 v[116:119], a[28:31], v[216:219], v[116:119]\n" \
"v_mfma_f32_16x16x32_bf16 v[124:127], a[28:31], v[220:223], v[124:127]\n" \
"s_waitcnt vmcnt(19)\n" \
"ds_read_b64_tr_b16 a[20:21], v241 offset:8704\n" \
"v_mfma_f32_16x16x32_bf16 v[124:127], a[24:27], v[212:215], v[124:127]\n" \
"s_waitcnt lgkmcnt(7)\n" \
"ds_read_b64_tr_b16 a[22:23], v241 offset:10752\n" \
"v_mfma_f32_16x16x32_bf16 v[64:67], a[0:3], a[192:195], v[64:67]\n" \
"ds_read_b64_tr_b16 a[24:25], v235 offset:512\n" \
"ds_read_b64_tr_b16 a[26:27], v235 offset:2560\n" \
"v_mfma_f32_16x16x32_bf16 v[64:67], a[4:7], a[200:203], v[64:67]\n" \
"buffer_load_dwordx4 a[84:87], v248, s[32:35], s38 offen offset:1024 sc1\n" \
"v_mfma_f32_16x16x32_bf16 v[72:75], a[4:7], a[204:207], v[72:75]\n" \
"ds_read_b64_tr_b16 a[28:29], v235 offset:8704\n" \
"ds_read_b64_tr_b16 a[30:31], v235 offset:10752\n" \
"v_mfma_f32_16x16x32_bf16 v[72:75], a[0:3], a[196:199], v[72:75]\n" \
"s_waitcnt vmcnt(10) lgkmcnt(8)\n" \
"buffer_store_dword v253, v246, s[32:35], s39 offen sc1\n" \
"v_mfma_f32_16x16x32_bf16 v[68:71], a[8:11], a[192:195], v[68:71]\n" \
"ds_read_b64_tr_b16 a[0:1], v241 offset:16384\n" \
"ds_read_b64_tr_b16 a[2:3], v241 offset:18432\n" \
"v_mfma_f32_16x16x32_bf16 v[68:71], a[12:15], a[200:203], v[68:71]\n" \
"v_mfma_f32_16x16x32_bf16 v[76:79], a[12:15], a[204:207], v[76:79]\n" \
"ds_read_b64_tr_b16 a[4:5], v241 offset:24576\n" \
"ds_read_b64_tr_b16 a[6:7], v241 offset:26624\n" \
"v_mfma_f32_16x16x32_bf16 v[76:79], a[8:11], a[196:199], v[76:79]\n" \
"s_waitcnt lgkmcnt(8)\n" \
"buffer_load_dwordx4 a[88:91], v248, s[32:35], s38 offen offset:2048 sc1\n" \
"v_lshrrev_b32 v214, 4, v251\n" \
"v_mfma_f32_16x16x32_bf16 v[80:83], a[16:19], a[192:195], v[80:83]\n" \
"ds_read_b64_tr_b16 a[8:9], v235 offset:16384\n" \
"ds_read_b64_tr_b16 a[10:11], v235 offset:18432\n" \
"v_mfma_f32_16x16x32_bf16 v[80:83], a[20:23], a[200:203], v[80:83]\n" \
"v_lshlrev_b32 v214, 13, v214\n" \
"v_and_or_b32 v214, v248, 48, v214\n" \
"v_mfma_f32_16x16x32_bf16 v[88:91], a[20:23], a[204:207], v[88:91]\n" \
"ds_read_b64_tr_b16 a[12:13], v235 offset:24576\n" \
"ds_read_b64_tr_b16 a[14:15], v235 offset:26624\n" \
"v_mfma_f32_16x16x32_bf16 v[88:91], a[16:19], a[196:199], v[88:91]\n" \
"s_waitcnt lgkmcnt(8)\n" \
"buffer_load_dwordx4 a[92:95], v248, s[32:35], s38 offen offset:3072 sc1\n" \
"v_bfe_u32 v208, v251, 2, 2\n" \
"v_mfma_f32_16x16x32_bf16 v[84:87], a[24:27], a[192:195], v[84:87]\n" \
"ds_read_b64_tr_b16 a[16:17], v241 offset:16896\n" \
"ds_read_b64_tr_b16 a[18:19], v241 offset:18944\n" \
"v_mfma_f32_16x16x32_bf16 v[84:87], a[28:31], a[200:203], v[84:87]\n" \
"v_lshl_or_b32 v214, v208, 10, v214\n" \
"v_mov_b32 v208, 0\n" \
"v_mfma_f32_16x16x32_bf16 v[92:95], a[28:31], a[204:207], v[92:95]\n" \
"ds_read_b64_tr_b16 a[20:21], v241 offset:25088\n" \
"ds_read_b64_tr_b16 a[22:23], v241 offset:27136\n" \
"v_mfma_f32_16x16x32_bf16 v[92:95], a[24:27], a[196:199], v[92:95]\n" \
"s_waitcnt lgkmcnt(8)\n" \
"buffer_load_dwordx4 v[172:175], v248, s[32:35], 0 offen offset:0 sc1\n" \
"v_mov_b32 v209, 0\n" \
"v_mfma_f32_16x16x32_bf16 v[64:67], a[0:3], a[208:211], v[64:67]\n" \
"ds_read_b64_tr_b16 a[24:25], v235 offset:16896\n" \
"ds_read_b64_tr_b16 a[26:27], v235 offset:18944\n" \
"v_mfma_f32_16x16x32_bf16 v[64:67], a[4:7], a[216:219], v[64:67]\n" \
"v_mov_b32 v212, 0\n" \
"v_mov_b32 v213, 0\n" \
"v_mfma_f32_16x16x32_bf16 v[72:75], a[4:7], a[220:223], v[72:75]\n" \
"ds_read_b64_tr_b16 a[28:29], v235 offset:25088\n" \
"ds_read_b64_tr_b16 a[30:31], v235 offset:27136\n" \
"v_mfma_f32_16x16x32_bf16 v[72:75], a[0:3], a[212:215], v[72:75]\n" \
"s_waitcnt lgkmcnt(8)\n" \
"buffer_load_dwordx4 v[176:179], v248, s[32:35], 0 offen offset:1024 sc1\n" \
"v_bfe_u32 v210, v251, 2, 2\n" \
"v_mfma_f32_16x16x32_bf16 v[68:71], a[8:11], a[208:211], v[68:71]\n" \
"ds_read_b64_tr_b16 a[0:1], v244 offset:0\n" \
"ds_read_b64_tr_b16 a[2:3], v244 offset:2048\n" \
"v_mfma_f32_16x16x32_bf16 v[68:71], a[12:15], a[216:219], v[68:71]\n" \
"v_lshrrev_b32 v211, 4, v251\n" \
"v_cmp_eq_u32 s[24:25], v210, v211\n" \
"v_mfma_f32_16x16x32_bf16 v[76:79], a[12:15], a[220:223], v[76:79]\n" \
"ds_read_b64_tr_b16 a[4:5], v244 offset:8192\n" \
"ds_read_b64_tr_b16 a[6:7], v244 offset:10240\n" \
"v_mfma_f32_16x16x32_bf16 v[76:79], a[8:11], a[212:215], v[76:79]\n" \
"s_waitcnt lgkmcnt(8)\n" \
"buffer_load_dwordx4 v[180:183], v248, s[32:35], 0 offen offset:2048 sc1\n" \
"v_and_b32 v210, 1, v251\n" \
"v_mfma_f32_16x16x32_bf16 v[80:83], a[16:19], a[208:211], v[80:83]\n" \
"ds_read_b64_tr_b16 a[8:9], v234 offset:0\n" \
"ds_read_b64_tr_b16 a[10:11], v234 offset:2048\n" \
"v_mfma_f32_16x16x32_bf16 v[80:83], a[20:23], a[216:219], v[80:83]\n" \
"v_lshlrev_b32 v210, 4, v210\n" \
"v_mov_b32 v211, 0x3f80\n" \
"v_mfma_f32_16x16x32_bf16 v[88:91], a[20:23], a[220:223], v[88:91]\n" \
"ds_read_b64_tr_b16 a[12:13], v234 offset:8192\n" \
"ds_read_b64_tr_b16 a[14:15], v234 offset:10240\n" \
"v_mfma_f32_16x16x32_bf16 v[88:91], a[16:19], a[212:215], v[88:91]\n" \
"s_waitcnt lgkmcnt(8)\n" \
"buffer_load_dwordx4 v[184:187], v248, s[32:35], 0 offen offset:3072 sc1\n" \
"v_lshlrev_b32 v210, v210, v211\n" \
"v_mfma_f32_16x16x32_bf16 v[84:87], a[24:27], a[208:211], v[84:87]\n" \
"ds_read_b64_tr_b16 a[16:17], v244 offset:512\n" \
"ds_read_b64_tr_b16 a[18:19], v244 offset:2560\n" \
"v_mfma_f32_16x16x32_bf16 v[84:87], a[28:31], a[216:219], v[84:87]\n" \
"v_cndmask_b32 v210, 0, v210, s[24:25]\n" \
"v_bfe_i32 v215, v251, 1, 1\n" \
"v_mfma_f32_16x16x32_bf16 v[92:95], a[28:31], a[220:223], v[92:95]\n" \
"ds_read_b64_tr_b16 a[20:21], v244 offset:8704\n" \
"ds_read_b64_tr_b16 a[22:23], v244 offset:10752\n" \
"v_mfma_f32_16x16x32_bf16 v[92:95], a[24:27], a[212:215], v[92:95]\n" \
"s_waitcnt lgkmcnt(8)\n" \
"global_load_dwordx2 v[168:169], v188, s[96:97] offset:0\n" \
"v_and_b32 v211, v215, v210\n" \
"v_mfma_f32_16x16x32_bf16 v[64:67], a[0:3], a[224:227], v[64:67]\n" \
"ds_read_b64_tr_b16 a[24:25], v234 offset:512\n" \
"ds_read_b64_tr_b16 a[26:27], v234 offset:2560\n" \
"v_mfma_f32_16x16x32_bf16 v[64:67], a[4:7], a[232:235], v[64:67]\n" \
"v_xor_b32 v210, v210, v211\n" \
"v_mfma_f32_16x16x32_bf16 v[72:75], a[4:7], a[236:239], v[72:75]\n" \
"ds_read_b64_tr_b16 a[28:29], v234 offset:8704\n" \
"ds_read_b64_tr_b16 a[30:31], v234 offset:10752\n" \
"v_mfma_f32_16x16x32_bf16 v[72:75], a[0:3], a[228:231], v[72:75]\n" \
"s_waitcnt lgkmcnt(8)\n" \
"global_load_dwordx2 v[170:171], v188, s[96:97] offset:32\n" \
"v_mfma_f32_16x16x32_bf16 v[68:71], a[8:11], a[224:227], v[68:71]\n" \
"ds_read_b64_tr_b16 a[0:1], v249 offset:0\n" \
"ds_read_b64_tr_b16 a[2:3], v249 offset:2048\n" \
"v_mfma_f32_16x16x32_bf16 v[68:71], a[12:15], a[232:235], v[68:71]\n" \
"v_mfma_f32_16x16x32_bf16 v[76:79], a[12:15], a[236:239], v[76:79]\n" \
"ds_read_b64_tr_b16 a[4:5], v249 offset:8192\n" \
"ds_read_b64_tr_b16 a[6:7], v249 offset:10240\n" \
"v_mfma_f32_16x16x32_bf16 v[76:79], a[8:11], a[228:231], v[76:79]\n" \
"s_waitcnt lgkmcnt(8)\n" \
"v_mfma_f32_16x16x32_bf16 v[80:83], a[16:19], a[224:227], v[80:83]\n" \
"ds_read_b64_tr_b16 a[8:9], v233 offset:0\n" \
"ds_read_b64_tr_b16 a[10:11], v233 offset:2048\n" \
"v_mfma_f32_16x16x32_bf16 v[80:83], a[20:23], a[232:235], v[80:83]\n" \
"v_mfma_f32_16x16x32_bf16 v[88:91], a[20:23], a[236:239], v[88:91]\n" \
"ds_read_b64_tr_b16 a[12:13], v233 offset:8192\n" \
"ds_read_b64_tr_b16 a[14:15], v233 offset:10240\n" \
"v_mfma_f32_16x16x32_bf16 v[88:91], a[16:19], a[228:231], v[88:91]\n" \
"s_waitcnt lgkmcnt(8)\n" \
"v_mfma_f32_16x16x32_bf16 v[84:87], a[24:27], a[224:227], v[84:87]\n" \
"ds_read_b64_tr_b16 a[16:17], v249 offset:512\n" \
"ds_read_b64_tr_b16 a[18:19], v249 offset:2560\n" \
"v_mfma_f32_16x16x32_bf16 v[84:87], a[28:31], a[232:235], v[84:87]\n" \
"v_mfma_f32_16x16x32_bf16 v[92:95], a[28:31], a[236:239], v[92:95]\n" \
"s_waitcnt vmcnt(15)\n" \
"ds_read_b64_tr_b16 a[20:21], v249 offset:8704\n" \
"v_mfma_f32_16x16x32_bf16 v[92:95], a[24:27], a[228:231], v[92:95]\n" \
"s_waitcnt lgkmcnt(7)\n" \
"ds_read_b64_tr_b16 a[22:23], v249 offset:10752\n" \
"v_mfma_f32_16x16x32_bf16 v[64:67], a[0:3], a[240:243], v[64:67]\n" \
"ds_read_b64_tr_b16 a[24:25], v233 offset:512\n" \
"ds_read_b64_tr_b16 a[26:27], v233 offset:2560\n" \
"v_mfma_f32_16x16x32_bf16 v[64:67], a[4:7], a[248:251], v[64:67]\n" \
"v_mfma_f32_16x16x32_bf16 v[72:75], a[4:7], a[252:255], v[72:75]\n" \
"ds_read_b64_tr_b16 a[28:29], v233 offset:8704\n" \
"ds_read_b64_tr_b16 a[30:31], v233 offset:10752\n" \
"v_mfma_f32_16x16x32_bf16 v[72:75], a[0:3], a[244:247], v[72:75]\n" \
"s_waitcnt lgkmcnt(8)\n" \
"v_mfma_f32_16x16x32_bf16 v[68:71], a[8:11], a[240:243], v[68:71]\n" \
"ds_read_b64_tr_b16 a[0:1], v239 offset:0\n" \
"ds_read_b64_tr_b16 a[2:3], v239 offset:2048\n" \
"v_mfma_f32_16x16x32_bf16 v[68:71], a[12:15], a[248:251], v[68:71]\n" \
"v_mfma_f32_16x16x32_bf16 v[76:79], a[12:15], a[252:255], v[76:79]\n" \
"ds_read_b64_tr_b16 a[4:5], v239 offset:8192\n" \
"ds_read_b64_tr_b16 a[6:7], v239 offset:10240\n" \
"v_mfma_f32_16x16x32_bf16 v[76:79], a[8:11], a[244:247], v[76:79]\n" \
"s_waitcnt lgkmcnt(8)\n" \
"v_mfma_f32_16x16x32_bf16 v[80:83], a[16:19], a[240:243], v[80:83]\n" \
"ds_read_b64_tr_b16 a[8:9], v232 offset:0\n" \
"ds_read_b64_tr_b16 a[10:11], v232 offset:2048\n" \
"v_mfma_f32_16x16x32_bf16 v[80:83], a[20:23], a[248:251], v[80:83]\n" \
"v_mfma_f32_16x16x32_bf16 v[88:91], a[20:23], a[252:255], v[88:91]\n" \
"ds_read_b64_tr_b16 a[12:13], v232 offset:8192\n" \
"ds_read_b64_tr_b16 a[14:15], v232 offset:10240\n" \
"v_mfma_f32_16x16x32_bf16 v[88:91], a[16:19], a[244:247], v[88:91]\n" \
"s_waitcnt lgkmcnt(8)\n" \
"v_mfma_f32_16x16x32_bf16 v[84:87], a[24:27], a[240:243], v[84:87]\n" \
"ds_read_b64_tr_b16 a[16:17], v239 offset:512\n" \
"ds_read_b64_tr_b16 a[18:19], v239 offset:2560\n" \
"v_mfma_f32_16x16x32_bf16 v[84:87], a[28:31], a[248:251], v[84:87]\n" \
"v_mfma_f32_16x16x32_bf16 v[92:95], a[28:31], a[252:255], v[92:95]\n" \
"s_waitcnt vmcnt(11)\n" \
"ds_read_b64_tr_b16 a[20:21], v239 offset:8704\n" \
"v_mfma_f32_16x16x32_bf16 v[92:95], a[24:27], a[244:247], v[92:95]\n" \
"s_waitcnt lgkmcnt(7)\n" \
"ds_read_b64_tr_b16 a[22:23], v239 offset:10752\n" \
"v_mfma_f32_16x16x32_bf16 v[64:67], a[0:3], a[64:67], v[64:67]\n" \
"ds_read_b64_tr_b16 a[24:25], v232 offset:512\n" \
"ds_read_b64_tr_b16 a[26:27], v232 offset:2560\n" \
"v_mfma_f32_16x16x32_bf16 v[64:67], a[4:7], a[72:75], v[64:67]\n" \
"v_mfma_f32_16x16x32_bf16 v[72:75], a[4:7], a[76:79], v[72:75]\n" \
"ds_read_b64_tr_b16 a[28:29], v232 offset:8704\n" \
"ds_read_b64_tr_b16 a[30:31], v232 offset:10752\n" \
"v_mfma_f32_16x16x32_bf16 v[72:75], a[0:3], a[68:71], v[72:75]\n" \
"s_waitcnt lgkmcnt(8)\n" \
"v_mfma_f32_16x16x32_bf16 v[68:71], a[8:11], a[64:67], v[68:71]\n" \
"ds_read_b64_tr_b16 a[0:1], v239 offset:16384\n" \
"ds_read_b64_tr_b16 a[2:3], v239 offset:18432\n" \
"v_mfma_f32_16x16x32_bf16 v[68:71], a[12:15], a[72:75], v[68:71]\n" \
"v_mfma_f32_16x16x32_bf16 v[76:79], a[12:15], a[76:79], v[76:79]\n" \
"ds_read_b64_tr_b16 a[4:5], v239 offset:24576\n" \
"ds_read_b64_tr_b16 a[6:7], v239 offset:26624\n" \
"v_mfma_f32_16x16x32_bf16 v[76:79], a[8:11], a[68:71], v[76:79]\n" \
"s_waitcnt lgkmcnt(8)\n" \
"v_mfma_f32_16x16x32_bf16 v[80:83], a[16:19], a[64:67], v[80:83]\n" \
"ds_read_b64_tr_b16 a[8:9], v232 offset:16384\n" \
"ds_read_b64_tr_b16 a[10:11], v232 offset:18432\n" \
"v_mfma_f32_16x16x32_bf16 v[80:83], a[20:23], a[72:75], v[80:83]\n" \
"v_mfma_f32_16x16x32_bf16 v[88:91], a[20:23], a[76:79], v[88:91]\n" \
"ds_read_b64_tr_b16 a[12:13], v232 offset:24576\n" \
"ds_read_b64_tr_b16 a[14:15], v232 offset:26624\n" \
"v_mfma_f32_16x16x32_bf16 v[88:91], a[16:19], a[68:71], v[88:91]\n" \
"s_waitcnt lgkmcnt(8)\n" \
"s_lshl2_add_u32 s74, s67, %[wid]\n" \
"s_lshl_b32 s74, s74, 7\n" \
"v_mfma_f32_16x16x32_bf16 v[84:87], a[24:27], a[64:67], v[84:87]\n" \
"s_add_u32 s74, s74, 94208\n" \
"ds_read_b64_tr_b16 a[16:17], v239 offset:16896\n" \
"ds_read_b64_tr_b16 a[18:19], v239 offset:18944\n" \
"v_mfma_f32_16x16x32_bf16 v[84:87], a[28:31], a[72:75], v[84:87]\n" \
"v_mov_b32 v238, 1\n" \
"v_mfma_f32_16x16x32_bf16 v[92:95], a[28:31], a[76:79], v[92:95]\n" \
"s_waitcnt vmcnt(6)\n" \
"ds_read_b64_tr_b16 a[20:21], v239 offset:25088\n" \
"v_mfma_f32_16x16x32_bf16 v[92:95], a[24:27], a[68:71], v[92:95]\n" \
"s_waitcnt lgkmcnt(7)\n" \
"ds_read_b64_tr_b16 a[22:23], v239 offset:27136\n" \
"v_mfma_f32_16x16x32_bf16 v[64:67], a[0:3], a[80:83], v[64:67]\n" \
"ds_read_b64_tr_b16 a[24:25], v232 offset:16896\n" \
"ds_read_b64_tr_b16 a[26:27], v232 offset:18944\n" \
"v_mfma_f32_16x16x32_bf16 v[64:67], a[4:7], a[88:91], v[64:67]\n" \
"v_mfma_f32_16x16x32_bf16 v[72:75], a[4:7], a[92:95], v[72:75]\n" \
"ds_read_b64_tr_b16 a[28:29], v232 offset:25088\n" \
"ds_read_b64_tr_b16 a[30:31], v232 offset:27136\n" \
"v_mfma_f32_16x16x32_bf16 v[72:75], a[0:3], a[84:87], v[72:75]\n" \
"s_waitcnt lgkmcnt(8)\n" \
"v_mfma_f32_16x16x32_bf16 v[68:71], a[8:11], a[80:83], v[68:71]\n" \
"ds_read_b64_tr_b16 a[0:1], v241 offset:4096\n" \
"ds_read_b64_tr_b16 a[2:3], v241 offset:6144\n" \
"v_mfma_f32_16x16x32_bf16 v[68:71], a[12:15], a[88:91], v[68:71]\n" \
"v_mfma_f32_16x16x32_bf16 v[76:79], a[12:15], a[92:95], v[76:79]\n" \
"ds_read_b64_tr_b16 a[4:5], v241 offset:12288\n" \
"ds_read_b64_tr_b16 a[6:7], v241 offset:14336\n" \
"v_mfma_f32_16x16x32_bf16 v[76:79], a[8:11], a[84:87], v[76:79]\n" \
"s_waitcnt lgkmcnt(8)\n" \
"v_mfma_f32_16x16x32_bf16 v[80:83], a[16:19], a[80:83], v[80:83]\n" \
"ds_read_b64_tr_b16 a[8:9], v235 offset:4096\n" \
"ds_read_b64_tr_b16 a[10:11], v235 offset:6144\n" \
"v_mfma_f32_16x16x32_bf16 v[80:83], a[20:23], a[88:91], v[80:83]\n" \
"v_mfma_f32_16x16x32_bf16 v[88:91], a[20:23], a[92:95], v[88:91]\n" \
"ds_read_b64_tr_b16 a[12:13], v235 offset:12288\n" \
"ds_read_b64_tr_b16 a[14:15], v235 offset:14336\n" \
"v_mfma_f32_16x16x32_bf16 v[88:91], a[16:19], a[84:87], v[88:91]\n" \
"s_waitcnt lgkmcnt(8)\n" \
"v_mfma_f32_16x16x32_bf16 v[84:87], a[24:27], a[80:83], v[84:87]\n" \
"ds_read_b64_tr_b16 a[16:17], v241 offset:20480\n" \
"ds_read_b64_tr_b16 a[18:19], v241 offset:22528\n" \
"v_mfma_f32_16x16x32_bf16 v[84:87], a[28:31], a[88:91], v[84:87]\n" \
"v_cvt_pk_bf16_f32 v64, v64, v65\n" \
"v_cvt_pk_bf16_f32 v65, v66, v67\n" \
"v_mfma_f32_16x16x32_bf16 v[92:95], a[28:31], a[92:95], v[92:95]\n" \
"ds_read_b64_tr_b16 a[20:21], v241 offset:28672\n" \
"ds_read_b64_tr_b16 a[22:23], v241 offset:30720\n" \
"v_mfma_f32_16x16x32_bf16 v[92:95], a[24:27], a[84:87], v[92:95]\n" \
"s_waitcnt lgkmcnt(8)\n" \
"v_cvt_pk_bf16_f32 v66, v68, v69\n" \
"v_cvt_pk_bf16_f32 v67, v70, v71\n" \
"v_mfma_f32_16x16x32_bf16 v[96:99], a[0:3], a[192:195], v[96:99]\n" \
"ds_read_b64_tr_b16 a[24:25], v235 offset:20480\n" \
"ds_read_b64_tr_b16 a[26:27], v235 offset:22528\n" \
"v_mfma_f32_16x16x32_bf16 v[96:99], a[4:7], a[200:203], v[96:99]\n" \
"s_mov_b64 exec, 1\n" \
"buffer_atomic_add v238, off, s[32:35], s74 sc0 sc1\n" \
"s_mov_b64 exec, -1\n" \
"v_mfma_f32_16x16x32_bf16 v[104:107], a[4:7], a[204:207], v[104:107]\n" \
"ds_read_b64_tr_b16 a[28:29], v235 offset:28672\n" \
"ds_read_b64_tr_b16 a[30:31], v235 offset:30720\n" \
"v_mfma_f32_16x16x32_bf16 v[104:107], a[0:3], a[196:199], v[104:107]\n" \
"s_waitcnt lgkmcnt(8)\n" \
"buffer_load_dword v245, v255, s[32:35], s18 offen sc1\n" \
"v_mfma_f32_16x16x32_bf16 v[100:103], a[8:11], a[192:195], v[100:103]\n" \
"ds_read_b64_tr_b16 a[0:1], v244 offset:4096\n" \
"ds_read_b64_tr_b16 a[2:3], v244 offset:6144\n" \
"v_mfma_f32_16x16x32_bf16 v[100:103], a[12:15], a[200:203], v[100:103]\n" \
"buffer_store_dwordx4 v[64:67], v250, s[32:35], s36 offen offset:0 sc1\n" \
"v_mfma_f32_16x16x32_bf16 v[108:111], a[12:15], a[204:207], v[108:111]\n" \
"ds_read_b64_tr_b16 a[4:5], v244 offset:12288\n" \
"ds_read_b64_tr_b16 a[6:7], v244 offset:14336\n" \
"v_mfma_f32_16x16x32_bf16 v[108:111], a[8:11], a[196:199], v[108:111]\n" \
"s_waitcnt lgkmcnt(8)\n" \
"v_cvt_pk_bf16_f32 v72, v72, v73\n" \
"v_cvt_pk_bf16_f32 v73, v74, v75\n" \
"v_mfma_f32_16x16x32_bf16 v[96:99], a[16:19], a[208:211], v[96:99]\n" \
"ds_read_b64_tr_b16 a[8:9], v234 offset:4096\n" \
"ds_read_b64_tr_b16 a[10:11], v234 offset:6144\n" \
"v_mfma_f32_16x16x32_bf16 v[96:99], a[20:23], a[216:219], v[96:99]\n" \
"v_cvt_pk_bf16_f32 v74, v76, v77\n" \
"v_cvt_pk_bf16_f32 v75, v78, v79\n" \
"v_mfma_f32_16x16x32_bf16 v[104:107], a[20:23], a[220:223], v[104:107]\n" \
"ds_read_b64_tr_b16 a[12:13], v234 offset:12288\n" \
"ds_read_b64_tr_b16 a[14:15], v234 offset:14336\n" \
"v_mfma_f32_16x16x32_bf16 v[104:107], a[16:19], a[212:215], v[104:107]\n" \
"s_waitcnt lgkmcnt(8)\n" \
"v_mfma_f32_16x16x32_bf16 v[100:103], a[24:27], a[208:211], v[100:103]\n" \
"ds_read_b64_tr_b16 a[16:17], v249 offset:4096\n" \
"ds_read_b64_tr_b16 a[18:19], v249 offset:6144\n" \
"v_mfma_f32_16x16x32_bf16 v[100:103], a[28:31], a[216:219], v[100:103]\n" \
"buffer_store_dwordx4 v[72:75], v250, s[32:35], s21 offen offset:0 sc1\n" \
"v_mfma_f32_16x16x32_bf16 v[108:111], a[28:31], a[220:223], v[108:111]\n" \
"ds_read_b64_tr_b16 a[20:21], v249 offset:12288\n" \
"ds_read_b64_tr_b16 a[22:23], v249 offset:14336\n" \
"v_mfma_f32_16x16x32_bf16 v[108:111], a[24:27], a[212:215], v[108:111]\n" \
"s_waitcnt lgkmcnt(8)\n" \
"v_cvt_pk_bf16_f32 v80, v80, v81\n" \
"v_cvt_pk_bf16_f32 v81, v82, v83\n" \
"v_mfma_f32_16x16x32_bf16 v[96:99], a[0:3], a[224:227], v[96:99]\n" \
"ds_read_b64_tr_b16 a[24:25], v233 offset:4096\n" \
"ds_read_b64_tr_b16 a[26:27], v233 offset:6144\n" \
"v_mfma_f32_16x16x32_bf16 v[96:99], a[4:7], a[232:235], v[96:99]\n" \
"v_cvt_pk_bf16_f32 v82, v84, v85\n" \
"v_cvt_pk_bf16_f32 v83, v86, v87\n" \
"v_mfma_f32_16x16x32_bf16 v[104:107], a[4:7], a[236:239], v[104:107]\n" \
"ds_read_b64_tr_b16 a[28:29], v233 offset:12288\n" \
"ds_read_b64_tr_b16 a[30:31], v233 offset:14336\n" \
"v_mfma_f32_16x16x32_bf16 v[104:107], a[0:3], a[228:231], v[104:107]\n" \
"s_waitcnt lgkmcnt(8)\n" \
"v_mfma_f32_16x16x32_bf16 v[100:103], a[8:11], a[224:227], v[100:103]\n" \
"ds_read_b64_tr_b16 a[0:1], v239 offset:4096\n" \
"ds_read_b64_tr_b16 a[2:3], v239 offset:6144\n" \
"v_mfma_f32_16x16x32_bf16 v[100:103], a[12:15], a[232:235], v[100:103]\n" \
"v_mfma_f32_16x16x32_bf16 v[108:111], a[12:15], a[236:239], v[108:111]\n" \
"ds_read_b64_tr_b16 a[4:5], v239 offset:12288\n" \
"ds_read_b64_tr_b16 a[6:7], v239 offset:14336\n" \
"v_mfma_f32_16x16x32_bf16 v[108:111], a[8:11], a[228:231], v[108:111]\n" \
"s_waitcnt lgkmcnt(8)\n" \
"buffer_store_dwordx4 v[80:83], v250, s[32:35], s36 offen offset:1024 sc1\n" \
"v_mfma_f32_16x16x32_bf16 v[96:99], a[16:19], a[240:243], v[96:99]\n" \
"ds_read_b64_tr_b16 a[8:9], v232 offset:4096\n" \
"ds_read_b64_tr_b16 a[10:11], v232 offset:6144\n" \
"v_mfma_f32_16x16x32_bf16 v[96:99], a[20:23], a[248:251], v[96:99]\n" \
"v_cvt_pk_bf16_f32 v88, v88, v89\n" \
"v_cvt_pk_bf16_f32 v89, v90, v91\n" \
"v_mfma_f32_16x16x32_bf16 v[104:107], a[20:23], a[252:255], v[104:107]\n" \
"ds_read_b64_tr_b16 a[12:13], v232 offset:12288\n" \
"ds_read_b64_tr_b16 a[14:15], v232 offset:14336\n" \
"v_mfma_f32_16x16x32_bf16 v[104:107], a[16:19], a[244:247], v[104:107]\n" \
"s_waitcnt lgkmcnt(8)\n" \
"v_cvt_pk_bf16_f32 v90, v92, v93\n" \
"v_cvt_pk_bf16_f32 v91, v94, v95\n" \
"v_mfma_f32_16x16x32_bf16 v[100:103], a[24:27], a[240:243], v[100:103]\n" \
"ds_read_b64_tr_b16 a[16:17], v239 offset:20480\n" \
"ds_read_b64_tr_b16 a[18:19], v239 offset:22528\n" \
"v_mfma_f32_16x16x32_bf16 v[100:103], a[28:31], a[248:251], v[100:103]\n" \
"v_mfma_f32_16x16x32_bf16 v[108:111], a[28:31], a[252:255], v[108:111]\n" \
"ds_read_b64_tr_b16 a[20:21], v239 offset:28672\n" \
"ds_read_b64_tr_b16 a[22:23], v239 offset:30720\n" \
"v_mfma_f32_16x16x32_bf16 v[108:111], a[24:27], a[244:247], v[108:111]\n" \
"s_waitcnt lgkmcnt(8)\n" \
"v_mfma_f32_16x16x32_bf16 v[96:99], a[0:3], a[64:67], v[96:99]\n" \
"ds_read_b64_tr_b16 a[24:25], v232 offset:20480\n" \
"ds_read_b64_tr_b16 a[26:27], v232 offset:22528\n" \
"v_mfma_f32_16x16x32_bf16 v[96:99], a[4:7], a[72:75], v[96:99]\n" \
"buffer_store_dwordx4 v[88:91], v250, s[32:35], s21 offen offset:1024 sc1\n" \
"v_mfma_f32_16x16x32_bf16 v[104:107], a[4:7], a[76:79], v[104:107]\n" \
"ds_read_b64_tr_b16 a[28:29], v232 offset:28672\n" \
"ds_read_b64_tr_b16 a[30:31], v232 offset:30720\n" \
"v_mfma_f32_16x16x32_bf16 v[104:107], a[0:3], a[68:71], v[104:107]\n" \
"s_waitcnt lgkmcnt(8)\n" \
"v_mfma_f32_16x16x32_bf16 v[100:103], a[8:11], a[64:67], v[100:103]\n" \
"ds_read_b64_tr_b16 a[0:1], v241 offset:4608\n" \
"ds_read_b64_tr_b16 a[2:3], v241 offset:6656\n" \
"v_mfma_f32_16x16x32_bf16 v[100:103], a[12:15], a[72:75], v[100:103]\n" \
"v_mfma_f32_16x16x32_bf16 v[108:111], a[12:15], a[76:79], v[108:111]\n" \
"ds_read_b64_tr_b16 a[4:5], v241 offset:12800\n" \
"ds_read_b64_tr_b16 a[6:7], v241 offset:14848\n" \
"v_mfma_f32_16x16x32_bf16 v[108:111], a[8:11], a[68:71], v[108:111]\n" \
"s_waitcnt lgkmcnt(8)\n" \
"v_mfma_f32_16x16x32_bf16 v[96:99], a[16:19], a[80:83], v[96:99]\n" \
"ds_read_b64_tr_b16 a[8:9], v235 offset:4608\n" \
"ds_read_b64_tr_b16 a[10:11], v235 offset:6656\n" \
"v_mfma_f32_16x16x32_bf16 v[96:99], a[20:23], a[88:91], v[96:99]\n" \
"v_mfma_f32_16x16x32_bf16 v[104:107], a[20:23], a[92:95], v[104:107]\n" \
"ds_read_b64_tr_b16 a[12:13], v235 offset:12800\n" \
"ds_read_b64_tr_b16 a[14:15], v235 offset:14848\n" \
"v_mfma_f32_16x16x32_bf16 v[104:107], a[16:19], a[84:87], v[104:107]\n" \
"s_waitcnt lgkmcnt(8)\n" \
"v_mfma_f32_16x16x32_bf16 v[100:103], a[24:27], a[80:83], v[100:103]\n" \
"ds_read_b64_tr_b16 a[16:17], v241 offset:20992\n" \
"ds_read_b64_tr_b16 a[18:19], v241 offset:23040\n" \
"v_mfma_f32_16x16x32_bf16 v[100:103], a[28:31], a[88:91], v[100:103]\n" \
"v_mfma_f32_16x16x32_bf16 v[108:111], a[28:31], a[92:95], v[108:111]\n" \
"ds_read_b64_tr_b16 a[20:21], v241 offset:29184\n" \
"ds_read_b64_tr_b16 a[22:23], v241 offset:31232\n" \
"v_mfma_f32_16x16x32_bf16 v[108:111], a[24:27], a[84:87], v[108:111]\n" \
"s_waitcnt vmcnt(6) lgkmcnt(8)\n" \
"v_lshlrev_b32 v136, 16, v172\n" \
"v_mfma_f32_16x16x32_bf16 v[112:115], a[0:3], a[192:195], v[112:115]\n" \
"ds_read_b64_tr_b16 a[24:25], v235 offset:20992\n" \
"ds_read_b64_tr_b16 a[26:27], v235 offset:23040\n" \
"v_mfma_f32_16x16x32_bf16 v[112:115], a[4:7], a[200:203], v[112:115]\n" \
"v_and_b32 v137, 0xffff0000, v172\n" \
"v_lshlrev_b32 v138, 16, v173\n" \
"v_mfma_f32_16x16x32_bf16 v[120:123], a[4:7], a[204:207], v[120:123]\n" \
"ds_read_b64_tr_b16 a[28:29], v235 offset:29184\n" \
"ds_read_b64_tr_b16 a[30:31], v235 offset:31232\n" \
"v_mfma_f32_16x16x32_bf16 v[120:123], a[0:3], a[196:199], v[120:123]\n" \
"s_waitcnt lgkmcnt(8)\n" \
"v_and_b32 v139, 0xffff0000, v173\n" \
"v_lshlrev_b32 v140, 16, v174\n" \
"v_mfma_f32_16x16x32_bf16 v[116:119], a[8:11], a[192:195], v[116:119]\n" \
"ds_read_b64_tr_b16 a[0:1], v244 offset:4608\n" \
"ds_read_b64_tr_b16 a[2:3], v244 offset:6656\n" \
"v_mfma_f32_16x16x32_bf16 v[116:119], a[12:15], a[200:203], v[116:119]\n" \
"v_cvt_pk_bf16_f32 v96, v96, v97\n" \
"v_cvt_pk_bf16_f32 v97, v98, v99\n" \
"v_mfma_f32_16x16x32_bf16 v[124:127], a[12:15], a[204:207], v[124:127]\n" \
"ds_read_b64_tr_b16 a[4:5], v244 offset:12800\n" \
"ds_read_b64_tr_b16 a[6:7], v244 offset:14848\n" \
"v_mfma_f32_16x16x32_bf16 v[124:127], a[8:11], a[196:199], v[124:127]\n" \
"s_waitcnt lgkmcnt(8)\n" \
"v_cvt_pk_bf16_f32 v98, v100, v101\n" \
"v_cvt_pk_bf16_f32 v99, v102, v103\n" \
"v_mfma_f32_16x16x32_bf16 v[112:115], a[16:19], a[208:211], v[112:115]\n" \
"ds_read_b64_tr_b16 a[8:9], v234 offset:4608\n" \
"ds_read_b64_tr_b16 a[10:11], v234 offset:6656\n" \
"v_mfma_f32_16x16x32_bf16 v[112:115], a[20:23], a[216:219], v[112:115]\n" \
"v_and_b32 v141, 0xffff0000, v174\n" \
"v_lshlrev_b32 v142, 16, v175\n" \
"v_mfma_f32_16x16x32_bf16 v[120:123], a[20:23], a[220:223], v[120:123]\n" \
"ds_read_b64_tr_b16 a[12:13], v234 offset:12800\n" \
"ds_read_b64_tr_b16 a[14:15], v234 offset:14848\n" \
"v_mfma_f32_16x16x32_bf16 v[120:123], a[16:19], a[212:215], v[120:123]\n" \
"s_waitcnt lgkmcnt(8)\n" \
"buffer_store_dwordx4 v[96:99], v250, s[32:35], s36 offen offset:2048 sc1\n" \
"v_mfma_f32_16x16x32_bf16 v[116:119], a[24:27], a[208:211], v[116:119]\n" \
"ds_read_b64_tr_b16 a[16:17], v249 offset:4608\n" \
"ds_read_b64_tr_b16 a[18:19], v249 offset:6656\n" \
"v_mfma_f32_16x16x32_bf16 v[116:119], a[28:31], a[216:219], v[116:119]\n" \
"v_cvt_pk_bf16_f32 v104, v104, v105\n" \
"v_cvt_pk_bf16_f32 v105, v106, v107\n" \
"v_mfma_f32_16x16x32_bf16 v[124:127], a[28:31], a[220:223], v[124:127]\n" \
"ds_read_b64_tr_b16 a[20:21], v249 offset:12800\n" \
"ds_read_b64_tr_b16 a[22:23], v249 offset:14848\n" \
"v_mfma_f32_16x16x32_bf16 v[124:127], a[24:27], a[212:215], v[124:127]\n" \
"s_waitcnt lgkmcnt(8)\n" \
"v_cvt_pk_bf16_f32 v106, v108, v109\n" \
"v_cvt_pk_bf16_f32 v107, v110, v111\n" \
"v_mfma_f32_16x16x32_bf16 v[112:115], a[0:3], a[224:227], v[112:115]\n" \
"ds_read_b64_tr_b16 a[24:25], v233 offset:4608\n" \
"ds_read_b64_tr_b16 a[26:27], v233 offset:6656\n" \
"v_mfma_f32_16x16x32_bf16 v[112:115], a[4:7], a[232:235], v[112:115]\n" \
"v_and_b32 v143, 0xffff0000, v175\n" \
"v_lshlrev_b32 v144, 16, v176\n" \
"v_mfma_f32_16x16x32_bf16 v[120:123], a[4:7], a[236:239], v[120:123]\n" \
"ds_read_b64_tr_b16 a[28:29], v233 offset:12800\n" \
"ds_read_b64_tr_b16 a[30:31], v233 offset:14848\n" \
"v_mfma_f32_16x16x32_bf16 v[120:123], a[0:3], a[228:231], v[120:123]\n" \
".Lst1_8_%=:\n" \
"s_cbranch_execz .Lst1_12_%=\n" \
"s_waitcnt lgkmcnt(8)\n" \
"v_and_b32 v145, 0xffff0000, v176\n" \
"v_lshlrev_b32 v146, 16, v177\n" \
"v_mfma_f32_16x16x32_bf16 v[116:119], a[8:11], a[224:227], v[116:119]\n" \
"ds_read_b64_tr_b16 a[0:1], v239 offset:4608\n" \
"ds_read_b64_tr_b16 a[2:3], v239 offset:6656\n" \
"v_mfma_f32_16x16x32_bf16 v[116:119], a[12:15], a[232:235], v[116:119]\n" \
"buffer_store_dwordx4 v[104:107], v250, s[32:35], s21 offen offset:2048 sc1\n" \
"v_mfma_f32_16x16x32_bf16 v[124:127], a[12:15], a[236:239], v[124:127]\n" \
"ds_read_b64_tr_b16 a[4:5], v239 offset:12800\n" \
"ds_read_b64_tr_b16 a[6:7], v239 offset:14848\n" \
"v_mfma_f32_16x16x32_bf16 v[124:127], a[8:11], a[228:231], v[124:127]\n" \
"s_waitcnt lgkmcnt(8)\n" \
"v_and_b32 v147, 0xffff0000, v177\n" \
"v_lshlrev_b32 v148, 16, v178\n" \
"v_mfma_f32_16x16x32_bf16 v[112:115], a[16:19], a[240:243], v[112:115]\n" \
"ds_read_b64_tr_b16 a[8:9], v232 offset:4608\n" \
"ds_read_b64_tr_b16 a[10:11], v232 offset:6656\n" \
"v_mfma_f32_16x16x32_bf16 v[112:115], a[20:23], a[248:251], v[112:115]\n" \
"v_and_b32 v149, 0xffff0000, v178\n" \
"v_lshlrev_b32 v150, 16, v179\n" \
"v_mfma_f32_16x16x32_bf16 v[120:123], a[20:23], a[252:255], v[120:123]\n" \
"ds_read_b64_tr_b16 a[12:13], v232 offset:12800\n" \
"ds_read_b64_tr_b16 a[14:15], v232 offset:14848\n" \
"v_mfma_f32_16x16x32_bf16 v[120:123], a[16:19], a[244:247], v[120:123]\n" \
"s_waitcnt lgkmcnt(8)\n" \
"v_and_b32 v151, 0xffff0000, v179\n" \
"v_lshlrev_b32 v152, 16, v180\n" \
"v_mfma_f32_16x16x32_bf16 v[116:119], a[24:27], a[240:243], v[116:119]\n" \
"ds_read_b64_tr_b16 a[16:17], v239 offset:20992\n" \
"ds_read_b64_tr_b16 a[18:19], v239 offset:23040\n" \
"v_mfma_f32_16x16x32_bf16 v[116:119], a[28:31], a[248:251], v[116:119]\n" \
"v_and_b32 v153, 0xffff0000, v180\n" \
"v_lshlrev_b32 v154, 16, v181\n" \
"v_mfma_f32_16x16x32_bf16 v[124:127], a[28:31], a[252:255], v[124:127]\n" \
"ds_read_b64_tr_b16 a[20:21], v239 offset:29184\n" \
"ds_read_b64_tr_b16 a[22:23], v239 offset:31232\n" \
"v_mfma_f32_16x16x32_bf16 v[124:127], a[24:27], a[244:247], v[124:127]\n" \
"s_waitcnt lgkmcnt(8)\n" \
"v_and_b32 v155, 0xffff0000, v181\n" \
"v_lshlrev_b32 v156, 16, v182\n" \
"v_mfma_f32_16x16x32_bf16 v[112:115], a[0:3], a[64:67], v[112:115]\n" \
"ds_read_b64_tr_b16 a[24:25], v232 offset:20992\n" \
"ds_read_b64_tr_b16 a[26:27], v232 offset:23040\n" \
"v_mfma_f32_16x16x32_bf16 v[112:115], a[4:7], a[72:75], v[112:115]\n" \
"v_and_b32 v157, 0xffff0000, v182\n" \
"v_lshlrev_b32 v158, 16, v183\n" \
"v_mfma_f32_16x16x32_bf16 v[120:123], a[4:7], a[76:79], v[120:123]\n" \
"ds_read_b64_tr_b16 a[28:29], v232 offset:29184\n" \
"ds_read_b64_tr_b16 a[30:31], v232 offset:31232\n" \
"v_mfma_f32_16x16x32_bf16 v[120:123], a[0:3], a[68:71], v[120:123]\n" \
"s_waitcnt lgkmcnt(8)\n" \
"v_and_b32 v159, 0xffff0000, v183\n" \
"v_lshlrev_b32 v160, 16, v184\n" \
"v_mfma_f32_16x16x32_bf16 v[116:119], a[8:11], a[64:67], v[116:119]\n" \
"v_and_b32 v161, 0xffff0000, v184\n" \
"v_lshlrev_b32 v162, 16, v185\n" \
"v_mfma_f32_16x16x32_bf16 v[116:119], a[12:15], a[72:75], v[116:119]\n" \
"v_and_b32 v163, 0xffff0000, v185\n" \
"v_lshlrev_b32 v164, 16, v186\n" \
"v_mfma_f32_16x16x32_bf16 v[124:127], a[12:15], a[76:79], v[124:127]\n" \
"v_and_b32 v165, 0xffff0000, v186\n" \
"v_lshlrev_b32 v166, 16, v187\n" \
"v_mfma_f32_16x16x32_bf16 v[124:127], a[8:11], a[68:71], v[124:127]\n" \
"s_waitcnt lgkmcnt(4)\n" \
"v_and_b32 v167, 0xffff0000, v187\n" \
"v_mfma_f32_16x16x32_bf16 v[112:115], a[16:19], a[80:83], v[112:115]\n" \
"s_waitcnt vmcnt(7)\n" \
"v_readfirstlane_b32 s81, v238\n" \
"v_mfma_f32_16x16x32_bf16 v[112:115], a[20:23], a[88:91], v[112:115]\n" \
"v_mfma_f32_16x16x32_bf16 v[120:123], a[20:23], a[92:95], v[120:123]\n" \
"s_mov_b32 s40, s81\n" \
"s_sub_u32 s86, 15, s67\n" \
"s_and_b32 s25, s40, 15\n" \
"v_mfma_f32_16x16x32_bf16 v[120:123], a[16:19], a[84:87], v[120:123]\n" \
"s_waitcnt lgkmcnt(0)\n" \
"s_lshl_b32 s25, s25, 1\n" \
"v_mfma_f32_16x16x32_bf16 v[116:119], a[24:27], a[80:83], v[116:119]\n" \
"s_cmp_lt_u32 s40, 16\n" \
"s_cselect_b32 s86, s67, s86\n" \
"s_lshl_b32 s95, s86, 2\n" \
"v_mfma_f32_16x16x32_bf16 v[116:119], a[28:31], a[88:91], v[116:119]\n" \
"s_add_u32 s95, s95, %[wid]\n" \
"s_lshl_b32 s95, s95, 3\n" \
"s_lshr_b32 s26, s25, 4\n" \
"v_mfma_f32_16x16x32_bf16 v[124:127], a[28:31], a[92:95], v[124:127]\n" \
"s_add_u32 s95, s95, s26\n" \
"s_lshl_b32 s95, s95, 6\n" \
"s_and_b32 s27, s25, 15\n" \
"v_mfma_f32_16x16x32_bf16 v[124:127], a[24:27], a[84:87], v[124:127]\n" \
"s_add_u32 s95, s95, s27\n" \
"s_lshl_b32 s95, s95, 6\n" \
"s_add_u32 s95, s95, 59768832\n" \
"v_cvt_pk_bf16_f32 v112, v112, v113\n" \
"v_cvt_pk_bf16_f32 v113, v114, v115\n" \
"v_cvt_pk_bf16_f32 v114, v116, v117\n" \
"v_cvt_pk_bf16_f32 v115, v118, v119\n" \
"buffer_store_dwordx4 v[112:115], v250, s[32:35], s36 offen offset:3072 sc1\n" \
"v_cvt_pk_bf16_f32 v120, v120, v121\n" \
"v_cvt_pk_bf16_f32 v121, v122, v123\n" \
"v_cvt_pk_bf16_f32 v122, v124, v125\n" \
"v_cvt_pk_bf16_f32 v123, v126, v127\n" \
"buffer_store_dwordx4 v[120:123], v250, s[32:35], s21 offen offset:3072 sc1\n" \
"s_waitcnt vmcnt(0)\n" \
"buffer_store_dword v253, v246, s[32:35], s39 offen offset:128 sc1\n" \
"s_mul_i32 s26, s26, 7\n" \
"s_xor_b32 s26, s26, %[wid]\n" \
"s_lshl3_add_u32 s85, s86, s26\n" \
"s_lshl4_add_u32 s85, s85, s27\n" \
"s_and_b32 s84, s40, 16\n" \
"s_lshl_b32 s84, s84, 3\n" \
"v_cmp_ne_u32 vcc, 0, v245\n" \
"s_lshl_b32 s38, s85, 10\n" \
"s_cmp_eq_u32 s84, 0\n" \
"s_cselect_b32 s75, vcc_lo, vcc_hi\n" \
"buffer_load_dwordx2 v[128:129], v188, s[44:47], s38 offen offset:0\n" \
"s_lshr_b32 s76, s75, 1\n" \
"buffer_load_dwordx2 v[130:131], v188, s[44:47], s38 offen offset:32\n" \
"s_and_b32 s76, s76, s75\n" \
"buffer_load_dwordx2 v[132:133], v188, s[44:47], s38 offen offset:1024\n" \
"s_lshr_b32 s77, s76, 2\n" \
"buffer_load_dwordx2 v[134:135], v188, s[44:47], s38 offen offset:1056\n" \
"s_and_b32 s76, s76, s77\n" \
"s_and_b32 s76, s76, 0x11111111\n" \
"s_mov_b32 s74, s76\n" \
"s_add_u32 s18, s18, s84\n" \
"s_add_u32 s22, s95, 0\n" \
"s_bitcmp1_b32 s76, 0\n" \
"s_cselect_b32 s22, s22, 1073741824\n" \
"buffer_load_dwordx4 v[0:3], v214, s[32:35], s22 offen sc1\n" \
"buffer_load_dwordx4 v[32:35], v214, s[32:35], s22 offen offset:64 sc1\n" \
"s_add_u32 s22, s95, 2097152\n" \
"s_bitcmp1_b32 s76, 4\n" \
"s_cselect_b32 s22, s22, 1073741824\n" \
"buffer_load_dwordx4 v[4:7], v214, s[32:35], s22 offen sc1\n" \
"buffer_load_dwordx4 v[36:39], v214, s[32:35], s22 offen offset:64 sc1\n" \
"s_add_u32 s22, s95, 4194304\n" \
"s_bitcmp1_b32 s76, 8\n" \
"s_cselect_b32 s22, s22, 1073741824\n" \
"buffer_load_dwordx4 v[8:11], v214, s[32:35], s22 offen sc1\n" \
"buffer_load_dwordx4 v[40:43], v214, s[32:35], s22 offen offset:64 sc1\n" \
"s_add_u32 s22, s95, 6291456\n" \
"s_bitcmp1_b32 s76, 12\n" \
"s_cselect_b32 s22, s22, 1073741824\n" \
"buffer_load_dwordx4 v[12:15], v214, s[32:35], s22 offen sc1\n" \
"buffer_load_dwordx4 v[44:47], v214, s[32:35], s22 offen offset:64 sc1\n" \
"s_add_u32 s22, s95, 8388608\n" \
"s_bitcmp1_b32 s76, 16\n" \
"s_cselect_b32 s22, s22, 1073741824\n" \
"buffer_load_dwordx4 v[16:19], v214, s[32:35], s22 offen sc1\n" \
"buffer_load_dwordx4 v[48:51], v214, s[32:35], s22 offen offset:64 sc1\n" \
"s_add_u32 s22, s95, 10485760\n" \
"s_bitcmp1_b32 s76, 20\n" \
"s_cselect_b32 s22, s22, 1073741824\n" \
"buffer_load_dwordx4 v[20:23], v214, s[32:35], s22 offen sc1\n" \
"buffer_load_dwordx4 v[52:55], v214, s[32:35], s22 offen offset:64 sc1\n" \
"s_add_u32 s22, s95, 12582912\n" \
"s_bitcmp1_b32 s76, 24\n" \
"s_cselect_b32 s22, s22, 1073741824\n" \
"buffer_load_dwordx4 v[24:27], v214, s[32:35], s22 offen sc1\n" \
"buffer_load_dwordx4 v[56:59], v214, s[32:35], s22 offen offset:64 sc1\n" \
"s_add_u32 s22, s95, 14680064\n" \
"s_bitcmp1_b32 s76, 28\n" \
"s_cselect_b32 s22, s22, 1073741824\n" \
"buffer_load_dwordx4 v[28:31], v214, s[32:35], s22 offen sc1\n" \
"buffer_load_dwordx4 v[60:63], v214, s[32:35], s22 offen offset:64 sc1\n" \
"v_lshlrev_b32 v236, 2, v191\n" \
"v_lshrrev_b32 v100, 4, v191\n" \
"v_bfe_u32 v101, v191, 1, 2\n" \
"v_lshlrev_b32 v101, 4, v101\n" \
"v_and_or_b32 v101, v191, 8, v101\n" \
"v_and_or_b32 v101, v236, 4, v101\n" \
"v_and_b32 v102, 3, v100\n" \
"v_lshl_or_b32 v239, v102, 10, v101\n" \
"v_lshrrev_b32 v102, 2, v100\n" \
"v_lshl_or_b32 v239, v102, 13, v239\n" \
"v_lshl_or_b32 v101, v100, 6, v101\n" \
"buffer_load_dword v172, v101, s[32:35], 0 offen offset:0 sc1\n" \
"buffer_load_dword v174, v101, s[32:35], 0 offen offset:1024 sc1\n" \
"buffer_load_dword v176, v101, s[32:35], 0 offen offset:2048 sc1\n" \
"buffer_load_dword v178, v101, s[32:35], 0 offen offset:3072 sc1\n" \
  \
".Ljoin_%=:\n" \
"s_nop 1\n" \
"buffer_load_dword v245, v252, s[32:35], s18 offen sc1\n" \
"s_xor_b32 s94, s83, 0xc000\n" \
"s_lshl3_add_u32 s24, %[wid], s94\n" \
"v_mov_b32 v232, s24\n" \
"v_mov_b32 v233, s94\n" \
"v_lshlrev_b32 v234, 11, v251\n" \
"v_cmp_gt_u32 vcc, 8, v251\n" \
"v_mov_b32 v235, 1073741824\n" \
"s_lshl2_add_u32 s38, s64, %[wid]\n" \
"v_cndmask_b32 v234, v235, v234, vcc\n" \
"v_lshlrev_b32 v235, 3, v251\n" \
"s_lshl_b32 s38, s38, 9\n" \
"s_add_u32 s38, s38, 557056\n" \
"s_lshl_b64 s[42:43], 1, %[bid]\n" \
"s_lshr_b32 s24, %[bid], 6\n" \
"s_cmp_eq_u32 s24, %[wid]\n" \
"s_cselect_b64 s[42:43], s[42:43], 0\n" \
"s_call_b64 s[98:99], .Lch3_%=\n" \
"s_mov_b32 s82, 0x100000\n" \
".Lg0_%=:\n" \
"s_waitcnt vmcnt(0)\n" \
"s_cmp_eq_u32 s74, 0x11111111\n" \
"s_cbranch_scc0 .Lga_%=\n" \
"v_mfma_f32_16x16x32_bf16 v[92:95], v[210:213], v[0:3], 0\n" \
"v_lshlrev_b32 v192, 16, v128\n" \
"v_mfma_f32_16x16x32_bf16 v[96:99], v[208:211], v[0:3], 0\n" \
"v_and_b32 v193, 0xffff0000, v128\n" \
"v_mfma_f32_16x16x32_bf16 v[92:95], v[210:213], v[4:7], v[92:95]\n" \
"v_lshlrev_b32 v194, 16, v129\n" \
"v_mfma_f32_16x16x32_bf16 v[96:99], v[208:211], v[4:7], v[96:99]\n" \
"v_and_b32 v195, 0xffff0000, v129\n" \
"v_mfma_f32_16x16x32_bf16 v[92:95], v[210:213], v[8:11], v[92:95]\n" \
"v_lshlrev_b32 v196, 16, v130\n" \
"v_mfma_f32_16x16x32_bf16 v[96:99], v[208:211], v[8:11], v[96:99]\n" \
"v_and_b32 v197, 0xffff0000, v130\n" \
"v_mfma_f32_16x16x32_bf16 v[92:95], v[210:213], v[12:15], v[92:95]\n" \
"v_lshlrev_b32 v198, 16, v131\n" \
"v_mfma_f32_16x16x32_bf16 v[96:99], v[208:211], v[12:15], v[96:99]\n" \
"v_and_b32 v199, 0xffff0000, v131\n" \
"v_mfma_f32_16x16x32_bf16 v[92:95], v[210:213], v[16:19], v[92:95]\n" \
"v_lshlrev_b32 v200, 16, v132\n" \
"v_mfma_f32_16x16x32_bf16 v[96:99], v[208:211], v[16:19], v[96:99]\n" \
"v_and_b32 v201, 0xffff0000, v132\n" \
"v_mfma_f32_16x16x32_bf16 v[92:95], v[210:213], v[20:23], v[92:95]\n" \
"v_lshlrev_b32 v202, 16, v133\n" \
"v_mfma_f32_16x16x32_bf16 v[96:99], v[208:211], v[20:23], v[96:99]\n" \
"v_and_b32 v203, 0xffff0000, v133\n" \
"v_mfma_f32_16x16x32_bf16 v[92:95], v[210:213], v[24:27], v[92:95]\n" \
"v_lshlrev_b32 v204, 16, v134\n" \
"v_mfma_f32_16x16x32_bf16 v[96:99], v[208:211], v[24:27], v[96:99]\n" \
"v_and_b32 v205, 0xffff0000, v134\n" \
"v_mfma_f32_16x16x32_bf16 v[92:95], v[210:213], v[28:31], v[92:95]\n" \
"v_lshlrev_b32 v206, 16, v135\n" \
"v_mfma_f32_16x16x32_bf16 v[96:99], v[208:211], v[28:31], v[96:99]\n" \
"v_and_b32 v207, 0xffff0000, v135\n" \
"s_nop 4\n" \
"v_pk_fma_f32 v[92:93], v[92:93], v[136:137], v[192:193]\n" \
"v_mfma_f32_16x16x32_bf16 v[64:67], v[210:213], v[32:35], 0\n" \
"v_pk_fma_f32 v[94:95], v[94:95], v[138:139], v[194:195]\n" \
"v_mfma_f32_16x16x32_bf16 v[68:71], v[208:211], v[32:35], 0\n" \
"v_pk_fma_f32 v[96:97], v[96:97], v[140:141], v[196:197]\n" \
"v_mfma_f32_16x16x32_bf16 v[64:67], v[210:213], v[36:39], v[64:67]\n" \
"v_pk_fma_f32 v[98:99], v[98:99], v[142:143], v[198:199]\n" \
"v_mfma_f32_16x16x32_bf16 v[68:71], v[208:211], v[36:39], v[68:71]\n" \
"v_cvt_pk_bf16_f32 v100, v92, v93\n" \
"v_mfma_f32_16x16x32_bf16 v[64:67], v[210:213], v[40:43], v[64:67]\n" \
"v_cvt_pk_bf16_f32 v101, v94, v95\n" \
"v_mfma_f32_16x16x32_bf16 v[68:71], v[208:211], v[40:43], v[68:71]\n" \
"v_cvt_pk_bf16_f32 v102, v96, v97\n" \
"v_mfma_f32_16x16x32_bf16 v[64:67], v[210:213], v[44:47], v[64:67]\n" \
"v_cvt_pk_bf16_f32 v103, v98, v99\n" \
"v_mfma_f32_16x16x32_bf16 v[68:71], v[208:211], v[44:47], v[68:71]\n" \
"v_lshlrev_b32 v92, 16, v100\n" \
"v_mfma_f32_16x16x32_bf16 v[64:67], v[210:213], v[48:51], v[64:67]\n" \
"v_and_b32 v93, 0xffff0000, v100\n" \
"v_mfma_f32_16x16x32_bf16 v[68:71], v[208:211], v[48:51], v[68:71]\n" \
"v_lshlrev_b32 v94, 16, v101\n" \
"v_mfma_f32_16x16x32_bf16 v[64:67], v[210:213], v[52:55], v[64:67]\n" \
"v_readfirstlane_b32 s78, v92\n" \
"v_mfma_f32_16x16x32_bf16 v[68:71], v[208:211], v[52:55], v[68:71]\n" \
"v_and_b32 v95, 0xffff0000, v101\n" \
"v_mfma_f32_16x16x32_bf16 v[64:67], v[210:213], v[56:59], v[64:67]\n" \
"v_lshlrev_b32 v96, 16, v102\n" \
"v_mfma_f32_16x16x32_bf16 v[68:71], v[208:211], v[56:59], v[68:71]\n" \
"s_mov_b32 s79, s78\n" \
"v_mfma_f32_16x16x32_bf16 v[64:67], v[210:213], v[60:63], v[64:67]\n" \
"v_and_b32 v97, 0xffff0000, v102\n" \
"v_mfma_f32_16x16x32_bf16 v[68:71], v[208:211], v[60:63], v[68:71]\n" \
"v_lshlrev_b32 v98, 16, v103\n" \
"s_nop 2\n" \
"v_and_b32 v99, 0xffff0000, v103\n" \
"s_nop 2\n" \
"v_pk_add_f32 v[104:105], v[92:93], s[78:79] neg_lo:[0,1] neg_hi:[0,1]\n" \
"v_pk_fma_f32 v[64:65], v[64:65], v[136:137], v[200:201]\n" \
"v_pk_add_f32 v[106:107], v[94:95], s[78:79] neg_lo:[0,1] neg_hi:[0,1]\n" \
"v_pk_fma_f32 v[66:67], v[66:67], v[138:139], v[202:203]\n" \
"v_pk_add_f32 v[108:109], v[96:97], s[78:79] neg_lo:[0,1] neg_hi:[0,1]\n" \
"v_pk_fma_f32 v[68:69], v[68:69], v[140:141], v[204:205]\n" \
"v_pk_add_f32 v[110:111], v[98:99], s[78:79] neg_lo:[0,1] neg_hi:[0,1]\n" \
"v_pk_fma_f32 v[70:71], v[70:71], v[142:143], v[206:207]\n" \
"v_pk_add_f32 v[112:113], v[104:105], v[106:107]\n" \
"v_cvt_pk_bf16_f32 v72, v64, v65\n" \
"v_pk_mul_f32 v[114:115], v[104:105], v[104:105]\n" \
"v_cvt_pk_bf16_f32 v73, v66, v67\n" \
"v_pk_add_f32 v[116:117], v[108:109], v[110:111]\n" \
"v_cvt_pk_bf16_f32 v74, v68, v69\n" \
"v_pk_mul_f32 v[118:119], v[108:109], v[108:109]\n" \
"v_cvt_pk_bf16_f32 v75, v70, v71\n" \
"v_pk_fma_f32 v[114:115], v[106:107], v[106:107], v[114:115]\n" \
"v_lshlrev_b32 v64, 16, v72\n" \
"v_pk_fma_f32 v[118:119], v[110:111], v[110:111], v[118:119]\n" \
"v_and_b32 v65, 0xffff0000, v72\n" \
"v_pk_add_f32 v[112:113], v[112:113], v[116:117]\n" \
"v_lshlrev_b32 v66, 16, v73\n" \
"v_pk_add_f32 v[114:115], v[114:115], v[118:119]\n" \
"v_readfirstlane_b32 s24, v64\n" \
"v_add_f32 v112, v112, v113\n" \
"v_and_b32 v67, 0xffff0000, v73\n" \
"v_add_f32 v114, v114, v115\n" \
"v_lshlrev_b32 v68, 16, v74\n" \
"s_nop 0\n" \
"v_permlane32_swap_b32 v112, v114\n" \
"s_mov_b32 s25, s24\n" \
"v_add_f32 v112, v112, v114\n" \
"v_and_b32 v69, 0xffff0000, v74\n" \
"s_nop 0\n" \
"v_add_f32_dpp v112, v112, v112 quad_perm:[1,0,3,2] row_mask:0xf bank_mask:0xf\n" \
"v_lshlrev_b32 v70, 16, v75\n" \
"s_nop 0\n" \
"v_add_f32_dpp v112, v112, v112 quad_perm:[2,3,0,1] row_mask:0xf bank_mask:0xf\n" \
"v_and_b32 v71, 0xffff0000, v75\n" \
"s_nop 0\n" \
"v_add_f32_dpp v112, v112, v112 row_half_mirror row_mask:0xf bank_mask:0xf\n" \
"v_pk_add_f32 v[76:77], v[64:65], s[24:25] neg_lo:[0,1] neg_hi:[0,1]\n" \
"s_nop 0\n" \
"v_add_f32_dpp v112, v112, v112 row_mirror row_mask:0xf bank_mask:0xf\n" \
"v_pk_add_f32 v[78:79], v[66:67], s[24:25] neg_lo:[0,1] neg_hi:[0,1]\n" \
"s_nop 0\n" \
"v_add_f32_dpp v112, v112, v112 row_bcast:15 row_mask:0xa bank_mask:0xf\n" \
"v_pk_add_f32 v[80:81], v[68:69], s[24:25] neg_lo:[0,1] neg_hi:[0,1]\n" \
"v_mul_f32 v112, 0x3b000000, v112\n" \
"v_pk_add_f32 v[82:83], v[70:71], s[24:25] neg_lo:[0,1] neg_hi:[0,1]\n" \
"v_readlane_b32 s80, v112, 31\n" \
"v_pk_add_f32 v[84:85], v[76:77], v[78:79]\n" \
"v_readlane_b32 s81, v112, 63\n" \
"v_pk_mul_f32 v[86:87], v[76:77], v[76:77]\n" \
"v_mov_b32 v113, s78\n" \
"v_pk_add_f32 v[88:89], v[80:81], v[82:83]\n" \
"v_mov_b32 v114, s81\n" \
"v_pk_mul_f32 v[90:91], v[80:81], v[80:81]\n" \
"v_add_f32 v113, s80, v113\n" \
"v_pk_fma_f32 v[86:87], v[78:79], v[78:79], v[86:87]\n" \
"v_fma_f32 v114, -s80, s80, v114\n" \
"v_pk_fma_f32 v[90:91], v[82:83], v[82:83], v[90:91]\n" \
"v_max_f32 v115, 0, v114\n" \
"v_pk_add_f32 v[84:85], v[84:85], v[88:89]\n" \
"v_cmp_u_f32 s[22:23], v114, v114\n" \
"v_pk_add_f32 v[86:87], v[86:87], v[90:91]\n" \
"s_nop 0\n" \
"v_cndmask_b32 v114, v115, v114, s[22:23]\n" \
"v_add_f32 v84, v84, v85\n" \
"v_cvt_pk_bf16_f32 v116, v114, v113\n" \
"v_add_f32 v86, v86, v87\n" \
"v_lshlrev_b32 v114, 16, v116\n" \
"s_nop 0\n" \
"v_permlane32_swap_b32 v84, v86\n" \
"v_and_b32 v112, 0xffff0000, v116\n" \
"v_add_f32 v84, v84, v86\n" \
"v_add_f32 v114, 0x3727c5ac, v114\n" \
"s_nop 0\n" \
"v_add_f32_dpp v84, v84, v84 quad_perm:[1,0,3,2] row_mask:0xf bank_mask:0xf\n" \
"v_cvt_pk_bf16_f32 v114, v114, v114\n" \
"s_nop 0\n" \
"v_add_f32_dpp v84, v84, v84 quad_perm:[2,3,0,1] row_mask:0xf bank_mask:0xf\n" \
"v_pk_add_f32 v[104:105], v[92:93], v[112:113] op_sel_hi:[1,0] neg_lo:[0,1] neg_hi:[0,1]\n" \
"s_nop 0\n" \
"v_add_f32_dpp v84, v84, v84 row_half_mirror row_mask:0xf bank_mask:0xf\n" \
"v_lshlrev_b32 v114, 16, v114\n" \
"s_nop 0\n" \
"v_add_f32_dpp v84, v84, v84 row_mirror row_mask:0xf bank_mask:0xf\n" \
"v_pk_add_f32 v[106:107], v[94:95], v[112:113] op_sel_hi:[1,0] neg_lo:[0,1] neg_hi:[0,1]\n" \
"s_nop 0\n" \
"v_add_f32_dpp v84, v84, v84 row_bcast:15 row_mask:0xa bank_mask:0xf\n" \
"v_sqrt_f32 v114, v114\n" \
"v_mul_f32 v84, 0x3b000000, v84\n" \
"v_pk_add_f32 v[108:109], v[96:97], v[112:113] op_sel_hi:[1,0] neg_lo:[0,1] neg_hi:[0,1]\n" \
"v_readlane_b32 s26, v84, 31\n" \
"v_pk_add_f32 v[110:111], v[98:99], v[112:113] op_sel_hi:[1,0] neg_lo:[0,1] neg_hi:[0,1]\n" \
"v_readlane_b32 s27, v84, 63\n" \
"v_cvt_pk_bf16_f32 v116, v114, v112\n" \
"v_mov_b32 v85, s24\n" \
"v_pk_mul_f32 v[104:105], v[104:105], v[152:153]\n" \
"v_mov_b32 v86, s27\n" \
"v_lshlrev_b32 v114, 16, v116\n" \
"v_add_f32 v85, s26, v85\n" \
"v_pk_mul_f32 v[106:107], v[106:107], v[154:155]\n" \
"v_fma_f32 v86, -s26, s26, v86\n" \
"v_rcp_f32 v114, v114\n" \
"v_max_f32 v87, 0, v86\n" \
"v_pk_mul_f32 v[108:109], v[108:109], v[156:157]\n" \
"v_cmp_u_f32 s[20:21], v86, v86\n" \
"v_pk_mul_f32 v[110:111], v[110:111], v[158:159]\n" \
"s_nop 0\n" \
"v_cndmask_b32 v86, v87, v86, s[20:21]\n" \
"v_readfirstlane_b32 s72, v116\n" \
"v_cvt_pk_bf16_f32 v88, v86, v85\n" \
"v_pk_fma_f32 v[104:105], v[104:105], v[114:115], v[144:145] op_sel_hi:[1,0,1]\n" \
"v_lshlrev_b32 v86, 16, v88\n" \
"v_pk_fma_f32 v[106:107], v[106:107], v[114:115], v[146:147] op_sel_hi:[1,0,1]\n" \
"v_and_b32 v84, 0xffff0000, v88\n" \
"v_pk_fma_f32 v[108:109], v[108:109], v[114:115], v[148:149] op_sel_hi:[1,0,1]\n" \
"v_add_f32 v86, 0x3727c5ac, v86\n" \
"v_pk_fma_f32 v[110:111], v[110:111], v[114:115], v[150:151] op_sel_hi:[1,0,1]\n" \
"v_cvt_pk_bf16_f32 v86, v86, v86\n" \
"v_cvt_pk_bf16_f32 v220, v104, v105\n" \
"v_pk_add_f32 v[76:77], v[64:65], v[84:85] op_sel_hi:[1,0] neg_lo:[0,1] neg_hi:[0,1]\n" \
"v_cvt_pk_bf16_f32 v221, v106, v107\n" \
"v_lshlrev_b32 v86, 16, v86\n" \
"v_cvt_pk_bf16_f32 v222, v108, v109\n" \
"v_pk_add_f32 v[78:79], v[66:67], v[84:85] op_sel_hi:[1,0] neg_lo:[0,1] neg_hi:[0,1]\n" \
"v_cvt_pk_bf16_f32 v223, v110, v111\n" \
"v_sqrt_f32 v86, v86\n" \
"v_dot2_f32_bf16 v112, v220, v168, 0\n" \
"v_pk_add_f32 v[80:81], v[68:69], v[84:85] op_sel_hi:[1,0] neg_lo:[0,1] neg_hi:[0,1]\n" \
"v_dot2_f32_bf16 v113, v221, v169, 0\n" \
"v_pk_add_f32 v[82:83], v[70:71], v[84:85] op_sel_hi:[1,0] neg_lo:[0,1] neg_hi:[0,1]\n" \
"v_dot2_f32_bf16 v112, v222, v170, v112\n" \
"v_cvt_pk_bf16_f32 v88, v86, v84\n" \
"v_dot2_f32_bf16 v113, v223, v171, v113\n" \
"v_pk_mul_f32 v[76:77], v[76:77], v[152:153]\n" \
"s_nop 2\n" \
"v_lshlrev_b32 v86, 16, v88\n" \
"v_add_f32 v112, v112, v113\n" \
"v_pk_mul_f32 v[78:79], v[78:79], v[154:155]\n" \
"s_nop 0\n" \
"v_add_f32_dpp v112, v112, v112 quad_perm:[1,0,3,2] row_mask:0xf bank_mask:0xf\n" \
"v_rcp_f32 v86, v86\n" \
"s_nop 0\n" \
"v_add_f32_dpp v112, v112, v112 quad_perm:[2,3,0,1] row_mask:0xf bank_mask:0xf\n" \
"v_pk_mul_f32 v[80:81], v[80:81], v[156:157]\n" \
"s_nop 0\n" \
"v_add_f32_dpp v112, v112, v112 row_half_mirror row_mask:0xf bank_mask:0xf\n" \
"v_pk_mul_f32 v[82:83], v[82:83], v[158:159]\n" \
"s_nop 0\n" \
"v_add_f32_dpp v112, v112, v112 row_mirror row_mask:0xf bank_mask:0xf\n" \
"v_readfirstlane_b32 s73, v88\n" \
"s_nop 0\n" \
"v_add_f32_dpp v112, v112, v112 row_bcast:15 row_mask:0xa bank_mask:0xf\n" \
"v_pk_fma_f32 v[76:77], v[76:77], v[86:87], v[144:145] op_sel_hi:[1,0,1]\n" \
"s_nop 0\n" \
"v_add_f32_dpp v112, v112, v112 row_bcast:31 row_mask:0xc bank_mask:0xf\n" \
"v_pk_fma_f32 v[78:79], v[78:79], v[86:87], v[146:147] op_sel_hi:[1,0,1]\n" \
"v_cvt_pk_bf16_f32 v112, v112, v112\n" \
"v_pk_fma_f32 v[80:81], v[80:81], v[86:87], v[148:149] op_sel_hi:[1,0,1]\n" \
"s_sub_u32 s79, 2047, s85\n" \
"v_pk_fma_f32 v[82:83], v[82:83], v[86:87], v[150:151] op_sel_hi:[1,0,1]\n" \
"s_lshl_b32 s79, s79, 5\n" \
"v_cvt_pk_bf16_f32 v216, v76, v77\n" \
"s_or_b32 s79, s79, 3\n" \
"v_cvt_pk_bf16_f32 v217, v78, v79\n" \
"v_readlane_b32 s78, v112, 63\n" \
"v_cvt_pk_bf16_f32 v218, v80, v81\n" \
"s_and_b32 s78, s78, 0xffff\n" \
"v_cvt_pk_bf16_f32 v219, v82, v83\n" \
"s_or_b32 s80, s78, 0x8000\n" \
"v_dot2_f32_bf16 v84, v216, v168, 0\n" \
"s_andn2_b32 s81, 0xffff, s78\n" \
"v_dot2_f32_bf16 v85, v217, v169, 0\n" \
"s_bitcmp1_b32 s78, 15\n" \
"s_cselect_b32 s80, s81, s80\n" \
"v_dot2_f32_bf16 v84, v218, v170, v84\n" \
"s_and_b32 s78, s78, 0x7fff\n" \
"v_dot2_f32_bf16 v85, v219, v171, v85\n" \
"s_cmp_gt_u32 s78, 0x7f80\n" \
"s_cselect_b32 s80, 0, s80\n" \
"s_nop 2\n" \
"s_pack_ll_b32_b16 s87, s79, s80\n" \
"v_add_f32 v84, v84, v85\n" \
"s_nop 1\n" \
"v_add_f32_dpp v84, v84, v84 quad_perm:[1,0,3,2] row_mask:0xf bank_mask:0xf\n" \
"s_nop 1\n" \
"v_add_f32_dpp v84, v84, v84 quad_perm:[2,3,0,1] row_mask:0xf bank_mask:0xf\n" \
"s_nop 1\n" \
"v_add_f32_dpp v84, v84, v84 row_half_mirror row_mask:0xf bank_mask:0xf\n" \
"s_nop 1\n" \
"v_add_f32_dpp v84, v84, v84 row_mirror row_mask:0xf bank_mask:0xf\n" \
"s_nop 1\n" \
"v_add_f32_dpp v84, v84, v84 row_bcast:15 row_mask:0xa bank_mask:0xf\n" \
"s_nop 1\n" \
"v_add_f32_dpp v84, v84, v84 row_bcast:31 row_mask:0xc bank_mask:0xf\n" \
"v_cvt_pk_bf16_f32 v84, v84, v84\n" \
"s_sub_u32 s25, 2046, s85\n" \
"s_lshl_b32 s25, s25, 5\n" \
"s_or_b32 s25, s25, 3\n" \
"v_readlane_b32 s24, v84, 63\n" \
"s_and_b32 s24, s24, 0xffff\n" \
"s_or_b32 s26, s24, 0x8000\n" \
"s_andn2_b32 s27, 0xffff, s24\n" \
"s_bitcmp1_b32 s24, 15\n" \
"s_cselect_b32 s26, s27, s26\n" \
"s_and_b32 s24, s24, 0x7fff\n" \
"s_cmp_gt_u32 s24, 0x7f80\n" \
"s_cselect_b32 s26, 0, s26\n" \
"s_pack_ll_b32_b16 s88, s25, s26\n" \
"s_cmp_gt_u32 s87, s88\n" \
"s_cselect_b32 s25, s87, s88\n" \
"s_cselect_b32 s24, s72, s73\n" \
  \
"v_mov_b32 v85, s25\n" \
"v_mov_b32 v84, s24\n" \
"ds_write_b64 v232, v[84:85]\n" \
"s_waitcnt lgkmcnt(0)\n" \
"s_barrier\n" \
"ds_read_b128 v[84:87], v233\n" \
"ds_read_b128 v[88:91], v233 offset:16\n" \
"s_lshl_b32 s26, %[bid], 3\n" \
"s_add_u32 s26, s26, 557056\n" \
"s_cmp_eq_u32 %[wid], 0\n" \
"s_cselect_b32 s26, s26, 1073741824\n" \
"s_waitcnt lgkmcnt(0)\n" \
"v_cmp_gt_u32 vcc, v85, v87\n" \
"v_max_u32 v85, v85, v87\n" \
"s_nop 0\n" \
"v_cndmask_b32 v84, v86, v84, vcc\n" \
"v_cmp_gt_u32 vcc, v89, v91\n" \
"v_max_u32 v89, v89, v91\n" \
"s_nop 0\n" \
"v_cndmask_b32 v88, v90, v88, vcc\n" \
"v_cmp_gt_u32 vcc, v85, v89\n" \
"v_max_u32 v85, v85, v89\n" \
"s_nop 0\n" \
"v_cndmask_b32 v84, v88, v84, vcc\n" \
"buffer_store_dwordx2 v[84:85], v234, s[32:35], s26 offen sc1\n" \
"buffer_load_dwordx2 v[86:87], v235, s[32:35], s38 offen sc1\n" \
"v_readfirstlane_b32 s92, v85\n" \
"v_lshlrev_b32 v104, 16, v220\n" \
"v_lshlrev_b32 v76, 16, v216\n" \
"v_and_b32 v105, 0xffff0000, v220\n" \
"v_and_b32 v77, 0xffff0000, v216\n" \
"v_lshlrev_b32 v106, 16, v221\n" \
"v_lshlrev_b32 v78, 16, v217\n" \
"v_and_b32 v107, 0xffff0000, v221\n" \
"v_and_b32 v79, 0xffff0000, v217\n" \
"v_lshlrev_b32 v108, 16, v222\n" \
"v_lshlrev_b32 v80, 16, v218\n" \
"v_and_b32 v109, 0xffff0000, v222\n" \
"v_and_b32 v81, 0xffff0000, v218\n" \
"v_lshlrev_b32 v110, 16, v223\n" \
"v_lshlrev_b32 v82, 16, v219\n" \
"v_and_b32 v111, 0xffff0000, v223\n" \
"v_and_b32 v83, 0xffff0000, v219\n" \
"v_pk_fma_f32 v[104:105], v[104:105], v[160:161], v[92:93]\n" \
"v_pk_fma_f32 v[76:77], v[76:77], v[160:161], v[64:65]\n" \
"v_pk_fma_f32 v[106:107], v[106:107], v[162:163], v[94:95]\n" \
"v_pk_fma_f32 v[78:79], v[78:79], v[162:163], v[66:67]\n" \
"v_pk_fma_f32 v[108:109], v[108:109], v[164:165], v[96:97]\n" \
"v_pk_fma_f32 v[80:81], v[80:81], v[164:165], v[68:69]\n" \
"v_pk_fma_f32 v[110:111], v[110:111], v[166:167], v[98:99]\n" \
"v_pk_fma_f32 v[82:83], v[82:83], v[166:167], v[70:71]\n" \
"v_cvt_pk_bf16_f32 v100, v104, v105\n" \
"v_cvt_pk_bf16_f32 v72, v76, v77\n" \
"v_cvt_pk_bf16_f32 v101, v106, v107\n" \
"v_cvt_pk_bf16_f32 v73, v78, v79\n" \
"v_cvt_pk_bf16_f32 v102, v108, v109\n" \
"v_cvt_pk_bf16_f32 v74, v80, v81\n" \
"v_cvt_pk_bf16_f32 v103, v110, v111\n" \
"v_cvt_pk_bf16_f32 v75, v82, v83\n" \
"s_mov_b32 vcc_lo, 0xcccccccc\n" \
"s_mov_b32 vcc_lo, 0xcccccccc\n" \
"s_mov_b32 vcc_hi, 0xcccccccc\n" \
"s_mov_b32 vcc_hi, 0xcccccccc\n" \
"v_mov_b32_dpp v104, v102 quad_perm:[0,2,0,2] row_mask:0xf bank_mask:0xf\n" \
"v_mov_b32_dpp v76, v74 quad_perm:[0,2,0,2] row_mask:0xf bank_mask:0xf\n" \
"v_mov_b32_dpp v105, v103 quad_perm:[0,2,0,2] row_mask:0xf bank_mask:0xf\n" \
"v_mov_b32_dpp v77, v75 quad_perm:[0,2,0,2] row_mask:0xf bank_mask:0xf\n" \
"v_mov_b32_dpp v106, v102 quad_perm:[1,3,1,3] row_mask:0xf bank_mask:0xf\n" \
"v_mov_b32_dpp v78, v74 quad_perm:[1,3,1,3] row_mask:0xf bank_mask:0xf\n" \
"v_mov_b32_dpp v107, v103 quad_perm:[1,3,1,3] row_mask:0xf bank_mask:0xf\n" \
"v_mov_b32_dpp v79, v75 quad_perm:[1,3,1,3] row_mask:0xf bank_mask:0xf\n" \
"v_cndmask_b32_dpp v224, v100, v104, vcc quad_perm:[0,2,0,2] row_mask:0xf bank_mask:0xf\n" \
"v_cndmask_b32_dpp v228, v72, v76, vcc quad_perm:[0,2,0,2] row_mask:0xf bank_mask:0xf\n" \
"v_cndmask_b32_dpp v225, v101, v105, vcc quad_perm:[0,2,0,2] row_mask:0xf bank_mask:0xf\n" \
"v_cndmask_b32_dpp v229, v73, v77, vcc quad_perm:[0,2,0,2] row_mask:0xf bank_mask:0xf\n" \
"v_cndmask_b32_dpp v226, v100, v106, vcc quad_perm:[1,3,1,3] row_mask:0xf bank_mask:0xf\n" \
"v_cndmask_b32_dpp v230, v72, v78, vcc quad_perm:[1,3,1,3] row_mask:0xf bank_mask:0xf\n" \
"v_cndmask_b32_dpp v227, v101, v107, vcc quad_perm:[1,3,1,3] row_mask:0xf bank_mask:0xf\n" \
"v_cndmask_b32_dpp v231, v73, v79, vcc quad_perm:[1,3,1,3] row_mask:0xf bank_mask:0xf\n" \
"v_accvgpr_read_b32 v180, a176\n" \
"v_accvgpr_read_b32 v181, a177\n" \
"v_accvgpr_read_b32 v182, a178\n" \
"v_accvgpr_read_b32 v183, a179\n" \
"v_accvgpr_read_b32 v184, a180\n" \
"v_accvgpr_read_b32 v185, a181\n" \
"v_accvgpr_read_b32 v186, a182\n" \
"v_accvgpr_read_b32 v187, a183\n" \
"s_mov_b32 s24, 0x05040100\n" \
"s_mov_b32 s25, 0x07060302\n" \
"v_accvgpr_read_b32 v32, a160\n" \
"v_accvgpr_read_b32 v33, a161\n" \
"v_accvgpr_read_b32 v34, a162\n" \
"v_accvgpr_read_b32 v35, a163\n" \
"v_accvgpr_read_b32 v36, a164\n" \
"v_accvgpr_read_b32 v37, a165\n" \
"v_accvgpr_read_b32 v38, a166\n" \
"v_accvgpr_read_b32 v39, a167\n" \
"v_accvgpr_read_b32 v40, a168\n" \
"v_accvgpr_read_b32 v41, a169\n" \
"v_accvgpr_read_b32 v42, a170\n" \
"v_accvgpr_read_b32 v43, a171\n" \
"v_accvgpr_read_b32 v44, a172\n" \
"v_accvgpr_read_b32 v45, a173\n" \
"v_accvgpr_read_b32 v46, a174\n" \
"v_accvgpr_read_b32 v47, a175\n" \
"v_perm_b32 v0, v36, v32, s24\n" \
"v_perm_b32 v1, v36, v32, s25\n" \
"v_perm_b32 v2, v37, v33, s24\n" \
"v_perm_b32 v3, v37, v33, s25\n" \
"v_perm_b32 v4, v38, v34, s24\n" \
"v_perm_b32 v5, v38, v34, s25\n" \
"v_perm_b32 v6, v39, v35, s24\n" \
"v_perm_b32 v7, v39, v35, s25\n" \
"v_perm_b32 v8, v44, v40, s24\n" \
"v_perm_b32 v9, v44, v40, s25\n" \
"v_perm_b32 v10, v45, v41, s24\n" \
"v_perm_b32 v11, v45, v41, s25\n" \
"v_perm_b32 v12, v46, v42, s24\n" \
"v_perm_b32 v13, v46, v42, s25\n" \
"v_perm_b32 v14, v47, v43, s24\n" \
"v_perm_b32 v15, v47, v43, s25\n" \
"v_lshlrev_b32 v16, 16, v180\n" \
"v_and_b32 v17, 0xffff0000, v180\n" \
"v_lshlrev_b32 v18, 16, v181\n" \
"v_and_b32 v19, 0xffff0000, v181\n" \
"v_lshlrev_b32 v20, 16, v182\n" \
"v_and_b32 v21, 0xffff0000, v182\n" \
"v_lshlrev_b32 v22, 16, v183\n" \
"v_and_b32 v23, 0xffff0000, v183\n" \
"v_lshlrev_b32 v24, 16, v184\n" \
"v_and_b32 v25, 0xffff0000, v184\n" \
"v_lshlrev_b32 v26, 16, v185\n" \
"v_and_b32 v27, 0xffff0000, v185\n" \
"v_lshlrev_b32 v28, 16, v186\n" \
"v_and_b32 v29, 0xffff0000, v186\n" \
"v_lshlrev_b32 v30, 16, v187\n" \
"v_and_b32 v31, 0xffff0000, v187\n" \
"v_and_b32 v173, 0xffff0000, v172\n" \
"v_lshlrev_b32 v172, 16, v172\n" \
"v_and_b32 v175, 0xffff0000, v174\n" \
"v_lshlrev_b32 v174, 16, v174\n" \
"v_and_b32 v177, 0xffff0000, v176\n" \
".Lst3_0_%=:\n" \
"s_cbranch_execz .Lst3_4_%=\n" \
"v_lshlrev_b32 v176, 16, v176\n" \
"v_and_b32 v179, 0xffff0000, v178\n" \
"v_lshlrev_b32 v178, 16, v178\n" \
"v_lshrrev_b32 v100, 4, v191\n" \
"v_lshl_add_u32 v180, v100, 4, s94\n" \
"v_add_u32 v180, 256, v180\n" \
"v_and_b32 v101, 28, v236\n" \
"v_add_u32 v181, s94, v101\n" \
"v_add_u32 v181, 256, v181\n" \
"v_lshrrev_b32 v101, 1, v191\n" \
"v_and_b32 v102, 1, v191\n" \
"v_lshlrev_b32 v102, 4, v102\n" \
"v_lshl_or_b32 v182, v101, 11, v102\n" \
"s_lshl_b32 s24, %[wid], 5\n" \
"s_add_u32 s24, s24, s94\n" \
"s_add_u32 s24, s24, 512\n" \
"v_bfe_u32 v101, v251, 4, 2\n" \
"v_lshl_add_u32 v183, v101, 2, v102\n" \
"v_add_u32 v183, s24, v183\n" \
"v_and_b32 v101, 3, v191\n" \
"s_add_u32 s24, s94, 512\n" \
"v_lshl_add_u32 v238, v101, 3, s24\n" \
"s_lshl3_add_u32 s24, s64, s65\n" \
"v_lshrrev_b32 v101, 2, v191\n" \
"v_cmp_eq_u32 vcc, s24, v101\n" \
"v_mov_b32 v237, 1073741824\n" \
"s_lshl_b32 s24, s24, 5\n" \
"s_add_u32 s37, s24, 98304\n" \
"v_cndmask_b32 v237, v237, v236, vcc\n" \
"s_add_u32 s29, s37, 262144\n" \
"s_cmp_lt_u32 %[wid], 3\n" \
"s_cselect_b32 s29, s29, 1073741824\n" \
"s_lshl_b32 s24, s69, 11\n" \
"s_add_u32 s24, s24, 98304\n" \
"s_cmp_lt_u32 s65, 28\n" \
"s_cselect_b32 s41, s24, 1073741824\n" \
"s_lshl_b32 s85, s85, 10\n" \
"s_cmp_eq_u32 s87, s92\n" \
"s_cselect_b32 s24, 1073741824, s85\n" \
"buffer_store_dwordx4 v[224:227], v248, s[52:55], s24 offen offset:0 sc1\n" \
"s_cmp_eq_u32 s88, s92\n" \
"s_cselect_b32 s24, 1073741824, s85\n" \
"buffer_store_dwordx4 v[228:231], v248, s[52:55], s24 offen offset:1024 sc1\n" \
"s_mov_b32 s82, 0x100000\n" \
"s_mov_b32 s93, 0\n" \
"s_waitcnt vmcnt(2)\n" \
".Lkp_%=:\n" \
"v_cndmask_b32 v87, v87, v85, s[42:43]\n" \
"v_cndmask_b32 v86, v86, v84, s[42:43]\n" \
"v_cmp_eq_u32 vcc, 0, v87\n" \
"v_mov_b32 v88, v87\n" \
"s_nop 1\n" \
"s_cbranch_vccnz .Lkr_%=\n" \
".Lkb_%=:\n" \
"v_max_u32_dpp v88, v88, v88 quad_perm:[1,0,3,2] row_mask:0xf bank_mask:0xf\n" \
"s_nop 1\n" \
"v_max_u32_dpp v88, v88, v88 quad_perm:[2,3,0,1] row_mask:0xf bank_mask:0xf\n" \
"s_nop 1\n" \
"v_max_u32_dpp v88, v88, v88 row_half_mirror row_mask:0xf bank_mask:0xf\n" \
"s_nop 1\n" \
"v_max_u32_dpp v88, v88, v88 row_mirror row_mask:0xf bank_mask:0xf\n" \
"s_nop 1\n" \
"v_max_u32_dpp v88, v88, v88 row_bcast:15 row_mask:0xa bank_mask:0xf\n" \
"s_nop 1\n" \
"v_max_u32_dpp v88, v88, v88 row_bcast:31 row_mask:0xc bank_mask:0xf\n" \
"s_nop 0\n" \
"v_readlane_b32 s25, v88, 63\n" \
"s_nop 1\n" \
"v_cmp_eq_u32 vcc, s25, v87\n" \
"v_mov_b32 v89, s25\n" \
"s_nop 2\n" \
"s_ff1_i32_b64 s26, vcc\n" \
"s_and_b32 s26, s26, 63\n" \
"v_readlane_b32 s24, v86, s26\n" \
"s_nop 1\n" \
"v_mov_b32 v88, s24\n" \
"ds_write_b64 v232, v[88:89] offset:64\n" \
"s_waitcnt lgkmcnt(0)\n" \
"s_barrier\n" \
"ds_read_b128 v[84:87], v233 offset:64\n" \
"ds_read_b128 v[88:91], v233 offset:80\n" \
"s_waitcnt lgkmcnt(0)\n" \
"v_cmp_gt_u32 vcc, v85, v87\n" \
"v_max_u32 v85, v85, v87\n" \
"s_nop 0\n" \
"v_cndmask_b32 v84, v86, v84, vcc\n" \
"v_cmp_gt_u32 vcc, v89, v91\n" \
"v_max_u32 v89, v89, v91\n" \
"s_nop 0\n" \
"v_cndmask_b32 v88, v90, v88, vcc\n" \
"v_cmp_gt_u32 vcc, v85, v89\n" \
"v_max_u32 v85, v85, v89\n" \
"s_nop 0\n" \
"v_cndmask_b32 v84, v88, v84, vcc\n" \
"v_readfirstlane_b32 s93, v85\n" \
"s_bfe_u32 s30, s93, 0xb0005\n" \
"s_sub_u32 s30, 2047, s30\n" \
"s_mov_b32 s22, 0x18100800\n" \
"s_mov_b32 s23, 0x01091119\n" \
"s_bfe_u32 s24, s30, 0x30004\n" \
"s_lshl_b32 s24, s24, 3\n" \
"s_lshr_b64 s[22:23], s[22:23], s24\n" \
"s_and_b32 s22, s22, 0xff\n" \
"s_lshr_b32 s25, s30, 7\n" \
"s_lshl_b32 s25, s25, 5\n" \
"s_add_u32 s25, s25, s22\n" \
"s_lshl_b32 s25, s25, 6\n" \
"s_and_b32 s24, s30, 15\n" \
"s_add_u32 s25, s25, s24\n" \
"s_lshl_b32 s25, s25, 6\n" \
"s_add_u32 s25, s25, 59768832\n" \
"s_lshl_b32 s30, s30, 10\n" \
  \
"s_add_u32 s24, s25, 0\n" \
"buffer_load_dword v32, v239, s[32:35], s24 offen sc1\n" \
"s_add_u32 s24, s25, 2097152\n" \
"buffer_load_dword v33, v239, s[32:35], s24 offen sc1\n" \
"s_add_u32 s24, s25, 4194304\n" \
"buffer_load_dword v34, v239, s[32:35], s24 offen sc1\n" \
"s_add_u32 s24, s25, 6291456\n" \
"buffer_load_dword v35, v239, s[32:35], s24 offen sc1\n" \
"s_add_u32 s24, s25, 8388608\n" \
"buffer_load_dword v36, v239, s[32:35], s24 offen sc1\n" \
"s_add_u32 s24, s25, 10485760\n" \
"buffer_load_dword v37, v239, s[32:35], s24 offen sc1\n" \
"s_add_u32 s24, s25, 12582912\n" \
"buffer_load_dword v38, v239, s[32:35], s24 offen sc1\n" \
"s_add_u32 s24, s25, 14680064\n" \
"buffer_load_dword v39, v239, s[32:35], s24 offen sc1\n" \
"buffer_load_dword v40, v236, s[44:47], s30 offen\n" \
"s_cmp_eq_u32 s93, s92\n" \
"s_cselect_b32 s92, 0, s92\n" \
"s_cmp_eq_u32 s87, s92\n" \
"s_cselect_b32 s24, s85, 1073741824\n" \
"buffer_store_dwordx4 v[224:227], v248, s[52:55], s24 offen offset:0 sc1\n" \
"s_cmp_eq_u32 s88, s92\n" \
"s_cselect_b32 s24, s85, 1073741824\n" \
"buffer_store_dwordx4 v[228:231], v248, s[52:55], s24 offen offset:1024 sc1\n" \
"v_lshlrev_b32 v86, 16, v84\n" \
"v_and_b32 v88, 0xffff0000, v84\n" \
"v_rcp_f32 v86, v86\n" \
"s_lshr_b32 s24, s93, 16\n" \
"s_and_b32 s25, s24, 0x7fff\n" \
"s_andn2_b32 s26, 0xffff, s24\n" \
"s_bitcmp1_b32 s24, 15\n" \
"s_cselect_b32 s24, s25, s26\n" \
"s_lshl_b32 s24, s24, 16\n" \
"v_mov_b32 v90, 0xbfb8aa3b\n" \
"v_mul_f32 v90, s24, v90\n" \
"v_exp_f32 v90, v90\n" \
"s_nop 0\n" \
"v_add_f32 v90, 1.0, v90\n" \
"v_rcp_f32 v90, v90\n" \
"s_nop 0\n" \
"v_cvt_pk_bf16_f32 v90, v90, v90\n" \
"v_lshlrev_b32 v90, 16, v90\n" \
"s_waitcnt vmcnt(2)\n" \
"v_lshlrev_b32 v44, 16, v32\n" \
"v_and_b32 v45, 0xffff0000, v32\n" \
"v_lshlrev_b32 v46, 16, v33\n" \
"v_and_b32 v47, 0xffff0000, v33\n" \
"v_lshlrev_b32 v48, 16, v34\n" \
"v_and_b32 v49, 0xffff0000, v34\n" \
"v_lshlrev_b32 v50, 16, v35\n" \
"v_and_b32 v51, 0xffff0000, v35\n" \
"v_lshlrev_b32 v52, 16, v36\n" \
"v_and_b32 v53, 0xffff0000, v36\n" \
"v_lshlrev_b32 v54, 16, v37\n" \
"v_and_b32 v55, 0xffff0000, v37\n" \
"v_lshlrev_b32 v56, 16, v38\n" \
"v_and_b32 v57, 0xffff0000, v38\n" \
"v_lshlrev_b32 v58, 16, v39\n" \
"v_and_b32 v59, 0xffff0000, v39\n" \
"v_pk_add_f32 v[44:45], v[44:45], v[46:47]\n" \
"v_pk_add_f32 v[48:49], v[48:49], v[50:51]\n" \
"v_pk_add_f32 v[52:53], v[52:53], v[54:55]\n" \
"v_pk_add_f32 v[56:57], v[56:57], v[58:59]\n" \
"v_pk_add_f32 v[44:45], v[44:45], v[48:49]\n" \
"v_pk_add_f32 v[52:53], v[52:53], v[56:57]\n" \
"v_lshlrev_b32 v60, 16, v40\n" \
"v_pk_add_f32 v[44:45], v[44:45], v[52:53]\n" \
"v_and_b32 v61, 0xffff0000, v40\n" \
"v_pk_fma_f32 v[60:61], v[44:45], v[172:173], v[60:61]\n" \
"v_cvt_pk_bf16_f32 v46, v60, v61\n" \
"v_lshlrev_b32 v60, 16, v46\n" \
"v_and_b32 v61, 0xffff0000, v46\n" \
"v_pk_add_f32 v[44:45], v[60:61], v[88:89] op_sel_hi:[1,0] neg_lo:[0,1] neg_hi:[0,1]\n" \
"v_pk_mul_f32 v[44:45], v[44:45], v[176:177]\n" \
"v_pk_fma_f32 v[62:63], v[44:45], v[86:87], v[174:175] op_sel_hi:[1,0,1]\n" \
"v_cvt_pk_bf16_f32 v46, v62, v63\n" \
"v_lshlrev_b32 v62, 16, v46\n" \
"v_and_b32 v63, 0xffff0000, v46\n" \
"v_dot2_f32_bf16 v64, v0, v46, 0\n" \
"v_dot2_f32_bf16 v65, v1, v46, 0\n" \
"v_dot2_f32_bf16 v66, v2, v46, 0\n" \
"v_dot2_f32_bf16 v67, v3, v46, 0\n" \
"v_dot2_f32_bf16 v68, v4, v46, 0\n" \
"v_dot2_f32_bf16 v69, v5, v46, 0\n" \
"v_dot2_f32_bf16 v70, v6, v46, 0\n" \
"v_dot2_f32_bf16 v71, v7, v46, 0\n" \
"v_dot2_f32_bf16 v72, v8, v46, 0\n" \
"v_dot2_f32_bf16 v73, v9, v46, 0\n" \
"v_dot2_f32_bf16 v74, v10, v46, 0\n" \
"v_dot2_f32_bf16 v75, v11, v46, 0\n" \
"v_dot2_f32_bf16 v76, v12, v46, 0\n" \
"v_dot2_f32_bf16 v77, v13, v46, 0\n" \
"v_dot2_f32_bf16 v78, v14, v46, 0\n" \
"v_dot2_f32_bf16 v79, v15, v46, 0\n" \
"s_nop 1\n" \
"v_permlane32_swap_b32 v64, v72\n" \
"v_permlane32_swap_b32 v65, v73\n" \
"v_permlane32_swap_b32 v66, v74\n" \
"v_permlane32_swap_b32 v67, v75\n" \
"v_permlane32_swap_b32 v68, v76\n" \
"v_permlane32_swap_b32 v69, v77\n" \
"v_permlane32_swap_b32 v70, v78\n" \
"v_permlane32_swap_b32 v71, v79\n" \
"v_pk_add_f32 v[64:65], v[64:65], v[72:73]\n" \
"v_pk_add_f32 v[66:67], v[66:67], v[74:75]\n" \
"v_pk_add_f32 v[68:69], v[68:69], v[76:77]\n" \
"v_pk_add_f32 v[70:71], v[70:71], v[78:79]\n" \
"s_nop 0\n" \
"v_permlane16_swap_b32 v64, v68\n" \
"v_permlane16_swap_b32 v65, v69\n" \
"v_permlane16_swap_b32 v66, v70\n" \
"v_permlane16_swap_b32 v67, v71\n" \
"v_pk_add_f32 v[64:65], v[64:65], v[68:69]\n" \
"v_pk_add_f32 v[66:67], v[66:67], v[70:71]\n" \
"s_nop 0\n" \
"v_add_f32_dpp v64, v64, v64 quad_perm:[1,0,3,2] row_mask:0xf bank_mask:0xf\n" \
"v_add_f32_dpp v65, v65, v65 quad_perm:[1,0,3,2] row_mask:0xf bank_mask:0xf\n" \
"v_add_f32_dpp v66, v66, v66 quad_perm:[1,0,3,2] row_mask:0xf bank_mask:0xf\n" \
"v_add_f32_dpp v67, v67, v67 quad_perm:[1,0,3,2] row_mask:0xf bank_mask:0xf\n" \
"v_add_f32_dpp v64, v64, v64 quad_perm:[2,3,0,1] row_mask:0xf bank_mask:0xf\n" \
"v_add_f32_dpp v65, v65, v65 quad_perm:[2,3,0,1] row_mask:0xf bank_mask:0xf\n" \
"v_add_f32_dpp v66, v66, v66 quad_perm:[2,3,0,1] row_mask:0xf bank_mask:0xf\n" \
"v_add_f32_dpp v67, v67, v67 quad_perm:[2,3,0,1] row_mask:0xf bank_mask:0xf\n" \
"v_add_f32_dpp v64, v64, v64 row_half_mirror row_mask:0xf bank_mask:0xf\n" \
"v_add_f32_dpp v65, v65, v65 row_half_mirror row_mask:0xf bank_mask:0xf\n" \
"v_add_f32_dpp v66, v66, v66 row_half_mirror row_mask:0xf bank_mask:0xf\n" \
"v_add_f32_dpp v67, v67, v67 row_half_mirror row_mask:0xf bank_mask:0xf\n" \
"v_add_f32_dpp v64, v64, v64 row_mirror row_mask:0xf bank_mask:0xf\n" \
"v_add_f32_dpp v65, v65, v65 row_mirror row_mask:0xf bank_mask:0xf\n" \
"v_add_f32_dpp v66, v66, v66 row_mirror row_mask:0xf bank_mask:0xf\n" \
"v_add_f32_dpp v67, v67, v67 row_mirror row_mask:0xf bank_mask:0xf\n" \
"ds_write_b128 v180, v[64:67]\n" \
"s_waitcnt lgkmcnt(0)\n" \
"s_barrier\n" \
"ds_read2_b32 v[64:65], v181 offset0:0 offset1:8\n" \
"ds_read2_b32 v[66:67], v181 offset0:16 offset1:24\n" \
"ds_read2_b32 v[68:69], v181 offset0:32 offset1:40\n" \
"ds_read2_b32 v[70:71], v181 offset0:48 offset1:56\n" \
"s_waitcnt lgkmcnt(0)\n" \
"v_pk_add_f32 v[64:65], v[64:65], v[66:67]\n" \
"v_pk_add_f32 v[68:69], v[68:69], v[70:71]\n" \
"v_pk_add_f32 v[64:65], v[64:65], v[68:69]\n" \
"v_cvt_pk_bf16_f32 v66, v64, v65\n" \
"v_lshlrev_b32 v64, 16, v66\n" \
"v_and_b32 v65, 0xffff0000, v66\n" \
"v_mul_f32 v66, 0xbfb8aa3b, v64\n" \
"v_exp_f32 v66, v66\n" \
"s_nop 0\n" \
"v_add_f32 v66, 1.0, v66\n" \
"v_rcp_f32 v66, v66\n" \
"s_nop 0\n" \
"v_mul_f32 v64, v64, v66\n" \
"v_cvt_pk_bf16_f32 v64, v64, v64\n" \
"v_lshlrev_b32 v64, 16, v64\n" \
"v_mul_f32 v64, v64, v65\n" \
"v_cvt_pk_bf16_f32 v64, v64, v64\n" \
"v_lshlrev_b32 v64, 16, v64\n" \
"s_nop 0\n" \
"v_readlane_b32 s16, v64, 0\n" \
"v_readlane_b32 s17, v64, 1\n" \
"v_readlane_b32 s18, v64, 2\n" \
"v_readlane_b32 s19, v64, 3\n" \
"v_readlane_b32 s20, v64, 4\n" \
"v_readlane_b32 s21, v64, 5\n" \
"v_readlane_b32 s22, v64, 6\n" \
"v_readlane_b32 s23, v64, 7\n" \
"v_mul_f32 v66, s16, v16\n" \
"v_mul_f32 v67, s16, v17\n" \
"v_mul_f32 v68, s17, v18\n" \
"v_mul_f32 v69, s17, v19\n" \
"v_fmac_f32 v66, s18, v20\n" \
"v_fmac_f32 v67, s18, v21\n" \
"v_fmac_f32 v68, s19, v22\n" \
"v_fmac_f32 v69, s19, v23\n" \
"v_fmac_f32 v66, s20, v24\n" \
"v_fmac_f32 v67, s20, v25\n" \
"v_fmac_f32 v68, s21, v26\n" \
"v_fmac_f32 v69, s21, v27\n" \
"v_fmac_f32 v66, s22, v28\n" \
"v_fmac_f32 v67, s22, v29\n" \
"v_fmac_f32 v68, s23, v30\n" \
"v_fmac_f32 v69, s23, v31\n" \
"v_pk_add_f32 v[66:67], v[66:67], v[68:69]\n" \
"v_min_u32 v66, -2, v66\n" \
"v_min_u32 v67, -2, v67\n" \
"buffer_store_dwordx2 v[66:67], v190, s[32:35], s41 offen sc1\n" \
  \
"s_cmp_ge_u32 %[bid], 64\n" \
"s_cbranch_scc1 .Lend_%=\n" \
"s_mov_b32 s82, 0x100000\n" \
"s_sleep 14\n" \
".Lfp_%=:\n" \
"buffer_load_dwordx4 v[64:67], v182, s[32:35], s37 offen sc1\n" \
"buffer_load_dwordx4 v[68:71], v182, s[32:35], s29 offen sc1\n" \
"s_waitcnt vmcnt(0)\n" \
"v_max3_u32 v72, v64, v65, v66\n" \
"v_max3_u32 v73, v67, v68, v69\n" \
"v_max3_u32 v72, v72, v70, v71\n" \
"v_max_u32 v72, v72, v73\n" \
"v_cmp_eq_u32 vcc, -1, v72\n" \
"v_pk_add_f32 v[64:65], v[64:65], v[68:69]\n" \
"v_pk_add_f32 v[66:67], v[66:67], v[70:71]\n" \
"s_nop 0\n" \
"s_cbranch_vccnz .Lfr_%=\n" \
".Lfb_%=:\n" \
"v_permlane32_swap_b32 v64, v66\n" \
"v_permlane32_swap_b32 v65, v67\n" \
"v_pk_add_f32 v[64:65], v[64:65], v[66:67]\n" \
"s_nop 1\n" \
"v_permlane16_swap_b32 v64, v65\n" \
"v_add_f32 v64, v64, v65\n" \
"s_nop 1\n" \
"v_add_f32_dpp v64, v64, v64 quad_perm:[2,3,0,1] row_mask:0xf bank_mask:0xf\n" \
"s_nop 1\n" \
"v_add_f32_dpp v64, v64, v64 row_ror:4 row_mask:0xf bank_mask:0xf\n" \
"s_nop 1\n" \
"v_add_f32_dpp v64, v64, v64 row_ror:8 row_mask:0xf bank_mask:0xf\n" \
"ds_write_b32 v183, v64\n" \
"s_waitcnt lgkmcnt(0)\n" \
"s_barrier\n" \
"ds_read2_b64 v[64:67], v238 offset0:0 offset1:4\n" \
"ds_read2_b64 v[68:71], v238 offset0:8 offset1:12\n" \
"s_waitcnt lgkmcnt(0)\n" \
"v_pk_add_f32 v[64:65], v[64:65], v[66:67]\n" \
"v_pk_add_f32 v[68:69], v[68:69], v[70:71]\n" \
"v_pk_add_f32 v[64:65], v[64:65], v[68:69]\n" \
"v_cvt_pk_bf16_f32 v46, v64, v65\n" \
"v_lshlrev_b32 v64, 16, v46\n" \
"v_and_b32 v65, 0xffff0000, v46\n" \
"v_pk_mul_f32 v[64:65], v[64:65], v[90:91] op_sel_hi:[1,0]\n" \
"v_cvt_pk_bf16_f32 v46, v64, v65\n" \
"v_lshlrev_b32 v64, 16, v46\n" \
"v_and_b32 v65, 0xffff0000, v46\n" \
"v_pk_add_f32 v[64:65], v[64:65], v[62:63]\n" \
"v_cvt_pk_bf16_f32 v46, v64, v65\n" \
"v_lshlrev_b32 v64, 16, v46\n" \
"v_and_b32 v65, 0xffff0000, v46\n" \
"v_pk_mul_f32 v[64:65], v[64:65], v[178:179]\n" \
"v_cvt_pk_bf16_f32 v46, v64, v65\n" \
"v_lshlrev_b32 v64, 16, v46\n" \
"v_and_b32 v65, 0xffff0000, v46\n" \
"v_pk_add_f32 v[64:65], v[64:65], v[60:61]\n" \
"v_cvt_pk_bf16_f32 v64, v64, v65\n" \
"buffer_store_dword v64, v237, s[52:55], s30 offen sc1\n" \
"s_branch .Lend_%=\n" \
".Lrare_%=:\n" \
  \
".Ldhi_%=:\n" \
".Lst0_14_%=:\n" \
"s_cbranch_execz .Lstb0_%=\n" \
"v_mfma_f32_16x16x32_bf16 v[128:131], a[128:131], v[192:195], 0\n" \
"s_add_u32 s85, s85, s67\n" \
"v_cvt_pk_bf16_f32 v221, v58, v59\n" \
"v_cvt_pk_bf16_f32 v222, v60, v61\n" \
"v_mfma_f32_16x16x32_bf16 v[128:131], a[132:135], v[196:199], v[128:131]\n" \
"s_barrier\n" \
"v_cvt_pk_bf16_f32 v223, v62, v63\n" \
"v_add_u32 v242, 0x10000, v248\n" \
"v_mfma_f32_16x16x32_bf16 v[128:131], a[136:139], v[200:203], v[128:131]\n" \
"s_lshl2_add_u32 s85, s85, %[wid]\n" \
"v_bfe_i32 v241, v251, 0, 1\n" \
"v_and_b32 v241, 0xaaaaaaaa, v241\n" \
"v_mfma_f32_16x16x32_bf16 v[128:131], a[140:143], v[204:207], v[128:131]\n" \
"s_lshl_b32 s85, s85, 2\n" \
"v_or_b32 v241, 0x44444444, v241\n" \
"v_xor_b32 v249, 0x4000, v249\n" \
"v_mfma_f32_16x16x32_bf16 v[132:135], a[156:159], v[204:207], 0\n" \
"s_waitcnt vmcnt(1)\n" \
"s_add_u32 s85, s85, 82048\n" \
"buffer_store_dword v250, off, s[32:35], s85 sc1\n" \
"v_mfma_f32_16x16x32_bf16 v[132:135], a[152:155], v[200:203], v[132:135]\n" \
"ds_read_b128 a[224:227], v242 offset:8192\n" \
"ds_read_b128 a[192:195], v242 offset:0\n" \
"v_mfma_f32_16x16x32_bf16 v[132:135], a[148:151], v[196:199], v[132:135]\n" \
"v_exp_f32 v128, v128\n" \
"v_mfma_f32_16x16x32_bf16 v[132:135], a[144:147], v[192:195], v[132:135]\n" \
"s_add_u32 s85, s94, 589824\n" \
"v_exp_f32 v129, v129\n" \
"v_mfma_f32_16x16x32_bf16 v[136:139], a[160:163], v[192:195], v[224:227]\n" \
"buffer_store_dwordx4 a[64:67], v248, s[32:35], s85 offen offset:0 sc1\n" \
"v_mfma_f32_16x16x32_bf16 v[136:139], a[164:167], v[196:199], v[136:139]\n" \
"v_exp_f32 v130, v130\n" \
"v_mfma_f32_16x16x32_bf16 v[136:139], a[168:171], v[200:203], v[136:139]\n" \
"v_exp_f32 v131, v131\n" \
"v_mfma_f32_16x16x32_bf16 v[136:139], a[172:175], v[204:207], v[136:139]\n" \
"v_cvt_pk_bf16_f32 v128, v128, v129\n" \
"v_cvt_pk_bf16_f32 v129, v130, v131\n" \
"v_mfma_f32_16x16x32_bf16 v[140:143], a[188:191], v[204:207], v[228:231]\n" \
"v_exp_f32 v132, v132\n" \
"v_mfma_f32_16x16x32_bf16 v[140:143], a[184:187], v[200:203], v[140:143]\n" \
"v_exp_f32 v133, v133\n" \
"v_mfma_f32_16x16x32_bf16 v[140:143], a[180:183], v[196:199], v[140:143]\n" \
"v_exp_f32 v134, v134\n" \
"v_mfma_f32_16x16x32_bf16 v[140:143], a[176:179], v[192:195], v[140:143]\n" \
"ds_read_b128 a[196:199], v242 offset:1024\n" \
"v_cvt_pk_bf16_f32 v130, v132, v133\n" \
"v_mfma_f32_16x16x32_bf16 v[144:147], a[128:131], v[208:211], 0\n" \
"v_exp_f32 v135, v135\n" \
"v_mfma_f32_16x16x32_bf16 v[144:147], a[132:135], v[212:215], v[144:147]\n" \
"v_exp_f32 v136, v136\n" \
"v_mfma_f32_16x16x32_bf16 v[144:147], a[136:139], v[216:219], v[144:147]\n" \
"ds_read_b128 a[228:231], v242 offset:9216\n" \
"v_cvt_pk_bf16_f32 v131, v134, v135\n" \
"v_mfma_f32_16x16x32_bf16 v[144:147], a[140:143], v[220:223], v[144:147]\n" \
"v_exp_f32 v137, v137\n" \
"v_mfma_f32_16x16x32_bf16 v[148:151], a[156:159], v[220:223], 0\n" \
"v_exp_f32 v138, v138\n" \
"v_mfma_f32_16x16x32_bf16 v[148:151], a[152:155], v[216:219], v[148:151]\n" \
"v_exp_f32 v139, v139\n" \
"v_mfma_f32_16x16x32_bf16 v[148:151], a[148:151], v[212:215], v[148:151]\n" \
"v_cvt_pk_bf16_f32 v132, v136, v137\n" \
"v_cvt_pk_bf16_f32 v133, v138, v139\n" \
"v_mfma_f32_16x16x32_bf16 v[148:151], a[144:147], v[208:211], v[148:151]\n" \
"v_exp_f32 v140, v140\n" \
"v_mfma_f32_16x16x32_bf16 v[152:155], a[160:163], v[208:211], 0\n" \
"v_exp_f32 v141, v141\n" \
"v_mfma_f32_16x16x32_bf16 v[152:155], a[164:167], v[212:215], v[152:155]\n" \
"s_add_u32 s85, s94, 589824\n" \
"ds_read_b128 a[232:235], v242 offset:10240\n" \
"v_cvt_pk_bf16_f32 v134, v140, v141\n" \
"v_mfma_f32_16x16x32_bf16 v[152:155], a[168:171], v[216:219], v[152:155]\n" \
"v_exp_f32 v142, v142\n" \
"v_mfma_f32_16x16x32_bf16 v[152:155], a[172:175], v[220:223], v[152:155]\n" \
"v_exp_f32 v143, v143\n" \
"v_mfma_f32_16x16x32_bf16 v[156:159], a[188:191], v[220:223], 0\n" \
"buffer_store_dwordx4 a[72:75], v248, s[32:35], s85 offen offset:1024 sc1\n" \
"v_mfma_f32_16x16x32_bf16 v[156:159], a[184:187], v[216:219], v[156:159]\n" \
"ds_read_b128 a[200:203], v242 offset:2048\n" \
"v_cvt_pk_bf16_f32 v135, v142, v143\n" \
"v_mfma_f32_16x16x32_bf16 v[156:159], a[180:183], v[212:215], v[156:159]\n" \
"ds_read_b128 a[204:207], v242 offset:3072\n" \
"ds_read_b128 a[236:239], v242 offset:11264\n" \
"v_exp_f32 v144, v144\n" \
"v_mfma_f32_16x16x32_bf16 v[156:159], a[176:179], v[208:211], v[156:159]\n" \
"ds_read_b128 a[240:243], v242 offset:12288\n" \
"ds_read_b128 a[208:211], v242 offset:4096\n" \
"v_cvt_pk_bf16_f32 v160, v64, v65\n" \
"v_cvt_pk_bf16_f32 v161, v66, v67\n" \
"v_cvt_pk_bf16_f32 v162, v68, v69\n" \
"v_cvt_pk_bf16_f32 v163, v70, v71\n" \
"v_smfmac_f32_16x16x64_bf16 a[32:35], a[48:51], v[128:135], v241\n" \
"s_waitcnt lgkmcnt(6)\n" \
"ds_read_b128 a[212:215], v242 offset:5120\n" \
"ds_read_b128 a[244:247], v242 offset:13312\n" \
"v_cvt_pk_bf16_f32 v164, v72, v73\n" \
"v_cvt_pk_bf16_f32 v165, v74, v75\n" \
"v_cvt_pk_bf16_f32 v166, v76, v77\n" \
"v_cvt_pk_bf16_f32 v167, v78, v79\n" \
"v_mfma_f32_16x16x32_bf16 v[0:3], a[224:227], v[132:135], 0\n" \
"v_exp_f32 v145, v145\n" \
"v_cvt_pk_bf16_f32 v168, v80, v81\n" \
"v_cvt_pk_bf16_f32 v169, v82, v83\n" \
"v_cvt_pk_bf16_f32 v170, v84, v85\n" \
"v_cvt_pk_bf16_f32 v171, v86, v87\n" \
"v_mfma_f32_16x16x32_bf16 v[0:3], a[192:195], v[128:131], v[0:3]\n" \
"v_exp_f32 v146, v146\n" \
"v_cvt_pk_bf16_f32 v172, v88, v89\n" \
"v_cvt_pk_bf16_f32 v173, v90, v91\n" \
"v_cvt_pk_bf16_f32 v174, v92, v93\n" \
"v_cvt_pk_bf16_f32 v175, v94, v95\n" \
"v_mfma_f32_16x16x32_bf16 v[4:7], a[196:199], v[128:131], 0\n" \
"ds_read_b128 a[248:251], v242 offset:14336\n" \
"v_cvt_pk_bf16_f32 v144, v144, v145\n" \
"v_mfma_f32_16x16x32_bf16 v[4:7], a[228:231], v[132:135], v[4:7]\n" \
"s_waitcnt lgkmcnt(5)\n" \
"v_exp_f32 v147, v147\n" \
"v_mfma_f32_16x16x32_bf16 v[8:11], a[232:235], v[132:135], 0\n" \
"ds_read_b128 a[216:219], v242 offset:6144\n" \
"v_cvt_pk_bf16_f32 v145, v146, v147\n" \
"v_mfma_f32_16x16x32_bf16 v[8:11], a[200:203], v[128:131], v[8:11]\n" \
"ds_read_b128 a[220:223], v242 offset:7168\n" \
"ds_read_b128 a[252:255], v242 offset:15360\n" \
"v_exp_f32 v148, v148\n" \
"v_mfma_f32_16x16x32_bf16 v[12:15], a[204:207], v[128:131], 0\n" \
"v_cvt_pk_bf16_f32 v176, v96, v97\n" \
"v_cvt_pk_bf16_f32 v177, v98, v99\n" \
"v_mfma_f32_16x16x32_bf16 v[12:15], a[236:239], v[132:135], v[12:15]\n" \
"s_waitcnt lgkmcnt(4)\n" \
"v_exp_f32 v149, v149\n" \
"v_mfma_f32_16x16x32_bf16 v[16:19], a[240:243], v[132:135], 0\n" \
"v_cvt_pk_bf16_f32 v146, v148, v149\n" \
"v_cvt_pk_bf16_f32 v178, v100, v101\n" \
"v_mfma_f32_16x16x32_bf16 v[16:19], a[208:211], v[128:131], v[16:19]\n" \
"v_exp_f32 v150, v150\n" \
"v_mfma_f32_16x16x32_bf16 v[20:23], a[212:215], v[128:131], 0\n" \
"s_add_u32 s85, s94, 589824\n" \
"v_cvt_pk_bf16_f32 v179, v102, v103\n" \
"v_cvt_pk_bf16_f32 v180, v104, v105\n" \
"v_mfma_f32_16x16x32_bf16 v[20:23], a[244:247], v[132:135], v[20:23]\n" \
"s_waitcnt lgkmcnt(0)\n" \
"v_exp_f32 v151, v151\n" \
"v_mfma_f32_16x16x32_bf16 v[24:27], a[248:251], v[132:135], 0\n" \
"buffer_store_dwordx4 a[80:83], v248, s[32:35], s85 offen offset:2048 sc1\n" \
"v_mfma_f32_16x16x32_bf16 v[24:27], a[216:219], v[128:131], v[24:27]\n" \
"v_exp_f32 v152, v152\n" \
"v_mfma_f32_16x16x32_bf16 v[28:31], a[220:223], v[128:131], 0\n" \
"v_cvt_pk_bf16_f32 v147, v150, v151\n" \
"v_cvt_pk_bf16_f32 v181, v106, v107\n" \
"v_mfma_f32_16x16x32_bf16 v[28:31], a[252:255], v[132:135], v[28:31]\n" \
"v_exp_f32 v153, v153\n" \
"v_mfma_f32_16x16x32_bf16 v[128:131], a[128:131], v[160:163], 0\n" \
"v_cvt_pk_bf16_f32 v148, v152, v153\n" \
"v_cvt_pk_bf16_f32 v182, v108, v109\n" \
"v_mfma_f32_16x16x32_bf16 v[128:131], a[132:135], v[164:167], v[128:131]\n" \
"v_exp_f32 v154, v154\n" \
"v_mfma_f32_16x16x32_bf16 v[128:131], a[136:139], v[168:171], v[128:131]\n" \
"ds_write_b128 v249, a[64:67] offset:32768\n" \
"v_cvt_pk_bf16_f32 v183, v110, v111\n" \
"v_mfma_f32_16x16x32_bf16 v[128:131], a[140:143], v[172:175], v[128:131]\n" \
"v_exp_f32 v155, v155\n" \
"v_mfma_f32_16x16x32_bf16 v[132:135], a[156:159], v[172:175], 0\n" \
"v_cvt_pk_bf16_f32 v149, v154, v155\n" \
"v_cvt_pk_bf16_f32 v184, v112, v113\n" \
"v_mfma_f32_16x16x32_bf16 v[132:135], a[152:155], v[168:171], v[132:135]\n" \
"v_exp_f32 v156, v156\n" \
"v_mfma_f32_16x16x32_bf16 v[132:135], a[148:151], v[164:167], v[132:135]\n" \
"v_cvt_pk_bf16_f32 v185, v114, v115\n" \
"v_cvt_pk_bf16_f32 v186, v116, v117\n" \
"v_mfma_f32_16x16x32_bf16 v[132:135], a[144:147], v[160:163], v[132:135]\n" \
"v_exp_f32 v157, v157\n" \
"v_mfma_f32_16x16x32_bf16 v[136:139], a[160:163], v[160:163], 0\n" \
"v_cvt_pk_bf16_f32 v150, v156, v157\n" \
"v_cvt_pk_bf16_f32 v187, v118, v119\n" \
"v_mfma_f32_16x16x32_bf16 v[136:139], a[164:167], v[164:167], v[136:139]\n" \
"v_exp_f32 v158, v158\n" \
"v_mfma_f32_16x16x32_bf16 v[136:139], a[168:171], v[168:171], v[136:139]\n" \
"v_cvt_pk_bf16_f32 v188, v120, v121\n" \
"v_cvt_pk_bf16_f32 v189, v122, v123\n" \
"v_mfma_f32_16x16x32_bf16 v[136:139], a[172:175], v[172:175], v[136:139]\n" \
"v_exp_f32 v159, v159\n" \
"v_mfma_f32_16x16x32_bf16 v[140:143], a[188:191], v[172:175], 0\n" \
"s_add_u32 s85, s94, 589824\n" \
"v_cvt_pk_bf16_f32 v151, v158, v159\n" \
"v_cvt_pk_bf16_f32 v190, v124, v125\n" \
"v_mfma_f32_16x16x32_bf16 v[140:143], a[184:187], v[168:171], v[140:143]\n" \
"buffer_store_dwordx4 a[88:91], v248, s[32:35], s85 offen offset:3072 sc1\n" \
"v_mfma_f32_16x16x32_bf16 v[140:143], a[180:183], v[164:167], v[140:143]\n" \
"v_exp_f32 v128, v128\n" \
"v_mfma_f32_16x16x32_bf16 v[140:143], a[176:179], v[160:163], v[140:143]\n" \
"ds_write_b128 v249, a[72:75] offset:33792\n" \
"v_cvt_pk_bf16_f32 v191, v126, v127\n" \
"v_smfmac_f32_16x16x64_bf16 a[36:39], a[48:51], v[144:151], v241\n" \
"v_mfma_f32_16x16x32_bf16 v[32:35], a[224:227], v[148:151], 0\n" \
"v_exp_f32 v129, v129\n" \
"v_mfma_f32_16x16x32_bf16 v[32:35], a[192:195], v[144:147], v[32:35]\n" \
"v_exp_f32 v130, v130\n" \
"v_mfma_f32_16x16x32_bf16 v[36:39], a[196:199], v[144:147], 0\n" \
"v_cvt_pk_bf16_f32 v128, v128, v129\n" \
"v_mfma_f32_16x16x32_bf16 v[36:39], a[228:231], v[148:151], v[36:39]\n" \
"v_exp_f32 v131, v131\n" \
"v_mfma_f32_16x16x32_bf16 v[40:43], a[232:235], v[148:151], 0\n" \
"v_cvt_pk_bf16_f32 v129, v130, v131\n" \
"v_mfma_f32_16x16x32_bf16 v[40:43], a[200:203], v[144:147], v[40:43]\n" \
"v_exp_f32 v132, v132\n" \
"v_mfma_f32_16x16x32_bf16 v[44:47], a[204:207], v[144:147], 0\n" \
"v_mfma_f32_16x16x32_bf16 v[44:47], a[236:239], v[148:151], v[44:47]\n" \
"v_exp_f32 v133, v133\n" \
"v_mfma_f32_16x16x32_bf16 v[48:51], a[240:243], v[148:151], 0\n" \
"v_cvt_pk_bf16_f32 v130, v132, v133\n" \
"v_mfma_f32_16x16x32_bf16 v[48:51], a[208:211], v[144:147], v[48:51]\n" \
"v_exp_f32 v134, v134\n" \
"v_mfma_f32_16x16x32_bf16 v[52:55], a[212:215], v[144:147], 0\n" \
"ds_write_b128 v249, a[80:83] offset:34816\n" \
"v_mfma_f32_16x16x32_bf16 v[52:55], a[244:247], v[148:151], v[52:55]\n" \
"v_exp_f32 v135, v135\n" \
"v_mfma_f32_16x16x32_bf16 v[56:59], a[248:251], v[148:151], 0\n" \
"v_cvt_pk_bf16_f32 v131, v134, v135\n" \
"v_mfma_f32_16x16x32_bf16 v[56:59], a[216:219], v[144:147], v[56:59]\n" \
"s_add_u32 s85, s94, 593920\n" \
"v_exp_f32 v136, v136\n" \
"v_mfma_f32_16x16x32_bf16 v[60:63], a[220:223], v[144:147], 0\n" \
"buffer_store_dwordx4 a[96:99], v248, s[32:35], s85 offen offset:0 sc1\n" \
"v_mfma_f32_16x16x32_bf16 v[60:63], a[252:255], v[148:151], v[60:63]\n" \
"v_exp_f32 v137, v137\n" \
"v_mfma_f32_16x16x32_bf16 v[144:147], a[128:131], v[176:179], 0\n" \
"v_cvt_pk_bf16_f32 v132, v136, v137\n" \
"v_mfma_f32_16x16x32_bf16 v[144:147], a[132:135], v[180:183], v[144:147]\n" \
"v_exp_f32 v138, v138\n" \
"v_mfma_f32_16x16x32_bf16 v[144:147], a[136:139], v[184:187], v[144:147]\n" \
"ds_read_b128 a[128:131], v248 offset:16384\n" \
"ds_read_b128 a[132:135], v248 offset:17408\n" \
"v_mfma_f32_16x16x32_bf16 v[144:147], a[140:143], v[188:191], v[144:147]\n" \
"v_exp_f32 v139, v139\n" \
"v_mfma_f32_16x16x32_bf16 v[148:151], a[156:159], v[188:191], 0\n" \
"ds_read_b128 a[136:139], v248 offset:18432\n" \
"v_cvt_pk_bf16_f32 v133, v138, v139\n" \
"v_mfma_f32_16x16x32_bf16 v[148:151], a[152:155], v[184:187], v[148:151]\n" \
"v_exp_f32 v140, v140\n" \
"v_mfma_f32_16x16x32_bf16 v[148:151], a[148:151], v[180:183], v[148:151]\n" \
"ds_read_b128 a[140:143], v248 offset:19456\n" \
"v_mfma_f32_16x16x32_bf16 v[148:151], a[144:147], v[176:179], v[148:151]\n" \
"v_exp_f32 v141, v141\n" \
"v_mfma_f32_16x16x32_bf16 v[152:155], a[160:163], v[176:179], 0\n" \
"ds_write_b128 v249, a[88:91] offset:35840\n" \
"v_cvt_pk_bf16_f32 v134, v140, v141\n" \
"v_mfma_f32_16x16x32_bf16 v[152:155], a[164:167], v[180:183], v[152:155]\n" \
"v_exp_f32 v142, v142\n" \
"v_mfma_f32_16x16x32_bf16 v[152:155], a[168:171], v[184:187], v[152:155]\n" \
"ds_read_b128 a[156:159], v248 offset:23552\n" \
"ds_read_b128 a[152:155], v248 offset:22528\n" \
"v_mfma_f32_16x16x32_bf16 v[152:155], a[172:175], v[188:191], v[152:155]\n" \
"v_exp_f32 v143, v143\n" \
"v_mfma_f32_16x16x32_bf16 v[156:159], a[188:191], v[188:191], 0\n" \
"v_exp_f32 v144, v144\n" \
"v_mfma_f32_16x16x32_bf16 v[156:159], a[184:187], v[184:187], v[156:159]\n" \
"v_exp_f32 v145, v145\n" \
"v_mfma_f32_16x16x32_bf16 v[156:159], a[180:183], v[180:183], v[156:159]\n" \
"v_exp_f32 v146, v146\n" \
"v_cvt_pk_bf16_f32 v135, v142, v143\n" \
"v_mfma_f32_16x16x32_bf16 v[156:159], a[176:179], v[176:179], v[156:159]\n" \
"s_add_u32 s85, s94, 593920\n" \
"v_exp_f32 v147, v147\n" \
"v_smfmac_f32_16x16x64_bf16 a[40:43], a[48:51], v[128:135], v241\n" \
"buffer_store_dwordx4 a[104:107], v248, s[32:35], s85 offen offset:1024 sc1\n" \
"v_mfma_f32_16x16x32_bf16 v[64:67], a[224:227], v[132:135], 0\n" \
"v_exp_f32 v148, v148\n" \
"v_mfma_f32_16x16x32_bf16 v[64:67], a[192:195], v[128:131], v[64:67]\n" \
"v_exp_f32 v149, v149\n" \
"v_mfma_f32_16x16x32_bf16 v[68:71], a[196:199], v[128:131], 0\n" \
"v_exp_f32 v150, v150\n" \
"v_mfma_f32_16x16x32_bf16 v[68:71], a[228:231], v[132:135], v[68:71]\n" \
"v_exp_f32 v151, v151\n" \
"v_mfma_f32_16x16x32_bf16 v[72:75], a[232:235], v[132:135], 0\n" \
"ds_write_b128 v249, a[96:99] offset:36864\n" \
"v_mov_b32 v240, v248\n" \
"v_mfma_f32_16x16x32_bf16 v[72:75], a[200:203], v[128:131], v[72:75]\n" \
".Lst0_15_%=:\n" \
"s_cbranch_execz .Lstb0_%=\n" \
"v_exp_f32 v152, v152\n" \
"v_mfma_f32_16x16x32_bf16 v[76:79], a[204:207], v[128:131], 0\n" \
"buffer_load_dword v253, v252, s[32:35], s81 offen sc1\n" \
"v_cvt_pk_bf16_f32 v144, v144, v145\n" \
"v_mfma_f32_16x16x32_bf16 v[76:79], a[236:239], v[132:135], v[76:79]\n" \
"v_exp_f32 v153, v153\n" \
"v_mfma_f32_16x16x32_bf16 v[80:83], a[240:243], v[132:135], 0\n" \
"v_cvt_pk_bf16_f32 v145, v146, v147\n" \
"v_cvt_pk_bf16_f32 v146, v148, v149\n" \
"v_mfma_f32_16x16x32_bf16 v[80:83], a[208:211], v[128:131], v[80:83]\n" \
"v_exp_f32 v154, v154\n" \
"v_mfma_f32_16x16x32_bf16 v[84:87], a[212:215], v[128:131], 0\n" \
"v_cvt_pk_bf16_f32 v147, v150, v151\n" \
"v_cvt_pk_bf16_f32 v148, v152, v153\n" \
"v_mfma_f32_16x16x32_bf16 v[84:87], a[244:247], v[132:135], v[84:87]\n" \
"v_exp_f32 v155, v155\n" \
"v_mfma_f32_16x16x32_bf16 v[88:91], a[248:251], v[132:135], 0\n" \
"ds_read_b128 a[148:151], v248 offset:21504\n" \
"v_cvt_pk_bf16_f32 v149, v154, v155\n" \
"v_mfma_f32_16x16x32_bf16 v[88:91], a[216:219], v[128:131], v[88:91]\n" \
"ds_read_b128 a[144:147], v248 offset:20480\n" \
"v_exp_f32 v156, v156\n" \
"v_mfma_f32_16x16x32_bf16 v[92:95], a[220:223], v[128:131], 0\n" \
"ds_read_b128 a[160:163], v248 offset:24576\n" \
"ds_read_b128 a[164:167], v248 offset:25600\n" \
"v_mfma_f32_16x16x32_bf16 v[92:95], a[252:255], v[132:135], v[92:95]\n" \
"s_waitcnt lgkmcnt(8)\n" \
"v_exp_f32 v157, v157\n" \
"v_mfma_f32_16x16x32_bf16 v[128:131], a[128:131], v[208:211], v[232:235]\n" \
"s_add_u32 s85, s94, 593920\n" \
"ds_write_b128 v249, a[104:107] offset:37888\n" \
"v_cvt_pk_bf16_f32 v150, v156, v157\n" \
"v_mfma_f32_16x16x32_bf16 v[128:131], a[132:135], v[212:215], v[128:131]\n" \
"v_exp_f32 v158, v158\n" \
"v_mfma_f32_16x16x32_bf16 v[128:131], a[136:139], v[216:219], v[128:131]\n" \
"buffer_store_dwordx4 a[112:115], v248, s[32:35], s85 offen offset:2048 sc1\n" \
"v_mfma_f32_16x16x32_bf16 v[128:131], a[140:143], v[220:223], v[128:131]\n" \
"s_waitcnt lgkmcnt(3)\n" \
"v_exp_f32 v159, v159\n" \
"v_mfma_f32_16x16x32_bf16 v[132:135], a[156:159], v[220:223], v[236:239]\n" \
"ds_read_b128 a[168:171], v248 offset:26624\n" \
"v_cvt_pk_bf16_f32 v151, v158, v159\n" \
"v_mfma_f32_16x16x32_bf16 v[132:135], a[152:155], v[216:219], v[132:135]\n" \
"ds_read_b128 a[172:175], v248 offset:27648\n" \
"ds_read_b128 a[188:191], v248 offset:31744\n" \
"v_mfma_f32_16x16x32_bf16 v[132:135], a[148:151], v[212:215], v[132:135]\n" \
"v_exp_f32 v128, v128\n" \
"v_mfma_f32_16x16x32_bf16 v[132:135], a[144:147], v[208:211], v[132:135]\n" \
"ds_read_b128 a[184:187], v248 offset:30720\n" \
"ds_read_b128 a[180:183], v248 offset:29696\n" \
"v_smfmac_f32_16x16x64_bf16 a[44:47], a[48:51], v[144:151], v241\n" \
"ds_read_b128 a[176:179], v248 offset:28672\n" \
"v_mfma_f32_16x16x32_bf16 v[96:99], a[224:227], v[148:151], 0\n" \
"v_mfma_f32_16x16x32_bf16 v[96:99], a[192:195], v[144:147], v[96:99]\n" \
"v_exp_f32 v129, v129\n" \
"v_mfma_f32_16x16x32_bf16 v[100:103], a[196:199], v[144:147], 0\n" \
"ds_read_b128 a[192:195], v242 offset:16384\n" \
"v_cvt_pk_bf16_f32 v128, v128, v129\n" \
"v_mfma_f32_16x16x32_bf16 v[100:103], a[228:231], v[148:151], v[100:103]\n" \
"ds_read_b128 a[196:199], v242 offset:17408\n" \
"v_mfma_f32_16x16x32_bf16 v[104:107], a[232:235], v[148:151], 0\n" \
"ds_write_b128 v249, a[112:115] offset:38912\n" \
"v_mfma_f32_16x16x32_bf16 v[104:107], a[200:203], v[144:147], v[104:107]\n" \
"v_mfma_f32_16x16x32_bf16 v[108:111], a[204:207], v[144:147], 0\n" \
"v_exp_f32 v130, v130\n" \
"v_mfma_f32_16x16x32_bf16 v[108:111], a[236:239], v[148:151], v[108:111]\n" \
"ds_read_b128 a[200:203], v242 offset:18432\n" \
"ds_read_b128 a[204:207], v242 offset:19456\n" \
"v_mfma_f32_16x16x32_bf16 v[112:115], a[240:243], v[148:151], 0\n" \
"v_mfma_f32_16x16x32_bf16 v[112:115], a[208:211], v[144:147], v[112:115]\n" \
"s_add_u32 s85, s94, 593920\n" \
"v_mfma_f32_16x16x32_bf16 v[116:119], a[212:215], v[144:147], 0\n" \
"v_exp_f32 v131, v131\n" \
"v_mfma_f32_16x16x32_bf16 v[116:119], a[244:247], v[148:151], v[116:119]\n" \
"buffer_store_dwordx4 a[120:123], v248, s[32:35], s85 offen offset:3072 sc1\n" \
"v_mfma_f32_16x16x32_bf16 v[120:123], a[248:251], v[148:151], 0\n" \
"ds_read_b128 a[208:211], v242 offset:20480\n" \
"v_cvt_pk_bf16_f32 v129, v130, v131\n" \
"v_mfma_f32_16x16x32_bf16 v[120:123], a[216:219], v[144:147], v[120:123]\n" \
"ds_read_b128 a[212:215], v242 offset:21504\n" \
"v_mfma_f32_16x16x32_bf16 v[124:127], a[220:223], v[144:147], 0\n" \
"v_exp_f32 v132, v132\n" \
"v_mfma_f32_16x16x32_bf16 v[124:127], a[252:255], v[148:151], v[124:127]\n" \
"ds_read_b128 a[216:219], v242 offset:22528\n" \
"ds_read_b128 a[220:223], v242 offset:23552\n" \
"v_mfma_f32_16x16x32_bf16 v[144:147], a[128:131], v[160:163], 0\n" \
"ds_read_b128 a[224:227], v242 offset:24576\n" \
"ds_read_b128 a[228:231], v242 offset:25600\n" \
"v_mfma_f32_16x16x32_bf16 v[144:147], a[132:135], v[164:167], v[144:147]\n" \
"ds_write_b128 v249, a[120:123] offset:39936\n" \
"v_mfma_f32_16x16x32_bf16 v[144:147], a[136:139], v[168:171], v[144:147]\n" \
"v_exp_f32 v133, v133\n" \
"v_mfma_f32_16x16x32_bf16 v[144:147], a[140:143], v[172:175], v[144:147]\n" \
"ds_read_b128 a[232:235], v242 offset:26624\n" \
"v_cvt_pk_bf16_f32 v130, v132, v133\n" \
"v_mfma_f32_16x16x32_bf16 v[148:151], a[156:159], v[172:175], 0\n" \
"ds_read_b128 a[236:239], v242 offset:27648\n" \
"ds_read_b128 a[240:243], v242 offset:28672\n" \
"v_mfma_f32_16x16x32_bf16 v[148:151], a[152:155], v[168:171], v[148:151]\n" \
"ds_read_b128 a[244:247], v242 offset:29696\n" \
"v_mfma_f32_16x16x32_bf16 v[148:151], a[148:151], v[164:167], v[148:151]\n" \
"v_mfma_f32_16x16x32_bf16 v[148:151], a[144:147], v[160:163], v[148:151]\n" \
"s_waitcnt lgkmcnt(15)\n" \
"v_exp_f32 v134, v134\n" \
"v_mfma_f32_16x16x32_bf16 v[152:155], a[160:163], v[160:163], 0\n" \
"ds_read_b128 a[248:251], v242 offset:30720\n" \
"v_mfma_f32_16x16x32_bf16 v[152:155], a[164:167], v[164:167], v[152:155]\n" \
"ds_read_b128 a[252:255], v242 offset:31744\n" \
"v_mfma_f32_16x16x32_bf16 v[152:155], a[168:171], v[168:171], v[152:155]\n" \
"v_mfma_f32_16x16x32_bf16 v[152:155], a[172:175], v[172:175], v[152:155]\n" \
"v_exp_f32 v135, v135\n" \
"v_mfma_f32_16x16x32_bf16 v[156:159], a[188:191], v[172:175], 0\n" \
"v_exp_f32 v144, v144\n" \
"v_mfma_f32_16x16x32_bf16 v[156:159], a[184:187], v[168:171], v[156:159]\n" \
"v_exp_f32 v145, v145\n" \
"v_mfma_f32_16x16x32_bf16 v[156:159], a[180:183], v[164:167], v[156:159]\n" \
"v_exp_f32 v146, v146\n" \
"v_cvt_pk_bf16_f32 v131, v134, v135\n" \
"v_mov_b32 v132, 0\n" \
"v_mov_b32 v133, 0\n" \
"v_mov_b32 v134, 0\n" \
"v_mov_b32 v135, 0\n" \
"v_mfma_f32_16x16x32_bf16 v[156:159], a[176:179], v[160:163], v[156:159]\n" \
"v_exp_f32 v147, v147\n" \
"v_smfmac_f32_16x16x64_bf16 a[36:39], a[48:51], v[128:135], v241\n" \
"s_waitcnt lgkmcnt(13)\n" \
"v_cvt_pk_bf16_f32 v144, v144, v145\n" \
"v_mfma_f32_16x16x32_bf16 v[32:35], a[192:195], v[128:131], v[32:35]\n" \
"v_exp_f32 v148, v148\n" \
"v_mfma_f32_16x16x32_bf16 v[36:39], a[196:199], v[128:131], v[36:39]\n" \
"v_exp_f32 v149, v149\n" \
"v_mfma_f32_16x16x32_bf16 v[40:43], a[200:203], v[128:131], v[40:43]\n" \
"v_exp_f32 v150, v150\n" \
"v_mfma_f32_16x16x32_bf16 v[44:47], a[204:207], v[128:131], v[44:47]\n" \
"s_waitcnt lgkmcnt(9)\n" \
"v_exp_f32 v151, v151\n" \
"v_mfma_f32_16x16x32_bf16 v[48:51], a[208:211], v[128:131], v[48:51]\n" \
"s_waitcnt vmcnt(2)\n" \
"v_cmp_eq_u32 vcc, 0, v253\n" \
"v_cvt_pk_bf16_f32 v145, v146, v147\n" \
"v_mfma_f32_16x16x32_bf16 v[52:55], a[212:215], v[128:131], v[52:55]\n" \
"v_exp_f32 v152, v152\n" \
"v_mfma_f32_16x16x32_bf16 v[56:59], a[216:219], v[128:131], v[56:59]\n" \
"v_cvt_pk_bf16_f32 v146, v148, v149\n" \
"v_cvt_pk_bf16_f32 v147, v150, v151\n" \
"v_mfma_f32_16x16x32_bf16 v[60:63], a[220:223], v[128:131], v[60:63]\n" \
"s_cbranch_vccnz .Lspw_%=\n" \
".Lsbw_%=:\n" \
"v_mfma_f32_16x16x32_bf16 v[128:131], a[128:131], v[176:179], 0\n" \
"s_mov_b32 m0, s19\n" \
"v_exp_f32 v153, v153\n" \
"v_mfma_f32_16x16x32_bf16 v[128:131], a[132:135], v[180:183], v[128:131]\n" \
"s_waitcnt lgkmcnt(6)\n" \
"s_barrier\n" \
"v_cmp_gt_u32 s[98:99], 32, v251\n" \
"v_mfma_f32_16x16x32_bf16 v[128:131], a[136:139], v[184:187], v[128:131]\n" \
"v_exp_f32 v154, v154\n" \
"v_mfma_f32_16x16x32_bf16 v[128:131], a[140:143], v[188:191], v[128:131]\n" \
"v_exp_f32 v155, v155\n" \
"v_mfma_f32_16x16x32_bf16 v[132:135], a[156:159], v[188:191], 0\n" \
"buffer_load_dwordx4 v248, s[32:35], s88 offen offset:0 sc1 lds\n" \
"v_mfma_f32_16x16x32_bf16 v[132:135], a[152:155], v[184:187], v[132:135]\n" \
"v_exp_f32 v156, v156\n" \
"v_mfma_f32_16x16x32_bf16 v[132:135], a[148:151], v[180:183], v[132:135]\n" \
"buffer_load_dwordx4 v248, s[32:35], s88 offen offset:1024 sc1 lds\n" \
"v_mfma_f32_16x16x32_bf16 v[132:135], a[144:147], v[176:179], v[132:135]\n" \
"v_exp_f32 v157, v157\n" \
"v_mfma_f32_16x16x32_bf16 v[136:139], a[160:163], v[176:179], 0\n" \
"v_cndmask_b32 v253, v253, 0, s[98:99]\n" \
"v_cvt_pk_bf16_f32 v148, v152, v153\n" \
"v_mfma_f32_16x16x32_bf16 v[136:139], a[164:167], v[180:183], v[136:139]\n" \
"v_exp_f32 v158, v158\n" \
"v_mfma_f32_16x16x32_bf16 v[136:139], a[168:171], v[184:187], v[136:139]\n" \
"buffer_load_dwordx4 v248, s[32:35], s88 offen offset:2048 sc1 lds\n" \
"v_mfma_f32_16x16x32_bf16 v[136:139], a[172:175], v[188:191], v[136:139]\n" \
"v_exp_f32 v159, v159\n" \
"v_mfma_f32_16x16x32_bf16 v[140:143], a[188:191], v[188:191], 0\n" \
"v_exp_f32 v128, v128\n" \
"v_mfma_f32_16x16x32_bf16 v[140:143], a[184:187], v[184:187], v[140:143]\n" \
"v_exp_f32 v129, v129\n" \
"v_mfma_f32_16x16x32_bf16 v[140:143], a[180:183], v[180:183], v[140:143]\n" \
"v_exp_f32 v130, v130\n" \
"v_cvt_pk_bf16_f32 v149, v154, v155\n" \
"v_cvt_pk_bf16_f32 v150, v156, v157\n" \
"v_cvt_pk_bf16_f32 v151, v158, v159\n" \
"v_mfma_f32_16x16x32_bf16 v[140:143], a[176:179], v[176:179], v[140:143]\n" \
"v_exp_f32 v131, v131\n" \
"v_smfmac_f32_16x16x64_bf16 a[40:43], a[48:51], v[144:151], v241\n" \
"s_waitcnt lgkmcnt(4)\n" \
"buffer_load_dwordx4 v248, s[32:35], s88 offen offset:3072 sc1 lds\n" \
"v_mfma_f32_16x16x32_bf16 v[64:67], a[224:227], v[148:151], v[64:67]\n" \
"s_add_u32 s89, s88, 589824\n" \
"v_exp_f32 v132, v132\n" \
"v_mfma_f32_16x16x32_bf16 v[64:67], a[192:195], v[144:147], v[64:67]\n" \
"s_add_u32 s88, s88, s27\n" \
"v_exp_f32 v133, v133\n" \
"v_mfma_f32_16x16x32_bf16 v[68:71], a[196:199], v[144:147], v[68:71]\n" \
"s_add_u32 s19, s19, 0x4000\n" \
"v_exp_f32 v134, v134\n" \
"v_mfma_f32_16x16x32_bf16 v[68:71], a[228:231], v[148:151], v[68:71]\n" \
"s_bitset0_b32 s19, 16\n" \
"v_exp_f32 v135, v135\n" \
"v_mfma_f32_16x16x32_bf16 v[72:75], a[232:235], v[148:151], v[72:75]\n" \
"s_cmp_eq_u32 s88, s16\n" \
"v_max_u32_dpp v253, v253, v253 quad_perm:[1,0,3,2] row_mask:0xf bank_mask:0xf\n" \
"v_cvt_pk_bf16_f32 v128, v128, v129\n" \
"v_mfma_f32_16x16x32_bf16 v[72:75], a[200:203], v[144:147], v[72:75]\n" \
"s_cmov_b32 s88, s21\n" \
"v_exp_f32 v136, v136\n" \
"v_mfma_f32_16x16x32_bf16 v[76:79], a[204:207], v[144:147], v[76:79]\n" \
"s_cmov_b32 s27, s91\n" \
"v_max_u32_dpp v253, v253, v253 quad_perm:[2,3,0,1] row_mask:0xf bank_mask:0xf\n" \
"v_cvt_pk_bf16_f32 v129, v130, v131\n" \
"v_mfma_f32_16x16x32_bf16 v[76:79], a[236:239], v[148:151], v[76:79]\n" \
"s_waitcnt lgkmcnt(0)\n" \
"v_exp_f32 v137, v137\n" \
"v_mfma_f32_16x16x32_bf16 v[80:83], a[240:243], v[148:151], v[80:83]\n" \
"s_cmov_b32 s16, s77\n" \
"v_max_u32_dpp v253, v253, v253 row_half_mirror row_mask:0xf bank_mask:0xf\n" \
"v_cvt_pk_bf16_f32 v130, v132, v133\n" \
"v_mfma_f32_16x16x32_bf16 v[80:83], a[208:211], v[144:147], v[80:83]\n" \
"s_cmov_b32 s21, s92\n" \
"ds_read_b128 a[128:131], v248 offset:32768\n" \
"ds_read_b128 a[132:135], v248 offset:33792\n" \
"ds_read_b128 a[136:139], v248 offset:34816\n" \
"ds_read_b128 a[140:143], v248 offset:35840\n" \
"v_exp_f32 v138, v138\n" \
"v_mfma_f32_16x16x32_bf16 v[84:87], a[212:215], v[144:147], v[84:87]\n" \
"s_cmovk_i32 s91, 0x4000\n" \
"v_max_u32_dpp v253, v253, v253 row_mirror row_mask:0xf bank_mask:0xf\n" \
"v_mov_b32 v254, v253\n" \
"v_mfma_f32_16x16x32_bf16 v[84:87], a[244:247], v[148:151], v[84:87]\n" \
"s_mov_b32 m0, s20\n" \
"v_exp_f32 v139, v139\n" \
"v_mfma_f32_16x16x32_bf16 v[88:91], a[248:251], v[148:151], v[88:91]\n" \
"buffer_load_dwordx4 v248, s[32:35], s89 offen offset:0 sc1 lds\n" \
"v_mfma_f32_16x16x32_bf16 v[88:91], a[216:219], v[144:147], v[88:91]\n" \
"ds_read_b128 a[156:159], v248 offset:39936\n" \
"ds_read_b128 a[152:155], v248 offset:38912\n" \
"ds_read_b128 a[148:151], v248 offset:37888\n" \
"ds_read_b128 a[144:147], v248 offset:36864\n" \
"v_exp_f32 v140, v140\n" \
"v_mfma_f32_16x16x32_bf16 v[92:95], a[220:223], v[144:147], v[92:95]\n" \
"buffer_load_dwordx4 v248, s[32:35], s89 offen offset:1024 sc1 lds\n" \
"v_mfma_f32_16x16x32_bf16 v[92:95], a[252:255], v[148:151], v[92:95]\n" \
"s_waitcnt lgkmcnt(4)\n" \
"v_exp_f32 v141, v141\n" \
"v_mfma_f32_16x16x32_bf16 v[144:147], a[128:131], v[176:179], v[232:235]\n" \
"buffer_load_dwordx4 v248, s[32:35], s89 offen offset:2048 sc1 lds\n" \
"v_mfma_f32_16x16x32_bf16 v[144:147], a[132:135], v[180:183], v[144:147]\n" \
"v_exp_f32 v142, v142\n" \
"v_mfma_f32_16x16x32_bf16 v[144:147], a[136:139], v[184:187], v[144:147]\n" \
"buffer_load_dwordx4 v248, s[32:35], s89 offen offset:3072 sc1 lds\n" \
"v_mfma_f32_16x16x32_bf16 v[144:147], a[140:143], v[188:191], v[144:147]\n" \
"s_waitcnt lgkmcnt(0)\n" \
"v_exp_f32 v143, v143\n" \
"v_mfma_f32_16x16x32_bf16 v[148:151], a[156:159], v[188:191], v[236:239]\n" \
"s_add_u32 s20, s20, 0x4000\n" \
"v_permlane16_swap_b32 v253, v254\n" \
"v_mfma_f32_16x16x32_bf16 v[148:151], a[152:155], v[184:187], v[148:151]\n" \
"s_and_b32 s20, s20, 0xffff\n" \
"v_max_u32 v253, v253, v254\n" \
"v_mov_b32 v254, v253\n" \
"v_mfma_f32_16x16x32_bf16 v[148:151], a[148:151], v[180:183], v[148:151]\n" \
"s_bitset1_b32 s20, 16\n" \
"v_exp_f32 v144, v144\n" \
"v_cvt_pk_bf16_f32 v131, v134, v135\n" \
"v_cvt_pk_bf16_f32 v132, v136, v137\n" \
"v_cvt_pk_bf16_f32 v133, v138, v139\n" \
"v_cvt_pk_bf16_f32 v134, v140, v141\n" \
"v_cvt_pk_bf16_f32 v135, v142, v143\n" \
"v_mfma_f32_16x16x32_bf16 v[148:151], a[144:147], v[176:179], v[148:151]\n" \
"s_mov_b32 m0, s19\n" \
"v_permlane32_swap_b32 v253, v254\n" \
"v_smfmac_f32_16x16x64_bf16 a[44:47], a[48:51], v[128:135], v241\n" \
"s_barrier\n" \
"buffer_load_dwordx4 v248, s[32:35], s88 offen offset:0 sc1 lds\n" \
"v_mfma_f32_16x16x32_bf16 v[96:99], a[224:227], v[132:135], v[96:99]\n" \
"buffer_load_dwordx4 v248, s[32:35], s88 offen offset:1024 sc1 lds\n" \
"v_mfma_f32_16x16x32_bf16 v[96:99], a[192:195], v[128:131], v[96:99]\n" \
"v_exp_f32 v145, v145\n" \
"v_mfma_f32_16x16x32_bf16 v[100:103], a[196:199], v[128:131], v[100:103]\n" \
"s_lshl_b32 s85, s68, 3\n" \
"buffer_load_dwordx4 v248, s[32:35], s88 offen offset:2048 sc1 lds\n" \
"v_mfma_f32_16x16x32_bf16 v[100:103], a[228:231], v[132:135], v[100:103]\n" \
"s_add_u32 s85, s85, s67\n" \
"buffer_load_dwordx4 v248, s[32:35], s88 offen offset:3072 sc1 lds\n" \
"v_mfma_f32_16x16x32_bf16 v[104:107], a[232:235], v[132:135], v[104:107]\n" \
"s_add_u32 s89, s88, 589824\n" \
"v_max_u32 v253, v253, v254\n" \
"v_max_f32 v254, 1.0, v253\n" \
"v_mfma_f32_16x16x32_bf16 v[104:107], a[200:203], v[128:131], v[104:107]\n" \
"s_add_u32 s88, s88, s27\n" \
"v_min_f32 v253, 1.0, v253\n" \
"v_cvt_pk_bf16_f32 v144, v144, v145\n" \
"v_mfma_f32_16x16x32_bf16 v[108:111], a[204:207], v[128:131], v[108:111]\n" \
"s_add_u32 s19, s19, 0x4000\n" \
"v_exp_f32 v146, v146\n" \
"v_mfma_f32_16x16x32_bf16 v[108:111], a[236:239], v[132:135], v[108:111]\n" \
"s_bitset0_b32 s19, 16\n" \
"v_rcp_f32 v254, v254\n" \
"v_mfma_f32_16x16x32_bf16 v[112:115], a[240:243], v[132:135], v[112:115]\n" \
"s_cmp_eq_u32 s88, s16\n" \
"v_rcp_f32 v253, v253\n" \
"v_mfma_f32_16x16x32_bf16 v[112:115], a[208:211], v[128:131], v[112:115]\n" \
"s_cmov_b32 s88, s21\n" \
"ds_read_b128 a[128:131], v248 offset:49152\n" \
"ds_read_b128 a[132:135], v248 offset:50176\n" \
"ds_read_b128 a[136:139], v248 offset:51200\n" \
"ds_read_b128 a[140:143], v248 offset:52224\n" \
"v_mul_f32 v250, 0x7e000000, v254\n" \
"v_mul_f32 v246, 0x0d800000, v253\n" \
"v_mfma_f32_16x16x32_bf16 v[116:119], a[212:215], v[128:131], v[116:119]\n" \
"s_cmov_b32 s27, s91\n" \
"v_exp_f32 v147, v147\n" \
"v_mfma_f32_16x16x32_bf16 v[116:119], a[244:247], v[132:135], v[116:119]\n" \
"s_cmov_b32 s16, s77\n" \
"ds_read_b128 a[156:159], v248 offset:56320\n" \
"v_cvt_pk_bf16_f32 v145, v146, v147\n" \
"v_mfma_f32_16x16x32_bf16 v[120:123], a[248:251], v[132:135], v[120:123]\n" \
"s_cmov_b32 s21, s92\n" \
"ds_read_b128 a[152:155], v248 offset:55296\n" \
"ds_read_b128 a[148:151], v248 offset:54272\n" \
"v_mfma_f32_16x16x32_bf16 v[120:123], a[216:219], v[128:131], v[120:123]\n" \
"s_cmovk_i32 s91, 0x4000\n" \
"ds_read_b128 a[144:147], v248 offset:53248\n" \
"ds_read_b128 a[160:163], v248 offset:57344\n" \
"v_mfma_f32_16x16x32_bf16 v[124:127], a[220:223], v[128:131], v[124:127]\n" \
"s_mov_b32 m0, s20\n" \
"v_exp_f32 v148, v148\n" \
"v_mfma_f32_16x16x32_bf16 v[124:127], a[252:255], v[132:135], v[124:127]\n" \
"s_waitcnt lgkmcnt(5)\n" \
"buffer_load_dwordx4 v248, s[32:35], s89 offen offset:0 sc1 lds\n" \
"v_mfma_f32_16x16x32_bf16 v[128:131], a[128:131], v[160:163], 0\n" \
"s_lshl2_add_u32 s85, s85, %[wid]\n" \
"buffer_load_dwordx4 v248, s[32:35], s89 offen offset:1024 sc1 lds\n" \
"v_mfma_f32_16x16x32_bf16 v[128:131], a[132:135], v[164:167], v[128:131]\n" \
"s_lshl_b32 s85, s85, 2\n" \
"buffer_load_dwordx4 v248, s[32:35], s89 offen offset:2048 sc1 lds\n" \
"ds_read_b128 a[164:167], v248 offset:58368\n" \
"ds_read_b128 a[168:171], v248 offset:59392\n" \
"ds_read_b128 a[172:175], v248 offset:60416\n" \
"v_mfma_f32_16x16x32_bf16 v[128:131], a[136:139], v[168:171], v[128:131]\n" \
"s_add_u32 s85, s85, 655360\n" \
"v_exp_f32 v149, v149\n" \
"v_mfma_f32_16x16x32_bf16 v[128:131], a[140:143], v[172:175], v[128:131]\n" \
"s_waitcnt lgkmcnt(4)\n" \
"buffer_load_dwordx4 v248, s[32:35], s89 offen offset:3072 sc1 lds\n" \
"v_mfma_f32_16x16x32_bf16 v[132:135], a[156:159], v[172:175], 0\n" \
"s_waitcnt vmcnt(16)\n" \
"s_add_u32 s20, s20, 0x4000\n" \
"buffer_store_dword a56, off, s[32:35], s85 sc1\n" \
"v_mfma_f32_16x16x32_bf16 v[132:135], a[152:155], v[168:171], v[132:135]\n" \
"s_and_b32 s20, s20, 0xffff\n" \
"ds_read_b128 a[192:195], v242 offset:32768\n" \
"ds_read_b128 a[196:199], v242 offset:33792\n" \
"ds_read_b128 a[200:203], v242 offset:34816\n" \
"ds_read_b128 a[188:191], v248 offset:64512\n" \
"ds_read_b128 a[204:207], v242 offset:35840\n" \
"ds_read_b128 a[184:187], v248 offset:63488\n" \
"ds_read_b128 a[180:183], v248 offset:62464\n" \
"ds_read_b128 a[176:179], v248 offset:61440\n" \
"v_cvt_pk_bf16_f32 v146, v148, v149\n" \
"v_mfma_f32_16x16x32_bf16 v[132:135], a[148:151], v[164:167], v[132:135]\n" \
"s_bitset1_b32 s20, 16\n" \
"ds_read_b128 a[208:211], v242 offset:36864\n" \
"ds_read_b128 a[212:215], v242 offset:37888\n" \
"v_mfma_f32_16x16x32_bf16 v[132:135], a[144:147], v[160:163], v[132:135]\n" \
"s_waitcnt lgkmcnt(10)\n" \
"v_exp_f32 v150, v150\n" \
"v_mfma_f32_16x16x32_bf16 v[136:139], a[160:163], v[160:163], v[224:227]\n" \
"s_mov_b32 m0, s19\n" \
"ds_read_b128 a[216:219], v242 offset:38912\n" \
"ds_read_b128 a[220:223], v242 offset:39936\n" \
"v_mfma_f32_16x16x32_bf16 v[136:139], a[164:167], v[164:167], v[136:139]\n" \
"buffer_load_dwordx4 v248, s[32:35], s88 offen offset:0 sc1 lds\n" \
"v_mfma_f32_16x16x32_bf16 v[136:139], a[168:171], v[168:171], v[136:139]\n" \
"buffer_load_dwordx4 v248, s[32:35], s88 offen offset:1024 sc1 lds\n" \
"v_mfma_f32_16x16x32_bf16 v[136:139], a[172:175], v[172:175], v[136:139]\n" \
"s_waitcnt lgkmcnt(4)\n" \
"v_exp_f32 v151, v151\n" \
"v_mfma_f32_16x16x32_bf16 v[140:143], a[188:191], v[172:175], v[228:231]\n" \
"v_exp_f32 v128, v128\n" \
"v_mfma_f32_16x16x32_bf16 v[140:143], a[184:187], v[168:171], v[140:143]\n" \
"v_exp_f32 v129, v129\n" \
"v_mfma_f32_16x16x32_bf16 v[140:143], a[180:183], v[164:167], v[140:143]\n" \
"v_exp_f32 v130, v130\n" \
"v_cvt_pk_bf16_f32 v147, v150, v151\n" \
"v_mov_b32 v148, 0\n" \
"v_mov_b32 v149, 0\n" \
"v_mov_b32 v150, 0\n" \
"v_mov_b32 v151, 0\n" \
"v_mfma_f32_16x16x32_bf16 v[140:143], a[176:179], v[160:163], v[140:143]\n" \
"v_exp_f32 v131, v131\n" \
"v_smfmac_f32_16x16x64_bf16 a[44:47], a[48:51], v[144:151], v241\n" \
"buffer_load_dwordx4 v248, s[32:35], s88 offen offset:2048 sc1 lds\n" \
"v_mfma_f32_16x16x32_bf16 v[96:99], a[192:195], v[144:147], v[96:99]\n" \
"v_exp_f32 v132, v132\n" \
"v_mfma_f32_16x16x32_bf16 v[100:103], a[196:199], v[144:147], v[100:103]\n" \
"v_exp_f32 v133, v133\n" \
"v_mfma_f32_16x16x32_bf16 v[104:107], a[200:203], v[144:147], v[104:107]\n" \
"v_exp_f32 v134, v134\n" \
"v_mfma_f32_16x16x32_bf16 v[108:111], a[204:207], v[144:147], v[108:111]\n" \
"s_waitcnt lgkmcnt(0)\n" \
"v_exp_f32 v135, v135\n" \
"v_mfma_f32_16x16x32_bf16 v[112:115], a[208:211], v[144:147], v[112:115]\n" \
"buffer_load_dwordx4 v248, s[32:35], s88 offen offset:3072 sc1 lds\n" \
"v_mfma_f32_16x16x32_bf16 v[116:119], a[212:215], v[144:147], v[116:119]\n" \
"s_add_u32 s89, s88, 589824\n" \
"v_exp_f32 v136, v136\n" \
"v_mfma_f32_16x16x32_bf16 v[120:123], a[216:219], v[144:147], v[120:123]\n" \
"s_add_u32 s88, s88, s27\n" \
"v_cvt_pk_bf16_f32 v128, v128, v129\n" \
"v_cvt_pk_bf16_f32 v129, v130, v131\n" \
"v_mfma_f32_16x16x32_bf16 v[124:127], a[220:223], v[144:147], v[124:127]\n" \
"s_add_u32 s19, s19, 0x4000\n" \
"v_exp_f32 v137, v137\n" \
"v_mfma_f32_16x16x32_bf16 v[144:147], a[128:131], v[176:179], 0\n" \
"s_waitcnt vmcnt(13)\n" \
"s_barrier\n" \
"v_cvt_pk_bf16_f32 v130, v132, v133\n" \
"v_mfma_f32_16x16x32_bf16 v[144:147], a[132:135], v[180:183], v[144:147]\n" \
"s_bitset0_b32 s19, 16\n" \
"v_exp_f32 v138, v138\n" \
"v_mfma_f32_16x16x32_bf16 v[144:147], a[136:139], v[184:187], v[144:147]\n" \
"s_cmp_eq_u32 s88, s16\n" \
"v_cvt_pk_bf16_f32 v131, v134, v135\n" \
"v_cvt_pk_bf16_f32 v132, v136, v137\n" \
"v_mfma_f32_16x16x32_bf16 v[144:147], a[140:143], v[188:191], v[144:147]\n" \
"s_cmov_b32 s88, s21\n" \
"v_exp_f32 v139, v139\n" \
"v_mfma_f32_16x16x32_bf16 v[148:151], a[156:159], v[188:191], 0\n" \
"s_cmov_b32 s27, s91\n" \
"ds_read_b128 a[224:227], v242 offset:57344\n" \
"v_cvt_pk_bf16_f32 v133, v138, v139\n" \
"v_mfma_f32_16x16x32_bf16 v[148:151], a[152:155], v[184:187], v[148:151]\n" \
"s_cmov_b32 s16, s77\n" \
"v_exp_f32 v140, v140\n" \
"v_mfma_f32_16x16x32_bf16 v[148:151], a[148:151], v[180:183], v[148:151]\n" \
"s_cmov_b32 s21, s92\n" \
"ds_read_b128 a[192:195], v242 offset:49152\n" \
"ds_read_b128 a[196:199], v242 offset:50176\n" \
"v_mfma_f32_16x16x32_bf16 v[148:151], a[144:147], v[176:179], v[148:151]\n" \
"s_cmovk_i32 s91, 0x4000\n" \
"v_exp_f32 v141, v141\n" \
"v_mfma_f32_16x16x32_bf16 v[152:155], a[160:163], v[176:179], 0\n" \
"s_mov_b32 m0, s20\n" \
"ds_read_b128 a[228:231], v242 offset:58368\n" \
"v_cvt_pk_bf16_f32 v134, v140, v141\n" \
"v_mfma_f32_16x16x32_bf16 v[152:155], a[164:167], v[180:183], v[152:155]\n" \
"v_exp_f32 v142, v142\n" \
"v_mfma_f32_16x16x32_bf16 v[152:155], a[168:171], v[184:187], v[152:155]\n" \
"buffer_load_dwordx4 v248, s[32:35], s89 offen offset:0 sc1 lds\n" \
"v_mfma_f32_16x16x32_bf16 v[152:155], a[172:175], v[188:191], v[152:155]\n" \
"v_exp_f32 v143, v143\n" \
"v_mfma_f32_16x16x32_bf16 v[156:159], a[188:191], v[188:191], 0\n" \
"buffer_load_dwordx4 v248, s[32:35], s89 offen offset:1024 sc1 lds\n" \
"v_mfma_f32_16x16x32_bf16 v[156:159], a[184:187], v[184:187], v[156:159]\n" \
"ds_read_b128 a[232:235], v242 offset:59392\n" \
"v_cvt_pk_bf16_f32 v135, v142, v143\n" \
"v_mfma_f32_16x16x32_bf16 v[156:159], a[180:183], v[180:183], v[156:159]\n" \
"ds_read_b128 a[200:203], v242 offset:51200\n" \
"ds_read_b128 a[204:207], v242 offset:52224\n" \
"ds_read_b128 a[236:239], v242 offset:60416\n" \
"v_exp_f32 v144, v144\n" \
"v_mfma_f32_16x16x32_bf16 v[156:159], a[176:179], v[176:179], v[156:159]\n" \
"buffer_load_dwordx4 v248, s[32:35], s89 offen offset:2048 sc1 lds\n" \
"v_smfmac_f32_16x16x64_bf16 a[40:43], a[48:51], v[128:135], v241\n" \
"s_waitcnt lgkmcnt(4)\n" \
"ds_read_b128 a[240:243], v242 offset:61440\n" \
"ds_read_b128 a[208:211], v242 offset:53248\n" \
"v_mfma_f32_16x16x32_bf16 v[64:67], a[224:227], v[132:135], v[64:67]\n" \
"v_exp_f32 v145, v145\n" \
"v_mfma_f32_16x16x32_bf16 v[64:67], a[192:195], v[128:131], v[64:67]\n" \
"ds_read_b128 a[212:215], v242 offset:54272\n" \
"ds_read_b128 a[128:131], v248 offset:0\n" \
"ds_read_b128 a[244:247], v242 offset:62464\n" \
"v_exp_f32 v146, v146\n" \
"v_mfma_f32_16x16x32_bf16 v[68:71], a[196:199], v[128:131], v[68:71]\n" \
"ds_read_b128 a[132:135], v248 offset:1024\n" \
"v_cvt_pk_bf16_f32 v144, v144, v145\n" \
"v_mfma_f32_16x16x32_bf16 v[68:71], a[228:231], v[132:135], v[68:71]\n" \
"s_waitcnt lgkmcnt(6)\n" \
"v_exp_f32 v147, v147\n" \
"v_mfma_f32_16x16x32_bf16 v[72:75], a[232:235], v[132:135], v[72:75]\n" \
"buffer_load_dwordx4 v248, s[32:35], s89 offen offset:3072 sc1 lds\n" \
"v_mfma_f32_16x16x32_bf16 v[72:75], a[200:203], v[128:131], v[72:75]\n" \
"s_add_u32 s20, s20, 0x4000\n" \
"ds_read_b128 a[136:139], v248 offset:2048\n" \
"ds_read_b128 a[140:143], v248 offset:3072\n" \
"ds_read_b128 a[248:251], v242 offset:63488\n" \
"ds_read_b128 a[216:219], v242 offset:55296\n" \
"ds_read_b128 a[220:223], v242 offset:56320\n" \
"ds_read_b128 a[156:159], v248 offset:7168\n" \
"ds_read_b128 a[252:255], v242 offset:64512\n" \
"v_exp_f32 v148, v148\n" \
"v_mfma_f32_16x16x32_bf16 v[76:79], a[204:207], v[128:131], v[76:79]\n" \
"s_and_b32 s20, s20, 0xffff\n" \
"ds_read_b128 a[152:155], v248 offset:6144\n" \
"v_cvt_pk_bf16_f32 v145, v146, v147\n" \
"v_mfma_f32_16x16x32_bf16 v[76:79], a[236:239], v[132:135], v[76:79]\n" \
"s_waitcnt lgkmcnt(9)\n" \
"v_exp_f32 v149, v149\n" \
"v_mfma_f32_16x16x32_bf16 v[80:83], a[240:243], v[132:135], v[80:83]\n" \
"s_bitset1_b32 s20, 16\n" \
"ds_read_b128 a[148:151], v248 offset:5120\n" \
"v_cvt_pk_bf16_f32 v146, v148, v149\n" \
"v_mfma_f32_16x16x32_bf16 v[80:83], a[208:211], v[128:131], v[80:83]\n" \
"v_exp_f32 v150, v150\n" \
"v_mfma_f32_16x16x32_bf16 v[84:87], a[212:215], v[128:131], v[84:87]\n" \
"ds_read_b128 a[144:147], v248 offset:4096\n" \
"ds_read_b128 a[160:163], v248 offset:8192\n" \
"v_mfma_f32_16x16x32_bf16 v[84:87], a[244:247], v[132:135], v[84:87]\n" \
"s_waitcnt lgkmcnt(4)\n" \
"v_exp_f32 v151, v151\n" \
"v_mfma_f32_16x16x32_bf16 v[88:91], a[248:251], v[132:135], v[88:91]\n" \
"ds_read_b128 a[164:167], v248 offset:9216\n" \
"v_cvt_pk_bf16_f32 v147, v150, v151\n" \
"v_mfma_f32_16x16x32_bf16 v[88:91], a[216:219], v[128:131], v[88:91]\n" \
"v_exp_f32 v152, v152\n" \
"v_mfma_f32_16x16x32_bf16 v[92:95], a[220:223], v[128:131], v[92:95]\n" \
"v_exp_f32 v153, v153\n" \
"v_mfma_f32_16x16x32_bf16 v[92:95], a[252:255], v[132:135], v[92:95]\n" \
"ds_read_b128 a[168:171], v248 offset:10240\n" \
"ds_read_b128 a[172:175], v248 offset:11264\n" \
"v_and_b32 v230, 7, v251\n" \
"v_lshrrev_b32 v232, 3, v251\n" \
"v_lshlrev_b32 v231, 4, v230\n" \
"v_mul_u32_u24 v230, 7168, v230\n" \
"v_lshl_add_u32 v230, v232, 4, v230\n" \
"v_lshl_or_b32 v232, v232, 14, v231\n" \
"v_and_b32 v231, 3, v251\n" \
"v_lshlrev_b32 v231, 7, v231\n" \
"v_bfe_u32 v237, v251, 4, 1\n" \
"v_lshlrev_b32 v237, 1, v237\n" \
"v_and_b32 v238, 3, v251\n" \
"v_xor_b32 v237, v237, v238\n" \
"v_lshrrev_b32 v238, 5, v251\n" \
"v_lshl_or_b32 v237, v238, 2, v237\n" \
"v_bfe_u32 v238, v251, 2, 3\n" \
"v_lshl_or_b32 v237, v238, 6, v237\n" \
"v_lshlrev_b32 v237, 4, v237\n" \
"v_and_b32 v238, 7, v251\n" \
"v_lshlrev_b32 v238, 7, v238\n" \
"s_branch .Ldj_%=\n" \
".Lspq_%=:\n" \
"s_mov_b32 s82, 0x100000\n" \
".Lsqq_%=:\n" \
"s_sleep 6\n" \
"buffer_load_dwordx2 v[204:205], v253, s[32:35], s81 offen sc1\n" \
"s_waitcnt vmcnt(0)\n" \
"v_cmp_ne_u64 vcc, v[204:205], v[202:203]\n" \
"s_cbranch_vccz .Lsbq_%=\n" \
"s_sub_u32 s82, s82, 1\n" \
"s_cbranch_scc0 .Lsqq_%=\n" \
"s_branch .Lsbq_%=\n" \
".Lsph_%=:\n" \
"s_mov_b32 s82, 0x100000\n" \
".Lsqh_%=:\n" \
"s_sleep 6\n" \
"buffer_load_dword v253, v252, s[32:35], s81 offen sc1\n" \
"s_waitcnt vmcnt(0)\n" \
"v_cmp_eq_u32 vcc, 0, v253\n" \
"s_cbranch_vccz .Lsbh_%=\n" \
"s_sub_u32 s82, s82, 1\n" \
"s_cbranch_scc0 .Lsqh_%=\n" \
"s_branch .Lsbh_%=\n" \
".Lspv_%=:\n" \
"s_mov_b32 s82, 0x100000\n" \
".Lsqv_%=:\n" \
"s_sleep 6\n" \
"buffer_load_dword v253, v252, s[32:35], s81 offen sc1\n" \
"s_waitcnt vmcnt(0)\n" \
"v_cmp_eq_u32 vcc, 0, v253\n" \
"s_cbranch_vccz .Lsbv_%=\n" \
"s_sub_u32 s82, s82, 1\n" \
"s_cbranch_scc0 .Lsqv_%=\n" \
"s_branch .Lsbv_%=\n" \
".Lspw_%=:\n" \
"s_mov_b32 s82, 0x100000\n" \
".Lsqw_%=:\n" \
"s_sleep 6\n" \
"buffer_load_dword v253, v252, s[32:35], s81 offen sc1\n" \
"s_waitcnt vmcnt(0)\n" \
"v_cmp_eq_u32 vcc, 0, v253\n" \
"s_cbranch_vccz .Lsbw_%=\n" \
"s_sub_u32 s82, s82, 1\n" \
"s_cbranch_scc0 .Lsqw_%=\n" \
"s_branch .Lsbw_%=\n" \
".Lchd_%=:\n" \
".Lst1_9_%=:\n" \
"s_cbranch_execz .Lst1_13_%=\n" \
"v_cmp_eq_u32 vcc, 0, v247\n" \
"s_sub_u32 s23, s18, 262144\n" \
"s_cmp_eq_u32 s24, s23\n" \
"s_cbranch_scc1 .Ldhd_%=\n" \
"s_mov_b32 s24, s23\n" \
"buffer_load_dword v247, v244, s[32:35], s26 offen sc1\n" \
"s_branch .Lsbhd_%=\n" \
".Ldhd_%=:\n" \
"s_cbranch_vccz .Lsbhd_%=\n" \
".Lsphd_%=:\n" \
"s_mov_b32 s82, 0x100000\n" \
".Lsqhd_%=:\n" \
"s_sleep 6\n" \
"buffer_load_dword v247, v244, s[32:35], s26 offen sc1\n" \
"s_waitcnt vmcnt(0)\n" \
"v_cmp_eq_u32 vcc, 0, v247\n" \
"s_cbranch_vccz .Lsbhd_%=\n" \
"s_sub_u32 s82, s82, 1\n" \
"s_cbranch_scc0 .Lsqhd_%=\n" \
"s_branch .Lsbhd_%=\n" \
".Land_%=:\n" \
".Lst1_10_%=:\n" \
"s_cbranch_execz .Lst1_14_%=\n" \
"s_waitcnt vmcnt(4)\n" \
"buffer_store_dword a56, off, s[32:35], s25 sc1\n" \
"buffer_store_dword a56, off, s[32:35], s78 sc1\n" \
"s_mov_b32 s78, 1073741824\n" \
"s_mov_b32 s76, 0\n" \
"s_branch .Labd_%=\n" \
".Lcht_%=:\n" \
".Lst1_11_%=:\n" \
"s_cbranch_execz .Lst1_15_%=\n" \
"v_cmp_eq_u32 vcc, 0, v247\n" \
"s_sub_u32 s23, s18, 262144\n" \
"s_cmp_eq_u32 s24, s23\n" \
"s_cbranch_scc1 .Ldht_%=\n" \
"s_mov_b32 s24, s23\n" \
"buffer_load_dword v247, v244, s[32:35], s26 offen sc1\n" \
"s_branch .Lsbht_%=\n" \
".Ldht_%=:\n" \
"s_cbranch_vccz .Lsbht_%=\n" \
".Lspht_%=:\n" \
"s_mov_b32 s82, 0x100000\n" \
".Lsqht_%=:\n" \
"s_sleep 6\n" \
"buffer_load_dword v247, v244, s[32:35], s26 offen sc1\n" \
"s_waitcnt vmcnt(0)\n" \
"v_cmp_eq_u32 vcc, 0, v247\n" \
"s_cbranch_vccz .Lsbht_%=\n" \
"s_sub_u32 s82, s82, 1\n" \
"s_cbranch_scc0 .Lsqht_%=\n" \
"s_branch .Lsbht_%=\n" \
".Lant_%=:\n" \
".Lst1_12_%=:\n" \
"s_cbranch_execz .Lstb1_%=\n" \
"s_waitcnt vmcnt(9)\n" \
"buffer_store_dword a56, off, s[32:35], s25 sc1\n" \
"buffer_store_dword a56, off, s[32:35], s78 sc1\n" \
"s_mov_b32 s78, 1073741824\n" \
"s_mov_b32 s76, 0\n" \
"s_branch .Labt_%=\n" \
".Lchtx_%=:\n" \
".Lst1_13_%=:\n" \
"s_cbranch_execz .Lstb1_%=\n" \
"v_cmp_eq_u32 vcc, 0, v247\n" \
"s_sub_u32 s23, s18, 262144\n" \
"s_cmp_eq_u32 s24, s23\n" \
"s_cbranch_scc1 .Ldhtx_%=\n" \
"s_mov_b32 s24, s23\n" \
"buffer_load_dword v247, v244, s[32:35], s26 offen sc1\n" \
"s_branch .Lsbhtx_%=\n" \
".Ldhtx_%=:\n" \
"s_cbranch_vccz .Lsbhtx_%=\n" \
".Lsphtx_%=:\n" \
"s_mov_b32 s82, 0x100000\n" \
".Lsqhtx_%=:\n" \
"s_sleep 6\n" \
"buffer_load_dword v247, v244, s[32:35], s26 offen sc1\n" \
"s_waitcnt vmcnt(0)\n" \
"v_cmp_eq_u32 vcc, 0, v247\n" \
"s_cbranch_vccz .Lsbhtx_%=\n" \
"s_sub_u32 s82, s82, 1\n" \
"s_cbranch_scc0 .Lsqhtx_%=\n" \
"s_branch .Lsbhtx_%=\n" \
".Lantx_%=:\n" \
".Lst1_14_%=:\n" \
"s_cbranch_execz .Lstb1_%=\n" \
"s_waitcnt vmcnt(10)\n" \
"buffer_store_dword a56, off, s[32:35], s25 sc1\n" \
"buffer_store_dword a56, off, s[32:35], s78 sc1\n" \
"s_mov_b32 s78, 1073741824\n" \
"s_mov_b32 s76, 0\n" \
"s_branch .Labtx_%=\n" \
".Lspl_%=:\n" \
"s_mov_b32 s82, 0x100000\n" \
".Lsql_%=:\n" \
"s_sleep 6\n" \
"buffer_load_dword v247, v255, s[32:35], s31 offen sc1\n" \
"s_waitcnt vmcnt(0)\n" \
"v_cmp_eq_u32 vcc, 0, v247\n" \
"s_cbranch_vccz .Lsbl_%=\n" \
"s_sub_u32 s82, s82, 1\n" \
"s_cbranch_scc0 .Lsql_%=\n" \
"s_branch .Lsbl_%=\n" \
".Lrh_%=:\n" \
"s_call_b64 s[98:99], .Lredo2_%=\n" \
"v_accvgpr_read_b32 v224, a40\n" \
"v_accvgpr_read_b32 v226, a41\n" \
"v_add_f32 v224, v224, v226\n" \
"v_rcp_f32 v224, v224\n" \
"s_nop 0\n" \
"v_mul_f32 v64, v64, v224\n" \
"v_mul_f32 v65, v65, v224\n" \
"v_mul_f32 v66, v66, v224\n" \
"v_mul_f32 v67, v67, v224\n" \
"v_mul_f32 v68, v68, v224\n" \
"v_mul_f32 v69, v69, v224\n" \
"v_mul_f32 v70, v70, v224\n" \
"v_mul_f32 v71, v71, v224\n" \
"v_cvt_pk_bf16_f32 v192, v64, v65\n" \
"v_cvt_pk_bf16_f32 v193, v66, v67\n" \
"v_cvt_pk_bf16_f32 v194, v68, v69\n" \
"v_cvt_pk_bf16_f32 v195, v70, v71\n" \
"v_mul_f32 v72, v72, v224\n" \
"v_mul_f32 v73, v73, v224\n" \
"v_mul_f32 v74, v74, v224\n" \
"v_mul_f32 v75, v75, v224\n" \
"v_mul_f32 v76, v76, v224\n" \
"v_mul_f32 v77, v77, v224\n" \
"v_mul_f32 v78, v78, v224\n" \
"v_mul_f32 v79, v79, v224\n" \
"v_cvt_pk_bf16_f32 v200, v72, v73\n" \
"v_cvt_pk_bf16_f32 v201, v74, v75\n" \
"v_cvt_pk_bf16_f32 v202, v76, v77\n" \
"v_cvt_pk_bf16_f32 v203, v78, v79\n" \
"v_mul_f32 v80, v80, v224\n" \
"v_mul_f32 v81, v81, v224\n" \
"v_mul_f32 v82, v82, v224\n" \
"v_mul_f32 v83, v83, v224\n" \
"v_mul_f32 v84, v84, v224\n" \
"v_mul_f32 v85, v85, v224\n" \
"v_mul_f32 v86, v86, v224\n" \
"v_mul_f32 v87, v87, v224\n" \
"v_cvt_pk_bf16_f32 v208, v80, v81\n" \
"v_cvt_pk_bf16_f32 v209, v82, v83\n" \
"v_cvt_pk_bf16_f32 v210, v84, v85\n" \
"v_cvt_pk_bf16_f32 v211, v86, v87\n" \
"v_mul_f32 v88, v88, v224\n" \
"v_mul_f32 v89, v89, v224\n" \
"v_mul_f32 v90, v90, v224\n" \
"v_mul_f32 v91, v91, v224\n" \
"v_mul_f32 v92, v92, v224\n" \
"v_mul_f32 v93, v93, v224\n" \
"v_mul_f32 v94, v94, v224\n" \
"v_mul_f32 v95, v95, v224\n" \
"buffer_store_dwordx4 v[192:195], v248, s[32:35], s39 offen offset:0 sc1\n" \
"buffer_store_dwordx4 v[200:203], v248, s[32:35], s39 offen offset:2048 sc1\n" \
"buffer_store_dwordx4 v[208:211], v248, s[32:35], s19 offen offset:0 sc1\n" \
"s_waitcnt vmcnt(0)\n" \
"s_branch .Lrhb_%=\n" \
".LspcA_%=:\n" \
"s_mov_b32 s82, 0x100000\n" \
".LsqcA_%=:\n" \
"s_sleep 6\n" \
"buffer_load_dword v247, v254, s[32:35], s41 offen sc1\n" \
"s_waitcnt vmcnt(0)\n" \
"v_cmp_eq_u32 vcc, 0, v247\n" \
"s_cbranch_vccz .LsbcA_%=\n" \
"s_sub_u32 s82, s82, 1\n" \
"s_cbranch_scc0 .LsqcA_%=\n" \
"s_branch .LsbcA_%=\n" \
".Lspg_%=:\n" \
"s_mov_b32 s82, 0x100000\n" \
".Lsqg_%=:\n" \
"s_sleep 6\n" \
"buffer_load_dwordx2 v[230:231], v232, s[32:35], s70 offen sc1\n" \
"s_waitcnt vmcnt(0)\n" \
"v_cmp_ne_u64 vcc, v[230:231], v[234:235]\n" \
"s_cbranch_vccz .Lsbg_%=\n" \
"s_sub_u32 s82, s82, 1\n" \
"s_cbranch_scc0 .Lsqg_%=\n" \
"s_branch .Lsbg_%=\n" \
".Lch3_%=:\n" \
".Lst1_15_%=:\n" \
"s_cbranch_execz .Lstb1_%=\n" \
"s_mov_b64 exec, 0\n" \
"s_cmp_eq_u32 %[wid], 0\n" \
"s_cbranch_scc1 .Lst3_0_%=\n" \
"s_cmp_eq_u32 %[wid], 1\n" \
"s_cbranch_scc1 .Lst3_1_%=\n" \
"s_cmp_eq_u32 %[wid], 2\n" \
"s_cbranch_scc1 .Lst3_2_%=\n" \
"s_branch .Lst3_3_%=\n" \
".Lstb3_%=:\n" \
"s_mov_b64 exec, -1\n" \
"s_setpc_b64 s[98:99]\n" \
".Lga_%=:\n" \
".Lst3_1_%=:\n" \
"s_cbranch_execz .Lstb3_%=\n" \
"v_cmp_ne_u32 vcc, 0, v245\n" \
"s_nop 3\n" \
"s_lshr_b32 s76, vcc_lo, 1\n" \
"s_and_b32 s76, s76, vcc_lo\n" \
"s_lshr_b32 s77, s76, 2\n" \
"s_and_b32 s76, s76, s77\n" \
"s_sub_u32 s82, s82, 1\n" \
"s_cselect_b32 s76, -1, s76\n" \
"s_and_b32 s76, s76, 0x11111111\n" \
"s_andn2_b32 s76, s76, s74\n" \
"s_cbranch_scc0 .Lgx_%=\n" \
"s_or_b32 s74, s74, s76\n" \
"s_bitcmp1_b32 s76, 0\n" \
"s_cbranch_scc0 .Lgs0_%=\n" \
"s_add_u32 s24, s95, 0\n" \
"buffer_load_dwordx4 v[0:3], v214, s[32:35], s24 offen sc1\n" \
"buffer_load_dwordx4 v[32:35], v214, s[32:35], s24 offen offset:64 sc1\n" \
".Lgs0_%=:\n" \
"s_bitcmp1_b32 s76, 4\n" \
"s_cbranch_scc0 .Lgs1_%=\n" \
"s_add_u32 s24, s95, 2097152\n" \
"buffer_load_dwordx4 v[4:7], v214, s[32:35], s24 offen sc1\n" \
"buffer_load_dwordx4 v[36:39], v214, s[32:35], s24 offen offset:64 sc1\n" \
".Lgs1_%=:\n" \
"s_bitcmp1_b32 s76, 8\n" \
"s_cbranch_scc0 .Lgs2_%=\n" \
"s_add_u32 s24, s95, 4194304\n" \
"buffer_load_dwordx4 v[8:11], v214, s[32:35], s24 offen sc1\n" \
"buffer_load_dwordx4 v[40:43], v214, s[32:35], s24 offen offset:64 sc1\n" \
".Lgs2_%=:\n" \
"s_bitcmp1_b32 s76, 12\n" \
"s_cbranch_scc0 .Lgs3_%=\n" \
"s_add_u32 s24, s95, 6291456\n" \
"buffer_load_dwordx4 v[12:15], v214, s[32:35], s24 offen sc1\n" \
"buffer_load_dwordx4 v[44:47], v214, s[32:35], s24 offen offset:64 sc1\n" \
".Lgs3_%=:\n" \
"s_bitcmp1_b32 s76, 16\n" \
"s_cbranch_scc0 .Lgs4_%=\n" \
"s_add_u32 s24, s95, 8388608\n" \
"buffer_load_dwordx4 v[16:19], v214, s[32:35], s24 offen sc1\n" \
"buffer_load_dwordx4 v[48:51], v214, s[32:35], s24 offen offset:64 sc1\n" \
".Lgs4_%=:\n" \
"s_bitcmp1_b32 s76, 20\n" \
"s_cbranch_scc0 .Lgs5_%=\n" \
"s_add_u32 s24, s95, 10485760\n" \
"buffer_load_dwordx4 v[20:23], v214, s[32:35], s24 offen sc1\n" \
"buffer_load_dwordx4 v[52:55], v214, s[32:35], s24 offen offset:64 sc1\n" \
".Lgs5_%=:\n" \
"s_bitcmp1_b32 s76, 24\n" \
"s_cbranch_scc0 .Lgs6_%=\n" \
"s_add_u32 s24, s95, 12582912\n" \
"buffer_load_dwordx4 v[24:27], v214, s[32:35], s24 offen sc1\n" \
"buffer_load_dwordx4 v[56:59], v214, s[32:35], s24 offen offset:64 sc1\n" \
".Lgs6_%=:\n" \
"s_bitcmp1_b32 s76, 28\n" \
"s_cbranch_scc0 .Lgs7_%=\n" \
"s_add_u32 s24, s95, 14680064\n" \
"buffer_load_dwordx4 v[28:31], v214, s[32:35], s24 offen sc1\n" \
"buffer_load_dwordx4 v[60:63], v214, s[32:35], s24 offen offset:64 sc1\n" \
".Lgs7_%=:\n" \
"s_cmp_eq_u32 s74, 0x11111111\n" \
"s_cbranch_scc1 .Lg0_%=\n" \
".Lgx_%=:\n" \
"s_not_b32 vcc_lo, vcc_lo\n" \
"s_mov_b32 vcc_hi, 0\n" \
"s_bcnt1_i32_b64 s27, vcc\n" \
"s_cmp_lt_u32 s27, 8\n" \
"s_cbranch_scc1 .Lnap1_%=\n" \
"s_sleep 10\n" \
".Lnap1_%=:\n" \
"s_sleep 2\n" \
"buffer_load_dword v245, v252, s[32:35], s18 offen sc1\n" \
"s_branch .Lg0_%=\n" \
".Lkr_%=:\n" \
".Lst3_2_%=:\n" \
"s_cbranch_execz .Lstb3_%=\n" \
"s_sub_u32 s82, s82, 1\n" \
"s_cbranch_scc1 .Lkb_%=\n" \
"s_cmp_lt_u32 s65, 28\n" \
"s_cbranch_scc1 .Lkn_%=\n" \
"v_max_u32_dpp v88, v88, v88 quad_perm:[1,0,3,2] row_mask:0xf bank_mask:0xf\n" \
"s_nop 1\n" \
"v_max_u32_dpp v88, v88, v88 quad_perm:[2,3,0,1] row_mask:0xf bank_mask:0xf\n" \
"s_nop 1\n" \
"v_max_u32_dpp v88, v88, v88 row_half_mirror row_mask:0xf bank_mask:0xf\n" \
"s_nop 1\n" \
"v_max_u32_dpp v88, v88, v88 row_mirror row_mask:0xf bank_mask:0xf\n" \
"s_nop 1\n" \
"v_max_u32_dpp v88, v88, v88 row_bcast:15 row_mask:0xa bank_mask:0xf\n" \
"s_nop 1\n" \
"v_max_u32_dpp v88, v88, v88 row_bcast:31 row_mask:0xc bank_mask:0xf\n" \
"s_nop 0\n" \
"v_readlane_b32 s25, v88, 63\n" \
"v_lshrrev_b32 v89, 4, v251\n" \
"v_and_b32 v90, 15, v251\n" \
"s_cmp_eq_u32 s25, s93\n" \
"s_cbranch_scc1 .Lkn_%=\n" \
"s_mov_b32 s93, s25\n" \
"s_bfe_u32 s25, s25, 0xb0005\n" \
"s_sub_u32 s25, 2047, s25\n" \
"s_mov_b32 s22, 0x18100800\n" \
"s_mov_b32 s23, 0x01091119\n" \
"s_bfe_u32 s24, s25, 0x30004\n" \
"s_lshl_b32 s24, s24, 3\n" \
"s_lshr_b64 s[22:23], s[22:23], s24\n" \
"s_and_b32 s22, s22, 0xff\n" \
"s_lshr_b32 s26, s25, 7\n" \
"s_lshl_b32 s26, s26, 5\n" \
"s_add_u32 s26, s26, s22\n" \
"s_lshl_b32 s26, s26, 6\n" \
"s_and_b32 s24, s25, 15\n" \
"s_add_u32 s26, s26, s24\n" \
"s_lshl_b32 s26, s26, 6\n" \
"s_add_u32 s26, s26, 59768832\n" \
"v_lshlrev_b32 v89, 21, v89\n" \
"v_and_b32 v91, 3, v251\n" \
"v_lshl_or_b32 v89, v91, 10, v89\n" \
"v_lshrrev_b32 v91, 2, v90\n" \
"v_lshl_or_b32 v89, v91, 13, v89\n" \
"v_lshlrev_b32 v90, 6, v90\n" \
"s_lshl_b32 s25, s25, 10\n" \
"s_add_u32 s24, s26, 8388608\n" \
"buffer_load_dword v91, v89, s[32:35], s26 offen sc1\n" \
"buffer_load_dword v91, v89, s[32:35], s24 offen sc1\n" \
"buffer_load_dword v91, v90, s[44:47], s25 offen\n" \
".Lkn_%=:\n" \
"s_bcnt1_i32_b64 s27, vcc\n" \
"s_cmp_lt_u32 s27, 6\n" \
"s_cbranch_scc1 .Lnap2_%=\n" \
"s_sleep 20\n" \
".Lnap2_%=:\n" \
"s_sleep 2\n" \
"buffer_load_dwordx2 v[86:87], v235, s[32:35], s38 offen sc1\n" \
"s_waitcnt vmcnt(0)\n" \
"s_branch .Lkp_%=\n" \
".Lfr_%=:\n" \
".Lst3_3_%=:\n" \
"s_cbranch_execz .Lstb3_%=\n" \
"s_sub_u32 s82, s82, 1\n" \
"s_cbranch_scc1 .Lfb_%=\n" \
"s_sleep 2\n" \
"s_branch .Lfp_%=\n" \
  \
".Lredo0_%=:\n" \
"s_nop 7\n" \
"v_and_b32 v228, 15, v251\n" \
"v_lshrrev_b32 v229, 4, v251\n" \
"v_lshlrev_b32 v229, 2, v229\n" \
"v_sub_u32 v228, v228, v229\n" \
"v_mov_b32 v231, 0xff800000\n" \
"s_mov_b32 s14, s67\n" \
"s_xor_b32 s15, %[wid], 0\n" \
"s_lshl3_add_u32 s15, s14, s15\n" \
"s_lshl_b32 s15, s15, 4\n" \
"v_add_u32 v226, s15, v228\n" \
"v_mov_b32 v224, 0xff7fffff\n" \
"s_xor_b32 s15, %[wid], 7\n" \
"s_lshl3_add_u32 s15, s14, s15\n" \
"s_lshl_b32 s15, s15, 4\n" \
"v_add_u32 v227, s15, v228\n" \
"v_mov_b32 v225, 0xff7fffff\n" \
"v_mov_b32 v0, 0\n" \
"v_mov_b32 v1, 0\n" \
"v_mov_b32 v2, 0\n" \
"v_mov_b32 v3, 0\n" \
"v_mov_b32 v4, 0\n" \
"v_mov_b32 v5, 0\n" \
"v_mov_b32 v6, 0\n" \
"v_mov_b32 v7, 0\n" \
"v_mov_b32 v8, 0\n" \
"v_mov_b32 v9, 0\n" \
"v_mov_b32 v10, 0\n" \
"v_mov_b32 v11, 0\n" \
"v_mov_b32 v12, 0\n" \
"v_mov_b32 v13, 0\n" \
"v_mov_b32 v14, 0\n" \
"v_mov_b32 v15, 0\n" \
"v_mov_b32 v16, 0\n" \
"v_mov_b32 v17, 0\n" \
"v_mov_b32 v18, 0\n" \
"v_mov_b32 v19, 0\n" \
"v_mov_b32 v20, 0\n" \
"v_mov_b32 v21, 0\n" \
"v_mov_b32 v22, 0\n" \
"v_mov_b32 v23, 0\n" \
"v_mov_b32 v24, 0\n" \
"v_mov_b32 v25, 0\n" \
"v_mov_b32 v26, 0\n" \
"v_mov_b32 v27, 0\n" \
"v_mov_b32 v28, 0\n" \
"v_mov_b32 v29, 0\n" \
"v_mov_b32 v30, 0\n" \
"v_mov_b32 v31, 0\n" \
"v_mov_b32 v32, 0\n" \
"v_mov_b32 v33, 0\n" \
"v_mov_b32 v34, 0\n" \
"v_mov_b32 v35, 0\n" \
"v_mov_b32 v36, 0\n" \
"v_mov_b32 v37, 0\n" \
"v_mov_b32 v38, 0\n" \
"v_mov_b32 v39, 0\n" \
"v_mov_b32 v40, 0\n" \
"v_mov_b32 v41, 0\n" \
"v_mov_b32 v42, 0\n" \
"v_mov_b32 v43, 0\n" \
"v_mov_b32 v44, 0\n" \
"v_mov_b32 v45, 0\n" \
"v_mov_b32 v46, 0\n" \
"v_mov_b32 v47, 0\n" \
"v_mov_b32 v48, 0\n" \
"v_mov_b32 v49, 0\n" \
"v_mov_b32 v50, 0\n" \
"v_mov_b32 v51, 0\n" \
"v_mov_b32 v52, 0\n" \
"v_mov_b32 v53, 0\n" \
"v_mov_b32 v54, 0\n" \
"v_mov_b32 v55, 0\n" \
"v_mov_b32 v56, 0\n" \
"v_mov_b32 v57, 0\n" \
"v_mov_b32 v58, 0\n" \
"v_mov_b32 v59, 0\n" \
"v_mov_b32 v60, 0\n" \
"v_mov_b32 v61, 0\n" \
"v_mov_b32 v62, 0\n" \
"v_mov_b32 v63, 0\n" \
"v_accvgpr_write_b32 a32, 0\n" \
"v_accvgpr_write_b32 a33, 0\n" \
"v_accvgpr_write_b32 a34, 0\n" \
"v_accvgpr_write_b32 a35, 0\n" \
"v_accvgpr_write_b32 a36, 0\n" \
"v_accvgpr_write_b32 a37, 0\n" \
"v_accvgpr_write_b32 a38, 0\n" \
"v_accvgpr_write_b32 a39, 0\n" \
"s_lshl1_add_u32 s14, s14, 1\n" \
"s_mov_b32 s15, s73\n" \
".Lslr0_%=:\n" \
"s_add_u32 s82, s15, 0\n" \
"buffer_load_dwordx4 a[64:67], v248, s[32:35], s82 offen offset:0 sc1\n" \
"buffer_load_dwordx4 a[68:71], v248, s[32:35], s82 offen offset:1024 sc1\n" \
"buffer_load_dwordx4 a[72:75], v248, s[32:35], s82 offen offset:2048 sc1\n" \
"buffer_load_dwordx4 a[76:79], v248, s[32:35], s82 offen offset:3072 sc1\n" \
"s_add_u32 s82, s15, 4096\n" \
"buffer_load_dwordx4 a[80:83], v248, s[32:35], s82 offen offset:0 sc1\n" \
"buffer_load_dwordx4 a[84:87], v248, s[32:35], s82 offen offset:1024 sc1\n" \
"buffer_load_dwordx4 a[88:91], v248, s[32:35], s82 offen offset:2048 sc1\n" \
"buffer_load_dwordx4 a[92:95], v248, s[32:35], s82 offen offset:3072 sc1\n" \
"s_add_u32 s82, s15, 8192\n" \
"buffer_load_dwordx4 a[96:99], v248, s[32:35], s82 offen offset:0 sc1\n" \
"buffer_load_dwordx4 a[100:103], v248, s[32:35], s82 offen offset:1024 sc1\n" \
"buffer_load_dwordx4 a[104:107], v248, s[32:35], s82 offen offset:2048 sc1\n" \
"buffer_load_dwordx4 a[108:111], v248, s[32:35], s82 offen offset:3072 sc1\n" \
"s_add_u32 s82, s15, 12288\n" \
"buffer_load_dwordx4 a[112:115], v248, s[32:35], s82 offen offset:0 sc1\n" \
"buffer_load_dwordx4 a[116:119], v248, s[32:35], s82 offen offset:1024 sc1\n" \
"buffer_load_dwordx4 a[120:123], v248, s[32:35], s82 offen offset:2048 sc1\n" \
"buffer_load_dwordx4 a[124:127], v248, s[32:35], s82 offen offset:3072 sc1\n" \
"s_waitcnt vmcnt(0)\n" \
"v_mfma_f32_16x16x32_bf16 v[128:131], a[64:67], v[192:195], 0\n" \
"v_mfma_f32_16x16x32_bf16 v[128:131], a[68:71], v[196:199], v[128:131]\n" \
"v_mfma_f32_16x16x32_bf16 v[128:131], a[72:75], v[200:203], v[128:131]\n" \
"v_mfma_f32_16x16x32_bf16 v[128:131], a[76:79], v[204:207], v[128:131]\n" \
"v_mfma_f32_16x16x32_bf16 v[132:135], a[92:95], v[204:207], 0\n" \
"v_mfma_f32_16x16x32_bf16 v[132:135], a[88:91], v[200:203], v[132:135]\n" \
"v_mfma_f32_16x16x32_bf16 v[132:135], a[84:87], v[196:199], v[132:135]\n" \
"v_mfma_f32_16x16x32_bf16 v[132:135], a[80:83], v[192:195], v[132:135]\n" \
"v_mfma_f32_16x16x32_bf16 v[136:139], a[96:99], v[192:195], 0\n" \
"v_mfma_f32_16x16x32_bf16 v[136:139], a[100:103], v[196:199], v[136:139]\n" \
"v_mfma_f32_16x16x32_bf16 v[136:139], a[104:107], v[200:203], v[136:139]\n" \
"v_mfma_f32_16x16x32_bf16 v[136:139], a[108:111], v[204:207], v[136:139]\n" \
"v_mfma_f32_16x16x32_bf16 v[140:143], a[124:127], v[204:207], 0\n" \
"v_mfma_f32_16x16x32_bf16 v[140:143], a[120:123], v[200:203], v[140:143]\n" \
"v_mfma_f32_16x16x32_bf16 v[140:143], a[116:119], v[196:199], v[140:143]\n" \
"v_mfma_f32_16x16x32_bf16 v[140:143], a[112:115], v[192:195], v[140:143]\n" \
"v_cmp_gt_i32 vcc, 0, v226\n" \
"s_nop 1\n" \
"v_cndmask_b32 v128, v128, v231, vcc\n" \
"v_cmp_gt_i32 vcc, 1, v226\n" \
"s_nop 1\n" \
"v_cndmask_b32 v129, v129, v231, vcc\n" \
"v_cmp_gt_i32 vcc, 2, v226\n" \
"s_nop 1\n" \
"v_cndmask_b32 v130, v130, v231, vcc\n" \
"v_cmp_gt_i32 vcc, 3, v226\n" \
"s_nop 1\n" \
"v_cndmask_b32 v131, v131, v231, vcc\n" \
"v_cmp_gt_i32 vcc, 16, v226\n" \
"s_nop 1\n" \
"v_cndmask_b32 v132, v132, v231, vcc\n" \
"v_cmp_gt_i32 vcc, 17, v226\n" \
"s_nop 1\n" \
"v_cndmask_b32 v133, v133, v231, vcc\n" \
"v_cmp_gt_i32 vcc, 18, v226\n" \
"s_nop 1\n" \
"v_cndmask_b32 v134, v134, v231, vcc\n" \
"v_cmp_gt_i32 vcc, 19, v226\n" \
"s_nop 1\n" \
"v_cndmask_b32 v135, v135, v231, vcc\n" \
"v_cmp_gt_i32 vcc, 32, v226\n" \
"s_nop 1\n" \
"v_cndmask_b32 v136, v136, v231, vcc\n" \
"v_cmp_gt_i32 vcc, 33, v226\n" \
"s_nop 1\n" \
"v_cndmask_b32 v137, v137, v231, vcc\n" \
"v_cmp_gt_i32 vcc, 34, v226\n" \
"s_nop 1\n" \
"v_cndmask_b32 v138, v138, v231, vcc\n" \
"v_cmp_gt_i32 vcc, 35, v226\n" \
"s_nop 1\n" \
"v_cndmask_b32 v139, v139, v231, vcc\n" \
"v_cmp_gt_i32 vcc, 48, v226\n" \
"s_nop 1\n" \
"v_cndmask_b32 v140, v140, v231, vcc\n" \
"v_cmp_gt_i32 vcc, 49, v226\n" \
"s_nop 1\n" \
"v_cndmask_b32 v141, v141, v231, vcc\n" \
"v_cmp_gt_i32 vcc, 50, v226\n" \
"s_nop 1\n" \
"v_cndmask_b32 v142, v142, v231, vcc\n" \
"v_cmp_gt_i32 vcc, 51, v226\n" \
"s_nop 1\n" \
"v_cndmask_b32 v143, v143, v231, vcc\n" \
"v_max3_f32 v228, v224, v128, v129\n" \
"v_max3_f32 v228, v228, v130, v131\n" \
"v_max3_f32 v228, v228, v132, v133\n" \
"v_max3_f32 v228, v228, v134, v135\n" \
"v_max3_f32 v228, v228, v136, v137\n" \
"v_max3_f32 v228, v228, v138, v139\n" \
"v_max3_f32 v228, v228, v140, v141\n" \
"v_max3_f32 v228, v228, v142, v143\n" \
"v_mov_b32 v229, v228\n" \
"s_nop 1\n" \
"v_permlane32_swap_b32 v228, v229\n" \
"v_max_f32 v228, v228, v229\n" \
"v_mov_b32 v229, v228\n" \
"s_nop 1\n" \
"v_permlane16_swap_b32 v228, v229\n" \
"v_max_f32 v228, v228, v229\n" \
"v_sub_f32 v230, v224, v228\n" \
"v_mov_b32 v224, v228\n" \
"v_exp_f32 v230, v230\n" \
"v_subrev_u32 v226, 64, v226\n" \
"s_add_u32 s82, s15, 589824\n" \
"buffer_load_dwordx4 a[64:67], v248, s[32:35], s82 offen offset:0 sc1\n" \
"buffer_load_dwordx4 a[68:71], v248, s[32:35], s82 offen offset:1024 sc1\n" \
"buffer_load_dwordx4 a[72:75], v248, s[32:35], s82 offen offset:2048 sc1\n" \
"buffer_load_dwordx4 a[76:79], v248, s[32:35], s82 offen offset:3072 sc1\n" \
"s_add_u32 s82, s15, 593920\n" \
"buffer_load_dwordx4 a[80:83], v248, s[32:35], s82 offen offset:0 sc1\n" \
"buffer_load_dwordx4 a[84:87], v248, s[32:35], s82 offen offset:1024 sc1\n" \
"buffer_load_dwordx4 a[88:91], v248, s[32:35], s82 offen offset:2048 sc1\n" \
"buffer_load_dwordx4 a[92:95], v248, s[32:35], s82 offen offset:3072 sc1\n" \
"s_add_u32 s82, s15, 598016\n" \
"buffer_load_dwordx4 a[96:99], v248, s[32:35], s82 offen offset:0 sc1\n" \
"buffer_load_dwordx4 a[100:103], v248, s[32:35], s82 offen offset:1024 sc1\n" \
"buffer_load_dwordx4 a[104:107], v248, s[32:35], s82 offen offset:2048 sc1\n" \
"buffer_load_dwordx4 a[108:111], v248, s[32:35], s82 offen offset:3072 sc1\n" \
"s_add_u32 s82, s15, 602112\n" \
"buffer_load_dwordx4 a[112:115], v248, s[32:35], s82 offen offset:0 sc1\n" \
"buffer_load_dwordx4 a[116:119], v248, s[32:35], s82 offen offset:1024 sc1\n" \
"buffer_load_dwordx4 a[120:123], v248, s[32:35], s82 offen offset:2048 sc1\n" \
"buffer_load_dwordx4 a[124:127], v248, s[32:35], s82 offen offset:3072 sc1\n" \
"s_waitcnt vmcnt(0)\n" \
"v_sub_f32 v128, v128, v228\n" \
"v_sub_f32 v129, v129, v228\n" \
"v_sub_f32 v130, v130, v228\n" \
"v_sub_f32 v131, v131, v228\n" \
"v_sub_f32 v132, v132, v228\n" \
"v_sub_f32 v133, v133, v228\n" \
"v_sub_f32 v134, v134, v228\n" \
"v_sub_f32 v135, v135, v228\n" \
"v_sub_f32 v136, v136, v228\n" \
"v_sub_f32 v137, v137, v228\n" \
"v_sub_f32 v138, v138, v228\n" \
"v_sub_f32 v139, v139, v228\n" \
"v_sub_f32 v140, v140, v228\n" \
"v_sub_f32 v141, v141, v228\n" \
"v_sub_f32 v142, v142, v228\n" \
"v_sub_f32 v143, v143, v228\n" \
"v_exp_f32 v128, v128\n" \
"v_exp_f32 v129, v129\n" \
"v_exp_f32 v130, v130\n" \
"v_exp_f32 v131, v131\n" \
"v_exp_f32 v132, v132\n" \
"v_exp_f32 v133, v133\n" \
"v_exp_f32 v134, v134\n" \
"v_exp_f32 v135, v135\n" \
"v_exp_f32 v136, v136\n" \
"v_exp_f32 v137, v137\n" \
"v_exp_f32 v138, v138\n" \
"v_exp_f32 v139, v139\n" \
"v_exp_f32 v140, v140\n" \
"v_exp_f32 v141, v141\n" \
"v_exp_f32 v142, v142\n" \
"v_exp_f32 v143, v143\n" \
"v_cvt_pk_bf16_f32 v128, v128, v129\n" \
"v_cvt_pk_bf16_f32 v129, v130, v131\n" \
"v_cvt_pk_bf16_f32 v130, v132, v133\n" \
"v_cvt_pk_bf16_f32 v131, v134, v135\n" \
"v_cvt_pk_bf16_f32 v132, v136, v137\n" \
"v_cvt_pk_bf16_f32 v133, v138, v139\n" \
"v_cvt_pk_bf16_f32 v134, v140, v141\n" \
"v_cvt_pk_bf16_f32 v135, v142, v143\n" \
"v_mul_f32 v0, v230, v0\n" \
"v_mul_f32 v1, v230, v1\n" \
"v_mul_f32 v2, v230, v2\n" \
"v_mul_f32 v3, v230, v3\n" \
"v_mul_f32 v4, v230, v4\n" \
"v_mul_f32 v5, v230, v5\n" \
"v_mul_f32 v6, v230, v6\n" \
"v_mul_f32 v7, v230, v7\n" \
"v_mul_f32 v8, v230, v8\n" \
"v_mul_f32 v9, v230, v9\n" \
"v_mul_f32 v10, v230, v10\n" \
"v_mul_f32 v11, v230, v11\n" \
"v_mul_f32 v12, v230, v12\n" \
"v_mul_f32 v13, v230, v13\n" \
"v_mul_f32 v14, v230, v14\n" \
"v_mul_f32 v15, v230, v15\n" \
"v_mul_f32 v16, v230, v16\n" \
"v_mul_f32 v17, v230, v17\n" \
"v_mul_f32 v18, v230, v18\n" \
"v_mul_f32 v19, v230, v19\n" \
"v_mul_f32 v20, v230, v20\n" \
"v_mul_f32 v21, v230, v21\n" \
"v_mul_f32 v22, v230, v22\n" \
"v_mul_f32 v23, v230, v23\n" \
"v_mul_f32 v24, v230, v24\n" \
"v_mul_f32 v25, v230, v25\n" \
"v_mul_f32 v26, v230, v26\n" \
"v_mul_f32 v27, v230, v27\n" \
"v_mul_f32 v28, v230, v28\n" \
"v_mul_f32 v29, v230, v29\n" \
"v_mul_f32 v30, v230, v30\n" \
"v_mul_f32 v31, v230, v31\n" \
"v_accvgpr_read_b32 v229, a32\n" \
"v_mul_f32 v229, v230, v229\n" \
"v_accvgpr_write_b32 a32, v229\n" \
"v_accvgpr_read_b32 v229, a33\n" \
"v_mul_f32 v229, v230, v229\n" \
"v_accvgpr_write_b32 a33, v229\n" \
"v_accvgpr_read_b32 v229, a34\n" \
"v_mul_f32 v229, v230, v229\n" \
"v_accvgpr_write_b32 a34, v229\n" \
"v_accvgpr_read_b32 v229, a35\n" \
"v_mul_f32 v229, v230, v229\n" \
"v_accvgpr_write_b32 a35, v229\n" \
"s_nop 1\n" \
"v_smfmac_f32_16x16x64_bf16 a[32:35], a[48:51], v[128:135], v241\n" \
"v_mfma_f32_16x16x32_bf16 v[0:3], a[96:99], v[132:135], v[0:3]\n" \
"v_mfma_f32_16x16x32_bf16 v[0:3], a[64:67], v[128:131], v[0:3]\n" \
"v_mfma_f32_16x16x32_bf16 v[4:7], a[68:71], v[128:131], v[4:7]\n" \
"v_mfma_f32_16x16x32_bf16 v[4:7], a[100:103], v[132:135], v[4:7]\n" \
"v_mfma_f32_16x16x32_bf16 v[8:11], a[104:107], v[132:135], v[8:11]\n" \
"v_mfma_f32_16x16x32_bf16 v[8:11], a[72:75], v[128:131], v[8:11]\n" \
"v_mfma_f32_16x16x32_bf16 v[12:15], a[76:79], v[128:131], v[12:15]\n" \
"v_mfma_f32_16x16x32_bf16 v[12:15], a[108:111], v[132:135], v[12:15]\n" \
"v_mfma_f32_16x16x32_bf16 v[16:19], a[112:115], v[132:135], v[16:19]\n" \
"v_mfma_f32_16x16x32_bf16 v[16:19], a[80:83], v[128:131], v[16:19]\n" \
"v_mfma_f32_16x16x32_bf16 v[20:23], a[84:87], v[128:131], v[20:23]\n" \
"v_mfma_f32_16x16x32_bf16 v[20:23], a[116:119], v[132:135], v[20:23]\n" \
"v_mfma_f32_16x16x32_bf16 v[24:27], a[120:123], v[132:135], v[24:27]\n" \
"v_mfma_f32_16x16x32_bf16 v[24:27], a[88:91], v[128:131], v[24:27]\n" \
"v_mfma_f32_16x16x32_bf16 v[28:31], a[92:95], v[128:131], v[28:31]\n" \
"v_mfma_f32_16x16x32_bf16 v[28:31], a[124:127], v[132:135], v[28:31]\n" \
"s_nop 7\n" \
"s_add_u32 s82, s15, 0\n" \
"buffer_load_dwordx4 a[64:67], v248, s[32:35], s82 offen offset:0 sc1\n" \
"buffer_load_dwordx4 a[68:71], v248, s[32:35], s82 offen offset:1024 sc1\n" \
"buffer_load_dwordx4 a[72:75], v248, s[32:35], s82 offen offset:2048 sc1\n" \
"buffer_load_dwordx4 a[76:79], v248, s[32:35], s82 offen offset:3072 sc1\n" \
"s_add_u32 s82, s15, 4096\n" \
"buffer_load_dwordx4 a[80:83], v248, s[32:35], s82 offen offset:0 sc1\n" \
"buffer_load_dwordx4 a[84:87], v248, s[32:35], s82 offen offset:1024 sc1\n" \
"buffer_load_dwordx4 a[88:91], v248, s[32:35], s82 offen offset:2048 sc1\n" \
"buffer_load_dwordx4 a[92:95], v248, s[32:35], s82 offen offset:3072 sc1\n" \
"s_add_u32 s82, s15, 8192\n" \
"buffer_load_dwordx4 a[96:99], v248, s[32:35], s82 offen offset:0 sc1\n" \
"buffer_load_dwordx4 a[100:103], v248, s[32:35], s82 offen offset:1024 sc1\n" \
"buffer_load_dwordx4 a[104:107], v248, s[32:35], s82 offen offset:2048 sc1\n" \
"buffer_load_dwordx4 a[108:111], v248, s[32:35], s82 offen offset:3072 sc1\n" \
"s_add_u32 s82, s15, 12288\n" \
"buffer_load_dwordx4 a[112:115], v248, s[32:35], s82 offen offset:0 sc1\n" \
"buffer_load_dwordx4 a[116:119], v248, s[32:35], s82 offen offset:1024 sc1\n" \
"buffer_load_dwordx4 a[120:123], v248, s[32:35], s82 offen offset:2048 sc1\n" \
"buffer_load_dwordx4 a[124:127], v248, s[32:35], s82 offen offset:3072 sc1\n" \
"s_waitcnt vmcnt(0)\n" \
"v_mfma_f32_16x16x32_bf16 v[128:131], a[64:67], v[208:211], 0\n" \
"v_mfma_f32_16x16x32_bf16 v[128:131], a[68:71], v[212:215], v[128:131]\n" \
"v_mfma_f32_16x16x32_bf16 v[128:131], a[72:75], v[216:219], v[128:131]\n" \
"v_mfma_f32_16x16x32_bf16 v[128:131], a[76:79], v[220:223], v[128:131]\n" \
"v_mfma_f32_16x16x32_bf16 v[132:135], a[92:95], v[220:223], 0\n" \
"v_mfma_f32_16x16x32_bf16 v[132:135], a[88:91], v[216:219], v[132:135]\n" \
"v_mfma_f32_16x16x32_bf16 v[132:135], a[84:87], v[212:215], v[132:135]\n" \
"v_mfma_f32_16x16x32_bf16 v[132:135], a[80:83], v[208:211], v[132:135]\n" \
"v_mfma_f32_16x16x32_bf16 v[136:139], a[96:99], v[208:211], 0\n" \
"v_mfma_f32_16x16x32_bf16 v[136:139], a[100:103], v[212:215], v[136:139]\n" \
"v_mfma_f32_16x16x32_bf16 v[136:139], a[104:107], v[216:219], v[136:139]\n" \
"v_mfma_f32_16x16x32_bf16 v[136:139], a[108:111], v[220:223], v[136:139]\n" \
"v_mfma_f32_16x16x32_bf16 v[140:143], a[124:127], v[220:223], 0\n" \
"v_mfma_f32_16x16x32_bf16 v[140:143], a[120:123], v[216:219], v[140:143]\n" \
"v_mfma_f32_16x16x32_bf16 v[140:143], a[116:119], v[212:215], v[140:143]\n" \
"v_mfma_f32_16x16x32_bf16 v[140:143], a[112:115], v[208:211], v[140:143]\n" \
"v_cmp_gt_i32 vcc, 0, v227\n" \
"s_nop 1\n" \
"v_cndmask_b32 v128, v128, v231, vcc\n" \
"v_cmp_gt_i32 vcc, 1, v227\n" \
"s_nop 1\n" \
"v_cndmask_b32 v129, v129, v231, vcc\n" \
"v_cmp_gt_i32 vcc, 2, v227\n" \
"s_nop 1\n" \
"v_cndmask_b32 v130, v130, v231, vcc\n" \
"v_cmp_gt_i32 vcc, 3, v227\n" \
"s_nop 1\n" \
"v_cndmask_b32 v131, v131, v231, vcc\n" \
"v_cmp_gt_i32 vcc, 16, v227\n" \
"s_nop 1\n" \
"v_cndmask_b32 v132, v132, v231, vcc\n" \
"v_cmp_gt_i32 vcc, 17, v227\n" \
"s_nop 1\n" \
"v_cndmask_b32 v133, v133, v231, vcc\n" \
"v_cmp_gt_i32 vcc, 18, v227\n" \
"s_nop 1\n" \
"v_cndmask_b32 v134, v134, v231, vcc\n" \
"v_cmp_gt_i32 vcc, 19, v227\n" \
"s_nop 1\n" \
"v_cndmask_b32 v135, v135, v231, vcc\n" \
"v_cmp_gt_i32 vcc, 32, v227\n" \
"s_nop 1\n" \
"v_cndmask_b32 v136, v136, v231, vcc\n" \
"v_cmp_gt_i32 vcc, 33, v227\n" \
"s_nop 1\n" \
"v_cndmask_b32 v137, v137, v231, vcc\n" \
"v_cmp_gt_i32 vcc, 34, v227\n" \
"s_nop 1\n" \
"v_cndmask_b32 v138, v138, v231, vcc\n" \
"v_cmp_gt_i32 vcc, 35, v227\n" \
"s_nop 1\n" \
"v_cndmask_b32 v139, v139, v231, vcc\n" \
"v_cmp_gt_i32 vcc, 48, v227\n" \
"s_nop 1\n" \
"v_cndmask_b32 v140, v140, v231, vcc\n" \
"v_cmp_gt_i32 vcc, 49, v227\n" \
"s_nop 1\n" \
"v_cndmask_b32 v141, v141, v231, vcc\n" \
"v_cmp_gt_i32 vcc, 50, v227\n" \
"s_nop 1\n" \
"v_cndmask_b32 v142, v142, v231, vcc\n" \
"v_cmp_gt_i32 vcc, 51, v227\n" \
"s_nop 1\n" \
"v_cndmask_b32 v143, v143, v231, vcc\n" \
"v_max3_f32 v228, v225, v128, v129\n" \
"v_max3_f32 v228, v228, v130, v131\n" \
"v_max3_f32 v228, v228, v132, v133\n" \
"v_max3_f32 v228, v228, v134, v135\n" \
"v_max3_f32 v228, v228, v136, v137\n" \
"v_max3_f32 v228, v228, v138, v139\n" \
"v_max3_f32 v228, v228, v140, v141\n" \
"v_max3_f32 v228, v228, v142, v143\n" \
"v_mov_b32 v229, v228\n" \
"s_nop 1\n" \
"v_permlane32_swap_b32 v228, v229\n" \
"v_max_f32 v228, v228, v229\n" \
"v_mov_b32 v229, v228\n" \
"s_nop 1\n" \
"v_permlane16_swap_b32 v228, v229\n" \
"v_max_f32 v228, v228, v229\n" \
"v_sub_f32 v230, v225, v228\n" \
"v_mov_b32 v225, v228\n" \
"v_exp_f32 v230, v230\n" \
"v_subrev_u32 v227, 64, v227\n" \
"s_add_u32 s82, s15, 589824\n" \
"buffer_load_dwordx4 a[64:67], v248, s[32:35], s82 offen offset:0 sc1\n" \
"buffer_load_dwordx4 a[68:71], v248, s[32:35], s82 offen offset:1024 sc1\n" \
"buffer_load_dwordx4 a[72:75], v248, s[32:35], s82 offen offset:2048 sc1\n" \
"buffer_load_dwordx4 a[76:79], v248, s[32:35], s82 offen offset:3072 sc1\n" \
"s_add_u32 s82, s15, 593920\n" \
"buffer_load_dwordx4 a[80:83], v248, s[32:35], s82 offen offset:0 sc1\n" \
"buffer_load_dwordx4 a[84:87], v248, s[32:35], s82 offen offset:1024 sc1\n" \
"buffer_load_dwordx4 a[88:91], v248, s[32:35], s82 offen offset:2048 sc1\n" \
"buffer_load_dwordx4 a[92:95], v248, s[32:35], s82 offen offset:3072 sc1\n" \
"s_add_u32 s82, s15, 598016\n" \
"buffer_load_dwordx4 a[96:99], v248, s[32:35], s82 offen offset:0 sc1\n" \
"buffer_load_dwordx4 a[100:103], v248, s[32:35], s82 offen offset:1024 sc1\n" \
"buffer_load_dwordx4 a[104:107], v248, s[32:35], s82 offen offset:2048 sc1\n" \
"buffer_load_dwordx4 a[108:111], v248, s[32:35], s82 offen offset:3072 sc1\n" \
"s_add_u32 s82, s15, 602112\n" \
"buffer_load_dwordx4 a[112:115], v248, s[32:35], s82 offen offset:0 sc1\n" \
"buffer_load_dwordx4 a[116:119], v248, s[32:35], s82 offen offset:1024 sc1\n" \
"buffer_load_dwordx4 a[120:123], v248, s[32:35], s82 offen offset:2048 sc1\n" \
"buffer_load_dwordx4 a[124:127], v248, s[32:35], s82 offen offset:3072 sc1\n" \
"s_waitcnt vmcnt(0)\n" \
"v_sub_f32 v128, v128, v228\n" \
"v_sub_f32 v129, v129, v228\n" \
"v_sub_f32 v130, v130, v228\n" \
"v_sub_f32 v131, v131, v228\n" \
"v_sub_f32 v132, v132, v228\n" \
"v_sub_f32 v133, v133, v228\n" \
"v_sub_f32 v134, v134, v228\n" \
"v_sub_f32 v135, v135, v228\n" \
"v_sub_f32 v136, v136, v228\n" \
"v_sub_f32 v137, v137, v228\n" \
"v_sub_f32 v138, v138, v228\n" \
"v_sub_f32 v139, v139, v228\n" \
"v_sub_f32 v140, v140, v228\n" \
"v_sub_f32 v141, v141, v228\n" \
"v_sub_f32 v142, v142, v228\n" \
"v_sub_f32 v143, v143, v228\n" \
"v_exp_f32 v128, v128\n" \
"v_exp_f32 v129, v129\n" \
"v_exp_f32 v130, v130\n" \
"v_exp_f32 v131, v131\n" \
"v_exp_f32 v132, v132\n" \
"v_exp_f32 v133, v133\n" \
"v_exp_f32 v134, v134\n" \
"v_exp_f32 v135, v135\n" \
"v_exp_f32 v136, v136\n" \
"v_exp_f32 v137, v137\n" \
"v_exp_f32 v138, v138\n" \
"v_exp_f32 v139, v139\n" \
"v_exp_f32 v140, v140\n" \
"v_exp_f32 v141, v141\n" \
"v_exp_f32 v142, v142\n" \
"v_exp_f32 v143, v143\n" \
"v_cvt_pk_bf16_f32 v128, v128, v129\n" \
"v_cvt_pk_bf16_f32 v129, v130, v131\n" \
"v_cvt_pk_bf16_f32 v130, v132, v133\n" \
"v_cvt_pk_bf16_f32 v131, v134, v135\n" \
"v_cvt_pk_bf16_f32 v132, v136, v137\n" \
"v_cvt_pk_bf16_f32 v133, v138, v139\n" \
"v_cvt_pk_bf16_f32 v134, v140, v141\n" \
"v_cvt_pk_bf16_f32 v135, v142, v143\n" \
"v_mul_f32 v32, v230, v32\n" \
"v_mul_f32 v33, v230, v33\n" \
"v_mul_f32 v34, v230, v34\n" \
"v_mul_f32 v35, v230, v35\n" \
"v_mul_f32 v36, v230, v36\n" \
"v_mul_f32 v37, v230, v37\n" \
"v_mul_f32 v38, v230, v38\n" \
"v_mul_f32 v39, v230, v39\n" \
"v_mul_f32 v40, v230, v40\n" \
"v_mul_f32 v41, v230, v41\n" \
"v_mul_f32 v42, v230, v42\n" \
"v_mul_f32 v43, v230, v43\n" \
"v_mul_f32 v44, v230, v44\n" \
"v_mul_f32 v45, v230, v45\n" \
"v_mul_f32 v46, v230, v46\n" \
"v_mul_f32 v47, v230, v47\n" \
"v_mul_f32 v48, v230, v48\n" \
"v_mul_f32 v49, v230, v49\n" \
"v_mul_f32 v50, v230, v50\n" \
"v_mul_f32 v51, v230, v51\n" \
"v_mul_f32 v52, v230, v52\n" \
"v_mul_f32 v53, v230, v53\n" \
"v_mul_f32 v54, v230, v54\n" \
"v_mul_f32 v55, v230, v55\n" \
"v_mul_f32 v56, v230, v56\n" \
"v_mul_f32 v57, v230, v57\n" \
"v_mul_f32 v58, v230, v58\n" \
"v_mul_f32 v59, v230, v59\n" \
"v_mul_f32 v60, v230, v60\n" \
"v_mul_f32 v61, v230, v61\n" \
"v_mul_f32 v62, v230, v62\n" \
"v_mul_f32 v63, v230, v63\n" \
"v_accvgpr_read_b32 v229, a36\n" \
"v_mul_f32 v229, v230, v229\n" \
"v_accvgpr_write_b32 a36, v229\n" \
"v_accvgpr_read_b32 v229, a37\n" \
"v_mul_f32 v229, v230, v229\n" \
"v_accvgpr_write_b32 a37, v229\n" \
"v_accvgpr_read_b32 v229, a38\n" \
"v_mul_f32 v229, v230, v229\n" \
"v_accvgpr_write_b32 a38, v229\n" \
"v_accvgpr_read_b32 v229, a39\n" \
"v_mul_f32 v229, v230, v229\n" \
"v_accvgpr_write_b32 a39, v229\n" \
"s_nop 1\n" \
"v_smfmac_f32_16x16x64_bf16 a[36:39], a[48:51], v[128:135], v241\n" \
"v_mfma_f32_16x16x32_bf16 v[32:35], a[96:99], v[132:135], v[32:35]\n" \
"v_mfma_f32_16x16x32_bf16 v[32:35], a[64:67], v[128:131], v[32:35]\n" \
"v_mfma_f32_16x16x32_bf16 v[36:39], a[68:71], v[128:131], v[36:39]\n" \
"v_mfma_f32_16x16x32_bf16 v[36:39], a[100:103], v[132:135], v[36:39]\n" \
"v_mfma_f32_16x16x32_bf16 v[40:43], a[104:107], v[132:135], v[40:43]\n" \
"v_mfma_f32_16x16x32_bf16 v[40:43], a[72:75], v[128:131], v[40:43]\n" \
"v_mfma_f32_16x16x32_bf16 v[44:47], a[76:79], v[128:131], v[44:47]\n" \
"v_mfma_f32_16x16x32_bf16 v[44:47], a[108:111], v[132:135], v[44:47]\n" \
"v_mfma_f32_16x16x32_bf16 v[48:51], a[112:115], v[132:135], v[48:51]\n" \
"v_mfma_f32_16x16x32_bf16 v[48:51], a[80:83], v[128:131], v[48:51]\n" \
"v_mfma_f32_16x16x32_bf16 v[52:55], a[84:87], v[128:131], v[52:55]\n" \
"v_mfma_f32_16x16x32_bf16 v[52:55], a[116:119], v[132:135], v[52:55]\n" \
"v_mfma_f32_16x16x32_bf16 v[56:59], a[120:123], v[132:135], v[56:59]\n" \
"v_mfma_f32_16x16x32_bf16 v[56:59], a[88:91], v[128:131], v[56:59]\n" \
"v_mfma_f32_16x16x32_bf16 v[60:63], a[92:95], v[128:131], v[60:63]\n" \
"v_mfma_f32_16x16x32_bf16 v[60:63], a[124:127], v[132:135], v[60:63]\n" \
"s_nop 7\n" \
"s_add_u32 s15, s15, 0x4000\n" \
"s_sub_u32 s14, s14, 1\n" \
"s_cbranch_scc0 .Lslr0_%=\n" \
"s_nop 7\n" \
"s_setpc_b64 s[98:99]\n" \
  \
".Lredo2_%=:\n" \
"s_nop 7\n" \
"v_and_b32 v196, 31, v251\n" \
"v_lshlrev_b32 v196, 2, v196\n" \
"s_branch .Lspr2_%=\n" \
".Lsbr2_%=:\n" \
"v_and_b32 v196, 15, v251\n" \
"v_lshrrev_b32 v197, 4, v251\n" \
"v_lshlrev_b32 v197, 2, v197\n" \
"v_sub_u32 v196, v196, v197\n" \
"v_mov_b32 v199, 0xff800000\n" \
"s_sub_u32 s14, 15, s67\n" \
"s_xor_b32 s15, %[wid], 0\n" \
"s_lshl3_add_u32 s15, s14, s15\n" \
"s_lshl_b32 s15, s15, 4\n" \
"v_add_u32 v194, s15, v196\n" \
"v_mov_b32 v192, 0xff7fffff\n" \
"s_xor_b32 s15, %[wid], 7\n" \
"s_lshl3_add_u32 s15, s14, s15\n" \
"s_lshl_b32 s15, s15, 4\n" \
"v_add_u32 v195, s15, v196\n" \
"v_mov_b32 v193, 0xff7fffff\n" \
"v_mov_b32 v64, 0\n" \
"v_mov_b32 v65, 0\n" \
"v_mov_b32 v66, 0\n" \
"v_mov_b32 v67, 0\n" \
"v_mov_b32 v68, 0\n" \
"v_mov_b32 v69, 0\n" \
"v_mov_b32 v70, 0\n" \
"v_mov_b32 v71, 0\n" \
"v_mov_b32 v72, 0\n" \
"v_mov_b32 v73, 0\n" \
"v_mov_b32 v74, 0\n" \
"v_mov_b32 v75, 0\n" \
"v_mov_b32 v76, 0\n" \
"v_mov_b32 v77, 0\n" \
"v_mov_b32 v78, 0\n" \
"v_mov_b32 v79, 0\n" \
"v_mov_b32 v80, 0\n" \
"v_mov_b32 v81, 0\n" \
"v_mov_b32 v82, 0\n" \
"v_mov_b32 v83, 0\n" \
"v_mov_b32 v84, 0\n" \
"v_mov_b32 v85, 0\n" \
"v_mov_b32 v86, 0\n" \
"v_mov_b32 v87, 0\n" \
"v_mov_b32 v88, 0\n" \
"v_mov_b32 v89, 0\n" \
"v_mov_b32 v90, 0\n" \
"v_mov_b32 v91, 0\n" \
"v_mov_b32 v92, 0\n" \
"v_mov_b32 v93, 0\n" \
"v_mov_b32 v94, 0\n" \
"v_mov_b32 v95, 0\n" \
"v_mov_b32 v96, 0\n" \
"v_mov_b32 v97, 0\n" \
"v_mov_b32 v98, 0\n" \
"v_mov_b32 v99, 0\n" \
"v_mov_b32 v100, 0\n" \
"v_mov_b32 v101, 0\n" \
"v_mov_b32 v102, 0\n" \
"v_mov_b32 v103, 0\n" \
"v_mov_b32 v104, 0\n" \
"v_mov_b32 v105, 0\n" \
"v_mov_b32 v106, 0\n" \
"v_mov_b32 v107, 0\n" \
"v_mov_b32 v108, 0\n" \
"v_mov_b32 v109, 0\n" \
"v_mov_b32 v110, 0\n" \
"v_mov_b32 v111, 0\n" \
"v_mov_b32 v112, 0\n" \
"v_mov_b32 v113, 0\n" \
"v_mov_b32 v114, 0\n" \
"v_mov_b32 v115, 0\n" \
"v_mov_b32 v116, 0\n" \
"v_mov_b32 v117, 0\n" \
"v_mov_b32 v118, 0\n" \
"v_mov_b32 v119, 0\n" \
"v_mov_b32 v120, 0\n" \
"v_mov_b32 v121, 0\n" \
"v_mov_b32 v122, 0\n" \
"v_mov_b32 v123, 0\n" \
"v_mov_b32 v124, 0\n" \
"v_mov_b32 v125, 0\n" \
"v_mov_b32 v126, 0\n" \
"v_mov_b32 v127, 0\n" \
"v_accvgpr_write_b32 a40, 0\n" \
"v_accvgpr_write_b32 a41, 0\n" \
"v_accvgpr_write_b32 a42, 0\n" \
"v_accvgpr_write_b32 a43, 0\n" \
"v_accvgpr_write_b32 a44, 0\n" \
"v_accvgpr_write_b32 a45, 0\n" \
"v_accvgpr_write_b32 a46, 0\n" \
"v_accvgpr_write_b32 a47, 0\n" \
"s_lshl1_add_u32 s14, s14, 1\n" \
"s_mov_b32 s15, s73\n" \
".Lslr2_%=:\n" \
"s_add_u32 s82, s15, 0\n" \
"buffer_load_dwordx4 a[128:131], v248, s[32:35], s82 offen offset:0 sc1\n" \
"buffer_load_dwordx4 a[132:135], v248, s[32:35], s82 offen offset:1024 sc1\n" \
"buffer_load_dwordx4 a[136:139], v248, s[32:35], s82 offen offset:2048 sc1\n" \
"buffer_load_dwordx4 a[140:143], v248, s[32:35], s82 offen offset:3072 sc1\n" \
"s_add_u32 s82, s15, 4096\n" \
"buffer_load_dwordx4 a[144:147], v248, s[32:35], s82 offen offset:0 sc1\n" \
"buffer_load_dwordx4 a[148:151], v248, s[32:35], s82 offen offset:1024 sc1\n" \
"buffer_load_dwordx4 a[152:155], v248, s[32:35], s82 offen offset:2048 sc1\n" \
"buffer_load_dwordx4 a[156:159], v248, s[32:35], s82 offen offset:3072 sc1\n" \
"s_add_u32 s82, s15, 8192\n" \
"buffer_load_dwordx4 a[160:163], v248, s[32:35], s82 offen offset:0 sc1\n" \
"buffer_load_dwordx4 a[164:167], v248, s[32:35], s82 offen offset:1024 sc1\n" \
"buffer_load_dwordx4 a[168:171], v248, s[32:35], s82 offen offset:2048 sc1\n" \
"buffer_load_dwordx4 a[172:175], v248, s[32:35], s82 offen offset:3072 sc1\n" \
"s_add_u32 s82, s15, 12288\n" \
"buffer_load_dwordx4 a[176:179], v248, s[32:35], s82 offen offset:0 sc1\n" \
"buffer_load_dwordx4 a[180:183], v248, s[32:35], s82 offen offset:1024 sc1\n" \
"buffer_load_dwordx4 a[184:187], v248, s[32:35], s82 offen offset:2048 sc1\n" \
"buffer_load_dwordx4 a[188:191], v248, s[32:35], s82 offen offset:3072 sc1\n" \
"s_waitcnt vmcnt(0)\n" \
"v_mfma_f32_16x16x32_bf16 v[128:131], a[128:131], v[160:163], 0\n" \
"v_mfma_f32_16x16x32_bf16 v[128:131], a[132:135], v[164:167], v[128:131]\n" \
"v_mfma_f32_16x16x32_bf16 v[128:131], a[136:139], v[168:171], v[128:131]\n" \
"v_mfma_f32_16x16x32_bf16 v[128:131], a[140:143], v[172:175], v[128:131]\n" \
"v_mfma_f32_16x16x32_bf16 v[132:135], a[156:159], v[172:175], 0\n" \
"v_mfma_f32_16x16x32_bf16 v[132:135], a[152:155], v[168:171], v[132:135]\n" \
"v_mfma_f32_16x16x32_bf16 v[132:135], a[148:151], v[164:167], v[132:135]\n" \
"v_mfma_f32_16x16x32_bf16 v[132:135], a[144:147], v[160:163], v[132:135]\n" \
"v_mfma_f32_16x16x32_bf16 v[136:139], a[160:163], v[160:163], 0\n" \
"v_mfma_f32_16x16x32_bf16 v[136:139], a[164:167], v[164:167], v[136:139]\n" \
"v_mfma_f32_16x16x32_bf16 v[136:139], a[168:171], v[168:171], v[136:139]\n" \
"v_mfma_f32_16x16x32_bf16 v[136:139], a[172:175], v[172:175], v[136:139]\n" \
"v_mfma_f32_16x16x32_bf16 v[140:143], a[188:191], v[172:175], 0\n" \
"v_mfma_f32_16x16x32_bf16 v[140:143], a[184:187], v[168:171], v[140:143]\n" \
"v_mfma_f32_16x16x32_bf16 v[140:143], a[180:183], v[164:167], v[140:143]\n" \
"v_mfma_f32_16x16x32_bf16 v[140:143], a[176:179], v[160:163], v[140:143]\n" \
"v_cmp_gt_i32 vcc, 0, v194\n" \
"s_nop 1\n" \
"v_cndmask_b32 v128, v128, v199, vcc\n" \
"v_cmp_gt_i32 vcc, 1, v194\n" \
"s_nop 1\n" \
"v_cndmask_b32 v129, v129, v199, vcc\n" \
"v_cmp_gt_i32 vcc, 2, v194\n" \
"s_nop 1\n" \
"v_cndmask_b32 v130, v130, v199, vcc\n" \
"v_cmp_gt_i32 vcc, 3, v194\n" \
"s_nop 1\n" \
"v_cndmask_b32 v131, v131, v199, vcc\n" \
"v_cmp_gt_i32 vcc, 16, v194\n" \
"s_nop 1\n" \
"v_cndmask_b32 v132, v132, v199, vcc\n" \
"v_cmp_gt_i32 vcc, 17, v194\n" \
"s_nop 1\n" \
"v_cndmask_b32 v133, v133, v199, vcc\n" \
"v_cmp_gt_i32 vcc, 18, v194\n" \
"s_nop 1\n" \
"v_cndmask_b32 v134, v134, v199, vcc\n" \
"v_cmp_gt_i32 vcc, 19, v194\n" \
"s_nop 1\n" \
"v_cndmask_b32 v135, v135, v199, vcc\n" \
"v_cmp_gt_i32 vcc, 32, v194\n" \
"s_nop 1\n" \
"v_cndmask_b32 v136, v136, v199, vcc\n" \
"v_cmp_gt_i32 vcc, 33, v194\n" \
"s_nop 1\n" \
"v_cndmask_b32 v137, v137, v199, vcc\n" \
"v_cmp_gt_i32 vcc, 34, v194\n" \
"s_nop 1\n" \
"v_cndmask_b32 v138, v138, v199, vcc\n" \
"v_cmp_gt_i32 vcc, 35, v194\n" \
"s_nop 1\n" \
"v_cndmask_b32 v139, v139, v199, vcc\n" \
"v_cmp_gt_i32 vcc, 48, v194\n" \
"s_nop 1\n" \
"v_cndmask_b32 v140, v140, v199, vcc\n" \
"v_cmp_gt_i32 vcc, 49, v194\n" \
"s_nop 1\n" \
"v_cndmask_b32 v141, v141, v199, vcc\n" \
"v_cmp_gt_i32 vcc, 50, v194\n" \
"s_nop 1\n" \
"v_cndmask_b32 v142, v142, v199, vcc\n" \
"v_cmp_gt_i32 vcc, 51, v194\n" \
"s_nop 1\n" \
"v_cndmask_b32 v143, v143, v199, vcc\n" \
"v_max3_f32 v196, v192, v128, v129\n" \
"v_max3_f32 v196, v196, v130, v131\n" \
"v_max3_f32 v196, v196, v132, v133\n" \
"v_max3_f32 v196, v196, v134, v135\n" \
"v_max3_f32 v196, v196, v136, v137\n" \
"v_max3_f32 v196, v196, v138, v139\n" \
"v_max3_f32 v196, v196, v140, v141\n" \
"v_max3_f32 v196, v196, v142, v143\n" \
"v_mov_b32 v197, v196\n" \
"s_nop 1\n" \
"v_permlane32_swap_b32 v196, v197\n" \
"v_max_f32 v196, v196, v197\n" \
"v_mov_b32 v197, v196\n" \
"s_nop 1\n" \
"v_permlane16_swap_b32 v196, v197\n" \
"v_max_f32 v196, v196, v197\n" \
"v_sub_f32 v198, v192, v196\n" \
"v_mov_b32 v192, v196\n" \
"v_exp_f32 v198, v198\n" \
"v_subrev_u32 v194, 64, v194\n" \
"s_add_u32 s82, s15, 589824\n" \
"buffer_load_dwordx4 a[192:195], v248, s[32:35], s82 offen offset:0 sc1\n" \
"buffer_load_dwordx4 a[196:199], v248, s[32:35], s82 offen offset:1024 sc1\n" \
"buffer_load_dwordx4 a[200:203], v248, s[32:35], s82 offen offset:2048 sc1\n" \
"buffer_load_dwordx4 a[204:207], v248, s[32:35], s82 offen offset:3072 sc1\n" \
"s_add_u32 s82, s15, 593920\n" \
"buffer_load_dwordx4 a[208:211], v248, s[32:35], s82 offen offset:0 sc1\n" \
"buffer_load_dwordx4 a[212:215], v248, s[32:35], s82 offen offset:1024 sc1\n" \
"buffer_load_dwordx4 a[216:219], v248, s[32:35], s82 offen offset:2048 sc1\n" \
"buffer_load_dwordx4 a[220:223], v248, s[32:35], s82 offen offset:3072 sc1\n" \
"s_add_u32 s82, s15, 598016\n" \
"buffer_load_dwordx4 a[224:227], v248, s[32:35], s82 offen offset:0 sc1\n" \
"buffer_load_dwordx4 a[228:231], v248, s[32:35], s82 offen offset:1024 sc1\n" \
"buffer_load_dwordx4 a[232:235], v248, s[32:35], s82 offen offset:2048 sc1\n" \
"buffer_load_dwordx4 a[236:239], v248, s[32:35], s82 offen offset:3072 sc1\n" \
"s_add_u32 s82, s15, 602112\n" \
"buffer_load_dwordx4 a[240:243], v248, s[32:35], s82 offen offset:0 sc1\n" \
"buffer_load_dwordx4 a[244:247], v248, s[32:35], s82 offen offset:1024 sc1\n" \
"buffer_load_dwordx4 a[248:251], v248, s[32:35], s82 offen offset:2048 sc1\n" \
"buffer_load_dwordx4 a[252:255], v248, s[32:35], s82 offen offset:3072 sc1\n" \
"s_waitcnt vmcnt(0)\n" \
"v_sub_f32 v128, v128, v196\n" \
"v_sub_f32 v129, v129, v196\n" \
"v_sub_f32 v130, v130, v196\n" \
"v_sub_f32 v131, v131, v196\n" \
"v_sub_f32 v132, v132, v196\n" \
"v_sub_f32 v133, v133, v196\n" \
"v_sub_f32 v134, v134, v196\n" \
"v_sub_f32 v135, v135, v196\n" \
"v_sub_f32 v136, v136, v196\n" \
"v_sub_f32 v137, v137, v196\n" \
"v_sub_f32 v138, v138, v196\n" \
"v_sub_f32 v139, v139, v196\n" \
"v_sub_f32 v140, v140, v196\n" \
"v_sub_f32 v141, v141, v196\n" \
"v_sub_f32 v142, v142, v196\n" \
"v_sub_f32 v143, v143, v196\n" \
"v_exp_f32 v128, v128\n" \
"v_exp_f32 v129, v129\n" \
"v_exp_f32 v130, v130\n" \
"v_exp_f32 v131, v131\n" \
"v_exp_f32 v132, v132\n" \
"v_exp_f32 v133, v133\n" \
"v_exp_f32 v134, v134\n" \
"v_exp_f32 v135, v135\n" \
"v_exp_f32 v136, v136\n" \
"v_exp_f32 v137, v137\n" \
"v_exp_f32 v138, v138\n" \
"v_exp_f32 v139, v139\n" \
"v_exp_f32 v140, v140\n" \
"v_exp_f32 v141, v141\n" \
"v_exp_f32 v142, v142\n" \
"v_exp_f32 v143, v143\n" \
"v_cvt_pk_bf16_f32 v128, v128, v129\n" \
"v_cvt_pk_bf16_f32 v129, v130, v131\n" \
"v_cvt_pk_bf16_f32 v130, v132, v133\n" \
"v_cvt_pk_bf16_f32 v131, v134, v135\n" \
"v_cvt_pk_bf16_f32 v132, v136, v137\n" \
"v_cvt_pk_bf16_f32 v133, v138, v139\n" \
"v_cvt_pk_bf16_f32 v134, v140, v141\n" \
"v_cvt_pk_bf16_f32 v135, v142, v143\n" \
"v_mul_f32 v64, v198, v64\n" \
"v_mul_f32 v65, v198, v65\n" \
"v_mul_f32 v66, v198, v66\n" \
"v_mul_f32 v67, v198, v67\n" \
"v_mul_f32 v68, v198, v68\n" \
"v_mul_f32 v69, v198, v69\n" \
"v_mul_f32 v70, v198, v70\n" \
"v_mul_f32 v71, v198, v71\n" \
"v_mul_f32 v72, v198, v72\n" \
"v_mul_f32 v73, v198, v73\n" \
"v_mul_f32 v74, v198, v74\n" \
"v_mul_f32 v75, v198, v75\n" \
"v_mul_f32 v76, v198, v76\n" \
"v_mul_f32 v77, v198, v77\n" \
"v_mul_f32 v78, v198, v78\n" \
"v_mul_f32 v79, v198, v79\n" \
"v_mul_f32 v80, v198, v80\n" \
"v_mul_f32 v81, v198, v81\n" \
"v_mul_f32 v82, v198, v82\n" \
"v_mul_f32 v83, v198, v83\n" \
"v_mul_f32 v84, v198, v84\n" \
"v_mul_f32 v85, v198, v85\n" \
"v_mul_f32 v86, v198, v86\n" \
"v_mul_f32 v87, v198, v87\n" \
"v_mul_f32 v88, v198, v88\n" \
"v_mul_f32 v89, v198, v89\n" \
"v_mul_f32 v90, v198, v90\n" \
"v_mul_f32 v91, v198, v91\n" \
"v_mul_f32 v92, v198, v92\n" \
"v_mul_f32 v93, v198, v93\n" \
"v_mul_f32 v94, v198, v94\n" \
"v_mul_f32 v95, v198, v95\n" \
"v_accvgpr_read_b32 v197, a40\n" \
"v_mul_f32 v197, v198, v197\n" \
"v_accvgpr_write_b32 a40, v197\n" \
"v_accvgpr_read_b32 v197, a41\n" \
"v_mul_f32 v197, v198, v197\n" \
"v_accvgpr_write_b32 a41, v197\n" \
"v_accvgpr_read_b32 v197, a42\n" \
"v_mul_f32 v197, v198, v197\n" \
"v_accvgpr_write_b32 a42, v197\n" \
"v_accvgpr_read_b32 v197, a43\n" \
"v_mul_f32 v197, v198, v197\n" \
"v_accvgpr_write_b32 a43, v197\n" \
"s_nop 1\n" \
"v_smfmac_f32_16x16x64_bf16 a[40:43], a[48:51], v[128:135], v241\n" \
"v_mfma_f32_16x16x32_bf16 v[64:67], a[224:227], v[132:135], v[64:67]\n" \
"v_mfma_f32_16x16x32_bf16 v[64:67], a[192:195], v[128:131], v[64:67]\n" \
"v_mfma_f32_16x16x32_bf16 v[68:71], a[196:199], v[128:131], v[68:71]\n" \
"v_mfma_f32_16x16x32_bf16 v[68:71], a[228:231], v[132:135], v[68:71]\n" \
"v_mfma_f32_16x16x32_bf16 v[72:75], a[232:235], v[132:135], v[72:75]\n" \
"v_mfma_f32_16x16x32_bf16 v[72:75], a[200:203], v[128:131], v[72:75]\n" \
"v_mfma_f32_16x16x32_bf16 v[76:79], a[204:207], v[128:131], v[76:79]\n" \
"v_mfma_f32_16x16x32_bf16 v[76:79], a[236:239], v[132:135], v[76:79]\n" \
"v_mfma_f32_16x16x32_bf16 v[80:83], a[240:243], v[132:135], v[80:83]\n" \
"v_mfma_f32_16x16x32_bf16 v[80:83], a[208:211], v[128:131], v[80:83]\n" \
"v_mfma_f32_16x16x32_bf16 v[84:87], a[212:215], v[128:131], v[84:87]\n" \
"v_mfma_f32_16x16x32_bf16 v[84:87], a[244:247], v[132:135], v[84:87]\n" \
"v_mfma_f32_16x16x32_bf16 v[88:91], a[248:251], v[132:135], v[88:91]\n" \
"v_mfma_f32_16x16x32_bf16 v[88:91], a[216:219], v[128:131], v[88:91]\n" \
"v_mfma_f32_16x16x32_bf16 v[92:95], a[220:223], v[128:131], v[92:95]\n" \
"v_mfma_f32_16x16x32_bf16 v[92:95], a[252:255], v[132:135], v[92:95]\n" \
"s_nop 7\n" \
"v_mfma_f32_16x16x32_bf16 v[128:131], a[128:131], v[176:179], 0\n" \
"v_mfma_f32_16x16x32_bf16 v[128:131], a[132:135], v[180:183], v[128:131]\n" \
"v_mfma_f32_16x16x32_bf16 v[128:131], a[136:139], v[184:187], v[128:131]\n" \
"v_mfma_f32_16x16x32_bf16 v[128:131], a[140:143], v[188:191], v[128:131]\n" \
"v_mfma_f32_16x16x32_bf16 v[132:135], a[156:159], v[188:191], 0\n" \
"v_mfma_f32_16x16x32_bf16 v[132:135], a[152:155], v[184:187], v[132:135]\n" \
"v_mfma_f32_16x16x32_bf16 v[132:135], a[148:151], v[180:183], v[132:135]\n" \
"v_mfma_f32_16x16x32_bf16 v[132:135], a[144:147], v[176:179], v[132:135]\n" \
"v_mfma_f32_16x16x32_bf16 v[136:139], a[160:163], v[176:179], 0\n" \
"v_mfma_f32_16x16x32_bf16 v[136:139], a[164:167], v[180:183], v[136:139]\n" \
"v_mfma_f32_16x16x32_bf16 v[136:139], a[168:171], v[184:187], v[136:139]\n" \
"v_mfma_f32_16x16x32_bf16 v[136:139], a[172:175], v[188:191], v[136:139]\n" \
"v_mfma_f32_16x16x32_bf16 v[140:143], a[188:191], v[188:191], 0\n" \
"v_mfma_f32_16x16x32_bf16 v[140:143], a[184:187], v[184:187], v[140:143]\n" \
"v_mfma_f32_16x16x32_bf16 v[140:143], a[180:183], v[180:183], v[140:143]\n" \
"v_mfma_f32_16x16x32_bf16 v[140:143], a[176:179], v[176:179], v[140:143]\n" \
"v_cmp_gt_i32 vcc, 0, v195\n" \
"s_nop 1\n" \
"v_cndmask_b32 v128, v128, v199, vcc\n" \
"v_cmp_gt_i32 vcc, 1, v195\n" \
"s_nop 1\n" \
"v_cndmask_b32 v129, v129, v199, vcc\n" \
"v_cmp_gt_i32 vcc, 2, v195\n" \
"s_nop 1\n" \
"v_cndmask_b32 v130, v130, v199, vcc\n" \
"v_cmp_gt_i32 vcc, 3, v195\n" \
"s_nop 1\n" \
"v_cndmask_b32 v131, v131, v199, vcc\n" \
"v_cmp_gt_i32 vcc, 16, v195\n" \
"s_nop 1\n" \
"v_cndmask_b32 v132, v132, v199, vcc\n" \
"v_cmp_gt_i32 vcc, 17, v195\n" \
"s_nop 1\n" \
"v_cndmask_b32 v133, v133, v199, vcc\n" \
"v_cmp_gt_i32 vcc, 18, v195\n" \
"s_nop 1\n" \
"v_cndmask_b32 v134, v134, v199, vcc\n" \
"v_cmp_gt_i32 vcc, 19, v195\n" \
"s_nop 1\n" \
"v_cndmask_b32 v135, v135, v199, vcc\n" \
"v_cmp_gt_i32 vcc, 32, v195\n" \
"s_nop 1\n" \
"v_cndmask_b32 v136, v136, v199, vcc\n" \
"v_cmp_gt_i32 vcc, 33, v195\n" \
"s_nop 1\n" \
"v_cndmask_b32 v137, v137, v199, vcc\n" \
"v_cmp_gt_i32 vcc, 34, v195\n" \
"s_nop 1\n" \
"v_cndmask_b32 v138, v138, v199, vcc\n" \
"v_cmp_gt_i32 vcc, 35, v195\n" \
"s_nop 1\n" \
"v_cndmask_b32 v139, v139, v199, vcc\n" \
"v_cmp_gt_i32 vcc, 48, v195\n" \
"s_nop 1\n" \
"v_cndmask_b32 v140, v140, v199, vcc\n" \
"v_cmp_gt_i32 vcc, 49, v195\n" \
"s_nop 1\n" \
"v_cndmask_b32 v141, v141, v199, vcc\n" \
"v_cmp_gt_i32 vcc, 50, v195\n" \
"s_nop 1\n" \
"v_cndmask_b32 v142, v142, v199, vcc\n" \
"v_cmp_gt_i32 vcc, 51, v195\n" \
"s_nop 1\n" \
"v_cndmask_b32 v143, v143, v199, vcc\n" \
"v_max3_f32 v196, v193, v128, v129\n" \
"v_max3_f32 v196, v196, v130, v131\n" \
"v_max3_f32 v196, v196, v132, v133\n" \
"v_max3_f32 v196, v196, v134, v135\n" \
"v_max3_f32 v196, v196, v136, v137\n" \
"v_max3_f32 v196, v196, v138, v139\n" \
"v_max3_f32 v196, v196, v140, v141\n" \
"v_max3_f32 v196, v196, v142, v143\n" \
"v_mov_b32 v197, v196\n" \
"s_nop 1\n" \
"v_permlane32_swap_b32 v196, v197\n" \
"v_max_f32 v196, v196, v197\n" \
"v_mov_b32 v197, v196\n" \
"s_nop 1\n" \
"v_permlane16_swap_b32 v196, v197\n" \
"v_max_f32 v196, v196, v197\n" \
"v_sub_f32 v198, v193, v196\n" \
"v_mov_b32 v193, v196\n" \
"v_exp_f32 v198, v198\n" \
"v_subrev_u32 v195, 64, v195\n" \
"v_sub_f32 v128, v128, v196\n" \
"v_sub_f32 v129, v129, v196\n" \
"v_sub_f32 v130, v130, v196\n" \
"v_sub_f32 v131, v131, v196\n" \
"v_sub_f32 v132, v132, v196\n" \
"v_sub_f32 v133, v133, v196\n" \
"v_sub_f32 v134, v134, v196\n" \
"v_sub_f32 v135, v135, v196\n" \
"v_sub_f32 v136, v136, v196\n" \
"v_sub_f32 v137, v137, v196\n" \
"v_sub_f32 v138, v138, v196\n" \
"v_sub_f32 v139, v139, v196\n" \
"v_sub_f32 v140, v140, v196\n" \
"v_sub_f32 v141, v141, v196\n" \
"v_sub_f32 v142, v142, v196\n" \
"v_sub_f32 v143, v143, v196\n" \
"v_exp_f32 v128, v128\n" \
"v_exp_f32 v129, v129\n" \
"v_exp_f32 v130, v130\n" \
"v_exp_f32 v131, v131\n" \
"v_exp_f32 v132, v132\n" \
"v_exp_f32 v133, v133\n" \
"v_exp_f32 v134, v134\n" \
"v_exp_f32 v135, v135\n" \
"v_exp_f32 v136, v136\n" \
"v_exp_f32 v137, v137\n" \
"v_exp_f32 v138, v138\n" \
"v_exp_f32 v139, v139\n" \
"v_exp_f32 v140, v140\n" \
"v_exp_f32 v141, v141\n" \
"v_exp_f32 v142, v142\n" \
"v_exp_f32 v143, v143\n" \
"v_cvt_pk_bf16_f32 v128, v128, v129\n" \
"v_cvt_pk_bf16_f32 v129, v130, v131\n" \
"v_cvt_pk_bf16_f32 v130, v132, v133\n" \
"v_cvt_pk_bf16_f32 v131, v134, v135\n" \
"v_cvt_pk_bf16_f32 v132, v136, v137\n" \
"v_cvt_pk_bf16_f32 v133, v138, v139\n" \
"v_cvt_pk_bf16_f32 v134, v140, v141\n" \
"v_cvt_pk_bf16_f32 v135, v142, v143\n" \
"v_mul_f32 v96, v198, v96\n" \
"v_mul_f32 v97, v198, v97\n" \
"v_mul_f32 v98, v198, v98\n" \
"v_mul_f32 v99, v198, v99\n" \
"v_mul_f32 v100, v198, v100\n" \
"v_mul_f32 v101, v198, v101\n" \
"v_mul_f32 v102, v198, v102\n" \
"v_mul_f32 v103, v198, v103\n" \
"v_mul_f32 v104, v198, v104\n" \
"v_mul_f32 v105, v198, v105\n" \
"v_mul_f32 v106, v198, v106\n" \
"v_mul_f32 v107, v198, v107\n" \
"v_mul_f32 v108, v198, v108\n" \
"v_mul_f32 v109, v198, v109\n" \
"v_mul_f32 v110, v198, v110\n" \
"v_mul_f32 v111, v198, v111\n" \
"v_mul_f32 v112, v198, v112\n" \
"v_mul_f32 v113, v198, v113\n" \
"v_mul_f32 v114, v198, v114\n" \
"v_mul_f32 v115, v198, v115\n" \
"v_mul_f32 v116, v198, v116\n" \
"v_mul_f32 v117, v198, v117\n" \
"v_mul_f32 v118, v198, v118\n" \
"v_mul_f32 v119, v198, v119\n" \
"v_mul_f32 v120, v198, v120\n" \
"v_mul_f32 v121, v198, v121\n" \
"v_mul_f32 v122, v198, v122\n" \
"v_mul_f32 v123, v198, v123\n" \
"v_mul_f32 v124, v198, v124\n" \
"v_mul_f32 v125, v198, v125\n" \
"v_mul_f32 v126, v198, v126\n" \
"v_mul_f32 v127, v198, v127\n" \
"v_accvgpr_read_b32 v197, a44\n" \
"v_mul_f32 v197, v198, v197\n" \
"v_accvgpr_write_b32 a44, v197\n" \
"v_accvgpr_read_b32 v197, a45\n" \
"v_mul_f32 v197, v198, v197\n" \
"v_accvgpr_write_b32 a45, v197\n" \
"v_accvgpr_read_b32 v197, a46\n" \
"v_mul_f32 v197, v198, v197\n" \
"v_accvgpr_write_b32 a46, v197\n" \
"v_accvgpr_read_b32 v197, a47\n" \
"v_mul_f32 v197, v198, v197\n" \
"v_accvgpr_write_b32 a47, v197\n" \
"s_nop 1\n" \
"v_smfmac_f32_16x16x64_bf16 a[44:47], a[48:51], v[128:135], v241\n" \
"v_mfma_f32_16x16x32_bf16 v[96:99], a[224:227], v[132:135], v[96:99]\n" \
"v_mfma_f32_16x16x32_bf16 v[96:99], a[192:195], v[128:131], v[96:99]\n" \
"v_mfma_f32_16x16x32_bf16 v[100:103], a[196:199], v[128:131], v[100:103]\n" \
"v_mfma_f32_16x16x32_bf16 v[100:103], a[228:231], v[132:135], v[100:103]\n" \
"v_mfma_f32_16x16x32_bf16 v[104:107], a[232:235], v[132:135], v[104:107]\n" \
"v_mfma_f32_16x16x32_bf16 v[104:107], a[200:203], v[128:131], v[104:107]\n" \
"v_mfma_f32_16x16x32_bf16 v[108:111], a[204:207], v[128:131], v[108:111]\n" \
"v_mfma_f32_16x16x32_bf16 v[108:111], a[236:239], v[132:135], v[108:111]\n" \
"v_mfma_f32_16x16x32_bf16 v[112:115], a[240:243], v[132:135], v[112:115]\n" \
"v_mfma_f32_16x16x32_bf16 v[112:115], a[208:211], v[128:131], v[112:115]\n" \
"v_mfma_f32_16x16x32_bf16 v[116:119], a[212:215], v[128:131], v[116:119]\n" \
"v_mfma_f32_16x16x32_bf16 v[116:119], a[244:247], v[132:135], v[116:119]\n" \
"v_mfma_f32_16x16x32_bf16 v[120:123], a[248:251], v[132:135], v[120:123]\n" \
"v_mfma_f32_16x16x32_bf16 v[120:123], a[216:219], v[128:131], v[120:123]\n" \
"v_mfma_f32_16x16x32_bf16 v[124:127], a[220:223], v[128:131], v[124:127]\n" \
"v_mfma_f32_16x16x32_bf16 v[124:127], a[252:255], v[132:135], v[124:127]\n" \
"s_nop 7\n" \
"s_add_u32 s15, s15, 0x4000\n" \
"s_sub_u32 s14, s14, 1\n" \
"s_cbranch_scc0 .Lslr2_%=\n" \
"s_nop 7\n" \
"s_setpc_b64 s[98:99]\n" \
".Lspr2_%=:\n" \
"s_mov_b32 s82, 0x100000\n" \
".Lsqr2_%=:\n" \
"s_sleep 6\n" \
"buffer_load_dword v197, v196, s[32:35], s26 offen sc1\n" \
"s_waitcnt vmcnt(0)\n" \
"v_cmp_eq_u32 vcc, 0, v197\n" \
"s_cbranch_vccz .Lsbr2_%=\n" \
"s_sub_u32 s82, s82, 1\n" \
"s_cbranch_scc0 .Lsqr2_%=\n" \
"s_branch .Lsbr2_%=\n" \
".Lend_%=:\n" \
".Lst3_4_%=:\n" \
"s_cbranch_execz .Lstb3_%=\n"
struct Desc { const void* p; unsigned bytes, flags; };
struct Args { Desc ckv, w_uk, w_uv, w_q, ws, w_mod, cvec, x, w_o, wg, wu, wd, out; const void* w_r; };
constexpr size_t WS_DBG = (size_t)56 << 20; constexpr unsigned WS_BYTES = 80u << 20, LDS_BYTES = 163840;
typedef const __attribute__((address_space(4))) Args* kargs_t;
typedef unsigned u32x16 __attribute__((ext_vector_type(16)));
#define VCLOB_LOW "v0","v1","v2","v3","v4","v5","v6","v7","v8","v9","v10","v11","v12","v13","v14","v15","v16","v17","v18","v19","v20","v21","v22","v23","v24","v25","v26","v27","v28","v29","v30","v31","v32","v33","v34","v35","v36","v37","v38","v39","v40","v41","v42","v43","v44","v45","v46","v47","v48","v49","v50","v51","v52","v53","v54","v55","v56","v57","v58","v59","v60","v61","v62","v63","v64","v65","v66","v67","v68","v69","v70","v71","v72","v73","v74","v75","v76","v77","v78","v79","v80","v81","v82","v83","v84","v85","v86","v87","v88","v89","v90","v91","v92","v93","v94","v95","v96","v97","v98","v99","v100","v101","v102","v103","v104","v105","v106","v107","v108","v109","v110","v111","v112","v113","v114","v115","v116","v117","v118","v119","v120","v121","v122","v123","v124","v125","v126","v127","v128","v129","v130","v131","v132","v133","v134","v135","v136","v137","v138","v139","v140","v141","v142","v143","v144","v145","v146","v147","v148","v149","v150","v151","v152","v153","v154","v155","v156","v157","v158","v159","v160","v161","v162","v163","v164","v165","v166","v167","v168","v169","v170","v171","v172","v173","v174","v175","v176","v177","v178","v179","v180","v181","v182","v183","v184","v185","v186","v187","v188","v189","v190","v191","v192","v193","v194","v195","v196","v197","v198","v199","v200","v201","v202","v203","v204","v205","v206","v207","v208","v209","v210","v211","v212","v213","v214","v215","v216","v217","v218","v219","v220","v221","v222","v223","v224","v225","v226","v227","v228","v229","v230","v231","v232","v233","v234","v235","v236","v237","v238","v239"
#define SCLOB "s14","s15","s16","s17","s18","s19","s20","s21","s22","s23","s24","s25","s26","s27","s28","s29","s30","s31","s32","s33","s34","s35","s36","s37","s38","s39","s40","s41","s42","s43","s44","s45","s46","s47","s48","s49","s50","s51","s52","s53","s54","s55","s56","s57","s58","s59","s60","s61","s62","s63","s64","s65","s66","s67","s68","s69","s70","s71","s72","s73","s74","s75","s76","s77","s78","s79","s80","s81","s82","s83","s84","s85","s86","s87","s88","s89","s90","s91","s92","s93","s94","s95","s96","s97","s98","s99","s100","s101"
__global__ __launch_bounds__(256, 1) __attribute__((aligned(4096))) void k_all(const Args a) {
    unsigned wid = __builtin_amdgcn_readfirstlane(threadIdx.x >> 6);
    u32x16 RB;
    asm volatile(MAIN_ASM : "={v[240:255]}"(RB)
                 : [ka] "s"(__builtin_amdgcn_kernarg_segment_ptr()), [bid] "s"(blockIdx.x), [wid] "s"(wid) : "memory", "vcc", "scc", VCLOB_LOW, SCLOB, ACLOB_ALL);
    asm volatile(".Lat_code_e:");
}
extern "C" void kernel_launch(void* const* d_in, const int* in_sizes, int n_in, void* d_out, int out_size, void* d_ws, size_t ws_size, hipStream_t stream) {
    Args a;
    auto D = [](const void* p, unsigned bytes) { return Desc{p, bytes, 0x00020000u}; };
    a.ckv = D(d_in[1], SEQ * DM * 2); a.w_uk = D(d_in[3], DM * HD * 2); a.w_uv = D(d_in[4], DM * HD * 2); a.w_q = D(d_in[2], DM * HD * 2);
    a.ws = D(d_ws, WS_BYTES); a.w_mod = D(d_in[12], DM * 6 * DM * 2); a.cvec = D(d_in[11], DM * 2); a.x = D(d_in[0], SEQ * DM * 2);
    a.w_o = D(d_in[5], HD * DM * 2); a.wg = D(d_in[8], DM * FF * 2); a.wu = D(d_in[9], DM * FF * 2); a.wd = D(d_in[10], FF * DM * 2);
    a.out = D(d_out, SEQ * DM * 2); a.w_r = d_in[7];
    (void)hipFuncSetAttribute((const void*)k_all, hipFuncAttributeMaxDynamicSharedMemorySize, LDS_BYTES);
    k_all<<<256, 256, LDS_BYTES, stream>>>(a);
}
```
